```python
import torch

def _softmax(x, axis=-1):
    return torch.softmax(x, dim=axis)

def _gelu(x):
    return torch.nn.functional.gelu(x, approximate='tanh')

def _silu(x):
    return torch.nn.functional.silu(x)

def _sigmoid(x):
    return torch.sigmoid(x)

def _layernorm(x, axis=-1, eps=1e-05):
    m = x.mean(dim=axis, keepdim=True)
    v = x.var(dim=axis, keepdim=True, unbiased=False)
    return (x - m) / torch.sqrt(v + eps)

def _scatter_add(tgt, idx, src, axis=0):
    return tgt.clone().index_add_(axis, idx, src)

def _segment_sum(src, seg, n):
    out = torch.zeros((n,) + src.shape[1:], dtype=src.dtype)
    return out.index_add_(0, seg, src)

def _pad(x, pw):
    flat = tuple((v for pair in reversed(pw) for v in pair))
    return torch.nn.functional.pad(x, flat)
kernel_name = 'gated_mix__sliding_local_dilated__memory_aug_recurrent_mem_tr__prompt'

def setup_inputs():

    _w = lambda a: (lambda t: t.to(torch.bfloat16) if t.is_floating_point() else t)(torch.as_tensor(a))

    def _A_setup():
        B, S, D = (1, 2048, 576)
        return {'x': _w((lambda *s: torch.randn(*s, dtype=torch.bfloat16) * 0.05)(B, S, D)), 'wqkv': _w((lambda *s: torch.randn(*s, dtype=torch.bfloat16) * 0.8)(D, 3 * D)), 'wo': _w((lambda *s: torch.randn(*s, dtype=torch.bfloat16) * 0.05)(D, D))}
    _A_in = _A_setup()

    def _B_setup():
        B, S, Dm = (1, 2048, 576)
        H, Dh = (32, 128)
        M, seg = (8, 32)
        seg = min(seg, S)
        S = S // seg * seg
        return {'x': _w((lambda *s: torch.randn(*s, dtype=torch.bfloat16) * 0.05)(B, S, Dm)), 'mem0': _w((lambda *s: torch.randn(*s, dtype=torch.bfloat16) * 1.6)(B, M, Dm)), 'Wq': _w((lambda *s: torch.randn(*s, dtype=torch.bfloat16) * 0.8)(Dm, H, Dh) * Dm ** (-0.5)), 'Wk': _w((lambda *s: torch.randn(*s, dtype=torch.bfloat16) * 0.8)(Dm, H, Dh) * Dm ** (-0.5)), 'Wv': _w((lambda *s: torch.randn(*s, dtype=torch.bfloat16) * 1.6)(Dm, H, Dh) * Dm ** (-0.5)), 'Wo': _w((lambda *s: torch.randn(*s, dtype=torch.bfloat16) * 1.6)(H, Dh, Dm) * (H * Dh) ** (-0.5)), 'seg_len': seg}
    _B_in = _B_setup()
    return {'A_x': _A_in['x'], 'A_wqkv': _A_in['wqkv'], 'A_wo': _A_in['wo'], 'B_x': _B_in['x'], 'B_mem0': _B_in['mem0'], 'B_Wq': _B_in['Wq'], 'B_Wk': _B_in['Wk'], 'B_Wv': _B_in['Wv'], 'B_Wo': _B_in['Wo'], 'B_seg_len': _B_in['seg_len'], 'gate_w': _w((lambda *s: torch.randn(*s, dtype=torch.bfloat16) * 0.05)(576) * 16.0)}

def reference(A_x, A_wqkv, A_wo, B_x, B_mem0, B_Wq, B_Wk, B_Wv, B_Wo, B_seg_len, gate_w):

    def _A_ref(x, wqkv, wo):
        B, S, D = x.shape
        H = 32
        Dh = D // H
        W, dil = (8, 2)
        qkv = (lambda x, *d: x.permute(*d))((x @ wqkv).reshape(B, S, 3, H, Dh), 2, 0, 3, 1, 4)
        q, k, v = (qkv[0], qkv[1], qkv[2])
        scale = Dh ** (-0.5)
        pos = torch.arange(S)
        off = torch.arange(W) * dil
        idx = pos[:, None] - off[None, :]
        mask = idx >= 0
        idx_c = torch.where(mask, idx, 0).reshape(-1)
        kg = k[:, :, idx_c, :].reshape(B, H, S, W, Dh)
        vg = v[:, :, idx_c, :].reshape(B, H, S, W, Dh)
        logits = torch.einsum('bhsd,bhswd->bhsw', q, kg) * scale
        logits = torch.where(mask[None, None, :, :], logits, -1000000000.0)
        attn = _softmax(logits, axis=-1)
        o = torch.einsum('bhsw,bhswd->bhsd', attn, vg)
        return (lambda x, *d: x.permute(*d))(o, 0, 2, 1, 3).reshape(B, S, D) @ wo

    def _B_ref(x, mem0, Wq, Wk, Wv, Wo, seg_len):
        B, S, Dm = x.shape
        H, Dh = (Wq.shape[1], Wq.shape[2])
        M = mem0.shape[1]
        scale = Dh ** (-0.5)
        n_seg = S // seg_len
        L = M + seg_len
        idx = torch.arange(L)
        mask = (idx[:, None] < M) | (idx[:, None] >= idx[None, :])
        mem = mem0
        outs = []
        for s in range(n_seg):
            xs = x[:, s * seg_len:(s + 1) * seg_len, :].clone()
            h = torch.concatenate([mem, xs], axis=1)
            q = torch.einsum('btd,dhe->bhte', h, Wq)
            k = torch.einsum('btd,dhe->bhte', h, Wk)
            v = torch.einsum('btd,dhe->bhte', h, Wv)
            scores = torch.einsum('bhqe,bhke->bhqk', q, k) * scale
            scores = torch.where(mask[None, None], scores, -1000000000.0)
            p = _softmax(scores, axis=-1)
            o = torch.einsum('bhqk,bhke->bhqe', p, v)
            o = torch.einsum('bhte,hed->btd', o, Wo)
            mem = o[:, :M, :].clone()
            outs.append(o[:, M:, :])
        return torch.concatenate(outs, axis=1)
    x = A_x
    g = _sigmoid((x * gate_w).sum(-1, keepdims=True))
    a = _A_ref(x, A_wqkv, A_wo)
    b = _B_ref(x, B_mem0, B_Wq, B_Wk, B_Wv, B_Wo, B_seg_len)
    return g * a + (1 - g) * b
```

```cpp
#include <hip/hip_runtime.h>
#include <stdint.h>
#define LAS __attribute__((address_space(3)))
#define GAS __attribute__((address_space(1)))
typedef unsigned v2u __attribute__((ext_vector_type(2)));
typedef unsigned v4u __attribute__((ext_vector_type(4)));
typedef unsigned v8u __attribute__((ext_vector_type(8)));
typedef unsigned v16u __attribute__((ext_vector_type(16)));
typedef unsigned v32u __attribute__((ext_vector_type(32)));
typedef float f4v __attribute__((ext_vector_type(4)));
typedef short s4v __attribute__((ext_vector_type(4)));
typedef __bf16 b8v __attribute__((ext_vector_type(8)));
typedef __bf16 b2v __attribute__((ext_vector_type(2)));
typedef unsigned short u16;
typedef GAS unsigned gu32;
typedef LAS unsigned char* lptr;
#define RLX_AGENT __ATOMIC_RELAXED, __HIP_MEMORY_SCOPE_AGENT

constexpr int DM = 576, NSEG = 64, LDS_BYTES = 160 * 1024, SPIN = 400000;
constexpr unsigned CH = 32768;
constexpr unsigned CTL = 16u << 20, CTL_BYTES = 108544;
constexpr unsigned C_TICK = CTL, C_OVF = CTL + 1024, C_TAB = CTL + CTL_BYTES;
constexpr unsigned C_KVP = CTL + 77824, C_HF = CTL + 86016, C_VF = CTL + 94208, C_QAP = CTL + 102400, C_AP = CTL + 106496;
constexpr unsigned WO_KV = 96u << 20;
constexpr unsigned WO_KM = 42u << 20, WO_VP = 46u << 20;
constexpr unsigned WO_O = 50u << 20;
constexpr unsigned WO_OA = 66u << 20;
constexpr unsigned WO_G = 69u << 20;
constexpr unsigned WO_ST = 70u << 20, WO_DB = 76u << 20, WO_Q = 80u << 20;
constexpr float QS = 0.08838834764831845f * 1.4426950408889634f;

struct Params { const u16* x; const u16* wqkv; const u16* wo_a; const u16* mem0; const u16* Wq; const u16* Wk; const u16* Wv; const u16* Wo; const u16* gate;
                u16* out; unsigned char* ws; };

__device__ __forceinline__ unsigned xcc_id() { return (unsigned)__builtin_amdgcn_s_getreg(20 | (3 << 11)) & 0xfu; }
__device__ __forceinline__ unsigned rfl(unsigned v) { return __builtin_amdgcn_readfirstlane(v); }
__device__ __forceinline__ f4v mfma32(v4u a, v4u b, f4v c) { return __builtin_amdgcn_mfma_f32_16x16x32_bf16(__builtin_bit_cast(b8v, a), __builtin_bit_cast(b8v, b), c, 0, 0, 0); }
__device__ __forceinline__ unsigned pack2(float lo, float hi) { b2v p; p.x = (__bf16)lo; p.y = (__bf16)hi; return __builtin_bit_cast(unsigned, p); }
__device__ __forceinline__ v4u pack8(f4v a, f4v b) { return (v4u){pack2(a.x, a.y), pack2(a.z, a.w), pack2(b.x, b.y), pack2(b.z, b.w)}; }
__device__ __forceinline__ float rbf(float x) { return (float)(__bf16)x; }
__device__ __forceinline__ float bflo(unsigned u) { return __uint_as_float(u << 16); }
__device__ __forceinline__ float bfhi(unsigned u) { return __uint_as_float(u & 0xffff0000u); }
__device__ __forceinline__ v2u lds_tr(lptr p) { return __builtin_bit_cast(v2u, __builtin_amdgcn_ds_read_tr16_b64_v4i16((LAS s4v*)p)); }
__device__ __forceinline__ v4u lds16(lptr p) { return *(LAS v4u*)p; }
#define WAIT_VM(n) asm volatile("s_waitcnt vmcnt(" #n ")" ::: "memory")
__device__ __forceinline__ void wg_barrier() { asm volatile("s_waitcnt lgkmcnt(0)\n\ts_barrier" ::: "memory"); }
#define DMA_BODY(FL) unsigned keep; asm volatile("s_mov_b32 %0, m0\n\ts_mov_b32 m0, %3\n\ts_nop 0\n\tglobal_load_lds_dwordx4 %1, %2" FL "\n\ts_mov_b32 m0, %0" \
                                              : "=&s"(keep) : "v"(off), "s"(base), "s"(dst) : "memory")
__device__ __forceinline__ void dma(const void* base, unsigned off, unsigned dst) { DMA_BODY(""); }
__device__ __forceinline__ void dma_dev(const void* base, unsigned off, unsigned dst) { DMA_BODY(" sc1"); }
__device__ __forceinline__ void st16(unsigned char* ws, unsigned off, v4u v) { asm volatile("global_store_dwordx4 %0, %1, %2 sc1\n\ts_nop 1" :: "v"(off), "v"(v), "s"(ws) : "memory"); }
__device__ __forceinline__ unsigned ctl_ld(unsigned char* ws, unsigned off) { return __hip_atomic_load((gu32*)(ws + off), RLX_AGENT); }
__device__ __forceinline__ void ctl_st(unsigned char* ws, unsigned off, unsigned v) { __hip_atomic_store((gu32*)(ws + off), v, RLX_AGENT); }
__device__ __forceinline__ unsigned ctl_add(unsigned char* ws, unsigned off, unsigned v) { return __hip_atomic_fetch_add((gu32*)(ws + off), v, RLX_AGENT); }
__device__ __forceinline__ bool wait_ge(unsigned char* ws, unsigned off, unsigned want) {
  for (int i = 0; i < SPIN; ++i) {
    if (__all(ctl_ld(ws, off) >= want)) return true;
    __builtin_amdgcn_s_sleep(4);
  }
  return false;
}
__device__ __forceinline__ unsigned chunk(unsigned long long map, unsigned g, unsigned c, unsigned off) {
  const unsigned m = (unsigned)(map >> (16 * g));
  return (m & 7) * 0x200000u + c * CH + (((m >> (3 + 3 * (c >> 4))) & 7) << 12) + off;
}
__device__ __forceinline__ float xor16(float v) { auto s = __builtin_amdgcn_permlane16_swap(__float_as_uint(v), __float_as_uint(v), false, false); return __uint_as_float((threadIdx.x & 16) ? s[0] : s[1]); }
__device__ __forceinline__ float xor32(float v) { auto s = __builtin_amdgcn_permlane32_swap(__float_as_uint(v), __float_as_uint(v), false, false); return __uint_as_float((threadIdx.x & 32) ? s[0] : s[1]); }
template <int C> __device__ __forceinline__ float dpp(float v) { return __int_as_float(__builtin_amdgcn_update_dpp(0, __float_as_int(v), C, 0xf, 0xf, true)); }
#define STAMP(s, i) do { } while (0)
__device__ __forceinline__ unsigned x_src(int wave, int lane) { return (8 * wave + (lane >> 3)) * 1152 + ((lane & 7) ^ ((4 * wave + (lane >> 4)) & 7)) * 16; }
__device__ __forceinline__ unsigned x_frag(int lane) { return (lane & 15) * 128 + ((lane >> 4) ^ ((lane >> 1) & 7)) * 16; }

#define UNIFORM(v, n) _Pragma("unroll") for (int i_ = 0; i_ < n; ++i_) v[i_] = rfl(v[i_])
#define PTR(v, i, ptr) do { const unsigned long long a_ = (unsigned long long)(ptr); v[i] = (unsigned)a_; v[(i) + 1] = (unsigned)(a_ >> 32); } while (0)
#define CLOB "v255", "a255", "s99", "vcc", "memory"

__global__ void __launch_bounds__(512) gm_setup(Params p) {
  __shared__ unsigned L[8];
  const int b = blockIdx.x, tid = threadIdx.x, lane = tid & 63, wave = rfl(tid >> 6);
  const unsigned base = (b & 7) * 0x200000u + (b >> 3) * 16 * CH;
  if (wave == 0) {
    unsigned b1 = 0, tp = 0, m[4] = {~0u, ~0u, ~0u, ~0u};
#pragma unroll 1
    for (int r = -2; r < 4; ++r) {
      const unsigned k = r == -2 ? lane & 7 : r == -1 ? 2 : b1 | (r & 1) | ((r & 2) << 1), off = base + (k << 12);
      unsigned v = 0; unsigned long long t0, t1; const unsigned long long on = r == -2 ? 255 : 1;
      asm volatile("s_mov_b64 exec, %5\n\ts_memtime %1\n\ts_waitcnt lgkmcnt(0)\n\tglobal_atomic_add %0, %3, %0, %4 sc0 sc1\n\ts_waitcnt vmcnt(0)\n\t"
                   "s_memtime %2\n\ts_waitcnt lgkmcnt(0)\n\ts_mov_b64 exec, -1" : "+v"(v), "=&s"(t0), "=&s"(t1) : "v"(off), "s"(p.ws), "s"(on) : "memory");
      unsigned t = (unsigned)(t1 - t0);
      if (r == 0 && tp < t) { b1 = 2; t = tp; }
      tp = t;
#pragma unroll
      for (int q = 0; q < 4; ++q) if (r >= 0 && (r & 3) == q) m[q] = min(m[q], t);
    }
    if (lane == 0) { L[0] = (m[1] + m[3] < m[0] + m[2]) | b1 | ((m[2] + m[3] < m[0] + m[1]) << 2); L[4] = m[0]; L[5] = m[1]; L[6] = m[2]; L[7] = m[3]; }
  }
  __syncthreads();
  const unsigned cls = L[0];
#pragma unroll
  for (int i = 0; i < 8; ++i) {
    const int c = 2 * i + (tid >> 8), pos = tid & 255;
    v4u v = {~0u, ~0u, ~0u, ~0u};
    if (i < 3 && b < 8 && (c < 4 || (c == 4 && pos < 128))) {
      const int line = c < 4 ? (c & 1) * 32 + (pos >> 3) : 64 + ((pos >> 3) & 7), j = pos & 7, m = 2 * (line & 3) + (j & 1), col = 32 * (line >> 2) + (j >> 1);
#pragma unroll
      for (int e = 0; e < 4; ++e) {
        const unsigned lo = p.mem0[m * DM + col + 8 * e], hi = p.mem0[m * DM + col + 8 * e + 4];
        v[e] = ((lo & 0x7fff) > 0x7f81 ? 0x7f81 : lo) | (((hi & 0x7fff) > 0x7f81 ? 0x7f81 : hi) << 16);
      }
    }
    st16(p.ws, base + c * CH + (cls << 12) + pos * 16, v);
  }
  if (tid < CTL_BYTES / 512) st16(p.ws, CTL + (b * (CTL_BYTES / 512) + tid) * 16, (v4u){0, 0, 0, 0});
  if (tid == 0) ctl_st(p.ws, C_TAB + b * 4, cls | (xcc_id() << 8));
  if (tid == 0 && b == 0) ctl_st(p.ws, C_TAB + 128, (unsigned)__builtin_amdgcn_s_memrealtime() * 24u);
}

__global__ void __launch_bounds__(256, 1) gm_kernel(Params p) {
  extern __shared__ __attribute__((aligned(16))) unsigned char smem[];
  const lptr lds = (lptr)smem;
  const int tid = threadIdx.x, lane = tid & 63, wave = rfl(tid >> 6);
  const unsigned tab = *(const gu32*)(p.ws + C_TAB + min(lane, 32) * 4);
  if (wave == 0) {
    const unsigned x = xcc_id() & 7;
    unsigned t = 0;
    if (lane == 0) t = ctl_add(p.ws, C_TICK + x * 128, 1u);
    t = rfl(t);
    int role = x * 32 + t;
    if (t >= 32) {
      unsigned j = 0;
      if (lane == 0) j = ctl_add(p.ws, C_OVF, 1u);
      j = rfl(j); role = -1;
      for (int i = 0; i < SPIN && role < 0; ++i) {
        const unsigned cnt = ctl_ld(p.ws, C_TICK + (lane & 7) * 128);
        unsigned sum = 0, left = j; int r = -1;
        for (int d = 0; d < 8; ++d) {
          const unsigned cd = __builtin_amdgcn_readlane(cnt, d), open = cd < 32 ? 32 - cd : 0;
          sum += cd;
          if (r < 0) { if (left < open) r = d * 32 + cd + left; else left -= open; }
        }
        if (sum == 256) role = r;
      }
    }
    if (lane == 0) *(LAS int*)lds = role;
  }
  wg_barrier();
  const int role = rfl(*(LAS int*)lds);
  wg_barrier();
  if (role < 0) return;
  STAMP(63, 15);
  const int n = lane & 15, g = lane >> 4, qq = n >> 2, c4 = n & 3, gam = role >> 5, tk = role & 31;
  v16u vin; v8u sa; v16u sb;
#pragma unroll
  for (int i = 0; i < 16; ++i) { vin[i] = 0; sb[i] = 0; }
#pragma unroll
  for (int i = 0; i < 8; ++i) sa[i] = 0;
  PTR(sa, 0, p.ws);
  vin[0] = lane * 16;
  if (gam < 4) {
    const unsigned m1pat = ((lane >> 5) * 4 + qq) * 128 + (g & 1) * 64 + c4 * 16, idx = (lane & 2) ? 0xEEEEu : 0x4444u;
    const int trrow = 16 * (g & 1) + 2 * (g >> 1) + 4 * (qq & 1) + (qq >> 1);
    unsigned long long cls = 0;
    {
      unsigned used = 0; int pg[4];
#pragma unroll
      for (int i = 0; i < 4; ++i) { const unsigned m = (unsigned)__ballot(lane < 8 && (tab >> 8) == (unsigned)i); pg[i] = m ? __builtin_ctz(m) : -1; if (m) used |= 1u << pg[i]; }
#pragma unroll
      for (int i = 0; i < 4; ++i) {
        if (pg[i] < 0) { pg[i] = __builtin_ctz(~used); used |= 1u << pg[i]; }
        unsigned m = pg[i];
#pragma unroll
        for (int k = 0; k < 4; ++k) m |= (__builtin_amdgcn_readlane(tab, pg[i] + 8 * k) & 7) << (3 + 3 * k);
        cls |= (unsigned long long)m << (16 * i);
      }
    }
    sb[4] = wave; sb[5] = __builtin_amdgcn_readlane(tab, 32); sb[7] = 2200; sb[10] = WO_ST + (role * 4 + wave) * 4096;
    if (tk < 16) {
      const int h = gam * 8 + (tk >> 1), half = tk & 1, flip = gam & 1;
      vin[1] = m1pat + wave * 1024; vin[2] = qq * 256 + g * 64 + c4 * 16;
      vin[3] = 42240 + (32 * wave + n) * 32 + 8 * (g ^ qq);
      vin[4] = 42240 + 4096 + (16 * wave + n) * 32 + 8 * (g ^ qq);
      vin[5] = 42240 + (4 * g + qq) * 32 + 8 * (c4 ^ g);
      vin[6] = idx; vin[7] = (lane & 2) ? 0x0c0c0c0cu : 0x07060302u; vin[8] = (lane & 2) ? 0x07060302u : 0x0c0c0c0cu;
#pragma unroll
      for (int i = 0; i < 3; ++i) {
        const int cg = i < 2 ? 8 * half + 2 * wave + (i ^ flip) : 16 + half, gr = (cg & 1) + 2 * (g >> 1), hs = ((0x3120 >> (4 * (gr ^ gam))) & 3) * 8 + (h & 7);
        vin[9 + i] = chunk(cls, gr, 28 + 4 * (cg >> 1) + (hs >> 4), (hs & 15) * 256 + (i == 2 ? (wave & 1) * 128 : 0) + (g & 1) * 64 + n * 4);
      }
      { const int row = 4 * (wave & 1) + 16 * (wave >> 1) + g, sw = 2 * ((row & 1) | ((row >> 1) & 2) | ((row >> 2) & 4)); vin[12] = row * 8192 + (n ^ sw) * 16; }
      { const int sw = 2 * ((trrow & 1) | ((trrow >> 1) & 2) | ((trrow >> 2) & 4)); vin[13] = trrow * 256 + ((4 * wave + (c4 >> 1)) ^ sw) * 16 + (c4 & 1) * 8; }
      vin[14] = (16 * wave + (lane >> 3)) * 1152 + ((lane & 7) ^ (2 * wave)) * 16;
      { const int row = 32 * (g & 1) + 4 * (g >> 1) + (qq & 1) + 16 * (qq >> 1); vin[15] = row * 128 + ((c4 >> 1) ^ (2 * ((row >> 4) & 3))) * 16 + (c4 & 1) * 8; }
      PTR(sa, 2, p.Wq + h * 128); PTR(sa, 4, p.Wk + h * 128); PTR(sa, 6, p.Wo + (size_t)h * 128 * DM + half * 256);
      sb[0] = chunk(cls, gam, 0, 0); sb[1] = C_KVP + h * 256; sb[2] = chunk(cls, gam, 16 + ((h & 7) >> 1), (h & 1) * 2048);
      sb[3] = WO_KV + h * 16384 + wave * 4096; sb[8] = 1024 - 512 * half; sb[9] = WO_KM + h * 2048 + wave * 512;
      sb[11] = __float_as_uint(QS); sb[12] = flip * 64; sb[13] = gam * 128; sb[14] = 2500; sb[15] = 720;
      UNIFORM(sa, 8); UNIFORM(sb, 16);
      asm volatile(
  "s_nop 4\n"
  "s_lshl_b32 s80, s48, 10\n"
  "s_lshl_b32 s78, s48, 11\n"
  "s_and_b32 s79, s80, 1024\n"
  "s_and_b32 s74, s78, 4096\n"
  "s_add_u32 s79, s79, s74\n"
  "s_add_u32 m0, s79, 0\n"
  "s_add_u32 s74, s40, 0\n"
  "s_addc_u32 s75, s41, 0\n"
  "global_load_lds_dwordx4 v12, s[74:75]\n"
  "s_add_u32 m0, s79, 2048\n"
  "s_add_u32 s74, s40, 65536\n"
  "s_addc_u32 s75, s41, 0\n"
  "global_load_lds_dwordx4 v12, s[74:75]\n"
  "s_add_u32 m0, s79, 8192\n"
  "s_add_u32 s74, s40, 262144\n"
  "s_addc_u32 s75, s41, 0\n"
  "global_load_lds_dwordx4 v12, s[74:75]\n"
  "s_add_u32 m0, s79, 10240\n"
  "s_add_u32 s74, s40, 327680\n"
  "s_addc_u32 s75, s41, 0\n"
  "global_load_lds_dwordx4 v12, s[74:75]\n"
  "s_add_u32 m0, s79, 16384\n"
  "s_add_u32 s74, s40, 524288\n"
  "s_addc_u32 s75, s41, 0\n"
  "global_load_lds_dwordx4 v12, s[74:75]\n"
  "s_add_u32 m0, s79, 18432\n"
  "s_add_u32 s74, s40, 589824\n"
  "s_addc_u32 s75, s41, 0\n"
  "global_load_lds_dwordx4 v12, s[74:75]\n"
  "s_add_u32 m0, s79, 24576\n"
  "s_add_u32 s74, s40, 786432\n"
  "s_addc_u32 s75, s41, 0\n"
  "global_load_lds_dwordx4 v12, s[74:75]\n"
  "s_add_u32 m0, s79, 26624\n"
  "s_add_u32 s74, s40, 851968\n"
  "s_addc_u32 s75, s41, 0\n"
  "global_load_lds_dwordx4 v12, s[74:75]\n"
  "s_add_u32 m0, s79, 32768\n"
  "s_add_u32 s74, s40, 1048576\n"
  "s_addc_u32 s75, s41, 0\n"
  "global_load_lds_dwordx4 v12, s[74:75]\n"
  "s_add_u32 m0, s79, 34816\n"
  "s_add_u32 s74, s40, 1114112\n"
  "s_addc_u32 s75, s41, 0\n"
  "global_load_lds_dwordx4 v12, s[74:75]\n"
  "s_add_u32 m0, s79, 40960\n"
  "s_add_u32 s74, s40, 1310720\n"
  "s_addc_u32 s75, s41, 0\n"
  "global_load_lds_dwordx4 v12, s[74:75]\n"
  "s_add_u32 m0, s79, 43008\n"
  "s_add_u32 s74, s40, 1376256\n"
  "s_addc_u32 s75, s41, 0\n"
  "global_load_lds_dwordx4 v12, s[74:75]\n"
  "s_add_u32 m0, s79, 49152\n"
  "s_add_u32 s74, s40, 1572864\n"
  "s_addc_u32 s75, s41, 0\n"
  "global_load_lds_dwordx4 v12, s[74:75]\n"
  "s_add_u32 m0, s79, 51200\n"
  "s_add_u32 s74, s40, 1638400\n"
  "s_addc_u32 s75, s41, 0\n"
  "global_load_lds_dwordx4 v12, s[74:75]\n"
  "s_add_u32 m0, s79, 57344\n"
  "s_add_u32 s74, s40, 1835008\n"
  "s_addc_u32 s75, s41, 0\n"
  "global_load_lds_dwordx4 v12, s[74:75]\n"
  "s_add_u32 m0, s79, 59392\n"
  "s_add_u32 s74, s40, 1900544\n"
  "s_addc_u32 s75, s41, 0\n"
  "global_load_lds_dwordx4 v12, s[74:75]\n"
  "s_add_u32 m0, s79, 65536\n"
  "s_add_u32 s74, s40, 2097152\n"
  "s_addc_u32 s75, s41, 0\n"
  "global_load_lds_dwordx4 v12, s[74:75]\n"
  "s_add_u32 m0, s79, 67584\n"
  "s_add_u32 s74, s40, 2162688\n"
  "s_addc_u32 s75, s41, 0\n"
  "global_load_lds_dwordx4 v12, s[74:75]\n"
  "s_add_u32 m0, s79, 73728\n"
  "s_add_u32 s74, s40, 2359296\n"
  "s_addc_u32 s75, s41, 0\n"
  "global_load_lds_dwordx4 v12, s[74:75]\n"
  "s_add_u32 m0, s79, 75776\n"
  "s_add_u32 s74, s40, 2424832\n"
  "s_addc_u32 s75, s41, 0\n"
  "global_load_lds_dwordx4 v12, s[74:75]\n"
  "s_add_u32 m0, s79, 81920\n"
  "s_add_u32 s74, s40, 2621440\n"
  "s_addc_u32 s75, s41, 0\n"
  "global_load_lds_dwordx4 v12, s[74:75]\n"
  "s_add_u32 m0, s79, 83968\n"
  "s_add_u32 s74, s40, 2686976\n"
  "s_addc_u32 s75, s41, 0\n"
  "global_load_lds_dwordx4 v12, s[74:75]\n"
  "s_add_u32 m0, s79, 90112\n"
  "s_add_u32 s74, s40, 2883584\n"
  "s_addc_u32 s75, s41, 0\n"
  "global_load_lds_dwordx4 v12, s[74:75]\n"
  "s_add_u32 m0, s79, 92160\n"
  "s_add_u32 s74, s40, 2949120\n"
  "s_addc_u32 s75, s41, 0\n"
  "global_load_lds_dwordx4 v12, s[74:75]\n"
  "s_add_u32 m0, s79, 98304\n"
  "s_add_u32 s74, s40, 3145728\n"
  "s_addc_u32 s75, s41, 0\n"
  "global_load_lds_dwordx4 v12, s[74:75]\n"
  "s_add_u32 m0, s79, 100352\n"
  "s_add_u32 s74, s40, 3211264\n"
  "s_addc_u32 s75, s41, 0\n"
  "global_load_lds_dwordx4 v12, s[74:75]\n"
  "s_add_u32 m0, s79, 106496\n"
  "s_add_u32 s74, s40, 3407872\n"
  "s_addc_u32 s75, s41, 0\n"
  "global_load_lds_dwordx4 v12, s[74:75]\n"
  "s_add_u32 m0, s79, 108544\n"
  "s_add_u32 s74, s40, 3473408\n"
  "s_addc_u32 s75, s41, 0\n"
  "global_load_lds_dwordx4 v12, s[74:75]\n"
  "s_add_u32 m0, s79, 114688\n"
  "s_add_u32 s74, s40, 3670016\n"
  "s_addc_u32 s75, s41, 0\n"
  "global_load_lds_dwordx4 v12, s[74:75]\n"
  "s_add_u32 m0, s79, 116736\n"
  "s_add_u32 s74, s40, 3735552\n"
  "s_addc_u32 s75, s41, 0\n"
  "global_load_lds_dwordx4 v12, s[74:75]\n"
  "s_add_u32 m0, s79, 122880\n"
  "s_add_u32 s74, s40, 3932160\n"
  "s_addc_u32 s75, s41, 0\n"
  "global_load_lds_dwordx4 v12, s[74:75]\n"
  "s_add_u32 m0, s79, 124928\n"
  "s_add_u32 s74, s40, 3997696\n"
  "s_addc_u32 s75, s41, 0\n"
  "global_load_lds_dwordx4 v12, s[74:75]\n"
  "s_waitcnt vmcnt(28) lgkmcnt(0)\n"
  "s_barrier\n"
  "s_add_u32 m0, s79, 131072\n"
  "s_add_u32 s74, s40, 4194304\n"
  "s_addc_u32 s75, s41, 0\n"
  "global_load_lds_dwordx4 v12, s[74:75]\n"
  "s_add_u32 m0, s79, 133120\n"
  "s_add_u32 s74, s40, 4259840\n"
  "s_addc_u32 s75, s41, 0\n"
  "global_load_lds_dwordx4 v12, s[74:75]\n"
  "s_add_u32 m0, s79, 139264\n"
  "s_add_u32 s74, s40, 4456448\n"
  "s_addc_u32 s75, s41, 0\n"
  "global_load_lds_dwordx4 v12, s[74:75]\n"
  "s_add_u32 m0, s79, 141312\n"
  "s_add_u32 s74, s40, 4521984\n"
  "s_addc_u32 s75, s41, 0\n"
  "global_load_lds_dwordx4 v12, s[74:75]\n"
  "v_add_u32 v20, 0, v13\n"
  "v_xor_b32 v21, 32, v20\n"
  "ds_read_b64_tr_b16 a[0:1], v20 offset:0\n"
  "ds_read_b64_tr_b16 a[2:3], v20 offset:2048\n"
  "ds_read_b64_tr_b16 a[4:5], v20 offset:8192\n"
  "ds_read_b64_tr_b16 a[6:7], v20 offset:10240\n"
  "ds_read_b64_tr_b16 a[8:9], v21 offset:0\n"
  "ds_read_b64_tr_b16 a[10:11], v21 offset:2048\n"
  "ds_read_b64_tr_b16 a[12:13], v21 offset:8192\n"
  "ds_read_b64_tr_b16 a[14:15], v21 offset:10240\n"
  "s_waitcnt vmcnt(28) lgkmcnt(0)\n"
  "s_barrier\n"
  "s_add_u32 m0, s78, 0\n"
  "s_add_u32 s74, s42, 0\n"
  "s_addc_u32 s75, s43, 0\n"
  "global_load_lds_dwordx4 v14, s[74:75]\n"
  "s_add_u32 m0, s78, 1024\n"
  "s_add_u32 s74, s42, 9216\n"
  "s_addc_u32 s75, s43, 0\n"
  "global_load_lds_dwordx4 v14, s[74:75]\n"
  "s_add_u32 m0, s78, 8192\n"
  "s_add_u32 s74, s42, 73728\n"
  "s_addc_u32 s75, s43, 0\n"
  "global_load_lds_dwordx4 v14, s[74:75]\n"
  "s_add_u32 m0, s78, 9216\n"
  "s_add_u32 s74, s42, 82944\n"
  "s_addc_u32 s75, s43, 0\n"
  "global_load_lds_dwordx4 v14, s[74:75]\n"
  "v_add_u32 v20, 16384, v13\n"
  "v_xor_b32 v21, 32, v20\n"
  "ds_read_b64_tr_b16 a[16:17], v20 offset:0\n"
  "ds_read_b64_tr_b16 a[18:19], v20 offset:2048\n"
  "ds_read_b64_tr_b16 a[20:21], v20 offset:8192\n"
  "ds_read_b64_tr_b16 a[22:23], v20 offset:10240\n"
  "ds_read_b64_tr_b16 a[24:25], v21 offset:0\n"
  "ds_read_b64_tr_b16 a[26:27], v21 offset:2048\n"
  "ds_read_b64_tr_b16 a[28:29], v21 offset:8192\n"
  "ds_read_b64_tr_b16 a[30:31], v21 offset:10240\n"
  "s_waitcnt vmcnt(28) lgkmcnt(0)\n"
  "s_barrier\n"
  "s_add_u32 m0, s78, 16384\n"
  "s_add_u32 s74, s42, 128\n"
  "s_addc_u32 s75, s43, 0\n"
  "global_load_lds_dwordx4 v14, s[74:75]\n"
  "s_add_u32 m0, s78, 17408\n"
  "s_add_u32 s74, s42, 9344\n"
  "s_addc_u32 s75, s43, 0\n"
  "global_load_lds_dwordx4 v14, s[74:75]\n"
  "s_add_u32 m0, s78, 24576\n"
  "s_add_u32 s74, s42, 73856\n"
  "s_addc_u32 s75, s43, 0\n"
  "global_load_lds_dwordx4 v14, s[74:75]\n"
  "s_add_u32 m0, s78, 25600\n"
  "s_add_u32 s74, s42, 83072\n"
  "s_addc_u32 s75, s43, 0\n"
  "global_load_lds_dwordx4 v14, s[74:75]\n"
  "v_add_u32 v20, 32768, v13\n"
  "v_xor_b32 v21, 32, v20\n"
  "ds_read_b64_tr_b16 a[32:33], v20 offset:0\n"
  "ds_read_b64_tr_b16 a[34:35], v20 offset:2048\n"
  "ds_read_b64_tr_b16 a[36:37], v20 offset:8192\n"
  "ds_read_b64_tr_b16 a[38:39], v20 offset:10240\n"
  "ds_read_b64_tr_b16 a[40:41], v21 offset:0\n"
  "ds_read_b64_tr_b16 a[42:43], v21 offset:2048\n"
  "ds_read_b64_tr_b16 a[44:45], v21 offset:8192\n"
  "ds_read_b64_tr_b16 a[46:47], v21 offset:10240\n"
  "s_waitcnt vmcnt(28) lgkmcnt(0)\n"
  "s_barrier\n"
  "s_add_u32 m0, s78, 32768\n"
  "s_add_u32 s74, s42, 256\n"
  "s_addc_u32 s75, s43, 0\n"
  "global_load_lds_dwordx4 v14, s[74:75]\n"
  "s_add_u32 m0, s78, 33792\n"
  "s_add_u32 s74, s42, 9472\n"
  "s_addc_u32 s75, s43, 0\n"
  "global_load_lds_dwordx4 v14, s[74:75]\n"
  "s_add_u32 m0, s78, 40960\n"
  "s_add_u32 s74, s42, 73984\n"
  "s_addc_u32 s75, s43, 0\n"
  "global_load_lds_dwordx4 v14, s[74:75]\n"
  "s_add_u32 m0, s78, 41984\n"
  "s_add_u32 s74, s42, 83200\n"
  "s_addc_u32 s75, s43, 0\n"
  "global_load_lds_dwordx4 v14, s[74:75]\n"
  "v_add_u32 v20, 49152, v13\n"
  "v_xor_b32 v21, 32, v20\n"
  "ds_read_b64_tr_b16 a[48:49], v20 offset:0\n"
  "ds_read_b64_tr_b16 a[50:51], v20 offset:2048\n"
  "ds_read_b64_tr_b16 a[52:53], v20 offset:8192\n"
  "ds_read_b64_tr_b16 a[54:55], v20 offset:10240\n"
  "ds_read_b64_tr_b16 a[56:57], v21 offset:0\n"
  "ds_read_b64_tr_b16 a[58:59], v21 offset:2048\n"
  "ds_read_b64_tr_b16 a[60:61], v21 offset:8192\n"
  "ds_read_b64_tr_b16 a[62:63], v21 offset:10240\n"
  "s_waitcnt vmcnt(28) lgkmcnt(0)\n"
  "s_barrier\n"
  "s_add_u32 m0, s78, 49152\n"
  "s_add_u32 s74, s42, 384\n"
  "s_addc_u32 s75, s43, 0\n"
  "global_load_lds_dwordx4 v14, s[74:75]\n"
  "s_add_u32 m0, s78, 50176\n"
  "s_add_u32 s74, s42, 9600\n"
  "s_addc_u32 s75, s43, 0\n"
  "global_load_lds_dwordx4 v14, s[74:75]\n"
  "s_add_u32 m0, s78, 57344\n"
  "s_add_u32 s74, s42, 74112\n"
  "s_addc_u32 s75, s43, 0\n"
  "global_load_lds_dwordx4 v14, s[74:75]\n"
  "s_add_u32 m0, s78, 58368\n"
  "s_add_u32 s74, s42, 83328\n"
  "s_addc_u32 s75, s43, 0\n"
  "global_load_lds_dwordx4 v14, s[74:75]\n"
  "v_add_u32 v20, 65536, v13\n"
  "v_xor_b32 v21, 32, v20\n"
  "ds_read_b64_tr_b16 a[64:65], v20 offset:0\n"
  "ds_read_b64_tr_b16 a[66:67], v20 offset:2048\n"
  "ds_read_b64_tr_b16 a[68:69], v20 offset:8192\n"
  "ds_read_b64_tr_b16 a[70:71], v20 offset:10240\n"
  "ds_read_b64_tr_b16 a[72:73], v21 offset:0\n"
  "ds_read_b64_tr_b16 a[74:75], v21 offset:2048\n"
  "ds_read_b64_tr_b16 a[76:77], v21 offset:8192\n"
  "ds_read_b64_tr_b16 a[78:79], v21 offset:10240\n"
  "s_waitcnt vmcnt(28) lgkmcnt(0)\n"
  "s_barrier\n"
  "s_add_u32 m0, s78, 65536\n"
  "s_add_u32 s74, s42, 0\n"
  "s_addc_u32 s75, s43, 0\n"
  "s_add_u32 s74, s74, s52\n"
  "s_addc_u32 s75, s75, 0\n"
  "global_load_lds_dwordx4 v14, s[74:75]\n"
  "s_add_u32 m0, s78, 66560\n"
  "s_add_u32 s74, s42, 9216\n"
  "s_addc_u32 s75, s43, 0\n"
  "s_add_u32 s74, s74, s52\n"
  "s_addc_u32 s75, s75, 0\n"
  "global_load_lds_dwordx4 v14, s[74:75]\n"
  "s_add_u32 m0, s78, 73728\n"
  "s_add_u32 s74, s42, 73728\n"
  "s_addc_u32 s75, s43, 0\n"
  "s_add_u32 s74, s74, s52\n"
  "s_addc_u32 s75, s75, 0\n"
  "global_load_lds_dwordx4 v14, s[74:75]\n"
  "s_add_u32 m0, s78, 74752\n"
  "s_add_u32 s74, s42, 82944\n"
  "s_addc_u32 s75, s43, 0\n"
  "s_add_u32 s74, s74, s52\n"
  "s_addc_u32 s75, s75, 0\n"
  "global_load_lds_dwordx4 v14, s[74:75]\n"
  "v_add_u32 v20, 81920, v13\n"
  "v_xor_b32 v21, 32, v20\n"
  "ds_read_b64_tr_b16 a[80:81], v20 offset:0\n"
  "ds_read_b64_tr_b16 a[82:83], v20 offset:2048\n"
  "ds_read_b64_tr_b16 a[84:85], v20 offset:8192\n"
  "ds_read_b64_tr_b16 a[86:87], v20 offset:10240\n"
  "ds_read_b64_tr_b16 a[88:89], v21 offset:0\n"
  "ds_read_b64_tr_b16 a[90:91], v21 offset:2048\n"
  "ds_read_b64_tr_b16 a[92:93], v21 offset:8192\n"
  "ds_read_b64_tr_b16 a[94:95], v21 offset:10240\n"
  "s_waitcnt vmcnt(28) lgkmcnt(0)\n"
  "s_barrier\n"
  "s_add_u32 m0, s79, 81920\n"
  "s_add_u32 s74, s38, 0\n"
  "s_addc_u32 s75, s39, 0\n"
  "global_load_lds_dwordx4 v12, s[74:75]\n"
  "s_add_u32 m0, s79, 83968\n"
  "s_add_u32 s74, s38, 65536\n"
  "s_addc_u32 s75, s39, 0\n"
  "global_load_lds_dwordx4 v12, s[74:75]\n"
  "s_add_u32 m0, s79, 90112\n"
  "s_add_u32 s74, s38, 262144\n"
  "s_addc_u32 s75, s39, 0\n"
  "global_load_lds_dwordx4 v12, s[74:75]\n"
  "s_add_u32 m0, s79, 92160\n"
  "s_add_u32 s74, s38, 327680\n"
  "s_addc_u32 s75, s39, 0\n"
  "global_load_lds_dwordx4 v12, s[74:75]\n"
  "v_add_u32 v20, 98304, v13\n"
  "v_xor_b32 v21, 32, v20\n"
  "ds_read_b64_tr_b16 a[96:97], v20 offset:0\n"
  "ds_read_b64_tr_b16 a[98:99], v20 offset:2048\n"
  "ds_read_b64_tr_b16 a[100:101], v20 offset:8192\n"
  "ds_read_b64_tr_b16 a[102:103], v20 offset:10240\n"
  "ds_read_b64_tr_b16 a[104:105], v21 offset:0\n"
  "ds_read_b64_tr_b16 a[106:107], v21 offset:2048\n"
  "ds_read_b64_tr_b16 a[108:109], v21 offset:8192\n"
  "ds_read_b64_tr_b16 a[110:111], v21 offset:10240\n"
  "s_waitcnt vmcnt(28) lgkmcnt(0)\n"
  "s_barrier\n"
  "s_add_u32 m0, s79, 98304\n"
  "s_add_u32 s74, s38, 524288\n"
  "s_addc_u32 s75, s39, 0\n"
  "global_load_lds_dwordx4 v12, s[74:75]\n"
  "s_add_u32 m0, s79, 100352\n"
  "s_add_u32 s74, s38, 589824\n"
  "s_addc_u32 s75, s39, 0\n"
  "global_load_lds_dwordx4 v12, s[74:75]\n"
  "s_add_u32 m0, s79, 106496\n"
  "s_add_u32 s74, s38, 786432\n"
  "s_addc_u32 s75, s39, 0\n"
  "global_load_lds_dwordx4 v12, s[74:75]\n"
  "s_add_u32 m0, s79, 108544\n"
  "s_add_u32 s74, s38, 851968\n"
  "s_addc_u32 s75, s39, 0\n"
  "global_load_lds_dwordx4 v12, s[74:75]\n"
  "v_add_u32 v20, 114688, v13\n"
  "v_xor_b32 v21, 32, v20\n"
  "ds_read_b64_tr_b16 a[112:113], v20 offset:0\n"
  "ds_read_b64_tr_b16 a[114:115], v20 offset:2048\n"
  "ds_read_b64_tr_b16 a[116:117], v20 offset:8192\n"
  "ds_read_b64_tr_b16 a[118:119], v20 offset:10240\n"
  "ds_read_b64_tr_b16 a[120:121], v21 offset:0\n"
  "ds_read_b64_tr_b16 a[122:123], v21 offset:2048\n"
  "ds_read_b64_tr_b16 a[124:125], v21 offset:8192\n"
  "ds_read_b64_tr_b16 a[126:127], v21 offset:10240\n"
  "s_waitcnt vmcnt(28) lgkmcnt(0)\n"
  "s_barrier\n"
  "s_add_u32 m0, s79, 114688\n"
  "s_add_u32 s74, s38, 1048576\n"
  "s_addc_u32 s75, s39, 0\n"
  "global_load_lds_dwordx4 v12, s[74:75]\n"
  "s_add_u32 m0, s79, 116736\n"
  "s_add_u32 s74, s38, 1114112\n"
  "s_addc_u32 s75, s39, 0\n"
  "global_load_lds_dwordx4 v12, s[74:75]\n"
  "s_add_u32 m0, s79, 122880\n"
  "s_add_u32 s74, s38, 1310720\n"
  "s_addc_u32 s75, s39, 0\n"
  "global_load_lds_dwordx4 v12, s[74:75]\n"
  "s_add_u32 m0, s79, 124928\n"
  "s_add_u32 s74, s38, 1376256\n"
  "s_addc_u32 s75, s39, 0\n"
  "global_load_lds_dwordx4 v12, s[74:75]\n"
  "v_add_u32 v20, 131072, v13\n"
  "v_xor_b32 v21, 32, v20\n"
  "ds_read_b64_tr_b16 a[128:129], v20 offset:0\n"
  "ds_read_b64_tr_b16 a[130:131], v20 offset:2048\n"
  "ds_read_b64_tr_b16 a[132:133], v20 offset:8192\n"
  "ds_read_b64_tr_b16 a[134:135], v20 offset:10240\n"
  "ds_read_b64_tr_b16 a[136:137], v21 offset:0\n"
  "ds_read_b64_tr_b16 a[138:139], v21 offset:2048\n"
  "ds_read_b64_tr_b16 a[140:141], v21 offset:8192\n"
  "ds_read_b64_tr_b16 a[142:143], v21 offset:10240\n"
  "s_waitcnt vmcnt(28) lgkmcnt(0)\n"
  "s_barrier\n"
  "s_add_u32 m0, s79, 131072\n"
  "s_add_u32 s74, s38, 1572864\n"
  "s_addc_u32 s75, s39, 0\n"
  "global_load_lds_dwordx4 v12, s[74:75]\n"
  "s_add_u32 m0, s79, 133120\n"
  "s_add_u32 s74, s38, 1638400\n"
  "s_addc_u32 s75, s39, 0\n"
  "global_load_lds_dwordx4 v12, s[74:75]\n"
  "s_add_u32 m0, s79, 139264\n"
  "s_add_u32 s74, s38, 1835008\n"
  "s_addc_u32 s75, s39, 0\n"
  "global_load_lds_dwordx4 v12, s[74:75]\n"
  "s_add_u32 m0, s79, 141312\n"
  "s_add_u32 s74, s38, 1900544\n"
  "s_addc_u32 s75, s39, 0\n"
  "global_load_lds_dwordx4 v12, s[74:75]\n"
  "s_cmp_lg_u32 s48, 0\n"
  "s_cbranch_scc1 HLS9\n"
  "s_xor_b32 s74, s56, 0\n"
  "v_xor_b32 v20, s74, v15\n"
  "v_add_u32 v20, 0, v20\n"
  "ds_read_b64_tr_b16 a[144:145], v20 offset:0\n"
  "ds_read_b64_tr_b16 a[146:147], v20 offset:256\n"
  "ds_read_b64_tr_b16 a[148:149], v20 offset:1024\n"
  "ds_read_b64_tr_b16 a[150:151], v20 offset:1280\n"
  "ds_read_b64_tr_b16 a[152:153], v20 offset:8192\n"
  "ds_read_b64_tr_b16 a[154:155], v20 offset:8448\n"
  "ds_read_b64_tr_b16 a[156:157], v20 offset:9216\n"
  "ds_read_b64_tr_b16 a[158:159], v20 offset:9472\n"
  "s_xor_b32 s74, s56, 32\n"
  "v_xor_b32 v20, s74, v15\n"
  "v_add_u32 v20, 0, v20\n"
  "ds_read_b64_tr_b16 a[160:161], v20 offset:0\n"
  "ds_read_b64_tr_b16 a[162:163], v20 offset:256\n"
  "ds_read_b64_tr_b16 a[164:165], v20 offset:1024\n"
  "ds_read_b64_tr_b16 a[166:167], v20 offset:1280\n"
  "ds_read_b64_tr_b16 a[168:169], v20 offset:8192\n"
  "ds_read_b64_tr_b16 a[170:171], v20 offset:8448\n"
  "ds_read_b64_tr_b16 a[172:173], v20 offset:9216\n"
  "ds_read_b64_tr_b16 a[174:175], v20 offset:9472\n"
  "s_xor_b32 s74, s56, 64\n"
  "v_xor_b32 v20, s74, v15\n"
  "v_add_u32 v20, 0, v20\n"
  "ds_read_b64_tr_b16 a[176:177], v20 offset:0\n"
  "ds_read_b64_tr_b16 a[178:179], v20 offset:256\n"
  "ds_read_b64_tr_b16 a[180:181], v20 offset:1024\n"
  "ds_read_b64_tr_b16 a[182:183], v20 offset:1280\n"
  "ds_read_b64_tr_b16 a[184:185], v20 offset:8192\n"
  "ds_read_b64_tr_b16 a[186:187], v20 offset:8448\n"
  "ds_read_b64_tr_b16 a[188:189], v20 offset:9216\n"
  "ds_read_b64_tr_b16 a[190:191], v20 offset:9472\n"
  "s_xor_b32 s74, s56, 96\n"
  "v_xor_b32 v20, s74, v15\n"
  "v_add_u32 v20, 0, v20\n"
  "ds_read_b64_tr_b16 a[192:193], v20 offset:0\n"
  "ds_read_b64_tr_b16 a[194:195], v20 offset:256\n"
  "ds_read_b64_tr_b16 a[196:197], v20 offset:1024\n"
  "ds_read_b64_tr_b16 a[198:199], v20 offset:1280\n"
  "ds_read_b64_tr_b16 a[200:201], v20 offset:8192\n"
  "ds_read_b64_tr_b16 a[202:203], v20 offset:8448\n"
  "ds_read_b64_tr_b16 a[204:205], v20 offset:9216\n"
  "ds_read_b64_tr_b16 a[206:207], v20 offset:9472\n"
  "HLS9:\n"
  "s_waitcnt vmcnt(28) lgkmcnt(0)\n"
  "s_barrier\n"
  "s_add_u32 m0, s79, 0\n"
  "s_add_u32 s74, s38, 2097152\n"
  "s_addc_u32 s75, s39, 0\n"
  "global_load_lds_dwordx4 v12, s[74:75]\n"
  "s_add_u32 m0, s79, 2048\n"
  "s_add_u32 s74, s38, 2162688\n"
  "s_addc_u32 s75, s39, 0\n"
  "global_load_lds_dwordx4 v12, s[74:75]\n"
  "s_add_u32 m0, s79, 8192\n"
  "s_add_u32 s74, s38, 2359296\n"
  "s_addc_u32 s75, s39, 0\n"
  "global_load_lds_dwordx4 v12, s[74:75]\n"
  "s_add_u32 m0, s79, 10240\n"
  "s_add_u32 s74, s38, 2424832\n"
  "s_addc_u32 s75, s39, 0\n"
  "global_load_lds_dwordx4 v12, s[74:75]\n"
  "s_cmp_lg_u32 s48, 1\n"
  "s_cbranch_scc1 HLS10\n"
  "s_xor_b32 s74, s56, 0\n"
  "v_xor_b32 v20, s74, v15\n"
  "v_add_u32 v20, 16384, v20\n"
  "ds_read_b64_tr_b16 a[144:145], v20 offset:0\n"
  "ds_read_b64_tr_b16 a[146:147], v20 offset:256\n"
  "ds_read_b64_tr_b16 a[148:149], v20 offset:1024\n"
  "ds_read_b64_tr_b16 a[150:151], v20 offset:1280\n"
  "ds_read_b64_tr_b16 a[152:153], v20 offset:8192\n"
  "ds_read_b64_tr_b16 a[154:155], v20 offset:8448\n"
  "ds_read_b64_tr_b16 a[156:157], v20 offset:9216\n"
  "ds_read_b64_tr_b16 a[158:159], v20 offset:9472\n"
  "s_xor_b32 s74, s56, 32\n"
  "v_xor_b32 v20, s74, v15\n"
  "v_add_u32 v20, 16384, v20\n"
  "ds_read_b64_tr_b16 a[160:161], v20 offset:0\n"
  "ds_read_b64_tr_b16 a[162:163], v20 offset:256\n"
  "ds_read_b64_tr_b16 a[164:165], v20 offset:1024\n"
  "ds_read_b64_tr_b16 a[166:167], v20 offset:1280\n"
  "ds_read_b64_tr_b16 a[168:169], v20 offset:8192\n"
  "ds_read_b64_tr_b16 a[170:171], v20 offset:8448\n"
  "ds_read_b64_tr_b16 a[172:173], v20 offset:9216\n"
  "ds_read_b64_tr_b16 a[174:175], v20 offset:9472\n"
  "s_xor_b32 s74, s56, 64\n"
  "v_xor_b32 v20, s74, v15\n"
  "v_add_u32 v20, 16384, v20\n"
  "ds_read_b64_tr_b16 a[176:177], v20 offset:0\n"
  "ds_read_b64_tr_b16 a[178:179], v20 offset:256\n"
  "ds_read_b64_tr_b16 a[180:181], v20 offset:1024\n"
  "ds_read_b64_tr_b16 a[182:183], v20 offset:1280\n"
  "ds_read_b64_tr_b16 a[184:185], v20 offset:8192\n"
  "ds_read_b64_tr_b16 a[186:187], v20 offset:8448\n"
  "ds_read_b64_tr_b16 a[188:189], v20 offset:9216\n"
  "ds_read_b64_tr_b16 a[190:191], v20 offset:9472\n"
  "s_xor_b32 s74, s56, 96\n"
  "v_xor_b32 v20, s74, v15\n"
  "v_add_u32 v20, 16384, v20\n"
  "ds_read_b64_tr_b16 a[192:193], v20 offset:0\n"
  "ds_read_b64_tr_b16 a[194:195], v20 offset:256\n"
  "ds_read_b64_tr_b16 a[196:197], v20 offset:1024\n"
  "ds_read_b64_tr_b16 a[198:199], v20 offset:1280\n"
  "ds_read_b64_tr_b16 a[200:201], v20 offset:8192\n"
  "ds_read_b64_tr_b16 a[202:203], v20 offset:8448\n"
  "ds_read_b64_tr_b16 a[204:205], v20 offset:9216\n"
  "ds_read_b64_tr_b16 a[206:207], v20 offset:9472\n"
  "HLS10:\n"
  "s_waitcnt vmcnt(28) lgkmcnt(0)\n"
  "s_barrier\n"
  "s_add_u32 m0, s79, 16384\n"
  "s_add_u32 s74, s38, 2621440\n"
  "s_addc_u32 s75, s39, 0\n"
  "global_load_lds_dwordx4 v12, s[74:75]\n"
  "s_add_u32 m0, s79, 18432\n"
  "s_add_u32 s74, s38, 2686976\n"
  "s_addc_u32 s75, s39, 0\n"
  "global_load_lds_dwordx4 v12, s[74:75]\n"
  "s_add_u32 m0, s79, 24576\n"
  "s_add_u32 s74, s38, 2883584\n"
  "s_addc_u32 s75, s39, 0\n"
  "global_load_lds_dwordx4 v12, s[74:75]\n"
  "s_add_u32 m0, s79, 26624\n"
  "s_add_u32 s74, s38, 2949120\n"
  "s_addc_u32 s75, s39, 0\n"
  "global_load_lds_dwordx4 v12, s[74:75]\n"
  "s_cmp_lg_u32 s48, 2\n"
  "s_cbranch_scc1 HLS11\n"
  "s_xor_b32 s74, s56, 0\n"
  "v_xor_b32 v20, s74, v15\n"
  "v_add_u32 v20, 32768, v20\n"
  "ds_read_b64_tr_b16 a[144:145], v20 offset:0\n"
  "ds_read_b64_tr_b16 a[146:147], v20 offset:256\n"
  "ds_read_b64_tr_b16 a[148:149], v20 offset:1024\n"
  "ds_read_b64_tr_b16 a[150:151], v20 offset:1280\n"
  "ds_read_b64_tr_b16 a[152:153], v20 offset:8192\n"
  "ds_read_b64_tr_b16 a[154:155], v20 offset:8448\n"
  "ds_read_b64_tr_b16 a[156:157], v20 offset:9216\n"
  "ds_read_b64_tr_b16 a[158:159], v20 offset:9472\n"
  "s_xor_b32 s74, s56, 32\n"
  "v_xor_b32 v20, s74, v15\n"
  "v_add_u32 v20, 32768, v20\n"
  "ds_read_b64_tr_b16 a[160:161], v20 offset:0\n"
  "ds_read_b64_tr_b16 a[162:163], v20 offset:256\n"
  "ds_read_b64_tr_b16 a[164:165], v20 offset:1024\n"
  "ds_read_b64_tr_b16 a[166:167], v20 offset:1280\n"
  "ds_read_b64_tr_b16 a[168:169], v20 offset:8192\n"
  "ds_read_b64_tr_b16 a[170:171], v20 offset:8448\n"
  "ds_read_b64_tr_b16 a[172:173], v20 offset:9216\n"
  "ds_read_b64_tr_b16 a[174:175], v20 offset:9472\n"
  "s_xor_b32 s74, s56, 64\n"
  "v_xor_b32 v20, s74, v15\n"
  "v_add_u32 v20, 32768, v20\n"
  "ds_read_b64_tr_b16 a[176:177], v20 offset:0\n"
  "ds_read_b64_tr_b16 a[178:179], v20 offset:256\n"
  "ds_read_b64_tr_b16 a[180:181], v20 offset:1024\n"
  "ds_read_b64_tr_b16 a[182:183], v20 offset:1280\n"
  "ds_read_b64_tr_b16 a[184:185], v20 offset:8192\n"
  "ds_read_b64_tr_b16 a[186:187], v20 offset:8448\n"
  "ds_read_b64_tr_b16 a[188:189], v20 offset:9216\n"
  "ds_read_b64_tr_b16 a[190:191], v20 offset:9472\n"
  "s_xor_b32 s74, s56, 96\n"
  "v_xor_b32 v20, s74, v15\n"
  "v_add_u32 v20, 32768, v20\n"
  "ds_read_b64_tr_b16 a[192:193], v20 offset:0\n"
  "ds_read_b64_tr_b16 a[194:195], v20 offset:256\n"
  "ds_read_b64_tr_b16 a[196:197], v20 offset:1024\n"
  "ds_read_b64_tr_b16 a[198:199], v20 offset:1280\n"
  "ds_read_b64_tr_b16 a[200:201], v20 offset:8192\n"
  "ds_read_b64_tr_b16 a[202:203], v20 offset:8448\n"
  "ds_read_b64_tr_b16 a[204:205], v20 offset:9216\n"
  "ds_read_b64_tr_b16 a[206:207], v20 offset:9472\n"
  "HLS11:\n"
  "s_waitcnt vmcnt(28) lgkmcnt(0)\n"
  "s_barrier\n"
  "s_add_u32 m0, s79, 32768\n"
  "s_add_u32 s74, s38, 3145728\n"
  "s_addc_u32 s75, s39, 0\n"
  "global_load_lds_dwordx4 v12, s[74:75]\n"
  "s_add_u32 m0, s79, 34816\n"
  "s_add_u32 s74, s38, 3211264\n"
  "s_addc_u32 s75, s39, 0\n"
  "global_load_lds_dwordx4 v12, s[74:75]\n"
  "s_add_u32 m0, s79, 40960\n"
  "s_add_u32 s74, s38, 3407872\n"
  "s_addc_u32 s75, s39, 0\n"
  "global_load_lds_dwordx4 v12, s[74:75]\n"
  "s_add_u32 m0, s79, 43008\n"
  "s_add_u32 s74, s38, 3473408\n"
  "s_addc_u32 s75, s39, 0\n"
  "global_load_lds_dwordx4 v12, s[74:75]\n"
  "s_cmp_lg_u32 s48, 3\n"
  "s_cbranch_scc1 HLS12\n"
  "s_xor_b32 s74, s56, 0\n"
  "v_xor_b32 v20, s74, v15\n"
  "v_add_u32 v20, 49152, v20\n"
  "ds_read_b64_tr_b16 a[144:145], v20 offset:0\n"
  "ds_read_b64_tr_b16 a[146:147], v20 offset:256\n"
  "ds_read_b64_tr_b16 a[148:149], v20 offset:1024\n"
  "ds_read_b64_tr_b16 a[150:151], v20 offset:1280\n"
  "ds_read_b64_tr_b16 a[152:153], v20 offset:8192\n"
  "ds_read_b64_tr_b16 a[154:155], v20 offset:8448\n"
  "ds_read_b64_tr_b16 a[156:157], v20 offset:9216\n"
  "ds_read_b64_tr_b16 a[158:159], v20 offset:9472\n"
  "s_xor_b32 s74, s56, 32\n"
  "v_xor_b32 v20, s74, v15\n"
  "v_add_u32 v20, 49152, v20\n"
  "ds_read_b64_tr_b16 a[160:161], v20 offset:0\n"
  "ds_read_b64_tr_b16 a[162:163], v20 offset:256\n"
  "ds_read_b64_tr_b16 a[164:165], v20 offset:1024\n"
  "ds_read_b64_tr_b16 a[166:167], v20 offset:1280\n"
  "ds_read_b64_tr_b16 a[168:169], v20 offset:8192\n"
  "ds_read_b64_tr_b16 a[170:171], v20 offset:8448\n"
  "ds_read_b64_tr_b16 a[172:173], v20 offset:9216\n"
  "ds_read_b64_tr_b16 a[174:175], v20 offset:9472\n"
  "s_xor_b32 s74, s56, 64\n"
  "v_xor_b32 v20, s74, v15\n"
  "v_add_u32 v20, 49152, v20\n"
  "ds_read_b64_tr_b16 a[176:177], v20 offset:0\n"
  "ds_read_b64_tr_b16 a[178:179], v20 offset:256\n"
  "ds_read_b64_tr_b16 a[180:181], v20 offset:1024\n"
  "ds_read_b64_tr_b16 a[182:183], v20 offset:1280\n"
  "ds_read_b64_tr_b16 a[184:185], v20 offset:8192\n"
  "ds_read_b64_tr_b16 a[186:187], v20 offset:8448\n"
  "ds_read_b64_tr_b16 a[188:189], v20 offset:9216\n"
  "ds_read_b64_tr_b16 a[190:191], v20 offset:9472\n"
  "s_xor_b32 s74, s56, 96\n"
  "v_xor_b32 v20, s74, v15\n"
  "v_add_u32 v20, 49152, v20\n"
  "ds_read_b64_tr_b16 a[192:193], v20 offset:0\n"
  "ds_read_b64_tr_b16 a[194:195], v20 offset:256\n"
  "ds_read_b64_tr_b16 a[196:197], v20 offset:1024\n"
  "ds_read_b64_tr_b16 a[198:199], v20 offset:1280\n"
  "ds_read_b64_tr_b16 a[200:201], v20 offset:8192\n"
  "ds_read_b64_tr_b16 a[202:203], v20 offset:8448\n"
  "ds_read_b64_tr_b16 a[204:205], v20 offset:9216\n"
  "ds_read_b64_tr_b16 a[206:207], v20 offset:9472\n"
  "HLS12:\n"
  "s_waitcnt vmcnt(28) lgkmcnt(0)\n"
  "s_barrier\n"
  "s_add_u32 m0, s79, 49152\n"
  "s_add_u32 s74, s38, 3670016\n"
  "s_addc_u32 s75, s39, 0\n"
  "global_load_lds_dwordx4 v12, s[74:75]\n"
  "s_add_u32 m0, s79, 51200\n"
  "s_add_u32 s74, s38, 3735552\n"
  "s_addc_u32 s75, s39, 0\n"
  "global_load_lds_dwordx4 v12, s[74:75]\n"
  "s_add_u32 m0, s79, 57344\n"
  "s_add_u32 s74, s38, 3932160\n"
  "s_addc_u32 s75, s39, 0\n"
  "global_load_lds_dwordx4 v12, s[74:75]\n"
  "s_add_u32 m0, s79, 59392\n"
  "s_add_u32 s74, s38, 3997696\n"
  "s_addc_u32 s75, s39, 0\n"
  "global_load_lds_dwordx4 v12, s[74:75]\n"
  "s_lshr_b32 s74, s52, 3\n"
  "s_and_b32 s74, s74, 64\n"
  "s_and_b32 s75, s48, 1\n"
  "s_lshl_b32 s75, s75, 5\n"
  "s_add_u32 s74, s74, s75\n"
  "v_xor_b32 v20, s74, v15\n"
  "v_add_u32 v20, 65536, v20\n"
  "ds_read_b64_tr_b16 a[208:209], v20 offset:0\n"
  "ds_read_b64_tr_b16 a[210:211], v20 offset:256\n"
  "ds_read_b64_tr_b16 a[212:213], v20 offset:1024\n"
  "ds_read_b64_tr_b16 a[214:215], v20 offset:1280\n"
  "ds_read_b64_tr_b16 a[216:217], v20 offset:8192\n"
  "ds_read_b64_tr_b16 a[218:219], v20 offset:8448\n"
  "ds_read_b64_tr_b16 a[220:221], v20 offset:9216\n"
  "ds_read_b64_tr_b16 a[222:223], v20 offset:9472\n"
  "s_waitcnt vmcnt(28) lgkmcnt(0)\n"
  "s_barrier\n"
  "s_add_u32 m0, s79, 65536\n"
  "s_add_u32 s74, s38, 4194304\n"
  "s_addc_u32 s75, s39, 0\n"
  "global_load_lds_dwordx4 v12, s[74:75]\n"
  "s_add_u32 m0, s79, 67584\n"
  "s_add_u32 s74, s38, 4259840\n"
  "s_addc_u32 s75, s39, 0\n"
  "global_load_lds_dwordx4 v12, s[74:75]\n"
  "s_add_u32 m0, s79, 73728\n"
  "s_add_u32 s74, s38, 4456448\n"
  "s_addc_u32 s75, s39, 0\n"
  "global_load_lds_dwordx4 v12, s[74:75]\n"
  "s_add_u32 m0, s79, 75776\n"
  "s_add_u32 s74, s38, 4521984\n"
  "s_addc_u32 s75, s39, 0\n"
  "global_load_lds_dwordx4 v12, s[74:75]\n"
  "v_add_u32 v20, 81920, v13\n"
  "v_xor_b32 v21, 32, v20\n"
  "ds_read_b64_tr_b16 v[112:113], v20 offset:0\n"
  "ds_read_b64_tr_b16 v[114:115], v20 offset:2048\n"
  "ds_read_b64_tr_b16 v[116:117], v20 offset:8192\n"
  "ds_read_b64_tr_b16 v[118:119], v20 offset:10240\n"
  "ds_read_b64_tr_b16 v[120:121], v21 offset:0\n"
  "ds_read_b64_tr_b16 v[122:123], v21 offset:2048\n"
  "ds_read_b64_tr_b16 v[124:125], v21 offset:8192\n"
  "ds_read_b64_tr_b16 v[126:127], v21 offset:10240\n"
  "s_waitcnt vmcnt(28) lgkmcnt(0)\n"
  "s_barrier\n"
  "v_add_u32 v20, 98304, v13\n"
  "v_xor_b32 v21, 32, v20\n"
  "ds_read_b64_tr_b16 v[128:129], v20 offset:0\n"
  "ds_read_b64_tr_b16 v[130:131], v20 offset:2048\n"
  "ds_read_b64_tr_b16 v[132:133], v20 offset:8192\n"
  "ds_read_b64_tr_b16 v[134:135], v20 offset:10240\n"
  "ds_read_b64_tr_b16 v[136:137], v21 offset:0\n"
  "ds_read_b64_tr_b16 v[138:139], v21 offset:2048\n"
  "ds_read_b64_tr_b16 v[140:141], v21 offset:8192\n"
  "ds_read_b64_tr_b16 v[142:143], v21 offset:10240\n"
  "s_waitcnt vmcnt(24) lgkmcnt(0)\n"
  "s_barrier\n"
  "v_add_u32 v20, 114688, v13\n"
  "v_xor_b32 v21, 32, v20\n"
  "ds_read_b64_tr_b16 v[144:145], v20 offset:0\n"
  "ds_read_b64_tr_b16 v[146:147], v20 offset:2048\n"
  "ds_read_b64_tr_b16 v[148:149], v20 offset:8192\n"
  "ds_read_b64_tr_b16 v[150:151], v20 offset:10240\n"
  "ds_read_b64_tr_b16 v[152:153], v21 offset:0\n"
  "ds_read_b64_tr_b16 v[154:155], v21 offset:2048\n"
  "ds_read_b64_tr_b16 v[156:157], v21 offset:8192\n"
  "ds_read_b64_tr_b16 v[158:159], v21 offset:10240\n"
  "s_waitcnt vmcnt(20) lgkmcnt(0)\n"
  "s_barrier\n"
  "v_add_u32 v20, 131072, v13\n"
  "v_xor_b32 v21, 32, v20\n"
  "ds_read_b64_tr_b16 v[160:161], v20 offset:0\n"
  "ds_read_b64_tr_b16 v[162:163], v20 offset:2048\n"
  "ds_read_b64_tr_b16 v[164:165], v20 offset:8192\n"
  "ds_read_b64_tr_b16 v[166:167], v20 offset:10240\n"
  "ds_read_b64_tr_b16 v[168:169], v21 offset:0\n"
  "ds_read_b64_tr_b16 v[170:171], v21 offset:2048\n"
  "ds_read_b64_tr_b16 v[172:173], v21 offset:8192\n"
  "ds_read_b64_tr_b16 v[174:175], v21 offset:10240\n"
  "s_waitcnt vmcnt(16) lgkmcnt(0)\n"
  "s_barrier\n"
  "v_add_u32 v20, 0, v13\n"
  "v_xor_b32 v21, 32, v20\n"
  "ds_read_b64_tr_b16 v[176:177], v20 offset:0\n"
  "ds_read_b64_tr_b16 v[178:179], v20 offset:2048\n"
  "ds_read_b64_tr_b16 v[180:181], v20 offset:8192\n"
  "ds_read_b64_tr_b16 v[182:183], v20 offset:10240\n"
  "ds_read_b64_tr_b16 v[184:185], v21 offset:0\n"
  "ds_read_b64_tr_b16 v[186:187], v21 offset:2048\n"
  "ds_read_b64_tr_b16 v[188:189], v21 offset:8192\n"
  "ds_read_b64_tr_b16 v[190:191], v21 offset:10240\n"
  "s_waitcnt vmcnt(12) lgkmcnt(0)\n"
  "s_barrier\n"
  "v_add_u32 v20, 16384, v13\n"
  "v_xor_b32 v21, 32, v20\n"
  "ds_read_b64_tr_b16 v[192:193], v20 offset:0\n"
  "ds_read_b64_tr_b16 v[194:195], v20 offset:2048\n"
  "ds_read_b64_tr_b16 v[196:197], v20 offset:8192\n"
  "ds_read_b64_tr_b16 v[198:199], v20 offset:10240\n"
  "ds_read_b64_tr_b16 v[200:201], v21 offset:0\n"
  "ds_read_b64_tr_b16 v[202:203], v21 offset:2048\n"
  "ds_read_b64_tr_b16 v[204:205], v21 offset:8192\n"
  "ds_read_b64_tr_b16 v[206:207], v21 offset:10240\n"
  "s_waitcnt vmcnt(8) lgkmcnt(0)\n"
  "s_barrier\n"
  "v_add_u32 v20, 32768, v13\n"
  "v_xor_b32 v21, 32, v20\n"
  "ds_read_b64_tr_b16 v[208:209], v20 offset:0\n"
  "ds_read_b64_tr_b16 v[210:211], v20 offset:2048\n"
  "ds_read_b64_tr_b16 v[212:213], v20 offset:8192\n"
  "ds_read_b64_tr_b16 v[214:215], v20 offset:10240\n"
  "ds_read_b64_tr_b16 v[216:217], v21 offset:0\n"
  "ds_read_b64_tr_b16 v[218:219], v21 offset:2048\n"
  "ds_read_b64_tr_b16 v[220:221], v21 offset:8192\n"
  "ds_read_b64_tr_b16 v[222:223], v21 offset:10240\n"
  "s_waitcnt vmcnt(4) lgkmcnt(0)\n"
  "s_barrier\n"
  "v_add_u32 v20, 49152, v13\n"
  "v_xor_b32 v21, 32, v20\n"
  "ds_read_b64_tr_b16 v[224:225], v20 offset:0\n"
  "ds_read_b64_tr_b16 v[226:227], v20 offset:2048\n"
  "ds_read_b64_tr_b16 v[228:229], v20 offset:8192\n"
  "ds_read_b64_tr_b16 v[230:231], v20 offset:10240\n"
  "ds_read_b64_tr_b16 v[232:233], v21 offset:0\n"
  "ds_read_b64_tr_b16 v[234:235], v21 offset:2048\n"
  "ds_read_b64_tr_b16 v[236:237], v21 offset:8192\n"
  "ds_read_b64_tr_b16 v[238:239], v21 offset:10240\n"
  "s_waitcnt vmcnt(0) lgkmcnt(0)\n"
  "s_barrier\n"
  "v_add_u32 v20, 65536, v13\n"
  "v_xor_b32 v21, 32, v20\n"
  "ds_read_b64_tr_b16 v[240:241], v20 offset:0\n"
  "ds_read_b64_tr_b16 v[242:243], v20 offset:2048\n"
  "ds_read_b64_tr_b16 v[244:245], v20 offset:8192\n"
  "ds_read_b64_tr_b16 v[246:247], v20 offset:10240\n"
  "ds_read_b64_tr_b16 v[248:249], v21 offset:0\n"
  "ds_read_b64_tr_b16 v[250:251], v21 offset:2048\n"
  "ds_read_b64_tr_b16 v[252:253], v21 offset:8192\n"
  "ds_read_b64_tr_b16 v[254:255], v21 offset:10240\n"
  "s_waitcnt lgkmcnt(0)\n"
  "s_barrier\n"
  "v_mov_b32 v16, 0x3f803f80\n"
  "v_mov_b32 v17, 0x3f803f80\n"
  "v_mov_b32 v18, 0x3f803f80\n"
  "v_mov_b32 v19, 0x3f803f80\n"
  "v_mov_b32 v77, s55\n"
  "s_cmp_lt_u32 s48, 2\n"
  "s_cselect_b64 s[38:39], -1, 0\n"
  "s_mov_b32 s2, s45\n"
  "s_lshr_b32 s3, s52, 8\n"
  "s_and_b32 s3, s3, 4\n"
  "s_add_u32 s3, s3, s48\n"
  "s_mov_b32 s7, s57\n"
  "s_mov_b32 s14, s58\n"
  "s_mov_b32 s15, s59\n"
  "s_mov_b32 s56, 0\n"
  "s_mov_b32 s57, 0\n"
  "s_mov_b32 s58, 100000\n"
  "s_mov_b32 s59, 0\n"
  "s_mov_b32 s60, 562036736\n"
  "s_mov_b32 s61, 1006632960\n"
  "s_mov_b32 s81, -1\n"
  "s_add_u32 s45, s44, 128000\n"
  "s_add_u32 s74, s36, s2\n"
  "s_addc_u32 s75, s37, 0\n"
  "PLHK:\n"
  "v_and_b32 v79, 128, v0\n"
  "global_load_dword v79, v79, s[74:75] sc1\n"
  "s_waitcnt vmcnt(0)\n"
  "v_readfirstlane_b32 s76, v79\n"
  "v_readlane_b32 s77, v79, 8\n"
  "s_min_u32 s76, s76, s77\n"
  "s_cmp_ge_u32 s76, 2\n"
  "s_cbranch_scc1 PDHK\n"
  "s_sub_u32 s58, s58, 1\n"
  "s_cbranch_scc1 QUITH\n"
  "s_sleep 4\n"
  "s_branch PLHK\n"
  "PDHK:\n"
  "s_add_u32 s40, s36, s47\n"
  "s_addc_u32 s41, s37, 0\n"
  "s_lshl_b32 s42, s80, 2\n"
  "s_mov_b32 m0, s42\n"
  "s_nop 0\n"
  "global_load_lds_dwordx4 v0, s[40:41] offset:0 sc1\n"
  "global_load_lds_dwordx4 v0, s[40:41] offset:1024 sc1\n"
  "global_load_lds_dwordx4 v0, s[40:41] offset:2048 sc1\n"
  "global_load_lds_dwordx4 v0, s[40:41] offset:3072 sc1\n"
  "s_add_u32 s40, s40, 524288\n"
  "s_addc_u32 s41, s41, 0\n"
  "v_mov_b64 v[56:57], 0\n"
  "v_mov_b64 v[58:59], 0\n"
  "v_mov_b64 v[60:61], 0\n"
  "v_mov_b64 v[62:63], 0\n"
  "v_mov_b64 v[64:65], 0\n"
  "v_mov_b64 v[66:67], 0\n"
  "v_mov_b64 v[68:69], 0\n"
  "v_mov_b64 v[70:71], 0\n"
  "v_mov_b64 v[72:73], 0\n"
  "v_mov_b64 v[74:75], 0\n"
  "ds_write_b64 v56, v[56:57] offset:42000\n"
  "s_mov_b32 s47, s49\n"
  "s_mov_b32 s32, 0\n"
  "s_mov_b32 s98, s49\n"
  "s_mov_b32 s33, 0\n"
  "s_mov_b32 s35, 0\n"
  "s_sub_u32 s6, s49, 0x100000\n"
  "s_mov_b32 s16, 0\n"
  "s_waitcnt lgkmcnt(0)\n"
  "s_barrier\n"
  "HTRY:\n"
  "s_mul_i32 s78, s57, 163840\n"
  "s_lshl_b32 s79, s32, 16\n"
  "s_add_u32 s79, s79, s78\n"
  "s_add_u32 s79, s79, s44\n"
  "s_add_u32 s62, s36, s79\n"
  "s_addc_u32 s63, s37, 0\n"
  "s_add_u32 s64, s62, 32768\n"
  "s_addc_u32 s65, s63, 0\n"
  "s_lshl_b32 s79, s32, 10\n"
  "s_add_u32 s79, s79, s78\n"
  "s_add_u32 s79, s79, s45\n"
  "s_add_u32 s66, s36, s79\n"
  "s_addc_u32 s67, s37, 0\n"
  "s_add_u32 m0, s80, 32768\n"
  "s_add_u32 s79, s80, 36864\n"
  "s_mov_b32 s34, 0\n"
  "WH:\n"
  "s_memrealtime s[74:75]\n"
  "s_waitcnt lgkmcnt(0)\n"
  "s_mul_i32 s74, s74, 24\n"
  "s_sub_i32 s74, s49, s74\n"
  "s_cmp_lt_i32 s74, 742\n"
  "s_cbranch_scc1 WFH\n"
  "s_cmp_gt_i32 s74, 0x40000\n"
  "s_cbranch_scc1 WEH\n"
  "s_sleep 2\n"
  "s_branch WH\n"
  "WFH:\n"
  "s_cmp_lt_i32 s74, 100\n"
  "s_cbranch_scc1 WEH\n"
  "s_memtime s[76:77]\n"
  "s_memrealtime s[20:21]\n"
  "s_nop 2\n"
  "s_memrealtime s[22:23]\n"
  "s_nop 2\n"
  "s_memrealtime s[24:25]\n"
  "s_nop 2\n"
  "s_memrealtime s[26:27]\n"
  "s_nop 2\n"
  "s_memrealtime s[28:29]\n"
  "s_nop 2\n"
  "s_memrealtime s[30:31]\n"
  "s_waitcnt lgkmcnt(0)\n"
  "s_add_u32 s20, s20, s22\n"
  "s_add_u32 s24, s24, s26\n"
  "s_add_u32 s28, s28, s30\n"
  "s_add_u32 s20, s20, s24\n"
  "s_add_u32 s20, s20, s28\n"
  "s_lshl_b32 s74, s20, 2\n"
  "s_sub_u32 s34, s74, s76\n"
  "s_sub_i32 s74, s49, s74\n"
  "s_sub_i32 s74, s74, 230\n"
  "s_max_i32 s74, s74, 0\n"
  "s_lshr_b32 s74, s74, 3\n"
  "s_lshl_b32 s74, s74, 2\n"
  "s_sub_i32 s74, 268, s74\n"
  "s_getpc_b64 s[76:77]\n"
  "s_add_u32 s76, s76, s74\n"
  "s_addc_u32 s77, s77, 0\n"
  "s_setpc_b64 s[76:77]\n"
  "s_nop 1\n"
  "s_nop 1\n"
  "s_nop 1\n"
  "s_nop 1\n"
  "s_nop 1\n"
  "s_nop 1\n"
  "s_nop 1\n"
  "s_nop 1\n"
  "s_nop 1\n"
  "s_nop 1\n"
  "s_nop 1\n"
  "s_nop 1\n"
  "s_nop 1\n"
  "s_nop 1\n"
  "s_nop 1\n"
  "s_nop 1\n"
  "s_nop 1\n"
  "s_nop 1\n"
  "s_nop 1\n"
  "s_nop 1\n"
  "s_nop 1\n"
  "s_nop 1\n"
  "s_nop 1\n"
  "s_nop 1\n"
  "s_nop 1\n"
  "s_nop 1\n"
  "s_nop 1\n"
  "s_nop 1\n"
  "s_nop 1\n"
  "s_nop 1\n"
  "s_nop 1\n"
  "s_nop 1\n"
  "s_nop 1\n"
  "s_nop 1\n"
  "s_nop 1\n"
  "s_nop 1\n"
  "s_nop 1\n"
  "s_nop 1\n"
  "s_nop 1\n"
  "s_nop 1\n"
  "s_nop 1\n"
  "s_nop 1\n"
  "s_nop 1\n"
  "s_nop 1\n"
  "s_nop 1\n"
  "s_nop 1\n"
  "s_nop 1\n"
  "s_nop 1\n"
  "s_nop 1\n"
  "s_nop 1\n"
  "s_nop 1\n"
  "s_nop 1\n"
  "s_nop 1\n"
  "s_nop 1\n"
  "s_nop 1\n"
  "s_nop 1\n"
  "s_nop 1\n"
  "s_nop 1\n"
  "s_nop 1\n"
  "s_nop 1\n"
  "s_nop 1\n"
  "s_nop 1\n"
  "s_nop 1\n"
  "s_nop 1\n"
  "WEH:\n"
  "global_load_lds_dwordx4 v1, s[62:63] sc1\n"
  "s_mov_b32 m0, s79\n"
  "s_cmp_lg_u32 s48, 3\n"
  "global_load_lds_dwordx4 v1, s[64:65] sc1\n"
  "s_cbranch_scc1 HN8\n"
  "s_mov_b32 m0, 40960\n"
  "s_nop 0\n"
  "global_load_lds_dwordx4 v1, s[66:67] sc1\n"
  "s_waitcnt vmcnt(3)\n"
  "s_branch HW8\n"
  "HN8:\n"
  "s_waitcnt vmcnt(2)\n"
  "HW8:\n"
  "s_and_b32 s75, s56, 1\n"
  "s_lshl_b32 s75, s75, 14\n"
  "v_add_u32 v76, s75, v0\n"
  "s_barrier\n"
  "ds_read_b128 v[80:83], v76 offset:0\n"
  "s_and_b32 s74, s56, 1\n"
  "s_lshl_b32 s75, s74, 2\n"
  "v_mov_b32 v79, s75\n"
  "s_sub_u32 s75, s49, s47\n"
  "ds_read_b128 v[84:87], v76 offset:1024\n"
  "v_mov_b32 v78, s75\n"
  "s_mov_b64 exec, 1\n"
  "ds_max_u32 v79, v78 offset:42000\n"
  "s_mov_b64 exec, -1\n"
  "ds_read_b128 v[88:91], v76 offset:2048\n"
  "s_lshl_b32 s74, s74, 16\n"
  "s_add_u32 s70, s36, s74\n"
  "s_addc_u32 s71, s37, 0\n"
  "s_lshl_b32 s74, s56, 10\n"
  "ds_read_b128 v[92:95], v76 offset:3072\n"
  "s_add_u32 s74, s74, 16785408\n"
  "s_add_u32 s18, s36, s74\n"
  "s_addc_u32 s19, s37, 0\n"
  "s_lshr_b32 s74, s80, 2\n"
  "ds_read_b128 v[96:99], v76 offset:4096\n"
  "s_add_u32 m0, s74, 50432\n"
  "s_nop 0\n"
  "global_load_lds_dword v0, s[18:19] sc1\n"
  "s_add_u32 s76, s36, s2\n"
  "ds_read_b128 v[100:103], v76 offset:5120\n"
  "s_addc_u32 s77, s37, 0\n"
  "s_add_u32 m0, s74, 51456\n"
  "s_mov_b64 exec, 0x101\n"
  "global_load_lds_dword v0, s[76:77] sc1\n"
  "s_mov_b64 exec, -1\n"
  "ds_read_b128 v[104:107], v76 offset:6144\n"
  "s_sub_u32 s74, s56, 1\n"
  "s_max_i32 s74, s74, 0\n"
  "v_mov_b32 v78, s74\n"
  "v_mov_b32 v79, 8192\n"
  "ds_read_b128 v[108:111], v76 offset:7168\n"
  "s_cmp_eq_u32 s3, 4\n"
  "s_cselect_b64 exec, 1, 0\n"
  "global_store_dword v79, v78, s[76:77] sc1\n"
  "s_mov_b64 exec, -1\n"
  "ds_read_b128 a[224:227], v76 offset:8192\n"
  "s_add_u32 s74, s16, s34\n"
  "s_sub_u32 s74, s74, s6\n"
  "v_mov_b32 v12, s74\n"
  "v_mov_b32 v79, s7\n"
  "ds_read_b128 a[228:231], v76 offset:9216\n"
  "s_cmp_lt_u32 s74, 6000\n"
  "s_cselect_b32 s75, s34, 0\n"
  "s_add_u32 s74, s56, s3\n"
  "s_and_b32 s74, s74, 7\n"
  "ds_read_b128 a[232:235], v76 offset:10240\n"
  "s_cselect_b32 s75, 0, s75\n"
  "s_cmp_lg_u32 s75, 0\n"
  "s_cselect_b64 exec, 1, 0\n"
  "global_atomic_umax v79, v12, s[18:19] offset:2048 sc1\n"
  "s_mov_b64 exec, -1\n"
  "ds_read_b128 a[236:239], v76 offset:11264\n"
  "s_sub_u32 s6, s6, 0x100000\n"
  "v_lshrrev_b32 v79, 1, v0\n"
  "s_add_u32 s72, s36, s53\n"
  "s_addc_u32 s73, s37, 0\n"
  "ds_read_b128 a[240:243], v76 offset:12288\n"
  "s_cmp_lg_u32 s33, 0\n"
  "s_cselect_b64 exec, -1, 0\n"
  "global_store_dwordx2 v79, v[14:15], s[72:73] sc1\n"
  "s_mov_b64 exec, -1\n"
  "ds_read_b128 a[244:247], v76 offset:13312\n"
  "s_lshl_b32 s74, s33, 16\n"
  "s_add_u32 s53, s53, s74\n"
  "s_mov_b32 s33, 0\n"
  "s_mul_i32 s74, s57, 131072\n"
  "ds_read_b128 a[248:251], v76 offset:14336\n"
  "s_add_u32 s74, s74, s46\n"
  "s_add_u32 s68, s36, s74\n"
  "s_addc_u32 s69, s37, 0\n"
  "s_and_b32 s74, s56, 1\n"
  "ds_read_b128 a[252:255], v76 offset:15360\n"
  "s_xor_b32 s74, s74, 1\n"
  "s_lshl_b32 s74, s74, 14\n"
  "s_add_u32 m0, s42, s74\n"
  "s_cmp_lg_u32 s48, 3\n"
  "s_waitcnt vmcnt(7) lgkmcnt(0)\n"
  "s_cbranch_scc0 HD8\n"
  "s_waitcnt vmcnt(6)\n"
  "HD8:\n"
  "s_barrier\n"
  "ds_read_b128 v[20:23], v0 offset:32768\n"
  "ds_read_b128 v[24:27], v0 offset:33792\n"
  "ds_read_b128 v[28:31], v0 offset:34816\n"
  "ds_read_b128 v[32:35], v0 offset:35840\n"
  "s_waitcnt lgkmcnt(3)\n"
  "v_smfmac_f32_16x16x64_bf16 v[56:59], v[20:23], v[112:119], v6\n"
  "v_smfmac_f32_16x16x64_bf16 v[60:63], v[20:23], v[120:127], v6\n"
  "s_waitcnt vmcnt(5)\n"
  "s_barrier\n"
  "ds_read_b128 v[36:39], v0 offset:36864\n"
  "s_waitcnt lgkmcnt(3)\n"
  "v_smfmac_f32_16x16x64_bf16 v[56:59], v[24:27], v[128:135], v6\n"
  "ds_read_b128 v[40:43], v0 offset:37888\n"
  "v_smfmac_f32_16x16x64_bf16 v[60:63], v[24:27], v[136:143], v6\n"
  "ds_read_b128 v[44:47], v0 offset:38912\n"
  "s_waitcnt lgkmcnt(4)\n"
  "v_smfmac_f32_16x16x64_bf16 v[56:59], v[28:31], v[144:151], v6\n"
  "ds_read_b128 v[48:51], v0 offset:39936\n"
  "v_smfmac_f32_16x16x64_bf16 v[60:63], v[28:31], v[152:159], v6\n"
  "ds_read_b128 v[52:55], v0 offset:40960\n"
  "s_waitcnt lgkmcnt(5)\n"
  "v_smfmac_f32_16x16x64_bf16 v[56:59], v[32:35], v[160:167], v6\n"
  "v_smfmac_f32_16x16x64_bf16 v[60:63], v[32:35], v[168:175], v6\n"
  "s_waitcnt lgkmcnt(4)\n"
  "v_smfmac_f32_16x16x64_bf16 v[56:59], v[36:39], v[176:183], v6\n"
  "v_smfmac_f32_16x16x64_bf16 v[60:63], v[36:39], v[184:191], v6\n"
  "s_waitcnt lgkmcnt(3)\n"
  "v_smfmac_f32_16x16x64_bf16 v[56:59], v[40:43], v[192:199], v6\n"
  "v_smfmac_f32_16x16x64_bf16 v[60:63], v[40:43], v[200:207], v6\n"
  "s_waitcnt lgkmcnt(2)\n"
  "v_smfmac_f32_16x16x64_bf16 v[56:59], v[44:47], v[208:215], v6\n"
  "v_smfmac_f32_16x16x64_bf16 v[60:63], v[44:47], v[216:223], v6\n"
  "s_waitcnt lgkmcnt(1)\n"
  "v_smfmac_f32_16x16x64_bf16 v[56:59], v[48:51], v[224:231], v6\n"
  "v_smfmac_f32_16x16x64_bf16 v[60:63], v[48:51], v[232:239], v6\n"
  "s_waitcnt lgkmcnt(0)\n"
  "v_smfmac_f32_16x16x64_bf16 v[56:59], v[52:55], v[240:247], v6\n"
  "v_smfmac_f32_16x16x64_bf16 v[60:63], v[52:55], v[248:255], v6\n"
  "v_smfmac_f32_16x16x64_bf16 v[64:67], v[20:23], a[0:7], v6\n"
  "v_smfmac_f32_16x16x64_bf16 v[68:71], v[20:23], a[8:15], v6\n"
  "v_add_f32 v56, v56, v58\n"
  "v_add_f32 v57, v57, v59\n"
  "v_smfmac_f32_16x16x64_bf16 v[64:67], v[24:27], a[16:23], v6\n"
  "v_cvt_pk_bf16_f32 v14, v56, v57\n"
  "v_smfmac_f32_16x16x64_bf16 v[68:71], v[24:27], a[24:31], v6\n"
  "ds_write2_b32 v3, v14, v14 offset0:0 offset1:1\n"
  "v_add_f32 v60, v60, v62\n"
  "v_smfmac_f32_16x16x64_bf16 v[64:67], v[28:31], a[32:39], v6\n"
  "v_add_f32 v61, v61, v63\n"
  "v_smfmac_f32_16x16x64_bf16 v[68:71], v[28:31], a[40:47], v6\n"
  "v_cvt_pk_bf16_f32 v15, v60, v61\n"
  "v_smfmac_f32_16x16x64_bf16 v[64:67], v[32:35], a[48:55], v6\n"
  "ds_write2_b32 v3, v15, v15 offset0:128 offset1:129\n"
  "v_cmp_u_f32 vcc, v56, v57\n"
  "v_smfmac_f32_16x16x64_bf16 v[68:71], v[32:35], a[56:63], v6\n"
  "v_smfmac_f32_16x16x64_bf16 v[64:67], v[36:39], a[64:71], v6\n"
  "ds_write_b64 v4, v[14:15]\n"
  "v_smfmac_f32_16x16x64_bf16 v[68:71], v[36:39], a[72:79], v6\n"
  "s_cbranch_vccnz HBADM\n"
  "HOKM:\n"
  "v_smfmac_f32_16x16x64_bf16 v[64:67], v[40:43], a[80:87], v6\n"
  "s_waitcnt lgkmcnt(1)\n"
  "s_barrier\n"
  "ds_read_b64_tr_b16 v[20:21], v5 offset:0\n"
  "ds_read_b64_tr_b16 v[22:23], v5 offset:512\n"
  "v_smfmac_f32_16x16x64_bf16 v[68:71], v[40:43], a[88:95], v6\n"
  "ds_read_b64_tr_b16 v[24:25], v5 offset:1024\n"
  "ds_read_b64_tr_b16 v[26:27], v5 offset:1536\n"
  "global_load_dwordx4 v[36:39], v2, s[68:69] offset:0 sc1\n"
  "v_smfmac_f32_16x16x64_bf16 v[64:67], v[44:47], a[96:103], v6\n"
  "ds_read_b64_tr_b16 v[28:29], v5 offset:2048\n"
  "ds_read_b64_tr_b16 v[30:31], v5 offset:2560\n"
  "v_smfmac_f32_16x16x64_bf16 v[68:71], v[44:47], a[104:111], v6\n"
  "ds_read_b64_tr_b16 v[32:33], v5 offset:3072\n"
  "ds_read_b64_tr_b16 v[34:35], v5 offset:3584\n"
  "v_smfmac_f32_16x16x64_bf16 v[64:67], v[48:51], a[112:119], v6\n"
  "v_smfmac_f32_16x16x64_bf16 v[68:71], v[48:51], a[120:127], v6\n"
  "global_load_dwordx4 v[40:43], v2, s[68:69] offset:1024 sc1\n"
  "s_waitcnt lgkmcnt(8)\n"
  "s_barrier\n"
  "v_smfmac_f32_16x16x64_bf16 v[64:67], v[52:55], a[128:135], v6\n"
  "ds_read_b64_tr_b16 v[44:45], v5 offset:4096\n"
  "ds_read_b64_tr_b16 v[46:47], v5 offset:4608\n"
  "v_smfmac_f32_16x16x64_bf16 v[68:71], v[52:55], a[136:143], v6\n"
  "ds_read_b64_tr_b16 v[48:49], v5 offset:5120\n"
  "ds_read_b64_tr_b16 v[50:51], v5 offset:5632\n"
  "s_waitcnt lgkmcnt(10)\n"
  "v_mfma_f32_16x16x32_bf16 v[56:59], v[80:83], v[20:23], 0\n"
  "s_waitcnt lgkmcnt(8)\n"
  "v_mfma_f32_16x16x32_bf16 v[56:59], v[88:91], v[24:27], v[56:59]\n"
  "v_add_f32 v64, v64, v66\n"
  "v_add_f32 v65, v65, v67\n"
  "s_waitcnt lgkmcnt(6)\n"
  "v_mfma_f32_16x16x32_bf16 v[56:59], v[96:99], v[28:31], v[56:59]\n"
  "v_cvt_pk_bf16_f32 v14, v64, v65\n"
  "v_add_f32 v68, v68, v70\n"
  "s_waitcnt lgkmcnt(4)\n"
  "v_mfma_f32_16x16x32_bf16 v[56:59], v[104:107], v[32:35], v[56:59]\n"
  "v_add_f32 v69, v69, v71\n"
  "v_cvt_pk_bf16_f32 v15, v68, v69\n"
  "v_mfma_f32_16x16x32_bf16 v[60:63], v[84:87], v[20:23], 0\n"
  "ds_write_b64 v4, v[14:15] offset:2048\n"
  "v_mfma_f32_16x16x32_bf16 v[60:63], v[92:95], v[24:27], v[60:63]\n"
  "v_mfma_f32_16x16x32_bf16 v[60:63], v[100:103], v[28:31], v[60:63]\n"
  "v_exp_f32 v56, v56\n"
  "v_mfma_f32_16x16x32_bf16 v[60:63], v[108:111], v[32:35], v[60:63]\n"
  "v_exp_f32 v57, v57\n"
  "v_exp_f32 v58, v58\n"
  "v_mfma_f32_32x32x16_bf16 v[80:95], v[72:75], v[72:75], 0\n"
  "s_waitcnt lgkmcnt(0)\n"
  "s_barrier\n"
  "ds_read_b64_tr_b16 v[96:97], v5 offset:6144\n"
  "ds_read_b64_tr_b16 v[98:99], v5 offset:6656\n"
  "v_exp_f32 v59, v59\n"
  "ds_read_b64_tr_b16 v[100:101], v5 offset:7168\n"
  "ds_read_b64_tr_b16 v[102:103], v5 offset:7680\n"
  "v_exp_f32 v60, v60\n"
  "v_exp_f32 v61, v61\n"
  "v_exp_f32 v62, v62\n"
  "s_waitcnt lgkmcnt(2)\n"
  "v_mfma_f32_16x16x32_bf16 v[52:55], v[96:99], v[44:47], 0\n"
  "v_exp_f32 v63, v63\n"
  "s_waitcnt lgkmcnt(0)\n"
  "v_mfma_f32_16x16x32_bf16 v[52:55], v[100:103], v[48:51], v[52:55]\n"
  "s_waitcnt vmcnt(5)\n"
  "global_load_lds_dwordx4 v0, s[40:41] offset:0 sc1\n"
  "v_perm_b32 v44, v57, v56, v7\n"
  "v_perm_b32 v45, v59, v58, v7\n"
  "v_perm_b32 v46, v61, v60, v7\n"
  "v_perm_b32 v47, v63, v62, v7\n"
  "v_perm_b32 v48, v57, v56, v8\n"
  "v_perm_b32 v49, v59, v58, v8\n"
  "v_mfma_f32_16x16x32_bf16 v[68:71], v[16:19], v[44:47], 0\n"
  "v_perm_b32 v50, v61, v60, v8\n"
  "v_perm_b32 v51, v63, v62, v8\n"
  "v_mfma_f32_16x16x32_bf16 v[20:23], a[224:227], v[44:47], 0\n"
  "v_mul_f32_dpp v64, v52, v77 quad_perm:[0,1,0,1] row_mask:0xf bank_mask:0xf\n"
  "v_mul_f32_dpp v65, v53, v77 quad_perm:[0,1,0,1] row_mask:0xf bank_mask:0xf\n"
  "v_mfma_f32_16x16x32_bf16 v[68:71], v[16:19], v[48:51], v[68:71]\n"
  "v_fmac_f32_dpp v64, v54, v77 quad_perm:[2,3,2,3] row_mask:0xf bank_mask:0xf\n"
  "v_fmac_f32_dpp v65, v55, v77 quad_perm:[2,3,2,3] row_mask:0xf bank_mask:0xf\n"
  "v_mfma_f32_16x16x32_bf16 v[20:23], a[228:231], v[48:51], v[20:23]\n"
  "v_exp_f32 v64, v64\n"
  "v_mfma_f32_16x16x32_bf16 v[24:27], a[232:235], v[44:47], 0\n"
  "global_load_lds_dwordx4 v0, s[40:41] offset:1024 sc1\n"
  "v_exp_f32 v65, v65\n"
  "v_mfma_f32_16x16x32_bf16 v[24:27], a[236:239], v[48:51], v[24:27]\n"
  "v_perm_b32 v12, v65, v64, v7\n"
  "v_perm_b32 v13, v65, v64, v8\n"
  "v_mfma_f32_16x16x32_bf16 v[28:31], a[240:243], v[44:47], 0\n"
  "HPV:\n"
  "s_nop 0\n"
  "v_mfma_f32_16x16x16_bf16 v[68:71], v[16:17], v[12:13], v[68:71]\n"
  "s_waitcnt vmcnt(3)\n"
  "v_mfma_f32_16x16x16_bf16 v[20:23], v[36:37], v[12:13], v[20:23]\n"
  "global_load_lds_dwordx4 v0, s[40:41] offset:2048 sc1\n"
  "v_mfma_f32_16x16x16_bf16 v[24:27], v[38:39], v[12:13], v[24:27]\n"
  "v_mfma_f32_16x16x32_bf16 v[28:31], a[244:247], v[48:51], v[28:31]\n"
  "v_rcp_f32 v78, v68\n"
  "v_mfma_f32_16x16x32_bf16 v[32:35], a[248:251], v[44:47], 0\n"
  "v_mul_f32 v20, v78, v20\n"
  "v_mul_f32 v21, v78, v21\n"
  "v_mfma_f32_16x16x32_bf16 v[32:35], a[252:255], v[48:51], v[32:35]\n"
  "v_mul_f32 v22, v78, v22\n"
  "v_mul_f32 v23, v78, v23\n"
  "s_waitcnt vmcnt(3)\n"
  "v_mfma_f32_16x16x16_bf16 v[28:31], v[40:41], v[12:13], v[28:31]\n"
  "v_cvt_pk_bf16_f32 v104, v20, v21\n"
  "v_cvt_pk_bf16_f32 v105, v22, v23\n"
  "v_mul_f32 v24, v78, v24\n"
  "v_mul_f32 v25, v78, v25\n"
  "v_mfma_f32_16x16x16_bf16 v[32:35], v[42:43], v[12:13], v[32:35]\n"
  "global_load_lds_dwordx4 v0, s[40:41] offset:3072 sc1\n"
  "v_mul_f32 v26, v78, v26\n"
  "v_mul_f32 v27, v78, v27\n"
  "v_cvt_pk_bf16_f32 v106, v24, v25\n"
  "v_cvt_pk_bf16_f32 v107, v26, v27\n"
  "v_subrev_u32 v79, s60, v68\n"
  "v_cmp_le_u32 vcc, s61, v79\n"
  "v_smfmac_f32_16x16x64_bf16 v[72:75], v[104:107], a[208:215], v6\n"
  "v_mul_f32 v28, v78, v28\n"
  "v_mul_f32 v29, v78, v29\n"
  "v_mul_f32 v30, v78, v30\n"
  "v_smfmac_f32_16x16x64_bf16 v[80:83], v[104:107], a[144:151], v6\n"
  "v_mul_f32 v31, v78, v31\n"
  "v_cvt_pk_bf16_f32 v108, v28, v29\n"
  "v_cvt_pk_bf16_f32 v109, v30, v31\n"
  "v_smfmac_f32_16x16x64_bf16 v[84:87], v[104:107], a[160:167], v6\n"
  "v_mul_f32 v32, v78, v32\n"
  "v_mul_f32 v33, v78, v33\n"
  "v_mul_f32 v34, v78, v34\n"
  "s_cbranch_vccnz HBADZ\n"
  "HOKZ:\n"
  "v_smfmac_f32_16x16x64_bf16 v[88:91], v[104:107], a[176:183], v6\n"
  "v_mul_f32 v35, v78, v35\n"
  "v_cvt_pk_bf16_f32 v110, v32, v33\n"
  "v_cvt_pk_bf16_f32 v111, v34, v35\n"
  "v_smfmac_f32_16x16x64_bf16 v[92:95], v[104:107], a[192:199], v6\n"
  "s_nop 0\n"
  "v_smfmac_f32_16x16x64_bf16 v[72:75], v[108:111], a[216:223], v6\n"
  "v_smfmac_f32_16x16x64_bf16 v[80:83], v[108:111], a[152:159], v6\n"
  "v_smfmac_f32_16x16x64_bf16 v[84:87], v[108:111], a[168:175], v6\n"
  "s_nop 0\n"
  "v_add_f32 v72, v72, v74\n"
  "v_smfmac_f32_16x16x64_bf16 v[88:91], v[108:111], a[184:191], v6\n"
  "v_add_f32 v73, v73, v75\n"
  "v_smfmac_f32_16x16x64_bf16 v[92:95], v[108:111], a[200:207], v6\n"
  "v_cvt_pk_bf16_f32 v74, v72, v73\n"
  "s_mov_b64 exec, s[38:39]\n"
  "global_store_dword v11, v74, s[70:71] offset:0 sc1\n"
  "s_mov_b64 exec, -1\n"
  "v_add_f32 v80, v80, v82\n"
  "v_add_f32 v81, v81, v83\n"
  "v_cvt_pk_bf16_f32 v82, v80, v81\n"
  "global_store_dword v9, v82, s[70:71] offset:0 sc1\n"
  "v_add_f32 v84, v84, v86\n"
  "v_add_f32 v85, v85, v87\n"
  "v_cvt_pk_bf16_f32 v86, v84, v85\n"
  "global_store_dword v9, v86, s[70:71] offset:128 sc1\n"
  "v_add_f32 v88, v88, v90\n"
  "v_add_f32 v89, v89, v91\n"
  "v_cvt_pk_bf16_f32 v90, v88, v89\n"
  "global_store_dword v10, v90, s[70:71] offset:0 sc1\n"
  "v_add_f32 v92, v92, v94\n"
  "v_add_f32 v93, v93, v95\n"
  "v_cvt_pk_bf16_f32 v94, v92, v93\n"
  "global_store_dword v10, v94, s[70:71] offset:128 sc1\n"
  "s_memtime s[16:17]\n"
  "v_add_f32 v79, v80, v81\n"
  "v_add_f32 v78, v84, v85\n"
  "v_add_f32 v79, v79, v78\n"
  "v_add_f32 v78, v88, v89\n"
  "v_add_f32 v79, v79, v78\n"
  "v_add_f32 v78, v92, v93\n"
  "v_add_f32 v79, v79, v78\n"
  "v_add_f32 v78, v72, v73\n"
  "v_add_f32 v79, v79, v78\n"
  "v_cmp_u_f32 vcc, v79, v79\n"
  "s_and_b32 s74, s56, 1\n"
  "s_lshl_b32 s74, s74, 2\n"
  "v_mov_b32 v78, s74\n"
  "v_mov_b64 v[56:57], 0\n"
  "v_mov_b64 v[58:59], 0\n"
  "v_mov_b64 v[60:61], 0\n"
  "v_mov_b64 v[62:63], 0\n"
  "v_mov_b64 v[64:65], 0\n"
  "v_mov_b64 v[66:67], 0\n"
  "v_mov_b64 v[68:69], 0\n"
  "v_mov_b64 v[70:71], 0\n"
  "s_cbranch_vccnz HBADV\n"
  "HOKV:\n"
  "s_memrealtime s[76:77]\n"
  "v_mov_b64 v[72:73], 0\n"
  "v_mov_b64 v[74:75], 0\n"
  "ds_read_b32 v78, v78 offset:42000\n"
  "s_lshr_b32 s74, s80, 2\n"
  "s_add_u32 s74, s74, 50432\n"
  "v_lshrrev_b32 v79, 2, v0\n"
  "v_add_u32 v79, s74, v79\n"
  "ds_read_b32 v12, v79 offset:1024\n"
  "ds_read_b32 v79, v79\n"
  "s_lshr_b32 s33, s52, 10\n"
  "s_add_u32 s56, s56, 1\n"
  "s_cmp_eq_u32 s56, 64\n"
  "s_cbranch_scc1 ENDH\n"
  "s_waitcnt lgkmcnt(1)\n"
  "v_readfirstlane_b32 s74, v12\n"
  "v_readlane_b32 s75, v12, 8\n"
  "s_min_u32 s74, s74, s75\n"
  "s_cmp_le_u32 s74, s56\n"
  "s_cbranch_scc1 HKW\n"
  "HKOK:\n"
  "s_cmp_lt_u32 s56, 63\n"
  "s_cselect_b32 s74, 524288, 0\n"
  "s_add_u32 s40, s40, s74\n"
  "s_addc_u32 s41, s41, 0\n"
  "s_add_u32 s57, s57, 1\n"
  "s_cmp_eq_u32 s57, 3\n"
  "s_cselect_b32 s57, 0, s57\n"
  "v_readfirstlane_b32 s49, v78\n"
  "s_waitcnt lgkmcnt(0)\n"
  "v_readlane_b32 s8, v79, 0\n"
  "v_readlane_b32 s9, v79, 8\n"
  "v_readlane_b32 s10, v79, 16\n"
  "v_readlane_b32 s11, v79, 24\n"
  "v_readlane_b32 s12, v79, 32\n"
  "v_readlane_b32 s13, v79, 40\n"
  "s_mul_i32 s99, s76, 24\n"
  "s_add_u32 s49, s49, s47\n"
  "s_lshr_b32 s35, s35, 1\n"
  "s_lshl_b32 s35, s35, 20\n"
  "s_sub_u32 s6, s49, s35\n"
  "s_mov_b32 s35, 0\n"
  "s_max_u32 s8, s8, s9\n"
  "s_max_u32 s10, s10, s11\n"
  "s_max_u32 s8, s8, s10\n"
  "s_max_u32 s12, s12, s13\n"
  "s_cmp_eq_u32 s8, 0\n"
  "s_cselect_b32 s8, s14, s8\n"
  "s_max_u32 s75, s8, s14\n"
  "s_mov_b32 s14, s8\n"
  "s_mov_b32 s8, s75\n"
  "s_cmp_eq_u32 s12, 0\n"
  "s_cselect_b32 s12, s15, s12\n"
  "s_max_u32 s75, s12, s15\n"
  "s_mov_b32 s15, s12\n"
  "s_mov_b32 s12, s75\n"
  "s_add_u32 s74, s8, 150\n"
  "s_add_u32 s50, s74, s12\n"
  "s_add_u32 s50, s50, 230\n"
  "s_add_u32 s49, s49, s50\n"
  "s_mov_b32 s98, s49\n"
  "s_mov_b32 s32, 0\n"
  "s_sub_i32 s74, s49, s99\n"
  "s_cmp_ge_i32 s74, 500\n"
  "s_cbranch_scc1 HTRY\n"
  "s_mov_b32 s32, 1\n"
  "s_add_u32 s99, s99, 680\n"
  "HSK:\n"
  "s_add_u32 s49, s49, s51\n"
  "s_sub_i32 s74, s49, s99\n"
  "s_cmp_lt_i32 s74, 0\n"
  "s_cbranch_scc1 HSK\n"
  "s_branch HTRY\n"
  "HBADM:\n"
  "s_waitcnt lgkmcnt(0)\n"
  "s_nop 3\n"
  "v_mov_b32 v76, v76\n"
  "s_nop 2\n"
  "v_mov_b32 v76, v76\n"
  "s_nop 2\n"
  "s_mov_b32 s74, 0xff820000\n"
  "v_max3_u32 v79, v20, v21, v22\n"
  "v_max3_u32 v79, v79, v23, v24\n"
  "v_max3_u32 v79, v79, v25, v26\n"
  "v_max3_u32 v79, v79, v27, v28\n"
  "v_max3_u32 v79, v79, v29, v30\n"
  "v_max3_u32 v79, v79, v31, v32\n"
  "v_max3_u32 v79, v79, v33, v34\n"
  "v_max3_u32 v79, v79, v35, v36\n"
  "v_max3_u32 v79, v79, v37, v38\n"
  "v_max3_u32 v79, v79, v39, v40\n"
  "v_max3_u32 v79, v79, v41, v42\n"
  "v_max3_u32 v79, v79, v43, v44\n"
  "v_max3_u32 v79, v79, v45, v46\n"
  "v_max3_u32 v79, v79, v47, v48\n"
  "v_max3_u32 v79, v79, v49, v50\n"
  "v_max3_u32 v79, v79, v51, v52\n"
  "v_max3_u32 v79, v79, v53, v54\n"
  "v_max_u32 v79, v79, v55\n"
  "v_cmp_le_u32 vcc, s74, v79\n"
  "s_cbranch_vccnz HBM2\n"
  "s_mov_b32 s74, 0x7fff7fff\n"
  "v_and_b32 v79, s74, v20\n"
  "v_and_b32 v12, s74, v21\n"
  "v_pk_max_u16 v79, v79, v12\n"
  "v_and_b32 v12, s74, v22\n"
  "v_pk_max_u16 v79, v79, v12\n"
  "v_and_b32 v12, s74, v23\n"
  "v_pk_max_u16 v79, v79, v12\n"
  "v_and_b32 v12, s74, v24\n"
  "v_pk_max_u16 v79, v79, v12\n"
  "v_and_b32 v12, s74, v25\n"
  "v_pk_max_u16 v79, v79, v12\n"
  "v_and_b32 v12, s74, v26\n"
  "v_pk_max_u16 v79, v79, v12\n"
  "v_and_b32 v12, s74, v27\n"
  "v_pk_max_u16 v79, v79, v12\n"
  "v_and_b32 v12, s74, v28\n"
  "v_pk_max_u16 v79, v79, v12\n"
  "v_and_b32 v12, s74, v29\n"
  "v_pk_max_u16 v79, v79, v12\n"
  "v_and_b32 v12, s74, v30\n"
  "v_pk_max_u16 v79, v79, v12\n"
  "v_and_b32 v12, s74, v31\n"
  "v_pk_max_u16 v79, v79, v12\n"
  "v_and_b32 v12, s74, v32\n"
  "v_pk_max_u16 v79, v79, v12\n"
  "v_and_b32 v12, s74, v33\n"
  "v_pk_max_u16 v79, v79, v12\n"
  "v_and_b32 v12, s74, v34\n"
  "v_pk_max_u16 v79, v79, v12\n"
  "v_and_b32 v12, s74, v35\n"
  "v_pk_max_u16 v79, v79, v12\n"
  "v_and_b32 v12, s74, v36\n"
  "v_pk_max_u16 v79, v79, v12\n"
  "v_and_b32 v12, s74, v37\n"
  "v_pk_max_u16 v79, v79, v12\n"
  "v_and_b32 v12, s74, v38\n"
  "v_pk_max_u16 v79, v79, v12\n"
  "v_and_b32 v12, s74, v39\n"
  "v_pk_max_u16 v79, v79, v12\n"
  "v_and_b32 v12, s74, v40\n"
  "v_pk_max_u16 v79, v79, v12\n"
  "v_and_b32 v12, s74, v41\n"
  "v_pk_max_u16 v79, v79, v12\n"
  "v_and_b32 v12, s74, v42\n"
  "v_pk_max_u16 v79, v79, v12\n"
  "v_and_b32 v12, s74, v43\n"
  "v_pk_max_u16 v79, v79, v12\n"
  "v_and_b32 v12, s74, v44\n"
  "v_pk_max_u16 v79, v79, v12\n"
  "v_and_b32 v12, s74, v45\n"
  "v_pk_max_u16 v79, v79, v12\n"
  "v_and_b32 v12, s74, v46\n"
  "v_pk_max_u16 v79, v79, v12\n"
  "v_and_b32 v12, s74, v47\n"
  "v_pk_max_u16 v79, v79, v12\n"
  "v_and_b32 v12, s74, v48\n"
  "v_pk_max_u16 v79, v79, v12\n"
  "v_and_b32 v12, s74, v49\n"
  "v_pk_max_u16 v79, v79, v12\n"
  "v_and_b32 v12, s74, v50\n"
  "v_pk_max_u16 v79, v79, v12\n"
  "v_and_b32 v12, s74, v51\n"
  "v_pk_max_u16 v79, v79, v12\n"
  "v_and_b32 v12, s74, v52\n"
  "v_pk_max_u16 v79, v79, v12\n"
  "v_and_b32 v12, s74, v53\n"
  "v_pk_max_u16 v79, v79, v12\n"
  "v_and_b32 v12, s74, v54\n"
  "v_pk_max_u16 v79, v79, v12\n"
  "v_and_b32 v12, s74, v55\n"
  "v_pk_max_u16 v79, v79, v12\n"
  "v_lshrrev_b32 v12, 16, v79\n"
  "v_and_b32 v79, 0xffff, v79\n"
  "v_max_u32 v79, v79, v12\n"
  "v_cmp_lt_u32 vcc, 0x7f81, v79\n"
  "s_cbranch_vccz HOKM\n"
  "HBM2:\n"
  "s_memrealtime s[76:77]\n"
  "v_mov_b64 v[56:57], 0\n"
  "v_mov_b64 v[58:59], 0\n"
  "v_mov_b64 v[60:61], 0\n"
  "v_mov_b64 v[62:63], 0\n"
  "v_mov_b64 v[64:65], 0\n"
  "v_mov_b64 v[66:67], 0\n"
  "v_mov_b64 v[68:69], 0\n"
  "v_mov_b64 v[70:71], 0\n"
  "s_barrier\n"
  "s_sub_u32 s58, s58, 1\n"
  "s_cbranch_scc1 QUITH\n"
  "s_add_u32 s59, s59, 1\n"
  "s_mov_b32 s32, 1\n"
  "s_waitcnt lgkmcnt(0)\n"
  "s_mul_i32 s99, s76, 24\n"
  "s_add_u32 s99, s99, 480\n"
  "s_branch HSK\n"
  "HKW:\n"
  "s_waitcnt vmcnt(0) lgkmcnt(0)\n"
  "s_nop 3\n"
  "v_mov_b32 v76, v76\n"
  "s_nop 2\n"
  "v_mov_b32 v76, v76\n"
  "s_nop 2\n"
  "s_add_u32 s74, s36, s2\n"
  "s_addc_u32 s75, s37, 0\n"
  "PLHK2:\n"
  "v_and_b32 v12, 128, v0\n"
  "global_load_dword v12, v12, s[74:75] sc1\n"
  "s_waitcnt vmcnt(0)\n"
  "v_readfirstlane_b32 s76, v12\n"
  "v_readlane_b32 s77, v12, 8\n"
  "s_min_u32 s76, s76, s77\n"
  "s_cmp_gt_u32 s76, s56\n"
  "s_cbranch_scc1 PDHK2\n"
  "s_sub_u32 s58, s58, 1\n"
  "s_cbranch_scc1 QUITH\n"
  "s_sleep 2\n"
  "s_branch PLHK2\n"
  "PDHK2:\n"
  "s_add_u32 s59, s59, 0x100\n"
  "global_load_lds_dwordx4 v0, s[40:41] offset:0 sc1\n"
  "global_load_lds_dwordx4 v0, s[40:41] offset:1024 sc1\n"
  "global_load_lds_dwordx4 v0, s[40:41] offset:2048 sc1\n"
  "global_load_lds_dwordx4 v0, s[40:41] offset:3072 sc1\n"
  "s_memrealtime s[76:77]\n"
  "s_waitcnt lgkmcnt(0)\n"
  "s_branch HKOK\n"
  "HBADV:\n"
  "s_waitcnt vmcnt(0) lgkmcnt(0)\n"
  "s_nop 3\n"
  "v_mov_b32 v76, v76\n"
  "s_nop 2\n"
  "v_mov_b32 v76, v76\n"
  "s_nop 2\n"
  "s_mov_b32 s74, 0x7fff7fff\n"
  "v_and_b32 v79, s74, v36\n"
  "v_and_b32 v78, s74, v37\n"
  "v_pk_max_u16 v79, v79, v78\n"
  "v_and_b32 v78, s74, v38\n"
  "v_pk_max_u16 v79, v79, v78\n"
  "v_and_b32 v78, s74, v39\n"
  "v_pk_max_u16 v79, v79, v78\n"
  "v_and_b32 v78, s74, v40\n"
  "v_pk_max_u16 v79, v79, v78\n"
  "v_and_b32 v78, s74, v41\n"
  "v_pk_max_u16 v79, v79, v78\n"
  "v_and_b32 v78, s74, v42\n"
  "v_pk_max_u16 v79, v79, v78\n"
  "v_and_b32 v78, s74, v43\n"
  "v_pk_max_u16 v79, v79, v78\n"
  "v_lshrrev_b32 v78, 16, v79\n"
  "v_and_b32 v79, 0xffff, v79\n"
  "v_max_u32 v79, v79, v78\n"
  "v_cmp_lt_u32 vcc, 0x7f81, v79\n"
  "s_cbranch_vccnz HVL\n"
  "s_mov_b32 s74, 0x7fff7fff\n"
  "s_mov_b32 s75, 0x7f817f81\n"
  "v_and_b32 v79, s74, v82\n"
  "v_pk_min_u16 v79, v79, s75\n"
  "v_bfi_b32 v82, s74, v79, v82\n"
  "v_and_b32 v79, s74, v86\n"
  "v_pk_min_u16 v79, v79, s75\n"
  "v_bfi_b32 v86, s74, v79, v86\n"
  "v_and_b32 v79, s74, v90\n"
  "v_pk_min_u16 v79, v79, s75\n"
  "v_bfi_b32 v90, s74, v79, v90\n"
  "v_and_b32 v79, s74, v94\n"
  "v_pk_min_u16 v79, v79, s75\n"
  "v_bfi_b32 v94, s74, v79, v94\n"
  "v_and_b32 v79, s74, v74\n"
  "v_pk_min_u16 v79, v79, s75\n"
  "v_bfi_b32 v74, s74, v79, v74\n"
  "s_mov_b64 exec, s[38:39]\n"
  "global_store_dword v11, v74, s[70:71] offset:0 sc1\n"
  "s_mov_b64 exec, -1\n"
  "global_store_dword v9, v82, s[70:71] offset:0 sc1\n"
  "global_store_dword v9, v86, s[70:71] offset:128 sc1\n"
  "global_store_dword v10, v90, s[70:71] offset:0 sc1\n"
  "global_store_dword v10, v94, s[70:71] offset:128 sc1\n"
  "s_and_b32 s74, s56, 1\n"
  "s_lshl_b32 s74, s74, 2\n"
  "v_mov_b32 v78, s74\n"
  "s_branch HOKV\n"
  "HVL:\n"
  "s_sub_u32 s58, s58, 1\n"
  "s_cbranch_scc1 QUITH\n"
  "s_add_u32 s59, s59, 0x10000\n"
  "s_mov_b32 s35, 2\n"
  "global_load_dwordx4 v[36:39], v2, s[68:69] offset:0 sc1\n"
  "global_load_dwordx4 v[40:43], v2, s[68:69] offset:1024 sc1\n"
  "s_mov_b32 s74, 0x7fff7fff\n"
  "s_waitcnt vmcnt(1)\n"
  "v_and_b32 v79, s74, v36\n"
  "v_and_b32 v78, s74, v37\n"
  "v_pk_max_u16 v79, v79, v78\n"
  "v_and_b32 v78, s74, v38\n"
  "v_pk_max_u16 v79, v79, v78\n"
  "v_and_b32 v78, s74, v39\n"
  "v_pk_max_u16 v79, v79, v78\n"
  "s_waitcnt vmcnt(0)\n"
  "v_and_b32 v78, s74, v40\n"
  "v_pk_max_u16 v79, v79, v78\n"
  "v_and_b32 v78, s74, v41\n"
  "v_pk_max_u16 v79, v79, v78\n"
  "v_and_b32 v78, s74, v42\n"
  "v_pk_max_u16 v79, v79, v78\n"
  "v_and_b32 v78, s74, v43\n"
  "v_pk_max_u16 v79, v79, v78\n"
  "v_lshrrev_b32 v78, 16, v79\n"
  "v_and_b32 v79, 0xffff, v79\n"
  "v_max_u32 v79, v79, v78\n"
  "v_cmp_lt_u32 vcc, 0x7f81, v79\n"
  "s_cbranch_vccz HREDO\n"
  "s_sleep 4\n"
  "s_branch HVL\n"
  "HREDO:\n"
  "v_mov_b64 v[80:81], 0\n"
  "v_mov_b64 v[82:83], 0\n"
  "v_mov_b64 v[84:85], 0\n"
  "v_mov_b64 v[86:87], 0\n"
  "v_mov_b64 v[88:89], 0\n"
  "v_mov_b64 v[90:91], 0\n"
  "v_mov_b64 v[92:93], 0\n"
  "v_mov_b64 v[94:95], 0\n"
  "v_mov_b64 v[72:73], 0\n"
  "v_mov_b64 v[74:75], 0\n"
  "v_mfma_f32_16x16x32_bf16 v[68:71], v[16:19], v[44:47], 0\n"
  "v_mfma_f32_16x16x32_bf16 v[20:23], a[224:227], v[44:47], 0\n"
  "v_mfma_f32_16x16x32_bf16 v[68:71], v[16:19], v[48:51], v[68:71]\n"
  "v_mfma_f32_16x16x32_bf16 v[20:23], a[228:231], v[48:51], v[20:23]\n"
  "v_mfma_f32_16x16x32_bf16 v[24:27], a[232:235], v[44:47], 0\n"
  "v_mfma_f32_16x16x32_bf16 v[24:27], a[236:239], v[48:51], v[24:27]\n"
  "v_mfma_f32_16x16x32_bf16 v[28:31], a[240:243], v[44:47], 0\n"
  "s_nop 7\n"
  "s_branch HPV\n"
  "HBADZ:\n"
  "s_waitcnt vmcnt(0) lgkmcnt(0)\n"
  "s_nop 3\n"
  "v_mov_b32 v76, v76\n"
  "s_nop 2\n"
  "v_mov_b32 v76, v76\n"
  "s_nop 2\n"
  "s_cmp_eq_u32 s81, s56\n"
  "s_cbranch_scc1 HOKZ\n"
  "s_mov_b32 s81, s56\n"
  "ds_read_b128 v[80:83], v76 offset:0\n"
  "ds_read_b128 v[84:87], v76 offset:1024\n"
  "ds_read_b128 v[88:91], v76 offset:2048\n"
  "ds_read_b128 v[92:95], v76 offset:3072\n"
  "ds_read_b128 v[96:99], v76 offset:4096\n"
  "ds_read_b128 v[100:103], v76 offset:5120\n"
  "ds_read_b128 v[104:107], v76 offset:6144\n"
  "ds_read_b128 v[108:111], v76 offset:7168\n"
  "ds_read_b64_tr_b16 v[20:21], v5 offset:0\n"
  "ds_read_b64_tr_b16 v[22:23], v5 offset:512\n"
  "ds_read_b64_tr_b16 v[24:25], v5 offset:1024\n"
  "ds_read_b64_tr_b16 v[26:27], v5 offset:1536\n"
  "ds_read_b64_tr_b16 v[28:29], v5 offset:2048\n"
  "ds_read_b64_tr_b16 v[30:31], v5 offset:2560\n"
  "ds_read_b64_tr_b16 v[32:33], v5 offset:3072\n"
  "ds_read_b64_tr_b16 v[34:35], v5 offset:3584\n"
  "ds_read_b64_tr_b16 v[44:45], v5 offset:4096\n"
  "ds_read_b64_tr_b16 v[46:47], v5 offset:4608\n"
  "ds_read_b64_tr_b16 v[48:49], v5 offset:5120\n"
  "ds_read_b64_tr_b16 v[50:51], v5 offset:5632\n"
  "s_waitcnt lgkmcnt(10)\n"
  "v_mfma_f32_16x16x32_bf16 v[56:59], v[80:83], v[20:23], 0\n"
  "s_waitcnt lgkmcnt(8)\n"
  "v_mfma_f32_16x16x32_bf16 v[56:59], v[88:91], v[24:27], v[56:59]\n"
  "s_waitcnt lgkmcnt(6)\n"
  "v_mfma_f32_16x16x32_bf16 v[56:59], v[96:99], v[28:31], v[56:59]\n"
  "s_waitcnt lgkmcnt(4)\n"
  "v_mfma_f32_16x16x32_bf16 v[56:59], v[104:107], v[32:35], v[56:59]\n"
  "v_mfma_f32_16x16x32_bf16 v[60:63], v[84:87], v[20:23], 0\n"
  "v_mfma_f32_16x16x32_bf16 v[60:63], v[92:95], v[24:27], v[60:63]\n"
  "v_mfma_f32_16x16x32_bf16 v[60:63], v[100:103], v[28:31], v[60:63]\n"
  "v_mfma_f32_16x16x32_bf16 v[60:63], v[108:111], v[32:35], v[60:63]\n"
  "ds_read_b64_tr_b16 v[96:97], v5 offset:6144\n"
  "ds_read_b64_tr_b16 v[98:99], v5 offset:6656\n"
  "ds_read_b64_tr_b16 v[100:101], v5 offset:7168\n"
  "ds_read_b64_tr_b16 v[102:103], v5 offset:7680\n"
  "s_waitcnt lgkmcnt(2)\n"
  "v_mfma_f32_16x16x32_bf16 v[52:55], v[96:99], v[44:47], 0\n"
  "s_waitcnt lgkmcnt(0)\n"
  "v_mfma_f32_16x16x32_bf16 v[52:55], v[100:103], v[48:51], v[52:55]\n"
  "s_nop 3\n"
  "v_mov_b32 v76, v76\n"
  "s_nop 2\n"
  "v_mul_f32_dpp v64, v52, v77 quad_perm:[0,1,0,1] row_mask:0xf bank_mask:0xf\n"
  "v_mul_f32_dpp v65, v53, v77 quad_perm:[0,1,0,1] row_mask:0xf bank_mask:0xf\n"
  "s_nop 0\n"
  "v_fmac_f32_dpp v64, v54, v77 quad_perm:[2,3,2,3] row_mask:0xf bank_mask:0xf\n"
  "v_fmac_f32_dpp v65, v55, v77 quad_perm:[2,3,2,3] row_mask:0xf bank_mask:0xf\n"
  "v_max3_f32 v79, v56, v57, v58\n"
  "v_max3_f32 v79, v79, v59, v60\n"
  "v_max3_f32 v79, v79, v61, v62\n"
  "v_max3_f32 v79, v79, v63, v64\n"
  "v_max_f32 v79, v79, v65\n"
  "v_mov_b32 v78, v79\n"
  "s_nop 1\n"
  "v_permlane16_swap_b32 v79, v78\n"
  "v_max_f32 v79, v79, v78\n"
  "v_mov_b32 v78, v79\n"
  "s_nop 1\n"
  "v_permlane32_swap_b32 v79, v78\n"
  "v_max_f32 v79, v79, v78\n"
  "v_sub_f32 v56, v56, v79\n"
  "v_sub_f32 v57, v57, v79\n"
  "v_sub_f32 v58, v58, v79\n"
  "v_sub_f32 v59, v59, v79\n"
  "v_sub_f32 v60, v60, v79\n"
  "v_sub_f32 v61, v61, v79\n"
  "v_sub_f32 v62, v62, v79\n"
  "v_sub_f32 v63, v63, v79\n"
  "v_sub_f32 v64, v64, v79\n"
  "v_sub_f32 v65, v65, v79\n"
  "v_exp_f32 v56, v56\n"
  "v_exp_f32 v57, v57\n"
  "v_exp_f32 v58, v58\n"
  "v_exp_f32 v59, v59\n"
  "v_exp_f32 v60, v60\n"
  "v_exp_f32 v61, v61\n"
  "v_exp_f32 v62, v62\n"
  "v_exp_f32 v63, v63\n"
  "v_perm_b32 v44, v57, v56, v7\n"
  "v_perm_b32 v45, v59, v58, v7\n"
  "v_perm_b32 v46, v61, v60, v7\n"
  "v_perm_b32 v47, v63, v62, v7\n"
  "v_perm_b32 v48, v57, v56, v8\n"
  "v_perm_b32 v49, v59, v58, v8\n"
  "v_perm_b32 v50, v61, v60, v8\n"
  "v_perm_b32 v51, v63, v62, v8\n"
  "v_exp_f32 v64, v64\n"
  "v_exp_f32 v65, v65\n"
  "s_nop 0\n"
  "v_perm_b32 v12, v65, v64, v7\n"
  "v_perm_b32 v13, v65, v64, v8\n"
  "s_nop 1\n"
  "s_branch HREDO\n"
  "ENDH:\n"
  "v_lshrrev_b32 v79, 1, v0\n"
  "s_add_u32 s72, s36, s53\n"
  "s_addc_u32 s73, s37, 0\n"
  "s_cmp_lg_u32 s33, 0\n"
  "s_cselect_b64 exec, -1, 0\n"
  "global_store_dwordx2 v79, v[14:15], s[72:73] sc1\n"
  "s_mov_b64 exec, -1\n"
  "s_lshl_b32 s74, s33, 16\n"
  "s_add_u32 s53, s53, s74\n"
  "s_mov_b32 s33, 0\n"
  "s_waitcnt vmcnt(0) lgkmcnt(0)\n"
  "s_barrier\n"
  "s_add_u32 s76, s36, s2\n"
  "s_addc_u32 s77, s37, 0\n"
  "v_mov_b32 v78, s56\n"
  "v_mov_b32 v79, 8192\n"
  "s_cmp_eq_u32 s3, 4\n"
  "s_cselect_b64 exec, 1, 0\n"
  "global_store_dword v79, v78, s[76:77] sc1\n"
  "QUITH:\n"
  "s_endpgm\n"
        : "+{v[0:15]}"(vin), "+{s[36:43]}"(sa), "+{s[44:59]}"(sb) :: CLOB);
    } else {
      const int ci = tk - 16, cv = gam * 16 + ci, h = cv >> 1, vt = (cv & 1) * 4 + wave;
      {
        const int row = 8 * wave + (lane >> 3);
#pragma unroll 6
        for (int j = 0; j < 18; ++j) dma(p.Wv + cv * 64 + (size_t)j * 32 * 4096, row * 8192 + ((lane & 7) ^ (2 * (((row >> 2) & 1) | ((row >> 3) & 2)))) * 16, wave * 1024 + j * 4096);
      }
      const int line = (2 * ci + (gam & 1)) * 4 + (gam >> 1) * 2 + (g & 1), kb = line >> 3, cp = ((lane >> 3) & 1) + 2 * (lane >> 5);
      const unsigned ms = chunk(cls, cp, kb < 8 ? (kb >> 2) : 4, (kb < 8 ? (kb & 3) : 0) * 1024 + (line & 7) * 128 + (lane & 7) * 16);
      const unsigned ms2 = ms + (kb < 8 ? 2 * CH : 1024), inb = chunk(cls, gam, 28 + 4 * ci, 0);
      const unsigned m2 = chunk(cls, gam, 16 + ((h & 7) >> 1), (h & 1) * 2048 + vt * 256);
      vin[1] = m1pat; vin[2] = lane * 4; vin[3] = ms; vin[4] = idx;
      vin[6] = trrow * 128 + ((2 * wave + (c4 >> 1)) ^ (2 * (((trrow >> 2) & 1) | ((trrow >> 3) & 2)))) * 16 + (c4 & 1) * 8;
      vin[7] = line * 128 + (lane & 7) * 16;
#pragma unroll
      for (int i = 0; i < 4; ++i) vin[8 + i] = ((lane & 7) >> 1) == i ? ((lane & 1) ? 0x3f800000u : 0x3f80u) : 0u;
      vin[12] = c4 * 18432 + (16 * g + 4 * wave + qq) * 16; vin[13] = ms2;
      sa[2] = C_VF + cv * 128; sa[5] = 512 + (gam & 1) * 128; sa[6] = 2500; sa[7] = 720;
      sb[0] = chunk(cls, gam, 0, 0) + (wave - 1) * 1024;
      sb[1] = wave == 3 ? chunk(cls, gam, 4, 0) : chunk(cls, gam, 1, (wave + 1) * 1024);
      sb[2] = m2; sb[3] = WO_VP + h * 2048 + vt * 256;
      sb[9] = inb; sb[12] = ci < 9 && wave == 0; sb[13] = wave == 1 ? 3072 : CH - 1024; sb[14] = WO_DB;
      UNIFORM(sa, 8); UNIFORM(sb, 16);
      asm volatile(
  "s_nop 4\n"
  "s_lshl_b32 s80, s48, 10\n"
  "s_waitcnt vmcnt(0)\n"
  "s_barrier\n"
  "v_add_u32 v60, 0x8000, v6\n"
  "ds_read_b64_tr_b16 v[96:97], v6 offset:0\n"
  "ds_read_b64_tr_b16 v[98:99], v6 offset:1024\n"
  "ds_read_b64_tr_b16 v[100:101], v6 offset:4096\n"
  "ds_read_b64_tr_b16 v[102:103], v6 offset:5120\n"
  "ds_read_b64_tr_b16 v[104:105], v6 offset:8192\n"
  "ds_read_b64_tr_b16 v[106:107], v6 offset:9216\n"
  "ds_read_b64_tr_b16 v[108:109], v6 offset:12288\n"
  "ds_read_b64_tr_b16 v[110:111], v6 offset:13312\n"
  "ds_read_b64_tr_b16 v[112:113], v6 offset:16384\n"
  "ds_read_b64_tr_b16 v[114:115], v6 offset:17408\n"
  "ds_read_b64_tr_b16 v[116:117], v6 offset:20480\n"
  "ds_read_b64_tr_b16 v[118:119], v6 offset:21504\n"
  "ds_read_b64_tr_b16 v[120:121], v6 offset:24576\n"
  "ds_read_b64_tr_b16 v[122:123], v6 offset:25600\n"
  "ds_read_b64_tr_b16 v[124:125], v6 offset:28672\n"
  "ds_read_b64_tr_b16 v[126:127], v6 offset:29696\n"
  "ds_read_b64_tr_b16 v[128:129], v60 offset:0\n"
  "ds_read_b64_tr_b16 v[130:131], v60 offset:1024\n"
  "ds_read_b64_tr_b16 v[132:133], v60 offset:4096\n"
  "ds_read_b64_tr_b16 v[134:135], v60 offset:5120\n"
  "ds_read_b64_tr_b16 v[136:137], v60 offset:8192\n"
  "ds_read_b64_tr_b16 v[138:139], v60 offset:9216\n"
  "ds_read_b64_tr_b16 v[140:141], v60 offset:12288\n"
  "ds_read_b64_tr_b16 v[142:143], v60 offset:13312\n"
  "ds_read_b64_tr_b16 v[144:145], v60 offset:16384\n"
  "ds_read_b64_tr_b16 v[146:147], v60 offset:17408\n"
  "ds_read_b64_tr_b16 v[148:149], v60 offset:20480\n"
  "ds_read_b64_tr_b16 v[150:151], v60 offset:21504\n"
  "ds_read_b64_tr_b16 v[152:153], v60 offset:24576\n"
  "ds_read_b64_tr_b16 v[154:155], v60 offset:25600\n"
  "ds_read_b64_tr_b16 v[156:157], v60 offset:28672\n"
  "ds_read_b64_tr_b16 v[158:159], v60 offset:29696\n"
  "ds_read_b64_tr_b16 v[160:161], v60 offset:32768\n"
  "ds_read_b64_tr_b16 v[162:163], v60 offset:33792\n"
  "ds_read_b64_tr_b16 v[164:165], v60 offset:36864\n"
  "ds_read_b64_tr_b16 v[166:167], v60 offset:37888\n"
  "v_mov_b32 v64, -1\n"
  "v_mov_b32 v65, -1\n"
  "v_mov_b32 v66, -1\n"
  "v_mov_b32 v67, -1\n"
  "s_mov_b32 s7, s41\n"
  "s_mov_b32 s14, s42\n"
  "s_mov_b32 s15, s43\n"
  "s_mov_b32 s6, 0\n"
  "s_mov_b32 s40, 0\n"
  "s_mov_b32 s41, 0\n"
  "s_mov_b32 s42, 100000\n"
  "s_mov_b32 s43, 0\n"
  "s_waitcnt lgkmcnt(0)\n"
  "s_barrier\n"
  "s_mul_i32 s74, s48, 18432\n"
  "v_add_u32 v60, s74, v0\n"
  "ds_write_b128 v60, v[96:99] offset:0\n"
  "ds_write_b128 v60, v[100:103] offset:1024\n"
  "ds_write_b128 v60, v[104:107] offset:2048\n"
  "ds_write_b128 v60, v[108:111] offset:3072\n"
  "ds_write_b128 v60, v[112:115] offset:4096\n"
  "ds_write_b128 v60, v[116:119] offset:5120\n"
  "ds_write_b128 v60, v[120:123] offset:6144\n"
  "ds_write_b128 v60, v[124:127] offset:7168\n"
  "ds_write_b128 v60, v[128:131] offset:8192\n"
  "ds_write_b128 v60, v[132:135] offset:9216\n"
  "ds_write_b128 v60, v[136:139] offset:10240\n"
  "ds_write_b128 v60, v[140:143] offset:11264\n"
  "ds_write_b128 v60, v[144:147] offset:12288\n"
  "ds_write_b128 v60, v[148:151] offset:13312\n"
  "ds_write_b128 v60, v[152:155] offset:14336\n"
  "ds_write_b128 v60, v[156:159] offset:15360\n"
  "ds_write_b128 v60, v[160:163] offset:16384\n"
  "ds_write_b128 v60, v[164:167] offset:17408\n"
  "s_waitcnt lgkmcnt(0)\n"
  "s_barrier\n"
  "ds_read_b128 v[96:99], v12 offset:0\n"
  "ds_read_b128 v[100:103], v12 offset:1024\n"
  "ds_read_b128 v[104:107], v12 offset:2048\n"
  "ds_read_b128 v[108:111], v12 offset:3072\n"
  "ds_read_b128 v[112:115], v12 offset:4096\n"
  "ds_read_b128 v[116:119], v12 offset:5120\n"
  "ds_read_b128 v[120:123], v12 offset:6144\n"
  "ds_read_b128 v[124:127], v12 offset:7168\n"
  "ds_read_b128 v[128:131], v12 offset:8192\n"
  "ds_read_b128 v[132:135], v12 offset:9216\n"
  "ds_read_b128 v[136:139], v12 offset:10240\n"
  "ds_read_b128 v[140:143], v12 offset:11264\n"
  "ds_read_b128 v[144:147], v12 offset:12288\n"
  "ds_read_b128 v[148:151], v12 offset:13312\n"
  "ds_read_b128 v[152:155], v12 offset:14336\n"
  "ds_read_b128 v[156:159], v12 offset:15360\n"
  "ds_read_b128 v[160:163], v12 offset:16384\n"
  "ds_read_b128 v[164:167], v12 offset:17408\n"
  "s_waitcnt lgkmcnt(0)\n"
  "s_barrier\n"
  "v_mov_b32 v60, 0\n"
  "v_mov_b32 v61, s49\n"
  "ds_write_b32 v60, v61 offset:9232\n"
  "s_sub_u32 s80, s80, 1024\n"
  "RLOOP:\n"
  "s_mov_b32 s52, 0\n"
  "s_mov_b32 s98, s49\n"
  "RTRY:\n"
  "v_mov_b64 v[56:57], 0\n"
  "v_mov_b64 v[58:59], 0\n"
  "s_mul_i32 s74, s41, 131072\n"
  "s_add_u32 s74, s74, s46\n"
  "s_add_u32 s68, s36, s74\n"
  "s_addc_u32 s69, s37, 0\n"
  "v_mov_b32 v61, 0\n"
  "s_cmp_eq_u32 s48, 0\n"
  "s_cbranch_scc1 NDV\n"
  "s_mul_i32 s78, s41, 163840\n"
  "s_lshl_b32 s79, s52, 16\n"
  "s_add_u32 s79, s79, s78\n"
  "s_add_u32 s79, s79, s44\n"
  "s_add_u32 s62, s36, s79\n"
  "s_addc_u32 s63, s37, 0\n"
  "s_add_u32 s64, s62, s57\n"
  "s_addc_u32 s65, s63, 0\n"
  "s_add_u32 s79, s78, s45\n"
  "s_cmp_eq_u32 s48, 3\n"
  "s_cselect_b32 s74, 10, 16\n"
  "s_lshl_b32 s74, s52, s74\n"
  "s_add_u32 s79, s79, s74\n"
  "s_add_u32 s66, s36, s79\n"
  "s_addc_u32 s67, s37, 0\n"
  "s_add_u32 m0, s80, 0\n"
  "s_add_u32 s79, s80, 3072\n"
  "s_add_u32 s78, s80, 6144\n"
  "s_add_u32 s81, s49, 0\n"
  "WV:\n"
  "s_memrealtime s[74:75]\n"
  "s_waitcnt lgkmcnt(0)\n"
  "s_mul_i32 s74, s74, 24\n"
  "s_sub_i32 s74, s81, s74\n"
  "s_cmp_lt_i32 s74, 742\n"
  "s_cbranch_scc1 WFV\n"
  "s_cmp_gt_i32 s74, 0x40000\n"
  "s_cbranch_scc1 WEV\n"
  "s_sleep 2\n"
  "s_branch WV\n"
  "WFV:\n"
  "s_cmp_lt_i32 s74, 100\n"
  "s_cbranch_scc1 WEV\n"
  "s_memtime s[76:77]\n"
  "s_memrealtime s[20:21]\n"
  "s_nop 2\n"
  "s_memrealtime s[22:23]\n"
  "s_nop 2\n"
  "s_memrealtime s[24:25]\n"
  "s_nop 2\n"
  "s_memrealtime s[26:27]\n"
  "s_nop 2\n"
  "s_memrealtime s[28:29]\n"
  "s_nop 2\n"
  "s_memrealtime s[30:31]\n"
  "s_waitcnt lgkmcnt(0)\n"
  "s_add_u32 s20, s20, s22\n"
  "s_add_u32 s24, s24, s26\n"
  "s_add_u32 s28, s28, s30\n"
  "s_add_u32 s20, s20, s24\n"
  "s_add_u32 s20, s20, s28\n"
  "s_lshl_b32 s74, s20, 2\n"
  "s_nop 0\n"
  "s_sub_i32 s74, s81, s74\n"
  "s_sub_i32 s74, s74, 230\n"
  "s_max_i32 s74, s74, 0\n"
  "s_lshr_b32 s74, s74, 3\n"
  "s_lshl_b32 s74, s74, 2\n"
  "s_sub_i32 s74, 268, s74\n"
  "s_getpc_b64 s[76:77]\n"
  "s_add_u32 s76, s76, s74\n"
  "s_addc_u32 s77, s77, 0\n"
  "s_setpc_b64 s[76:77]\n"
  "s_nop 1\n"
  "s_nop 1\n"
  "s_nop 1\n"
  "s_nop 1\n"
  "s_nop 1\n"
  "s_nop 1\n"
  "s_nop 1\n"
  "s_nop 1\n"
  "s_nop 1\n"
  "s_nop 1\n"
  "s_nop 1\n"
  "s_nop 1\n"
  "s_nop 1\n"
  "s_nop 1\n"
  "s_nop 1\n"
  "s_nop 1\n"
  "s_nop 1\n"
  "s_nop 1\n"
  "s_nop 1\n"
  "s_nop 1\n"
  "s_nop 1\n"
  "s_nop 1\n"
  "s_nop 1\n"
  "s_nop 1\n"
  "s_nop 1\n"
  "s_nop 1\n"
  "s_nop 1\n"
  "s_nop 1\n"
  "s_nop 1\n"
  "s_nop 1\n"
  "s_nop 1\n"
  "s_nop 1\n"
  "s_nop 1\n"
  "s_nop 1\n"
  "s_nop 1\n"
  "s_nop 1\n"
  "s_nop 1\n"
  "s_nop 1\n"
  "s_nop 1\n"
  "s_nop 1\n"
  "s_nop 1\n"
  "s_nop 1\n"
  "s_nop 1\n"
  "s_nop 1\n"
  "s_nop 1\n"
  "s_nop 1\n"
  "s_nop 1\n"
  "s_nop 1\n"
  "s_nop 1\n"
  "s_nop 1\n"
  "s_nop 1\n"
  "s_nop 1\n"
  "s_nop 1\n"
  "s_nop 1\n"
  "s_nop 1\n"
  "s_nop 1\n"
  "s_nop 1\n"
  "s_nop 1\n"
  "s_nop 1\n"
  "s_nop 1\n"
  "s_nop 1\n"
  "s_nop 1\n"
  "s_nop 1\n"
  "s_nop 1\n"
  "WEV:\n"
  "global_load_lds_dwordx4 v1, s[62:63] sc1\n"
  "s_mov_b32 m0, s79\n"
  "s_nop 0\n"
  "global_load_lds_dwordx4 v1, s[64:65] sc1\n"
  "s_mov_b32 m0, s78\n"
  "s_nop 0\n"
  "global_load_lds_dwordx4 v1, s[66:67] sc1\n"
  "s_waitcnt vmcnt(2)\n"
  "NDV:\n"
  "s_waitcnt lgkmcnt(0)\n"
  "s_barrier\n"
  "ds_read_b32 v60, v61 offset:9232\n"
  "ds_read_b128 v[20:23], v0 offset:0\n"
  "ds_read_b128 v[24:27], v0 offset:1024\n"
  "ds_read_b128 v[28:31], v0 offset:2048\n"
  "s_waitcnt lgkmcnt(3)\n"
  "v_readfirstlane_b32 s74, v60\n"
  "s_sub_i32 s75, s74, s49\n"
  "s_cmp_gt_i32 s75, 0\n"
  "s_cbranch_scc1 RLATE\n"
  "s_cmp_eq_u32 s48, 0\n"
  "s_cbranch_scc1 RNW\n"
  "s_waitcnt vmcnt(0)\n"
  "RNW:\n"
  "s_barrier\n"
  "ds_read_b128 v[32:35], v0 offset:3072\n"
  "ds_read_b128 v[36:39], v0 offset:4096\n"
  "s_waitcnt lgkmcnt(4)\n"
  "v_smfmac_f32_16x16x64_bf16 v[56:59], v[20:23], v[96:103], v4\n"
  "ds_read_b128 v[40:43], v0 offset:5120\n"
  "s_waitcnt lgkmcnt(4)\n"
  "v_smfmac_f32_16x16x64_bf16 v[56:59], v[24:27], v[104:111], v4\n"
  "ds_read_b128 v[44:47], v0 offset:6144\n"
  "s_waitcnt lgkmcnt(4)\n"
  "v_smfmac_f32_16x16x64_bf16 v[56:59], v[28:31], v[112:119], v4\n"
  "ds_read_b128 v[48:51], v0 offset:7168\n"
  "s_waitcnt lgkmcnt(4)\n"
  "v_smfmac_f32_16x16x64_bf16 v[56:59], v[32:35], v[120:127], v4\n"
  "ds_read_b128 v[52:55], v0 offset:8192\n"
  "s_waitcnt lgkmcnt(4)\n"
  "v_smfmac_f32_16x16x64_bf16 v[56:59], v[36:39], v[128:135], v4\n"
  "s_waitcnt lgkmcnt(3)\n"
  "v_smfmac_f32_16x16x64_bf16 v[56:59], v[40:43], v[136:143], v4\n"
  "s_waitcnt lgkmcnt(2)\n"
  "v_smfmac_f32_16x16x64_bf16 v[56:59], v[44:47], v[144:151], v4\n"
  "s_waitcnt lgkmcnt(1)\n"
  "v_smfmac_f32_16x16x64_bf16 v[56:59], v[48:51], v[152:159], v4\n"
  "s_waitcnt lgkmcnt(0)\n"
  "v_smfmac_f32_16x16x64_bf16 v[56:59], v[52:55], v[160:167], v4\n"
  "s_nop 3\n"
  "v_mov_b32 v7, v7\n"
  "s_nop 2\n"
  "v_add_f32 v56, v56, v58\n"
  "v_add_f32 v57, v57, v59\n"
  "v_cvt_pk_bf16_f32 v58, v56, v57\n"
  "global_store_dword v2, v58, s[68:69] sc1\n"
  "v_cmp_u_f32 vcc, v56, v57\n"
  "s_cbranch_vccnz RBADM\n"
  "ROKM:\n"
  "s_lshl_b32 s74, s40, 10\n"
  "s_add_u32 s74, s74, 16785408\n"
  "s_add_u32 s18, s36, s74\n"
  "s_addc_u32 s19, s37, 0\n"
  "global_load_dword v59, v0, s[18:19] sc1\n"
  "s_add_u32 s74, s36, s47\n"
  "s_addc_u32 s75, s37, 0\n"
  "global_store_dword v2, v58, s[74:75] sc1\n"
  "s_add_u32 s47, s47, 65536\n"
  "s_add_u32 s74, s41, 2\n"
  "s_cmp_ge_u32 s74, 3\n"
  "s_cselect_b32 s75, 3, 0\n"
  "s_sub_u32 s74, s74, s75\n"
  "s_mul_i32 s74, s74, 131072\n"
  "s_add_u32 s76, s74, s46\n"
  "s_add_u32 s74, s36, s76\n"
  "s_addc_u32 s75, s37, 0\n"
  "global_store_dword v2, v64, s[74:75] sc1\n"
  "s_waitcnt vmcnt(2)\n"
  "v_readlane_b32 s8, v59, 0\n"
  "v_readlane_b32 s9, v59, 8\n"
  "v_readlane_b32 s10, v59, 16\n"
  "v_readlane_b32 s11, v59, 24\n"
  "v_readlane_b32 s12, v59, 32\n"
  "v_readlane_b32 s13, v59, 40\n"
  "s_max_u32 s8, s8, s9\n"
  "s_max_u32 s10, s10, s11\n"
  "s_max_u32 s8, s8, s10\n"
  "s_max_u32 s12, s12, s13\n"
  "s_cmp_eq_u32 s8, 0\n"
  "s_cselect_b32 s8, s14, s8\n"
  "s_max_u32 s74, s8, s14\n"
  "s_mov_b32 s14, s8\n"
  "s_mov_b32 s8, s74\n"
  "s_cmp_eq_u32 s12, 0\n"
  "s_cselect_b32 s12, s15, s12\n"
  "s_max_u32 s74, s12, s15\n"
  "s_mov_b32 s15, s12\n"
  "s_mov_b32 s12, s74\n"
  "s_add_u32 s55, s8, 150\n"
  "s_add_u32 s50, s55, s12\n"
  "s_add_u32 s50, s50, 230\n"
  "s_add_u32 s74, s36, s38\n"
  "s_addc_u32 s75, s37, 0\n"
  "v_mov_b32 v60, 0\n"
  "v_mov_b32 v61, s40\n"
  "s_cmp_eq_u32 s48, 1\n"
  "s_cselect_b64 exec, 1, 0\n"
  "global_store_dword v60, v61, s[74:75] sc1\n"
  "s_mov_b64 exec, -1\n"
  "s_add_u32 s40, s40, 1\n"
  "s_cmp_eq_u32 s40, 64\n"
  "s_cbranch_scc1 ENDV\n"
  "s_add_u32 s81, s49, s55\n"
  "s_add_u32 s49, s49, s50\n"
  "s_mov_b32 s78, s41\n"
  "s_add_u32 s41, s41, 1\n"
  "s_cmp_eq_u32 s41, 3\n"
  "s_cselect_b32 s41, 0, s41\n"
  "s_cmp_lg_u32 s56, 0\n"
  "s_cbranch_scc1 RUNIT\n"
  "s_waitcnt vmcnt(0)\n"
  "s_branch RTAIL\n"
  "RUNIT:\n"
  "s_cmp_eq_u32 s40, 1\n"
  "s_cbranch_scc1 RNOA\n"
  "v_mov_b32 v60, s7\n"
  "v_mov_b32 v61, s6\n"
  "s_mov_b64 exec, 1\n"
  "global_atomic_umax v60, v61, s[18:19] offset:2048 sc1\n"
  "s_mov_b64 exec, -1\n"
  "global_store_dwordx4 v0, v[64:67], s[70:71] offset:0 sc1\n"
  "global_store_dwordx4 v0, v[64:67], s[70:71] offset:1024 sc1\n"
  "global_store_dwordx4 v0, v[64:67], s[70:71] offset:2048 sc1\n"
  "global_store_dwordx4 v0, v[64:67], s[70:71] offset:3072 sc1\n"
  "global_store_dwordx4 v0, v[64:67], s[66:67] offset:0 sc1\n"
  "global_store_dwordx4 v0, v[64:67], s[66:67] offset:1024 sc1\n"
  "global_store_dwordx4 v0, v[64:67], s[66:67] offset:2048 sc1\n"
  "global_store_dwordx4 v0, v[64:67], s[66:67] offset:3072 sc1\n"
  "global_store_dwordx4 v3, v[64:67], s[62:63] sc1\n"
  "global_store_dwordx4 v13, v[64:67], s[62:63] sc1\n"
  "RNOA:\n"
  "s_mul_i32 s78, s78, 163840\n"
  "s_add_u32 s62, s36, s78\n"
  "s_addc_u32 s63, s37, 0\n"
  "s_mul_i32 s78, s41, 163840\n"
  "s_add_u32 s72, s36, s78\n"
  "s_addc_u32 s73, s37, 0\n"
  "s_and_b32 s74, s40, 1\n"
  "s_xor_b32 s74, s74, 1\n"
  "s_lshl_b32 s74, s74, 16\n"
  "s_add_u32 s74, s74, s53\n"
  "s_add_u32 s70, s36, s74\n"
  "s_addc_u32 s71, s37, 0\n"
  "s_add_u32 s66, s70, 32768\n"
  "s_addc_u32 s67, s71, 0\n"
  "s_memrealtime s[74:75]\n"
  "s_waitcnt lgkmcnt(0)\n"
  "s_mul_i32 s78, s74, 24\n"
  "s_add_u32 s78, s78, 480\n"
  "s_sub_i32 s74, s78, s81\n"
  "s_cmp_gt_i32 s74, 0\n"
  "s_cselect_b32 s78, s78, s81\n"
  "s_sub_i32 s64, s49, s78\n"
  "s_sub_i32 s64, s64, 230\n"
  "WR3:\n"
  "s_memrealtime s[74:75]\n"
  "s_waitcnt lgkmcnt(0)\n"
  "s_mul_i32 s74, s74, 24\n"
  "s_sub_i32 s74, s81, s74\n"
  "s_cmp_lt_i32 s74, 742\n"
  "s_cbranch_scc1 WFR3\n"
  "s_cmp_gt_i32 s74, 0x40000\n"
  "s_cbranch_scc1 WER3\n"
  "s_sleep 2\n"
  "s_branch WR3\n"
  "WFR3:\n"
  "s_cmp_lt_i32 s74, 100\n"
  "s_cbranch_scc1 WER3\n"
  "s_memtime s[76:77]\n"
  "s_memrealtime s[20:21]\n"
  "s_nop 2\n"
  "s_memrealtime s[22:23]\n"
  "s_nop 2\n"
  "s_memrealtime s[24:25]\n"
  "s_nop 2\n"
  "s_memrealtime s[26:27]\n"
  "s_nop 2\n"
  "s_memrealtime s[28:29]\n"
  "s_nop 2\n"
  "s_memrealtime s[30:31]\n"
  "s_waitcnt lgkmcnt(0)\n"
  "s_add_u32 s20, s20, s22\n"
  "s_add_u32 s24, s24, s26\n"
  "s_add_u32 s28, s28, s30\n"
  "s_add_u32 s20, s20, s24\n"
  "s_add_u32 s20, s20, s28\n"
  "s_lshl_b32 s74, s20, 2\n"
  "s_nop 0\n"
  "s_sub_i32 s74, s81, s74\n"
  "s_sub_i32 s74, s74, 230\n"
  "s_max_i32 s74, s74, 0\n"
  "s_lshr_b32 s74, s74, 3\n"
  "s_lshl_b32 s74, s74, 2\n"
  "s_sub_i32 s74, 268, s74\n"
  "s_getpc_b64 s[76:77]\n"
  "s_add_u32 s76, s76, s74\n"
  "s_addc_u32 s77, s77, 0\n"
  "s_setpc_b64 s[76:77]\n"
  "s_nop 1\n"
  "s_nop 1\n"
  "s_nop 1\n"
  "s_nop 1\n"
  "s_nop 1\n"
  "s_nop 1\n"
  "s_nop 1\n"
  "s_nop 1\n"
  "s_nop 1\n"
  "s_nop 1\n"
  "s_nop 1\n"
  "s_nop 1\n"
  "s_nop 1\n"
  "s_nop 1\n"
  "s_nop 1\n"
  "s_nop 1\n"
  "s_nop 1\n"
  "s_nop 1\n"
  "s_nop 1\n"
  "s_nop 1\n"
  "s_nop 1\n"
  "s_nop 1\n"
  "s_nop 1\n"
  "s_nop 1\n"
  "s_nop 1\n"
  "s_nop 1\n"
  "s_nop 1\n"
  "s_nop 1\n"
  "s_nop 1\n"
  "s_nop 1\n"
  "s_nop 1\n"
  "s_nop 1\n"
  "s_nop 1\n"
  "s_nop 1\n"
  "s_nop 1\n"
  "s_nop 1\n"
  "s_nop 1\n"
  "s_nop 1\n"
  "s_nop 1\n"
  "s_nop 1\n"
  "s_nop 1\n"
  "s_nop 1\n"
  "s_nop 1\n"
  "s_nop 1\n"
  "s_nop 1\n"
  "s_nop 1\n"
  "s_nop 1\n"
  "s_nop 1\n"
  "s_nop 1\n"
  "s_nop 1\n"
  "s_nop 1\n"
  "s_nop 1\n"
  "s_nop 1\n"
  "s_nop 1\n"
  "s_nop 1\n"
  "s_nop 1\n"
  "s_nop 1\n"
  "s_nop 1\n"
  "s_nop 1\n"
  "s_nop 1\n"
  "s_nop 1\n"
  "s_nop 1\n"
  "s_nop 1\n"
  "s_nop 1\n"
  "WER3:\n"
  "s_memtime s[76:77]\n"
  "global_load_dwordx4 v[168:171], v0, s[70:71] offset:0 sc1\n"
  "global_load_dwordx4 v[172:175], v0, s[70:71] offset:1024 sc1\n"
  "global_load_dwordx4 v[176:179], v0, s[70:71] offset:2048 sc1\n"
  "global_load_dwordx4 v[180:183], v0, s[70:71] offset:3072 sc1\n"
  "global_load_dwordx4 v[184:187], v0, s[66:67] offset:0 sc1\n"
  "global_load_dwordx4 v[188:191], v0, s[66:67] offset:1024 sc1\n"
  "global_load_dwordx4 v[192:195], v0, s[66:67] offset:2048 sc1\n"
  "global_load_dwordx4 v[196:199], v0, s[66:67] offset:3072 sc1\n"
  "s_waitcnt vmcnt(7)\n"
  "v_mfma_f32_16x16x32_bf16 v[84:87], v[168:171], v[8:11], 0\n"
  "s_waitcnt vmcnt(6)\n"
  "v_mfma_f32_16x16x32_bf16 v[84:87], v[172:175], v[8:11], v[84:87]\n"
  "s_waitcnt vmcnt(5)\n"
  "v_mfma_f32_16x16x32_bf16 v[84:87], v[176:179], v[8:11], v[84:87]\n"
  "s_waitcnt vmcnt(4)\n"
  "v_mfma_f32_16x16x32_bf16 v[84:87], v[180:183], v[8:11], v[84:87]\n"
  "s_waitcnt vmcnt(3)\n"
  "v_mfma_f32_16x16x32_bf16 v[84:87], v[184:187], v[8:11], v[84:87]\n"
  "s_memtime s[74:75]\n"
  "s_waitcnt vmcnt(2)\n"
  "v_mfma_f32_16x16x32_bf16 v[84:87], v[188:191], v[8:11], v[84:87]\n"
  "s_waitcnt vmcnt(1)\n"
  "v_mfma_f32_16x16x32_bf16 v[84:87], v[192:195], v[8:11], v[84:87]\n"
  "s_waitcnt vmcnt(0)\n"
  "v_mfma_f32_16x16x32_bf16 v[84:87], v[196:199], v[8:11], v[84:87]\n"
  "s_waitcnt lgkmcnt(0)\n"
  "s_sub_i32 s74, s74, s76\n"
  "s_cmp_gt_i32 s74, s64\n"
  "s_cbranch_scc1 RSLOW\n"
  "s_nop 3\n"
  "v_cvt_pk_bf16_f32 v88, v84, v85\n"
  "v_cvt_pk_bf16_f32 v89, v86, v87\n"
  "v_cvt_pk_bf16_f32 v90, v84, v85\n"
  "v_cvt_pk_bf16_f32 v91, v86, v87\n"
  "s_nop 0\n"
  "v_permlane32_swap_b32 v88, v90\n"
  "v_permlane32_swap_b32 v89, v91\n"
  "global_store_dwordx4 v3, v[88:91], s[72:73] sc1\n"
  "s_memtime s[74:75]\n"
  "global_store_dwordx4 v13, v[88:91], s[72:73] sc1\n"
  "v_add_f32 v60, v84, v85\n"
  "v_add_f32 v61, v86, v87\n"
  "v_cmp_u_f32 vcc, v60, v61\n"
  "s_cbranch_vccnz RAGAIN\n"
  "s_waitcnt lgkmcnt(0)\n"
  "s_sub_u32 s6, s74, s76\n"
  "RDONE:\n"
  "RTAIL:\n"
  "s_branch RLOOP\n"
  "RBADM:\n"
  "s_waitcnt lgkmcnt(0)\n"
  "s_nop 3\n"
  "v_mov_b32 v7, v7\n"
  "s_nop 2\n"
  "v_mov_b32 v7, v7\n"
  "s_nop 2\n"
  "s_mov_b32 s74, 0xff820000\n"
  "v_max3_u32 v60, v20, v21, v22\n"
  "v_max3_u32 v60, v60, v23, v24\n"
  "v_max3_u32 v60, v60, v25, v26\n"
  "v_max3_u32 v60, v60, v27, v28\n"
  "v_max3_u32 v60, v60, v29, v30\n"
  "v_max3_u32 v60, v60, v31, v32\n"
  "v_max3_u32 v60, v60, v33, v34\n"
  "v_max3_u32 v60, v60, v35, v36\n"
  "v_max3_u32 v60, v60, v37, v38\n"
  "v_max3_u32 v60, v60, v39, v40\n"
  "v_max3_u32 v60, v60, v41, v42\n"
  "v_max3_u32 v60, v60, v43, v44\n"
  "v_max3_u32 v60, v60, v45, v46\n"
  "v_max3_u32 v60, v60, v47, v48\n"
  "v_max3_u32 v60, v60, v49, v50\n"
  "v_max3_u32 v60, v60, v51, v52\n"
  "v_max3_u32 v60, v60, v53, v54\n"
  "v_max_u32 v60, v60, v55\n"
  "v_cmp_le_u32 vcc, s74, v60\n"
  "s_cbranch_vccnz REV\n"
  "s_mov_b32 s74, 0x7fff7fff\n"
  "v_and_b32 v60, s74, v20\n"
  "v_and_b32 v61, s74, v21\n"
  "v_pk_max_u16 v60, v60, v61\n"
  "v_and_b32 v61, s74, v22\n"
  "v_pk_max_u16 v60, v60, v61\n"
  "v_and_b32 v61, s74, v23\n"
  "v_pk_max_u16 v60, v60, v61\n"
  "v_and_b32 v61, s74, v24\n"
  "v_pk_max_u16 v60, v60, v61\n"
  "v_and_b32 v61, s74, v25\n"
  "v_pk_max_u16 v60, v60, v61\n"
  "v_and_b32 v61, s74, v26\n"
  "v_pk_max_u16 v60, v60, v61\n"
  "v_and_b32 v61, s74, v27\n"
  "v_pk_max_u16 v60, v60, v61\n"
  "v_and_b32 v61, s74, v28\n"
  "v_pk_max_u16 v60, v60, v61\n"
  "v_and_b32 v61, s74, v29\n"
  "v_pk_max_u16 v60, v60, v61\n"
  "v_and_b32 v61, s74, v30\n"
  "v_pk_max_u16 v60, v60, v61\n"
  "v_and_b32 v61, s74, v31\n"
  "v_pk_max_u16 v60, v60, v61\n"
  "v_and_b32 v61, s74, v32\n"
  "v_pk_max_u16 v60, v60, v61\n"
  "v_and_b32 v61, s74, v33\n"
  "v_pk_max_u16 v60, v60, v61\n"
  "v_and_b32 v61, s74, v34\n"
  "v_pk_max_u16 v60, v60, v61\n"
  "v_and_b32 v61, s74, v35\n"
  "v_pk_max_u16 v60, v60, v61\n"
  "v_and_b32 v61, s74, v36\n"
  "v_pk_max_u16 v60, v60, v61\n"
  "v_and_b32 v61, s74, v37\n"
  "v_pk_max_u16 v60, v60, v61\n"
  "v_and_b32 v61, s74, v38\n"
  "v_pk_max_u16 v60, v60, v61\n"
  "v_and_b32 v61, s74, v39\n"
  "v_pk_max_u16 v60, v60, v61\n"
  "v_and_b32 v61, s74, v40\n"
  "v_pk_max_u16 v60, v60, v61\n"
  "v_and_b32 v61, s74, v41\n"
  "v_pk_max_u16 v60, v60, v61\n"
  "v_and_b32 v61, s74, v42\n"
  "v_pk_max_u16 v60, v60, v61\n"
  "v_and_b32 v61, s74, v43\n"
  "v_pk_max_u16 v60, v60, v61\n"
  "v_and_b32 v61, s74, v44\n"
  "v_pk_max_u16 v60, v60, v61\n"
  "v_and_b32 v61, s74, v45\n"
  "v_pk_max_u16 v60, v60, v61\n"
  "v_and_b32 v61, s74, v46\n"
  "v_pk_max_u16 v60, v60, v61\n"
  "v_and_b32 v61, s74, v47\n"
  "v_pk_max_u16 v60, v60, v61\n"
  "v_and_b32 v61, s74, v48\n"
  "v_pk_max_u16 v60, v60, v61\n"
  "v_and_b32 v61, s74, v49\n"
  "v_pk_max_u16 v60, v60, v61\n"
  "v_and_b32 v61, s74, v50\n"
  "v_pk_max_u16 v60, v60, v61\n"
  "v_and_b32 v61, s74, v51\n"
  "v_pk_max_u16 v60, v60, v61\n"
  "v_and_b32 v61, s74, v52\n"
  "v_pk_max_u16 v60, v60, v61\n"
  "v_and_b32 v61, s74, v53\n"
  "v_pk_max_u16 v60, v60, v61\n"
  "v_and_b32 v61, s74, v54\n"
  "v_pk_max_u16 v60, v60, v61\n"
  "v_and_b32 v61, s74, v55\n"
  "v_pk_max_u16 v60, v60, v61\n"
  "v_lshrrev_b32 v61, 16, v60\n"
  "v_and_b32 v60, 0xffff, v60\n"
  "v_max_u32 v60, v60, v61\n"
  "v_cmp_lt_u32 vcc, 0x7f81, v60\n"
  "s_cbranch_vccnz REV\n"
  "s_mov_b32 s74, 0x7fff7fff\n"
  "s_mov_b32 s75, 0x7f817f81\n"
  "v_and_b32 v60, s74, v58\n"
  "v_pk_min_u16 v60, v60, s75\n"
  "v_bfi_b32 v58, s74, v60, v58\n"
  "s_waitcnt vmcnt(0)\n"
  "global_store_dword v2, v58, s[68:69] sc1\n"
  "s_branch ROKM\n"
  "REV:\n"
  "s_sub_u32 s42, s42, 1\n"
  "s_cbranch_scc1 QUITV\n"
  "s_add_u32 s43, s43, 1\n"
  "s_memrealtime s[74:75]\n"
  "s_waitcnt lgkmcnt(0)\n"
  "s_mul_i32 s78, s74, 24\n"
  "s_add_u32 s78, s78, 650\n"
  "RSV:\n"
  "s_add_u32 s49, s49, s51\n"
  "s_sub_i32 s74, s49, s78\n"
  "s_cmp_lt_i32 s74, 0\n"
  "s_cbranch_scc1 RSV\n"
  "AGV:\n"
  "v_mov_b32 v60, s49\n"
  "v_mov_b32 v61, s48\n"
  "v_lshlrev_b32 v61, 2, v61\n"
  "ds_write_b32 v61, v60 offset:9216\n"
  "s_waitcnt lgkmcnt(0)\n"
  "s_barrier\n"
  "v_mov_b32 v61, 0\n"
  "ds_read_b128 v[20:23], v61 offset:9216\n"
  "s_waitcnt lgkmcnt(0)\n"
  "v_readfirstlane_b32 s74, v20\n"
  "s_sub_i32 s75, s74, s49\n"
  "s_cmp_gt_i32 s75, 0\n"
  "s_cselect_b32 s49, s74, s49\n"
  "v_readfirstlane_b32 s74, v21\n"
  "s_sub_i32 s75, s74, s49\n"
  "s_cmp_gt_i32 s75, 0\n"
  "s_cselect_b32 s49, s74, s49\n"
  "v_readfirstlane_b32 s74, v22\n"
  "s_sub_i32 s75, s74, s49\n"
  "s_cmp_gt_i32 s75, 0\n"
  "s_cselect_b32 s49, s74, s49\n"
  "v_readfirstlane_b32 s74, v23\n"
  "s_sub_i32 s75, s74, s49\n"
  "s_cmp_gt_i32 s75, 0\n"
  "s_cselect_b32 s49, s74, s49\n"
  "s_cmp_lg_u32 s49, s98\n"
  "s_cselect_b32 s52, 1, 0\n"
  "s_branch RTRY\n"
  "RLATE:\n"
  "s_waitcnt lgkmcnt(0)\n"
  "s_mov_b32 s49, s74\n"
  "s_mov_b32 s52, 1\n"
  "s_add_u32 s43, s43, 1\n"
  "s_barrier\n"
  "s_branch RTRY\n"
  "RSLOW:\n"
  "s_waitcnt lgkmcnt(0)\n"
  "s_nop 3\n"
  "v_mov_b32 v7, v7\n"
  "s_nop 2\n"
  "v_mov_b32 v7, v7\n"
  "s_nop 2\n"
  "s_add_u32 s6, s74, 110\n"
  "s_cmp_gt_i32 s64, 0\n"
  "s_cselect_b32 s6, s6, 0\n"
  "v_add_f32 v60, v84, v85\n"
  "v_add_f32 v61, v86, v87\n"
  "v_cmp_u_f32 vcc, v60, v61\n"
  "s_cbranch_vccz RFULL\n"
  "RAGAIN:\n"
  "s_waitcnt lgkmcnt(0)\n"
  "s_nop 3\n"
  "v_mov_b32 v7, v7\n"
  "s_nop 2\n"
  "v_mov_b32 v7, v7\n"
  "s_nop 2\n"
  "s_mov_b32 s6, 0\n"
  "RAG2:\n"
  "s_sub_u32 s42, s42, 1\n"
  "s_cbranch_scc1 QUITV\n"
  "s_add_u32 s43, s43, 0x10000\n"
  "global_load_dwordx4 v[168:171], v0, s[70:71] offset:0 sc1\n"
  "global_load_dwordx4 v[172:175], v0, s[70:71] offset:1024 sc1\n"
  "global_load_dwordx4 v[176:179], v0, s[70:71] offset:2048 sc1\n"
  "global_load_dwordx4 v[180:183], v0, s[70:71] offset:3072 sc1\n"
  "global_load_dwordx4 v[184:187], v0, s[66:67] offset:0 sc1\n"
  "global_load_dwordx4 v[188:191], v0, s[66:67] offset:1024 sc1\n"
  "global_load_dwordx4 v[192:195], v0, s[66:67] offset:2048 sc1\n"
  "global_load_dwordx4 v[196:199], v0, s[66:67] offset:3072 sc1\n"
  "s_waitcnt vmcnt(7)\n"
  "v_mfma_f32_16x16x32_bf16 v[84:87], v[168:171], v[8:11], 0\n"
  "s_waitcnt vmcnt(6)\n"
  "v_mfma_f32_16x16x32_bf16 v[84:87], v[172:175], v[8:11], v[84:87]\n"
  "s_waitcnt vmcnt(5)\n"
  "v_mfma_f32_16x16x32_bf16 v[84:87], v[176:179], v[8:11], v[84:87]\n"
  "s_waitcnt vmcnt(4)\n"
  "v_mfma_f32_16x16x32_bf16 v[84:87], v[180:183], v[8:11], v[84:87]\n"
  "s_waitcnt vmcnt(3)\n"
  "v_mfma_f32_16x16x32_bf16 v[84:87], v[184:187], v[8:11], v[84:87]\n"
  "s_waitcnt vmcnt(2)\n"
  "v_mfma_f32_16x16x32_bf16 v[84:87], v[188:191], v[8:11], v[84:87]\n"
  "s_waitcnt vmcnt(1)\n"
  "v_mfma_f32_16x16x32_bf16 v[84:87], v[192:195], v[8:11], v[84:87]\n"
  "s_waitcnt vmcnt(0)\n"
  "v_mfma_f32_16x16x32_bf16 v[84:87], v[196:199], v[8:11], v[84:87]\n"
  "s_nop 3\n"
  "v_mov_b32 v7, v7\n"
  "s_nop 2\n"
  "v_add_f32 v60, v84, v85\n"
  "v_add_f32 v61, v86, v87\n"
  "v_cmp_u_f32 vcc, v60, v61\n"
  "s_cbranch_vccz RFULL\n"
  "s_mov_b32 s74, 0xff820000\n"
  "v_max3_u32 v60, v168, v169, v170\n"
  "v_max3_u32 v60, v60, v171, v172\n"
  "v_max3_u32 v60, v60, v173, v174\n"
  "v_max3_u32 v60, v60, v175, v176\n"
  "v_max3_u32 v60, v60, v177, v178\n"
  "v_max3_u32 v60, v60, v179, v180\n"
  "v_max3_u32 v60, v60, v181, v182\n"
  "v_max3_u32 v60, v60, v183, v184\n"
  "v_max3_u32 v60, v60, v185, v186\n"
  "v_max3_u32 v60, v60, v187, v188\n"
  "v_max3_u32 v60, v60, v189, v190\n"
  "v_max3_u32 v60, v60, v191, v192\n"
  "v_max3_u32 v60, v60, v193, v194\n"
  "v_max3_u32 v60, v60, v195, v196\n"
  "v_max3_u32 v60, v60, v197, v198\n"
  "v_max_u32 v60, v60, v199\n"
  "v_cmp_le_u32 vcc, s74, v60\n"
  "s_cbranch_vccnz RAG2\n"
  "s_mov_b32 s74, 0x7fff7fff\n"
  "v_and_b32 v60, s74, v168\n"
  "v_and_b32 v61, s74, v169\n"
  "v_pk_max_u16 v60, v60, v61\n"
  "v_and_b32 v61, s74, v170\n"
  "v_pk_max_u16 v60, v60, v61\n"
  "v_and_b32 v61, s74, v171\n"
  "v_pk_max_u16 v60, v60, v61\n"
  "v_and_b32 v61, s74, v172\n"
  "v_pk_max_u16 v60, v60, v61\n"
  "v_and_b32 v61, s74, v173\n"
  "v_pk_max_u16 v60, v60, v61\n"
  "v_and_b32 v61, s74, v174\n"
  "v_pk_max_u16 v60, v60, v61\n"
  "v_and_b32 v61, s74, v175\n"
  "v_pk_max_u16 v60, v60, v61\n"
  "v_and_b32 v61, s74, v176\n"
  "v_pk_max_u16 v60, v60, v61\n"
  "v_and_b32 v61, s74, v177\n"
  "v_pk_max_u16 v60, v60, v61\n"
  "v_and_b32 v61, s74, v178\n"
  "v_pk_max_u16 v60, v60, v61\n"
  "v_and_b32 v61, s74, v179\n"
  "v_pk_max_u16 v60, v60, v61\n"
  "v_and_b32 v61, s74, v180\n"
  "v_pk_max_u16 v60, v60, v61\n"
  "v_and_b32 v61, s74, v181\n"
  "v_pk_max_u16 v60, v60, v61\n"
  "v_and_b32 v61, s74, v182\n"
  "v_pk_max_u16 v60, v60, v61\n"
  "v_and_b32 v61, s74, v183\n"
  "v_pk_max_u16 v60, v60, v61\n"
  "v_and_b32 v61, s74, v184\n"
  "v_pk_max_u16 v60, v60, v61\n"
  "v_and_b32 v61, s74, v185\n"
  "v_pk_max_u16 v60, v60, v61\n"
  "v_and_b32 v61, s74, v186\n"
  "v_pk_max_u16 v60, v60, v61\n"
  "v_and_b32 v61, s74, v187\n"
  "v_pk_max_u16 v60, v60, v61\n"
  "v_and_b32 v61, s74, v188\n"
  "v_pk_max_u16 v60, v60, v61\n"
  "v_and_b32 v61, s74, v189\n"
  "v_pk_max_u16 v60, v60, v61\n"
  "v_and_b32 v61, s74, v190\n"
  "v_pk_max_u16 v60, v60, v61\n"
  "v_and_b32 v61, s74, v191\n"
  "v_pk_max_u16 v60, v60, v61\n"
  "v_and_b32 v61, s74, v192\n"
  "v_pk_max_u16 v60, v60, v61\n"
  "v_and_b32 v61, s74, v193\n"
  "v_pk_max_u16 v60, v60, v61\n"
  "v_and_b32 v61, s74, v194\n"
  "v_pk_max_u16 v60, v60, v61\n"
  "v_and_b32 v61, s74, v195\n"
  "v_pk_max_u16 v60, v60, v61\n"
  "v_and_b32 v61, s74, v196\n"
  "v_pk_max_u16 v60, v60, v61\n"
  "v_and_b32 v61, s74, v197\n"
  "v_pk_max_u16 v60, v60, v61\n"
  "v_and_b32 v61, s74, v198\n"
  "v_pk_max_u16 v60, v60, v61\n"
  "v_and_b32 v61, s74, v199\n"
  "v_pk_max_u16 v60, v60, v61\n"
  "v_lshrrev_b32 v61, 16, v60\n"
  "v_and_b32 v60, 0xffff, v60\n"
  "v_max_u32 v60, v60, v61\n"
  "v_cmp_lt_u32 vcc, 0x7f81, v60\n"
  "s_cbranch_vccnz RAG2\n"
  "RFULL:\n"
  "v_cvt_pk_bf16_f32 v88, v84, v85\n"
  "v_cvt_pk_bf16_f32 v89, v86, v87\n"
  "v_cvt_pk_bf16_f32 v90, v84, v85\n"
  "v_cvt_pk_bf16_f32 v91, v86, v87\n"
  "s_nop 0\n"
  "v_permlane32_swap_b32 v88, v90\n"
  "v_permlane32_swap_b32 v89, v91\n"
  "s_mov_b32 s74, 0x7fff7fff\n"
  "s_mov_b32 s75, 0x7f817f81\n"
  "v_and_b32 v60, s74, v88\n"
  "v_pk_min_u16 v60, v60, s75\n"
  "v_bfi_b32 v88, s74, v60, v88\n"
  "v_and_b32 v60, s74, v89\n"
  "v_pk_min_u16 v60, v60, s75\n"
  "v_bfi_b32 v89, s74, v60, v89\n"
  "v_and_b32 v60, s74, v90\n"
  "v_pk_min_u16 v60, v60, s75\n"
  "v_bfi_b32 v90, s74, v60, v90\n"
  "v_and_b32 v60, s74, v91\n"
  "v_pk_min_u16 v60, v60, s75\n"
  "v_bfi_b32 v91, s74, v60, v91\n"
  "s_add_u32 s43, s43, 0x100\n"
  "s_memrealtime s[74:75]\n"
  "s_waitcnt lgkmcnt(0)\n"
  "s_mul_i32 s78, s74, 24\n"
  "s_add_u32 s78, s78, 750\n"
  "s_mov_b32 s81, s49\n"
  "RSG:\n"
  "s_add_u32 s79, s81, s51\n"
  "s_sub_i32 s74, s79, s78\n"
  "s_cmp_ge_i32 s74, 0\n"
  "s_cbranch_scc1 RGOT\n"
  "s_mov_b32 s81, s79\n"
  "s_branch RSG\n"
  "RGOT:\n"
  "v_mov_b32 v60, 0\n"
  "v_mov_b32 v61, s79\n"
  "ds_write_b32 v60, v61 offset:9232\n"
  "s_add_u32 s81, s81, 200\n"
  "WR4:\n"
  "s_memrealtime s[74:75]\n"
  "s_waitcnt lgkmcnt(0)\n"
  "s_mul_i32 s74, s74, 24\n"
  "s_sub_i32 s74, s81, s74\n"
  "s_cmp_lt_i32 s74, 742\n"
  "s_cbranch_scc1 WFR4\n"
  "s_cmp_gt_i32 s74, 0x40000\n"
  "s_cbranch_scc1 WER4\n"
  "s_sleep 2\n"
  "s_branch WR4\n"
  "WFR4:\n"
  "s_cmp_lt_i32 s74, 100\n"
  "s_cbranch_scc1 WER4\n"
  "s_memtime s[76:77]\n"
  "s_memrealtime s[20:21]\n"
  "s_nop 2\n"
  "s_memrealtime s[22:23]\n"
  "s_nop 2\n"
  "s_memrealtime s[24:25]\n"
  "s_nop 2\n"
  "s_memrealtime s[26:27]\n"
  "s_nop 2\n"
  "s_memrealtime s[28:29]\n"
  "s_nop 2\n"
  "s_memrealtime s[30:31]\n"
  "s_waitcnt lgkmcnt(0)\n"
  "s_add_u32 s20, s20, s22\n"
  "s_add_u32 s24, s24, s26\n"
  "s_add_u32 s28, s28, s30\n"
  "s_add_u32 s20, s20, s24\n"
  "s_add_u32 s20, s20, s28\n"
  "s_lshl_b32 s74, s20, 2\n"
  "s_nop 0\n"
  "s_sub_i32 s74, s81, s74\n"
  "s_sub_i32 s74, s74, 230\n"
  "s_max_i32 s74, s74, 0\n"
  "s_lshr_b32 s74, s74, 3\n"
  "s_lshl_b32 s74, s74, 2\n"
  "s_sub_i32 s74, 268, s74\n"
  "s_getpc_b64 s[76:77]\n"
  "s_add_u32 s76, s76, s74\n"
  "s_addc_u32 s77, s77, 0\n"
  "s_setpc_b64 s[76:77]\n"
  "s_nop 1\n"
  "s_nop 1\n"
  "s_nop 1\n"
  "s_nop 1\n"
  "s_nop 1\n"
  "s_nop 1\n"
  "s_nop 1\n"
  "s_nop 1\n"
  "s_nop 1\n"
  "s_nop 1\n"
  "s_nop 1\n"
  "s_nop 1\n"
  "s_nop 1\n"
  "s_nop 1\n"
  "s_nop 1\n"
  "s_nop 1\n"
  "s_nop 1\n"
  "s_nop 1\n"
  "s_nop 1\n"
  "s_nop 1\n"
  "s_nop 1\n"
  "s_nop 1\n"
  "s_nop 1\n"
  "s_nop 1\n"
  "s_nop 1\n"
  "s_nop 1\n"
  "s_nop 1\n"
  "s_nop 1\n"
  "s_nop 1\n"
  "s_nop 1\n"
  "s_nop 1\n"
  "s_nop 1\n"
  "s_nop 1\n"
  "s_nop 1\n"
  "s_nop 1\n"
  "s_nop 1\n"
  "s_nop 1\n"
  "s_nop 1\n"
  "s_nop 1\n"
  "s_nop 1\n"
  "s_nop 1\n"
  "s_nop 1\n"
  "s_nop 1\n"
  "s_nop 1\n"
  "s_nop 1\n"
  "s_nop 1\n"
  "s_nop 1\n"
  "s_nop 1\n"
  "s_nop 1\n"
  "s_nop 1\n"
  "s_nop 1\n"
  "s_nop 1\n"
  "s_nop 1\n"
  "s_nop 1\n"
  "s_nop 1\n"
  "s_nop 1\n"
  "s_nop 1\n"
  "s_nop 1\n"
  "s_nop 1\n"
  "s_nop 1\n"
  "s_nop 1\n"
  "s_nop 1\n"
  "s_nop 1\n"
  "s_nop 1\n"
  "WER4:\n"
  "global_store_dwordx4 v3, v[88:91], s[72:73] sc1\n"
  "global_store_dwordx4 v13, v[88:91], s[72:73] sc1\n"
  "s_branch RDONE\n"
  "ENDV:\n"
  "s_waitcnt vmcnt(0) lgkmcnt(0)\n"
  "s_barrier\n"
  "s_add_u32 s74, s36, s38\n"
  "s_addc_u32 s75, s37, 0\n"
  "v_mov_b32 v60, 0\n"
  "v_mov_b32 v61, s40\n"
  "s_cmp_eq_u32 s48, 1\n"
  "s_cselect_b64 exec, 1, 0\n"
  "global_store_dword v60, v61, s[74:75] sc1\n"
  "QUITV:\n"
  "s_endpgm\n"
        : "+{v[0:15]}"(vin), "+{s[36:43]}"(sa), "+{s[44:59]}"(sb) :: CLOB);
    }
    return;
  }
  const int fd = gam - 4;
  if (fd >= 2) {
    const int h = (fd - 2) * 16 + (tk >> 1), isq = tk & 1;
    { const int row = 4 * wave + g, sw = 2 * ((row & 3) | ((row >> 1) & 4)); vin[1] = row * 8192 + (n ^ sw) * 16; }
    vin[2] = (8 * g + qq) * 256 + ((4 * wave + (c4 >> 1)) ^ (2 * (qq | ((g & 1) << 2)))) * 16 + (c4 & 1) * 8;
    vin[3] = x_src(wave, lane); vin[9] = x_frag(lane);
    { const int row = 8 * wave + (lane >> 3); vin[4] = row * 8192 + ((lane & 7) ^ (2 * (((row >> 1) & 1) | ((row >> 2) & 2)))) * 16; }
    vin[5] = (8 * g + qq) * 128 + ((2 * wave + (c4 >> 1)) ^ (2 * ((qq >> 1) | ((g & 1) << 1)))) * 16 + (c4 & 1) * 8;
    PTR(sa, 2, (isq ? p.Wq : p.Wk) + h * 128); PTR(sa, 4, p.Wv + h * 128 + isq * 64); PTR(sa, 6, p.x);
    sb[0] = (isq ? WO_Q + h * 8192 : WO_KV + h * 16384) + wave * 2048; sb[1] = WO_KV + h * 16384 + (8 + 4 * isq + wave) * 1024;
    sb[2] = C_KVP + (2 * h + isq) * 128; sb[3] = wave; sb[4] = __float_as_uint(isq ? 1.0f : QS); sb[5] = isq ? 32 * 8192 : 32 * 16384;
    sb[6] = WO_ST + (role * 4 + wave) * 4096;
    UNIFORM(sa, 8); UNIFORM(sb, 16);
    asm volatile(
  "s_nop 4\n"
  "s_lshl_b32 s80, s47, 10\n"
  "v_mov_b32 v8, s48\n"
  "s_add_u32 s58, s80, 122880\n"
  "s_add_u32 m0, s58, 0\n"
  "s_nop 0\n"
  "global_load_lds_dwordx4 v3, s[42:43] offset:0\n"
  "s_add_u32 m0, s58, 3968\n"
  "s_nop 0\n"
  "global_load_lds_dwordx4 v3, s[42:43] offset:128\n"
  "s_add_u32 m0, s58, 7936\n"
  "s_nop 0\n"
  "global_load_lds_dwordx4 v3, s[42:43] offset:256\n"
  "s_add_u32 m0, s58, 11904\n"
  "s_nop 0\n"
  "global_load_lds_dwordx4 v3, s[42:43] offset:384\n"
  "s_add_u32 m0, s58, 15872\n"
  "s_nop 0\n"
  "global_load_lds_dwordx4 v3, s[42:43] offset:512\n"
  "s_add_u32 m0, s58, 19840\n"
  "s_nop 0\n"
  "global_load_lds_dwordx4 v3, s[42:43] offset:640\n"
  "s_add_u32 m0, s58, 23808\n"
  "s_nop 0\n"
  "global_load_lds_dwordx4 v3, s[42:43] offset:768\n"
  "s_add_u32 m0, s58, 27776\n"
  "s_nop 0\n"
  "global_load_lds_dwordx4 v3, s[42:43] offset:896\n"
  "s_add_u32 m0, s58, 31744\n"
  "s_nop 0\n"
  "global_load_lds_dwordx4 v3, s[42:43] offset:1024\n"
  "s_add_u32 m0, s80, 0\n"
  "s_add_u32 s74, s38, 0\n"
  "s_addc_u32 s75, s39, 0\n"
  "global_load_lds_dwordx4 v1, s[74:75]\n"
  "s_add_u32 m0, s80, 4096\n"
  "s_add_u32 s74, s38, 131072\n"
  "s_addc_u32 s75, s39, 0\n"
  "global_load_lds_dwordx4 v1, s[74:75]\n"
  "s_add_u32 m0, s80, 8192\n"
  "s_add_u32 s74, s38, 262144\n"
  "s_addc_u32 s75, s39, 0\n"
  "global_load_lds_dwordx4 v1, s[74:75]\n"
  "s_add_u32 m0, s80, 12288\n"
  "s_add_u32 s74, s38, 393216\n"
  "s_addc_u32 s75, s39, 0\n"
  "global_load_lds_dwordx4 v1, s[74:75]\n"
  "s_add_u32 m0, s80, 16384\n"
  "s_add_u32 s74, s40, 0\n"
  "s_addc_u32 s75, s41, 0\n"
  "global_load_lds_dwordx4 v4, s[74:75]\n"
  "s_add_u32 m0, s80, 20480\n"
  "s_add_u32 s74, s40, 262144\n"
  "s_addc_u32 s75, s41, 0\n"
  "global_load_lds_dwordx4 v4, s[74:75]\n"
  "s_add_u32 m0, s80, 24576\n"
  "s_add_u32 s74, s38, 524288\n"
  "s_addc_u32 s75, s39, 0\n"
  "global_load_lds_dwordx4 v1, s[74:75]\n"
  "s_add_u32 m0, s80, 28672\n"
  "s_add_u32 s74, s38, 655360\n"
  "s_addc_u32 s75, s39, 0\n"
  "global_load_lds_dwordx4 v1, s[74:75]\n"
  "s_add_u32 m0, s80, 32768\n"
  "s_add_u32 s74, s38, 786432\n"
  "s_addc_u32 s75, s39, 0\n"
  "global_load_lds_dwordx4 v1, s[74:75]\n"
  "s_add_u32 m0, s80, 36864\n"
  "s_add_u32 s74, s38, 917504\n"
  "s_addc_u32 s75, s39, 0\n"
  "global_load_lds_dwordx4 v1, s[74:75]\n"
  "s_add_u32 m0, s80, 40960\n"
  "s_add_u32 s74, s40, 524288\n"
  "s_addc_u32 s75, s41, 0\n"
  "global_load_lds_dwordx4 v4, s[74:75]\n"
  "s_add_u32 m0, s80, 45056\n"
  "s_add_u32 s74, s40, 786432\n"
  "s_addc_u32 s75, s41, 0\n"
  "global_load_lds_dwordx4 v4, s[74:75]\n"
  "s_add_u32 m0, s80, 49152\n"
  "s_add_u32 s74, s38, 1048576\n"
  "s_addc_u32 s75, s39, 0\n"
  "global_load_lds_dwordx4 v1, s[74:75]\n"
  "s_add_u32 m0, s80, 53248\n"
  "s_add_u32 s74, s38, 1179648\n"
  "s_addc_u32 s75, s39, 0\n"
  "global_load_lds_dwordx4 v1, s[74:75]\n"
  "s_add_u32 m0, s80, 57344\n"
  "s_add_u32 s74, s38, 1310720\n"
  "s_addc_u32 s75, s39, 0\n"
  "global_load_lds_dwordx4 v1, s[74:75]\n"
  "s_add_u32 m0, s80, 61440\n"
  "s_add_u32 s74, s38, 1441792\n"
  "s_addc_u32 s75, s39, 0\n"
  "global_load_lds_dwordx4 v1, s[74:75]\n"
  "s_add_u32 m0, s80, 65536\n"
  "s_add_u32 s74, s40, 1048576\n"
  "s_addc_u32 s75, s41, 0\n"
  "global_load_lds_dwordx4 v4, s[74:75]\n"
  "s_add_u32 m0, s80, 69632\n"
  "s_add_u32 s74, s40, 1310720\n"
  "s_addc_u32 s75, s41, 0\n"
  "global_load_lds_dwordx4 v4, s[74:75]\n"
  "s_add_u32 m0, s80, 73728\n"
  "s_add_u32 s74, s38, 1572864\n"
  "s_addc_u32 s75, s39, 0\n"
  "global_load_lds_dwordx4 v1, s[74:75]\n"
  "s_add_u32 m0, s80, 77824\n"
  "s_add_u32 s74, s38, 1703936\n"
  "s_addc_u32 s75, s39, 0\n"
  "global_load_lds_dwordx4 v1, s[74:75]\n"
  "s_add_u32 m0, s80, 81920\n"
  "s_add_u32 s74, s38, 1835008\n"
  "s_addc_u32 s75, s39, 0\n"
  "global_load_lds_dwordx4 v1, s[74:75]\n"
  "s_add_u32 m0, s80, 86016\n"
  "s_add_u32 s74, s38, 1966080\n"
  "s_addc_u32 s75, s39, 0\n"
  "global_load_lds_dwordx4 v1, s[74:75]\n"
  "s_add_u32 m0, s80, 90112\n"
  "s_add_u32 s74, s40, 1572864\n"
  "s_addc_u32 s75, s41, 0\n"
  "global_load_lds_dwordx4 v4, s[74:75]\n"
  "s_add_u32 m0, s80, 94208\n"
  "s_add_u32 s74, s40, 1835008\n"
  "s_addc_u32 s75, s41, 0\n"
  "global_load_lds_dwordx4 v4, s[74:75]\n"
  "s_add_u32 s60, s36, s44\n"
  "s_addc_u32 s61, s37, 0\n"
  "s_add_u32 s62, s36, s45\n"
  "s_addc_u32 s63, s37, 0\n"
  "s_add_u32 s64, s36, s46\n"
  "s_addc_u32 s65, s37, 0\n"
  "s_waitcnt vmcnt(18) lgkmcnt(0)\n"
  "s_barrier\n"
  "s_add_u32 m0, s80, 98304\n"
  "s_add_u32 s74, s38, 2097152\n"
  "s_addc_u32 s75, s39, 0\n"
  "global_load_lds_dwordx4 v1, s[74:75]\n"
  "s_add_u32 m0, s80, 102400\n"
  "s_add_u32 s74, s38, 2228224\n"
  "s_addc_u32 s75, s39, 0\n"
  "global_load_lds_dwordx4 v1, s[74:75]\n"
  "s_add_u32 m0, s80, 106496\n"
  "s_add_u32 s74, s38, 2359296\n"
  "s_addc_u32 s75, s39, 0\n"
  "global_load_lds_dwordx4 v1, s[74:75]\n"
  "s_add_u32 m0, s80, 110592\n"
  "s_add_u32 s74, s38, 2490368\n"
  "s_addc_u32 s75, s39, 0\n"
  "global_load_lds_dwordx4 v1, s[74:75]\n"
  "s_add_u32 m0, s80, 114688\n"
  "s_add_u32 s74, s40, 2097152\n"
  "s_addc_u32 s75, s41, 0\n"
  "global_load_lds_dwordx4 v4, s[74:75]\n"
  "s_add_u32 m0, s80, 118784\n"
  "s_add_u32 s74, s40, 2359296\n"
  "s_addc_u32 s75, s41, 0\n"
  "global_load_lds_dwordx4 v4, s[74:75]\n"
  "v_xor_b32 v6, 0, v2\n"
  "v_add_u32 v6, 0, v6\n"
  "ds_read_b64_tr_b16 a[0:1], v6 offset:0\n"
  "ds_read_b64_tr_b16 a[2:3], v6 offset:1024\n"
  "ds_read_b64_tr_b16 a[4:5], v6 offset:8192\n"
  "ds_read_b64_tr_b16 a[6:7], v6 offset:9216\n"
  "v_xor_b32 v6, 32, v2\n"
  "v_add_u32 v6, 0, v6\n"
  "ds_read_b64_tr_b16 a[72:73], v6 offset:0\n"
  "ds_read_b64_tr_b16 a[74:75], v6 offset:1024\n"
  "ds_read_b64_tr_b16 a[76:77], v6 offset:8192\n"
  "ds_read_b64_tr_b16 a[78:79], v6 offset:9216\n"
  "v_add_u32 v6, 16384, v5\n"
  "ds_read_b64_tr_b16 a[144:145], v6 offset:0\n"
  "ds_read_b64_tr_b16 a[146:147], v6 offset:512\n"
  "ds_read_b64_tr_b16 a[148:149], v6 offset:4096\n"
  "ds_read_b64_tr_b16 a[150:151], v6 offset:4608\n"
  "s_waitcnt vmcnt(18) lgkmcnt(0)\n"
  "s_barrier\n"
  "s_add_u32 m0, s80, 0\n"
  "s_add_u32 s74, s38, 2621440\n"
  "s_addc_u32 s75, s39, 0\n"
  "global_load_lds_dwordx4 v1, s[74:75]\n"
  "s_add_u32 m0, s80, 4096\n"
  "s_add_u32 s74, s38, 2752512\n"
  "s_addc_u32 s75, s39, 0\n"
  "global_load_lds_dwordx4 v1, s[74:75]\n"
  "s_add_u32 m0, s80, 8192\n"
  "s_add_u32 s74, s38, 2883584\n"
  "s_addc_u32 s75, s39, 0\n"
  "global_load_lds_dwordx4 v1, s[74:75]\n"
  "s_add_u32 m0, s80, 12288\n"
  "s_add_u32 s74, s38, 3014656\n"
  "s_addc_u32 s75, s39, 0\n"
  "global_load_lds_dwordx4 v1, s[74:75]\n"
  "s_add_u32 m0, s80, 16384\n"
  "s_add_u32 s74, s40, 2621440\n"
  "s_addc_u32 s75, s41, 0\n"
  "global_load_lds_dwordx4 v4, s[74:75]\n"
  "s_add_u32 m0, s80, 20480\n"
  "s_add_u32 s74, s40, 2883584\n"
  "s_addc_u32 s75, s41, 0\n"
  "global_load_lds_dwordx4 v4, s[74:75]\n"
  "v_xor_b32 v6, 0, v2\n"
  "v_add_u32 v6, 24576, v6\n"
  "ds_read_b64_tr_b16 a[8:9], v6 offset:0\n"
  "ds_read_b64_tr_b16 a[10:11], v6 offset:1024\n"
  "ds_read_b64_tr_b16 a[12:13], v6 offset:8192\n"
  "ds_read_b64_tr_b16 a[14:15], v6 offset:9216\n"
  "v_xor_b32 v6, 32, v2\n"
  "v_add_u32 v6, 24576, v6\n"
  "ds_read_b64_tr_b16 a[80:81], v6 offset:0\n"
  "ds_read_b64_tr_b16 a[82:83], v6 offset:1024\n"
  "ds_read_b64_tr_b16 a[84:85], v6 offset:8192\n"
  "ds_read_b64_tr_b16 a[86:87], v6 offset:9216\n"
  "v_add_u32 v6, 40960, v5\n"
  "ds_read_b64_tr_b16 a[152:153], v6 offset:0\n"
  "ds_read_b64_tr_b16 a[154:155], v6 offset:512\n"
  "ds_read_b64_tr_b16 a[156:157], v6 offset:4096\n"
  "ds_read_b64_tr_b16 a[158:159], v6 offset:4608\n"
  "s_waitcnt vmcnt(18) lgkmcnt(0)\n"
  "s_barrier\n"
  "s_add_u32 m0, s80, 24576\n"
  "s_add_u32 s74, s38, 3145728\n"
  "s_addc_u32 s75, s39, 0\n"
  "global_load_lds_dwordx4 v1, s[74:75]\n"
  "s_add_u32 m0, s80, 28672\n"
  "s_add_u32 s74, s38, 3276800\n"
  "s_addc_u32 s75, s39, 0\n"
  "global_load_lds_dwordx4 v1, s[74:75]\n"
  "s_add_u32 m0, s80, 32768\n"
  "s_add_u32 s74, s38, 3407872\n"
  "s_addc_u32 s75, s39, 0\n"
  "global_load_lds_dwordx4 v1, s[74:75]\n"
  "s_add_u32 m0, s80, 36864\n"
  "s_add_u32 s74, s38, 3538944\n"
  "s_addc_u32 s75, s39, 0\n"
  "global_load_lds_dwordx4 v1, s[74:75]\n"
  "s_add_u32 m0, s80, 40960\n"
  "s_add_u32 s74, s40, 3145728\n"
  "s_addc_u32 s75, s41, 0\n"
  "global_load_lds_dwordx4 v4, s[74:75]\n"
  "s_add_u32 m0, s80, 45056\n"
  "s_add_u32 s74, s40, 3407872\n"
  "s_addc_u32 s75, s41, 0\n"
  "global_load_lds_dwordx4 v4, s[74:75]\n"
  "v_xor_b32 v6, 0, v2\n"
  "v_add_u32 v6, 49152, v6\n"
  "ds_read_b64_tr_b16 a[16:17], v6 offset:0\n"
  "ds_read_b64_tr_b16 a[18:19], v6 offset:1024\n"
  "ds_read_b64_tr_b16 a[20:21], v6 offset:8192\n"
  "ds_read_b64_tr_b16 a[22:23], v6 offset:9216\n"
  "v_xor_b32 v6, 32, v2\n"
  "v_add_u32 v6, 49152, v6\n"
  "ds_read_b64_tr_b16 a[88:89], v6 offset:0\n"
  "ds_read_b64_tr_b16 a[90:91], v6 offset:1024\n"
  "ds_read_b64_tr_b16 a[92:93], v6 offset:8192\n"
  "ds_read_b64_tr_b16 a[94:95], v6 offset:9216\n"
  "v_add_u32 v6, 65536, v5\n"
  "ds_read_b64_tr_b16 a[160:161], v6 offset:0\n"
  "ds_read_b64_tr_b16 a[162:163], v6 offset:512\n"
  "ds_read_b64_tr_b16 a[164:165], v6 offset:4096\n"
  "ds_read_b64_tr_b16 a[166:167], v6 offset:4608\n"
  "s_waitcnt vmcnt(18) lgkmcnt(0)\n"
  "s_barrier\n"
  "s_add_u32 m0, s80, 49152\n"
  "s_add_u32 s74, s38, 3670016\n"
  "s_addc_u32 s75, s39, 0\n"
  "global_load_lds_dwordx4 v1, s[74:75]\n"
  "s_add_u32 m0, s80, 53248\n"
  "s_add_u32 s74, s38, 3801088\n"
  "s_addc_u32 s75, s39, 0\n"
  "global_load_lds_dwordx4 v1, s[74:75]\n"
  "s_add_u32 m0, s80, 57344\n"
  "s_add_u32 s74, s38, 3932160\n"
  "s_addc_u32 s75, s39, 0\n"
  "global_load_lds_dwordx4 v1, s[74:75]\n"
  "s_add_u32 m0, s80, 61440\n"
  "s_add_u32 s74, s38, 4063232\n"
  "s_addc_u32 s75, s39, 0\n"
  "global_load_lds_dwordx4 v1, s[74:75]\n"
  "s_add_u32 m0, s80, 65536\n"
  "s_add_u32 s74, s40, 3670016\n"
  "s_addc_u32 s75, s41, 0\n"
  "global_load_lds_dwordx4 v4, s[74:75]\n"
  "s_add_u32 m0, s80, 69632\n"
  "s_add_u32 s74, s40, 3932160\n"
  "s_addc_u32 s75, s41, 0\n"
  "global_load_lds_dwordx4 v4, s[74:75]\n"
  "v_xor_b32 v6, 0, v2\n"
  "v_add_u32 v6, 73728, v6\n"
  "ds_read_b64_tr_b16 a[24:25], v6 offset:0\n"
  "ds_read_b64_tr_b16 a[26:27], v6 offset:1024\n"
  "ds_read_b64_tr_b16 a[28:29], v6 offset:8192\n"
  "ds_read_b64_tr_b16 a[30:31], v6 offset:9216\n"
  "v_xor_b32 v6, 32, v2\n"
  "v_add_u32 v6, 73728, v6\n"
  "ds_read_b64_tr_b16 a[96:97], v6 offset:0\n"
  "ds_read_b64_tr_b16 a[98:99], v6 offset:1024\n"
  "ds_read_b64_tr_b16 a[100:101], v6 offset:8192\n"
  "ds_read_b64_tr_b16 a[102:103], v6 offset:9216\n"
  "v_add_u32 v6, 90112, v5\n"
  "ds_read_b64_tr_b16 a[168:169], v6 offset:0\n"
  "ds_read_b64_tr_b16 a[170:171], v6 offset:512\n"
  "ds_read_b64_tr_b16 a[172:173], v6 offset:4096\n"
  "ds_read_b64_tr_b16 a[174:175], v6 offset:4608\n"
  "s_waitcnt vmcnt(18) lgkmcnt(0)\n"
  "s_barrier\n"
  "s_add_u32 m0, s80, 73728\n"
  "s_add_u32 s74, s38, 4194304\n"
  "s_addc_u32 s75, s39, 0\n"
  "global_load_lds_dwordx4 v1, s[74:75]\n"
  "s_add_u32 m0, s80, 77824\n"
  "s_add_u32 s74, s38, 4325376\n"
  "s_addc_u32 s75, s39, 0\n"
  "global_load_lds_dwordx4 v1, s[74:75]\n"
  "s_add_u32 m0, s80, 81920\n"
  "s_add_u32 s74, s38, 4456448\n"
  "s_addc_u32 s75, s39, 0\n"
  "global_load_lds_dwordx4 v1, s[74:75]\n"
  "s_add_u32 m0, s80, 86016\n"
  "s_add_u32 s74, s38, 4587520\n"
  "s_addc_u32 s75, s39, 0\n"
  "global_load_lds_dwordx4 v1, s[74:75]\n"
  "s_add_u32 m0, s80, 90112\n"
  "s_add_u32 s74, s40, 4194304\n"
  "s_addc_u32 s75, s41, 0\n"
  "global_load_lds_dwordx4 v4, s[74:75]\n"
  "s_add_u32 m0, s80, 94208\n"
  "s_add_u32 s74, s40, 4456448\n"
  "s_addc_u32 s75, s41, 0\n"
  "global_load_lds_dwordx4 v4, s[74:75]\n"
  "v_xor_b32 v6, 0, v2\n"
  "v_add_u32 v6, 98304, v6\n"
  "ds_read_b64_tr_b16 a[32:33], v6 offset:0\n"
  "ds_read_b64_tr_b16 a[34:35], v6 offset:1024\n"
  "ds_read_b64_tr_b16 a[36:37], v6 offset:8192\n"
  "ds_read_b64_tr_b16 a[38:39], v6 offset:9216\n"
  "v_xor_b32 v6, 32, v2\n"
  "v_add_u32 v6, 98304, v6\n"
  "ds_read_b64_tr_b16 a[104:105], v6 offset:0\n"
  "ds_read_b64_tr_b16 a[106:107], v6 offset:1024\n"
  "ds_read_b64_tr_b16 a[108:109], v6 offset:8192\n"
  "ds_read_b64_tr_b16 a[110:111], v6 offset:9216\n"
  "v_add_u32 v6, 114688, v5\n"
  "ds_read_b64_tr_b16 a[176:177], v6 offset:0\n"
  "ds_read_b64_tr_b16 a[178:179], v6 offset:512\n"
  "ds_read_b64_tr_b16 a[180:181], v6 offset:4096\n"
  "ds_read_b64_tr_b16 a[182:183], v6 offset:4608\n"
  "s_waitcnt vmcnt(18) lgkmcnt(0)\n"
  "s_barrier\n"
  "v_xor_b32 v6, 0, v2\n"
  "v_add_u32 v6, 0, v6\n"
  "ds_read_b64_tr_b16 a[40:41], v6 offset:0\n"
  "ds_read_b64_tr_b16 a[42:43], v6 offset:1024\n"
  "ds_read_b64_tr_b16 a[44:45], v6 offset:8192\n"
  "ds_read_b64_tr_b16 a[46:47], v6 offset:9216\n"
  "v_xor_b32 v6, 32, v2\n"
  "v_add_u32 v6, 0, v6\n"
  "ds_read_b64_tr_b16 a[112:113], v6 offset:0\n"
  "ds_read_b64_tr_b16 a[114:115], v6 offset:1024\n"
  "ds_read_b64_tr_b16 a[116:117], v6 offset:8192\n"
  "ds_read_b64_tr_b16 a[118:119], v6 offset:9216\n"
  "v_add_u32 v6, 16384, v5\n"
  "ds_read_b64_tr_b16 a[184:185], v6 offset:0\n"
  "ds_read_b64_tr_b16 a[186:187], v6 offset:512\n"
  "ds_read_b64_tr_b16 a[188:189], v6 offset:4096\n"
  "ds_read_b64_tr_b16 a[190:191], v6 offset:4608\n"
  "s_waitcnt vmcnt(12) lgkmcnt(0)\n"
  "s_barrier\n"
  "v_xor_b32 v6, 0, v2\n"
  "v_add_u32 v6, 24576, v6\n"
  "ds_read_b64_tr_b16 a[48:49], v6 offset:0\n"
  "ds_read_b64_tr_b16 a[50:51], v6 offset:1024\n"
  "ds_read_b64_tr_b16 a[52:53], v6 offset:8192\n"
  "ds_read_b64_tr_b16 a[54:55], v6 offset:9216\n"
  "v_xor_b32 v6, 32, v2\n"
  "v_add_u32 v6, 24576, v6\n"
  "ds_read_b64_tr_b16 a[120:121], v6 offset:0\n"
  "ds_read_b64_tr_b16 a[122:123], v6 offset:1024\n"
  "ds_read_b64_tr_b16 a[124:125], v6 offset:8192\n"
  "ds_read_b64_tr_b16 a[126:127], v6 offset:9216\n"
  "v_add_u32 v6, 40960, v5\n"
  "ds_read_b64_tr_b16 a[192:193], v6 offset:0\n"
  "ds_read_b64_tr_b16 a[194:195], v6 offset:512\n"
  "ds_read_b64_tr_b16 a[196:197], v6 offset:4096\n"
  "ds_read_b64_tr_b16 a[198:199], v6 offset:4608\n"
  "s_waitcnt vmcnt(6) lgkmcnt(0)\n"
  "s_barrier\n"
  "v_xor_b32 v6, 0, v2\n"
  "v_add_u32 v6, 49152, v6\n"
  "ds_read_b64_tr_b16 a[56:57], v6 offset:0\n"
  "ds_read_b64_tr_b16 a[58:59], v6 offset:1024\n"
  "ds_read_b64_tr_b16 a[60:61], v6 offset:8192\n"
  "ds_read_b64_tr_b16 a[62:63], v6 offset:9216\n"
  "v_xor_b32 v6, 32, v2\n"
  "v_add_u32 v6, 49152, v6\n"
  "ds_read_b64_tr_b16 a[128:129], v6 offset:0\n"
  "ds_read_b64_tr_b16 a[130:131], v6 offset:1024\n"
  "ds_read_b64_tr_b16 a[132:133], v6 offset:8192\n"
  "ds_read_b64_tr_b16 a[134:135], v6 offset:9216\n"
  "v_add_u32 v6, 65536, v5\n"
  "ds_read_b64_tr_b16 a[200:201], v6 offset:0\n"
  "ds_read_b64_tr_b16 a[202:203], v6 offset:512\n"
  "ds_read_b64_tr_b16 a[204:205], v6 offset:4096\n"
  "ds_read_b64_tr_b16 a[206:207], v6 offset:4608\n"
  "s_waitcnt vmcnt(0) lgkmcnt(0)\n"
  "s_barrier\n"
  "v_xor_b32 v6, 0, v2\n"
  "v_add_u32 v6, 73728, v6\n"
  "ds_read_b64_tr_b16 a[64:65], v6 offset:0\n"
  "ds_read_b64_tr_b16 a[66:67], v6 offset:1024\n"
  "ds_read_b64_tr_b16 a[68:69], v6 offset:8192\n"
  "ds_read_b64_tr_b16 a[70:71], v6 offset:9216\n"
  "v_xor_b32 v6, 32, v2\n"
  "v_add_u32 v6, 73728, v6\n"
  "ds_read_b64_tr_b16 a[136:137], v6 offset:0\n"
  "ds_read_b64_tr_b16 a[138:139], v6 offset:1024\n"
  "ds_read_b64_tr_b16 a[140:141], v6 offset:8192\n"
  "ds_read_b64_tr_b16 a[142:143], v6 offset:9216\n"
  "v_add_u32 v6, 90112, v5\n"
  "ds_read_b64_tr_b16 a[208:209], v6 offset:0\n"
  "ds_read_b64_tr_b16 a[210:211], v6 offset:512\n"
  "ds_read_b64_tr_b16 a[212:213], v6 offset:4096\n"
  "ds_read_b64_tr_b16 a[214:215], v6 offset:4608\n"
  "s_mov_b32 s56, 0\n"
  "s_mov_b32 s57, 122880\n"
  "v_add_u32 v12, 122880, v9\n"
  "v_xor_b32 v13, 64, v12\n"
  "s_waitcnt lgkmcnt(0)\n"
  "s_barrier\n"
  "s_add_u32 s58, s80, 12288\n"
  "s_add_u32 s42, s42, 36864\n"
  "s_addc_u32 s43, s43, 0\n"
  "ds_read_b128 v[16:19], v12 offset:0\n"
  "ds_read_b128 v[20:23], v12 offset:2048\n"
  "ds_read_b128 v[24:27], v13 offset:0\n"
  "ds_read_b128 v[28:31], v13 offset:2048\n"
  "ds_read_b128 v[32:35], v12 offset:4096\n"
  "ds_read_b128 v[36:39], v12 offset:6144\n"
  "s_add_u32 m0, s58, 0\n"
  "s_nop 0\n"
  "global_load_lds_dwordx4 v3, s[42:43] offset:0\n"
  "s_add_u32 m0, s58, 3968\n"
  "s_nop 0\n"
  "global_load_lds_dwordx4 v3, s[42:43] offset:128\n"
  "s_add_u32 m0, s58, 7936\n"
  "s_nop 0\n"
  "global_load_lds_dwordx4 v3, s[42:43] offset:256\n"
  "s_add_u32 m0, s58, 11904\n"
  "s_nop 0\n"
  "global_load_lds_dwordx4 v3, s[42:43] offset:384\n"
  "s_add_u32 m0, s58, 15872\n"
  "s_nop 0\n"
  "global_load_lds_dwordx4 v3, s[42:43] offset:512\n"
  "s_add_u32 m0, s58, 19840\n"
  "s_nop 0\n"
  "global_load_lds_dwordx4 v3, s[42:43] offset:640\n"
  "s_add_u32 m0, s58, 23808\n"
  "s_nop 0\n"
  "global_load_lds_dwordx4 v3, s[42:43] offset:768\n"
  "s_add_u32 m0, s58, 27776\n"
  "s_nop 0\n"
  "global_load_lds_dwordx4 v3, s[42:43] offset:896\n"
  "s_add_u32 m0, s58, 31744\n"
  "s_nop 0\n"
  "global_load_lds_dwordx4 v3, s[42:43] offset:1024\n"
  "v_mov_b64 v[10:11], v[12:13]\n"
  "s_waitcnt lgkmcnt(5)\n"
  "v_mfma_f32_16x16x32_bf16 v[48:51], a[0:3], v[16:19], 0\n"
  "ds_read_b128 v[40:43], v11 offset:4096\n"
  "s_waitcnt lgkmcnt(5)\n"
  "v_mfma_f32_16x16x32_bf16 v[52:55], a[0:3], v[20:23], 0\n"
  "ds_read_b128 v[44:47], v11 offset:6144\n"
  "v_mfma_f32_16x16x32_bf16 v[56:59], a[72:75], v[16:19], 0\n"
  "s_add_u32 s57, s57, 36864\n"
  "s_cmp_eq_u32 s57, 159744\n"
  "v_mfma_f32_16x16x32_bf16 v[60:63], a[72:75], v[20:23], 0\n"
  "s_cselect_b32 s57, 12288, s57\n"
  "v_add_u32 v12, s57, v9\n"
  "v_mfma_f32_16x16x32_bf16 v[64:67], v[16:19], a[144:147], 0\n"
  "v_xor_b32 v13, 64, v12\n"
  "s_add_u32 s58, s57, 36864\n"
  "v_mfma_f32_16x16x32_bf16 v[68:71], v[20:23], a[144:147], 0\n"
  "s_cmp_eq_u32 s57, 122880\n"
  "s_cselect_b32 s58, 12288, s58\n"
  "s_waitcnt lgkmcnt(5)\n"
  "v_mfma_f32_16x16x32_bf16 v[48:51], a[4:7], v[24:27], v[48:51]\n"
  "ds_read_b128 v[16:19], v10 offset:8192\n"
  "s_waitcnt lgkmcnt(5)\n"
  "v_mfma_f32_16x16x32_bf16 v[52:55], a[4:7], v[28:31], v[52:55]\n"
  "ds_read_b128 v[20:23], v10 offset:10240\n"
  "v_mfma_f32_16x16x32_bf16 v[56:59], a[76:79], v[24:27], v[56:59]\n"
  "s_add_u32 s58, s58, s80\n"
  "s_cmp_lt_u32 s56, 62\n"
  "v_mfma_f32_16x16x32_bf16 v[60:63], a[76:79], v[28:31], v[60:63]\n"
  "s_cselect_b32 s74, 36864, 0\n"
  "s_add_u32 s42, s42, s74\n"
  "v_mfma_f32_16x16x32_bf16 v[64:67], v[24:27], a[148:151], v[64:67]\n"
  "s_addc_u32 s43, s43, 0\n"
  "v_mfma_f32_16x16x32_bf16 v[68:71], v[28:31], a[148:151], v[68:71]\n"
  "s_waitcnt lgkmcnt(5)\n"
  "v_mfma_f32_16x16x32_bf16 v[48:51], a[8:11], v[32:35], v[48:51]\n"
  "ds_read_b128 v[24:27], v11 offset:8192\n"
  "s_waitcnt lgkmcnt(5)\n"
  "v_mfma_f32_16x16x32_bf16 v[52:55], a[8:11], v[36:39], v[52:55]\n"
  "ds_read_b128 v[28:31], v11 offset:10240\n"
  "v_mfma_f32_16x16x32_bf16 v[56:59], a[80:83], v[32:35], v[56:59]\n"
  "v_mfma_f32_16x16x32_bf16 v[60:63], a[80:83], v[36:39], v[60:63]\n"
  "v_mfma_f32_16x16x32_bf16 v[64:67], v[32:35], a[152:155], v[64:67]\n"
  "v_mfma_f32_16x16x32_bf16 v[68:71], v[36:39], a[152:155], v[68:71]\n"
  "s_waitcnt lgkmcnt(5)\n"
  "v_mfma_f32_16x16x32_bf16 v[48:51], a[12:15], v[40:43], v[48:51]\n"
  "ds_read_b128 v[32:35], v10 offset:12288\n"
  "s_waitcnt lgkmcnt(5)\n"
  "v_mfma_f32_16x16x32_bf16 v[52:55], a[12:15], v[44:47], v[52:55]\n"
  "ds_read_b128 v[36:39], v10 offset:14336\n"
  "v_mfma_f32_16x16x32_bf16 v[56:59], a[84:87], v[40:43], v[56:59]\n"
  "v_mfma_f32_16x16x32_bf16 v[60:63], a[84:87], v[44:47], v[60:63]\n"
  "v_mfma_f32_16x16x32_bf16 v[64:67], v[40:43], a[156:159], v[64:67]\n"
  "v_mfma_f32_16x16x32_bf16 v[68:71], v[44:47], a[156:159], v[68:71]\n"
  "s_waitcnt lgkmcnt(5)\n"
  "v_mfma_f32_16x16x32_bf16 v[48:51], a[16:19], v[16:19], v[48:51]\n"
  "ds_read_b128 v[40:43], v11 offset:12288\n"
  "s_waitcnt lgkmcnt(5)\n"
  "v_mfma_f32_16x16x32_bf16 v[52:55], a[16:19], v[20:23], v[52:55]\n"
  "ds_read_b128 v[44:47], v11 offset:14336\n"
  "v_mfma_f32_16x16x32_bf16 v[56:59], a[88:91], v[16:19], v[56:59]\n"
  "v_mfma_f32_16x16x32_bf16 v[60:63], a[88:91], v[20:23], v[60:63]\n"
  "v_mfma_f32_16x16x32_bf16 v[64:67], v[16:19], a[160:163], v[64:67]\n"
  "v_mfma_f32_16x16x32_bf16 v[68:71], v[20:23], a[160:163], v[68:71]\n"
  "s_waitcnt lgkmcnt(5)\n"
  "v_mfma_f32_16x16x32_bf16 v[48:51], a[20:23], v[24:27], v[48:51]\n"
  "ds_read_b128 v[16:19], v10 offset:16384\n"
  "s_waitcnt lgkmcnt(5)\n"
  "v_mfma_f32_16x16x32_bf16 v[52:55], a[20:23], v[28:31], v[52:55]\n"
  "ds_read_b128 v[20:23], v10 offset:18432\n"
  "v_mfma_f32_16x16x32_bf16 v[56:59], a[92:95], v[24:27], v[56:59]\n"
  "v_mfma_f32_16x16x32_bf16 v[60:63], a[92:95], v[28:31], v[60:63]\n"
  "v_mfma_f32_16x16x32_bf16 v[64:67], v[24:27], a[164:167], v[64:67]\n"
  "v_mfma_f32_16x16x32_bf16 v[68:71], v[28:31], a[164:167], v[68:71]\n"
  "s_waitcnt lgkmcnt(5)\n"
  "v_mfma_f32_16x16x32_bf16 v[48:51], a[24:27], v[32:35], v[48:51]\n"
  "ds_read_b128 v[24:27], v11 offset:16384\n"
  "s_waitcnt lgkmcnt(5)\n"
  "v_mfma_f32_16x16x32_bf16 v[52:55], a[24:27], v[36:39], v[52:55]\n"
  "ds_read_b128 v[28:31], v11 offset:18432\n"
  "v_mfma_f32_16x16x32_bf16 v[56:59], a[96:99], v[32:35], v[56:59]\n"
  "v_mfma_f32_16x16x32_bf16 v[60:63], a[96:99], v[36:39], v[60:63]\n"
  "s_add_u32 m0, s58, 0\n"
  "s_nop 0\n"
  "global_load_lds_dwordx4 v3, s[42:43] offset:0\n"
  "v_mfma_f32_16x16x32_bf16 v[64:67], v[32:35], a[168:171], v[64:67]\n"
  "v_mfma_f32_16x16x32_bf16 v[68:71], v[36:39], a[168:171], v[68:71]\n"
  "s_waitcnt lgkmcnt(5)\n"
  "v_mfma_f32_16x16x32_bf16 v[48:51], a[28:31], v[40:43], v[48:51]\n"
  "ds_read_b128 v[32:35], v10 offset:20480\n"
  "s_waitcnt lgkmcnt(5)\n"
  "v_mfma_f32_16x16x32_bf16 v[52:55], a[28:31], v[44:47], v[52:55]\n"
  "ds_read_b128 v[36:39], v10 offset:22528\n"
  "v_mfma_f32_16x16x32_bf16 v[56:59], a[100:103], v[40:43], v[56:59]\n"
  "v_mfma_f32_16x16x32_bf16 v[60:63], a[100:103], v[44:47], v[60:63]\n"
  "s_add_u32 m0, s58, 3968\n"
  "s_nop 0\n"
  "global_load_lds_dwordx4 v3, s[42:43] offset:128\n"
  "v_mfma_f32_16x16x32_bf16 v[64:67], v[40:43], a[172:175], v[64:67]\n"
  "v_mfma_f32_16x16x32_bf16 v[68:71], v[44:47], a[172:175], v[68:71]\n"
  "s_waitcnt lgkmcnt(5)\n"
  "v_mfma_f32_16x16x32_bf16 v[48:51], a[32:35], v[16:19], v[48:51]\n"
  "ds_read_b128 v[40:43], v11 offset:20480\n"
  "s_waitcnt lgkmcnt(5)\n"
  "v_mfma_f32_16x16x32_bf16 v[52:55], a[32:35], v[20:23], v[52:55]\n"
  "ds_read_b128 v[44:47], v11 offset:22528\n"
  "v_mfma_f32_16x16x32_bf16 v[56:59], a[104:107], v[16:19], v[56:59]\n"
  "v_mfma_f32_16x16x32_bf16 v[60:63], a[104:107], v[20:23], v[60:63]\n"
  "s_add_u32 m0, s58, 7936\n"
  "s_nop 0\n"
  "global_load_lds_dwordx4 v3, s[42:43] offset:256\n"
  "v_mfma_f32_16x16x32_bf16 v[64:67], v[16:19], a[176:179], v[64:67]\n"
  "v_mfma_f32_16x16x32_bf16 v[68:71], v[20:23], a[176:179], v[68:71]\n"
  "s_waitcnt lgkmcnt(5)\n"
  "v_mfma_f32_16x16x32_bf16 v[48:51], a[36:39], v[24:27], v[48:51]\n"
  "ds_read_b128 v[16:19], v10 offset:24576\n"
  "s_waitcnt lgkmcnt(5)\n"
  "v_mfma_f32_16x16x32_bf16 v[52:55], a[36:39], v[28:31], v[52:55]\n"
  "ds_read_b128 v[20:23], v10 offset:26624\n"
  "v_mfma_f32_16x16x32_bf16 v[56:59], a[108:111], v[24:27], v[56:59]\n"
  "v_mfma_f32_16x16x32_bf16 v[60:63], a[108:111], v[28:31], v[60:63]\n"
  "s_add_u32 m0, s58, 11904\n"
  "s_nop 0\n"
  "global_load_lds_dwordx4 v3, s[42:43] offset:384\n"
  "v_mfma_f32_16x16x32_bf16 v[64:67], v[24:27], a[180:183], v[64:67]\n"
  "v_mfma_f32_16x16x32_bf16 v[68:71], v[28:31], a[180:183], v[68:71]\n"
  "s_waitcnt lgkmcnt(5)\n"
  "v_mfma_f32_16x16x32_bf16 v[48:51], a[40:43], v[32:35], v[48:51]\n"
  "ds_read_b128 v[24:27], v11 offset:24576\n"
  "s_waitcnt lgkmcnt(5)\n"
  "v_mfma_f32_16x16x32_bf16 v[52:55], a[40:43], v[36:39], v[52:55]\n"
  "ds_read_b128 v[28:31], v11 offset:26624\n"
  "v_mfma_f32_16x16x32_bf16 v[56:59], a[112:115], v[32:35], v[56:59]\n"
  "v_mfma_f32_16x16x32_bf16 v[60:63], a[112:115], v[36:39], v[60:63]\n"
  "s_add_u32 m0, s58, 15872\n"
  "s_nop 0\n"
  "global_load_lds_dwordx4 v3, s[42:43] offset:512\n"
  "v_mfma_f32_16x16x32_bf16 v[64:67], v[32:35], a[184:187], v[64:67]\n"
  "v_mfma_f32_16x16x32_bf16 v[68:71], v[36:39], a[184:187], v[68:71]\n"
  "s_waitcnt lgkmcnt(5)\n"
  "v_mfma_f32_16x16x32_bf16 v[48:51], a[44:47], v[40:43], v[48:51]\n"
  "ds_read_b128 v[32:35], v10 offset:28672\n"
  "s_waitcnt lgkmcnt(5)\n"
  "v_mfma_f32_16x16x32_bf16 v[52:55], a[44:47], v[44:47], v[52:55]\n"
  "ds_read_b128 v[36:39], v10 offset:30720\n"
  "v_mfma_f32_16x16x32_bf16 v[56:59], a[116:119], v[40:43], v[56:59]\n"
  "v_mfma_f32_16x16x32_bf16 v[60:63], a[116:119], v[44:47], v[60:63]\n"
  "s_add_u32 m0, s58, 19840\n"
  "s_nop 0\n"
  "global_load_lds_dwordx4 v3, s[42:43] offset:640\n"
  "v_mfma_f32_16x16x32_bf16 v[64:67], v[40:43], a[188:191], v[64:67]\n"
  "v_mfma_f32_16x16x32_bf16 v[68:71], v[44:47], a[188:191], v[68:71]\n"
  "s_waitcnt lgkmcnt(5)\n"
  "v_mfma_f32_16x16x32_bf16 v[48:51], a[48:51], v[16:19], v[48:51]\n"
  "ds_read_b128 v[40:43], v11 offset:28672\n"
  "s_waitcnt lgkmcnt(5)\n"
  "v_mfma_f32_16x16x32_bf16 v[52:55], a[48:51], v[20:23], v[52:55]\n"
  "ds_read_b128 v[44:47], v11 offset:30720\n"
  "v_mfma_f32_16x16x32_bf16 v[56:59], a[120:123], v[16:19], v[56:59]\n"
  "v_mfma_f32_16x16x32_bf16 v[60:63], a[120:123], v[20:23], v[60:63]\n"
  "s_add_u32 m0, s58, 23808\n"
  "s_nop 0\n"
  "global_load_lds_dwordx4 v3, s[42:43] offset:768\n"
  "v_mfma_f32_16x16x32_bf16 v[64:67], v[16:19], a[192:195], v[64:67]\n"
  "v_mfma_f32_16x16x32_bf16 v[68:71], v[20:23], a[192:195], v[68:71]\n"
  "s_waitcnt lgkmcnt(5)\n"
  "v_mfma_f32_16x16x32_bf16 v[48:51], a[52:55], v[24:27], v[48:51]\n"
  "ds_read_b128 v[16:19], v10 offset:32768\n"
  "s_waitcnt lgkmcnt(5)\n"
  "v_mfma_f32_16x16x32_bf16 v[52:55], a[52:55], v[28:31], v[52:55]\n"
  "ds_read_b128 v[20:23], v10 offset:34816\n"
  "v_mfma_f32_16x16x32_bf16 v[56:59], a[124:127], v[24:27], v[56:59]\n"
  "v_mfma_f32_16x16x32_bf16 v[60:63], a[124:127], v[28:31], v[60:63]\n"
  "s_add_u32 m0, s58, 27776\n"
  "s_nop 0\n"
  "global_load_lds_dwordx4 v3, s[42:43] offset:896\n"
  "v_mfma_f32_16x16x32_bf16 v[64:67], v[24:27], a[196:199], v[64:67]\n"
  "v_mfma_f32_16x16x32_bf16 v[68:71], v[28:31], a[196:199], v[68:71]\n"
  "s_waitcnt lgkmcnt(5)\n"
  "v_mfma_f32_16x16x32_bf16 v[48:51], a[56:59], v[32:35], v[48:51]\n"
  "ds_read_b128 v[24:27], v11 offset:32768\n"
  "s_waitcnt lgkmcnt(5)\n"
  "v_mfma_f32_16x16x32_bf16 v[52:55], a[56:59], v[36:39], v[52:55]\n"
  "ds_read_b128 v[28:31], v11 offset:34816\n"
  "v_mfma_f32_16x16x32_bf16 v[56:59], a[128:131], v[32:35], v[56:59]\n"
  "v_mfma_f32_16x16x32_bf16 v[60:63], a[128:131], v[36:39], v[60:63]\n"
  "s_add_u32 m0, s58, 31744\n"
  "s_nop 0\n"
  "global_load_lds_dwordx4 v3, s[42:43] offset:1024\n"
  "v_mfma_f32_16x16x32_bf16 v[64:67], v[32:35], a[200:203], v[64:67]\n"
  "v_mfma_f32_16x16x32_bf16 v[68:71], v[36:39], a[200:203], v[68:71]\n"
  "s_waitcnt vmcnt(9)\n"
  "s_barrier\n"
  "s_waitcnt lgkmcnt(5)\n"
  "v_mfma_f32_16x16x32_bf16 v[48:51], a[60:63], v[40:43], v[48:51]\n"
  "ds_read_b128 v[32:35], v12 offset:0\n"
  "s_waitcnt lgkmcnt(5)\n"
  "v_mfma_f32_16x16x32_bf16 v[52:55], a[60:63], v[44:47], v[52:55]\n"
  "ds_read_b128 v[36:39], v12 offset:2048\n"
  "v_mfma_f32_16x16x32_bf16 v[56:59], a[132:135], v[40:43], v[56:59]\n"
  "v_mfma_f32_16x16x32_bf16 v[60:63], a[132:135], v[44:47], v[60:63]\n"
  "v_mfma_f32_16x16x32_bf16 v[64:67], v[40:43], a[204:207], v[64:67]\n"
  "v_mfma_f32_16x16x32_bf16 v[68:71], v[44:47], a[204:207], v[68:71]\n"
  "s_waitcnt lgkmcnt(5)\n"
  "v_mfma_f32_16x16x32_bf16 v[48:51], a[64:67], v[16:19], v[48:51]\n"
  "ds_read_b128 v[40:43], v13 offset:0\n"
  "s_waitcnt lgkmcnt(5)\n"
  "v_mfma_f32_16x16x32_bf16 v[52:55], a[64:67], v[20:23], v[52:55]\n"
  "ds_read_b128 v[44:47], v13 offset:2048\n"
  "v_mfma_f32_16x16x32_bf16 v[56:59], a[136:139], v[16:19], v[56:59]\n"
  "v_mfma_f32_16x16x32_bf16 v[60:63], a[136:139], v[20:23], v[60:63]\n"
  "v_mfma_f32_16x16x32_bf16 v[64:67], v[16:19], a[208:211], v[64:67]\n"
  "v_mfma_f32_16x16x32_bf16 v[68:71], v[20:23], a[208:211], v[68:71]\n"
  "s_waitcnt lgkmcnt(5)\n"
  "v_mfma_f32_16x16x32_bf16 v[48:51], a[68:71], v[24:27], v[48:51]\n"
  "ds_read_b128 v[16:19], v12 offset:4096\n"
  "s_waitcnt lgkmcnt(5)\n"
  "v_mfma_f32_16x16x32_bf16 v[52:55], a[68:71], v[28:31], v[52:55]\n"
  "ds_read_b128 v[20:23], v12 offset:6144\n"
  "v_mfma_f32_16x16x32_bf16 v[56:59], a[140:143], v[24:27], v[56:59]\n"
  "v_mfma_f32_16x16x32_bf16 v[60:63], a[140:143], v[28:31], v[60:63]\n"
  "v_mfma_f32_16x16x32_bf16 v[64:67], v[24:27], a[212:215], v[64:67]\n"
  "v_mfma_f32_16x16x32_bf16 v[68:71], v[28:31], a[212:215], v[68:71]\n"
  "s_add_u32 s56, s56, 1\n"
  "PJL:\n"
  "v_mov_b64 v[10:11], v[12:13]\n"
  "s_waitcnt lgkmcnt(5)\n"
  "v_mfma_f32_16x16x32_bf16 v[72:75], a[0:3], v[32:35], 0\n"
  "ds_read_b128 v[24:27], v11 offset:4096\n"
  "s_waitcnt lgkmcnt(5)\n"
  "v_mfma_f32_16x16x32_bf16 v[76:79], a[0:3], v[36:39], 0\n"
  "ds_read_b128 v[28:31], v11 offset:6144\n"
  "v_mfma_f32_16x16x32_bf16 v[80:83], a[72:75], v[32:35], 0\n"
  "s_add_u32 s57, s57, 36864\n"
  "s_cmp_eq_u32 s57, 159744\n"
  "v_mfma_f32_16x16x32_bf16 v[84:87], a[72:75], v[36:39], 0\n"
  "s_cselect_b32 s57, 12288, s57\n"
  "v_mul_f32 v48, v8, v48\n"
  "v_mfma_f32_16x16x32_bf16 v[88:91], v[32:35], a[144:147], 0\n"
  "v_mul_f32 v49, v8, v49\n"
  "v_mul_f32 v50, v8, v50\n"
  "v_mfma_f32_16x16x32_bf16 v[92:95], v[36:39], a[144:147], 0\n"
  "v_mul_f32 v51, v8, v51\n"
  "v_cvt_pk_bf16_f32 v96, v48, v49\n"
  "s_waitcnt lgkmcnt(5)\n"
  "v_mfma_f32_16x16x32_bf16 v[72:75], a[4:7], v[40:43], v[72:75]\n"
  "ds_read_b128 v[32:35], v10 offset:8192\n"
  "s_waitcnt lgkmcnt(5)\n"
  "v_mfma_f32_16x16x32_bf16 v[76:79], a[4:7], v[44:47], v[76:79]\n"
  "ds_read_b128 v[36:39], v10 offset:10240\n"
  "v_mfma_f32_16x16x32_bf16 v[80:83], a[76:79], v[40:43], v[80:83]\n"
  "v_cvt_pk_bf16_f32 v97, v50, v51\n"
  "v_mul_f32 v56, v8, v56\n"
  "v_mfma_f32_16x16x32_bf16 v[84:87], a[76:79], v[44:47], v[84:87]\n"
  "v_mul_f32 v57, v8, v57\n"
  "v_mul_f32 v58, v8, v58\n"
  "v_mfma_f32_16x16x32_bf16 v[88:91], v[40:43], a[148:151], v[88:91]\n"
  "v_mul_f32 v59, v8, v59\n"
  "v_cvt_pk_bf16_f32 v98, v56, v57\n"
  "v_mfma_f32_16x16x32_bf16 v[92:95], v[44:47], a[148:151], v[92:95]\n"
  "v_cvt_pk_bf16_f32 v99, v58, v59\n"
  "global_store_dwordx4 v0, v[96:99], s[60:61] offset:0 sc1\n"
  "s_waitcnt lgkmcnt(5)\n"
  "v_mfma_f32_16x16x32_bf16 v[72:75], a[8:11], v[16:19], v[72:75]\n"
  "ds_read_b128 v[40:43], v11 offset:8192\n"
  "s_waitcnt lgkmcnt(5)\n"
  "v_mfma_f32_16x16x32_bf16 v[76:79], a[8:11], v[20:23], v[76:79]\n"
  "ds_read_b128 v[44:47], v11 offset:10240\n"
  "v_mfma_f32_16x16x32_bf16 v[80:83], a[80:83], v[16:19], v[80:83]\n"
  "v_mul_f32 v52, v8, v52\n"
  "v_mul_f32 v53, v8, v53\n"
  "v_mfma_f32_16x16x32_bf16 v[84:87], a[80:83], v[20:23], v[84:87]\n"
  "v_mul_f32 v54, v8, v54\n"
  "v_mul_f32 v55, v8, v55\n"
  "v_mfma_f32_16x16x32_bf16 v[88:91], v[16:19], a[152:155], v[88:91]\n"
  "v_cvt_pk_bf16_f32 v100, v52, v53\n"
  "v_cvt_pk_bf16_f32 v101, v54, v55\n"
  "v_mfma_f32_16x16x32_bf16 v[92:95], v[20:23], a[152:155], v[92:95]\n"
  "v_mul_f32 v60, v8, v60\n"
  "v_mul_f32 v61, v8, v61\n"
  "s_waitcnt lgkmcnt(5)\n"
  "v_mfma_f32_16x16x32_bf16 v[72:75], a[12:15], v[24:27], v[72:75]\n"
  "ds_read_b128 v[16:19], v10 offset:12288\n"
  "s_waitcnt lgkmcnt(5)\n"
  "v_mfma_f32_16x16x32_bf16 v[76:79], a[12:15], v[28:31], v[76:79]\n"
  "ds_read_b128 v[20:23], v10 offset:14336\n"
  "v_mfma_f32_16x16x32_bf16 v[80:83], a[84:87], v[24:27], v[80:83]\n"
  "v_mul_f32 v62, v8, v62\n"
  "v_mul_f32 v63, v8, v63\n"
  "v_mfma_f32_16x16x32_bf16 v[84:87], a[84:87], v[28:31], v[84:87]\n"
  "v_cvt_pk_bf16_f32 v102, v60, v61\n"
  "v_cvt_pk_bf16_f32 v103, v62, v63\n"
  "v_mfma_f32_16x16x32_bf16 v[88:91], v[24:27], a[156:159], v[88:91]\n"
  "global_store_dwordx4 v0, v[100:103], s[60:61] offset:1024 sc1\n"
  "v_cvt_pk_bf16_f32 v104, v64, v65\n"
  "v_mfma_f32_16x16x32_bf16 v[92:95], v[28:31], a[156:159], v[92:95]\n"
  "v_cvt_pk_bf16_f32 v105, v66, v67\n"
  "v_cvt_pk_bf16_f32 v106, v68, v69\n"
  "s_waitcnt lgkmcnt(5)\n"
  "v_mfma_f32_16x16x32_bf16 v[72:75], a[16:19], v[32:35], v[72:75]\n"
  "ds_read_b128 v[24:27], v11 offset:12288\n"
  "s_waitcnt lgkmcnt(5)\n"
  "v_mfma_f32_16x16x32_bf16 v[76:79], a[16:19], v[36:39], v[76:79]\n"
  "ds_read_b128 v[28:31], v11 offset:14336\n"
  "v_mfma_f32_16x16x32_bf16 v[80:83], a[88:91], v[32:35], v[80:83]\n"
  "v_cvt_pk_bf16_f32 v107, v70, v71\n"
  "global_store_dwordx4 v0, v[104:107], s[62:63] offset:0 sc1\n"
  "v_mfma_f32_16x16x32_bf16 v[84:87], a[88:91], v[36:39], v[84:87]\n"
  "s_add_u32 s60, s60, s49\n"
  "s_addc_u32 s61, s61, 0\n"
  "v_mfma_f32_16x16x32_bf16 v[88:91], v[32:35], a[160:163], v[88:91]\n"
  "s_add_u32 s62, s62, 524288\n"
  "s_addc_u32 s63, s63, 0\n"
  "v_mfma_f32_16x16x32_bf16 v[92:95], v[36:39], a[160:163], v[92:95]\n"
  "v_add_u32 v12, s57, v9\n"
  "v_xor_b32 v13, 64, v12\n"
  "s_waitcnt lgkmcnt(5)\n"
  "v_mfma_f32_16x16x32_bf16 v[72:75], a[20:23], v[40:43], v[72:75]\n"
  "ds_read_b128 v[32:35], v10 offset:16384\n"
  "s_waitcnt lgkmcnt(5)\n"
  "v_mfma_f32_16x16x32_bf16 v[76:79], a[20:23], v[44:47], v[76:79]\n"
  "ds_read_b128 v[36:39], v10 offset:18432\n"
  "v_mfma_f32_16x16x32_bf16 v[80:83], a[92:95], v[40:43], v[80:83]\n"
  "s_add_u32 s58, s57, 36864\n"
  "s_cmp_eq_u32 s57, 122880\n"
  "v_mfma_f32_16x16x32_bf16 v[84:87], a[92:95], v[44:47], v[84:87]\n"
  "s_cselect_b32 s58, 12288, s58\n"
  "s_add_u32 s58, s58, s80\n"
  "v_mfma_f32_16x16x32_bf16 v[88:91], v[40:43], a[164:167], v[88:91]\n"
  "s_cmp_lt_u32 s56, 62\n"
  "s_cselect_b32 s74, 36864, 0\n"
  "v_mfma_f32_16x16x32_bf16 v[92:95], v[44:47], a[164:167], v[92:95]\n"
  "s_add_u32 s42, s42, s74\n"
  "s_addc_u32 s43, s43, 0\n"
  "s_waitcnt lgkmcnt(5)\n"
  "v_mfma_f32_16x16x32_bf16 v[72:75], a[24:27], v[16:19], v[72:75]\n"
  "ds_read_b128 v[40:43], v11 offset:16384\n"
  "s_waitcnt lgkmcnt(5)\n"
  "v_mfma_f32_16x16x32_bf16 v[76:79], a[24:27], v[20:23], v[76:79]\n"
  "ds_read_b128 v[44:47], v11 offset:18432\n"
  "v_mfma_f32_16x16x32_bf16 v[80:83], a[96:99], v[16:19], v[80:83]\n"
  "v_mfma_f32_16x16x32_bf16 v[84:87], a[96:99], v[20:23], v[84:87]\n"
  "s_add_u32 m0, s58, 0\n"
  "s_nop 0\n"
  "global_load_lds_dwordx4 v3, s[42:43] offset:0\n"
  "v_mfma_f32_16x16x32_bf16 v[88:91], v[16:19], a[168:171], v[88:91]\n"
  "v_mfma_f32_16x16x32_bf16 v[92:95], v[20:23], a[168:171], v[92:95]\n"
  "s_waitcnt lgkmcnt(5)\n"
  "v_mfma_f32_16x16x32_bf16 v[72:75], a[28:31], v[24:27], v[72:75]\n"
  "ds_read_b128 v[16:19], v10 offset:20480\n"
  "s_waitcnt lgkmcnt(5)\n"
  "v_mfma_f32_16x16x32_bf16 v[76:79], a[28:31], v[28:31], v[76:79]\n"
  "ds_read_b128 v[20:23], v10 offset:22528\n"
  "v_mfma_f32_16x16x32_bf16 v[80:83], a[100:103], v[24:27], v[80:83]\n"
  "v_mfma_f32_16x16x32_bf16 v[84:87], a[100:103], v[28:31], v[84:87]\n"
  "s_add_u32 m0, s58, 3968\n"
  "s_nop 0\n"
  "global_load_lds_dwordx4 v3, s[42:43] offset:128\n"
  "v_mfma_f32_16x16x32_bf16 v[88:91], v[24:27], a[172:175], v[88:91]\n"
  "v_mfma_f32_16x16x32_bf16 v[92:95], v[28:31], a[172:175], v[92:95]\n"
  "s_waitcnt lgkmcnt(5)\n"
  "v_mfma_f32_16x16x32_bf16 v[72:75], a[32:35], v[32:35], v[72:75]\n"
  "ds_read_b128 v[24:27], v11 offset:20480\n"
  "s_waitcnt lgkmcnt(5)\n"
  "v_mfma_f32_16x16x32_bf16 v[76:79], a[32:35], v[36:39], v[76:79]\n"
  "ds_read_b128 v[28:31], v11 offset:22528\n"
  "v_mfma_f32_16x16x32_bf16 v[80:83], a[104:107], v[32:35], v[80:83]\n"
  "v_mfma_f32_16x16x32_bf16 v[84:87], a[104:107], v[36:39], v[84:87]\n"
  "s_add_u32 m0, s58, 7936\n"
  "s_nop 0\n"
  "global_load_lds_dwordx4 v3, s[42:43] offset:256\n"
  "v_mfma_f32_16x16x32_bf16 v[88:91], v[32:35], a[176:179], v[88:91]\n"
  "v_mfma_f32_16x16x32_bf16 v[92:95], v[36:39], a[176:179], v[92:95]\n"
  "s_waitcnt lgkmcnt(5)\n"
  "v_mfma_f32_16x16x32_bf16 v[72:75], a[36:39], v[40:43], v[72:75]\n"
  "ds_read_b128 v[32:35], v10 offset:24576\n"
  "s_waitcnt lgkmcnt(5)\n"
  "v_mfma_f32_16x16x32_bf16 v[76:79], a[36:39], v[44:47], v[76:79]\n"
  "ds_read_b128 v[36:39], v10 offset:26624\n"
  "v_mfma_f32_16x16x32_bf16 v[80:83], a[108:111], v[40:43], v[80:83]\n"
  "v_mfma_f32_16x16x32_bf16 v[84:87], a[108:111], v[44:47], v[84:87]\n"
  "s_add_u32 m0, s58, 11904\n"
  "s_nop 0\n"
  "global_load_lds_dwordx4 v3, s[42:43] offset:384\n"
  "v_mfma_f32_16x16x32_bf16 v[88:91], v[40:43], a[180:183], v[88:91]\n"
  "v_mfma_f32_16x16x32_bf16 v[92:95], v[44:47], a[180:183], v[92:95]\n"
  "s_waitcnt lgkmcnt(5)\n"
  "v_mfma_f32_16x16x32_bf16 v[72:75], a[40:43], v[16:19], v[72:75]\n"
  "ds_read_b128 v[40:43], v11 offset:24576\n"
  "s_waitcnt lgkmcnt(5)\n"
  "v_mfma_f32_16x16x32_bf16 v[76:79], a[40:43], v[20:23], v[76:79]\n"
  "ds_read_b128 v[44:47], v11 offset:26624\n"
  "v_mfma_f32_16x16x32_bf16 v[80:83], a[112:115], v[16:19], v[80:83]\n"
  "v_mfma_f32_16x16x32_bf16 v[84:87], a[112:115], v[20:23], v[84:87]\n"
  "s_add_u32 m0, s58, 15872\n"
  "s_nop 0\n"
  "global_load_lds_dwordx4 v3, s[42:43] offset:512\n"
  "v_mfma_f32_16x16x32_bf16 v[88:91], v[16:19], a[184:187], v[88:91]\n"
  "v_mfma_f32_16x16x32_bf16 v[92:95], v[20:23], a[184:187], v[92:95]\n"
  "s_waitcnt lgkmcnt(5)\n"
  "v_mfma_f32_16x16x32_bf16 v[72:75], a[44:47], v[24:27], v[72:75]\n"
  "ds_read_b128 v[16:19], v10 offset:28672\n"
  "s_waitcnt lgkmcnt(5)\n"
  "v_mfma_f32_16x16x32_bf16 v[76:79], a[44:47], v[28:31], v[76:79]\n"
  "ds_read_b128 v[20:23], v10 offset:30720\n"
  "v_mfma_f32_16x16x32_bf16 v[80:83], a[116:119], v[24:27], v[80:83]\n"
  "v_mfma_f32_16x16x32_bf16 v[84:87], a[116:119], v[28:31], v[84:87]\n"
  "s_add_u32 m0, s58, 19840\n"
  "s_nop 0\n"
  "global_load_lds_dwordx4 v3, s[42:43] offset:640\n"
  "v_mfma_f32_16x16x32_bf16 v[88:91], v[24:27], a[188:191], v[88:91]\n"
  "v_mfma_f32_16x16x32_bf16 v[92:95], v[28:31], a[188:191], v[92:95]\n"
  "s_waitcnt lgkmcnt(5)\n"
  "v_mfma_f32_16x16x32_bf16 v[72:75], a[48:51], v[32:35], v[72:75]\n"
  "ds_read_b128 v[24:27], v11 offset:28672\n"
  "s_waitcnt lgkmcnt(5)\n"
  "v_mfma_f32_16x16x32_bf16 v[76:79], a[48:51], v[36:39], v[76:79]\n"
  "ds_read_b128 v[28:31], v11 offset:30720\n"
  "v_mfma_f32_16x16x32_bf16 v[80:83], a[120:123], v[32:35], v[80:83]\n"
  "v_mfma_f32_16x16x32_bf16 v[84:87], a[120:123], v[36:39], v[84:87]\n"
  "s_add_u32 m0, s58, 23808\n"
  "s_nop 0\n"
  "global_load_lds_dwordx4 v3, s[42:43] offset:768\n"
  "v_mfma_f32_16x16x32_bf16 v[88:91], v[32:35], a[192:195], v[88:91]\n"
  "v_mfma_f32_16x16x32_bf16 v[92:95], v[36:39], a[192:195], v[92:95]\n"
  "s_waitcnt lgkmcnt(5)\n"
  "v_mfma_f32_16x16x32_bf16 v[72:75], a[52:55], v[40:43], v[72:75]\n"
  "ds_read_b128 v[32:35], v10 offset:32768\n"
  "s_waitcnt lgkmcnt(5)\n"
  "v_mfma_f32_16x16x32_bf16 v[76:79], a[52:55], v[44:47], v[76:79]\n"
  "ds_read_b128 v[36:39], v10 offset:34816\n"
  "v_mfma_f32_16x16x32_bf16 v[80:83], a[124:127], v[40:43], v[80:83]\n"
  "v_mfma_f32_16x16x32_bf16 v[84:87], a[124:127], v[44:47], v[84:87]\n"
  "s_add_u32 m0, s58, 27776\n"
  "s_nop 0\n"
  "global_load_lds_dwordx4 v3, s[42:43] offset:896\n"
  "v_mfma_f32_16x16x32_bf16 v[88:91], v[40:43], a[196:199], v[88:91]\n"
  "v_mfma_f32_16x16x32_bf16 v[92:95], v[44:47], a[196:199], v[92:95]\n"
  "s_waitcnt lgkmcnt(5)\n"
  "v_mfma_f32_16x16x32_bf16 v[72:75], a[56:59], v[16:19], v[72:75]\n"
  "ds_read_b128 v[40:43], v11 offset:32768\n"
  "s_waitcnt lgkmcnt(5)\n"
  "v_mfma_f32_16x16x32_bf16 v[76:79], a[56:59], v[20:23], v[76:79]\n"
  "ds_read_b128 v[44:47], v11 offset:34816\n"
  "v_mfma_f32_16x16x32_bf16 v[80:83], a[128:131], v[16:19], v[80:83]\n"
  "v_mfma_f32_16x16x32_bf16 v[84:87], a[128:131], v[20:23], v[84:87]\n"
  "s_add_u32 m0, s58, 31744\n"
  "s_nop 0\n"
  "global_load_lds_dwordx4 v3, s[42:43] offset:1024\n"
  "v_mfma_f32_16x16x32_bf16 v[88:91], v[16:19], a[200:203], v[88:91]\n"
  "v_mfma_f32_16x16x32_bf16 v[92:95], v[20:23], a[200:203], v[92:95]\n"
  "s_waitcnt vmcnt(9)\n"
  "s_barrier\n"
  "s_waitcnt lgkmcnt(5)\n"
  "v_mfma_f32_16x16x32_bf16 v[72:75], a[60:63], v[24:27], v[72:75]\n"
  "ds_read_b128 v[16:19], v12 offset:0\n"
  "s_waitcnt lgkmcnt(5)\n"
  "v_mfma_f32_16x16x32_bf16 v[76:79], a[60:63], v[28:31], v[76:79]\n"
  "ds_read_b128 v[20:23], v12 offset:2048\n"
  "v_mfma_f32_16x16x32_bf16 v[80:83], a[132:135], v[24:27], v[80:83]\n"
  "v_mov_b32 v6, 0\n"
  "v_mov_b32 v7, s56\n"
  "s_cmp_eq_u32 s47, 3\n"
  "s_cselect_b64 exec, 1, 0\n"
  "global_store_dword v6, v7, s[64:65] sc1\n"
  "s_mov_b64 exec, -1\n"
  "v_mfma_f32_16x16x32_bf16 v[84:87], a[132:135], v[28:31], v[84:87]\n"
  "v_mfma_f32_16x16x32_bf16 v[88:91], v[24:27], a[204:207], v[88:91]\n"
  "v_mfma_f32_16x16x32_bf16 v[92:95], v[28:31], a[204:207], v[92:95]\n"
  "s_waitcnt lgkmcnt(5)\n"
  "v_mfma_f32_16x16x32_bf16 v[72:75], a[64:67], v[32:35], v[72:75]\n"
  "ds_read_b128 v[24:27], v13 offset:0\n"
  "s_waitcnt lgkmcnt(5)\n"
  "v_mfma_f32_16x16x32_bf16 v[76:79], a[64:67], v[36:39], v[76:79]\n"
  "ds_read_b128 v[28:31], v13 offset:2048\n"
  "v_mfma_f32_16x16x32_bf16 v[80:83], a[136:139], v[32:35], v[80:83]\n"
  "v_mfma_f32_16x16x32_bf16 v[84:87], a[136:139], v[36:39], v[84:87]\n"
  "v_mfma_f32_16x16x32_bf16 v[88:91], v[32:35], a[208:211], v[88:91]\n"
  "v_mfma_f32_16x16x32_bf16 v[92:95], v[36:39], a[208:211], v[92:95]\n"
  "s_waitcnt lgkmcnt(5)\n"
  "v_mfma_f32_16x16x32_bf16 v[72:75], a[68:71], v[40:43], v[72:75]\n"
  "ds_read_b128 v[32:35], v12 offset:4096\n"
  "s_waitcnt lgkmcnt(5)\n"
  "v_mfma_f32_16x16x32_bf16 v[76:79], a[68:71], v[44:47], v[76:79]\n"
  "ds_read_b128 v[36:39], v12 offset:6144\n"
  "v_mfma_f32_16x16x32_bf16 v[80:83], a[140:143], v[40:43], v[80:83]\n"
  "v_mfma_f32_16x16x32_bf16 v[84:87], a[140:143], v[44:47], v[84:87]\n"
  "v_mfma_f32_16x16x32_bf16 v[88:91], v[40:43], a[212:215], v[88:91]\n"
  "v_mfma_f32_16x16x32_bf16 v[92:95], v[44:47], a[212:215], v[92:95]\n"
  "s_add_u32 s56, s56, 1\n"
  "s_cmp_eq_u32 s56, 64\n"
  "s_cbranch_scc1 PJE\n"
  "v_mov_b64 v[10:11], v[12:13]\n"
  "s_waitcnt lgkmcnt(5)\n"
  "v_mfma_f32_16x16x32_bf16 v[48:51], a[0:3], v[16:19], 0\n"
  "ds_read_b128 v[40:43], v11 offset:4096\n"
  "s_waitcnt lgkmcnt(5)\n"
  "v_mfma_f32_16x16x32_bf16 v[52:55], a[0:3], v[20:23], 0\n"
  "ds_read_b128 v[44:47], v11 offset:6144\n"
  "v_mfma_f32_16x16x32_bf16 v[56:59], a[72:75], v[16:19], 0\n"
  "s_add_u32 s57, s57, 36864\n"
  "s_cmp_eq_u32 s57, 159744\n"
  "v_mfma_f32_16x16x32_bf16 v[60:63], a[72:75], v[20:23], 0\n"
  "s_cselect_b32 s57, 12288, s57\n"
  "v_mul_f32 v72, v8, v72\n"
  "v_mfma_f32_16x16x32_bf16 v[64:67], v[16:19], a[144:147], 0\n"
  "v_mul_f32 v73, v8, v73\n"
  "v_mul_f32 v74, v8, v74\n"
  "v_mfma_f32_16x16x32_bf16 v[68:71], v[20:23], a[144:147], 0\n"
  "v_mul_f32 v75, v8, v75\n"
  "v_cvt_pk_bf16_f32 v96, v72, v73\n"
  "s_waitcnt lgkmcnt(5)\n"
  "v_mfma_f32_16x16x32_bf16 v[48:51], a[4:7], v[24:27], v[48:51]\n"
  "ds_read_b128 v[16:19], v10 offset:8192\n"
  "s_waitcnt lgkmcnt(5)\n"
  "v_mfma_f32_16x16x32_bf16 v[52:55], a[4:7], v[28:31], v[52:55]\n"
  "ds_read_b128 v[20:23], v10 offset:10240\n"
  "v_mfma_f32_16x16x32_bf16 v[56:59], a[76:79], v[24:27], v[56:59]\n"
  "v_cvt_pk_bf16_f32 v97, v74, v75\n"
  "v_mul_f32 v80, v8, v80\n"
  "v_mfma_f32_16x16x32_bf16 v[60:63], a[76:79], v[28:31], v[60:63]\n"
  "v_mul_f32 v81, v8, v81\n"
  "v_mul_f32 v82, v8, v82\n"
  "v_mfma_f32_16x16x32_bf16 v[64:67], v[24:27], a[148:151], v[64:67]\n"
  "v_mul_f32 v83, v8, v83\n"
  "v_cvt_pk_bf16_f32 v98, v80, v81\n"
  "v_mfma_f32_16x16x32_bf16 v[68:71], v[28:31], a[148:151], v[68:71]\n"
  "v_cvt_pk_bf16_f32 v99, v82, v83\n"
  "global_store_dwordx4 v0, v[96:99], s[60:61] offset:0 sc1\n"
  "s_waitcnt lgkmcnt(5)\n"
  "v_mfma_f32_16x16x32_bf16 v[48:51], a[8:11], v[32:35], v[48:51]\n"
  "ds_read_b128 v[24:27], v11 offset:8192\n"
  "s_waitcnt lgkmcnt(5)\n"
  "v_mfma_f32_16x16x32_bf16 v[52:55], a[8:11], v[36:39], v[52:55]\n"
  "ds_read_b128 v[28:31], v11 offset:10240\n"
  "v_mfma_f32_16x16x32_bf16 v[56:59], a[80:83], v[32:35], v[56:59]\n"
  "v_mul_f32 v76, v8, v76\n"
  "v_mul_f32 v77, v8, v77\n"
  "v_mfma_f32_16x16x32_bf16 v[60:63], a[80:83], v[36:39], v[60:63]\n"
  "v_mul_f32 v78, v8, v78\n"
  "v_mul_f32 v79, v8, v79\n"
  "v_mfma_f32_16x16x32_bf16 v[64:67], v[32:35], a[152:155], v[64:67]\n"
  "v_cvt_pk_bf16_f32 v100, v76, v77\n"
  "v_cvt_pk_bf16_f32 v101, v78, v79\n"
  "v_mfma_f32_16x16x32_bf16 v[68:71], v[36:39], a[152:155], v[68:71]\n"
  "v_mul_f32 v84, v8, v84\n"
  "v_mul_f32 v85, v8, v85\n"
  "s_waitcnt lgkmcnt(5)\n"
  "v_mfma_f32_16x16x32_bf16 v[48:51], a[12:15], v[40:43], v[48:51]\n"
  "ds_read_b128 v[32:35], v10 offset:12288\n"
  "s_waitcnt lgkmcnt(5)\n"
  "v_mfma_f32_16x16x32_bf16 v[52:55], a[12:15], v[44:47], v[52:55]\n"
  "ds_read_b128 v[36:39], v10 offset:14336\n"
  "v_mfma_f32_16x16x32_bf16 v[56:59], a[84:87], v[40:43], v[56:59]\n"
  "v_mul_f32 v86, v8, v86\n"
  "v_mul_f32 v87, v8, v87\n"
  "v_mfma_f32_16x16x32_bf16 v[60:63], a[84:87], v[44:47], v[60:63]\n"
  "v_cvt_pk_bf16_f32 v102, v84, v85\n"
  "v_cvt_pk_bf16_f32 v103, v86, v87\n"
  "v_mfma_f32_16x16x32_bf16 v[64:67], v[40:43], a[156:159], v[64:67]\n"
  "global_store_dwordx4 v0, v[100:103], s[60:61] offset:1024 sc1\n"
  "v_cvt_pk_bf16_f32 v104, v88, v89\n"
  "v_mfma_f32_16x16x32_bf16 v[68:71], v[44:47], a[156:159], v[68:71]\n"
  "v_cvt_pk_bf16_f32 v105, v90, v91\n"
  "v_cvt_pk_bf16_f32 v106, v92, v93\n"
  "s_waitcnt lgkmcnt(5)\n"
  "v_mfma_f32_16x16x32_bf16 v[48:51], a[16:19], v[16:19], v[48:51]\n"
  "ds_read_b128 v[40:43], v11 offset:12288\n"
  "s_waitcnt lgkmcnt(5)\n"
  "v_mfma_f32_16x16x32_bf16 v[52:55], a[16:19], v[20:23], v[52:55]\n"
  "ds_read_b128 v[44:47], v11 offset:14336\n"
  "v_mfma_f32_16x16x32_bf16 v[56:59], a[88:91], v[16:19], v[56:59]\n"
  "v_cvt_pk_bf16_f32 v107, v94, v95\n"
  "global_store_dwordx4 v0, v[104:107], s[62:63] offset:0 sc1\n"
  "v_mfma_f32_16x16x32_bf16 v[60:63], a[88:91], v[20:23], v[60:63]\n"
  "s_add_u32 s60, s60, s49\n"
  "s_addc_u32 s61, s61, 0\n"
  "v_mfma_f32_16x16x32_bf16 v[64:67], v[16:19], a[160:163], v[64:67]\n"
  "s_add_u32 s62, s62, 524288\n"
  "s_addc_u32 s63, s63, 0\n"
  "v_mfma_f32_16x16x32_bf16 v[68:71], v[20:23], a[160:163], v[68:71]\n"
  "v_add_u32 v12, s57, v9\n"
  "v_xor_b32 v13, 64, v12\n"
  "s_waitcnt lgkmcnt(5)\n"
  "v_mfma_f32_16x16x32_bf16 v[48:51], a[20:23], v[24:27], v[48:51]\n"
  "ds_read_b128 v[16:19], v10 offset:16384\n"
  "s_waitcnt lgkmcnt(5)\n"
  "v_mfma_f32_16x16x32_bf16 v[52:55], a[20:23], v[28:31], v[52:55]\n"
  "ds_read_b128 v[20:23], v10 offset:18432\n"
  "v_mfma_f32_16x16x32_bf16 v[56:59], a[92:95], v[24:27], v[56:59]\n"
  "s_add_u32 s58, s57, 36864\n"
  "s_cmp_eq_u32 s57, 122880\n"
  "v_mfma_f32_16x16x32_bf16 v[60:63], a[92:95], v[28:31], v[60:63]\n"
  "s_cselect_b32 s58, 12288, s58\n"
  "s_add_u32 s58, s58, s80\n"
  "v_mfma_f32_16x16x32_bf16 v[64:67], v[24:27], a[164:167], v[64:67]\n"
  "s_cmp_lt_u32 s56, 62\n"
  "s_cselect_b32 s74, 36864, 0\n"
  "v_mfma_f32_16x16x32_bf16 v[68:71], v[28:31], a[164:167], v[68:71]\n"
  "s_add_u32 s42, s42, s74\n"
  "s_addc_u32 s43, s43, 0\n"
  "s_waitcnt lgkmcnt(5)\n"
  "v_mfma_f32_16x16x32_bf16 v[48:51], a[24:27], v[32:35], v[48:51]\n"
  "ds_read_b128 v[24:27], v11 offset:16384\n"
  "s_waitcnt lgkmcnt(5)\n"
  "v_mfma_f32_16x16x32_bf16 v[52:55], a[24:27], v[36:39], v[52:55]\n"
  "ds_read_b128 v[28:31], v11 offset:18432\n"
  "v_mfma_f32_16x16x32_bf16 v[56:59], a[96:99], v[32:35], v[56:59]\n"
  "v_mfma_f32_16x16x32_bf16 v[60:63], a[96:99], v[36:39], v[60:63]\n"
  "s_add_u32 m0, s58, 0\n"
  "s_nop 0\n"
  "global_load_lds_dwordx4 v3, s[42:43] offset:0\n"
  "v_mfma_f32_16x16x32_bf16 v[64:67], v[32:35], a[168:171], v[64:67]\n"
  "v_mfma_f32_16x16x32_bf16 v[68:71], v[36:39], a[168:171], v[68:71]\n"
  "s_waitcnt lgkmcnt(5)\n"
  "v_mfma_f32_16x16x32_bf16 v[48:51], a[28:31], v[40:43], v[48:51]\n"
  "ds_read_b128 v[32:35], v10 offset:20480\n"
  "s_waitcnt lgkmcnt(5)\n"
  "v_mfma_f32_16x16x32_bf16 v[52:55], a[28:31], v[44:47], v[52:55]\n"
  "ds_read_b128 v[36:39], v10 offset:22528\n"
  "v_mfma_f32_16x16x32_bf16 v[56:59], a[100:103], v[40:43], v[56:59]\n"
  "v_mfma_f32_16x16x32_bf16 v[60:63], a[100:103], v[44:47], v[60:63]\n"
  "s_add_u32 m0, s58, 3968\n"
  "s_nop 0\n"
  "global_load_lds_dwordx4 v3, s[42:43] offset:128\n"
  "v_mfma_f32_16x16x32_bf16 v[64:67], v[40:43], a[172:175], v[64:67]\n"
  "v_mfma_f32_16x16x32_bf16 v[68:71], v[44:47], a[172:175], v[68:71]\n"
  "s_waitcnt lgkmcnt(5)\n"
  "v_mfma_f32_16x16x32_bf16 v[48:51], a[32:35], v[16:19], v[48:51]\n"
  "ds_read_b128 v[40:43], v11 offset:20480\n"
  "s_waitcnt lgkmcnt(5)\n"
  "v_mfma_f32_16x16x32_bf16 v[52:55], a[32:35], v[20:23], v[52:55]\n"
  "ds_read_b128 v[44:47], v11 offset:22528\n"
  "v_mfma_f32_16x16x32_bf16 v[56:59], a[104:107], v[16:19], v[56:59]\n"
  "v_mfma_f32_16x16x32_bf16 v[60:63], a[104:107], v[20:23], v[60:63]\n"
  "s_add_u32 m0, s58, 7936\n"
  "s_nop 0\n"
  "global_load_lds_dwordx4 v3, s[42:43] offset:256\n"
  "v_mfma_f32_16x16x32_bf16 v[64:67], v[16:19], a[176:179], v[64:67]\n"
  "v_mfma_f32_16x16x32_bf16 v[68:71], v[20:23], a[176:179], v[68:71]\n"
  "s_waitcnt lgkmcnt(5)\n"
  "v_mfma_f32_16x16x32_bf16 v[48:51], a[36:39], v[24:27], v[48:51]\n"
  "ds_read_b128 v[16:19], v10 offset:24576\n"
  "s_waitcnt lgkmcnt(5)\n"
  "v_mfma_f32_16x16x32_bf16 v[52:55], a[36:39], v[28:31], v[52:55]\n"
  "ds_read_b128 v[20:23], v10 offset:26624\n"
  "v_mfma_f32_16x16x32_bf16 v[56:59], a[108:111], v[24:27], v[56:59]\n"
  "v_mfma_f32_16x16x32_bf16 v[60:63], a[108:111], v[28:31], v[60:63]\n"
  "s_add_u32 m0, s58, 11904\n"
  "s_nop 0\n"
  "global_load_lds_dwordx4 v3, s[42:43] offset:384\n"
  "v_mfma_f32_16x16x32_bf16 v[64:67], v[24:27], a[180:183], v[64:67]\n"
  "v_mfma_f32_16x16x32_bf16 v[68:71], v[28:31], a[180:183], v[68:71]\n"
  "s_waitcnt lgkmcnt(5)\n"
  "v_mfma_f32_16x16x32_bf16 v[48:51], a[40:43], v[32:35], v[48:51]\n"
  "ds_read_b128 v[24:27], v11 offset:24576\n"
  "s_waitcnt lgkmcnt(5)\n"
  "v_mfma_f32_16x16x32_bf16 v[52:55], a[40:43], v[36:39], v[52:55]\n"
  "ds_read_b128 v[28:31], v11 offset:26624\n"
  "v_mfma_f32_16x16x32_bf16 v[56:59], a[112:115], v[32:35], v[56:59]\n"
  "v_mfma_f32_16x16x32_bf16 v[60:63], a[112:115], v[36:39], v[60:63]\n"
  "s_add_u32 m0, s58, 15872\n"
  "s_nop 0\n"
  "global_load_lds_dwordx4 v3, s[42:43] offset:512\n"
  "v_mfma_f32_16x16x32_bf16 v[64:67], v[32:35], a[184:187], v[64:67]\n"
  "v_mfma_f32_16x16x32_bf16 v[68:71], v[36:39], a[184:187], v[68:71]\n"
  "s_waitcnt lgkmcnt(5)\n"
  "v_mfma_f32_16x16x32_bf16 v[48:51], a[44:47], v[40:43], v[48:51]\n"
  "ds_read_b128 v[32:35], v10 offset:28672\n"
  "s_waitcnt lgkmcnt(5)\n"
  "v_mfma_f32_16x16x32_bf16 v[52:55], a[44:47], v[44:47], v[52:55]\n"
  "ds_read_b128 v[36:39], v10 offset:30720\n"
  "v_mfma_f32_16x16x32_bf16 v[56:59], a[116:119], v[40:43], v[56:59]\n"
  "v_mfma_f32_16x16x32_bf16 v[60:63], a[116:119], v[44:47], v[60:63]\n"
  "s_add_u32 m0, s58, 19840\n"
  "s_nop 0\n"
  "global_load_lds_dwordx4 v3, s[42:43] offset:640\n"
  "v_mfma_f32_16x16x32_bf16 v[64:67], v[40:43], a[188:191], v[64:67]\n"
  "v_mfma_f32_16x16x32_bf16 v[68:71], v[44:47], a[188:191], v[68:71]\n"
  "s_waitcnt lgkmcnt(5)\n"
  "v_mfma_f32_16x16x32_bf16 v[48:51], a[48:51], v[16:19], v[48:51]\n"
  "ds_read_b128 v[40:43], v11 offset:28672\n"
  "s_waitcnt lgkmcnt(5)\n"
  "v_mfma_f32_16x16x32_bf16 v[52:55], a[48:51], v[20:23], v[52:55]\n"
  "ds_read_b128 v[44:47], v11 offset:30720\n"
  "v_mfma_f32_16x16x32_bf16 v[56:59], a[120:123], v[16:19], v[56:59]\n"
  "v_mfma_f32_16x16x32_bf16 v[60:63], a[120:123], v[20:23], v[60:63]\n"
  "s_add_u32 m0, s58, 23808\n"
  "s_nop 0\n"
  "global_load_lds_dwordx4 v3, s[42:43] offset:768\n"
  "v_mfma_f32_16x16x32_bf16 v[64:67], v[16:19], a[192:195], v[64:67]\n"
  "v_mfma_f32_16x16x32_bf16 v[68:71], v[20:23], a[192:195], v[68:71]\n"
  "s_waitcnt lgkmcnt(5)\n"
  "v_mfma_f32_16x16x32_bf16 v[48:51], a[52:55], v[24:27], v[48:51]\n"
  "ds_read_b128 v[16:19], v10 offset:32768\n"
  "s_waitcnt lgkmcnt(5)\n"
  "v_mfma_f32_16x16x32_bf16 v[52:55], a[52:55], v[28:31], v[52:55]\n"
  "ds_read_b128 v[20:23], v10 offset:34816\n"
  "v_mfma_f32_16x16x32_bf16 v[56:59], a[124:127], v[24:27], v[56:59]\n"
  "v_mfma_f32_16x16x32_bf16 v[60:63], a[124:127], v[28:31], v[60:63]\n"
  "s_add_u32 m0, s58, 27776\n"
  "s_nop 0\n"
  "global_load_lds_dwordx4 v3, s[42:43] offset:896\n"
  "v_mfma_f32_16x16x32_bf16 v[64:67], v[24:27], a[196:199], v[64:67]\n"
  "v_mfma_f32_16x16x32_bf16 v[68:71], v[28:31], a[196:199], v[68:71]\n"
  "s_waitcnt lgkmcnt(5)\n"
  "v_mfma_f32_16x16x32_bf16 v[48:51], a[56:59], v[32:35], v[48:51]\n"
  "ds_read_b128 v[24:27], v11 offset:32768\n"
  "s_waitcnt lgkmcnt(5)\n"
  "v_mfma_f32_16x16x32_bf16 v[52:55], a[56:59], v[36:39], v[52:55]\n"
  "ds_read_b128 v[28:31], v11 offset:34816\n"
  "v_mfma_f32_16x16x32_bf16 v[56:59], a[128:131], v[32:35], v[56:59]\n"
  "v_mfma_f32_16x16x32_bf16 v[60:63], a[128:131], v[36:39], v[60:63]\n"
  "s_add_u32 m0, s58, 31744\n"
  "s_nop 0\n"
  "global_load_lds_dwordx4 v3, s[42:43] offset:1024\n"
  "v_mfma_f32_16x16x32_bf16 v[64:67], v[32:35], a[200:203], v[64:67]\n"
  "v_mfma_f32_16x16x32_bf16 v[68:71], v[36:39], a[200:203], v[68:71]\n"
  "s_waitcnt vmcnt(9)\n"
  "s_barrier\n"
  "s_waitcnt lgkmcnt(5)\n"
  "v_mfma_f32_16x16x32_bf16 v[48:51], a[60:63], v[40:43], v[48:51]\n"
  "ds_read_b128 v[32:35], v12 offset:0\n"
  "s_waitcnt lgkmcnt(5)\n"
  "v_mfma_f32_16x16x32_bf16 v[52:55], a[60:63], v[44:47], v[52:55]\n"
  "ds_read_b128 v[36:39], v12 offset:2048\n"
  "v_mfma_f32_16x16x32_bf16 v[56:59], a[132:135], v[40:43], v[56:59]\n"
  "v_mov_b32 v6, 0\n"
  "v_mov_b32 v7, s56\n"
  "s_cmp_eq_u32 s47, 3\n"
  "s_cselect_b64 exec, 1, 0\n"
  "global_store_dword v6, v7, s[64:65] sc1\n"
  "s_mov_b64 exec, -1\n"
  "v_mfma_f32_16x16x32_bf16 v[60:63], a[132:135], v[44:47], v[60:63]\n"
  "v_mfma_f32_16x16x32_bf16 v[64:67], v[40:43], a[204:207], v[64:67]\n"
  "v_mfma_f32_16x16x32_bf16 v[68:71], v[44:47], a[204:207], v[68:71]\n"
  "s_waitcnt lgkmcnt(5)\n"
  "v_mfma_f32_16x16x32_bf16 v[48:51], a[64:67], v[16:19], v[48:51]\n"
  "ds_read_b128 v[40:43], v13 offset:0\n"
  "s_waitcnt lgkmcnt(5)\n"
  "v_mfma_f32_16x16x32_bf16 v[52:55], a[64:67], v[20:23], v[52:55]\n"
  "ds_read_b128 v[44:47], v13 offset:2048\n"
  "v_mfma_f32_16x16x32_bf16 v[56:59], a[136:139], v[16:19], v[56:59]\n"
  "v_mfma_f32_16x16x32_bf16 v[60:63], a[136:139], v[20:23], v[60:63]\n"
  "v_mfma_f32_16x16x32_bf16 v[64:67], v[16:19], a[208:211], v[64:67]\n"
  "v_mfma_f32_16x16x32_bf16 v[68:71], v[20:23], a[208:211], v[68:71]\n"
  "s_waitcnt lgkmcnt(5)\n"
  "v_mfma_f32_16x16x32_bf16 v[48:51], a[68:71], v[24:27], v[48:51]\n"
  "ds_read_b128 v[16:19], v12 offset:4096\n"
  "s_waitcnt lgkmcnt(5)\n"
  "v_mfma_f32_16x16x32_bf16 v[52:55], a[68:71], v[28:31], v[52:55]\n"
  "ds_read_b128 v[20:23], v12 offset:6144\n"
  "v_mfma_f32_16x16x32_bf16 v[56:59], a[140:143], v[24:27], v[56:59]\n"
  "v_mfma_f32_16x16x32_bf16 v[60:63], a[140:143], v[28:31], v[60:63]\n"
  "v_mfma_f32_16x16x32_bf16 v[64:67], v[24:27], a[212:215], v[64:67]\n"
  "v_mfma_f32_16x16x32_bf16 v[68:71], v[28:31], a[212:215], v[68:71]\n"
  "s_add_u32 s56, s56, 1\n"
  "s_branch PJL\n"
  "PJE:\n"
  "s_waitcnt vmcnt(0) lgkmcnt(0)\n"
  "s_nop 3\n"
  "v_mov_b32 v7, v7\n"
  "s_nop 2\n"
  "v_mov_b32 v7, v7\n"
  "s_nop 2\n"
  "v_mul_f32 v72, v8, v72\n"
  "v_mul_f32 v73, v8, v73\n"
  "v_mul_f32 v74, v8, v74\n"
  "v_mul_f32 v75, v8, v75\n"
  "v_cvt_pk_bf16_f32 v96, v72, v73\n"
  "v_cvt_pk_bf16_f32 v97, v74, v75\n"
  "v_mul_f32 v80, v8, v80\n"
  "v_mul_f32 v81, v8, v81\n"
  "v_mul_f32 v82, v8, v82\n"
  "v_mul_f32 v83, v8, v83\n"
  "v_cvt_pk_bf16_f32 v98, v80, v81\n"
  "v_cvt_pk_bf16_f32 v99, v82, v83\n"
  "global_store_dwordx4 v0, v[96:99], s[60:61] offset:0 sc1\n"
  "v_mul_f32 v76, v8, v76\n"
  "v_mul_f32 v77, v8, v77\n"
  "v_mul_f32 v78, v8, v78\n"
  "v_mul_f32 v79, v8, v79\n"
  "v_cvt_pk_bf16_f32 v100, v76, v77\n"
  "v_cvt_pk_bf16_f32 v101, v78, v79\n"
  "v_mul_f32 v84, v8, v84\n"
  "v_mul_f32 v85, v8, v85\n"
  "v_mul_f32 v86, v8, v86\n"
  "v_mul_f32 v87, v8, v87\n"
  "v_cvt_pk_bf16_f32 v102, v84, v85\n"
  "v_cvt_pk_bf16_f32 v103, v86, v87\n"
  "global_store_dwordx4 v0, v[100:103], s[60:61] offset:1024 sc1\n"
  "v_cvt_pk_bf16_f32 v104, v88, v89\n"
  "v_cvt_pk_bf16_f32 v105, v90, v91\n"
  "v_cvt_pk_bf16_f32 v106, v92, v93\n"
  "v_cvt_pk_bf16_f32 v107, v94, v95\n"
  "global_store_dwordx4 v0, v[104:107], s[62:63] offset:0 sc1\n"
  "s_add_u32 s60, s60, s49\n"
  "s_addc_u32 s61, s61, 0\n"
  "s_add_u32 s62, s62, 524288\n"
  "s_addc_u32 s63, s63, 0\n"
  "s_waitcnt vmcnt(0)\n"
  "s_barrier\n"
  "v_mov_b32 v6, 0\n"
  "v_mov_b32 v7, s56\n"
  "s_cmp_eq_u32 s47, 3\n"
  "s_cselect_b64 exec, 1, 0\n"
  "global_store_dword v6, v7, s[64:65] sc1\n"
  "s_mov_b64 exec, -1\n"
  "s_endpgm\n"
      : "+{v[0:15]}"(vin), "+{s[36:43]}"(sa), "+{s[44:59]}"(sb) :: CLOB);
  } else if (fd == 0 && tk >= 18 && tk < 29) {
    const int h = (tk - 18) * 3 + wave;
    if (wave == 3 || h >= 32) return;
    vin[1] = lane < 2 ? C_KVP + (2 * h + lane) * 128 : lane == 2 ? C_HF + h * 256 : C_VF + (2 * h + (lane & 1)) * 128;
    vin[2] = lane < 2 ? 2 : 1; vin[3] = qq * 256 + g * 64 + c4 * 16;
    vin[4] = wave * 2048 + lane * 16; vin[5] = wave * 2048 + c4 * 128 + (4 * g + qq) * 8;
    vin[6] = n - 4 * g;
    sb[0] = WO_KV + h * 16384 + 4096; sb[1] = WO_Q + h * 8192 + 4096; sb[2] = WO_KM + h * 2048; sb[3] = WO_VP + h * 2048;
    sb[4] = WO_O + h * 8192 + 4096; sb[5] = C_QAP + h * 128; sb[6] = __float_as_uint(QS); sb[7] = WO_ST + (role * 4 + wave) * 4096;
    UNIFORM(sa, 8); UNIFORM(sb, 16);
    asm volatile(
  "s_nop 4\n"
  "v_cmp_le_i32 s[8:9], 0, v6\n"
  "v_cmp_le_i32 s[10:11], 1, v6\n"
  "v_cmp_le_i32 s[12:13], 2, v6\n"
  "v_cmp_le_i32 s[14:15], 3, v6\n"
  "v_mov_b64 v[156:157], 0\n"
  "v_mov_b64 v[158:159], 0\n"
  "v_mov_b64 v[160:161], 0\n"
  "v_mov_b64 v[162:163], 0\n"
  "v_mov_b64 v[164:165], 0\n"
  "v_mov_b64 v[166:167], 0\n"
  "v_mov_b64 v[168:169], 0\n"
  "v_mov_b64 v[170:171], 0\n"
  "v_mov_b64 v[172:173], 0\n"
  "v_mov_b64 v[174:175], 0\n"
  "v_mov_b64 v[176:177], 0\n"
  "v_mov_b64 v[178:179], 0\n"
  "v_mov_b64 v[180:181], 0\n"
  "v_mov_b64 v[182:183], 0\n"
  "v_mov_b64 v[184:185], 0\n"
  "v_mov_b64 v[186:187], 0\n"
  "v_mov_b64 v[150:151], 0\n"
  "v_mov_b32 v12, 0xff7fffff\n"
  "s_mov_b32 s56, 0\n"
  "s_mov_b32 s57, 400000\n"
  "s_add_u32 s60, s36, s44\n"
  "s_addc_u32 s61, s37, 0\n"
  "s_add_u32 s62, s36, s45\n"
  "s_addc_u32 s63, s37, 0\n"
  "s_add_u32 s64, s36, s46\n"
  "s_addc_u32 s65, s37, 0\n"
  "s_add_u32 s66, s36, s47\n"
  "s_addc_u32 s67, s37, 0\n"
  "s_add_u32 s68, s36, s48\n"
  "s_addc_u32 s69, s37, 0\n"
  "s_add_u32 s70, s36, s49\n"
  "s_addc_u32 s71, s37, 0\n"
  "TPS:\n"
  "global_load_dword v9, v1, s[36:37] sc1\n"
  "v_add_u32 v7, s56, v2\n"
  "v_add_u32 v7, -1, v7\n"
  "v_min_i32 v7, 64, v7\n"
  "s_waitcnt vmcnt(0)\n"
  "v_cmp_gt_i32 vcc, v7, v9\n"
  "s_cbranch_vccz TGS\n"
  "s_sub_u32 s57, s57, 1\n"
  "s_cbranch_scc1 TQUIT\n"
  "s_sleep 2\n"
  "s_branch TPS\n"
  "TGS:\n"
  "global_load_dwordx4 v[16:19], v0, s[62:63] offset:-4096 sc1\n"
  "global_load_dwordx4 v[20:23], v0, s[62:63] offset:-3072 sc1\n"
  "global_load_dwordx4 v[24:27], v0, s[62:63] offset:-2048 sc1\n"
  "global_load_dwordx4 v[28:31], v0, s[62:63] offset:-1024 sc1\n"
  "global_load_dwordx4 v[32:35], v0, s[62:63] offset:0 sc1\n"
  "global_load_dwordx4 v[36:39], v0, s[62:63] offset:1024 sc1\n"
  "global_load_dwordx4 v[40:43], v0, s[62:63] offset:2048 sc1\n"
  "global_load_dwordx4 v[44:47], v0, s[62:63] offset:3072 sc1\n"
  "global_load_dwordx4 a[0:3], v0, s[60:61] offset:-4096 sc1\n"
  "global_load_dwordx4 a[4:7], v0, s[60:61] offset:-3072 sc1\n"
  "global_load_dwordx4 a[8:11], v0, s[60:61] offset:-2048 sc1\n"
  "global_load_dwordx4 a[12:15], v0, s[60:61] offset:-1024 sc1\n"
  "global_load_dwordx4 a[16:19], v0, s[60:61] offset:0 sc1\n"
  "global_load_dwordx4 a[20:23], v0, s[60:61] offset:1024 sc1\n"
  "global_load_dwordx4 a[24:27], v0, s[60:61] offset:2048 sc1\n"
  "global_load_dwordx4 a[28:31], v0, s[60:61] offset:3072 sc1\n"
  "s_add_u32 s60, s60, 8192\n"
  "s_addc_u32 s61, s61, 0\n"
  "global_load_dwordx4 a[32:35], v0, s[60:61] offset:-4096 sc1\n"
  "global_load_dwordx4 a[36:39], v0, s[60:61] offset:-3072 sc1\n"
  "global_load_dwordx4 a[40:43], v0, s[60:61] offset:-2048 sc1\n"
  "global_load_dwordx4 a[44:47], v0, s[60:61] offset:-1024 sc1\n"
  "global_load_dwordx4 a[48:51], v0, s[60:61] offset:0 sc1\n"
  "global_load_dwordx4 a[52:55], v0, s[60:61] offset:1024 sc1\n"
  "global_load_dwordx4 a[56:59], v0, s[60:61] offset:2048 sc1\n"
  "global_load_dwordx4 a[60:63], v0, s[60:61] offset:3072 sc1\n"
  "s_mov_b32 s56, -1\n"
  "s_cmp_eq_u32 s56, s56\n"
  "s_cselect_b32 s74, 262144, 0\n"
  "s_add_u32 s62, s62, s74\n"
  "s_addc_u32 s63, s63, 0\n"
  "s_lshl_b32 s74, s74, 1\n"
  "s_sub_u32 s60, s60, 8192\n"
  "s_subb_u32 s61, s61, 0\n"
  "s_add_u32 s60, s60, s74\n"
  "s_addc_u32 s61, s61, 0\n"
  "s_mov_b32 s56, 0\n"
  "TP0:\n"
  "global_load_dword v9, v1, s[36:37] sc1\n"
  "v_add_u32 v7, s56, v2\n"
  "v_add_u32 v7, 0, v7\n"
  "v_min_i32 v7, 64, v7\n"
  "s_waitcnt vmcnt(0)\n"
  "v_cmp_gt_i32 vcc, v7, v9\n"
  "s_cbranch_vccz TG0\n"
  "s_sub_u32 s57, s57, 1\n"
  "s_cbranch_scc1 TQUIT\n"
  "s_sleep 2\n"
  "s_branch TP0\n"
  "TG0:\n"
  "TGO0:\n"
  "s_waitcnt vmcnt(8)\n"
  "global_load_dwordx4 v[80:83], v0, s[64:65] offset:0 sc1\n"
  "global_load_dwordx4 v[84:87], v0, s[64:65] offset:1024 sc1\n"
  "global_load_dwordx4 v[96:99], v3, s[66:67] offset:0 sc1\n"
  "global_load_dwordx4 v[100:103], v3, s[66:67] offset:1024 sc1\n"
  "global_load_dword v9, v1, s[36:37] sc1\n"
  "s_add_u32 s64, s64, 65536\n"
  "s_addc_u32 s65, s65, 0\n"
  "s_add_u32 s66, s66, 65536\n"
  "s_addc_u32 s67, s67, 0\n"
  "v_mfma_f32_16x16x32_bf16 v[120:123], a[0:3], v[16:19], 0\n"
  "global_load_dwordx4 v[48:51], v0, s[62:63] offset:-4096 sc1\n"
  "v_mfma_f32_16x16x32_bf16 v[124:127], a[0:3], v[20:23], 0\n"
  "global_load_dwordx4 v[52:55], v0, s[62:63] offset:-3072 sc1\n"
  "v_mfma_f32_16x16x32_bf16 v[128:131], a[4:7], v[20:23], 0\n"
  "global_load_dwordx4 v[56:59], v0, s[62:63] offset:-2048 sc1\n"
  "v_mfma_f32_16x16x32_bf16 v[120:123], a[8:11], v[24:27], v[120:123]\n"
  "global_load_dwordx4 v[60:63], v0, s[62:63] offset:-1024 sc1\n"
  "v_mfma_f32_16x16x32_bf16 v[124:127], a[8:11], v[28:31], v[124:127]\n"
  "global_load_dwordx4 v[64:67], v0, s[62:63] offset:0 sc1\n"
  "v_mfma_f32_16x16x32_bf16 v[128:131], a[12:15], v[28:31], v[128:131]\n"
  "global_load_dwordx4 v[68:71], v0, s[62:63] offset:1024 sc1\n"
  "v_mfma_f32_16x16x32_bf16 v[120:123], a[16:19], v[32:35], v[120:123]\n"
  "global_load_dwordx4 v[72:75], v0, s[62:63] offset:2048 sc1\n"
  "v_mfma_f32_16x16x32_bf16 v[124:127], a[16:19], v[36:39], v[124:127]\n"
  "global_load_dwordx4 v[76:79], v0, s[62:63] offset:3072 sc1\n"
  "v_mfma_f32_16x16x32_bf16 v[128:131], a[20:23], v[36:39], v[128:131]\n"
  "v_mfma_f32_16x16x32_bf16 v[120:123], a[24:27], v[40:43], v[120:123]\n"
  "v_mfma_f32_16x16x32_bf16 v[124:127], a[24:27], v[44:47], v[124:127]\n"
  "v_mfma_f32_16x16x32_bf16 v[128:131], a[28:31], v[44:47], v[128:131]\n"
  "global_load_dwordx4 a[0:3], v0, s[60:61] offset:-4096 sc1\n"
  "global_load_dwordx4 a[4:7], v0, s[60:61] offset:-3072 sc1\n"
  "global_load_dwordx4 a[8:11], v0, s[60:61] offset:-2048 sc1\n"
  "global_load_dwordx4 a[12:15], v0, s[60:61] offset:-1024 sc1\n"
  "global_load_dwordx4 a[16:19], v0, s[60:61] offset:0 sc1\n"
  "global_load_dwordx4 a[20:23], v0, s[60:61] offset:1024 sc1\n"
  "global_load_dwordx4 a[24:27], v0, s[60:61] offset:2048 sc1\n"
  "global_load_dwordx4 a[28:31], v0, s[60:61] offset:3072 sc1\n"
  "s_add_u32 s60, s60, 8192\n"
  "s_addc_u32 s61, s61, 0\n"
  "global_load_dwordx4 a[64:67], v0, s[60:61] offset:-4096 sc1\n"
  "global_load_dwordx4 a[68:71], v0, s[60:61] offset:-3072 sc1\n"
  "global_load_dwordx4 a[72:75], v0, s[60:61] offset:-2048 sc1\n"
  "global_load_dwordx4 a[76:79], v0, s[60:61] offset:-1024 sc1\n"
  "global_load_dwordx4 a[80:83], v0, s[60:61] offset:0 sc1\n"
  "global_load_dwordx4 a[84:87], v0, s[60:61] offset:1024 sc1\n"
  "global_load_dwordx4 a[88:91], v0, s[60:61] offset:2048 sc1\n"
  "global_load_dwordx4 a[92:95], v0, s[60:61] offset:3072 sc1\n"
  "s_cmp_lt_u32 s56, 62\n"
  "s_cselect_b32 s74, 262144, 0\n"
  "s_add_u32 s62, s62, s74\n"
  "s_addc_u32 s63, s63, 0\n"
  "s_lshl_b32 s74, s74, 1\n"
  "s_sub_u32 s60, s60, 8192\n"
  "s_subb_u32 s61, s61, 0\n"
  "s_add_u32 s60, s60, s74\n"
  "s_addc_u32 s61, s61, 0\n"
  "s_waitcnt vmcnt(28)\n"
  "ds_write_b128 v4, v[80:83] offset:0\n"
  "s_waitcnt vmcnt(27)\n"
  "ds_write_b128 v4, v[84:87] offset:1024\n"
  "v_add_u32 v7, s56, v2\n"
  "v_add_u32 v7, 1, v7\n"
  "v_min_i32 v7, 64, v7\n"
  "s_waitcnt vmcnt(24)\n"
  "v_cmp_gt_i32 s[16:17], v7, v9\n"
  "v_mov_b32 v7, 0\n"
  "v_mov_b32 v8, s56\n"
  "s_mov_b64 exec, 1\n"
  "global_store_dword v7, v8, s[70:71] sc1\n"
  "s_mov_b64 exec, -1\n"
  "s_waitcnt lgkmcnt(0)\n"
  "ds_read_b64_tr_b16 v[88:89], v5 offset:0\n"
  "ds_read_b64_tr_b16 v[90:91], v5 offset:512\n"
  "ds_read_b64_tr_b16 v[92:93], v5 offset:1024\n"
  "ds_read_b64_tr_b16 v[94:95], v5 offset:1536\n"
  "v_cndmask_b32 v120, v12, v120, s[8:9]\n"
  "v_cndmask_b32 v128, v12, v128, s[8:9]\n"
  "v_cndmask_b32 v121, v12, v121, s[10:11]\n"
  "v_cndmask_b32 v129, v12, v129, s[10:11]\n"
  "v_cndmask_b32 v122, v12, v122, s[12:13]\n"
  "v_cndmask_b32 v130, v12, v130, s[12:13]\n"
  "v_cndmask_b32 v123, v12, v123, s[14:15]\n"
  "v_cndmask_b32 v131, v12, v131, s[14:15]\n"
  "s_waitcnt lgkmcnt(3)\n"
  "v_mfma_f32_16x16x16_bf16 v[132:135], v[88:89], v[16:17], 0\n"
  "v_mfma_f32_16x16x16_bf16 v[140:143], v[88:89], v[18:19], 0\n"
  "s_waitcnt lgkmcnt(2)\n"
  "v_mfma_f32_16x16x16_bf16 v[132:135], v[90:91], v[24:25], v[132:135]\n"
  "v_mfma_f32_16x16x16_bf16 v[140:143], v[90:91], v[26:27], v[140:143]\n"
  "s_waitcnt lgkmcnt(1)\n"
  "v_mfma_f32_16x16x16_bf16 v[132:135], v[92:93], v[32:33], v[132:135]\n"
  "v_mfma_f32_16x16x16_bf16 v[140:143], v[92:93], v[34:35], v[140:143]\n"
  "s_waitcnt lgkmcnt(0)\n"
  "v_mfma_f32_16x16x16_bf16 v[132:135], v[94:95], v[40:41], v[132:135]\n"
  "v_mfma_f32_16x16x16_bf16 v[140:143], v[94:95], v[42:43], v[140:143]\n"
  "v_mfma_f32_16x16x16_bf16 v[136:139], v[88:89], v[20:21], 0\n"
  "v_mfma_f32_16x16x16_bf16 v[144:147], v[88:89], v[22:23], 0\n"
  "v_mfma_f32_16x16x16_bf16 v[136:139], v[90:91], v[28:29], v[136:139]\n"
  "v_mfma_f32_16x16x16_bf16 v[144:147], v[90:91], v[30:31], v[144:147]\n"
  "v_mfma_f32_16x16x16_bf16 v[136:139], v[92:93], v[36:37], v[136:139]\n"
  "v_mfma_f32_16x16x16_bf16 v[144:147], v[92:93], v[38:39], v[144:147]\n"
  "v_mfma_f32_16x16x16_bf16 v[136:139], v[94:95], v[44:45], v[136:139]\n"
  "v_mfma_f32_16x16x16_bf16 v[144:147], v[94:95], v[46:47], v[144:147]\n"
  "v_add_f32 v132, v132, v142\n"
  "v_add_f32 v133, v133, v143\n"
  "v_mul_f32 v132, s50, v132\n"
  "v_mul_f32 v133, s50, v133\n"
  "v_max_f32 v10, v132, v133\n"
  "v_max3_f32 v10, v10, v120, v121\n"
  "v_max3_f32 v10, v10, v122, v123\n"
  "v_mov_b32 v11, v10\n"
  "s_nop 1\n"
  "v_permlane16_swap_b32 v10, v11\n"
  "v_max_f32 v10, v10, v11\n"
  "v_mov_b32 v11, v10\n"
  "s_nop 1\n"
  "v_permlane32_swap_b32 v10, v11\n"
  "v_max_f32 v10, v10, v11\n"
  "v_sub_f32 v120, v120, v10\n"
  "v_sub_f32 v121, v121, v10\n"
  "v_sub_f32 v122, v122, v10\n"
  "v_sub_f32 v123, v123, v10\n"
  "v_sub_f32 v132, v132, v10\n"
  "v_sub_f32 v133, v133, v10\n"
  "v_exp_f32 v120, v120\n"
  "v_exp_f32 v121, v121\n"
  "v_exp_f32 v122, v122\n"
  "v_exp_f32 v123, v123\n"
  "v_exp_f32 v132, v132\n"
  "v_exp_f32 v133, v133\n"
  "v_add_f32 v10, v120, v121\n"
  "v_add_f32 v10, v10, v122\n"
  "v_add_f32 v10, v10, v123\n"
  "v_add_f32 v10, v10, v132\n"
  "v_add_f32 v10, v10, v133\n"
  "v_mov_b32 v11, v10\n"
  "s_nop 1\n"
  "v_permlane16_swap_b32 v10, v11\n"
  "v_add_f32 v10, v10, v11\n"
  "v_mov_b32 v11, v10\n"
  "s_nop 1\n"
  "v_permlane32_swap_b32 v10, v11\n"
  "v_add_f32 v10, v10, v11\n"
  "v_rcp_f32 v10, v10\n"
  "s_nop 0\n"
  "v_mul_f32 v120, v10, v120\n"
  "v_mul_f32 v121, v10, v121\n"
  "v_mul_f32 v122, v10, v122\n"
  "v_mul_f32 v123, v10, v123\n"
  "v_mul_f32 v132, v10, v132\n"
  "v_mul_f32 v133, v10, v133\n"
  "v_cvt_pk_bf16_f32 v148, v120, v121\n"
  "v_cvt_pk_bf16_f32 v149, v122, v123\n"
  "v_cvt_pk_bf16_f32 v156, v132, v133\n"
  "v_cvt_pk_bf16_f32 v161, v132, v133\n"
  "v_cvt_pk_bf16_f32 v166, v132, v133\n"
  "v_cvt_pk_bf16_f32 v171, v132, v133\n"
  "v_mfma_f32_16x16x32_bf16 v[188:191], a[32:35], v[148:151], 0\n"
  "v_add_f32 v136, v136, v146\n"
  "v_add_f32 v137, v137, v147\n"
  "v_mfma_f32_16x16x32_bf16 v[188:191], v[96:99], v[156:159], v[188:191]\n"
  "v_mul_f32 v136, s50, v136\n"
  "v_mul_f32 v137, s50, v137\n"
  "v_mfma_f32_16x16x32_bf16 v[192:195], a[36:39], v[148:151], 0\n"
  "v_max_f32 v10, v136, v137\n"
  "v_max3_f32 v10, v10, v124, v125\n"
  "v_mfma_f32_16x16x32_bf16 v[192:195], v[96:99], v[160:163], v[192:195]\n"
  "v_max3_f32 v10, v10, v126, v127\n"
  "v_max3_f32 v10, v10, v128, v129\n"
  "v_mfma_f32_16x16x32_bf16 v[196:199], a[40:43], v[148:151], 0\n"
  "v_max3_f32 v10, v10, v130, v131\n"
  "v_mov_b32 v11, v10\n"
  "s_nop 1\n"
  "v_permlane16_swap_b32 v10, v11\n"
  "v_max_f32 v10, v10, v11\n"
  "v_mov_b32 v11, v10\n"
  "s_nop 1\n"
  "v_permlane32_swap_b32 v10, v11\n"
  "v_max_f32 v10, v10, v11\n"
  "v_mfma_f32_16x16x32_bf16 v[196:199], v[96:99], v[164:167], v[196:199]\n"
  "v_sub_f32 v124, v124, v10\n"
  "v_sub_f32 v125, v125, v10\n"
  "v_mfma_f32_16x16x32_bf16 v[200:203], a[44:47], v[148:151], 0\n"
  "v_sub_f32 v126, v126, v10\n"
  "v_sub_f32 v127, v127, v10\n"
  "v_mfma_f32_16x16x32_bf16 v[200:203], v[96:99], v[168:171], v[200:203]\n"
  "v_sub_f32 v128, v128, v10\n"
  "v_sub_f32 v129, v129, v10\n"
  "v_mfma_f32_16x16x32_bf16 v[204:207], a[48:51], v[148:151], 0\n"
  "v_sub_f32 v130, v130, v10\n"
  "v_sub_f32 v131, v131, v10\n"
  "v_mfma_f32_16x16x32_bf16 v[204:207], v[100:103], v[156:159], v[204:207]\n"
  "v_sub_f32 v136, v136, v10\n"
  "v_sub_f32 v137, v137, v10\n"
  "v_mfma_f32_16x16x32_bf16 v[208:211], a[52:55], v[148:151], 0\n"
  "v_exp_f32 v124, v124\n"
  "v_mfma_f32_16x16x32_bf16 v[208:211], v[100:103], v[160:163], v[208:211]\n"
  "v_exp_f32 v125, v125\n"
  "v_mfma_f32_16x16x32_bf16 v[212:215], a[56:59], v[148:151], 0\n"
  "v_exp_f32 v126, v126\n"
  "v_mfma_f32_16x16x32_bf16 v[212:215], v[100:103], v[164:167], v[212:215]\n"
  "v_exp_f32 v127, v127\n"
  "v_mfma_f32_16x16x32_bf16 v[216:219], a[60:63], v[148:151], 0\n"
  "v_exp_f32 v128, v128\n"
  "v_mfma_f32_16x16x32_bf16 v[216:219], v[100:103], v[168:171], v[216:219]\n"
  "v_exp_f32 v129, v129\n"
  "v_exp_f32 v130, v130\n"
  "v_exp_f32 v131, v131\n"
  "v_exp_f32 v136, v136\n"
  "v_exp_f32 v137, v137\n"
  "v_add_f32 v10, v124, v125\n"
  "v_add_f32 v10, v10, v126\n"
  "v_add_f32 v10, v10, v127\n"
  "v_add_f32 v10, v10, v128\n"
  "v_add_f32 v10, v10, v129\n"
  "v_add_f32 v10, v10, v130\n"
  "v_add_f32 v10, v10, v131\n"
  "v_add_f32 v10, v10, v136\n"
  "v_add_f32 v10, v10, v137\n"
  "v_mov_b32 v11, v10\n"
  "s_nop 1\n"
  "v_permlane16_swap_b32 v10, v11\n"
  "v_add_f32 v10, v10, v11\n"
  "v_mov_b32 v11, v10\n"
  "s_nop 1\n"
  "v_permlane32_swap_b32 v10, v11\n"
  "v_add_f32 v10, v10, v11\n"
  "v_rcp_f32 v10, v10\n"
  "s_nop 0\n"
  "v_mul_f32 v124, v10, v124\n"
  "v_mul_f32 v125, v10, v125\n"
  "v_mul_f32 v126, v10, v126\n"
  "v_mul_f32 v127, v10, v127\n"
  "v_mul_f32 v128, v10, v128\n"
  "v_mul_f32 v129, v10, v129\n"
  "v_mul_f32 v130, v10, v130\n"
  "v_mul_f32 v131, v10, v131\n"
  "v_mul_f32 v136, v10, v136\n"
  "v_mul_f32 v137, v10, v137\n"
  "v_cvt_pk_bf16_f32 v152, v124, v125\n"
  "v_cvt_pk_bf16_f32 v153, v126, v127\n"
  "v_cvt_pk_bf16_f32 v154, v128, v129\n"
  "v_cvt_pk_bf16_f32 v155, v130, v131\n"
  "v_cvt_pk_bf16_f32 v172, v136, v137\n"
  "v_cvt_pk_bf16_f32 v177, v136, v137\n"
  "v_cvt_pk_bf16_f32 v182, v136, v137\n"
  "v_cvt_pk_bf16_f32 v187, v136, v137\n"
  "v_cvt_pk_bf16_f32 v220, v188, v189\n"
  "v_cvt_pk_bf16_f32 v221, v190, v191\n"
  "v_cvt_pk_bf16_f32 v222, v192, v193\n"
  "v_cvt_pk_bf16_f32 v223, v194, v195\n"
  "global_store_dwordx4 v0, v[220:223], s[68:69] offset:-4096 sc1\n"
  "v_mfma_f32_16x16x32_bf16 v[188:191], a[32:35], v[152:155], 0\n"
  "v_cvt_pk_bf16_f32 v224, v196, v197\n"
  "v_cvt_pk_bf16_f32 v225, v198, v199\n"
  "v_mfma_f32_16x16x32_bf16 v[188:191], v[96:99], v[172:175], v[188:191]\n"
  "v_cvt_pk_bf16_f32 v226, v200, v201\n"
  "v_cvt_pk_bf16_f32 v227, v202, v203\n"
  "v_mfma_f32_16x16x32_bf16 v[192:195], a[36:39], v[152:155], 0\n"
  "global_store_dwordx4 v0, v[224:227], s[68:69] offset:-3072 sc1\n"
  "v_mfma_f32_16x16x32_bf16 v[192:195], v[96:99], v[176:179], v[192:195]\n"
  "v_mfma_f32_16x16x32_bf16 v[196:199], a[40:43], v[152:155], 0\n"
  "v_cvt_pk_bf16_f32 v228, v204, v205\n"
  "v_cvt_pk_bf16_f32 v229, v206, v207\n"
  "v_mfma_f32_16x16x32_bf16 v[196:199], v[96:99], v[180:183], v[196:199]\n"
  "v_cvt_pk_bf16_f32 v230, v208, v209\n"
  "v_cvt_pk_bf16_f32 v231, v210, v211\n"
  "v_mfma_f32_16x16x32_bf16 v[200:203], a[44:47], v[152:155], 0\n"
  "global_store_dwordx4 v0, v[228:231], s[68:69] offset:-2048 sc1\n"
  "v_mfma_f32_16x16x32_bf16 v[200:203], v[96:99], v[184:187], v[200:203]\n"
  "v_mfma_f32_16x16x32_bf16 v[204:207], a[48:51], v[152:155], 0\n"
  "v_cvt_pk_bf16_f32 v232, v212, v213\n"
  "v_cvt_pk_bf16_f32 v233, v214, v215\n"
  "v_mfma_f32_16x16x32_bf16 v[204:207], v[100:103], v[172:175], v[204:207]\n"
  "v_cvt_pk_bf16_f32 v234, v216, v217\n"
  "v_cvt_pk_bf16_f32 v235, v218, v219\n"
  "v_mfma_f32_16x16x32_bf16 v[208:211], a[52:55], v[152:155], 0\n"
  "global_store_dwordx4 v0, v[232:235], s[68:69] offset:-1024 sc1\n"
  "v_mfma_f32_16x16x32_bf16 v[208:211], v[100:103], v[176:179], v[208:211]\n"
  "v_mfma_f32_16x16x32_bf16 v[212:215], a[56:59], v[152:155], 0\n"
  "v_cvt_pk_bf16_f32 v220, v188, v189\n"
  "v_cvt_pk_bf16_f32 v221, v190, v191\n"
  "v_mfma_f32_16x16x32_bf16 v[212:215], v[100:103], v[180:183], v[212:215]\n"
  "v_cvt_pk_bf16_f32 v222, v192, v193\n"
  "v_cvt_pk_bf16_f32 v223, v194, v195\n"
  "v_mfma_f32_16x16x32_bf16 v[216:219], a[60:63], v[152:155], 0\n"
  "global_store_dwordx4 v0, v[220:223], s[68:69] offset:0 sc1\n"
  "v_mfma_f32_16x16x32_bf16 v[216:219], v[100:103], v[184:187], v[216:219]\n"
  "v_cvt_pk_bf16_f32 v224, v196, v197\n"
  "v_cvt_pk_bf16_f32 v225, v198, v199\n"
  "v_cvt_pk_bf16_f32 v226, v200, v201\n"
  "v_cvt_pk_bf16_f32 v227, v202, v203\n"
  "global_store_dwordx4 v0, v[224:227], s[68:69] offset:1024 sc1\n"
  "v_cvt_pk_bf16_f32 v228, v204, v205\n"
  "v_cvt_pk_bf16_f32 v229, v206, v207\n"
  "v_cvt_pk_bf16_f32 v230, v208, v209\n"
  "v_cvt_pk_bf16_f32 v231, v210, v211\n"
  "global_store_dwordx4 v0, v[228:231], s[68:69] offset:2048 sc1\n"
  "v_cvt_pk_bf16_f32 v232, v212, v213\n"
  "v_cvt_pk_bf16_f32 v233, v214, v215\n"
  "v_cvt_pk_bf16_f32 v234, v216, v217\n"
  "v_cvt_pk_bf16_f32 v235, v218, v219\n"
  "global_store_dwordx4 v0, v[232:235], s[68:69] offset:3072 sc1\n"
  "s_add_u32 s68, s68, 262144\n"
  "s_addc_u32 s69, s69, 0\n"
  "s_add_u32 s56, s56, 1\n"
  "s_cmp_eq_u32 s56, 64\n"
  "s_cbranch_scc1 TEND\n"
  "s_cmp_eq_u64 s[16:17], 0\n"
  "s_cbranch_scc1 TGO1\n"
  "s_waitcnt vmcnt(0)\n"
  "v_mov_b32 v7, 0\n"
  "v_mov_b32 v8, s56\n"
  "s_mov_b64 exec, 1\n"
  "global_store_dword v7, v8, s[70:71] sc1\n"
  "s_mov_b64 exec, -1\n"
  "TP1:\n"
  "global_load_dword v9, v1, s[36:37] sc1\n"
  "v_add_u32 v7, s56, v2\n"
  "v_add_u32 v7, 0, v7\n"
  "v_min_i32 v7, 64, v7\n"
  "s_waitcnt vmcnt(0)\n"
  "v_cmp_gt_i32 vcc, v7, v9\n"
  "s_cbranch_vccz TG1\n"
  "s_sub_u32 s57, s57, 1\n"
  "s_cbranch_scc1 TQUIT\n"
  "s_sleep 2\n"
  "s_branch TP1\n"
  "TG1:\n"
  "TGO1:\n"
  "s_waitcnt vmcnt(8)\n"
  "global_load_dwordx4 v[80:83], v0, s[64:65] offset:0 sc1\n"
  "global_load_dwordx4 v[84:87], v0, s[64:65] offset:1024 sc1\n"
  "global_load_dwordx4 v[96:99], v3, s[66:67] offset:0 sc1\n"
  "global_load_dwordx4 v[100:103], v3, s[66:67] offset:1024 sc1\n"
  "global_load_dword v9, v1, s[36:37] sc1\n"
  "s_add_u32 s64, s64, 65536\n"
  "s_addc_u32 s65, s65, 0\n"
  "s_add_u32 s66, s66, 65536\n"
  "s_addc_u32 s67, s67, 0\n"
  "v_mfma_f32_16x16x32_bf16 v[120:123], a[0:3], v[48:51], 0\n"
  "global_load_dwordx4 v[16:19], v0, s[62:63] offset:-4096 sc1\n"
  "v_mfma_f32_16x16x32_bf16 v[124:127], a[0:3], v[52:55], 0\n"
  "global_load_dwordx4 v[20:23], v0, s[62:63] offset:-3072 sc1\n"
  "v_mfma_f32_16x16x32_bf16 v[128:131], a[4:7], v[52:55], 0\n"
  "global_load_dwordx4 v[24:27], v0, s[62:63] offset:-2048 sc1\n"
  "v_mfma_f32_16x16x32_bf16 v[120:123], a[8:11], v[56:59], v[120:123]\n"
  "global_load_dwordx4 v[28:31], v0, s[62:63] offset:-1024 sc1\n"
  "v_mfma_f32_16x16x32_bf16 v[124:127], a[8:11], v[60:63], v[124:127]\n"
  "global_load_dwordx4 v[32:35], v0, s[62:63] offset:0 sc1\n"
  "v_mfma_f32_16x16x32_bf16 v[128:131], a[12:15], v[60:63], v[128:131]\n"
  "global_load_dwordx4 v[36:39], v0, s[62:63] offset:1024 sc1\n"
  "v_mfma_f32_16x16x32_bf16 v[120:123], a[16:19], v[64:67], v[120:123]\n"
  "global_load_dwordx4 v[40:43], v0, s[62:63] offset:2048 sc1\n"
  "v_mfma_f32_16x16x32_bf16 v[124:127], a[16:19], v[68:71], v[124:127]\n"
  "global_load_dwordx4 v[44:47], v0, s[62:63] offset:3072 sc1\n"
  "v_mfma_f32_16x16x32_bf16 v[128:131], a[20:23], v[68:71], v[128:131]\n"
  "v_mfma_f32_16x16x32_bf16 v[120:123], a[24:27], v[72:75], v[120:123]\n"
  "v_mfma_f32_16x16x32_bf16 v[124:127], a[24:27], v[76:79], v[124:127]\n"
  "v_mfma_f32_16x16x32_bf16 v[128:131], a[28:31], v[76:79], v[128:131]\n"
  "global_load_dwordx4 a[0:3], v0, s[60:61] offset:-4096 sc1\n"
  "global_load_dwordx4 a[4:7], v0, s[60:61] offset:-3072 sc1\n"
  "global_load_dwordx4 a[8:11], v0, s[60:61] offset:-2048 sc1\n"
  "global_load_dwordx4 a[12:15], v0, s[60:61] offset:-1024 sc1\n"
  "global_load_dwordx4 a[16:19], v0, s[60:61] offset:0 sc1\n"
  "global_load_dwordx4 a[20:23], v0, s[60:61] offset:1024 sc1\n"
  "global_load_dwordx4 a[24:27], v0, s[60:61] offset:2048 sc1\n"
  "global_load_dwordx4 a[28:31], v0, s[60:61] offset:3072 sc1\n"
  "s_add_u32 s60, s60, 8192\n"
  "s_addc_u32 s61, s61, 0\n"
  "global_load_dwordx4 a[32:35], v0, s[60:61] offset:-4096 sc1\n"
  "global_load_dwordx4 a[36:39], v0, s[60:61] offset:-3072 sc1\n"
  "global_load_dwordx4 a[40:43], v0, s[60:61] offset:-2048 sc1\n"
  "global_load_dwordx4 a[44:47], v0, s[60:61] offset:-1024 sc1\n"
  "global_load_dwordx4 a[48:51], v0, s[60:61] offset:0 sc1\n"
  "global_load_dwordx4 a[52:55], v0, s[60:61] offset:1024 sc1\n"
  "global_load_dwordx4 a[56:59], v0, s[60:61] offset:2048 sc1\n"
  "global_load_dwordx4 a[60:63], v0, s[60:61] offset:3072 sc1\n"
  "s_cmp_lt_u32 s56, 62\n"
  "s_cselect_b32 s74, 262144, 0\n"
  "s_add_u32 s62, s62, s74\n"
  "s_addc_u32 s63, s63, 0\n"
  "s_lshl_b32 s74, s74, 1\n"
  "s_sub_u32 s60, s60, 8192\n"
  "s_subb_u32 s61, s61, 0\n"
  "s_add_u32 s60, s60, s74\n"
  "s_addc_u32 s61, s61, 0\n"
  "s_waitcnt vmcnt(28)\n"
  "ds_write_b128 v4, v[80:83] offset:0\n"
  "s_waitcnt vmcnt(27)\n"
  "ds_write_b128 v4, v[84:87] offset:1024\n"
  "v_add_u32 v7, s56, v2\n"
  "v_add_u32 v7, 1, v7\n"
  "v_min_i32 v7, 64, v7\n"
  "s_waitcnt vmcnt(24)\n"
  "v_cmp_gt_i32 s[16:17], v7, v9\n"
  "v_mov_b32 v7, 0\n"
  "v_mov_b32 v8, s56\n"
  "s_mov_b64 exec, 1\n"
  "global_store_dword v7, v8, s[70:71] sc1\n"
  "s_mov_b64 exec, -1\n"
  "s_waitcnt lgkmcnt(0)\n"
  "ds_read_b64_tr_b16 v[88:89], v5 offset:0\n"
  "ds_read_b64_tr_b16 v[90:91], v5 offset:512\n"
  "ds_read_b64_tr_b16 v[92:93], v5 offset:1024\n"
  "ds_read_b64_tr_b16 v[94:95], v5 offset:1536\n"
  "v_cndmask_b32 v120, v12, v120, s[8:9]\n"
  "v_cndmask_b32 v128, v12, v128, s[8:9]\n"
  "v_cndmask_b32 v121, v12, v121, s[10:11]\n"
  "v_cndmask_b32 v129, v12, v129, s[10:11]\n"
  "v_cndmask_b32 v122, v12, v122, s[12:13]\n"
  "v_cndmask_b32 v130, v12, v130, s[12:13]\n"
  "v_cndmask_b32 v123, v12, v123, s[14:15]\n"
  "v_cndmask_b32 v131, v12, v131, s[14:15]\n"
  "s_waitcnt lgkmcnt(3)\n"
  "v_mfma_f32_16x16x16_bf16 v[132:135], v[88:89], v[48:49], 0\n"
  "v_mfma_f32_16x16x16_bf16 v[140:143], v[88:89], v[50:51], 0\n"
  "s_waitcnt lgkmcnt(2)\n"
  "v_mfma_f32_16x16x16_bf16 v[132:135], v[90:91], v[56:57], v[132:135]\n"
  "v_mfma_f32_16x16x16_bf16 v[140:143], v[90:91], v[58:59], v[140:143]\n"
  "s_waitcnt lgkmcnt(1)\n"
  "v_mfma_f32_16x16x16_bf16 v[132:135], v[92:93], v[64:65], v[132:135]\n"
  "v_mfma_f32_16x16x16_bf16 v[140:143], v[92:93], v[66:67], v[140:143]\n"
  "s_waitcnt lgkmcnt(0)\n"
  "v_mfma_f32_16x16x16_bf16 v[132:135], v[94:95], v[72:73], v[132:135]\n"
  "v_mfma_f32_16x16x16_bf16 v[140:143], v[94:95], v[74:75], v[140:143]\n"
  "v_mfma_f32_16x16x16_bf16 v[136:139], v[88:89], v[52:53], 0\n"
  "v_mfma_f32_16x16x16_bf16 v[144:147], v[88:89], v[54:55], 0\n"
  "v_mfma_f32_16x16x16_bf16 v[136:139], v[90:91], v[60:61], v[136:139]\n"
  "v_mfma_f32_16x16x16_bf16 v[144:147], v[90:91], v[62:63], v[144:147]\n"
  "v_mfma_f32_16x16x16_bf16 v[136:139], v[92:93], v[68:69], v[136:139]\n"
  "v_mfma_f32_16x16x16_bf16 v[144:147], v[92:93], v[70:71], v[144:147]\n"
  "v_mfma_f32_16x16x16_bf16 v[136:139], v[94:95], v[76:77], v[136:139]\n"
  "v_mfma_f32_16x16x16_bf16 v[144:147], v[94:95], v[78:79], v[144:147]\n"
  "v_add_f32 v132, v132, v142\n"
  "v_add_f32 v133, v133, v143\n"
  "v_mul_f32 v132, s50, v132\n"
  "v_mul_f32 v133, s50, v133\n"
  "v_max_f32 v10, v132, v133\n"
  "v_max3_f32 v10, v10, v120, v121\n"
  "v_max3_f32 v10, v10, v122, v123\n"
  "v_mov_b32 v11, v10\n"
  "s_nop 1\n"
  "v_permlane16_swap_b32 v10, v11\n"
  "v_max_f32 v10, v10, v11\n"
  "v_mov_b32 v11, v10\n"
  "s_nop 1\n"
  "v_permlane32_swap_b32 v10, v11\n"
  "v_max_f32 v10, v10, v11\n"
  "v_sub_f32 v120, v120, v10\n"
  "v_sub_f32 v121, v121, v10\n"
  "v_sub_f32 v122, v122, v10\n"
  "v_sub_f32 v123, v123, v10\n"
  "v_sub_f32 v132, v132, v10\n"
  "v_sub_f32 v133, v133, v10\n"
  "v_exp_f32 v120, v120\n"
  "v_exp_f32 v121, v121\n"
  "v_exp_f32 v122, v122\n"
  "v_exp_f32 v123, v123\n"
  "v_exp_f32 v132, v132\n"
  "v_exp_f32 v133, v133\n"
  "v_add_f32 v10, v120, v121\n"
  "v_add_f32 v10, v10, v122\n"
  "v_add_f32 v10, v10, v123\n"
  "v_add_f32 v10, v10, v132\n"
  "v_add_f32 v10, v10, v133\n"
  "v_mov_b32 v11, v10\n"
  "s_nop 1\n"
  "v_permlane16_swap_b32 v10, v11\n"
  "v_add_f32 v10, v10, v11\n"
  "v_mov_b32 v11, v10\n"
  "s_nop 1\n"
  "v_permlane32_swap_b32 v10, v11\n"
  "v_add_f32 v10, v10, v11\n"
  "v_rcp_f32 v10, v10\n"
  "s_nop 0\n"
  "v_mul_f32 v120, v10, v120\n"
  "v_mul_f32 v121, v10, v121\n"
  "v_mul_f32 v122, v10, v122\n"
  "v_mul_f32 v123, v10, v123\n"
  "v_mul_f32 v132, v10, v132\n"
  "v_mul_f32 v133, v10, v133\n"
  "v_cvt_pk_bf16_f32 v148, v120, v121\n"
  "v_cvt_pk_bf16_f32 v149, v122, v123\n"
  "v_cvt_pk_bf16_f32 v156, v132, v133\n"
  "v_cvt_pk_bf16_f32 v161, v132, v133\n"
  "v_cvt_pk_bf16_f32 v166, v132, v133\n"
  "v_cvt_pk_bf16_f32 v171, v132, v133\n"
  "v_mfma_f32_16x16x32_bf16 v[188:191], a[64:67], v[148:151], 0\n"
  "v_add_f32 v136, v136, v146\n"
  "v_add_f32 v137, v137, v147\n"
  "v_mfma_f32_16x16x32_bf16 v[188:191], v[96:99], v[156:159], v[188:191]\n"
  "v_mul_f32 v136, s50, v136\n"
  "v_mul_f32 v137, s50, v137\n"
  "v_mfma_f32_16x16x32_bf16 v[192:195], a[68:71], v[148:151], 0\n"
  "v_max_f32 v10, v136, v137\n"
  "v_max3_f32 v10, v10, v124, v125\n"
  "v_mfma_f32_16x16x32_bf16 v[192:195], v[96:99], v[160:163], v[192:195]\n"
  "v_max3_f32 v10, v10, v126, v127\n"
  "v_max3_f32 v10, v10, v128, v129\n"
  "v_mfma_f32_16x16x32_bf16 v[196:199], a[72:75], v[148:151], 0\n"
  "v_max3_f32 v10, v10, v130, v131\n"
  "v_mov_b32 v11, v10\n"
  "s_nop 1\n"
  "v_permlane16_swap_b32 v10, v11\n"
  "v_max_f32 v10, v10, v11\n"
  "v_mov_b32 v11, v10\n"
  "s_nop 1\n"
  "v_permlane32_swap_b32 v10, v11\n"
  "v_max_f32 v10, v10, v11\n"
  "v_mfma_f32_16x16x32_bf16 v[196:199], v[96:99], v[164:167], v[196:199]\n"
  "v_sub_f32 v124, v124, v10\n"
  "v_sub_f32 v125, v125, v10\n"
  "v_mfma_f32_16x16x32_bf16 v[200:203], a[76:79], v[148:151], 0\n"
  "v_sub_f32 v126, v126, v10\n"
  "v_sub_f32 v127, v127, v10\n"
  "v_mfma_f32_16x16x32_bf16 v[200:203], v[96:99], v[168:171], v[200:203]\n"
  "v_sub_f32 v128, v128, v10\n"
  "v_sub_f32 v129, v129, v10\n"
  "v_mfma_f32_16x16x32_bf16 v[204:207], a[80:83], v[148:151], 0\n"
  "v_sub_f32 v130, v130, v10\n"
  "v_sub_f32 v131, v131, v10\n"
  "v_mfma_f32_16x16x32_bf16 v[204:207], v[100:103], v[156:159], v[204:207]\n"
  "v_sub_f32 v136, v136, v10\n"
  "v_sub_f32 v137, v137, v10\n"
  "v_mfma_f32_16x16x32_bf16 v[208:211], a[84:87], v[148:151], 0\n"
  "v_exp_f32 v124, v124\n"
  "v_mfma_f32_16x16x32_bf16 v[208:211], v[100:103], v[160:163], v[208:211]\n"
  "v_exp_f32 v125, v125\n"
  "v_mfma_f32_16x16x32_bf16 v[212:215], a[88:91], v[148:151], 0\n"
  "v_exp_f32 v126, v126\n"
  "v_mfma_f32_16x16x32_bf16 v[212:215], v[100:103], v[164:167], v[212:215]\n"
  "v_exp_f32 v127, v127\n"
  "v_mfma_f32_16x16x32_bf16 v[216:219], a[92:95], v[148:151], 0\n"
  "v_exp_f32 v128, v128\n"
  "v_mfma_f32_16x16x32_bf16 v[216:219], v[100:103], v[168:171], v[216:219]\n"
  "v_exp_f32 v129, v129\n"
  "v_exp_f32 v130, v130\n"
  "v_exp_f32 v131, v131\n"
  "v_exp_f32 v136, v136\n"
  "v_exp_f32 v137, v137\n"
  "v_add_f32 v10, v124, v125\n"
  "v_add_f32 v10, v10, v126\n"
  "v_add_f32 v10, v10, v127\n"
  "v_add_f32 v10, v10, v128\n"
  "v_add_f32 v10, v10, v129\n"
  "v_add_f32 v10, v10, v130\n"
  "v_add_f32 v10, v10, v131\n"
  "v_add_f32 v10, v10, v136\n"
  "v_add_f32 v10, v10, v137\n"
  "v_mov_b32 v11, v10\n"
  "s_nop 1\n"
  "v_permlane16_swap_b32 v10, v11\n"
  "v_add_f32 v10, v10, v11\n"
  "v_mov_b32 v11, v10\n"
  "s_nop 1\n"
  "v_permlane32_swap_b32 v10, v11\n"
  "v_add_f32 v10, v10, v11\n"
  "v_rcp_f32 v10, v10\n"
  "s_nop 0\n"
  "v_mul_f32 v124, v10, v124\n"
  "v_mul_f32 v125, v10, v125\n"
  "v_mul_f32 v126, v10, v126\n"
  "v_mul_f32 v127, v10, v127\n"
  "v_mul_f32 v128, v10, v128\n"
  "v_mul_f32 v129, v10, v129\n"
  "v_mul_f32 v130, v10, v130\n"
  "v_mul_f32 v131, v10, v131\n"
  "v_mul_f32 v136, v10, v136\n"
  "v_mul_f32 v137, v10, v137\n"
  "v_cvt_pk_bf16_f32 v152, v124, v125\n"
  "v_cvt_pk_bf16_f32 v153, v126, v127\n"
  "v_cvt_pk_bf16_f32 v154, v128, v129\n"
  "v_cvt_pk_bf16_f32 v155, v130, v131\n"
  "v_cvt_pk_bf16_f32 v172, v136, v137\n"
  "v_cvt_pk_bf16_f32 v177, v136, v137\n"
  "v_cvt_pk_bf16_f32 v182, v136, v137\n"
  "v_cvt_pk_bf16_f32 v187, v136, v137\n"
  "v_cvt_pk_bf16_f32 v220, v188, v189\n"
  "v_cvt_pk_bf16_f32 v221, v190, v191\n"
  "v_cvt_pk_bf16_f32 v222, v192, v193\n"
  "v_cvt_pk_bf16_f32 v223, v194, v195\n"
  "global_store_dwordx4 v0, v[220:223], s[68:69] offset:-4096 sc1\n"
  "v_mfma_f32_16x16x32_bf16 v[188:191], a[64:67], v[152:155], 0\n"
  "v_cvt_pk_bf16_f32 v224, v196, v197\n"
  "v_cvt_pk_bf16_f32 v225, v198, v199\n"
  "v_mfma_f32_16x16x32_bf16 v[188:191], v[96:99], v[172:175], v[188:191]\n"
  "v_cvt_pk_bf16_f32 v226, v200, v201\n"
  "v_cvt_pk_bf16_f32 v227, v202, v203\n"
  "v_mfma_f32_16x16x32_bf16 v[192:195], a[68:71], v[152:155], 0\n"
  "global_store_dwordx4 v0, v[224:227], s[68:69] offset:-3072 sc1\n"
  "v_mfma_f32_16x16x32_bf16 v[192:195], v[96:99], v[176:179], v[192:195]\n"
  "v_mfma_f32_16x16x32_bf16 v[196:199], a[72:75], v[152:155], 0\n"
  "v_cvt_pk_bf16_f32 v228, v204, v205\n"
  "v_cvt_pk_bf16_f32 v229, v206, v207\n"
  "v_mfma_f32_16x16x32_bf16 v[196:199], v[96:99], v[180:183], v[196:199]\n"
  "v_cvt_pk_bf16_f32 v230, v208, v209\n"
  "v_cvt_pk_bf16_f32 v231, v210, v211\n"
  "v_mfma_f32_16x16x32_bf16 v[200:203], a[76:79], v[152:155], 0\n"
  "global_store_dwordx4 v0, v[228:231], s[68:69] offset:-2048 sc1\n"
  "v_mfma_f32_16x16x32_bf16 v[200:203], v[96:99], v[184:187], v[200:203]\n"
  "v_mfma_f32_16x16x32_bf16 v[204:207], a[80:83], v[152:155], 0\n"
  "v_cvt_pk_bf16_f32 v232, v212, v213\n"
  "v_cvt_pk_bf16_f32 v233, v214, v215\n"
  "v_mfma_f32_16x16x32_bf16 v[204:207], v[100:103], v[172:175], v[204:207]\n"
  "v_cvt_pk_bf16_f32 v234, v216, v217\n"
  "v_cvt_pk_bf16_f32 v235, v218, v219\n"
  "v_mfma_f32_16x16x32_bf16 v[208:211], a[84:87], v[152:155], 0\n"
  "global_store_dwordx4 v0, v[232:235], s[68:69] offset:-1024 sc1\n"
  "v_mfma_f32_16x16x32_bf16 v[208:211], v[100:103], v[176:179], v[208:211]\n"
  "v_mfma_f32_16x16x32_bf16 v[212:215], a[88:91], v[152:155], 0\n"
  "v_cvt_pk_bf16_f32 v220, v188, v189\n"
  "v_cvt_pk_bf16_f32 v221, v190, v191\n"
  "v_mfma_f32_16x16x32_bf16 v[212:215], v[100:103], v[180:183], v[212:215]\n"
  "v_cvt_pk_bf16_f32 v222, v192, v193\n"
  "v_cvt_pk_bf16_f32 v223, v194, v195\n"
  "v_mfma_f32_16x16x32_bf16 v[216:219], a[92:95], v[152:155], 0\n"
  "global_store_dwordx4 v0, v[220:223], s[68:69] offset:0 sc1\n"
  "v_mfma_f32_16x16x32_bf16 v[216:219], v[100:103], v[184:187], v[216:219]\n"
  "v_cvt_pk_bf16_f32 v224, v196, v197\n"
  "v_cvt_pk_bf16_f32 v225, v198, v199\n"
  "v_cvt_pk_bf16_f32 v226, v200, v201\n"
  "v_cvt_pk_bf16_f32 v227, v202, v203\n"
  "global_store_dwordx4 v0, v[224:227], s[68:69] offset:1024 sc1\n"
  "v_cvt_pk_bf16_f32 v228, v204, v205\n"
  "v_cvt_pk_bf16_f32 v229, v206, v207\n"
  "v_cvt_pk_bf16_f32 v230, v208, v209\n"
  "v_cvt_pk_bf16_f32 v231, v210, v211\n"
  "global_store_dwordx4 v0, v[228:231], s[68:69] offset:2048 sc1\n"
  "v_cvt_pk_bf16_f32 v232, v212, v213\n"
  "v_cvt_pk_bf16_f32 v233, v214, v215\n"
  "v_cvt_pk_bf16_f32 v234, v216, v217\n"
  "v_cvt_pk_bf16_f32 v235, v218, v219\n"
  "global_store_dwordx4 v0, v[232:235], s[68:69] offset:3072 sc1\n"
  "s_add_u32 s68, s68, 262144\n"
  "s_addc_u32 s69, s69, 0\n"
  "s_add_u32 s56, s56, 1\n"
  "s_cmp_eq_u32 s56, 64\n"
  "s_cbranch_scc1 TEND\n"
  "s_cmp_eq_u64 s[16:17], 0\n"
  "s_cbranch_scc1 TGO0\n"
  "s_waitcnt vmcnt(0)\n"
  "v_mov_b32 v7, 0\n"
  "v_mov_b32 v8, s56\n"
  "s_mov_b64 exec, 1\n"
  "global_store_dword v7, v8, s[70:71] sc1\n"
  "s_mov_b64 exec, -1\n"
  "s_branch TP0\n"
  "TEND:\n"
  "s_waitcnt vmcnt(0)\n"
  "v_mov_b32 v7, 0\n"
  "v_mov_b32 v8, s56\n"
  "s_mov_b64 exec, 1\n"
  "global_store_dword v7, v8, s[70:71] sc1\n"
  "s_mov_b64 exec, -1\n"
  "TQUIT:\n"
  "s_endpgm\n"
      : "+{v[0:15]}"(vin), "+{s[36:43]}"(sa), "+{s[44:59]}"(sb) :: CLOB);
  } else if (tk < 18) {
    const int rt = fd, cg = tk;
    v32u wa; v8u wb;
#pragma unroll
    for (int i = 0; i < 5; ++i) {
      const int gr = 20 * wave + 4 * i + g;
#pragma unroll
      for (int ct = 0; ct < 2; ++ct)
#pragma unroll
        for (int e = 0; e < 4; ++e) {
          const int k = 8 * (gr % 5) + 2 * e; const bool ok = k % 20 < 18;
          const u16* q = p.wo_a + (size_t)(ok ? 36 * (gr / 5) + 18 * (k / 20) + k % 20 : 0) * DM + 32 * cg + 16 * ct + n;
          const unsigned v = ok ? (unsigned)q[0] | ((unsigned)q[DM] << 16) : 0u;
          if (i < 4) wa[(i * 2 + ct) * 4 + e] = v; else wb[ct * 4 + e] = v;
        }
    }
    { const int row = lane >> 2; vin[1] = row * 1152 + (c4 ^ (2 * ((row >> 2) & 1))) * 16; }
    vin[2] = wave * 32768 + (4 * g + qq) * 64 + ((c4 >> 1) ^ (2 * (g & 1))) * 16 + (c4 & 1) * 8;
    vin[3] = (lane & 8) ? C_AP - CTL + (4 * wave + c4) * 128 : C_QAP - CTL + (8 * wave + (lane & 7)) * 128;
    vin[4] = wave * 512 + (16 * rt + 4 * wave + g) * 4;
    vin[5] = (n >> 3) * 1024 + (16 * ((n >> 1) & 3) + 4 * wave + g) * 16 + (n & 1) * 8;
    vin[6] = ((16 * rt + 4 * wave + g) * DM + 32 * cg + 2 * n) * 2;
    PTR(sa, 2, p.Wo + (size_t)wave * 1024 * DM + 32 * cg); PTR(sa, 4, p.out);
    sb[0] = WO_O + wave * 65536 + rt * 4096; sb[1] = WO_OA + rt * 20480 + wave * 5120 + 2048; sb[2] = WO_G; sb[3] = wave;
    sb[4] = WO_ST + (role * 4 + wave) * 4096;
    UNIFORM(sa, 8); UNIFORM(sb, 16);
    asm volatile(
  "s_nop 4\n"
  "s_lshl_b32 s80, s47, 15\n"
  "s_add_u32 m0, s80, 0\n"
  "s_nop 0\n"
  "global_load_lds_dwordx4 v1, s[38:39]\n"
  "s_add_u32 s38, s38, 18432\n"
  "s_addc_u32 s39, s39, 0\n"
  "s_add_u32 m0, s80, 1024\n"
  "s_nop 0\n"
  "global_load_lds_dwordx4 v1, s[38:39]\n"
  "s_add_u32 s38, s38, 18432\n"
  "s_addc_u32 s39, s39, 0\n"
  "s_add_u32 m0, s80, 2048\n"
  "s_nop 0\n"
  "global_load_lds_dwordx4 v1, s[38:39]\n"
  "s_add_u32 s38, s38, 18432\n"
  "s_addc_u32 s39, s39, 0\n"
  "s_add_u32 m0, s80, 3072\n"
  "s_nop 0\n"
  "global_load_lds_dwordx4 v1, s[38:39]\n"
  "s_add_u32 s38, s38, 18432\n"
  "s_addc_u32 s39, s39, 0\n"
  "s_add_u32 m0, s80, 4096\n"
  "s_nop 0\n"
  "global_load_lds_dwordx4 v1, s[38:39]\n"
  "s_add_u32 s38, s38, 18432\n"
  "s_addc_u32 s39, s39, 0\n"
  "s_add_u32 m0, s80, 5120\n"
  "s_nop 0\n"
  "global_load_lds_dwordx4 v1, s[38:39]\n"
  "s_add_u32 s38, s38, 18432\n"
  "s_addc_u32 s39, s39, 0\n"
  "s_add_u32 m0, s80, 6144\n"
  "s_nop 0\n"
  "global_load_lds_dwordx4 v1, s[38:39]\n"
  "s_add_u32 s38, s38, 18432\n"
  "s_addc_u32 s39, s39, 0\n"
  "s_add_u32 m0, s80, 7168\n"
  "s_nop 0\n"
  "global_load_lds_dwordx4 v1, s[38:39]\n"
  "s_add_u32 s38, s38, 18432\n"
  "s_addc_u32 s39, s39, 0\n"
  "s_add_u32 m0, s80, 8192\n"
  "s_nop 0\n"
  "global_load_lds_dwordx4 v1, s[38:39]\n"
  "s_add_u32 s38, s38, 18432\n"
  "s_addc_u32 s39, s39, 0\n"
  "s_add_u32 m0, s80, 9216\n"
  "s_nop 0\n"
  "global_load_lds_dwordx4 v1, s[38:39]\n"
  "s_add_u32 s38, s38, 18432\n"
  "s_addc_u32 s39, s39, 0\n"
  "s_add_u32 m0, s80, 10240\n"
  "s_nop 0\n"
  "global_load_lds_dwordx4 v1, s[38:39]\n"
  "s_add_u32 s38, s38, 18432\n"
  "s_addc_u32 s39, s39, 0\n"
  "s_add_u32 m0, s80, 11264\n"
  "s_nop 0\n"
  "global_load_lds_dwordx4 v1, s[38:39]\n"
  "s_add_u32 s38, s38, 18432\n"
  "s_addc_u32 s39, s39, 0\n"
  "s_add_u32 m0, s80, 12288\n"
  "s_nop 0\n"
  "global_load_lds_dwordx4 v1, s[38:39]\n"
  "s_add_u32 s38, s38, 18432\n"
  "s_addc_u32 s39, s39, 0\n"
  "s_add_u32 m0, s80, 13312\n"
  "s_nop 0\n"
  "global_load_lds_dwordx4 v1, s[38:39]\n"
  "s_add_u32 s38, s38, 18432\n"
  "s_addc_u32 s39, s39, 0\n"
  "s_add_u32 m0, s80, 14336\n"
  "s_nop 0\n"
  "global_load_lds_dwordx4 v1, s[38:39]\n"
  "s_add_u32 s38, s38, 18432\n"
  "s_addc_u32 s39, s39, 0\n"
  "s_add_u32 m0, s80, 15360\n"
  "s_nop 0\n"
  "global_load_lds_dwordx4 v1, s[38:39]\n"
  "s_add_u32 s38, s38, 18432\n"
  "s_addc_u32 s39, s39, 0\n"
  "s_add_u32 m0, s80, 16384\n"
  "s_nop 0\n"
  "global_load_lds_dwordx4 v1, s[38:39]\n"
  "s_add_u32 s38, s38, 18432\n"
  "s_addc_u32 s39, s39, 0\n"
  "s_add_u32 m0, s80, 17408\n"
  "s_nop 0\n"
  "global_load_lds_dwordx4 v1, s[38:39]\n"
  "s_add_u32 s38, s38, 18432\n"
  "s_addc_u32 s39, s39, 0\n"
  "s_add_u32 m0, s80, 18432\n"
  "s_nop 0\n"
  "global_load_lds_dwordx4 v1, s[38:39]\n"
  "s_add_u32 s38, s38, 18432\n"
  "s_addc_u32 s39, s39, 0\n"
  "s_add_u32 m0, s80, 19456\n"
  "s_nop 0\n"
  "global_load_lds_dwordx4 v1, s[38:39]\n"
  "s_add_u32 s38, s38, 18432\n"
  "s_addc_u32 s39, s39, 0\n"
  "s_add_u32 m0, s80, 20480\n"
  "s_nop 0\n"
  "global_load_lds_dwordx4 v1, s[38:39]\n"
  "s_add_u32 s38, s38, 18432\n"
  "s_addc_u32 s39, s39, 0\n"
  "s_add_u32 m0, s80, 21504\n"
  "s_nop 0\n"
  "global_load_lds_dwordx4 v1, s[38:39]\n"
  "s_add_u32 s38, s38, 18432\n"
  "s_addc_u32 s39, s39, 0\n"
  "s_add_u32 m0, s80, 22528\n"
  "s_nop 0\n"
  "global_load_lds_dwordx4 v1, s[38:39]\n"
  "s_add_u32 s38, s38, 18432\n"
  "s_addc_u32 s39, s39, 0\n"
  "s_add_u32 m0, s80, 23552\n"
  "s_nop 0\n"
  "global_load_lds_dwordx4 v1, s[38:39]\n"
  "s_add_u32 s38, s38, 18432\n"
  "s_addc_u32 s39, s39, 0\n"
  "s_add_u32 m0, s80, 24576\n"
  "s_nop 0\n"
  "global_load_lds_dwordx4 v1, s[38:39]\n"
  "s_add_u32 s38, s38, 18432\n"
  "s_addc_u32 s39, s39, 0\n"
  "s_add_u32 m0, s80, 25600\n"
  "s_nop 0\n"
  "global_load_lds_dwordx4 v1, s[38:39]\n"
  "s_add_u32 s38, s38, 18432\n"
  "s_addc_u32 s39, s39, 0\n"
  "s_add_u32 m0, s80, 26624\n"
  "s_nop 0\n"
  "global_load_lds_dwordx4 v1, s[38:39]\n"
  "s_add_u32 s38, s38, 18432\n"
  "s_addc_u32 s39, s39, 0\n"
  "s_add_u32 m0, s80, 27648\n"
  "s_nop 0\n"
  "global_load_lds_dwordx4 v1, s[38:39]\n"
  "s_add_u32 s38, s38, 18432\n"
  "s_addc_u32 s39, s39, 0\n"
  "s_add_u32 m0, s80, 28672\n"
  "s_nop 0\n"
  "global_load_lds_dwordx4 v1, s[38:39]\n"
  "s_add_u32 s38, s38, 18432\n"
  "s_addc_u32 s39, s39, 0\n"
  "s_add_u32 m0, s80, 29696\n"
  "s_nop 0\n"
  "global_load_lds_dwordx4 v1, s[38:39]\n"
  "s_add_u32 s38, s38, 18432\n"
  "s_addc_u32 s39, s39, 0\n"
  "s_add_u32 m0, s80, 30720\n"
  "s_nop 0\n"
  "global_load_lds_dwordx4 v1, s[38:39]\n"
  "s_add_u32 s38, s38, 18432\n"
  "s_addc_u32 s39, s39, 0\n"
  "s_add_u32 m0, s80, 31744\n"
  "s_nop 0\n"
  "global_load_lds_dwordx4 v1, s[38:39]\n"
  "s_add_u32 s38, s38, 18432\n"
  "s_addc_u32 s39, s39, 0\n"
  "v_xor_b32 v7, 32, v2\n"
  "s_waitcnt vmcnt(30)\n"
  "ds_read_b64_tr_b16 a[0:1], v2 offset:0\n"
  "ds_read_b64_tr_b16 a[2:3], v2 offset:1024\n"
  "ds_read_b64_tr_b16 a[4:5], v7 offset:0\n"
  "ds_read_b64_tr_b16 a[6:7], v7 offset:1024\n"
  "s_waitcnt vmcnt(28)\n"
  "ds_read_b64_tr_b16 a[8:9], v2 offset:2048\n"
  "ds_read_b64_tr_b16 a[10:11], v2 offset:3072\n"
  "ds_read_b64_tr_b16 a[12:13], v7 offset:2048\n"
  "ds_read_b64_tr_b16 a[14:15], v7 offset:3072\n"
  "s_waitcnt vmcnt(26)\n"
  "ds_read_b64_tr_b16 a[16:17], v2 offset:4096\n"
  "ds_read_b64_tr_b16 a[18:19], v2 offset:5120\n"
  "ds_read_b64_tr_b16 a[20:21], v7 offset:4096\n"
  "ds_read_b64_tr_b16 a[22:23], v7 offset:5120\n"
  "s_waitcnt vmcnt(24)\n"
  "ds_read_b64_tr_b16 a[24:25], v2 offset:6144\n"
  "ds_read_b64_tr_b16 a[26:27], v2 offset:7168\n"
  "ds_read_b64_tr_b16 a[28:29], v7 offset:6144\n"
  "ds_read_b64_tr_b16 a[30:31], v7 offset:7168\n"
  "s_waitcnt vmcnt(22)\n"
  "ds_read_b64_tr_b16 a[32:33], v2 offset:8192\n"
  "ds_read_b64_tr_b16 a[34:35], v2 offset:9216\n"
  "ds_read_b64_tr_b16 a[36:37], v7 offset:8192\n"
  "ds_read_b64_tr_b16 a[38:39], v7 offset:9216\n"
  "s_waitcnt vmcnt(20)\n"
  "ds_read_b64_tr_b16 a[40:41], v2 offset:10240\n"
  "ds_read_b64_tr_b16 a[42:43], v2 offset:11264\n"
  "ds_read_b64_tr_b16 a[44:45], v7 offset:10240\n"
  "ds_read_b64_tr_b16 a[46:47], v7 offset:11264\n"
  "s_waitcnt vmcnt(18)\n"
  "ds_read_b64_tr_b16 a[48:49], v2 offset:12288\n"
  "ds_read_b64_tr_b16 a[50:51], v2 offset:13312\n"
  "ds_read_b64_tr_b16 a[52:53], v7 offset:12288\n"
  "ds_read_b64_tr_b16 a[54:55], v7 offset:13312\n"
  "s_waitcnt vmcnt(16)\n"
  "ds_read_b64_tr_b16 a[56:57], v2 offset:14336\n"
  "ds_read_b64_tr_b16 a[58:59], v2 offset:15360\n"
  "ds_read_b64_tr_b16 a[60:61], v7 offset:14336\n"
  "ds_read_b64_tr_b16 a[62:63], v7 offset:15360\n"
  "s_waitcnt vmcnt(14)\n"
  "ds_read_b64_tr_b16 a[64:65], v2 offset:16384\n"
  "ds_read_b64_tr_b16 a[66:67], v2 offset:17408\n"
  "ds_read_b64_tr_b16 a[68:69], v7 offset:16384\n"
  "ds_read_b64_tr_b16 a[70:71], v7 offset:17408\n"
  "s_waitcnt vmcnt(12)\n"
  "ds_read_b64_tr_b16 a[72:73], v2 offset:18432\n"
  "ds_read_b64_tr_b16 a[74:75], v2 offset:19456\n"
  "ds_read_b64_tr_b16 a[76:77], v7 offset:18432\n"
  "ds_read_b64_tr_b16 a[78:79], v7 offset:19456\n"
  "s_waitcnt vmcnt(10)\n"
  "ds_read_b64_tr_b16 a[80:81], v2 offset:20480\n"
  "ds_read_b64_tr_b16 a[82:83], v2 offset:21504\n"
  "ds_read_b64_tr_b16 a[84:85], v7 offset:20480\n"
  "ds_read_b64_tr_b16 a[86:87], v7 offset:21504\n"
  "s_waitcnt vmcnt(8)\n"
  "ds_read_b64_tr_b16 a[88:89], v2 offset:22528\n"
  "ds_read_b64_tr_b16 a[90:91], v2 offset:23552\n"
  "ds_read_b64_tr_b16 a[92:93], v7 offset:22528\n"
  "ds_read_b64_tr_b16 a[94:95], v7 offset:23552\n"
  "s_waitcnt vmcnt(6)\n"
  "ds_read_b64_tr_b16 a[96:97], v2 offset:24576\n"
  "ds_read_b64_tr_b16 a[98:99], v2 offset:25600\n"
  "ds_read_b64_tr_b16 a[100:101], v7 offset:24576\n"
  "ds_read_b64_tr_b16 a[102:103], v7 offset:25600\n"
  "s_waitcnt vmcnt(4)\n"
  "ds_read_b64_tr_b16 a[104:105], v2 offset:26624\n"
  "ds_read_b64_tr_b16 a[106:107], v2 offset:27648\n"
  "ds_read_b64_tr_b16 a[108:109], v7 offset:26624\n"
  "ds_read_b64_tr_b16 a[110:111], v7 offset:27648\n"
  "s_waitcnt vmcnt(2)\n"
  "ds_read_b64_tr_b16 a[112:113], v2 offset:28672\n"
  "ds_read_b64_tr_b16 a[114:115], v2 offset:29696\n"
  "ds_read_b64_tr_b16 a[116:117], v7 offset:28672\n"
  "ds_read_b64_tr_b16 a[118:119], v7 offset:29696\n"
  "s_waitcnt vmcnt(0)\n"
  "ds_read_b64_tr_b16 a[120:121], v2 offset:30720\n"
  "ds_read_b64_tr_b16 a[122:123], v2 offset:31744\n"
  "ds_read_b64_tr_b16 a[124:125], v7 offset:30720\n"
  "ds_read_b64_tr_b16 a[126:127], v7 offset:31744\n"
  "s_waitcnt lgkmcnt(0)\n"
  "s_add_u32 m0, s80, 0\n"
  "s_nop 0\n"
  "global_load_lds_dwordx4 v1, s[38:39]\n"
  "s_add_u32 s38, s38, 18432\n"
  "s_addc_u32 s39, s39, 0\n"
  "s_add_u32 m0, s80, 1024\n"
  "s_nop 0\n"
  "global_load_lds_dwordx4 v1, s[38:39]\n"
  "s_add_u32 s38, s38, 18432\n"
  "s_addc_u32 s39, s39, 0\n"
  "s_add_u32 m0, s80, 2048\n"
  "s_nop 0\n"
  "global_load_lds_dwordx4 v1, s[38:39]\n"
  "s_add_u32 s38, s38, 18432\n"
  "s_addc_u32 s39, s39, 0\n"
  "s_add_u32 m0, s80, 3072\n"
  "s_nop 0\n"
  "global_load_lds_dwordx4 v1, s[38:39]\n"
  "s_add_u32 s38, s38, 18432\n"
  "s_addc_u32 s39, s39, 0\n"
  "s_add_u32 m0, s80, 4096\n"
  "s_nop 0\n"
  "global_load_lds_dwordx4 v1, s[38:39]\n"
  "s_add_u32 s38, s38, 18432\n"
  "s_addc_u32 s39, s39, 0\n"
  "s_add_u32 m0, s80, 5120\n"
  "s_nop 0\n"
  "global_load_lds_dwordx4 v1, s[38:39]\n"
  "s_add_u32 s38, s38, 18432\n"
  "s_addc_u32 s39, s39, 0\n"
  "s_add_u32 m0, s80, 6144\n"
  "s_nop 0\n"
  "global_load_lds_dwordx4 v1, s[38:39]\n"
  "s_add_u32 s38, s38, 18432\n"
  "s_addc_u32 s39, s39, 0\n"
  "s_add_u32 m0, s80, 7168\n"
  "s_nop 0\n"
  "global_load_lds_dwordx4 v1, s[38:39]\n"
  "s_add_u32 s38, s38, 18432\n"
  "s_addc_u32 s39, s39, 0\n"
  "s_add_u32 m0, s80, 8192\n"
  "s_nop 0\n"
  "global_load_lds_dwordx4 v1, s[38:39]\n"
  "s_add_u32 s38, s38, 18432\n"
  "s_addc_u32 s39, s39, 0\n"
  "s_add_u32 m0, s80, 9216\n"
  "s_nop 0\n"
  "global_load_lds_dwordx4 v1, s[38:39]\n"
  "s_add_u32 s38, s38, 18432\n"
  "s_addc_u32 s39, s39, 0\n"
  "s_add_u32 m0, s80, 10240\n"
  "s_nop 0\n"
  "global_load_lds_dwordx4 v1, s[38:39]\n"
  "s_add_u32 s38, s38, 18432\n"
  "s_addc_u32 s39, s39, 0\n"
  "s_add_u32 m0, s80, 11264\n"
  "s_nop 0\n"
  "global_load_lds_dwordx4 v1, s[38:39]\n"
  "s_add_u32 s38, s38, 18432\n"
  "s_addc_u32 s39, s39, 0\n"
  "s_add_u32 m0, s80, 12288\n"
  "s_nop 0\n"
  "global_load_lds_dwordx4 v1, s[38:39]\n"
  "s_add_u32 s38, s38, 18432\n"
  "s_addc_u32 s39, s39, 0\n"
  "s_add_u32 m0, s80, 13312\n"
  "s_nop 0\n"
  "global_load_lds_dwordx4 v1, s[38:39]\n"
  "s_add_u32 s38, s38, 18432\n"
  "s_addc_u32 s39, s39, 0\n"
  "s_add_u32 m0, s80, 14336\n"
  "s_nop 0\n"
  "global_load_lds_dwordx4 v1, s[38:39]\n"
  "s_add_u32 s38, s38, 18432\n"
  "s_addc_u32 s39, s39, 0\n"
  "s_add_u32 m0, s80, 15360\n"
  "s_nop 0\n"
  "global_load_lds_dwordx4 v1, s[38:39]\n"
  "s_add_u32 s38, s38, 18432\n"
  "s_addc_u32 s39, s39, 0\n"
  "s_add_u32 m0, s80, 16384\n"
  "s_nop 0\n"
  "global_load_lds_dwordx4 v1, s[38:39]\n"
  "s_add_u32 s38, s38, 18432\n"
  "s_addc_u32 s39, s39, 0\n"
  "s_add_u32 m0, s80, 17408\n"
  "s_nop 0\n"
  "global_load_lds_dwordx4 v1, s[38:39]\n"
  "s_add_u32 s38, s38, 18432\n"
  "s_addc_u32 s39, s39, 0\n"
  "s_add_u32 m0, s80, 18432\n"
  "s_nop 0\n"
  "global_load_lds_dwordx4 v1, s[38:39]\n"
  "s_add_u32 s38, s38, 18432\n"
  "s_addc_u32 s39, s39, 0\n"
  "s_add_u32 m0, s80, 19456\n"
  "s_nop 0\n"
  "global_load_lds_dwordx4 v1, s[38:39]\n"
  "s_add_u32 s38, s38, 18432\n"
  "s_addc_u32 s39, s39, 0\n"
  "s_add_u32 m0, s80, 20480\n"
  "s_nop 0\n"
  "global_load_lds_dwordx4 v1, s[38:39]\n"
  "s_add_u32 s38, s38, 18432\n"
  "s_addc_u32 s39, s39, 0\n"
  "s_add_u32 m0, s80, 21504\n"
  "s_nop 0\n"
  "global_load_lds_dwordx4 v1, s[38:39]\n"
  "s_add_u32 s38, s38, 18432\n"
  "s_addc_u32 s39, s39, 0\n"
  "s_add_u32 m0, s80, 22528\n"
  "s_nop 0\n"
  "global_load_lds_dwordx4 v1, s[38:39]\n"
  "s_add_u32 s38, s38, 18432\n"
  "s_addc_u32 s39, s39, 0\n"
  "s_add_u32 m0, s80, 23552\n"
  "s_nop 0\n"
  "global_load_lds_dwordx4 v1, s[38:39]\n"
  "s_add_u32 s38, s38, 18432\n"
  "s_addc_u32 s39, s39, 0\n"
  "s_add_u32 m0, s80, 24576\n"
  "s_nop 0\n"
  "global_load_lds_dwordx4 v1, s[38:39]\n"
  "s_add_u32 s38, s38, 18432\n"
  "s_addc_u32 s39, s39, 0\n"
  "s_add_u32 m0, s80, 25600\n"
  "s_nop 0\n"
  "global_load_lds_dwordx4 v1, s[38:39]\n"
  "s_add_u32 s38, s38, 18432\n"
  "s_addc_u32 s39, s39, 0\n"
  "s_add_u32 m0, s80, 26624\n"
  "s_nop 0\n"
  "global_load_lds_dwordx4 v1, s[38:39]\n"
  "s_add_u32 s38, s38, 18432\n"
  "s_addc_u32 s39, s39, 0\n"
  "s_add_u32 m0, s80, 27648\n"
  "s_nop 0\n"
  "global_load_lds_dwordx4 v1, s[38:39]\n"
  "s_add_u32 s38, s38, 18432\n"
  "s_addc_u32 s39, s39, 0\n"
  "s_add_u32 m0, s80, 28672\n"
  "s_nop 0\n"
  "global_load_lds_dwordx4 v1, s[38:39]\n"
  "s_add_u32 s38, s38, 18432\n"
  "s_addc_u32 s39, s39, 0\n"
  "s_add_u32 m0, s80, 29696\n"
  "s_nop 0\n"
  "global_load_lds_dwordx4 v1, s[38:39]\n"
  "s_add_u32 s38, s38, 18432\n"
  "s_addc_u32 s39, s39, 0\n"
  "s_add_u32 m0, s80, 30720\n"
  "s_nop 0\n"
  "global_load_lds_dwordx4 v1, s[38:39]\n"
  "s_add_u32 s38, s38, 18432\n"
  "s_addc_u32 s39, s39, 0\n"
  "s_add_u32 m0, s80, 31744\n"
  "s_nop 0\n"
  "global_load_lds_dwordx4 v1, s[38:39]\n"
  "s_add_u32 s38, s38, 18432\n"
  "s_addc_u32 s39, s39, 0\n"
  "v_xor_b32 v7, 32, v2\n"
  "s_waitcnt vmcnt(30)\n"
  "ds_read_b64_tr_b16 a[128:129], v2 offset:0\n"
  "ds_read_b64_tr_b16 a[130:131], v2 offset:1024\n"
  "ds_read_b64_tr_b16 a[132:133], v7 offset:0\n"
  "ds_read_b64_tr_b16 a[134:135], v7 offset:1024\n"
  "s_waitcnt vmcnt(28)\n"
  "ds_read_b64_tr_b16 a[136:137], v2 offset:2048\n"
  "ds_read_b64_tr_b16 a[138:139], v2 offset:3072\n"
  "ds_read_b64_tr_b16 a[140:141], v7 offset:2048\n"
  "ds_read_b64_tr_b16 a[142:143], v7 offset:3072\n"
  "s_waitcnt vmcnt(26)\n"
  "ds_read_b64_tr_b16 a[144:145], v2 offset:4096\n"
  "ds_read_b64_tr_b16 a[146:147], v2 offset:5120\n"
  "ds_read_b64_tr_b16 a[148:149], v7 offset:4096\n"
  "ds_read_b64_tr_b16 a[150:151], v7 offset:5120\n"
  "s_waitcnt vmcnt(24)\n"
  "ds_read_b64_tr_b16 a[152:153], v2 offset:6144\n"
  "ds_read_b64_tr_b16 a[154:155], v2 offset:7168\n"
  "ds_read_b64_tr_b16 a[156:157], v7 offset:6144\n"
  "ds_read_b64_tr_b16 a[158:159], v7 offset:7168\n"
  "s_waitcnt vmcnt(22)\n"
  "ds_read_b64_tr_b16 a[160:161], v2 offset:8192\n"
  "ds_read_b64_tr_b16 a[162:163], v2 offset:9216\n"
  "ds_read_b64_tr_b16 a[164:165], v7 offset:8192\n"
  "ds_read_b64_tr_b16 a[166:167], v7 offset:9216\n"
  "s_waitcnt vmcnt(20)\n"
  "ds_read_b64_tr_b16 a[168:169], v2 offset:10240\n"
  "ds_read_b64_tr_b16 a[170:171], v2 offset:11264\n"
  "ds_read_b64_tr_b16 a[172:173], v7 offset:10240\n"
  "ds_read_b64_tr_b16 a[174:175], v7 offset:11264\n"
  "s_waitcnt vmcnt(18)\n"
  "ds_read_b64_tr_b16 a[176:177], v2 offset:12288\n"
  "ds_read_b64_tr_b16 a[178:179], v2 offset:13312\n"
  "ds_read_b64_tr_b16 a[180:181], v7 offset:12288\n"
  "ds_read_b64_tr_b16 a[182:183], v7 offset:13312\n"
  "s_waitcnt vmcnt(16)\n"
  "ds_read_b64_tr_b16 a[184:185], v2 offset:14336\n"
  "ds_read_b64_tr_b16 a[186:187], v2 offset:15360\n"
  "ds_read_b64_tr_b16 a[188:189], v7 offset:14336\n"
  "ds_read_b64_tr_b16 a[190:191], v7 offset:15360\n"
  "s_waitcnt vmcnt(14)\n"
  "ds_read_b64_tr_b16 a[192:193], v2 offset:16384\n"
  "ds_read_b64_tr_b16 a[194:195], v2 offset:17408\n"
  "ds_read_b64_tr_b16 a[196:197], v7 offset:16384\n"
  "ds_read_b64_tr_b16 a[198:199], v7 offset:17408\n"
  "s_waitcnt vmcnt(12)\n"
  "ds_read_b64_tr_b16 a[200:201], v2 offset:18432\n"
  "ds_read_b64_tr_b16 a[202:203], v2 offset:19456\n"
  "ds_read_b64_tr_b16 a[204:205], v7 offset:18432\n"
  "ds_read_b64_tr_b16 a[206:207], v7 offset:19456\n"
  "s_waitcnt vmcnt(10)\n"
  "ds_read_b64_tr_b16 a[208:209], v2 offset:20480\n"
  "ds_read_b64_tr_b16 a[210:211], v2 offset:21504\n"
  "ds_read_b64_tr_b16 a[212:213], v7 offset:20480\n"
  "ds_read_b64_tr_b16 a[214:215], v7 offset:21504\n"
  "s_waitcnt vmcnt(8)\n"
  "ds_read_b64_tr_b16 a[216:217], v2 offset:22528\n"
  "ds_read_b64_tr_b16 a[218:219], v2 offset:23552\n"
  "ds_read_b64_tr_b16 a[220:221], v7 offset:22528\n"
  "ds_read_b64_tr_b16 a[222:223], v7 offset:23552\n"
  "s_waitcnt vmcnt(6)\n"
  "ds_read_b64_tr_b16 a[224:225], v2 offset:24576\n"
  "ds_read_b64_tr_b16 a[226:227], v2 offset:25600\n"
  "ds_read_b64_tr_b16 a[228:229], v7 offset:24576\n"
  "ds_read_b64_tr_b16 a[230:231], v7 offset:25600\n"
  "s_waitcnt vmcnt(4)\n"
  "ds_read_b64_tr_b16 a[232:233], v2 offset:26624\n"
  "ds_read_b64_tr_b16 a[234:235], v2 offset:27648\n"
  "ds_read_b64_tr_b16 a[236:237], v7 offset:26624\n"
  "ds_read_b64_tr_b16 a[238:239], v7 offset:27648\n"
  "s_waitcnt vmcnt(2)\n"
  "ds_read_b64_tr_b16 a[240:241], v2 offset:28672\n"
  "ds_read_b64_tr_b16 a[242:243], v2 offset:29696\n"
  "ds_read_b64_tr_b16 a[244:245], v7 offset:28672\n"
  "ds_read_b64_tr_b16 a[246:247], v7 offset:29696\n"
  "s_waitcnt vmcnt(0)\n"
  "ds_read_b64_tr_b16 a[248:249], v2 offset:30720\n"
  "ds_read_b64_tr_b16 a[250:251], v2 offset:31744\n"
  "ds_read_b64_tr_b16 a[252:253], v7 offset:30720\n"
  "ds_read_b64_tr_b16 a[254:255], v7 offset:31744\n"
  "s_waitcnt lgkmcnt(0)\n"
  "s_barrier\n"
  "s_mov_b32 s56, 0\n"
  "s_mov_b32 s57, 400000\n"
  "s_add_u32 s60, s36, s44\n"
  "s_addc_u32 s61, s37, 0\n"
  "s_add_u32 s62, s36, s45\n"
  "s_addc_u32 s63, s37, 0\n"
  "s_add_u32 s64, s36, s46\n"
  "s_addc_u32 s65, s37, 0\n"
  "s_add_u32 s66, s36, 16777216\n"
  "s_addc_u32 s67, s37, 0\n"
  "s_lshl_b32 s74, s47, 12\n"
  "v_add_u32 v2, s74, v0\n"
  "OPOLL:\n"
  "global_load_dword v7, v3, s[66:67] sc1\n"
  "s_waitcnt vmcnt(0)\n"
  "v_cmp_ge_u32 vcc, s56, v7\n"
  "s_cbranch_vccz OGO\n"
  "s_sub_u32 s57, s57, 1\n"
  "s_cbranch_scc1 OQUIT\n"
  "s_sleep 4\n"
  "s_branch OPOLL\n"
  "OGO:\n"
  "global_load_dwordx4 v[56:59], v0, s[60:61] offset:0 sc1\n"
  "global_load_dwordx4 v[60:63], v0, s[60:61] offset:1024 sc1\n"
  "global_load_dwordx4 v[64:67], v0, s[60:61] offset:2048 sc1\n"
  "global_load_dwordx4 v[68:71], v0, s[60:61] offset:3072 sc1\n"
  "s_add_u32 s60, s60, 8192\n"
  "s_addc_u32 s61, s61, 0\n"
  "global_load_dwordx4 v[72:75], v0, s[60:61] offset:0 sc1\n"
  "global_load_dwordx4 v[76:79], v0, s[60:61] offset:1024 sc1\n"
  "global_load_dwordx4 v[80:83], v0, s[60:61] offset:2048 sc1\n"
  "global_load_dwordx4 v[84:87], v0, s[60:61] offset:3072 sc1\n"
  "s_add_u32 s60, s60, 8192\n"
  "s_addc_u32 s61, s61, 0\n"
  "global_load_dwordx4 v[88:91], v0, s[60:61] offset:0 sc1\n"
  "global_load_dwordx4 v[92:95], v0, s[60:61] offset:1024 sc1\n"
  "global_load_dwordx4 v[96:99], v0, s[60:61] offset:2048 sc1\n"
  "global_load_dwordx4 v[100:103], v0, s[60:61] offset:3072 sc1\n"
  "s_add_u32 s60, s60, 8192\n"
  "s_addc_u32 s61, s61, 0\n"
  "global_load_dwordx4 v[104:107], v0, s[60:61] offset:0 sc1\n"
  "global_load_dwordx4 v[108:111], v0, s[60:61] offset:1024 sc1\n"
  "global_load_dwordx4 v[112:115], v0, s[60:61] offset:2048 sc1\n"
  "global_load_dwordx4 v[116:119], v0, s[60:61] offset:3072 sc1\n"
  "s_add_u32 s60, s60, 8192\n"
  "s_addc_u32 s61, s61, 0\n"
  "global_load_dwordx4 v[120:123], v0, s[60:61] offset:0 sc1\n"
  "global_load_dwordx4 v[124:127], v0, s[60:61] offset:1024 sc1\n"
  "global_load_dwordx4 v[128:131], v0, s[60:61] offset:2048 sc1\n"
  "global_load_dword v132, v3, s[66:67] sc1\n"
  "s_waitcnt vmcnt(19)\n"
  "v_mfma_f32_16x16x32_bf16 v[136:139], a[0:3], v[56:59], 0\n"
  "v_mfma_f32_16x16x32_bf16 v[140:143], a[4:7], v[56:59], 0\n"
  "global_load_dwordx4 v[56:59], v0, s[60:61] offset:3072 sc1\n"
  "s_add_u32 s60, s60, 8192\n"
  "s_addc_u32 s61, s61, 0\n"
  "s_waitcnt vmcnt(19)\n"
  "v_mfma_f32_16x16x32_bf16 v[136:139], a[8:11], v[60:63], v[136:139]\n"
  "v_mfma_f32_16x16x32_bf16 v[140:143], a[12:15], v[60:63], v[140:143]\n"
  "global_load_dwordx4 v[60:63], v0, s[60:61] offset:0 sc1\n"
  "s_waitcnt vmcnt(19)\n"
  "v_mfma_f32_16x16x32_bf16 v[136:139], a[16:19], v[64:67], v[136:139]\n"
  "v_mfma_f32_16x16x32_bf16 v[140:143], a[20:23], v[64:67], v[140:143]\n"
  "global_load_dwordx4 v[64:67], v0, s[60:61] offset:1024 sc1\n"
  "s_waitcnt vmcnt(19)\n"
  "v_mfma_f32_16x16x32_bf16 v[136:139], a[24:27], v[68:71], v[136:139]\n"
  "v_mfma_f32_16x16x32_bf16 v[140:143], a[28:31], v[68:71], v[140:143]\n"
  "global_load_dwordx4 v[68:71], v0, s[60:61] offset:2048 sc1\n"
  "s_waitcnt vmcnt(19)\n"
  "v_mfma_f32_16x16x32_bf16 v[136:139], a[32:35], v[72:75], v[136:139]\n"
  "v_mfma_f32_16x16x32_bf16 v[140:143], a[36:39], v[72:75], v[140:143]\n"
  "global_load_dwordx4 v[72:75], v0, s[60:61] offset:3072 sc1\n"
  "s_add_u32 s60, s60, 8192\n"
  "s_addc_u32 s61, s61, 0\n"
  "s_waitcnt vmcnt(19)\n"
  "v_mfma_f32_16x16x32_bf16 v[136:139], a[40:43], v[76:79], v[136:139]\n"
  "v_mfma_f32_16x16x32_bf16 v[140:143], a[44:47], v[76:79], v[140:143]\n"
  "global_load_dwordx4 v[76:79], v0, s[60:61] offset:0 sc1\n"
  "s_waitcnt vmcnt(19)\n"
  "v_mfma_f32_16x16x32_bf16 v[136:139], a[48:51], v[80:83], v[136:139]\n"
  "v_mfma_f32_16x16x32_bf16 v[140:143], a[52:55], v[80:83], v[140:143]\n"
  "global_load_dwordx4 v[80:83], v0, s[60:61] offset:1024 sc1\n"
  "s_waitcnt vmcnt(19)\n"
  "v_mfma_f32_16x16x32_bf16 v[136:139], a[56:59], v[84:87], v[136:139]\n"
  "v_mfma_f32_16x16x32_bf16 v[140:143], a[60:63], v[84:87], v[140:143]\n"
  "global_load_dwordx4 v[84:87], v0, s[60:61] offset:2048 sc1\n"
  "s_waitcnt vmcnt(19)\n"
  "v_mfma_f32_16x16x32_bf16 v[136:139], a[64:67], v[88:91], v[136:139]\n"
  "v_mfma_f32_16x16x32_bf16 v[140:143], a[68:71], v[88:91], v[140:143]\n"
  "global_load_dwordx4 v[88:91], v0, s[60:61] offset:3072 sc1\n"
  "s_add_u32 s60, s60, 8192\n"
  "s_addc_u32 s61, s61, 0\n"
  "s_waitcnt vmcnt(19)\n"
  "v_mfma_f32_16x16x32_bf16 v[136:139], a[72:75], v[92:95], v[136:139]\n"
  "v_mfma_f32_16x16x32_bf16 v[140:143], a[76:79], v[92:95], v[140:143]\n"
  "global_load_dwordx4 v[92:95], v0, s[60:61] offset:0 sc1\n"
  "s_waitcnt vmcnt(19)\n"
  "v_mfma_f32_16x16x32_bf16 v[136:139], a[80:83], v[96:99], v[136:139]\n"
  "v_mfma_f32_16x16x32_bf16 v[140:143], a[84:87], v[96:99], v[140:143]\n"
  "global_load_dwordx4 v[96:99], v0, s[60:61] offset:1024 sc1\n"
  "s_waitcnt vmcnt(19)\n"
  "v_mfma_f32_16x16x32_bf16 v[136:139], a[88:91], v[100:103], v[136:139]\n"
  "v_mfma_f32_16x16x32_bf16 v[140:143], a[92:95], v[100:103], v[140:143]\n"
  "global_load_dwordx4 v[100:103], v0, s[60:61] offset:2048 sc1\n"
  "s_waitcnt vmcnt(19)\n"
  "v_mfma_f32_16x16x32_bf16 v[136:139], a[96:99], v[104:107], v[136:139]\n"
  "v_mfma_f32_16x16x32_bf16 v[140:143], a[100:103], v[104:107], v[140:143]\n"
  "global_load_dwordx4 v[104:107], v0, s[60:61] offset:3072 sc1\n"
  "s_add_u32 s60, s60, 204800\n"
  "s_addc_u32 s61, s61, 0\n"
  "s_waitcnt vmcnt(19)\n"
  "v_mfma_f32_16x16x32_bf16 v[136:139], a[104:107], v[108:111], v[136:139]\n"
  "v_mfma_f32_16x16x32_bf16 v[140:143], a[108:111], v[108:111], v[140:143]\n"
  "global_load_dwordx4 v[108:111], v0, s[62:63] offset:-2048 sc1\n"
  "s_waitcnt vmcnt(19)\n"
  "v_mfma_f32_16x16x32_bf16 v[136:139], a[112:115], v[112:115], v[136:139]\n"
  "v_mfma_f32_16x16x32_bf16 v[140:143], a[116:119], v[112:115], v[140:143]\n"
  "global_load_dwordx4 v[112:115], v0, s[62:63] offset:-1024 sc1\n"
  "s_waitcnt vmcnt(19)\n"
  "v_mfma_f32_16x16x32_bf16 v[136:139], a[120:123], v[116:119], v[136:139]\n"
  "v_mfma_f32_16x16x32_bf16 v[140:143], a[124:127], v[116:119], v[140:143]\n"
  "global_load_dwordx4 v[116:119], v0, s[62:63] offset:0 sc1\n"
  "s_waitcnt vmcnt(19)\n"
  "v_mfma_f32_16x16x32_bf16 v[136:139], a[128:131], v[120:123], v[136:139]\n"
  "v_mfma_f32_16x16x32_bf16 v[140:143], a[132:135], v[120:123], v[140:143]\n"
  "global_load_dwordx4 v[120:123], v0, s[62:63] offset:1024 sc1\n"
  "s_waitcnt vmcnt(19)\n"
  "v_mfma_f32_16x16x32_bf16 v[136:139], a[136:139], v[124:127], v[136:139]\n"
  "v_mfma_f32_16x16x32_bf16 v[140:143], a[140:143], v[124:127], v[140:143]\n"
  "global_load_dwordx4 v[124:127], v0, s[62:63] offset:2048 sc1\n"
  "s_add_u32 s62, s62, 40960\n"
  "s_addc_u32 s63, s63, 0\n"
  "s_waitcnt vmcnt(19)\n"
  "v_mfma_f32_16x16x32_bf16 v[136:139], a[144:147], v[128:131], v[136:139]\n"
  "v_mfma_f32_16x16x32_bf16 v[140:143], a[148:151], v[128:131], v[140:143]\n"
  "global_load_dword v128, v4, s[64:65] sc1\n"
  "s_add_u32 s64, s64, 2048\n"
  "s_addc_u32 s65, s65, 0\n"
  "s_add_u32 s74, s56, 2\n"
  "s_waitcnt vmcnt(19)\n"
  "v_cmp_gt_u32 vcc, s74, v132\n"
  "OMID:\n"
  "s_cbranch_vccnz ODRAIN\n"
  "s_waitcnt vmcnt(18)\n"
  "v_mfma_f32_16x16x32_bf16 v[136:139], a[152:155], v[56:59], v[136:139]\n"
  "v_mfma_f32_16x16x32_bf16 v[140:143], a[156:159], v[56:59], v[140:143]\n"
  "global_load_dwordx4 v[56:59], v0, s[60:61] offset:0 sc1\n"
  "s_waitcnt vmcnt(18)\n"
  "v_mfma_f32_16x16x32_bf16 v[136:139], a[160:163], v[60:63], v[136:139]\n"
  "v_mfma_f32_16x16x32_bf16 v[140:143], a[164:167], v[60:63], v[140:143]\n"
  "global_load_dwordx4 v[60:63], v0, s[60:61] offset:1024 sc1\n"
  "s_waitcnt vmcnt(18)\n"
  "v_mfma_f32_16x16x32_bf16 v[136:139], a[168:171], v[64:67], v[136:139]\n"
  "v_mfma_f32_16x16x32_bf16 v[140:143], a[172:175], v[64:67], v[140:143]\n"
  "global_load_dwordx4 v[64:67], v0, s[60:61] offset:2048 sc1\n"
  "s_waitcnt vmcnt(18)\n"
  "v_mfma_f32_16x16x32_bf16 v[136:139], a[176:179], v[68:71], v[136:139]\n"
  "v_mfma_f32_16x16x32_bf16 v[140:143], a[180:183], v[68:71], v[140:143]\n"
  "global_load_dwordx4 v[68:71], v0, s[60:61] offset:3072 sc1\n"
  "s_add_u32 s60, s60, 8192\n"
  "s_addc_u32 s61, s61, 0\n"
  "s_waitcnt vmcnt(18)\n"
  "v_mfma_f32_16x16x32_bf16 v[136:139], a[184:187], v[72:75], v[136:139]\n"
  "v_mfma_f32_16x16x32_bf16 v[140:143], a[188:191], v[72:75], v[140:143]\n"
  "global_load_dwordx4 v[72:75], v0, s[60:61] offset:0 sc1\n"
  "s_waitcnt vmcnt(18)\n"
  "v_mfma_f32_16x16x32_bf16 v[136:139], a[192:195], v[76:79], v[136:139]\n"
  "v_mfma_f32_16x16x32_bf16 v[140:143], a[196:199], v[76:79], v[140:143]\n"
  "global_load_dwordx4 v[76:79], v0, s[60:61] offset:1024 sc1\n"
  "s_waitcnt vmcnt(18)\n"
  "v_mfma_f32_16x16x32_bf16 v[136:139], a[200:203], v[80:83], v[136:139]\n"
  "v_mfma_f32_16x16x32_bf16 v[140:143], a[204:207], v[80:83], v[140:143]\n"
  "global_load_dwordx4 v[80:83], v0, s[60:61] offset:2048 sc1\n"
  "s_waitcnt vmcnt(18)\n"
  "v_mfma_f32_16x16x32_bf16 v[136:139], a[208:211], v[84:87], v[136:139]\n"
  "v_mfma_f32_16x16x32_bf16 v[140:143], a[212:215], v[84:87], v[140:143]\n"
  "global_load_dwordx4 v[84:87], v0, s[60:61] offset:3072 sc1\n"
  "s_add_u32 s60, s60, 8192\n"
  "s_addc_u32 s61, s61, 0\n"
  "s_waitcnt vmcnt(18)\n"
  "v_mfma_f32_16x16x32_bf16 v[136:139], a[216:219], v[88:91], v[136:139]\n"
  "v_mfma_f32_16x16x32_bf16 v[140:143], a[220:223], v[88:91], v[140:143]\n"
  "global_load_dwordx4 v[88:91], v0, s[60:61] offset:0 sc1\n"
  "s_waitcnt vmcnt(18)\n"
  "v_mfma_f32_16x16x32_bf16 v[136:139], a[224:227], v[92:95], v[136:139]\n"
  "v_mfma_f32_16x16x32_bf16 v[140:143], a[228:231], v[92:95], v[140:143]\n"
  "global_load_dwordx4 v[92:95], v0, s[60:61] offset:1024 sc1\n"
  "s_waitcnt vmcnt(18)\n"
  "v_mfma_f32_16x16x32_bf16 v[136:139], a[232:235], v[96:99], v[136:139]\n"
  "v_mfma_f32_16x16x32_bf16 v[140:143], a[236:239], v[96:99], v[140:143]\n"
  "global_load_dwordx4 v[96:99], v0, s[60:61] offset:2048 sc1\n"
  "s_waitcnt vmcnt(18)\n"
  "v_mfma_f32_16x16x32_bf16 v[136:139], a[240:243], v[100:103], v[136:139]\n"
  "v_mfma_f32_16x16x32_bf16 v[140:143], a[244:247], v[100:103], v[140:143]\n"
  "global_load_dwordx4 v[100:103], v0, s[60:61] offset:3072 sc1\n"
  "s_add_u32 s60, s60, 8192\n"
  "s_addc_u32 s61, s61, 0\n"
  "s_waitcnt vmcnt(18)\n"
  "v_mfma_f32_16x16x32_bf16 v[136:139], a[248:251], v[104:107], v[136:139]\n"
  "v_mfma_f32_16x16x32_bf16 v[140:143], a[252:255], v[104:107], v[140:143]\n"
  "global_load_dwordx4 v[104:107], v0, s[60:61] offset:0 sc1\n"
  "s_waitcnt vmcnt(18)\n"
  "v_mfma_f32_16x16x32_bf16 v[144:147], v[16:19], v[108:111], 0\n"
  "v_mfma_f32_16x16x32_bf16 v[148:151], v[20:23], v[108:111], 0\n"
  "global_load_dwordx4 v[108:111], v0, s[60:61] offset:1024 sc1\n"
  "s_waitcnt vmcnt(18)\n"
  "v_mfma_f32_16x16x32_bf16 v[144:147], v[24:27], v[112:115], v[144:147]\n"
  "v_mfma_f32_16x16x32_bf16 v[148:151], v[28:31], v[112:115], v[148:151]\n"
  "global_load_dwordx4 v[112:115], v0, s[60:61] offset:2048 sc1\n"
  "s_waitcnt vmcnt(18)\n"
  "v_mfma_f32_16x16x32_bf16 v[144:147], v[32:35], v[116:119], v[144:147]\n"
  "v_mfma_f32_16x16x32_bf16 v[148:151], v[36:39], v[116:119], v[148:151]\n"
  "global_load_dwordx4 v[116:119], v0, s[60:61] offset:3072 sc1\n"
  "s_add_u32 s60, s60, 8192\n"
  "s_addc_u32 s61, s61, 0\n"
  "s_waitcnt vmcnt(18)\n"
  "v_mfma_f32_16x16x32_bf16 v[144:147], v[40:43], v[120:123], v[144:147]\n"
  "v_mfma_f32_16x16x32_bf16 v[148:151], v[44:47], v[120:123], v[148:151]\n"
  "global_load_dwordx4 v[120:123], v0, s[60:61] offset:0 sc1\n"
  "s_waitcnt vmcnt(18)\n"
  "v_mfma_f32_16x16x32_bf16 v[144:147], v[48:51], v[124:127], v[144:147]\n"
  "v_mfma_f32_16x16x32_bf16 v[148:151], v[52:55], v[124:127], v[148:151]\n"
  "global_load_dwordx4 v[124:127], v0, s[60:61] offset:1024 sc1\n"
  "s_waitcnt vmcnt(18)\n"
  "v_mov_b32 v168, v128\n"
  "global_load_dwordx4 v[128:131], v0, s[60:61] offset:2048 sc1\n"
  "global_load_dword v132, v3, s[66:67] sc1\n"
  "ds_write_b128 v2, v[136:139] offset:0\n"
  "ds_write_b128 v2, v[140:143] offset:1024\n"
  "ds_write_b128 v2, v[144:147] offset:2048\n"
  "ds_write_b128 v2, v[148:151] offset:3072\n"
  "v_xor_b32 v2, 0x4000, v2\n"
  "s_add_u32 s56, s56, 1\n"
  "s_waitcnt vmcnt(19)\n"
  "v_mfma_f32_16x16x32_bf16 v[136:139], a[0:3], v[56:59], 0\n"
  "v_mfma_f32_16x16x32_bf16 v[140:143], a[4:7], v[56:59], 0\n"
  "global_load_dwordx4 v[56:59], v0, s[60:61] offset:3072 sc1\n"
  "s_add_u32 s60, s60, 8192\n"
  "s_addc_u32 s61, s61, 0\n"
  "s_waitcnt vmcnt(19)\n"
  "v_mfma_f32_16x16x32_bf16 v[136:139], a[8:11], v[60:63], v[136:139]\n"
  "v_mfma_f32_16x16x32_bf16 v[140:143], a[12:15], v[60:63], v[140:143]\n"
  "global_load_dwordx4 v[60:63], v0, s[60:61] offset:0 sc1\n"
  "s_waitcnt vmcnt(19)\n"
  "v_mfma_f32_16x16x32_bf16 v[136:139], a[16:19], v[64:67], v[136:139]\n"
  "v_mfma_f32_16x16x32_bf16 v[140:143], a[20:23], v[64:67], v[140:143]\n"
  "global_load_dwordx4 v[64:67], v0, s[60:61] offset:1024 sc1\n"
  "s_waitcnt vmcnt(19)\n"
  "v_mfma_f32_16x16x32_bf16 v[136:139], a[24:27], v[68:71], v[136:139]\n"
  "v_mfma_f32_16x16x32_bf16 v[140:143], a[28:31], v[68:71], v[140:143]\n"
  "s_waitcnt lgkmcnt(0)\n"
  "s_barrier\n"
  "ds_read_b64 v[152:153], v5 offset:0\n"
  "ds_read_b64 v[160:161], v5 offset:2048\n"
  "ds_read_b64 v[154:155], v5 offset:4096\n"
  "ds_read_b64 v[162:163], v5 offset:6144\n"
  "ds_read_b64 v[156:157], v5 offset:8192\n"
  "ds_read_b64 v[164:165], v5 offset:10240\n"
  "ds_read_b64 v[158:159], v5 offset:12288\n"
  "ds_read_b64 v[166:167], v5 offset:14336\n"
  "global_load_dwordx4 v[68:71], v0, s[60:61] offset:2048 sc1\n"
  "s_waitcnt vmcnt(19)\n"
  "v_mfma_f32_16x16x32_bf16 v[136:139], a[32:35], v[72:75], v[136:139]\n"
  "v_mfma_f32_16x16x32_bf16 v[140:143], a[36:39], v[72:75], v[140:143]\n"
  "global_load_dwordx4 v[72:75], v0, s[60:61] offset:3072 sc1\n"
  "s_add_u32 s60, s60, 8192\n"
  "s_addc_u32 s61, s61, 0\n"
  "s_waitcnt vmcnt(19)\n"
  "v_mfma_f32_16x16x32_bf16 v[136:139], a[40:43], v[76:79], v[136:139]\n"
  "v_mfma_f32_16x16x32_bf16 v[140:143], a[44:47], v[76:79], v[140:143]\n"
  "v_xor_b32 v5, 0x4000, v5\n"
  "v_sub_f32 v7, 1.0, v168\n"
  "v_cvt_pk_bf16_f32 v7, v7, v7\n"
  "global_load_dwordx4 v[76:79], v0, s[60:61] offset:0 sc1\n"
  "s_waitcnt vmcnt(19)\n"
  "v_mfma_f32_16x16x32_bf16 v[136:139], a[48:51], v[80:83], v[136:139]\n"
  "v_mfma_f32_16x16x32_bf16 v[140:143], a[52:55], v[80:83], v[140:143]\n"
  "v_lshlrev_b32 v7, 16, v7\n"
  "s_waitcnt lgkmcnt(5)\n"
  "v_add_f32 v152, v152, v154\n"
  "s_waitcnt lgkmcnt(1)\n"
  "v_add_f32 v156, v156, v158\n"
  "global_load_dwordx4 v[80:83], v0, s[60:61] offset:1024 sc1\n"
  "s_waitcnt vmcnt(19)\n"
  "v_mfma_f32_16x16x32_bf16 v[136:139], a[56:59], v[84:87], v[136:139]\n"
  "v_mfma_f32_16x16x32_bf16 v[140:143], a[60:63], v[84:87], v[140:143]\n"
  "v_add_f32 v152, v152, v156\n"
  "v_add_f32 v153, v153, v155\n"
  "v_add_f32 v157, v157, v159\n"
  "global_load_dwordx4 v[84:87], v0, s[60:61] offset:2048 sc1\n"
  "s_waitcnt vmcnt(19)\n"
  "v_mfma_f32_16x16x32_bf16 v[136:139], a[64:67], v[88:91], v[136:139]\n"
  "v_mfma_f32_16x16x32_bf16 v[140:143], a[68:71], v[88:91], v[140:143]\n"
  "v_add_f32 v153, v153, v157\n"
  "v_cvt_pk_bf16_f32 v152, v152, v153\n"
  "v_and_b32 v153, 0xffff0000, v152\n"
  "global_load_dwordx4 v[88:91], v0, s[60:61] offset:3072 sc1\n"
  "s_add_u32 s60, s60, 8192\n"
  "s_addc_u32 s61, s61, 0\n"
  "s_waitcnt vmcnt(19)\n"
  "v_mfma_f32_16x16x32_bf16 v[136:139], a[72:75], v[92:95], v[136:139]\n"
  "v_mfma_f32_16x16x32_bf16 v[140:143], a[76:79], v[92:95], v[140:143]\n"
  "v_lshlrev_b32 v152, 16, v152\n"
  "v_mul_f32 v152, v7, v152\n"
  "v_mul_f32 v153, v7, v153\n"
  "global_load_dwordx4 v[92:95], v0, s[60:61] offset:0 sc1\n"
  "s_waitcnt vmcnt(19)\n"
  "v_mfma_f32_16x16x32_bf16 v[136:139], a[80:83], v[96:99], v[136:139]\n"
  "v_mfma_f32_16x16x32_bf16 v[140:143], a[84:87], v[96:99], v[140:143]\n"
  "v_cvt_pk_bf16_f32 v152, v152, v153\n"
  "v_and_b32 v153, 0xffff0000, v152\n"
  "v_lshlrev_b32 v152, 16, v152\n"
  "global_load_dwordx4 v[96:99], v0, s[60:61] offset:1024 sc1\n"
  "s_waitcnt vmcnt(19)\n"
  "v_mfma_f32_16x16x32_bf16 v[136:139], a[88:91], v[100:103], v[136:139]\n"
  "v_mfma_f32_16x16x32_bf16 v[140:143], a[92:95], v[100:103], v[140:143]\n"
  "v_add_f32 v160, v160, v162\n"
  "s_waitcnt lgkmcnt(0)\n"
  "v_add_f32 v164, v164, v166\n"
  "v_add_f32 v160, v160, v164\n"
  "global_load_dwordx4 v[100:103], v0, s[60:61] offset:2048 sc1\n"
  "s_waitcnt vmcnt(19)\n"
  "v_mfma_f32_16x16x32_bf16 v[136:139], a[96:99], v[104:107], v[136:139]\n"
  "v_mfma_f32_16x16x32_bf16 v[140:143], a[100:103], v[104:107], v[140:143]\n"
  "v_add_f32 v161, v161, v163\n"
  "v_add_f32 v165, v165, v167\n"
  "v_add_f32 v161, v161, v165\n"
  "global_load_dwordx4 v[104:107], v0, s[60:61] offset:3072 sc1\n"
  "s_add_u32 s60, s60, 204800\n"
  "s_addc_u32 s61, s61, 0\n"
  "s_waitcnt vmcnt(19)\n"
  "v_mfma_f32_16x16x32_bf16 v[136:139], a[104:107], v[108:111], v[136:139]\n"
  "v_mfma_f32_16x16x32_bf16 v[140:143], a[108:111], v[108:111], v[140:143]\n"
  "v_cvt_pk_bf16_f32 v160, v160, v161\n"
  "v_and_b32 v161, 0xffff0000, v160\n"
  "v_lshlrev_b32 v160, 16, v160\n"
  "global_load_dwordx4 v[108:111], v0, s[62:63] offset:-2048 sc1\n"
  "s_waitcnt vmcnt(19)\n"
  "v_mfma_f32_16x16x32_bf16 v[136:139], a[112:115], v[112:115], v[136:139]\n"
  "v_mfma_f32_16x16x32_bf16 v[140:143], a[116:119], v[112:115], v[140:143]\n"
  "v_mul_f32 v160, v168, v160\n"
  "v_mul_f32 v161, v168, v161\n"
  "v_cvt_pk_bf16_f32 v160, v160, v161\n"
  "global_load_dwordx4 v[112:115], v0, s[62:63] offset:-1024 sc1\n"
  "s_waitcnt vmcnt(19)\n"
  "v_mfma_f32_16x16x32_bf16 v[136:139], a[120:123], v[116:119], v[136:139]\n"
  "v_mfma_f32_16x16x32_bf16 v[140:143], a[124:127], v[116:119], v[140:143]\n"
  "v_and_b32 v161, 0xffff0000, v160\n"
  "v_lshlrev_b32 v160, 16, v160\n"
  "v_add_f32 v152, v152, v160\n"
  "global_load_dwordx4 v[116:119], v0, s[62:63] offset:0 sc1\n"
  "s_waitcnt vmcnt(19)\n"
  "v_mfma_f32_16x16x32_bf16 v[136:139], a[128:131], v[120:123], v[136:139]\n"
  "v_mfma_f32_16x16x32_bf16 v[140:143], a[132:135], v[120:123], v[140:143]\n"
  "v_add_f32 v153, v153, v161\n"
  "v_cvt_pk_bf16_f32 v152, v152, v153\n"
  "global_load_dwordx4 v[120:123], v0, s[62:63] offset:1024 sc1\n"
  "s_waitcnt vmcnt(19)\n"
  "v_mfma_f32_16x16x32_bf16 v[136:139], a[136:139], v[124:127], v[136:139]\n"
  "v_mfma_f32_16x16x32_bf16 v[140:143], a[140:143], v[124:127], v[140:143]\n"
  "global_load_dwordx4 v[124:127], v0, s[62:63] offset:2048 sc1\n"
  "s_add_u32 s62, s62, 40960\n"
  "s_addc_u32 s63, s63, 0\n"
  "s_waitcnt vmcnt(19)\n"
  "v_mfma_f32_16x16x32_bf16 v[136:139], a[144:147], v[128:131], v[136:139]\n"
  "v_mfma_f32_16x16x32_bf16 v[140:143], a[148:151], v[128:131], v[140:143]\n"
  "global_store_dword v6, v152, s[40:41]\n"
  "s_add_u32 s40, s40, 36864\n"
  "s_addc_u32 s41, s41, 0\n"
  "global_load_dword v128, v4, s[64:65] sc1\n"
  "s_add_u32 s64, s64, 2048\n"
  "s_addc_u32 s65, s65, 0\n"
  "s_add_u32 s74, s56, 2\n"
  "s_waitcnt vmcnt(20)\n"
  "v_cmp_gt_u32 vcc, s74, v132\n"
  "s_branch OMID\n"
  "ODRAIN:\n"
  "s_waitcnt vmcnt(18)\n"
  "v_mfma_f32_16x16x32_bf16 v[136:139], a[152:155], v[56:59], v[136:139]\n"
  "v_mfma_f32_16x16x32_bf16 v[140:143], a[156:159], v[56:59], v[140:143]\n"
  "s_waitcnt vmcnt(17)\n"
  "v_mfma_f32_16x16x32_bf16 v[136:139], a[160:163], v[60:63], v[136:139]\n"
  "v_mfma_f32_16x16x32_bf16 v[140:143], a[164:167], v[60:63], v[140:143]\n"
  "s_waitcnt vmcnt(16)\n"
  "v_mfma_f32_16x16x32_bf16 v[136:139], a[168:171], v[64:67], v[136:139]\n"
  "v_mfma_f32_16x16x32_bf16 v[140:143], a[172:175], v[64:67], v[140:143]\n"
  "s_waitcnt vmcnt(15)\n"
  "v_mfma_f32_16x16x32_bf16 v[136:139], a[176:179], v[68:71], v[136:139]\n"
  "v_mfma_f32_16x16x32_bf16 v[140:143], a[180:183], v[68:71], v[140:143]\n"
  "s_waitcnt vmcnt(14)\n"
  "v_mfma_f32_16x16x32_bf16 v[136:139], a[184:187], v[72:75], v[136:139]\n"
  "v_mfma_f32_16x16x32_bf16 v[140:143], a[188:191], v[72:75], v[140:143]\n"
  "s_waitcnt vmcnt(13)\n"
  "v_mfma_f32_16x16x32_bf16 v[136:139], a[192:195], v[76:79], v[136:139]\n"
  "v_mfma_f32_16x16x32_bf16 v[140:143], a[196:199], v[76:79], v[140:143]\n"
  "s_waitcnt vmcnt(12)\n"
  "v_mfma_f32_16x16x32_bf16 v[136:139], a[200:203], v[80:83], v[136:139]\n"
  "v_mfma_f32_16x16x32_bf16 v[140:143], a[204:207], v[80:83], v[140:143]\n"
  "s_waitcnt vmcnt(11)\n"
  "v_mfma_f32_16x16x32_bf16 v[136:139], a[208:211], v[84:87], v[136:139]\n"
  "v_mfma_f32_16x16x32_bf16 v[140:143], a[212:215], v[84:87], v[140:143]\n"
  "s_waitcnt vmcnt(10)\n"
  "v_mfma_f32_16x16x32_bf16 v[136:139], a[216:219], v[88:91], v[136:139]\n"
  "v_mfma_f32_16x16x32_bf16 v[140:143], a[220:223], v[88:91], v[140:143]\n"
  "s_waitcnt vmcnt(9)\n"
  "v_mfma_f32_16x16x32_bf16 v[136:139], a[224:227], v[92:95], v[136:139]\n"
  "v_mfma_f32_16x16x32_bf16 v[140:143], a[228:231], v[92:95], v[140:143]\n"
  "s_waitcnt vmcnt(8)\n"
  "v_mfma_f32_16x16x32_bf16 v[136:139], a[232:235], v[96:99], v[136:139]\n"
  "v_mfma_f32_16x16x32_bf16 v[140:143], a[236:239], v[96:99], v[140:143]\n"
  "s_waitcnt vmcnt(7)\n"
  "v_mfma_f32_16x16x32_bf16 v[136:139], a[240:243], v[100:103], v[136:139]\n"
  "v_mfma_f32_16x16x32_bf16 v[140:143], a[244:247], v[100:103], v[140:143]\n"
  "s_waitcnt vmcnt(6)\n"
  "v_mfma_f32_16x16x32_bf16 v[136:139], a[248:251], v[104:107], v[136:139]\n"
  "v_mfma_f32_16x16x32_bf16 v[140:143], a[252:255], v[104:107], v[140:143]\n"
  "s_waitcnt vmcnt(5)\n"
  "v_mfma_f32_16x16x32_bf16 v[144:147], v[16:19], v[108:111], 0\n"
  "v_mfma_f32_16x16x32_bf16 v[148:151], v[20:23], v[108:111], 0\n"
  "s_waitcnt vmcnt(4)\n"
  "v_mfma_f32_16x16x32_bf16 v[144:147], v[24:27], v[112:115], v[144:147]\n"
  "v_mfma_f32_16x16x32_bf16 v[148:151], v[28:31], v[112:115], v[148:151]\n"
  "s_waitcnt vmcnt(3)\n"
  "v_mfma_f32_16x16x32_bf16 v[144:147], v[32:35], v[116:119], v[144:147]\n"
  "v_mfma_f32_16x16x32_bf16 v[148:151], v[36:39], v[116:119], v[148:151]\n"
  "s_waitcnt vmcnt(2)\n"
  "v_mfma_f32_16x16x32_bf16 v[144:147], v[40:43], v[120:123], v[144:147]\n"
  "v_mfma_f32_16x16x32_bf16 v[148:151], v[44:47], v[120:123], v[148:151]\n"
  "s_waitcnt vmcnt(1)\n"
  "v_mfma_f32_16x16x32_bf16 v[144:147], v[48:51], v[124:127], v[144:147]\n"
  "v_mfma_f32_16x16x32_bf16 v[148:151], v[52:55], v[124:127], v[148:151]\n"
  "s_waitcnt vmcnt(0)\n"
  "v_mov_b32 v168, v128\n"
  "ds_write_b128 v2, v[136:139] offset:0\n"
  "ds_write_b128 v2, v[140:143] offset:1024\n"
  "s_nop 0\n"
  "ds_write_b128 v2, v[144:147] offset:2048\n"
  "s_nop 1\n"
  "ds_write_b128 v2, v[148:151] offset:3072\n"
  "v_xor_b32 v2, 0x4000, v2\n"
  "s_add_u32 s56, s56, 1\n"
  "s_waitcnt lgkmcnt(0)\n"
  "s_barrier\n"
  "ds_read_b64 v[152:153], v5 offset:0\n"
  "ds_read_b64 v[160:161], v5 offset:2048\n"
  "ds_read_b64 v[154:155], v5 offset:4096\n"
  "ds_read_b64 v[162:163], v5 offset:6144\n"
  "ds_read_b64 v[156:157], v5 offset:8192\n"
  "ds_read_b64 v[164:165], v5 offset:10240\n"
  "ds_read_b64 v[158:159], v5 offset:12288\n"
  "ds_read_b64 v[166:167], v5 offset:14336\n"
  "v_xor_b32 v5, 0x4000, v5\n"
  "v_sub_f32 v7, 1.0, v168\n"
  "v_cvt_pk_bf16_f32 v7, v7, v7\n"
  "v_lshlrev_b32 v7, 16, v7\n"
  "s_waitcnt lgkmcnt(5)\n"
  "v_add_f32 v152, v152, v154\n"
  "s_waitcnt lgkmcnt(1)\n"
  "v_add_f32 v156, v156, v158\n"
  "v_add_f32 v152, v152, v156\n"
  "v_add_f32 v153, v153, v155\n"
  "v_add_f32 v157, v157, v159\n"
  "v_add_f32 v153, v153, v157\n"
  "v_cvt_pk_bf16_f32 v152, v152, v153\n"
  "v_and_b32 v153, 0xffff0000, v152\n"
  "v_lshlrev_b32 v152, 16, v152\n"
  "v_mul_f32 v152, v7, v152\n"
  "v_mul_f32 v153, v7, v153\n"
  "v_cvt_pk_bf16_f32 v152, v152, v153\n"
  "v_and_b32 v153, 0xffff0000, v152\n"
  "v_lshlrev_b32 v152, 16, v152\n"
  "v_add_f32 v160, v160, v162\n"
  "s_waitcnt lgkmcnt(0)\n"
  "v_add_f32 v164, v164, v166\n"
  "v_add_f32 v160, v160, v164\n"
  "v_add_f32 v161, v161, v163\n"
  "v_add_f32 v165, v165, v167\n"
  "v_add_f32 v161, v161, v165\n"
  "v_cvt_pk_bf16_f32 v160, v160, v161\n"
  "v_and_b32 v161, 0xffff0000, v160\n"
  "v_lshlrev_b32 v160, 16, v160\n"
  "v_mul_f32 v160, v168, v160\n"
  "v_mul_f32 v161, v168, v161\n"
  "v_cvt_pk_bf16_f32 v160, v160, v161\n"
  "v_and_b32 v161, 0xffff0000, v160\n"
  "v_lshlrev_b32 v160, 16, v160\n"
  "v_add_f32 v152, v152, v160\n"
  "v_add_f32 v153, v153, v161\n"
  "v_cvt_pk_bf16_f32 v152, v152, v153\n"
  "global_store_dword v6, v152, s[40:41]\n"
  "s_add_u32 s40, s40, 36864\n"
  "s_addc_u32 s41, s41, 0\n"
  "s_cmp_lt_u32 s56, 64\n"
  "s_cbranch_scc1 OPOLL\n"
  "OQUIT:\n"
  "s_endpgm\n"
      : "+{v[0:15]}"(vin), "+{v[16:47]}"(wa), "+{v[48:55]}"(wb), "+{s[36:43]}"(sa), "+{s[44:59]}"(sb) :: CLOB);
  } else if (fd == 1 || tk < 31) {
    const int c = fd == 1 ? tk - 18 : tk - 15, grp = min(lane / 10, 5), pos = lane % 10;
    const bool real = lane < 60 && pos < 9;
    const u16* src = p.wqkv + (size_t)wave * 1728 + (grp >> 1) * DM + 36 * c + (grp & 1) * 18 + 2 * min(pos, 8);
#pragma unroll 1
    for (int b = 0; b < 2; ++b) {
      unsigned v[72];
#pragma unroll
      for (int i = 0; i < 72; ++i) v[i] = *(const gu32*)(src + (size_t)(b * 72 + i) * 4 * 1728);
#pragma unroll
      for (int i = 0; i < 72; ++i) if (lane != 60) *(LAS unsigned*)(lds + ((b * 72 + i) * 4 + wave) * 264 + lane * 4) = real ? v[i] : 0u;
    }
#pragma unroll
    for (int i = 0; i < 3; ++i) { const int r = min(tid + 256 * i, 575); *(LAS unsigned*)(lds + r * 264 + 240) = p.gate[r]; }
    wg_barrier();
    const int pt = tid & 3, hh = (tid >> 2) & 1, tok = tid >> 3;
    constexpr int RS = 304, OI = 64 * RS;
    vin[0] = (8 * g + qq) * 264 + wave * 64 + c4 * 8; vin[1] = x_src(wave, lane); vin[2] = x_frag(lane);
    vin[3] = OI + tok * 80 + hh * 40 + pt * 8; vin[4] = OI + (8 * wave + (lane & 7)) * 80 + min(lane >> 3, 4) * 16;
    vin[5] = (wave >> 1) * 20480 + (lane >> 3) * 256 + ((wave & 1) * 8 + (lane & 7)) * 16; vin[6] = (lane & 31) * RS + 288;
#pragma unroll
    for (int t = 0; t < 2; ++t) {
      const int g4 = 8 * wave + 4 * t + g;
      vin[10 + t] = n * RS + 48 * (g4 / 5) + 8 * (g4 % 5);
      vin[13 + t] = ((tok - 2 * (2 * pt + t)) & 63) * RS + 96 + 48 * hh;
    }
    vin[12] = tok * RS + 48 * hh;
    PTR(sa, 2, p.x); PTR(sa, 4, __ballot(tok >= 4 * pt)); PTR(sa, 6, __ballot(tok >= 4 * pt + 2));
    sb[0] = WO_OA + c * 1280; sb[1] = WO_G + c * 128; sb[2] = C_AP + c * 128; sb[3] = wave; sb[4] = WO_ST + (role * 4 + wave) * 4096;
    UNIFORM(sa, 8); UNIFORM(sb, 16);
    asm volatile(
  "s_nop 4\n"
  "s_lshl_b32 s80, s47, 10\n"
  "v_add_u32 v7, 0, v0\n"
  "ds_read_b64_tr_b16 a[0:1], v7 offset:0\n"
  "ds_read_b64_tr_b16 a[2:3], v7 offset:1056\n"
  "ds_read_b64_tr_b16 a[4:5], v7 offset:8448\n"
  "ds_read_b64_tr_b16 a[6:7], v7 offset:9504\n"
  "ds_read_b64_tr_b16 a[8:9], v7 offset:16896\n"
  "ds_read_b64_tr_b16 a[10:11], v7 offset:17952\n"
  "ds_read_b64_tr_b16 a[12:13], v7 offset:25344\n"
  "ds_read_b64_tr_b16 a[14:15], v7 offset:26400\n"
  "ds_read_b64_tr_b16 a[16:17], v7 offset:33792\n"
  "ds_read_b64_tr_b16 a[18:19], v7 offset:34848\n"
  "ds_read_b64_tr_b16 a[20:21], v7 offset:42240\n"
  "ds_read_b64_tr_b16 a[22:23], v7 offset:43296\n"
  "v_add_u32 v7, 32, v0\n"
  "ds_read_b64_tr_b16 a[72:73], v7 offset:0\n"
  "ds_read_b64_tr_b16 a[74:75], v7 offset:1056\n"
  "ds_read_b64_tr_b16 a[76:77], v7 offset:8448\n"
  "ds_read_b64_tr_b16 a[78:79], v7 offset:9504\n"
  "ds_read_b64_tr_b16 a[80:81], v7 offset:16896\n"
  "ds_read_b64_tr_b16 a[82:83], v7 offset:17952\n"
  "ds_read_b64_tr_b16 a[84:85], v7 offset:25344\n"
  "ds_read_b64_tr_b16 a[86:87], v7 offset:26400\n"
  "ds_read_b64_tr_b16 a[88:89], v7 offset:33792\n"
  "ds_read_b64_tr_b16 a[90:91], v7 offset:34848\n"
  "ds_read_b64_tr_b16 a[92:93], v7 offset:42240\n"
  "ds_read_b64_tr_b16 a[94:95], v7 offset:43296\n"
  "v_add_u32 v7, 50688, v0\n"
  "ds_read_b64_tr_b16 a[24:25], v7 offset:0\n"
  "ds_read_b64_tr_b16 a[26:27], v7 offset:1056\n"
  "ds_read_b64_tr_b16 a[28:29], v7 offset:8448\n"
  "ds_read_b64_tr_b16 a[30:31], v7 offset:9504\n"
  "ds_read_b64_tr_b16 a[32:33], v7 offset:16896\n"
  "ds_read_b64_tr_b16 a[34:35], v7 offset:17952\n"
  "ds_read_b64_tr_b16 a[36:37], v7 offset:25344\n"
  "ds_read_b64_tr_b16 a[38:39], v7 offset:26400\n"
  "ds_read_b64_tr_b16 a[40:41], v7 offset:33792\n"
  "ds_read_b64_tr_b16 a[42:43], v7 offset:34848\n"
  "ds_read_b64_tr_b16 a[44:45], v7 offset:42240\n"
  "ds_read_b64_tr_b16 a[46:47], v7 offset:43296\n"
  "v_add_u32 v7, 50720, v0\n"
  "ds_read_b64_tr_b16 a[96:97], v7 offset:0\n"
  "ds_read_b64_tr_b16 a[98:99], v7 offset:1056\n"
  "ds_read_b64_tr_b16 a[100:101], v7 offset:8448\n"
  "ds_read_b64_tr_b16 a[102:103], v7 offset:9504\n"
  "ds_read_b64_tr_b16 a[104:105], v7 offset:16896\n"
  "ds_read_b64_tr_b16 a[106:107], v7 offset:17952\n"
  "ds_read_b64_tr_b16 a[108:109], v7 offset:25344\n"
  "ds_read_b64_tr_b16 a[110:111], v7 offset:26400\n"
  "ds_read_b64_tr_b16 a[112:113], v7 offset:33792\n"
  "ds_read_b64_tr_b16 a[114:115], v7 offset:34848\n"
  "ds_read_b64_tr_b16 a[116:117], v7 offset:42240\n"
  "ds_read_b64_tr_b16 a[118:119], v7 offset:43296\n"
  "v_add_u32 v7, 101376, v0\n"
  "ds_read_b64_tr_b16 a[48:49], v7 offset:0\n"
  "ds_read_b64_tr_b16 a[50:51], v7 offset:1056\n"
  "ds_read_b64_tr_b16 a[52:53], v7 offset:8448\n"
  "ds_read_b64_tr_b16 a[54:55], v7 offset:9504\n"
  "ds_read_b64_tr_b16 a[56:57], v7 offset:16896\n"
  "ds_read_b64_tr_b16 a[58:59], v7 offset:17952\n"
  "ds_read_b64_tr_b16 a[60:61], v7 offset:25344\n"
  "ds_read_b64_tr_b16 a[62:63], v7 offset:26400\n"
  "ds_read_b64_tr_b16 a[64:65], v7 offset:33792\n"
  "ds_read_b64_tr_b16 a[66:67], v7 offset:34848\n"
  "ds_read_b64_tr_b16 a[68:69], v7 offset:42240\n"
  "ds_read_b64_tr_b16 a[70:71], v7 offset:43296\n"
  "v_add_u32 v7, 101408, v0\n"
  "ds_read_b64_tr_b16 a[120:121], v7 offset:0\n"
  "ds_read_b64_tr_b16 a[122:123], v7 offset:1056\n"
  "ds_read_b64_tr_b16 a[124:125], v7 offset:8448\n"
  "ds_read_b64_tr_b16 a[126:127], v7 offset:9504\n"
  "ds_read_b64_tr_b16 a[128:129], v7 offset:16896\n"
  "ds_read_b64_tr_b16 a[130:131], v7 offset:17952\n"
  "ds_read_b64_tr_b16 a[132:133], v7 offset:25344\n"
  "ds_read_b64_tr_b16 a[134:135], v7 offset:26400\n"
  "ds_read_b64_tr_b16 a[136:137], v7 offset:33792\n"
  "ds_read_b64_tr_b16 a[138:139], v7 offset:34848\n"
  "ds_read_b64_tr_b16 a[140:141], v7 offset:42240\n"
  "ds_read_b64_tr_b16 a[142:143], v7 offset:43296\n"
  "s_waitcnt lgkmcnt(0)\n"
  "s_barrier\n"
  "s_add_u32 s58, s80, 22016\n"
  "s_add_u32 m0, s58, 0\n"
  "s_nop 0\n"
  "global_load_lds_dwordx4 v1, s[38:39] offset:0\n"
  "s_add_u32 m0, s58, 3968\n"
  "s_nop 0\n"
  "global_load_lds_dwordx4 v1, s[38:39] offset:128\n"
  "s_add_u32 m0, s58, 7936\n"
  "s_nop 0\n"
  "global_load_lds_dwordx4 v1, s[38:39] offset:256\n"
  "s_add_u32 m0, s58, 11904\n"
  "s_nop 0\n"
  "global_load_lds_dwordx4 v1, s[38:39] offset:384\n"
  "s_add_u32 m0, s58, 15872\n"
  "s_nop 0\n"
  "global_load_lds_dwordx4 v1, s[38:39] offset:512\n"
  "s_add_u32 m0, s58, 19840\n"
  "s_nop 0\n"
  "global_load_lds_dwordx4 v1, s[38:39] offset:640\n"
  "s_add_u32 m0, s58, 23808\n"
  "s_nop 0\n"
  "global_load_lds_dwordx4 v1, s[38:39] offset:768\n"
  "s_add_u32 m0, s58, 27776\n"
  "s_nop 0\n"
  "global_load_lds_dwordx4 v1, s[38:39] offset:896\n"
  "s_add_u32 m0, s58, 31744\n"
  "s_nop 0\n"
  "global_load_lds_dwordx4 v1, s[38:39] offset:1024\n"
  "v_mbcnt_lo_u32_b32 v157, -1, 0\n"
  "v_mbcnt_hi_u32_b32 v157, -1, v157\n"
  "v_lshlrev_b32 v7, 4, v157\n"
  "v_add_u32 v7, s80, v7\n"
  "v_lshlrev_b32 v157, 2, v157\n"
  "v_mov_b64 v[152:153], 0\n"
  "v_mov_b64 v[154:155], 0\n"
  "v_mov_b32 v158, 0xce6e6b28\n"
  "ds_write_b128 v7, v[152:155] offset:9728\n"
  "ds_write_b128 v7, v[152:155] offset:13824\n"
  "ds_write_b128 v7, v[152:155] offset:17920\n"
  "v_cmp_gt_u32 vcc, 9728, v13\n"
  "v_mov_b32 v7, 4294957568\n"
  "v_mov_b32 v8, 9728\n"
  "v_cndmask_b32 v7, v7, v8, vcc\n"
  "v_add_u32 v64, v13, v7\n"
  "v_cmp_gt_u32 vcc, 9728, v14\n"
  "v_mov_b32 v7, 4294957568\n"
  "v_mov_b32 v8, 9728\n"
  "v_cndmask_b32 v7, v7, v8, vcc\n"
  "v_add_u32 v65, v14, v7\n"
  "s_add_u32 s60, s36, s44\n"
  "s_addc_u32 s61, s37, 0\n"
  "s_add_u32 s62, s36, s45\n"
  "s_addc_u32 s63, s37, 0\n"
  "s_add_u32 s64, s36, s46\n"
  "s_addc_u32 s65, s37, 0\n"
  "s_mov_b32 s56, 0\n"
  "s_mov_b32 s66, 0x05040100\n"
  "s_mov_b32 s67, 0x07060302\n"
  "s_cmp_eq_u32 s47, 3\n"
  "s_cselect_b32 s68, -1, 0\n"
  "s_mov_b32 s69, 0\n"
  "s_mov_b32 s8, 286331153\n"
  "s_mov_b32 s9, 286331153\n"
  "s_mov_b32 s10, 572662306\n"
  "s_mov_b32 s11, 572662306\n"
  "s_mov_b32 s12, 1145324612\n"
  "s_mov_b32 s13, 1145324612\n"
  "s_mov_b32 s14, 2290649224\n"
  "s_mov_b32 s15, 2290649224\n"
  "s_waitcnt vmcnt(0)\n"
  "s_mov_b32 s56, -1\n"
  "s_waitcnt vmcnt(3) lgkmcnt(0)\n"
  "s_barrier\n"
  "v_add_u32 v66, 22016, v2\n"
  "v_xor_b32 v67, 64, v66\n"
  "s_add_u32 s58, s80, 58880\n"
  "s_cmp_lt_i32 s56, 62\n"
  "s_cselect_b32 s74, 36864, 0\n"
  "s_add_u32 s38, s38, s74\n"
  "s_addc_u32 s39, s39, 0\n"
  "ds_read_b128 v[16:19], v66 offset:0\n"
  "ds_read_b128 v[20:23], v66 offset:2048\n"
  "ds_read_b128 v[24:27], v67 offset:0\n"
  "ds_read_b128 v[28:31], v67 offset:2048\n"
  "ds_read_b128 v[32:35], v66 offset:4096\n"
  "ds_read_b128 v[36:39], v66 offset:6144\n"
  "s_waitcnt lgkmcnt(5)\n"
  "v_mfma_f32_16x16x32_bf16 v[48:51], a[0:3], v[16:19], 0\n"
  "ds_read_b128 v[40:43], v67 offset:4096\n"
  "s_waitcnt lgkmcnt(5)\n"
  "v_mfma_f32_16x16x32_bf16 v[52:55], a[0:3], v[20:23], 0\n"
  "ds_read_b128 v[44:47], v67 offset:6144\n"
  "v_mfma_f32_16x16x32_bf16 v[56:59], a[72:75], v[16:19], 0\n"
  "s_add_u32 m0, s58, 0\n"
  "s_nop 0\n"
  "global_load_lds_dwordx4 v1, s[38:39] offset:0\n"
  "v_mfma_f32_16x16x32_bf16 v[60:63], a[72:75], v[20:23], 0\n"
  "s_waitcnt lgkmcnt(5)\n"
  "v_mfma_f32_16x16x32_bf16 v[48:51], a[4:7], v[24:27], v[48:51]\n"
  "ds_read_b128 v[16:19], v66 offset:8192\n"
  "s_waitcnt lgkmcnt(5)\n"
  "v_mfma_f32_16x16x32_bf16 v[52:55], a[4:7], v[28:31], v[52:55]\n"
  "ds_read_b128 v[20:23], v66 offset:10240\n"
  "v_mfma_f32_16x16x32_bf16 v[56:59], a[76:79], v[24:27], v[56:59]\n"
  "s_add_u32 m0, s58, 3968\n"
  "s_nop 0\n"
  "global_load_lds_dwordx4 v1, s[38:39] offset:128\n"
  "v_mfma_f32_16x16x32_bf16 v[60:63], a[76:79], v[28:31], v[60:63]\n"
  "s_waitcnt lgkmcnt(5)\n"
  "v_mfma_f32_16x16x32_bf16 v[48:51], a[8:11], v[32:35], v[48:51]\n"
  "ds_read_b128 v[24:27], v67 offset:8192\n"
  "s_waitcnt lgkmcnt(5)\n"
  "v_mfma_f32_16x16x32_bf16 v[52:55], a[8:11], v[36:39], v[52:55]\n"
  "ds_read_b128 v[28:31], v67 offset:10240\n"
  "v_mfma_f32_16x16x32_bf16 v[56:59], a[80:83], v[32:35], v[56:59]\n"
  "s_add_u32 m0, s58, 7936\n"
  "s_nop 0\n"
  "global_load_lds_dwordx4 v1, s[38:39] offset:256\n"
  "v_mfma_f32_16x16x32_bf16 v[60:63], a[80:83], v[36:39], v[60:63]\n"
  "s_waitcnt lgkmcnt(5)\n"
  "v_mfma_f32_16x16x32_bf16 v[48:51], a[12:15], v[40:43], v[48:51]\n"
  "ds_read_b128 v[32:35], v66 offset:12288\n"
  "s_waitcnt lgkmcnt(5)\n"
  "v_mfma_f32_16x16x32_bf16 v[52:55], a[12:15], v[44:47], v[52:55]\n"
  "ds_read_b128 v[36:39], v66 offset:14336\n"
  "v_mfma_f32_16x16x32_bf16 v[56:59], a[84:87], v[40:43], v[56:59]\n"
  "s_add_u32 m0, s58, 11904\n"
  "s_nop 0\n"
  "global_load_lds_dwordx4 v1, s[38:39] offset:384\n"
  "v_mfma_f32_16x16x32_bf16 v[60:63], a[84:87], v[44:47], v[60:63]\n"
  "s_waitcnt lgkmcnt(5)\n"
  "v_mfma_f32_16x16x32_bf16 v[48:51], a[16:19], v[16:19], v[48:51]\n"
  "ds_read_b128 v[40:43], v67 offset:12288\n"
  "s_waitcnt lgkmcnt(5)\n"
  "v_mfma_f32_16x16x32_bf16 v[52:55], a[16:19], v[20:23], v[52:55]\n"
  "ds_read_b128 v[44:47], v67 offset:14336\n"
  "v_mfma_f32_16x16x32_bf16 v[56:59], a[88:91], v[16:19], v[56:59]\n"
  "s_add_u32 m0, s58, 15872\n"
  "s_nop 0\n"
  "global_load_lds_dwordx4 v1, s[38:39] offset:512\n"
  "v_mfma_f32_16x16x32_bf16 v[60:63], a[88:91], v[20:23], v[60:63]\n"
  "s_waitcnt lgkmcnt(5)\n"
  "v_mfma_f32_16x16x32_bf16 v[48:51], a[20:23], v[24:27], v[48:51]\n"
  "ds_read_b128 v[16:19], v66 offset:16384\n"
  "s_waitcnt lgkmcnt(5)\n"
  "v_mfma_f32_16x16x32_bf16 v[52:55], a[20:23], v[28:31], v[52:55]\n"
  "ds_read_b128 v[20:23], v66 offset:18432\n"
  "v_mfma_f32_16x16x32_bf16 v[56:59], a[92:95], v[24:27], v[56:59]\n"
  "s_add_u32 m0, s58, 19840\n"
  "s_nop 0\n"
  "global_load_lds_dwordx4 v1, s[38:39] offset:640\n"
  "v_mfma_f32_16x16x32_bf16 v[60:63], a[92:95], v[28:31], v[60:63]\n"
  "s_waitcnt lgkmcnt(5)\n"
  "v_mfma_f32_16x16x32_bf16 v[48:51], a[24:27], v[32:35], v[48:51]\n"
  "ds_read_b128 v[24:27], v67 offset:16384\n"
  "s_waitcnt lgkmcnt(5)\n"
  "v_mfma_f32_16x16x32_bf16 v[52:55], a[24:27], v[36:39], v[52:55]\n"
  "ds_read_b128 v[28:31], v67 offset:18432\n"
  "v_mfma_f32_16x16x32_bf16 v[56:59], a[96:99], v[32:35], v[56:59]\n"
  "s_add_u32 m0, s58, 23808\n"
  "s_nop 0\n"
  "global_load_lds_dwordx4 v1, s[38:39] offset:768\n"
  "v_mfma_f32_16x16x32_bf16 v[60:63], a[96:99], v[36:39], v[60:63]\n"
  "s_waitcnt lgkmcnt(5)\n"
  "v_mfma_f32_16x16x32_bf16 v[48:51], a[28:31], v[40:43], v[48:51]\n"
  "ds_read_b128 v[32:35], v66 offset:20480\n"
  "s_waitcnt lgkmcnt(5)\n"
  "v_mfma_f32_16x16x32_bf16 v[52:55], a[28:31], v[44:47], v[52:55]\n"
  "ds_read_b128 v[36:39], v66 offset:22528\n"
  "v_mfma_f32_16x16x32_bf16 v[56:59], a[100:103], v[40:43], v[56:59]\n"
  "s_add_u32 m0, s58, 27776\n"
  "s_nop 0\n"
  "global_load_lds_dwordx4 v1, s[38:39] offset:896\n"
  "v_mfma_f32_16x16x32_bf16 v[60:63], a[100:103], v[44:47], v[60:63]\n"
  "s_waitcnt lgkmcnt(5)\n"
  "v_mfma_f32_16x16x32_bf16 v[48:51], a[32:35], v[16:19], v[48:51]\n"
  "ds_read_b128 v[40:43], v67 offset:20480\n"
  "s_waitcnt lgkmcnt(5)\n"
  "v_mfma_f32_16x16x32_bf16 v[52:55], a[32:35], v[20:23], v[52:55]\n"
  "ds_read_b128 v[44:47], v67 offset:22528\n"
  "v_mfma_f32_16x16x32_bf16 v[56:59], a[104:107], v[16:19], v[56:59]\n"
  "s_add_u32 m0, s58, 31744\n"
  "s_nop 0\n"
  "global_load_lds_dwordx4 v1, s[38:39] offset:1024\n"
  "v_mfma_f32_16x16x32_bf16 v[60:63], a[104:107], v[20:23], v[60:63]\n"
  "s_waitcnt lgkmcnt(5)\n"
  "v_mfma_f32_16x16x32_bf16 v[48:51], a[36:39], v[24:27], v[48:51]\n"
  "ds_read_b128 v[16:19], v66 offset:24576\n"
  "s_waitcnt lgkmcnt(5)\n"
  "v_mfma_f32_16x16x32_bf16 v[52:55], a[36:39], v[28:31], v[52:55]\n"
  "ds_read_b128 v[20:23], v66 offset:26624\n"
  "v_mfma_f32_16x16x32_bf16 v[56:59], a[108:111], v[24:27], v[56:59]\n"
  "v_mfma_f32_16x16x32_bf16 v[60:63], a[108:111], v[28:31], v[60:63]\n"
  "s_waitcnt lgkmcnt(5)\n"
  "v_mfma_f32_16x16x32_bf16 v[48:51], a[40:43], v[32:35], v[48:51]\n"
  "ds_read_b128 v[24:27], v67 offset:24576\n"
  "s_waitcnt lgkmcnt(5)\n"
  "v_mfma_f32_16x16x32_bf16 v[52:55], a[40:43], v[36:39], v[52:55]\n"
  "ds_read_b128 v[28:31], v67 offset:26624\n"
  "v_mfma_f32_16x16x32_bf16 v[56:59], a[112:115], v[32:35], v[56:59]\n"
  "v_mfma_f32_16x16x32_bf16 v[60:63], a[112:115], v[36:39], v[60:63]\n"
  "s_waitcnt lgkmcnt(5)\n"
  "v_mfma_f32_16x16x32_bf16 v[48:51], a[44:47], v[40:43], v[48:51]\n"
  "ds_read_b128 v[32:35], v66 offset:28672\n"
  "s_waitcnt lgkmcnt(5)\n"
  "v_mfma_f32_16x16x32_bf16 v[52:55], a[44:47], v[44:47], v[52:55]\n"
  "ds_read_b128 v[36:39], v66 offset:30720\n"
  "v_mfma_f32_16x16x32_bf16 v[56:59], a[116:119], v[40:43], v[56:59]\n"
  "v_mfma_f32_16x16x32_bf16 v[60:63], a[116:119], v[44:47], v[60:63]\n"
  "s_waitcnt lgkmcnt(5)\n"
  "v_mfma_f32_16x16x32_bf16 v[48:51], a[48:51], v[16:19], v[48:51]\n"
  "ds_read_b128 v[40:43], v67 offset:28672\n"
  "s_waitcnt lgkmcnt(5)\n"
  "v_mfma_f32_16x16x32_bf16 v[52:55], a[48:51], v[20:23], v[52:55]\n"
  "ds_read_b128 v[44:47], v67 offset:30720\n"
  "v_mfma_f32_16x16x32_bf16 v[56:59], a[120:123], v[16:19], v[56:59]\n"
  "v_mfma_f32_16x16x32_bf16 v[60:63], a[120:123], v[20:23], v[60:63]\n"
  "s_waitcnt lgkmcnt(5)\n"
  "v_mfma_f32_16x16x32_bf16 v[48:51], a[52:55], v[24:27], v[48:51]\n"
  "ds_read_b128 v[16:19], v66 offset:32768\n"
  "s_waitcnt lgkmcnt(5)\n"
  "v_mfma_f32_16x16x32_bf16 v[52:55], a[52:55], v[28:31], v[52:55]\n"
  "ds_read_b128 v[20:23], v66 offset:34816\n"
  "v_mfma_f32_16x16x32_bf16 v[56:59], a[124:127], v[24:27], v[56:59]\n"
  "v_mfma_f32_16x16x32_bf16 v[60:63], a[124:127], v[28:31], v[60:63]\n"
  "s_waitcnt lgkmcnt(5)\n"
  "v_mfma_f32_16x16x32_bf16 v[48:51], a[56:59], v[32:35], v[48:51]\n"
  "ds_read_b128 v[24:27], v67 offset:32768\n"
  "s_waitcnt lgkmcnt(5)\n"
  "v_mfma_f32_16x16x32_bf16 v[52:55], a[56:59], v[36:39], v[52:55]\n"
  "ds_read_b128 v[28:31], v67 offset:34816\n"
  "v_mfma_f32_16x16x32_bf16 v[56:59], a[128:131], v[32:35], v[56:59]\n"
  "v_mfma_f32_16x16x32_bf16 v[60:63], a[128:131], v[36:39], v[60:63]\n"
  "s_waitcnt lgkmcnt(5)\n"
  "v_mfma_f32_16x16x32_bf16 v[48:51], a[60:63], v[40:43], v[48:51]\n"
  "s_waitcnt lgkmcnt(4)\n"
  "v_mfma_f32_16x16x32_bf16 v[52:55], a[60:63], v[44:47], v[52:55]\n"
  "v_mfma_f32_16x16x32_bf16 v[56:59], a[132:135], v[40:43], v[56:59]\n"
  "v_mfma_f32_16x16x32_bf16 v[60:63], a[132:135], v[44:47], v[60:63]\n"
  "s_waitcnt lgkmcnt(3)\n"
  "v_mfma_f32_16x16x32_bf16 v[48:51], a[64:67], v[16:19], v[48:51]\n"
  "s_waitcnt lgkmcnt(2)\n"
  "v_mfma_f32_16x16x32_bf16 v[52:55], a[64:67], v[20:23], v[52:55]\n"
  "v_mfma_f32_16x16x32_bf16 v[56:59], a[136:139], v[16:19], v[56:59]\n"
  "v_mfma_f32_16x16x32_bf16 v[60:63], a[136:139], v[20:23], v[60:63]\n"
  "s_waitcnt lgkmcnt(1)\n"
  "v_mfma_f32_16x16x32_bf16 v[48:51], a[68:71], v[24:27], v[48:51]\n"
  "s_waitcnt lgkmcnt(0)\n"
  "v_mfma_f32_16x16x32_bf16 v[52:55], a[68:71], v[28:31], v[52:55]\n"
  "v_mfma_f32_16x16x32_bf16 v[56:59], a[140:143], v[24:27], v[56:59]\n"
  "v_mfma_f32_16x16x32_bf16 v[60:63], a[140:143], v[28:31], v[60:63]\n"
  "v_cvt_pk_bf16_f32 v48, v48, v49\n"
  "v_cvt_pk_bf16_f32 v49, v50, v51\n"
  "v_cvt_pk_bf16_f32 v52, v52, v53\n"
  "v_cvt_pk_bf16_f32 v53, v54, v55\n"
  "s_nop 0\n"
  "v_cvt_pk_bf16_f32 v56, v56, v57\n"
  "v_cvt_pk_bf16_f32 v57, v58, v59\n"
  "s_nop 0\n"
  "v_cvt_pk_bf16_f32 v60, v60, v61\n"
  "v_cvt_pk_bf16_f32 v61, v62, v63\n"
  "ds_write_b64 v10, v[48:49] offset:0\n"
  "ds_write_b64 v10, v[52:53] offset:4864\n"
  "ds_write_b64 v11, v[56:57] offset:0\n"
  "ds_write_b64 v11, v[60:61] offset:4864\n"
  "s_waitcnt vmcnt(0)\n"
  "s_add_u32 s56, s56, 1\n"
  "ALOOP:\n"
  "s_waitcnt vmcnt(3) lgkmcnt(0)\n"
  "s_barrier\n"
  "v_add_u32 v66, 58880, v2\n"
  "v_xor_b32 v67, 64, v66\n"
  "s_add_u32 s58, s80, 22016\n"
  "s_cmp_lt_i32 s56, 62\n"
  "s_cselect_b32 s74, 36864, 0\n"
  "s_add_u32 s38, s38, s74\n"
  "s_addc_u32 s39, s39, 0\n"
  "ds_read_b128 v[16:19], v66 offset:0\n"
  "ds_read_b128 v[20:23], v66 offset:2048\n"
  "ds_read_b128 v[24:27], v67 offset:0\n"
  "ds_read_b128 v[28:31], v67 offset:2048\n"
  "ds_read_b128 v[32:35], v66 offset:4096\n"
  "ds_read_b128 v[36:39], v66 offset:6144\n"
  "s_waitcnt lgkmcnt(5)\n"
  "v_mfma_f32_16x16x32_bf16 v[48:51], a[0:3], v[16:19], 0\n"
  "ds_read_b128 v[40:43], v67 offset:4096\n"
  "ds_read_b128 v[68:71], v12 offset:0\n"
  "s_nop 0\n"
  "s_waitcnt lgkmcnt(6)\n"
  "v_mfma_f32_16x16x32_bf16 v[52:55], a[0:3], v[20:23], 0\n"
  "ds_read_b128 v[44:47], v67 offset:6144\n"
  "ds_read_b128 v[72:75], v12 offset:16\n"
  "s_nop 0\n"
  "v_mfma_f32_16x16x32_bf16 v[56:59], a[72:75], v[16:19], 0\n"
  "s_add_u32 m0, s58, 0\n"
  "s_nop 0\n"
  "global_load_lds_dwordx4 v1, s[38:39] offset:0\n"
  "v_mfma_f32_16x16x32_bf16 v[60:63], a[72:75], v[20:23], 0\n"
  "ds_read_b32 v76, v12 offset:32\n"
  "s_nop 0\n"
  "s_waitcnt lgkmcnt(8)\n"
  "v_mfma_f32_16x16x32_bf16 v[48:51], a[4:7], v[24:27], v[48:51]\n"
  "ds_read_b128 v[16:19], v66 offset:8192\n"
  "ds_read_b128 v[80:83], v13 offset:0\n"
  "s_nop 0\n"
  "s_waitcnt lgkmcnt(9)\n"
  "v_mfma_f32_16x16x32_bf16 v[52:55], a[4:7], v[28:31], v[52:55]\n"
  "ds_read_b128 v[20:23], v66 offset:10240\n"
  "ds_read_b128 v[84:87], v13 offset:16\n"
  "s_nop 0\n"
  "v_mfma_f32_16x16x32_bf16 v[56:59], a[76:79], v[24:27], v[56:59]\n"
  "s_add_u32 m0, s58, 3968\n"
  "s_nop 0\n"
  "global_load_lds_dwordx4 v1, s[38:39] offset:128\n"
  "v_mfma_f32_16x16x32_bf16 v[60:63], a[76:79], v[28:31], v[60:63]\n"
  "ds_read_b32 v88, v13 offset:32\n"
  "s_nop 0\n"
  "s_waitcnt lgkmcnt(11)\n"
  "v_mfma_f32_16x16x32_bf16 v[48:51], a[8:11], v[32:35], v[48:51]\n"
  "ds_read_b128 v[24:27], v67 offset:8192\n"
  "ds_read_b128 v[92:95], v14 offset:0\n"
  "s_nop 0\n"
  "s_waitcnt lgkmcnt(12)\n"
  "v_mfma_f32_16x16x32_bf16 v[52:55], a[8:11], v[36:39], v[52:55]\n"
  "ds_read_b128 v[28:31], v67 offset:10240\n"
  "ds_read_b128 v[96:99], v14 offset:16\n"
  "s_nop 0\n"
  "v_mfma_f32_16x16x32_bf16 v[56:59], a[80:83], v[32:35], v[56:59]\n"
  "s_add_u32 m0, s58, 7936\n"
  "s_nop 0\n"
  "global_load_lds_dwordx4 v1, s[38:39] offset:256\n"
  "v_mfma_f32_16x16x32_bf16 v[60:63], a[80:83], v[36:39], v[60:63]\n"
  "ds_read_b32 v100, v14 offset:32\n"
  "s_nop 0\n"
  "s_waitcnt lgkmcnt(14)\n"
  "v_mfma_f32_16x16x32_bf16 v[48:51], a[12:15], v[40:43], v[48:51]\n"
  "ds_read_b128 v[32:35], v66 offset:12288\n"
  "ds_read_b32 v156, v6 offset:0\n"
  "s_nop 0\n"
  "s_waitcnt lgkmcnt(14)\n"
  "v_mfma_f32_16x16x32_bf16 v[52:55], a[12:15], v[44:47], v[52:55]\n"
  "ds_read_b128 v[36:39], v66 offset:14336\n"
  "ds_read_b128 v[104:107], v13 offset:96\n"
  "s_nop 0\n"
  "v_mfma_f32_16x16x32_bf16 v[56:59], a[84:87], v[40:43], v[56:59]\n"
  "s_add_u32 m0, s58, 11904\n"
  "s_nop 0\n"
  "global_load_lds_dwordx4 v1, s[38:39] offset:384\n"
  "v_mfma_f32_16x16x32_bf16 v[60:63], a[84:87], v[44:47], v[60:63]\n"
  "ds_read_b128 v[108:111], v13 offset:112\n"
  "s_nop 0\n"
  "s_waitcnt lgkmcnt(14)\n"
  "v_mfma_f32_16x16x32_bf16 v[48:51], a[16:19], v[16:19], v[48:51]\n"
  "ds_read_b128 v[40:43], v67 offset:12288\n"
  "ds_read_b32 v112, v13 offset:128\n"
  "s_nop 0\n"
  "s_waitcnt lgkmcnt(14)\n"
  "v_mfma_f32_16x16x32_bf16 v[52:55], a[16:19], v[20:23], v[52:55]\n"
  "ds_read_b128 v[44:47], v67 offset:14336\n"
  "ds_read_b128 v[116:119], v14 offset:96\n"
  "s_nop 0\n"
  "v_mfma_f32_16x16x32_bf16 v[56:59], a[88:91], v[16:19], v[56:59]\n"
  "s_add_u32 m0, s58, 15872\n"
  "s_nop 0\n"
  "global_load_lds_dwordx4 v1, s[38:39] offset:512\n"
  "v_mfma_f32_16x16x32_bf16 v[60:63], a[88:91], v[20:23], v[60:63]\n"
  "ds_read_b128 v[120:123], v14 offset:112\n"
  "s_nop 0\n"
  "s_waitcnt lgkmcnt(14)\n"
  "v_mfma_f32_16x16x32_bf16 v[48:51], a[20:23], v[24:27], v[48:51]\n"
  "ds_read_b128 v[16:19], v66 offset:16384\n"
  "ds_read_b32 v124, v14 offset:128\n"
  "s_nop 0\n"
  "s_waitcnt lgkmcnt(14)\n"
  "v_mfma_f32_16x16x32_bf16 v[52:55], a[20:23], v[28:31], v[52:55]\n"
  "ds_read_b128 v[20:23], v66 offset:18432\n"
  "v_dot2_f32_bf16 v128, v68, v80, 0\n"
  "v_dot2_f32_bf16 v129, v68, v92, 0\n"
  "v_mfma_f32_16x16x32_bf16 v[56:59], a[92:95], v[24:27], v[56:59]\n"
  "s_add_u32 m0, s58, 19840\n"
  "s_nop 0\n"
  "global_load_lds_dwordx4 v1, s[38:39] offset:640\n"
  "v_mfma_f32_16x16x32_bf16 v[60:63], a[92:95], v[28:31], v[60:63]\n"
  "v_dot2_f32_bf16 v128, v69, v81, v128\n"
  "v_dot2_f32_bf16 v129, v69, v93, v129\n"
  "s_waitcnt lgkmcnt(12)\n"
  "v_mfma_f32_16x16x32_bf16 v[48:51], a[24:27], v[32:35], v[48:51]\n"
  "ds_read_b128 v[24:27], v67 offset:16384\n"
  "v_dot2_f32_bf16 v128, v70, v82, v128\n"
  "v_dot2_f32_bf16 v129, v70, v94, v129\n"
  "s_waitcnt lgkmcnt(11)\n"
  "v_mfma_f32_16x16x32_bf16 v[52:55], a[24:27], v[36:39], v[52:55]\n"
  "ds_read_b128 v[28:31], v67 offset:18432\n"
  "v_dot2_f32_bf16 v128, v71, v83, v128\n"
  "v_dot2_f32_bf16 v129, v71, v95, v129\n"
  "v_mfma_f32_16x16x32_bf16 v[56:59], a[96:99], v[32:35], v[56:59]\n"
  "s_add_u32 m0, s58, 23808\n"
  "s_nop 0\n"
  "global_load_lds_dwordx4 v1, s[38:39] offset:768\n"
  "v_mfma_f32_16x16x32_bf16 v[60:63], a[96:99], v[36:39], v[60:63]\n"
  "v_dot2_f32_bf16 v128, v72, v84, v128\n"
  "v_dot2_f32_bf16 v129, v72, v96, v129\n"
  "s_waitcnt lgkmcnt(9)\n"
  "v_mfma_f32_16x16x32_bf16 v[48:51], a[28:31], v[40:43], v[48:51]\n"
  "ds_read_b128 v[32:35], v66 offset:20480\n"
  "v_dot2_f32_bf16 v128, v73, v85, v128\n"
  "v_dot2_f32_bf16 v129, v73, v97, v129\n"
  "s_waitcnt lgkmcnt(8)\n"
  "v_mfma_f32_16x16x32_bf16 v[52:55], a[28:31], v[44:47], v[52:55]\n"
  "ds_read_b128 v[36:39], v66 offset:22528\n"
  "v_dot2_f32_bf16 v128, v74, v86, v128\n"
  "v_dot2_f32_bf16 v129, v74, v98, v129\n"
  "v_mfma_f32_16x16x32_bf16 v[56:59], a[100:103], v[40:43], v[56:59]\n"
  "s_add_u32 m0, s58, 27776\n"
  "s_nop 0\n"
  "global_load_lds_dwordx4 v1, s[38:39] offset:896\n"
  "v_mfma_f32_16x16x32_bf16 v[60:63], a[100:103], v[44:47], v[60:63]\n"
  "v_dot2_f32_bf16 v128, v75, v87, v128\n"
  "v_dot2_f32_bf16 v129, v75, v99, v129\n"
  "s_waitcnt lgkmcnt(6)\n"
  "v_mfma_f32_16x16x32_bf16 v[48:51], a[32:35], v[16:19], v[48:51]\n"
  "ds_read_b128 v[40:43], v67 offset:20480\n"
  "v_dot2_f32_bf16 v128, v76, v88, v128\n"
  "v_dot2_f32_bf16 v129, v76, v100, v129\n"
  "s_waitcnt lgkmcnt(5)\n"
  "v_mfma_f32_16x16x32_bf16 v[52:55], a[32:35], v[20:23], v[52:55]\n"
  "ds_read_b128 v[44:47], v67 offset:22528\n"
  "v_lshlrev_b32 v156, 16, v156\n"
  "v_mul_f32 v156, 0xbfb8aa3b, v156\n"
  "v_mfma_f32_16x16x32_bf16 v[56:59], a[104:107], v[16:19], v[56:59]\n"
  "s_add_u32 m0, s58, 31744\n"
  "s_nop 0\n"
  "global_load_lds_dwordx4 v1, s[38:39] offset:1024\n"
  "v_mfma_f32_16x16x32_bf16 v[60:63], a[104:107], v[20:23], v[60:63]\n"
  "v_exp_f32 v156, v156\n"
  "s_waitcnt lgkmcnt(5)\n"
  "v_mfma_f32_16x16x32_bf16 v[48:51], a[36:39], v[24:27], v[48:51]\n"
  "ds_read_b128 v[16:19], v66 offset:24576\n"
  "v_cvt_pk_bf16_f32 v128, v128, v128\n"
  "v_lshlrev_b32 v128, 16, v128\n"
  "s_waitcnt lgkmcnt(5)\n"
  "v_mfma_f32_16x16x32_bf16 v[52:55], a[36:39], v[28:31], v[52:55]\n"
  "ds_read_b128 v[20:23], v66 offset:26624\n"
  "v_mul_f32 v128, 0x3e715bef, v128\n"
  "v_cvt_pk_bf16_f32 v128, v128, v128\n"
  "v_mfma_f32_16x16x32_bf16 v[56:59], a[108:111], v[24:27], v[56:59]\n"
  "v_lshlrev_b32 v128, 16, v128\n"
  "v_cndmask_b32 v128, v158, v128, s[40:41]\n"
  "v_mfma_f32_16x16x32_bf16 v[60:63], a[108:111], v[28:31], v[60:63]\n"
  "v_cvt_pk_bf16_f32 v129, v129, v129\n"
  "v_lshlrev_b32 v129, 16, v129\n"
  "s_waitcnt lgkmcnt(5)\n"
  "v_mfma_f32_16x16x32_bf16 v[48:51], a[40:43], v[32:35], v[48:51]\n"
  "ds_read_b128 v[24:27], v67 offset:24576\n"
  "v_mul_f32 v129, 0x3e715bef, v129\n"
  "v_cvt_pk_bf16_f32 v129, v129, v129\n"
  "s_waitcnt lgkmcnt(5)\n"
  "v_mfma_f32_16x16x32_bf16 v[52:55], a[40:43], v[36:39], v[52:55]\n"
  "ds_read_b128 v[28:31], v67 offset:26624\n"
  "v_lshlrev_b32 v129, 16, v129\n"
  "v_cndmask_b32 v129, v158, v129, s[42:43]\n"
  "v_mfma_f32_16x16x32_bf16 v[56:59], a[112:115], v[32:35], v[56:59]\n"
  "v_max_f32 v150, v128, v129\n"
  "v_perm_b32 v132, v116, v104, s66\n"
  "v_mfma_f32_16x16x32_bf16 v[60:63], a[112:115], v[36:39], v[60:63]\n"
  "v_perm_b32 v133, v116, v104, s67\n"
  "v_max_f32_dpp v150, v150, v150 quad_perm:[1,0,3,2] row_mask:0xf bank_mask:0xf\n"
  "s_waitcnt lgkmcnt(5)\n"
  "v_mfma_f32_16x16x32_bf16 v[48:51], a[44:47], v[40:43], v[48:51]\n"
  "ds_read_b128 v[32:35], v66 offset:28672\n"
  "v_perm_b32 v134, v117, v105, s66\n"
  "v_perm_b32 v135, v117, v105, s67\n"
  "s_waitcnt lgkmcnt(5)\n"
  "v_mfma_f32_16x16x32_bf16 v[52:55], a[44:47], v[44:47], v[52:55]\n"
  "ds_read_b128 v[36:39], v66 offset:30720\n"
  "v_max_f32_dpp v150, v150, v150 quad_perm:[2,3,0,1] row_mask:0xf bank_mask:0xf\n"
  "v_add_f32 v156, 1.0, v156\n"
  "v_mfma_f32_16x16x32_bf16 v[56:59], a[116:119], v[40:43], v[56:59]\n"
  "v_rcp_f32 v156, v156\n"
  "v_mfma_f32_16x16x32_bf16 v[60:63], a[116:119], v[44:47], v[60:63]\n"
  "v_sub_f32 v128, v128, v150\n"
  "v_mul_f32 v128, 0x3fb8aa3b, v128\n"
  "s_waitcnt lgkmcnt(5)\n"
  "v_mfma_f32_16x16x32_bf16 v[48:51], a[48:51], v[16:19], v[48:51]\n"
  "ds_read_b128 v[40:43], v67 offset:28672\n"
  "v_exp_f32 v128, v128\n"
  "s_waitcnt lgkmcnt(5)\n"
  "v_mfma_f32_16x16x32_bf16 v[52:55], a[48:51], v[20:23], v[52:55]\n"
  "ds_read_b128 v[44:47], v67 offset:30720\n"
  "v_sub_f32 v129, v129, v150\n"
  "v_mul_f32 v129, 0x3fb8aa3b, v129\n"
  "v_mfma_f32_16x16x32_bf16 v[56:59], a[120:123], v[16:19], v[56:59]\n"
  "s_waitcnt vmcnt(9)\n"
  "s_barrier\n"
  "v_mov_b32 v7, 0\n"
  "v_mov_b32 v8, s56\n"
  "s_cmp_eq_u32 s47, 0\n"
  "s_cselect_b64 exec, 1, 0\n"
  "global_store_dword v7, v8, s[64:65] sc1\n"
  "s_mov_b64 exec, -1\n"
  "v_mfma_f32_16x16x32_bf16 v[60:63], a[120:123], v[20:23], v[60:63]\n"
  "v_exp_f32 v129, v129\n"
  "s_waitcnt lgkmcnt(5)\n"
  "v_mfma_f32_16x16x32_bf16 v[48:51], a[52:55], v[24:27], v[48:51]\n"
  "ds_read_b128 v[16:19], v66 offset:32768\n"
  "v_cvt_pk_bf16_f32 v156, v156, v156\n"
  "v_lshlrev_b32 v156, 16, v156\n"
  "s_waitcnt lgkmcnt(5)\n"
  "v_mfma_f32_16x16x32_bf16 v[52:55], a[52:55], v[28:31], v[52:55]\n"
  "ds_read_b128 v[20:23], v66 offset:34816\n"
  "v_add_f32 v150, v128, v129\n"
  "v_perm_b32 v136, v118, v106, s66\n"
  "v_mfma_f32_16x16x32_bf16 v[56:59], a[124:127], v[24:27], v[56:59]\n"
  "v_perm_b32 v137, v118, v106, s67\n"
  "v_add_f32_dpp v150, v150, v150 quad_perm:[1,0,3,2] row_mask:0xf bank_mask:0xf\n"
  "v_mfma_f32_16x16x32_bf16 v[60:63], a[124:127], v[28:31], v[60:63]\n"
  "v_perm_b32 v138, v119, v107, s66\n"
  "v_perm_b32 v139, v119, v107, s67\n"
  "s_waitcnt lgkmcnt(5)\n"
  "v_mfma_f32_16x16x32_bf16 v[48:51], a[56:59], v[32:35], v[48:51]\n"
  "ds_read_b128 v[24:27], v67 offset:32768\n"
  "v_add_f32_dpp v150, v150, v150 quad_perm:[2,3,0,1] row_mask:0xf bank_mask:0xf\n"
  "v_rcp_f32 v150, v150\n"
  "s_waitcnt lgkmcnt(5)\n"
  "v_mfma_f32_16x16x32_bf16 v[52:55], a[56:59], v[36:39], v[52:55]\n"
  "ds_read_b128 v[28:31], v67 offset:34816\n"
  "s_mov_b64 s[40:41], -1\n"
  "s_mov_b64 s[42:43], -1\n"
  "v_mfma_f32_16x16x32_bf16 v[56:59], a[128:131], v[32:35], v[56:59]\n"
  "v_mul_f32 v128, v150, v128\n"
  "v_mul_f32 v129, v150, v129\n"
  "v_mfma_f32_16x16x32_bf16 v[60:63], a[128:131], v[36:39], v[60:63]\n"
  "v_cvt_pk_bf16_f32 v130, v128, v129\n"
  "v_perm_b32 v140, v120, v108, s66\n"
  "s_waitcnt lgkmcnt(5)\n"
  "v_mfma_f32_16x16x32_bf16 v[48:51], a[60:63], v[40:43], v[48:51]\n"
  "v_perm_b32 v141, v120, v108, s67\n"
  "v_perm_b32 v142, v121, v109, s66\n"
  "s_waitcnt lgkmcnt(4)\n"
  "v_mfma_f32_16x16x32_bf16 v[52:55], a[60:63], v[44:47], v[52:55]\n"
  "v_perm_b32 v143, v121, v109, s67\n"
  "v_perm_b32 v144, v122, v110, s66\n"
  "v_mfma_f32_16x16x32_bf16 v[56:59], a[132:135], v[40:43], v[56:59]\n"
  "v_perm_b32 v145, v122, v110, s67\n"
  "v_perm_b32 v146, v123, v111, s66\n"
  "v_mfma_f32_16x16x32_bf16 v[60:63], a[132:135], v[44:47], v[60:63]\n"
  "v_perm_b32 v147, v123, v111, s67\n"
  "v_perm_b32 v148, v124, v112, s66\n"
  "s_waitcnt lgkmcnt(3)\n"
  "v_mfma_f32_16x16x32_bf16 v[48:51], a[64:67], v[16:19], v[48:51]\n"
  "v_perm_b32 v149, v124, v112, s67\n"
  "v_dot2_f32_bf16 v132, v132, v130, 0\n"
  "s_waitcnt lgkmcnt(2)\n"
  "v_mfma_f32_16x16x32_bf16 v[52:55], a[64:67], v[20:23], v[52:55]\n"
  "v_dot2_f32_bf16 v133, v133, v130, 0\n"
  "v_dot2_f32_bf16 v134, v134, v130, 0\n"
  "v_mfma_f32_16x16x32_bf16 v[56:59], a[136:139], v[16:19], v[56:59]\n"
  "v_dot2_f32_bf16 v135, v135, v130, 0\n"
  "v_dot2_f32_bf16 v136, v136, v130, 0\n"
  "v_mfma_f32_16x16x32_bf16 v[60:63], a[136:139], v[20:23], v[60:63]\n"
  "v_dot2_f32_bf16 v137, v137, v130, 0\n"
  "v_dot2_f32_bf16 v138, v138, v130, 0\n"
  "s_waitcnt lgkmcnt(1)\n"
  "v_mfma_f32_16x16x32_bf16 v[48:51], a[68:71], v[24:27], v[48:51]\n"
  "v_dot2_f32_bf16 v139, v139, v130, 0\n"
  "v_dot2_f32_bf16 v140, v140, v130, 0\n"
  "s_waitcnt lgkmcnt(0)\n"
  "v_mfma_f32_16x16x32_bf16 v[52:55], a[68:71], v[28:31], v[52:55]\n"
  "v_dot2_f32_bf16 v141, v141, v130, 0\n"
  "v_dot2_f32_bf16 v142, v142, v130, 0\n"
  "v_mfma_f32_16x16x32_bf16 v[56:59], a[140:143], v[24:27], v[56:59]\n"
  "v_dot2_f32_bf16 v143, v143, v130, 0\n"
  "v_dot2_f32_bf16 v144, v144, v130, 0\n"
  "v_mfma_f32_16x16x32_bf16 v[60:63], a[140:143], v[28:31], v[60:63]\n"
  "v_dot2_f32_bf16 v145, v145, v130, 0\n"
  "v_dot2_f32_bf16 v146, v146, v130, 0\n"
  "v_dot2_f32_bf16 v147, v147, v130, 0\n"
  "v_dot2_f32_bf16 v148, v148, v130, 0\n"
  "v_dot2_f32_bf16 v149, v149, v130, 0\n"
  "v_add_f32_dpp v132, v132, v132 quad_perm:[1,0,3,2] row_mask:0xf bank_mask:0xf\n"
  "v_add_f32_dpp v133, v133, v133 quad_perm:[1,0,3,2] row_mask:0xf bank_mask:0xf\n"
  "v_add_f32_dpp v134, v134, v134 quad_perm:[1,0,3,2] row_mask:0xf bank_mask:0xf\n"
  "v_add_f32_dpp v135, v135, v135 quad_perm:[1,0,3,2] row_mask:0xf bank_mask:0xf\n"
  "v_add_f32_dpp v136, v136, v136 quad_perm:[1,0,3,2] row_mask:0xf bank_mask:0xf\n"
  "v_add_f32_dpp v137, v137, v137 quad_perm:[1,0,3,2] row_mask:0xf bank_mask:0xf\n"
  "v_add_f32_dpp v138, v138, v138 quad_perm:[1,0,3,2] row_mask:0xf bank_mask:0xf\n"
  "v_add_f32_dpp v139, v139, v139 quad_perm:[1,0,3,2] row_mask:0xf bank_mask:0xf\n"
  "v_add_f32_dpp v140, v140, v140 quad_perm:[1,0,3,2] row_mask:0xf bank_mask:0xf\n"
  "v_add_f32_dpp v141, v141, v141 quad_perm:[1,0,3,2] row_mask:0xf bank_mask:0xf\n"
  "v_add_f32_dpp v142, v142, v142 quad_perm:[1,0,3,2] row_mask:0xf bank_mask:0xf\n"
  "v_add_f32_dpp v143, v143, v143 quad_perm:[1,0,3,2] row_mask:0xf bank_mask:0xf\n"
  "v_add_f32_dpp v144, v144, v144 quad_perm:[1,0,3,2] row_mask:0xf bank_mask:0xf\n"
  "v_add_f32_dpp v145, v145, v145 quad_perm:[1,0,3,2] row_mask:0xf bank_mask:0xf\n"
  "v_add_f32_dpp v146, v146, v146 quad_perm:[1,0,3,2] row_mask:0xf bank_mask:0xf\n"
  "v_add_f32_dpp v147, v147, v147 quad_perm:[1,0,3,2] row_mask:0xf bank_mask:0xf\n"
  "v_add_f32_dpp v148, v148, v148 quad_perm:[1,0,3,2] row_mask:0xf bank_mask:0xf\n"
  "v_add_f32_dpp v149, v149, v149 quad_perm:[1,0,3,2] row_mask:0xf bank_mask:0xf\n"
  "v_add_f32_dpp v132, v132, v132 quad_perm:[2,3,0,1] row_mask:0xf bank_mask:0xf\n"
  "v_add_f32_dpp v133, v133, v133 quad_perm:[2,3,0,1] row_mask:0xf bank_mask:0xf\n"
  "v_add_f32_dpp v134, v134, v134 quad_perm:[2,3,0,1] row_mask:0xf bank_mask:0xf\n"
  "v_add_f32_dpp v135, v135, v135 quad_perm:[2,3,0,1] row_mask:0xf bank_mask:0xf\n"
  "v_add_f32_dpp v136, v136, v136 quad_perm:[2,3,0,1] row_mask:0xf bank_mask:0xf\n"
  "v_add_f32_dpp v137, v137, v137 quad_perm:[2,3,0,1] row_mask:0xf bank_mask:0xf\n"
  "v_add_f32_dpp v138, v138, v138 quad_perm:[2,3,0,1] row_mask:0xf bank_mask:0xf\n"
  "v_add_f32_dpp v139, v139, v139 quad_perm:[2,3,0,1] row_mask:0xf bank_mask:0xf\n"
  "v_add_f32_dpp v140, v140, v140 quad_perm:[2,3,0,1] row_mask:0xf bank_mask:0xf\n"
  "v_add_f32_dpp v141, v141, v141 quad_perm:[2,3,0,1] row_mask:0xf bank_mask:0xf\n"
  "v_add_f32_dpp v142, v142, v142 quad_perm:[2,3,0,1] row_mask:0xf bank_mask:0xf\n"
  "v_add_f32_dpp v143, v143, v143 quad_perm:[2,3,0,1] row_mask:0xf bank_mask:0xf\n"
  "v_add_f32_dpp v144, v144, v144 quad_perm:[2,3,0,1] row_mask:0xf bank_mask:0xf\n"
  "v_add_f32_dpp v145, v145, v145 quad_perm:[2,3,0,1] row_mask:0xf bank_mask:0xf\n"
  "v_add_f32_dpp v146, v146, v146 quad_perm:[2,3,0,1] row_mask:0xf bank_mask:0xf\n"
  "v_add_f32_dpp v147, v147, v147 quad_perm:[2,3,0,1] row_mask:0xf bank_mask:0xf\n"
  "v_add_f32_dpp v148, v148, v148 quad_perm:[2,3,0,1] row_mask:0xf bank_mask:0xf\n"
  "v_add_f32_dpp v149, v149, v149 quad_perm:[2,3,0,1] row_mask:0xf bank_mask:0xf\n"
  "v_cvt_pk_bf16_f32 v132, v132, v133\n"
  "v_cvt_pk_bf16_f32 v133, v134, v135\n"
  "v_cvt_pk_bf16_f32 v134, v136, v137\n"
  "v_cvt_pk_bf16_f32 v135, v138, v139\n"
  "v_cvt_pk_bf16_f32 v136, v140, v141\n"
  "v_cvt_pk_bf16_f32 v137, v142, v143\n"
  "v_cvt_pk_bf16_f32 v138, v144, v145\n"
  "v_cvt_pk_bf16_f32 v139, v146, v147\n"
  "v_cvt_pk_bf16_f32 v140, v148, v149\n"
  "v_cndmask_b32 v132, v132, v134, s[10:11]\n"
  "v_cndmask_b32 v133, v133, v135, s[10:11]\n"
  "v_cndmask_b32 v132, v132, v136, s[12:13]\n"
  "v_cndmask_b32 v133, v133, v137, s[12:13]\n"
  "v_cndmask_b32 v132, v132, v138, s[14:15]\n"
  "v_cndmask_b32 v133, v133, v139, s[14:15]\n"
  "ds_write_b64 v3, v[132:133]\n"
  "s_mov_b64 exec, s[8:9]\n"
  "ds_write_b32 v3, v140 offset:32\n"
  "s_mov_b64 exec, -1\n"
  "ds_read_b128 v[152:155], v4\n"
  "v_cvt_pk_bf16_f32 v48, v48, v49\n"
  "v_cvt_pk_bf16_f32 v49, v50, v51\n"
  "v_cvt_pk_bf16_f32 v52, v52, v53\n"
  "v_cvt_pk_bf16_f32 v53, v54, v55\n"
  "v_cvt_pk_bf16_f32 v56, v56, v57\n"
  "v_cvt_pk_bf16_f32 v57, v58, v59\n"
  "v_cvt_pk_bf16_f32 v60, v60, v61\n"
  "v_cvt_pk_bf16_f32 v61, v62, v63\n"
  "ds_write_b64 v10, v[48:49] offset:9728\n"
  "ds_write_b64 v10, v[52:53] offset:14592\n"
  "ds_write_b64 v11, v[56:57] offset:9728\n"
  "ds_write_b64 v11, v[60:61] offset:14592\n"
  "s_mov_b64 exec, s[68:69]\n"
  "global_store_dword v157, v156, s[62:63] sc1\n"
  "s_bfm_b64 exec, 40, 0\n"
  "s_waitcnt lgkmcnt(4)\n"
  "global_store_dwordx4 v5, v[152:155], s[60:61] sc1\n"
  "s_mov_b64 exec, -1\n"
  "s_add_u32 s60, s60, 40960\n"
  "s_addc_u32 s61, s61, 0\n"
  "s_add_u32 s62, s62, 2048\n"
  "s_addc_u32 s63, s63, 0\n"
  "s_add_u32 s56, s56, 1\n"
  "s_cmp_eq_u32 s56, 63\n"
  "s_cbranch_scc1 AEND\n"
  "s_waitcnt vmcnt(3) lgkmcnt(0)\n"
  "s_barrier\n"
  "v_add_u32 v66, 22016, v2\n"
  "v_xor_b32 v67, 64, v66\n"
  "s_add_u32 s58, s80, 58880\n"
  "s_cmp_lt_i32 s56, 62\n"
  "s_cselect_b32 s74, 36864, 0\n"
  "s_add_u32 s38, s38, s74\n"
  "s_addc_u32 s39, s39, 0\n"
  "ds_read_b128 v[16:19], v66 offset:0\n"
  "ds_read_b128 v[20:23], v66 offset:2048\n"
  "ds_read_b128 v[24:27], v67 offset:0\n"
  "ds_read_b128 v[28:31], v67 offset:2048\n"
  "ds_read_b128 v[32:35], v66 offset:4096\n"
  "ds_read_b128 v[36:39], v66 offset:6144\n"
  "s_waitcnt lgkmcnt(5)\n"
  "v_mfma_f32_16x16x32_bf16 v[48:51], a[0:3], v[16:19], 0\n"
  "ds_read_b128 v[40:43], v67 offset:4096\n"
  "ds_read_b128 v[68:71], v12 offset:9728\n"
  "s_nop 0\n"
  "s_waitcnt lgkmcnt(6)\n"
  "v_mfma_f32_16x16x32_bf16 v[52:55], a[0:3], v[20:23], 0\n"
  "ds_read_b128 v[44:47], v67 offset:6144\n"
  "ds_read_b128 v[72:75], v12 offset:9744\n"
  "s_nop 0\n"
  "v_mfma_f32_16x16x32_bf16 v[56:59], a[72:75], v[16:19], 0\n"
  "s_add_u32 m0, s58, 0\n"
  "s_nop 0\n"
  "global_load_lds_dwordx4 v1, s[38:39] offset:0\n"
  "v_mfma_f32_16x16x32_bf16 v[60:63], a[72:75], v[20:23], 0\n"
  "ds_read_b32 v76, v12 offset:9760\n"
  "s_nop 0\n"
  "s_waitcnt lgkmcnt(8)\n"
  "v_mfma_f32_16x16x32_bf16 v[48:51], a[4:7], v[24:27], v[48:51]\n"
  "ds_read_b128 v[16:19], v66 offset:8192\n"
  "ds_read_b128 v[80:83], v64 offset:0\n"
  "s_nop 0\n"
  "s_waitcnt lgkmcnt(9)\n"
  "v_mfma_f32_16x16x32_bf16 v[52:55], a[4:7], v[28:31], v[52:55]\n"
  "ds_read_b128 v[20:23], v66 offset:10240\n"
  "ds_read_b128 v[84:87], v64 offset:16\n"
  "s_nop 0\n"
  "v_mfma_f32_16x16x32_bf16 v[56:59], a[76:79], v[24:27], v[56:59]\n"
  "s_add_u32 m0, s58, 3968\n"
  "s_nop 0\n"
  "global_load_lds_dwordx4 v1, s[38:39] offset:128\n"
  "v_mfma_f32_16x16x32_bf16 v[60:63], a[76:79], v[28:31], v[60:63]\n"
  "ds_read_b32 v88, v64 offset:32\n"
  "s_nop 0\n"
  "s_waitcnt lgkmcnt(11)\n"
  "v_mfma_f32_16x16x32_bf16 v[48:51], a[8:11], v[32:35], v[48:51]\n"
  "ds_read_b128 v[24:27], v67 offset:8192\n"
  "ds_read_b128 v[92:95], v65 offset:0\n"
  "s_nop 0\n"
  "s_waitcnt lgkmcnt(12)\n"
  "v_mfma_f32_16x16x32_bf16 v[52:55], a[8:11], v[36:39], v[52:55]\n"
  "ds_read_b128 v[28:31], v67 offset:10240\n"
  "ds_read_b128 v[96:99], v65 offset:16\n"
  "s_nop 0\n"
  "v_mfma_f32_16x16x32_bf16 v[56:59], a[80:83], v[32:35], v[56:59]\n"
  "s_add_u32 m0, s58, 7936\n"
  "s_nop 0\n"
  "global_load_lds_dwordx4 v1, s[38:39] offset:256\n"
  "v_mfma_f32_16x16x32_bf16 v[60:63], a[80:83], v[36:39], v[60:63]\n"
  "ds_read_b32 v100, v65 offset:32\n"
  "s_nop 0\n"
  "s_waitcnt lgkmcnt(14)\n"
  "v_mfma_f32_16x16x32_bf16 v[48:51], a[12:15], v[40:43], v[48:51]\n"
  "ds_read_b128 v[32:35], v66 offset:12288\n"
  "ds_read_b32 v156, v6 offset:9728\n"
  "s_nop 0\n"
  "s_waitcnt lgkmcnt(14)\n"
  "v_mfma_f32_16x16x32_bf16 v[52:55], a[12:15], v[44:47], v[52:55]\n"
  "ds_read_b128 v[36:39], v66 offset:14336\n"
  "ds_read_b128 v[104:107], v64 offset:96\n"
  "s_nop 0\n"
  "v_mfma_f32_16x16x32_bf16 v[56:59], a[84:87], v[40:43], v[56:59]\n"
  "s_add_u32 m0, s58, 11904\n"
  "s_nop 0\n"
  "global_load_lds_dwordx4 v1, s[38:39] offset:384\n"
  "v_mfma_f32_16x16x32_bf16 v[60:63], a[84:87], v[44:47], v[60:63]\n"
  "ds_read_b128 v[108:111], v64 offset:112\n"
  "s_nop 0\n"
  "s_waitcnt lgkmcnt(14)\n"
  "v_mfma_f32_16x16x32_bf16 v[48:51], a[16:19], v[16:19], v[48:51]\n"
  "ds_read_b128 v[40:43], v67 offset:12288\n"
  "ds_read_b32 v112, v64 offset:128\n"
  "s_nop 0\n"
  "s_waitcnt lgkmcnt(14)\n"
  "v_mfma_f32_16x16x32_bf16 v[52:55], a[16:19], v[20:23], v[52:55]\n"
  "ds_read_b128 v[44:47], v67 offset:14336\n"
  "ds_read_b128 v[116:119], v65 offset:96\n"
  "s_nop 0\n"
  "v_mfma_f32_16x16x32_bf16 v[56:59], a[88:91], v[16:19], v[56:59]\n"
  "s_add_u32 m0, s58, 15872\n"
  "s_nop 0\n"
  "global_load_lds_dwordx4 v1, s[38:39] offset:512\n"
  "v_mfma_f32_16x16x32_bf16 v[60:63], a[88:91], v[20:23], v[60:63]\n"
  "ds_read_b128 v[120:123], v65 offset:112\n"
  "s_nop 0\n"
  "s_waitcnt lgkmcnt(14)\n"
  "v_mfma_f32_16x16x32_bf16 v[48:51], a[20:23], v[24:27], v[48:51]\n"
  "ds_read_b128 v[16:19], v66 offset:16384\n"
  "ds_read_b32 v124, v65 offset:128\n"
  "s_nop 0\n"
  "s_waitcnt lgkmcnt(14)\n"
  "v_mfma_f32_16x16x32_bf16 v[52:55], a[20:23], v[28:31], v[52:55]\n"
  "ds_read_b128 v[20:23], v66 offset:18432\n"
  "v_dot2_f32_bf16 v128, v68, v80, 0\n"
  "v_dot2_f32_bf16 v129, v68, v92, 0\n"
  "v_mfma_f32_16x16x32_bf16 v[56:59], a[92:95], v[24:27], v[56:59]\n"
  "s_add_u32 m0, s58, 19840\n"
  "s_nop 0\n"
  "global_load_lds_dwordx4 v1, s[38:39] offset:640\n"
  "v_mfma_f32_16x16x32_bf16 v[60:63], a[92:95], v[28:31], v[60:63]\n"
  "v_dot2_f32_bf16 v128, v69, v81, v128\n"
  "v_dot2_f32_bf16 v129, v69, v93, v129\n"
  "s_waitcnt lgkmcnt(12)\n"
  "v_mfma_f32_16x16x32_bf16 v[48:51], a[24:27], v[32:35], v[48:51]\n"
  "ds_read_b128 v[24:27], v67 offset:16384\n"
  "v_dot2_f32_bf16 v128, v70, v82, v128\n"
  "v_dot2_f32_bf16 v129, v70, v94, v129\n"
  "s_waitcnt lgkmcnt(11)\n"
  "v_mfma_f32_16x16x32_bf16 v[52:55], a[24:27], v[36:39], v[52:55]\n"
  "ds_read_b128 v[28:31], v67 offset:18432\n"
  "v_dot2_f32_bf16 v128, v71, v83, v128\n"
  "v_dot2_f32_bf16 v129, v71, v95, v129\n"
  "v_mfma_f32_16x16x32_bf16 v[56:59], a[96:99], v[32:35], v[56:59]\n"
  "s_add_u32 m0, s58, 23808\n"
  "s_nop 0\n"
  "global_load_lds_dwordx4 v1, s[38:39] offset:768\n"
  "v_mfma_f32_16x16x32_bf16 v[60:63], a[96:99], v[36:39], v[60:63]\n"
  "v_dot2_f32_bf16 v128, v72, v84, v128\n"
  "v_dot2_f32_bf16 v129, v72, v96, v129\n"
  "s_waitcnt lgkmcnt(9)\n"
  "v_mfma_f32_16x16x32_bf16 v[48:51], a[28:31], v[40:43], v[48:51]\n"
  "ds_read_b128 v[32:35], v66 offset:20480\n"
  "v_dot2_f32_bf16 v128, v73, v85, v128\n"
  "v_dot2_f32_bf16 v129, v73, v97, v129\n"
  "s_waitcnt lgkmcnt(8)\n"
  "v_mfma_f32_16x16x32_bf16 v[52:55], a[28:31], v[44:47], v[52:55]\n"
  "ds_read_b128 v[36:39], v66 offset:22528\n"
  "v_dot2_f32_bf16 v128, v74, v86, v128\n"
  "v_dot2_f32_bf16 v129, v74, v98, v129\n"
  "v_mfma_f32_16x16x32_bf16 v[56:59], a[100:103], v[40:43], v[56:59]\n"
  "s_add_u32 m0, s58, 27776\n"
  "s_nop 0\n"
  "global_load_lds_dwordx4 v1, s[38:39] offset:896\n"
  "v_mfma_f32_16x16x32_bf16 v[60:63], a[100:103], v[44:47], v[60:63]\n"
  "v_dot2_f32_bf16 v128, v75, v87, v128\n"
  "v_dot2_f32_bf16 v129, v75, v99, v129\n"
  "s_waitcnt lgkmcnt(6)\n"
  "v_mfma_f32_16x16x32_bf16 v[48:51], a[32:35], v[16:19], v[48:51]\n"
  "ds_read_b128 v[40:43], v67 offset:20480\n"
  "v_dot2_f32_bf16 v128, v76, v88, v128\n"
  "v_dot2_f32_bf16 v129, v76, v100, v129\n"
  "s_waitcnt lgkmcnt(5)\n"
  "v_mfma_f32_16x16x32_bf16 v[52:55], a[32:35], v[20:23], v[52:55]\n"
  "ds_read_b128 v[44:47], v67 offset:22528\n"
  "v_lshlrev_b32 v156, 16, v156\n"
  "v_mul_f32 v156, 0xbfb8aa3b, v156\n"
  "v_mfma_f32_16x16x32_bf16 v[56:59], a[104:107], v[16:19], v[56:59]\n"
  "s_add_u32 m0, s58, 31744\n"
  "s_nop 0\n"
  "global_load_lds_dwordx4 v1, s[38:39] offset:1024\n"
  "v_mfma_f32_16x16x32_bf16 v[60:63], a[104:107], v[20:23], v[60:63]\n"
  "v_exp_f32 v156, v156\n"
  "s_waitcnt lgkmcnt(5)\n"
  "v_mfma_f32_16x16x32_bf16 v[48:51], a[36:39], v[24:27], v[48:51]\n"
  "ds_read_b128 v[16:19], v66 offset:24576\n"
  "v_cvt_pk_bf16_f32 v128, v128, v128\n"
  "v_lshlrev_b32 v128, 16, v128\n"
  "s_waitcnt lgkmcnt(5)\n"
  "v_mfma_f32_16x16x32_bf16 v[52:55], a[36:39], v[28:31], v[52:55]\n"
  "ds_read_b128 v[20:23], v66 offset:26624\n"
  "v_mul_f32 v128, 0x3e715bef, v128\n"
  "v_cvt_pk_bf16_f32 v128, v128, v128\n"
  "v_mfma_f32_16x16x32_bf16 v[56:59], a[108:111], v[24:27], v[56:59]\n"
  "v_lshlrev_b32 v128, 16, v128\n"
  "v_cndmask_b32 v128, v158, v128, s[40:41]\n"
  "v_mfma_f32_16x16x32_bf16 v[60:63], a[108:111], v[28:31], v[60:63]\n"
  "v_cvt_pk_bf16_f32 v129, v129, v129\n"
  "v_lshlrev_b32 v129, 16, v129\n"
  "s_waitcnt lgkmcnt(5)\n"
  "v_mfma_f32_16x16x32_bf16 v[48:51], a[40:43], v[32:35], v[48:51]\n"
  "ds_read_b128 v[24:27], v67 offset:24576\n"
  "v_mul_f32 v129, 0x3e715bef, v129\n"
  "v_cvt_pk_bf16_f32 v129, v129, v129\n"
  "s_waitcnt lgkmcnt(5)\n"
  "v_mfma_f32_16x16x32_bf16 v[52:55], a[40:43], v[36:39], v[52:55]\n"
  "ds_read_b128 v[28:31], v67 offset:26624\n"
  "v_lshlrev_b32 v129, 16, v129\n"
  "v_cndmask_b32 v129, v158, v129, s[42:43]\n"
  "v_mfma_f32_16x16x32_bf16 v[56:59], a[112:115], v[32:35], v[56:59]\n"
  "v_max_f32 v150, v128, v129\n"
  "v_perm_b32 v132, v116, v104, s66\n"
  "v_mfma_f32_16x16x32_bf16 v[60:63], a[112:115], v[36:39], v[60:63]\n"
  "v_perm_b32 v133, v116, v104, s67\n"
  "v_max_f32_dpp v150, v150, v150 quad_perm:[1,0,3,2] row_mask:0xf bank_mask:0xf\n"
  "s_waitcnt lgkmcnt(5)\n"
  "v_mfma_f32_16x16x32_bf16 v[48:51], a[44:47], v[40:43], v[48:51]\n"
  "ds_read_b128 v[32:35], v66 offset:28672\n"
  "v_perm_b32 v134, v117, v105, s66\n"
  "v_perm_b32 v135, v117, v105, s67\n"
  "s_waitcnt lgkmcnt(5)\n"
  "v_mfma_f32_16x16x32_bf16 v[52:55], a[44:47], v[44:47], v[52:55]\n"
  "ds_read_b128 v[36:39], v66 offset:30720\n"
  "v_max_f32_dpp v150, v150, v150 quad_perm:[2,3,0,1] row_mask:0xf bank_mask:0xf\n"
  "v_add_f32 v156, 1.0, v156\n"
  "v_mfma_f32_16x16x32_bf16 v[56:59], a[116:119], v[40:43], v[56:59]\n"
  "v_rcp_f32 v156, v156\n"
  "v_mfma_f32_16x16x32_bf16 v[60:63], a[116:119], v[44:47], v[60:63]\n"
  "v_sub_f32 v128, v128, v150\n"
  "v_mul_f32 v128, 0x3fb8aa3b, v128\n"
  "s_waitcnt lgkmcnt(5)\n"
  "v_mfma_f32_16x16x32_bf16 v[48:51], a[48:51], v[16:19], v[48:51]\n"
  "ds_read_b128 v[40:43], v67 offset:28672\n"
  "v_exp_f32 v128, v128\n"
  "s_waitcnt lgkmcnt(5)\n"
  "v_mfma_f32_16x16x32_bf16 v[52:55], a[48:51], v[20:23], v[52:55]\n"
  "ds_read_b128 v[44:47], v67 offset:30720\n"
  "v_sub_f32 v129, v129, v150\n"
  "v_mul_f32 v129, 0x3fb8aa3b, v129\n"
  "v_mfma_f32_16x16x32_bf16 v[56:59], a[120:123], v[16:19], v[56:59]\n"
  "s_waitcnt vmcnt(9)\n"
  "s_barrier\n"
  "v_mov_b32 v7, 0\n"
  "v_mov_b32 v8, s56\n"
  "s_cmp_eq_u32 s47, 0\n"
  "s_cselect_b64 exec, 1, 0\n"
  "global_store_dword v7, v8, s[64:65] sc1\n"
  "s_mov_b64 exec, -1\n"
  "v_mfma_f32_16x16x32_bf16 v[60:63], a[120:123], v[20:23], v[60:63]\n"
  "v_exp_f32 v129, v129\n"
  "s_waitcnt lgkmcnt(5)\n"
  "v_mfma_f32_16x16x32_bf16 v[48:51], a[52:55], v[24:27], v[48:51]\n"
  "ds_read_b128 v[16:19], v66 offset:32768\n"
  "v_cvt_pk_bf16_f32 v156, v156, v156\n"
  "v_lshlrev_b32 v156, 16, v156\n"
  "s_waitcnt lgkmcnt(5)\n"
  "v_mfma_f32_16x16x32_bf16 v[52:55], a[52:55], v[28:31], v[52:55]\n"
  "ds_read_b128 v[20:23], v66 offset:34816\n"
  "v_add_f32 v150, v128, v129\n"
  "v_perm_b32 v136, v118, v106, s66\n"
  "v_mfma_f32_16x16x32_bf16 v[56:59], a[124:127], v[24:27], v[56:59]\n"
  "v_perm_b32 v137, v118, v106, s67\n"
  "v_add_f32_dpp v150, v150, v150 quad_perm:[1,0,3,2] row_mask:0xf bank_mask:0xf\n"
  "v_mfma_f32_16x16x32_bf16 v[60:63], a[124:127], v[28:31], v[60:63]\n"
  "v_perm_b32 v138, v119, v107, s66\n"
  "v_perm_b32 v139, v119, v107, s67\n"
  "s_waitcnt lgkmcnt(5)\n"
  "v_mfma_f32_16x16x32_bf16 v[48:51], a[56:59], v[32:35], v[48:51]\n"
  "ds_read_b128 v[24:27], v67 offset:32768\n"
  "v_add_f32_dpp v150, v150, v150 quad_perm:[2,3,0,1] row_mask:0xf bank_mask:0xf\n"
  "v_rcp_f32 v150, v150\n"
  "s_waitcnt lgkmcnt(5)\n"
  "v_mfma_f32_16x16x32_bf16 v[52:55], a[56:59], v[36:39], v[52:55]\n"
  "ds_read_b128 v[28:31], v67 offset:34816\n"
  "s_mov_b64 s[40:41], -1\n"
  "s_mov_b64 s[42:43], -1\n"
  "v_mfma_f32_16x16x32_bf16 v[56:59], a[128:131], v[32:35], v[56:59]\n"
  "v_mul_f32 v128, v150, v128\n"
  "v_mul_f32 v129, v150, v129\n"
  "v_mfma_f32_16x16x32_bf16 v[60:63], a[128:131], v[36:39], v[60:63]\n"
  "v_cvt_pk_bf16_f32 v130, v128, v129\n"
  "v_perm_b32 v140, v120, v108, s66\n"
  "s_waitcnt lgkmcnt(5)\n"
  "v_mfma_f32_16x16x32_bf16 v[48:51], a[60:63], v[40:43], v[48:51]\n"
  "v_perm_b32 v141, v120, v108, s67\n"
  "v_perm_b32 v142, v121, v109, s66\n"
  "s_waitcnt lgkmcnt(4)\n"
  "v_mfma_f32_16x16x32_bf16 v[52:55], a[60:63], v[44:47], v[52:55]\n"
  "v_perm_b32 v143, v121, v109, s67\n"
  "v_perm_b32 v144, v122, v110, s66\n"
  "v_mfma_f32_16x16x32_bf16 v[56:59], a[132:135], v[40:43], v[56:59]\n"
  "v_perm_b32 v145, v122, v110, s67\n"
  "v_perm_b32 v146, v123, v111, s66\n"
  "v_mfma_f32_16x16x32_bf16 v[60:63], a[132:135], v[44:47], v[60:63]\n"
  "v_perm_b32 v147, v123, v111, s67\n"
  "v_perm_b32 v148, v124, v112, s66\n"
  "s_waitcnt lgkmcnt(3)\n"
  "v_mfma_f32_16x16x32_bf16 v[48:51], a[64:67], v[16:19], v[48:51]\n"
  "v_perm_b32 v149, v124, v112, s67\n"
  "v_dot2_f32_bf16 v132, v132, v130, 0\n"
  "s_waitcnt lgkmcnt(2)\n"
  "v_mfma_f32_16x16x32_bf16 v[52:55], a[64:67], v[20:23], v[52:55]\n"
  "v_dot2_f32_bf16 v133, v133, v130, 0\n"
  "v_dot2_f32_bf16 v134, v134, v130, 0\n"
  "v_mfma_f32_16x16x32_bf16 v[56:59], a[136:139], v[16:19], v[56:59]\n"
  "v_dot2_f32_bf16 v135, v135, v130, 0\n"
  "v_dot2_f32_bf16 v136, v136, v130, 0\n"
  "v_mfma_f32_16x16x32_bf16 v[60:63], a[136:139], v[20:23], v[60:63]\n"
  "v_dot2_f32_bf16 v137, v137, v130, 0\n"
  "v_dot2_f32_bf16 v138, v138, v130, 0\n"
  "s_waitcnt lgkmcnt(1)\n"
  "v_mfma_f32_16x16x32_bf16 v[48:51], a[68:71], v[24:27], v[48:51]\n"
  "v_dot2_f32_bf16 v139, v139, v130, 0\n"
  "v_dot2_f32_bf16 v140, v140, v130, 0\n"
  "s_waitcnt lgkmcnt(0)\n"
  "v_mfma_f32_16x16x32_bf16 v[52:55], a[68:71], v[28:31], v[52:55]\n"
  "v_dot2_f32_bf16 v141, v141, v130, 0\n"
  "v_dot2_f32_bf16 v142, v142, v130, 0\n"
  "v_mfma_f32_16x16x32_bf16 v[56:59], a[140:143], v[24:27], v[56:59]\n"
  "v_dot2_f32_bf16 v143, v143, v130, 0\n"
  "v_dot2_f32_bf16 v144, v144, v130, 0\n"
  "v_mfma_f32_16x16x32_bf16 v[60:63], a[140:143], v[28:31], v[60:63]\n"
  "v_dot2_f32_bf16 v145, v145, v130, 0\n"
  "v_dot2_f32_bf16 v146, v146, v130, 0\n"
  "v_dot2_f32_bf16 v147, v147, v130, 0\n"
  "v_dot2_f32_bf16 v148, v148, v130, 0\n"
  "v_dot2_f32_bf16 v149, v149, v130, 0\n"
  "v_add_f32_dpp v132, v132, v132 quad_perm:[1,0,3,2] row_mask:0xf bank_mask:0xf\n"
  "v_add_f32_dpp v133, v133, v133 quad_perm:[1,0,3,2] row_mask:0xf bank_mask:0xf\n"
  "v_add_f32_dpp v134, v134, v134 quad_perm:[1,0,3,2] row_mask:0xf bank_mask:0xf\n"
  "v_add_f32_dpp v135, v135, v135 quad_perm:[1,0,3,2] row_mask:0xf bank_mask:0xf\n"
  "v_add_f32_dpp v136, v136, v136 quad_perm:[1,0,3,2] row_mask:0xf bank_mask:0xf\n"
  "v_add_f32_dpp v137, v137, v137 quad_perm:[1,0,3,2] row_mask:0xf bank_mask:0xf\n"
  "v_add_f32_dpp v138, v138, v138 quad_perm:[1,0,3,2] row_mask:0xf bank_mask:0xf\n"
  "v_add_f32_dpp v139, v139, v139 quad_perm:[1,0,3,2] row_mask:0xf bank_mask:0xf\n"
  "v_add_f32_dpp v140, v140, v140 quad_perm:[1,0,3,2] row_mask:0xf bank_mask:0xf\n"
  "v_add_f32_dpp v141, v141, v141 quad_perm:[1,0,3,2] row_mask:0xf bank_mask:0xf\n"
  "v_add_f32_dpp v142, v142, v142 quad_perm:[1,0,3,2] row_mask:0xf bank_mask:0xf\n"
  "v_add_f32_dpp v143, v143, v143 quad_perm:[1,0,3,2] row_mask:0xf bank_mask:0xf\n"
  "v_add_f32_dpp v144, v144, v144 quad_perm:[1,0,3,2] row_mask:0xf bank_mask:0xf\n"
  "v_add_f32_dpp v145, v145, v145 quad_perm:[1,0,3,2] row_mask:0xf bank_mask:0xf\n"
  "v_add_f32_dpp v146, v146, v146 quad_perm:[1,0,3,2] row_mask:0xf bank_mask:0xf\n"
  "v_add_f32_dpp v147, v147, v147 quad_perm:[1,0,3,2] row_mask:0xf bank_mask:0xf\n"
  "v_add_f32_dpp v148, v148, v148 quad_perm:[1,0,3,2] row_mask:0xf bank_mask:0xf\n"
  "v_add_f32_dpp v149, v149, v149 quad_perm:[1,0,3,2] row_mask:0xf bank_mask:0xf\n"
  "v_add_f32_dpp v132, v132, v132 quad_perm:[2,3,0,1] row_mask:0xf bank_mask:0xf\n"
  "v_add_f32_dpp v133, v133, v133 quad_perm:[2,3,0,1] row_mask:0xf bank_mask:0xf\n"
  "v_add_f32_dpp v134, v134, v134 quad_perm:[2,3,0,1] row_mask:0xf bank_mask:0xf\n"
  "v_add_f32_dpp v135, v135, v135 quad_perm:[2,3,0,1] row_mask:0xf bank_mask:0xf\n"
  "v_add_f32_dpp v136, v136, v136 quad_perm:[2,3,0,1] row_mask:0xf bank_mask:0xf\n"
  "v_add_f32_dpp v137, v137, v137 quad_perm:[2,3,0,1] row_mask:0xf bank_mask:0xf\n"
  "v_add_f32_dpp v138, v138, v138 quad_perm:[2,3,0,1] row_mask:0xf bank_mask:0xf\n"
  "v_add_f32_dpp v139, v139, v139 quad_perm:[2,3,0,1] row_mask:0xf bank_mask:0xf\n"
  "v_add_f32_dpp v140, v140, v140 quad_perm:[2,3,0,1] row_mask:0xf bank_mask:0xf\n"
  "v_add_f32_dpp v141, v141, v141 quad_perm:[2,3,0,1] row_mask:0xf bank_mask:0xf\n"
  "v_add_f32_dpp v142, v142, v142 quad_perm:[2,3,0,1] row_mask:0xf bank_mask:0xf\n"
  "v_add_f32_dpp v143, v143, v143 quad_perm:[2,3,0,1] row_mask:0xf bank_mask:0xf\n"
  "v_add_f32_dpp v144, v144, v144 quad_perm:[2,3,0,1] row_mask:0xf bank_mask:0xf\n"
  "v_add_f32_dpp v145, v145, v145 quad_perm:[2,3,0,1] row_mask:0xf bank_mask:0xf\n"
  "v_add_f32_dpp v146, v146, v146 quad_perm:[2,3,0,1] row_mask:0xf bank_mask:0xf\n"
  "v_add_f32_dpp v147, v147, v147 quad_perm:[2,3,0,1] row_mask:0xf bank_mask:0xf\n"
  "v_add_f32_dpp v148, v148, v148 quad_perm:[2,3,0,1] row_mask:0xf bank_mask:0xf\n"
  "v_add_f32_dpp v149, v149, v149 quad_perm:[2,3,0,1] row_mask:0xf bank_mask:0xf\n"
  "v_cvt_pk_bf16_f32 v132, v132, v133\n"
  "v_cvt_pk_bf16_f32 v133, v134, v135\n"
  "v_cvt_pk_bf16_f32 v134, v136, v137\n"
  "v_cvt_pk_bf16_f32 v135, v138, v139\n"
  "v_cvt_pk_bf16_f32 v136, v140, v141\n"
  "v_cvt_pk_bf16_f32 v137, v142, v143\n"
  "v_cvt_pk_bf16_f32 v138, v144, v145\n"
  "v_cvt_pk_bf16_f32 v139, v146, v147\n"
  "v_cvt_pk_bf16_f32 v140, v148, v149\n"
  "v_cndmask_b32 v132, v132, v134, s[10:11]\n"
  "v_cndmask_b32 v133, v133, v135, s[10:11]\n"
  "v_cndmask_b32 v132, v132, v136, s[12:13]\n"
  "v_cndmask_b32 v133, v133, v137, s[12:13]\n"
  "v_cndmask_b32 v132, v132, v138, s[14:15]\n"
  "v_cndmask_b32 v133, v133, v139, s[14:15]\n"
  "ds_write_b64 v3, v[132:133]\n"
  "s_mov_b64 exec, s[8:9]\n"
  "ds_write_b32 v3, v140 offset:32\n"
  "s_mov_b64 exec, -1\n"
  "ds_read_b128 v[152:155], v4\n"
  "v_cvt_pk_bf16_f32 v48, v48, v49\n"
  "v_cvt_pk_bf16_f32 v49, v50, v51\n"
  "v_cvt_pk_bf16_f32 v52, v52, v53\n"
  "v_cvt_pk_bf16_f32 v53, v54, v55\n"
  "v_cvt_pk_bf16_f32 v56, v56, v57\n"
  "v_cvt_pk_bf16_f32 v57, v58, v59\n"
  "v_cvt_pk_bf16_f32 v60, v60, v61\n"
  "v_cvt_pk_bf16_f32 v61, v62, v63\n"
  "ds_write_b64 v10, v[48:49] offset:0\n"
  "ds_write_b64 v10, v[52:53] offset:4864\n"
  "ds_write_b64 v11, v[56:57] offset:0\n"
  "ds_write_b64 v11, v[60:61] offset:4864\n"
  "s_mov_b64 exec, s[68:69]\n"
  "global_store_dword v157, v156, s[62:63] sc1\n"
  "s_bfm_b64 exec, 40, 0\n"
  "s_waitcnt lgkmcnt(4)\n"
  "global_store_dwordx4 v5, v[152:155], s[60:61] sc1\n"
  "s_mov_b64 exec, -1\n"
  "s_add_u32 s60, s60, 40960\n"
  "s_addc_u32 s61, s61, 0\n"
  "s_add_u32 s62, s62, 2048\n"
  "s_addc_u32 s63, s63, 0\n"
  "s_add_u32 s56, s56, 1\n"
  "s_branch ALOOP\n"
  "AEND:\n"
  "s_waitcnt vmcnt(0) lgkmcnt(0)\n"
  "s_nop 3\n"
  "v_mov_b32 v8, v8\n"
  "s_nop 2\n"
  "v_mov_b32 v8, v8\n"
  "s_nop 2\n"
  "s_barrier\n"
  "ds_read_b128 v[68:71], v12 offset:9728\n"
  "ds_read_b128 v[72:75], v12 offset:9744\n"
  "ds_read_b32 v76, v12 offset:9760\n"
  "ds_read_b128 v[80:83], v64 offset:0\n"
  "ds_read_b128 v[84:87], v64 offset:16\n"
  "ds_read_b32 v88, v64 offset:32\n"
  "ds_read_b128 v[92:95], v65 offset:0\n"
  "ds_read_b128 v[96:99], v65 offset:16\n"
  "ds_read_b32 v100, v65 offset:32\n"
  "ds_read_b32 v156, v6 offset:9728\n"
  "ds_read_b128 v[104:107], v64 offset:96\n"
  "ds_read_b128 v[108:111], v64 offset:112\n"
  "ds_read_b32 v112, v64 offset:128\n"
  "ds_read_b128 v[116:119], v65 offset:96\n"
  "ds_read_b128 v[120:123], v65 offset:112\n"
  "ds_read_b32 v124, v65 offset:128\n"
  "s_waitcnt lgkmcnt(12)\n"
  "v_dot2_f32_bf16 v128, v68, v80, 0\n"
  "s_waitcnt lgkmcnt(9)\n"
  "v_dot2_f32_bf16 v129, v68, v92, 0\n"
  "v_dot2_f32_bf16 v128, v69, v81, v128\n"
  "v_dot2_f32_bf16 v129, v69, v93, v129\n"
  "v_dot2_f32_bf16 v128, v70, v82, v128\n"
  "v_dot2_f32_bf16 v129, v70, v94, v129\n"
  "v_dot2_f32_bf16 v128, v71, v83, v128\n"
  "v_dot2_f32_bf16 v129, v71, v95, v129\n"
  "v_dot2_f32_bf16 v128, v72, v84, v128\n"
  "s_waitcnt lgkmcnt(8)\n"
  "v_dot2_f32_bf16 v129, v72, v96, v129\n"
  "v_dot2_f32_bf16 v128, v73, v85, v128\n"
  "v_dot2_f32_bf16 v129, v73, v97, v129\n"
  "v_dot2_f32_bf16 v128, v74, v86, v128\n"
  "v_dot2_f32_bf16 v129, v74, v98, v129\n"
  "v_dot2_f32_bf16 v128, v75, v87, v128\n"
  "v_dot2_f32_bf16 v129, v75, v99, v129\n"
  "v_dot2_f32_bf16 v128, v76, v88, v128\n"
  "s_waitcnt lgkmcnt(7)\n"
  "v_dot2_f32_bf16 v129, v76, v100, v129\n"
  "s_waitcnt lgkmcnt(6)\n"
  "v_lshlrev_b32 v156, 16, v156\n"
  "v_mul_f32 v156, 0xbfb8aa3b, v156\n"
  "v_exp_f32 v156, v156\n"
  "v_cvt_pk_bf16_f32 v128, v128, v128\n"
  "v_lshlrev_b32 v128, 16, v128\n"
  "v_mul_f32 v128, 0x3e715bef, v128\n"
  "v_cvt_pk_bf16_f32 v128, v128, v128\n"
  "v_lshlrev_b32 v128, 16, v128\n"
  "v_cndmask_b32 v128, v158, v128, s[40:41]\n"
  "v_cvt_pk_bf16_f32 v129, v129, v129\n"
  "v_lshlrev_b32 v129, 16, v129\n"
  "v_mul_f32 v129, 0x3e715bef, v129\n"
  "v_cvt_pk_bf16_f32 v129, v129, v129\n"
  "v_lshlrev_b32 v129, 16, v129\n"
  "v_cndmask_b32 v129, v158, v129, s[42:43]\n"
  "v_max_f32 v150, v128, v129\n"
  "s_waitcnt lgkmcnt(2)\n"
  "v_perm_b32 v132, v116, v104, s66\n"
  "v_perm_b32 v133, v116, v104, s67\n"
  "v_max_f32_dpp v150, v150, v150 quad_perm:[1,0,3,2] row_mask:0xf bank_mask:0xf\n"
  "v_perm_b32 v134, v117, v105, s66\n"
  "v_perm_b32 v135, v117, v105, s67\n"
  "v_max_f32_dpp v150, v150, v150 quad_perm:[2,3,0,1] row_mask:0xf bank_mask:0xf\n"
  "v_add_f32 v156, 1.0, v156\n"
  "v_rcp_f32 v156, v156\n"
  "v_sub_f32 v128, v128, v150\n"
  "v_mul_f32 v128, 0x3fb8aa3b, v128\n"
  "v_exp_f32 v128, v128\n"
  "v_sub_f32 v129, v129, v150\n"
  "v_mul_f32 v129, 0x3fb8aa3b, v129\n"
  "v_exp_f32 v129, v129\n"
  "v_cvt_pk_bf16_f32 v156, v156, v156\n"
  "v_lshlrev_b32 v156, 16, v156\n"
  "v_add_f32 v150, v128, v129\n"
  "v_perm_b32 v136, v118, v106, s66\n"
  "v_perm_b32 v137, v118, v106, s67\n"
  "v_add_f32_dpp v150, v150, v150 quad_perm:[1,0,3,2] row_mask:0xf bank_mask:0xf\n"
  "v_perm_b32 v138, v119, v107, s66\n"
  "v_perm_b32 v139, v119, v107, s67\n"
  "v_add_f32_dpp v150, v150, v150 quad_perm:[2,3,0,1] row_mask:0xf bank_mask:0xf\n"
  "v_rcp_f32 v150, v150\n"
  "s_mov_b64 s[40:41], -1\n"
  "s_mov_b64 s[42:43], -1\n"
  "v_mul_f32 v128, v150, v128\n"
  "v_mul_f32 v129, v150, v129\n"
  "v_cvt_pk_bf16_f32 v130, v128, v129\n"
  "s_waitcnt lgkmcnt(1)\n"
  "v_perm_b32 v140, v120, v108, s66\n"
  "v_perm_b32 v141, v120, v108, s67\n"
  "v_perm_b32 v142, v121, v109, s66\n"
  "v_perm_b32 v143, v121, v109, s67\n"
  "v_perm_b32 v144, v122, v110, s66\n"
  "v_perm_b32 v145, v122, v110, s67\n"
  "v_perm_b32 v146, v123, v111, s66\n"
  "v_perm_b32 v147, v123, v111, s67\n"
  "s_waitcnt lgkmcnt(0)\n"
  "v_perm_b32 v148, v124, v112, s66\n"
  "v_perm_b32 v149, v124, v112, s67\n"
  "v_dot2_f32_bf16 v132, v132, v130, 0\n"
  "v_dot2_f32_bf16 v133, v133, v130, 0\n"
  "v_dot2_f32_bf16 v134, v134, v130, 0\n"
  "v_dot2_f32_bf16 v135, v135, v130, 0\n"
  "v_dot2_f32_bf16 v136, v136, v130, 0\n"
  "v_dot2_f32_bf16 v137, v137, v130, 0\n"
  "v_dot2_f32_bf16 v138, v138, v130, 0\n"
  "v_dot2_f32_bf16 v139, v139, v130, 0\n"
  "v_dot2_f32_bf16 v140, v140, v130, 0\n"
  "v_dot2_f32_bf16 v141, v141, v130, 0\n"
  "v_dot2_f32_bf16 v142, v142, v130, 0\n"
  "v_dot2_f32_bf16 v143, v143, v130, 0\n"
  "v_dot2_f32_bf16 v144, v144, v130, 0\n"
  "v_dot2_f32_bf16 v145, v145, v130, 0\n"
  "v_dot2_f32_bf16 v146, v146, v130, 0\n"
  "v_dot2_f32_bf16 v147, v147, v130, 0\n"
  "v_dot2_f32_bf16 v148, v148, v130, 0\n"
  "v_dot2_f32_bf16 v149, v149, v130, 0\n"
  "v_add_f32_dpp v132, v132, v132 quad_perm:[1,0,3,2] row_mask:0xf bank_mask:0xf\n"
  "v_add_f32_dpp v133, v133, v133 quad_perm:[1,0,3,2] row_mask:0xf bank_mask:0xf\n"
  "v_add_f32_dpp v134, v134, v134 quad_perm:[1,0,3,2] row_mask:0xf bank_mask:0xf\n"
  "v_add_f32_dpp v135, v135, v135 quad_perm:[1,0,3,2] row_mask:0xf bank_mask:0xf\n"
  "v_add_f32_dpp v136, v136, v136 quad_perm:[1,0,3,2] row_mask:0xf bank_mask:0xf\n"
  "v_add_f32_dpp v137, v137, v137 quad_perm:[1,0,3,2] row_mask:0xf bank_mask:0xf\n"
  "v_add_f32_dpp v138, v138, v138 quad_perm:[1,0,3,2] row_mask:0xf bank_mask:0xf\n"
  "v_add_f32_dpp v139, v139, v139 quad_perm:[1,0,3,2] row_mask:0xf bank_mask:0xf\n"
  "v_add_f32_dpp v140, v140, v140 quad_perm:[1,0,3,2] row_mask:0xf bank_mask:0xf\n"
  "v_add_f32_dpp v141, v141, v141 quad_perm:[1,0,3,2] row_mask:0xf bank_mask:0xf\n"
  "v_add_f32_dpp v142, v142, v142 quad_perm:[1,0,3,2] row_mask:0xf bank_mask:0xf\n"
  "v_add_f32_dpp v143, v143, v143 quad_perm:[1,0,3,2] row_mask:0xf bank_mask:0xf\n"
  "v_add_f32_dpp v144, v144, v144 quad_perm:[1,0,3,2] row_mask:0xf bank_mask:0xf\n"
  "v_add_f32_dpp v145, v145, v145 quad_perm:[1,0,3,2] row_mask:0xf bank_mask:0xf\n"
  "v_add_f32_dpp v146, v146, v146 quad_perm:[1,0,3,2] row_mask:0xf bank_mask:0xf\n"
  "v_add_f32_dpp v147, v147, v147 quad_perm:[1,0,3,2] row_mask:0xf bank_mask:0xf\n"
  "v_add_f32_dpp v148, v148, v148 quad_perm:[1,0,3,2] row_mask:0xf bank_mask:0xf\n"
  "v_add_f32_dpp v149, v149, v149 quad_perm:[1,0,3,2] row_mask:0xf bank_mask:0xf\n"
  "v_add_f32_dpp v132, v132, v132 quad_perm:[2,3,0,1] row_mask:0xf bank_mask:0xf\n"
  "v_add_f32_dpp v133, v133, v133 quad_perm:[2,3,0,1] row_mask:0xf bank_mask:0xf\n"
  "v_add_f32_dpp v134, v134, v134 quad_perm:[2,3,0,1] row_mask:0xf bank_mask:0xf\n"
  "v_add_f32_dpp v135, v135, v135 quad_perm:[2,3,0,1] row_mask:0xf bank_mask:0xf\n"
  "v_add_f32_dpp v136, v136, v136 quad_perm:[2,3,0,1] row_mask:0xf bank_mask:0xf\n"
  "v_add_f32_dpp v137, v137, v137 quad_perm:[2,3,0,1] row_mask:0xf bank_mask:0xf\n"
  "v_add_f32_dpp v138, v138, v138 quad_perm:[2,3,0,1] row_mask:0xf bank_mask:0xf\n"
  "v_add_f32_dpp v139, v139, v139 quad_perm:[2,3,0,1] row_mask:0xf bank_mask:0xf\n"
  "v_add_f32_dpp v140, v140, v140 quad_perm:[2,3,0,1] row_mask:0xf bank_mask:0xf\n"
  "v_add_f32_dpp v141, v141, v141 quad_perm:[2,3,0,1] row_mask:0xf bank_mask:0xf\n"
  "v_add_f32_dpp v142, v142, v142 quad_perm:[2,3,0,1] row_mask:0xf bank_mask:0xf\n"
  "v_add_f32_dpp v143, v143, v143 quad_perm:[2,3,0,1] row_mask:0xf bank_mask:0xf\n"
  "v_add_f32_dpp v144, v144, v144 quad_perm:[2,3,0,1] row_mask:0xf bank_mask:0xf\n"
  "v_add_f32_dpp v145, v145, v145 quad_perm:[2,3,0,1] row_mask:0xf bank_mask:0xf\n"
  "v_add_f32_dpp v146, v146, v146 quad_perm:[2,3,0,1] row_mask:0xf bank_mask:0xf\n"
  "v_add_f32_dpp v147, v147, v147 quad_perm:[2,3,0,1] row_mask:0xf bank_mask:0xf\n"
  "v_add_f32_dpp v148, v148, v148 quad_perm:[2,3,0,1] row_mask:0xf bank_mask:0xf\n"
  "v_add_f32_dpp v149, v149, v149 quad_perm:[2,3,0,1] row_mask:0xf bank_mask:0xf\n"
  "v_cvt_pk_bf16_f32 v132, v132, v133\n"
  "v_cvt_pk_bf16_f32 v133, v134, v135\n"
  "v_cvt_pk_bf16_f32 v134, v136, v137\n"
  "v_cvt_pk_bf16_f32 v135, v138, v139\n"
  "v_cvt_pk_bf16_f32 v136, v140, v141\n"
  "v_cvt_pk_bf16_f32 v137, v142, v143\n"
  "v_cvt_pk_bf16_f32 v138, v144, v145\n"
  "v_cvt_pk_bf16_f32 v139, v146, v147\n"
  "v_cvt_pk_bf16_f32 v140, v148, v149\n"
  "v_cndmask_b32 v132, v132, v134, s[10:11]\n"
  "v_cndmask_b32 v133, v133, v135, s[10:11]\n"
  "v_cndmask_b32 v132, v132, v136, s[12:13]\n"
  "v_cndmask_b32 v133, v133, v137, s[12:13]\n"
  "v_cndmask_b32 v132, v132, v138, s[14:15]\n"
  "v_cndmask_b32 v133, v133, v139, s[14:15]\n"
  "ds_write_b64 v3, v[132:133]\n"
  "s_mov_b64 exec, s[8:9]\n"
  "ds_write_b32 v3, v140 offset:32\n"
  "s_mov_b64 exec, -1\n"
  "ds_read_b128 v[152:155], v4\n"
  "s_mov_b64 exec, s[68:69]\n"
  "global_store_dword v157, v156, s[62:63] sc1\n"
  "s_bfm_b64 exec, 40, 0\n"
  "s_waitcnt lgkmcnt(0)\n"
  "global_store_dwordx4 v5, v[152:155], s[60:61] sc1\n"
  "s_mov_b64 exec, -1\n"
  "s_add_u32 s60, s60, 40960\n"
  "s_addc_u32 s61, s61, 0\n"
  "s_add_u32 s62, s62, 2048\n"
  "s_addc_u32 s63, s63, 0\n"
  "s_mov_b32 s56, 64\n"
  "s_waitcnt vmcnt(0)\n"
  "s_barrier\n"
  "v_mov_b32 v7, 0\n"
  "v_mov_b32 v8, s56\n"
  "s_cmp_eq_u32 s47, 0\n"
  "s_cselect_b64 exec, 1, 0\n"
  "global_store_dword v7, v8, s[64:65] sc1\n"
  "s_mov_b64 exec, -1\n"
  "s_endpgm\n"
      : "+{v[0:15]}"(vin), "+{s[36:43]}"(sa), "+{s[44:59]}"(sb) :: CLOB);
  }
}

extern "C" void kernel_launch(void* const* d_in, const int* in_sizes, int n_in, void* d_out, int out_size, void* d_ws, size_t ws_size,
                              hipStream_t stream) {
  Params p;
  p.x = (const u16*)d_in[0]; p.wqkv = (const u16*)d_in[1]; p.wo_a = (const u16*)d_in[2];
  p.mem0 = (const u16*)d_in[4]; p.Wq = (const u16*)d_in[5]; p.Wk = (const u16*)d_in[6]; p.Wv = (const u16*)d_in[7];
  p.Wo = (const u16*)d_in[8]; p.gate = (const u16*)d_in[10];
  p.out = (u16*)d_out; p.ws = (unsigned char*)d_ws + ((0x200000ull - ((unsigned long long)d_ws & 0x1FFFFFull)) & 0x1FFFFFull);
  (void)hipFuncSetAttribute((const void*)gm_kernel, hipFuncAttributeMaxDynamicSharedMemorySize, LDS_BYTES);
  hipLaunchKernelGGL(gm_setup, dim3(32), dim3(512), 0, stream, p);
  hipLaunchKernelGGL(gm_kernel, dim3(256), dim3(256), LDS_BYTES, stream, p);
}
```

```cpp
#include <hip/hip_runtime.h>
#include <stdint.h>
#define LAS __attribute__((address_space(3)))
#define GAS __attribute__((address_space(1)))
typedef unsigned v2u __attribute__((ext_vector_type(2)));
typedef unsigned v4u __attribute__((ext_vector_type(4)));
typedef unsigned v8u __attribute__((ext_vector_type(8)));
typedef unsigned v16u __attribute__((ext_vector_type(16)));
typedef unsigned v32u __attribute__((ext_vector_type(32)));
typedef float f4v __attribute__((ext_vector_type(4)));
typedef short s4v __attribute__((ext_vector_type(4)));
typedef __bf16 b8v __attribute__((ext_vector_type(8)));
typedef __bf16 b2v __attribute__((ext_vector_type(2)));
typedef unsigned short u16;
typedef GAS unsigned gu32;
typedef LAS unsigned char* lptr;
#define RLX_AGENT __ATOMIC_RELAXED, __HIP_MEMORY_SCOPE_AGENT

constexpr int DM = 576, NSEG = 64, LDS_BYTES = 160 * 1024, SPIN = 400000;
constexpr unsigned CH = 32768;
constexpr unsigned CTL = 16u << 20, CTL_BYTES = 11 * 16384;
constexpr unsigned C_TICK = CTL, C_OVF = CTL + 1024, C_TAB = CTL + CTL_BYTES;
constexpr unsigned C_REP = CTL + 16384, C_KVP = CTL + 163840, C_HF = CTL + 24576, C_VF = CTL + 40960, C_QAP = CTL + 57344, C_AP = C_QAP + 4096;
constexpr unsigned WO_B = 20u << 20;
constexpr unsigned KVH = 0;
constexpr unsigned WO_KM = 150u << 20, WO_VP = 154u << 20;
constexpr unsigned WO_OA = 158u << 20;
constexpr unsigned WO_G = 161u << 20;
constexpr unsigned WO_ST = 162u << 20, WO_DB = 168u << 20;
constexpr float QS = 0.08838834764831845f * 1.4426950408889634f;

struct Params { const u16* x; const u16* wqkv; const u16* wo_a; const u16* mem0; const u16* Wq; const u16* Wk; const u16* Wv; const u16* Wo; const u16* gate;
                u16* out; unsigned char* ws; };

__device__ __forceinline__ unsigned xcc_id() { return (unsigned)__builtin_amdgcn_s_getreg(20 | (3 << 11)) & 0xfu; }
__device__ __forceinline__ unsigned rfl(unsigned v) { return __builtin_amdgcn_readfirstlane(v); }
__device__ __forceinline__ f4v mfma32(v4u a, v4u b, f4v c) { return __builtin_amdgcn_mfma_f32_16x16x32_bf16(__builtin_bit_cast(b8v, a), __builtin_bit_cast(b8v, b), c, 0, 0, 0); }
__device__ __forceinline__ unsigned pack2(float lo, float hi) { b2v p; p.x = (__bf16)lo; p.y = (__bf16)hi; return __builtin_bit_cast(unsigned, p); }
__device__ __forceinline__ v4u pack8(f4v a, f4v b) { return (v4u){pack2(a.x, a.y), pack2(a.z, a.w), pack2(b.x, b.y), pack2(b.z, b.w)}; }
__device__ __forceinline__ float rbf(float x) { return (float)(__bf16)x; }
__device__ __forceinline__ float bflo(unsigned u) { return __uint_as_float(u << 16); }
__device__ __forceinline__ float bfhi(unsigned u) { return __uint_as_float(u & 0xffff0000u); }
__device__ __forceinline__ v2u lds_tr(lptr p) { return __builtin_bit_cast(v2u, __builtin_amdgcn_ds_read_tr16_b64_v4i16((LAS s4v*)p)); }
__device__ __forceinline__ v4u lds16(lptr p) { return *(LAS v4u*)p; }
#define WAIT_VM(n) asm volatile("s_waitcnt vmcnt(" #n ")" ::: "memory")
__device__ __forceinline__ void wg_barrier() { asm volatile("s_waitcnt lgkmcnt(0)\n\ts_barrier" ::: "memory"); }
#define DMA_BODY(FL) unsigned keep; asm volatile("s_mov_b32 %0, m0\n\ts_mov_b32 m0, %3\n\ts_nop 0\n\tglobal_load_lds_dwordx4 %1, %2" FL "\n\ts_mov_b32 m0, %0" \
                                              : "=&s"(keep) : "v"(off), "s"(base), "s"(dst) : "memory")
__device__ __forceinline__ void dma(const void* base, unsigned off, unsigned dst) { DMA_BODY(""); }
__device__ __forceinline__ void dma_dev(const void* base, unsigned off, unsigned dst) { DMA_BODY(" sc1"); }
__device__ __forceinline__ void st16(unsigned char* ws, unsigned off, v4u v) { asm volatile("global_store_dwordx4 %0, %1, %2 sc1\n\ts_nop 1" :: "v"(off), "v"(v), "s"(ws) : "memory"); }
__device__ __forceinline__ unsigned ctl_ld(unsigned char* ws, unsigned off) { return __hip_atomic_load((gu32*)(ws + off), RLX_AGENT); }
__device__ __forceinline__ void ctl_st(unsigned char* ws, unsigned off, unsigned v) { __hip_atomic_store((gu32*)(ws + off), v, RLX_AGENT); }
__device__ __forceinline__ unsigned ctl_add(unsigned char* ws, unsigned off, unsigned v) { return __hip_atomic_fetch_add((gu32*)(ws + off), v, RLX_AGENT); }
__device__ __forceinline__ bool wait_ge(unsigned char* ws, unsigned off, unsigned want) {
  for (int i = 0; i < SPIN; ++i) {
    if (__all(ctl_ld(ws, off) >= want)) return true;
    __builtin_amdgcn_s_sleep(4);
  }
  return false;
}
__device__ __forceinline__ unsigned chunk(unsigned long long map, unsigned g, unsigned c, unsigned off) {
  const unsigned m = (unsigned)(map >> (16 * g));
  return (m & 7) * 0x200000u + c * CH + (((m >> (3 + 3 * (c >> 4))) & 7) << 12) + off;
}
__device__ __forceinline__ float xor16(float v) { auto s = __builtin_amdgcn_permlane16_swap(__float_as_uint(v), __float_as_uint(v), false, false); return __uint_as_float((threadIdx.x & 16) ? s[0] : s[1]); }
__device__ __forceinline__ float xor32(float v) { auto s = __builtin_amdgcn_permlane32_swap(__float_as_uint(v), __float_as_uint(v), false, false); return __uint_as_float((threadIdx.x & 32) ? s[0] : s[1]); }
template <int C> __device__ __forceinline__ float dpp(float v) { return __int_as_float(__builtin_amdgcn_update_dpp(0, __float_as_int(v), C, 0xf, 0xf, true)); }
#define STAMP(s, i) do { } while (0)
__device__ __forceinline__ unsigned x_src(int wave, int lane) { return (8 * wave + (lane >> 3)) * 1152 + ((lane & 7) ^ ((4 * wave + (lane >> 4)) & 7)) * 16; }
__device__ __forceinline__ unsigned x_frag(int lane) { return (lane & 15) * 128 + ((lane >> 4) ^ ((lane >> 1) & 7)) * 16; }

#define UNIFORM(v, n) _Pragma("unroll") for (int i_ = 0; i_ < n; ++i_) v[i_] = rfl(v[i_])
#define PTR(v, i, ptr) do { const unsigned long long a_ = (unsigned long long)(ptr); v[i] = (unsigned)a_; v[(i) + 1] = (unsigned)(a_ >> 32); } while (0)
#define CLOB "v255", "a255", "s101", "vcc", "memory"

__global__ void __launch_bounds__(512) gm_setup(Params p) {
  __shared__ unsigned L[8];
  const int b = blockIdx.x, tid = threadIdx.x, lane = tid & 63, wave = rfl(tid >> 6);
  const unsigned base = b < 32 ? (b & 7) * 0x200000u + (b >> 3) * 16 * CH : b < 96 ? WO_B + (b - 32) * 0x200000u : CTL + 0x100000u;
  if (wave == 0) {
    unsigned b1 = 0, tp = 0, m[4] = {~0u, ~0u, ~0u, ~0u};
#pragma unroll 1
    for (int r = -2; r < (b < 32 ? 4 : 1); ++r) {
      const unsigned k = r == -2 ? lane & 7 : r == -1 ? 2 : b1 | (r & 1) | ((r & 2) << 1), off = base + (k << 12);
      unsigned v = 0; unsigned long long t0, t1; const unsigned long long on = r == -2 ? 255 : 1;
      asm volatile("s_mov_b64 exec, %5\n\ts_memtime %1\n\ts_waitcnt lgkmcnt(0)\n\tglobal_atomic_add %0, %3, %0, %4 sc0 sc1\n\ts_waitcnt vmcnt(0)\n\t"
                   "s_memtime %2\n\ts_waitcnt lgkmcnt(0)\n\ts_mov_b64 exec, -1" : "+v"(v), "=&s"(t0), "=&s"(t1) : "v"(off), "s"(p.ws), "s"(on) : "memory");
      unsigned t = (unsigned)(t1 - t0);
      if (r == 0 && tp < t) { b1 = 2; t = tp; }
      tp = t;
#pragma unroll
      for (int q = 0; q < 4; ++q) if (r >= 0 && (r & 3) == q) m[q] = min(m[q], t);
    }
    if (lane == 0) { L[0] = (m[1] + m[3] < m[0] + m[2]) | b1 | ((m[2] + m[3] < m[0] + m[1]) << 2); L[4] = m[0]; L[5] = m[1]; L[6] = m[2]; L[7] = m[3]; }
  }
  __syncthreads();
  const unsigned cls = L[0];
  if (b >= 32) {
    if (tid == 0) ctl_st(p.ws, C_TAB + (b < 96 ? 128 + b * 4 : 132), ((cls >> 1) ^ (xcc_id() >> 2) ^ 1) & 1);
    if (b < 96 && tid < CTL_BYTES / 1024) st16(p.ws, CTL + ((b - 32) * (CTL_BYTES / 1024) + tid) * 16, (v4u){0, 0, 0, 0});
    return;
  }
#pragma unroll
  for (int i = 0; i < 8; ++i) {
    const int c = 2 * i + (tid >> 8), pos = tid & 255;
    v4u v = {~0u, ~0u, ~0u, ~0u};
    if (i < 3 && b < 8 && (c < 4 || (c == 4 && pos < 128))) {
      const int line = c < 4 ? (c & 1) * 32 + (pos >> 3) : 64 + ((pos >> 3) & 7), j = pos & 7, m = 2 * (line & 3) + (j & 1), col = 32 * (line >> 2) + (j >> 1);
#pragma unroll
      for (int e = 0; e < 4; ++e) {
        const unsigned lo = p.mem0[m * DM + col + 8 * e], hi = p.mem0[m * DM + col + 8 * e + 4];
        v[e] = ((lo & 0x7fff) > 0x7f81 ? 0x7f81 : lo) | (((hi & 0x7fff) > 0x7f81 ? 0x7f81 : hi) << 16);
      }
    }
    st16(p.ws, base + c * CH + (cls << 12) + pos * 16, v);
  }
  if (tid == 0) ctl_st(p.ws, C_TAB + b * 4, cls | (xcc_id() << 8));
  if (tid == 0 && b == 0) ctl_st(p.ws, C_TAB + 128, (unsigned)__builtin_amdgcn_s_memrealtime() * 24u);
}

__global__ void __launch_bounds__(256, 1) gm_kernel(Params p) {
  extern __shared__ __attribute__((aligned(16))) unsigned char smem[];
  const lptr lds = (lptr)smem;
  const int tid = threadIdx.x, lane = tid & 63, wave = rfl(tid >> 6);
  const unsigned tab = *(const gu32*)(p.ws + C_TAB + min(lane, 33) * 4);
  const unsigned long long pol = __ballot(*(const gu32*)(p.ws + C_TAB + 256 + lane * 4) == 1u);
  if (wave == 0) {
    const unsigned x = xcc_id() & 7;
    unsigned t = 0;
    if (lane == 0) t = ctl_add(p.ws, C_TICK + x * 128, 1u);
    t = rfl(t);
    int role = x * 32 + t;
    if (t >= 32) {
      unsigned j = 0;
      if (lane == 0) j = ctl_add(p.ws, C_OVF, 1u);
      j = rfl(j); role = -1;
      for (int i = 0; i < SPIN && role < 0; ++i) {
        const unsigned cnt = ctl_ld(p.ws, C_TICK + (lane & 7) * 128);
        unsigned sum = 0, left = j; int r = -1;
        for (int d = 0; d < 8; ++d) {
          const unsigned cd = __builtin_amdgcn_readlane(cnt, d), open = cd < 32 ? 32 - cd : 0;
          sum += cd;
          if (r < 0) { if (left < open) r = d * 32 + cd + left; else left -= open; }
        }
        if (sum == 256) role = r;
      }
    }
    if (lane == 0) *(LAS int*)lds = role;
  }
  wg_barrier();
  const int role = rfl(*(LAS int*)lds);
  wg_barrier();
  if (role < 0) return;
  STAMP(63, 15);
  const int n = lane & 15, g = lane >> 4, qq = n >> 2, c4 = n & 3, gam = role >> 5, tk = role & 31;
  const unsigned cx = __builtin_amdgcn_readlane(tab, 33) == 1u ? 0u : 8192u;
  v16u vin; v8u sa; v16u sb;
#pragma unroll
  for (int i = 0; i < 16; ++i) { vin[i] = 0; sb[i] = 0; }
#pragma unroll
  for (int i = 0; i < 8; ++i) sa[i] = 0;
  PTR(sa, 0, p.ws);
  vin[0] = lane * 16;
  if (gam < 4) {
    const unsigned m1pat = ((lane >> 5) * 4 + qq) * 128 + (g & 1) * 64 + c4 * 16, idx = (lane & 2) ? 0xEEEEu : 0x4444u;
    const int trrow = 16 * (g & 1) + 2 * (g >> 1) + 4 * (qq & 1) + (qq >> 1);
    unsigned long long cls = 0;
    {
      unsigned used = 0; int pg[4];
#pragma unroll
      for (int i = 0; i < 4; ++i) { const unsigned m = (unsigned)__ballot(lane < 8 && (tab >> 8) == (unsigned)i); pg[i] = m ? __builtin_ctz(m) : -1; if (m) used |= 1u << pg[i]; }
#pragma unroll
      for (int i = 0; i < 4; ++i) {
        if (pg[i] < 0) { pg[i] = __builtin_ctz(~used); used |= 1u << pg[i]; }
        unsigned m = pg[i];
#pragma unroll
        for (int k = 0; k < 4; ++k) m |= (__builtin_amdgcn_readlane(tab, pg[i] + 8 * k) & 7) << (3 + 3 * k);
        cls |= (unsigned long long)m << (16 * i);
      }
    }
    sb[4] = wave; sb[10] = WO_ST + (role * 4 + wave) * 4096;
    if (tk < 16) {
      const int h = gam * 8 + (tk >> 1), half = tk & 1, flip = gam & 1;
      vin[1] = m1pat + wave * 1024; vin[2] = qq * 256 + g * 64 + c4 * 16;
      vin[3] = 42240 + (32 * wave + n) * 32 + 8 * (g ^ qq);
      vin[4] = 42240 + 4096 + (16 * wave + n) * 32 + 8 * (g ^ qq);
      vin[5] = 42240 + (4 * g + qq) * 32 + 8 * (c4 ^ g);
      vin[6] = idx; vin[7] = (lane & 2) ? 0x0c0c0c0cu : 0x07060302u; vin[8] = (lane & 2) ? 0x07060302u : 0x0c0c0c0cu;
#pragma unroll
      for (int i = 0; i < 3; ++i) {
        const int cg = i < 2 ? 8 * half + 2 * wave + (i ^ flip) : 16 + half, gr = (cg & 1) + 2 * (g >> 1), hs = ((0x3120 >> (4 * (gr ^ gam))) & 3) * 8 + (h & 7);
        vin[9 + i] = chunk(cls, gr, 28 + 4 * (cg >> 1) + (hs >> 4), (hs & 15) * 256 + (i == 2 ? (wave & 1) * 128 : 0) + (g & 1) * 64 + n * 4);
      }
      { const int row = 4 * (wave & 1) + 16 * (wave >> 1) + g, sw = 2 * ((row & 1) | ((row >> 1) & 2) | ((row >> 2) & 4)); vin[12] = row * 8192 + (n ^ sw) * 16; }
      { const int sw = 2 * ((trrow & 1) | ((trrow >> 1) & 2) | ((trrow >> 2) & 4)); vin[13] = trrow * 256 + ((4 * wave + (c4 >> 1)) ^ sw) * 16 + (c4 & 1) * 8; }
      vin[14] = (16 * wave + (lane >> 3)) * 1152 + ((lane & 7) ^ (2 * wave)) * 16;
      { const int row = 32 * (g & 1) + 4 * (g >> 1) + (qq & 1) + 16 * (qq >> 1); vin[15] = row * 128 + ((c4 >> 1) ^ (2 * ((row >> 4) & 3))) * 16 + (c4 & 1) * 8; }
      PTR(sa, 2, p.Wq + h * 128); PTR(sa, 4, p.Wk + h * 128); PTR(sa, 6, p.Wo + (size_t)h * 128 * DM + half * 256);
      sb[0] = chunk(cls, gam, 0, 0); sb[1] = (C_KVP ^ cx) + h * 256; sb[2] = chunk(cls, gam, 16 + ((h & 7) >> 1), (h & 1) * 2048);
      sb[3] = KVH + h * 65536 + (wave >> 1) * 16384 + (wave & 1) * 4096; sb[6] = (C_HF ^ cx) + h * 256; sb[7] = C_REP ^ cx; sb[8] = 1024 - 512 * half; sb[9] = WO_KM + h * 2048 + wave * 512;
      sb[11] = __float_as_uint(QS); sb[12] = flip * 64; sb[13] = gam * 128; sb[14] = 2500; sb[15] = 720;
      UNIFORM(sa, 8); UNIFORM(sb, 16);
      asm volatile(
  "s_nop 4\n"
  "s_lshl_b32 s80, s48, 10\n"
  "s_lshl_b32 s78, s48, 11\n"
  "s_and_b32 s79, s80, 1024\n"
  "s_and_b32 s74, s78, 4096\n"
  "s_add_u32 s79, s79, s74\n"
  "s_add_u32 m0, s79, 0\n"
  "s_add_u32 s74, s40, 0\n"
  "s_addc_u32 s75, s41, 0\n"
  "global_load_lds_dwordx4 v12, s[74:75]\n"
  "s_add_u32 m0, s79, 2048\n"
  "s_add_u32 s74, s40, 65536\n"
  "s_addc_u32 s75, s41, 0\n"
  "global_load_lds_dwordx4 v12, s[74:75]\n"
  "s_add_u32 m0, s79, 8192\n"
  "s_add_u32 s74, s40, 262144\n"
  "s_addc_u32 s75, s41, 0\n"
  "global_load_lds_dwordx4 v12, s[74:75]\n"
  "s_add_u32 m0, s79, 10240\n"
  "s_add_u32 s74, s40, 327680\n"
  "s_addc_u32 s75, s41, 0\n"
  "global_load_lds_dwordx4 v12, s[74:75]\n"
  "s_add_u32 m0, s79, 16384\n"
  "s_add_u32 s74, s40, 524288\n"
  "s_addc_u32 s75, s41, 0\n"
  "global_load_lds_dwordx4 v12, s[74:75]\n"
  "s_add_u32 m0, s79, 18432\n"
  "s_add_u32 s74, s40, 589824\n"
  "s_addc_u32 s75, s41, 0\n"
  "global_load_lds_dwordx4 v12, s[74:75]\n"
  "s_add_u32 m0, s79, 24576\n"
  "s_add_u32 s74, s40, 786432\n"
  "s_addc_u32 s75, s41, 0\n"
  "global_load_lds_dwordx4 v12, s[74:75]\n"
  "s_add_u32 m0, s79, 26624\n"
  "s_add_u32 s74, s40, 851968\n"
  "s_addc_u32 s75, s41, 0\n"
  "global_load_lds_dwordx4 v12, s[74:75]\n"
  "s_add_u32 m0, s79, 32768\n"
  "s_add_u32 s74, s40, 1048576\n"
  "s_addc_u32 s75, s41, 0\n"
  "global_load_lds_dwordx4 v12, s[74:75]\n"
  "s_add_u32 m0, s79, 34816\n"
  "s_add_u32 s74, s40, 1114112\n"
  "s_addc_u32 s75, s41, 0\n"
  "global_load_lds_dwordx4 v12, s[74:75]\n"
  "s_add_u32 m0, s79, 40960\n"
  "s_add_u32 s74, s40, 1310720\n"
  "s_addc_u32 s75, s41, 0\n"
  "global_load_lds_dwordx4 v12, s[74:75]\n"
  "s_add_u32 m0, s79, 43008\n"
  "s_add_u32 s74, s40, 1376256\n"
  "s_addc_u32 s75, s41, 0\n"
  "global_load_lds_dwordx4 v12, s[74:75]\n"
  "s_add_u32 m0, s79, 49152\n"
  "s_add_u32 s74, s40, 1572864\n"
  "s_addc_u32 s75, s41, 0\n"
  "global_load_lds_dwordx4 v12, s[74:75]\n"
  "s_add_u32 m0, s79, 51200\n"
  "s_add_u32 s74, s40, 1638400\n"
  "s_addc_u32 s75, s41, 0\n"
  "global_load_lds_dwordx4 v12, s[74:75]\n"
  "s_add_u32 m0, s79, 57344\n"
  "s_add_u32 s74, s40, 1835008\n"
  "s_addc_u32 s75, s41, 0\n"
  "global_load_lds_dwordx4 v12, s[74:75]\n"
  "s_add_u32 m0, s79, 59392\n"
  "s_add_u32 s74, s40, 1900544\n"
  "s_addc_u32 s75, s41, 0\n"
  "global_load_lds_dwordx4 v12, s[74:75]\n"
  "s_add_u32 m0, s79, 65536\n"
  "s_add_u32 s74, s40, 2097152\n"
  "s_addc_u32 s75, s41, 0\n"
  "global_load_lds_dwordx4 v12, s[74:75]\n"
  "s_add_u32 m0, s79, 67584\n"
  "s_add_u32 s74, s40, 2162688\n"
  "s_addc_u32 s75, s41, 0\n"
  "global_load_lds_dwordx4 v12, s[74:75]\n"
  "s_add_u32 m0, s79, 73728\n"
  "s_add_u32 s74, s40, 2359296\n"
  "s_addc_u32 s75, s41, 0\n"
  "global_load_lds_dwordx4 v12, s[74:75]\n"
  "s_add_u32 m0, s79, 75776\n"
  "s_add_u32 s74, s40, 2424832\n"
  "s_addc_u32 s75, s41, 0\n"
  "global_load_lds_dwordx4 v12, s[74:75]\n"
  "s_add_u32 m0, s79, 81920\n"
  "s_add_u32 s74, s40, 2621440\n"
  "s_addc_u32 s75, s41, 0\n"
  "global_load_lds_dwordx4 v12, s[74:75]\n"
  "s_add_u32 m0, s79, 83968\n"
  "s_add_u32 s74, s40, 2686976\n"
  "s_addc_u32 s75, s41, 0\n"
  "global_load_lds_dwordx4 v12, s[74:75]\n"
  "s_add_u32 m0, s79, 90112\n"
  "s_add_u32 s74, s40, 2883584\n"
  "s_addc_u32 s75, s41, 0\n"
  "global_load_lds_dwordx4 v12, s[74:75]\n"
  "s_add_u32 m0, s79, 92160\n"
  "s_add_u32 s74, s40, 2949120\n"
  "s_addc_u32 s75, s41, 0\n"
  "global_load_lds_dwordx4 v12, s[74:75]\n"
  "s_add_u32 m0, s79, 98304\n"
  "s_add_u32 s74, s40, 3145728\n"
  "s_addc_u32 s75, s41, 0\n"
  "global_load_lds_dwordx4 v12, s[74:75]\n"
  "s_add_u32 m0, s79, 100352\n"
  "s_add_u32 s74, s40, 3211264\n"
  "s_addc_u32 s75, s41, 0\n"
  "global_load_lds_dwordx4 v12, s[74:75]\n"
  "s_add_u32 m0, s79, 106496\n"
  "s_add_u32 s74, s40, 3407872\n"
  "s_addc_u32 s75, s41, 0\n"
  "global_load_lds_dwordx4 v12, s[74:75]\n"
  "s_add_u32 m0, s79, 108544\n"
  "s_add_u32 s74, s40, 3473408\n"
  "s_addc_u32 s75, s41, 0\n"
  "global_load_lds_dwordx4 v12, s[74:75]\n"
  "s_add_u32 m0, s79, 114688\n"
  "s_add_u32 s74, s40, 3670016\n"
  "s_addc_u32 s75, s41, 0\n"
  "global_load_lds_dwordx4 v12, s[74:75]\n"
  "s_add_u32 m0, s79, 116736\n"
  "s_add_u32 s74, s40, 3735552\n"
  "s_addc_u32 s75, s41, 0\n"
  "global_load_lds_dwordx4 v12, s[74:75]\n"
  "s_add_u32 m0, s79, 122880\n"
  "s_add_u32 s74, s40, 3932160\n"
  "s_addc_u32 s75, s41, 0\n"
  "global_load_lds_dwordx4 v12, s[74:75]\n"
  "s_add_u32 m0, s79, 124928\n"
  "s_add_u32 s74, s40, 3997696\n"
  "s_addc_u32 s75, s41, 0\n"
  "global_load_lds_dwordx4 v12, s[74:75]\n"
  "s_waitcnt vmcnt(28) lgkmcnt(0)\n"
  "s_barrier\n"
  "s_add_u32 m0, s79, 131072\n"
  "s_add_u32 s74, s40, 4194304\n"
  "s_addc_u32 s75, s41, 0\n"
  "global_load_lds_dwordx4 v12, s[74:75]\n"
  "s_add_u32 m0, s79, 133120\n"
  "s_add_u32 s74, s40, 4259840\n"
  "s_addc_u32 s75, s41, 0\n"
  "global_load_lds_dwordx4 v12, s[74:75]\n"
  "s_add_u32 m0, s79, 139264\n"
  "s_add_u32 s74, s40, 4456448\n"
  "s_addc_u32 s75, s41, 0\n"
  "global_load_lds_dwordx4 v12, s[74:75]\n"
  "s_add_u32 m0, s79, 141312\n"
  "s_add_u32 s74, s40, 4521984\n"
  "s_addc_u32 s75, s41, 0\n"
  "global_load_lds_dwordx4 v12, s[74:75]\n"
  "v_add_u32 v20, 0, v13\n"
  "v_xor_b32 v21, 32, v20\n"
  "ds_read_b64_tr_b16 a[0:1], v20 offset:0\n"
  "ds_read_b64_tr_b16 a[2:3], v20 offset:2048\n"
  "ds_read_b64_tr_b16 a[4:5], v20 offset:8192\n"
  "ds_read_b64_tr_b16 a[6:7], v20 offset:10240\n"
  "ds_read_b64_tr_b16 a[8:9], v21 offset:0\n"
  "ds_read_b64_tr_b16 a[10:11], v21 offset:2048\n"
  "ds_read_b64_tr_b16 a[12:13], v21 offset:8192\n"
  "ds_read_b64_tr_b16 a[14:15], v21 offset:10240\n"
  "s_waitcnt vmcnt(28) lgkmcnt(0)\n"
  "s_barrier\n"
  "s_add_u32 m0, s78, 0\n"
  "s_add_u32 s74, s42, 0\n"
  "s_addc_u32 s75, s43, 0\n"
  "global_load_lds_dwordx4 v14, s[74:75]\n"
  "s_add_u32 m0, s78, 1024\n"
  "s_add_u32 s74, s42, 9216\n"
  "s_addc_u32 s75, s43, 0\n"
  "global_load_lds_dwordx4 v14, s[74:75]\n"
  "s_add_u32 m0, s78, 8192\n"
  "s_add_u32 s74, s42, 73728\n"
  "s_addc_u32 s75, s43, 0\n"
  "global_load_lds_dwordx4 v14, s[74:75]\n"
  "s_add_u32 m0, s78, 9216\n"
  "s_add_u32 s74, s42, 82944\n"
  "s_addc_u32 s75, s43, 0\n"
  "global_load_lds_dwordx4 v14, s[74:75]\n"
  "v_add_u32 v20, 16384, v13\n"
  "v_xor_b32 v21, 32, v20\n"
  "ds_read_b64_tr_b16 a[16:17], v20 offset:0\n"
  "ds_read_b64_tr_b16 a[18:19], v20 offset:2048\n"
  "ds_read_b64_tr_b16 a[20:21], v20 offset:8192\n"
  "ds_read_b64_tr_b16 a[22:23], v20 offset:10240\n"
  "ds_read_b64_tr_b16 a[24:25], v21 offset:0\n"
  "ds_read_b64_tr_b16 a[26:27], v21 offset:2048\n"
  "ds_read_b64_tr_b16 a[28:29], v21 offset:8192\n"
  "ds_read_b64_tr_b16 a[30:31], v21 offset:10240\n"
  "s_waitcnt vmcnt(28) lgkmcnt(0)\n"
  "s_barrier\n"
  "s_add_u32 m0, s78, 16384\n"
  "s_add_u32 s74, s42, 128\n"
  "s_addc_u32 s75, s43, 0\n"
  "global_load_lds_dwordx4 v14, s[74:75]\n"
  "s_add_u32 m0, s78, 17408\n"
  "s_add_u32 s74, s42, 9344\n"
  "s_addc_u32 s75, s43, 0\n"
  "global_load_lds_dwordx4 v14, s[74:75]\n"
  "s_add_u32 m0, s78, 24576\n"
  "s_add_u32 s74, s42, 73856\n"
  "s_addc_u32 s75, s43, 0\n"
  "global_load_lds_dwordx4 v14, s[74:75]\n"
  "s_add_u32 m0, s78, 25600\n"
  "s_add_u32 s74, s42, 83072\n"
  "s_addc_u32 s75, s43, 0\n"
  "global_load_lds_dwordx4 v14, s[74:75]\n"
  "v_add_u32 v20, 32768, v13\n"
  "v_xor_b32 v21, 32, v20\n"
  "ds_read_b64_tr_b16 a[32:33], v20 offset:0\n"
  "ds_read_b64_tr_b16 a[34:35], v20 offset:2048\n"
  "ds_read_b64_tr_b16 a[36:37], v20 offset:8192\n"
  "ds_read_b64_tr_b16 a[38:39], v20 offset:10240\n"
  "ds_read_b64_tr_b16 a[40:41], v21 offset:0\n"
  "ds_read_b64_tr_b16 a[42:43], v21 offset:2048\n"
  "ds_read_b64_tr_b16 a[44:45], v21 offset:8192\n"
  "ds_read_b64_tr_b16 a[46:47], v21 offset:10240\n"
  "s_waitcnt vmcnt(28) lgkmcnt(0)\n"
  "s_barrier\n"
  "s_add_u32 m0, s78, 32768\n"
  "s_add_u32 s74, s42, 256\n"
  "s_addc_u32 s75, s43, 0\n"
  "global_load_lds_dwordx4 v14, s[74:75]\n"
  "s_add_u32 m0, s78, 33792\n"
  "s_add_u32 s74, s42, 9472\n"
  "s_addc_u32 s75, s43, 0\n"
  "global_load_lds_dwordx4 v14, s[74:75]\n"
  "s_add_u32 m0, s78, 40960\n"
  "s_add_u32 s74, s42, 73984\n"
  "s_addc_u32 s75, s43, 0\n"
  "global_load_lds_dwordx4 v14, s[74:75]\n"
  "s_add_u32 m0, s78, 41984\n"
  "s_add_u32 s74, s42, 83200\n"
  "s_addc_u32 s75, s43, 0\n"
  "global_load_lds_dwordx4 v14, s[74:75]\n"
  "v_add_u32 v20, 49152, v13\n"
  "v_xor_b32 v21, 32, v20\n"
  "ds_read_b64_tr_b16 a[48:49], v20 offset:0\n"
  "ds_read_b64_tr_b16 a[50:51], v20 offset:2048\n"
  "ds_read_b64_tr_b16 a[52:53], v20 offset:8192\n"
  "ds_read_b64_tr_b16 a[54:55], v20 offset:10240\n"
  "ds_read_b64_tr_b16 a[56:57], v21 offset:0\n"
  "ds_read_b64_tr_b16 a[58:59], v21 offset:2048\n"
  "ds_read_b64_tr_b16 a[60:61], v21 offset:8192\n"
  "ds_read_b64_tr_b16 a[62:63], v21 offset:10240\n"
  "s_waitcnt vmcnt(28) lgkmcnt(0)\n"
  "s_barrier\n"
  "s_add_u32 m0, s78, 49152\n"
  "s_add_u32 s74, s42, 384\n"
  "s_addc_u32 s75, s43, 0\n"
  "global_load_lds_dwordx4 v14, s[74:75]\n"
  "s_add_u32 m0, s78, 50176\n"
  "s_add_u32 s74, s42, 9600\n"
  "s_addc_u32 s75, s43, 0\n"
  "global_load_lds_dwordx4 v14, s[74:75]\n"
  "s_add_u32 m0, s78, 57344\n"
  "s_add_u32 s74, s42, 74112\n"
  "s_addc_u32 s75, s43, 0\n"
  "global_load_lds_dwordx4 v14, s[74:75]\n"
  "s_add_u32 m0, s78, 58368\n"
  "s_add_u32 s74, s42, 83328\n"
  "s_addc_u32 s75, s43, 0\n"
  "global_load_lds_dwordx4 v14, s[74:75]\n"
  "v_add_u32 v20, 65536, v13\n"
  "v_xor_b32 v21, 32, v20\n"
  "ds_read_b64_tr_b16 a[64:65], v20 offset:0\n"
  "ds_read_b64_tr_b16 a[66:67], v20 offset:2048\n"
  "ds_read_b64_tr_b16 a[68:69], v20 offset:8192\n"
  "ds_read_b64_tr_b16 a[70:71], v20 offset:10240\n"
  "ds_read_b64_tr_b16 a[72:73], v21 offset:0\n"
  "ds_read_b64_tr_b16 a[74:75], v21 offset:2048\n"
  "ds_read_b64_tr_b16 a[76:77], v21 offset:8192\n"
  "ds_read_b64_tr_b16 a[78:79], v21 offset:10240\n"
  "s_waitcnt vmcnt(28) lgkmcnt(0)\n"
  "s_barrier\n"
  "s_add_u32 m0, s78, 65536\n"
  "s_add_u32 s74, s42, 0\n"
  "s_addc_u32 s75, s43, 0\n"
  "s_add_u32 s74, s74, s52\n"
  "s_addc_u32 s75, s75, 0\n"
  "global_load_lds_dwordx4 v14, s[74:75]\n"
  "s_add_u32 m0, s78, 66560\n"
  "s_add_u32 s74, s42, 9216\n"
  "s_addc_u32 s75, s43, 0\n"
  "s_add_u32 s74, s74, s52\n"
  "s_addc_u32 s75, s75, 0\n"
  "global_load_lds_dwordx4 v14, s[74:75]\n"
  "s_add_u32 m0, s78, 73728\n"
  "s_add_u32 s74, s42, 73728\n"
  "s_addc_u32 s75, s43, 0\n"
  "s_add_u32 s74, s74, s52\n"
  "s_addc_u32 s75, s75, 0\n"
  "global_load_lds_dwordx4 v14, s[74:75]\n"
  "s_add_u32 m0, s78, 74752\n"
  "s_add_u32 s74, s42, 82944\n"
  "s_addc_u32 s75, s43, 0\n"
  "s_add_u32 s74, s74, s52\n"
  "s_addc_u32 s75, s75, 0\n"
  "global_load_lds_dwordx4 v14, s[74:75]\n"
  "v_add_u32 v20, 81920, v13\n"
  "v_xor_b32 v21, 32, v20\n"
  "ds_read_b64_tr_b16 a[80:81], v20 offset:0\n"
  "ds_read_b64_tr_b16 a[82:83], v20 offset:2048\n"
  "ds_read_b64_tr_b16 a[84:85], v20 offset:8192\n"
  "ds_read_b64_tr_b16 a[86:87], v20 offset:10240\n"
  "ds_read_b64_tr_b16 a[88:89], v21 offset:0\n"
  "ds_read_b64_tr_b16 a[90:91], v21 offset:2048\n"
  "ds_read_b64_tr_b16 a[92:93], v21 offset:8192\n"
  "ds_read_b64_tr_b16 a[94:95], v21 offset:10240\n"
  "s_waitcnt vmcnt(28) lgkmcnt(0)\n"
  "s_barrier\n"
  "s_add_u32 m0, s79, 81920\n"
  "s_add_u32 s74, s38, 0\n"
  "s_addc_u32 s75, s39, 0\n"
  "global_load_lds_dwordx4 v12, s[74:75]\n"
  "s_add_u32 m0, s79, 83968\n"
  "s_add_u32 s74, s38, 65536\n"
  "s_addc_u32 s75, s39, 0\n"
  "global_load_lds_dwordx4 v12, s[74:75]\n"
  "s_add_u32 m0, s79, 90112\n"
  "s_add_u32 s74, s38, 262144\n"
  "s_addc_u32 s75, s39, 0\n"
  "global_load_lds_dwordx4 v12, s[74:75]\n"
  "s_add_u32 m0, s79, 92160\n"
  "s_add_u32 s74, s38, 327680\n"
  "s_addc_u32 s75, s39, 0\n"
  "global_load_lds_dwordx4 v12, s[74:75]\n"
  "v_add_u32 v20, 98304, v13\n"
  "v_xor_b32 v21, 32, v20\n"
  "ds_read_b64_tr_b16 a[96:97], v20 offset:0\n"
  "ds_read_b64_tr_b16 a[98:99], v20 offset:2048\n"
  "ds_read_b64_tr_b16 a[100:101], v20 offset:8192\n"
  "ds_read_b64_tr_b16 a[102:103], v20 offset:10240\n"
  "ds_read_b64_tr_b16 a[104:105], v21 offset:0\n"
  "ds_read_b64_tr_b16 a[106:107], v21 offset:2048\n"
  "ds_read_b64_tr_b16 a[108:109], v21 offset:8192\n"
  "ds_read_b64_tr_b16 a[110:111], v21 offset:10240\n"
  "s_waitcnt vmcnt(28) lgkmcnt(0)\n"
  "s_barrier\n"
  "s_add_u32 m0, s79, 98304\n"
  "s_add_u32 s74, s38, 524288\n"
  "s_addc_u32 s75, s39, 0\n"
  "global_load_lds_dwordx4 v12, s[74:75]\n"
  "s_add_u32 m0, s79, 100352\n"
  "s_add_u32 s74, s38, 589824\n"
  "s_addc_u32 s75, s39, 0\n"
  "global_load_lds_dwordx4 v12, s[74:75]\n"
  "s_add_u32 m0, s79, 106496\n"
  "s_add_u32 s74, s38, 786432\n"
  "s_addc_u32 s75, s39, 0\n"
  "global_load_lds_dwordx4 v12, s[74:75]\n"
  "s_add_u32 m0, s79, 108544\n"
  "s_add_u32 s74, s38, 851968\n"
  "s_addc_u32 s75, s39, 0\n"
  "global_load_lds_dwordx4 v12, s[74:75]\n"
  "v_add_u32 v20, 114688, v13\n"
  "v_xor_b32 v21, 32, v20\n"
  "ds_read_b64_tr_b16 a[112:113], v20 offset:0\n"
  "ds_read_b64_tr_b16 a[114:115], v20 offset:2048\n"
  "ds_read_b64_tr_b16 a[116:117], v20 offset:8192\n"
  "ds_read_b64_tr_b16 a[118:119], v20 offset:10240\n"
  "ds_read_b64_tr_b16 a[120:121], v21 offset:0\n"
  "ds_read_b64_tr_b16 a[122:123], v21 offset:2048\n"
  "ds_read_b64_tr_b16 a[124:125], v21 offset:8192\n"
  "ds_read_b64_tr_b16 a[126:127], v21 offset:10240\n"
  "s_waitcnt vmcnt(28) lgkmcnt(0)\n"
  "s_barrier\n"
  "s_add_u32 m0, s79, 114688\n"
  "s_add_u32 s74, s38, 1048576\n"
  "s_addc_u32 s75, s39, 0\n"
  "global_load_lds_dwordx4 v12, s[74:75]\n"
  "s_add_u32 m0, s79, 116736\n"
  "s_add_u32 s74, s38, 1114112\n"
  "s_addc_u32 s75, s39, 0\n"
  "global_load_lds_dwordx4 v12, s[74:75]\n"
  "s_add_u32 m0, s79, 122880\n"
  "s_add_u32 s74, s38, 1310720\n"
  "s_addc_u32 s75, s39, 0\n"
  "global_load_lds_dwordx4 v12, s[74:75]\n"
  "s_add_u32 m0, s79, 124928\n"
  "s_add_u32 s74, s38, 1376256\n"
  "s_addc_u32 s75, s39, 0\n"
  "global_load_lds_dwordx4 v12, s[74:75]\n"
  "v_add_u32 v20, 131072, v13\n"
  "v_xor_b32 v21, 32, v20\n"
  "ds_read_b64_tr_b16 a[128:129], v20 offset:0\n"
  "ds_read_b64_tr_b16 a[130:131], v20 offset:2048\n"
  "ds_read_b64_tr_b16 a[132:133], v20 offset:8192\n"
  "ds_read_b64_tr_b16 a[134:135], v20 offset:10240\n"
  "ds_read_b64_tr_b16 a[136:137], v21 offset:0\n"
  "ds_read_b64_tr_b16 a[138:139], v21 offset:2048\n"
  "ds_read_b64_tr_b16 a[140:141], v21 offset:8192\n"
  "ds_read_b64_tr_b16 a[142:143], v21 offset:10240\n"
  "s_waitcnt vmcnt(28) lgkmcnt(0)\n"
  "s_barrier\n"
  "s_add_u32 m0, s79, 131072\n"
  "s_add_u32 s74, s38, 1572864\n"
  "s_addc_u32 s75, s39, 0\n"
  "global_load_lds_dwordx4 v12, s[74:75]\n"
  "s_add_u32 m0, s79, 133120\n"
  "s_add_u32 s74, s38, 1638400\n"
  "s_addc_u32 s75, s39, 0\n"
  "global_load_lds_dwordx4 v12, s[74:75]\n"
  "s_add_u32 m0, s79, 139264\n"
  "s_add_u32 s74, s38, 1835008\n"
  "s_addc_u32 s75, s39, 0\n"
  "global_load_lds_dwordx4 v12, s[74:75]\n"
  "s_add_u32 m0, s79, 141312\n"
  "s_add_u32 s74, s38, 1900544\n"
  "s_addc_u32 s75, s39, 0\n"
  "global_load_lds_dwordx4 v12, s[74:75]\n"
  "s_cmp_lg_u32 s48, 0\n"
  "s_cbranch_scc1 HLS9\n"
  "s_xor_b32 s74, s56, 0\n"
  "v_xor_b32 v20, s74, v15\n"
  "v_add_u32 v20, 0, v20\n"
  "ds_read_b64_tr_b16 a[144:145], v20 offset:0\n"
  "ds_read_b64_tr_b16 a[146:147], v20 offset:256\n"
  "ds_read_b64_tr_b16 a[148:149], v20 offset:1024\n"
  "ds_read_b64_tr_b16 a[150:151], v20 offset:1280\n"
  "ds_read_b64_tr_b16 a[152:153], v20 offset:8192\n"
  "ds_read_b64_tr_b16 a[154:155], v20 offset:8448\n"
  "ds_read_b64_tr_b16 a[156:157], v20 offset:9216\n"
  "ds_read_b64_tr_b16 a[158:159], v20 offset:9472\n"
  "s_xor_b32 s74, s56, 32\n"
  "v_xor_b32 v20, s74, v15\n"
  "v_add_u32 v20, 0, v20\n"
  "ds_read_b64_tr_b16 a[160:161], v20 offset:0\n"
  "ds_read_b64_tr_b16 a[162:163], v20 offset:256\n"
  "ds_read_b64_tr_b16 a[164:165], v20 offset:1024\n"
  "ds_read_b64_tr_b16 a[166:167], v20 offset:1280\n"
  "ds_read_b64_tr_b16 a[168:169], v20 offset:8192\n"
  "ds_read_b64_tr_b16 a[170:171], v20 offset:8448\n"
  "ds_read_b64_tr_b16 a[172:173], v20 offset:9216\n"
  "ds_read_b64_tr_b16 a[174:175], v20 offset:9472\n"
  "s_xor_b32 s74, s56, 64\n"
  "v_xor_b32 v20, s74, v15\n"
  "v_add_u32 v20, 0, v20\n"
  "ds_read_b64_tr_b16 a[176:177], v20 offset:0\n"
  "ds_read_b64_tr_b16 a[178:179], v20 offset:256\n"
  "ds_read_b64_tr_b16 a[180:181], v20 offset:1024\n"
  "ds_read_b64_tr_b16 a[182:183], v20 offset:1280\n"
  "ds_read_b64_tr_b16 a[184:185], v20 offset:8192\n"
  "ds_read_b64_tr_b16 a[186:187], v20 offset:8448\n"
  "ds_read_b64_tr_b16 a[188:189], v20 offset:9216\n"
  "ds_read_b64_tr_b16 a[190:191], v20 offset:9472\n"
  "s_xor_b32 s74, s56, 96\n"
  "v_xor_b32 v20, s74, v15\n"
  "v_add_u32 v20, 0, v20\n"
  "ds_read_b64_tr_b16 a[192:193], v20 offset:0\n"
  "ds_read_b64_tr_b16 a[194:195], v20 offset:256\n"
  "ds_read_b64_tr_b16 a[196:197], v20 offset:1024\n"
  "ds_read_b64_tr_b16 a[198:199], v20 offset:1280\n"
  "ds_read_b64_tr_b16 a[200:201], v20 offset:8192\n"
  "ds_read_b64_tr_b16 a[202:203], v20 offset:8448\n"
  "ds_read_b64_tr_b16 a[204:205], v20 offset:9216\n"
  "ds_read_b64_tr_b16 a[206:207], v20 offset:9472\n"
  "HLS9:\n"
  "s_waitcnt vmcnt(28) lgkmcnt(0)\n"
  "s_barrier\n"
  "s_add_u32 m0, s79, 0\n"
  "s_add_u32 s74, s38, 2097152\n"
  "s_addc_u32 s75, s39, 0\n"
  "global_load_lds_dwordx4 v12, s[74:75]\n"
  "s_add_u32 m0, s79, 2048\n"
  "s_add_u32 s74, s38, 2162688\n"
  "s_addc_u32 s75, s39, 0\n"
  "global_load_lds_dwordx4 v12, s[74:75]\n"
  "s_add_u32 m0, s79, 8192\n"
  "s_add_u32 s74, s38, 2359296\n"
  "s_addc_u32 s75, s39, 0\n"
  "global_load_lds_dwordx4 v12, s[74:75]\n"
  "s_add_u32 m0, s79, 10240\n"
  "s_add_u32 s74, s38, 2424832\n"
  "s_addc_u32 s75, s39, 0\n"
  "global_load_lds_dwordx4 v12, s[74:75]\n"
  "s_cmp_lg_u32 s48, 1\n"
  "s_cbranch_scc1 HLS10\n"
  "s_xor_b32 s74, s56, 0\n"
  "v_xor_b32 v20, s74, v15\n"
  "v_add_u32 v20, 16384, v20\n"
  "ds_read_b64_tr_b16 a[144:145], v20 offset:0\n"
  "ds_read_b64_tr_b16 a[146:147], v20 offset:256\n"
  "ds_read_b64_tr_b16 a[148:149], v20 offset:1024\n"
  "ds_read_b64_tr_b16 a[150:151], v20 offset:1280\n"
  "ds_read_b64_tr_b16 a[152:153], v20 offset:8192\n"
  "ds_read_b64_tr_b16 a[154:155], v20 offset:8448\n"
  "ds_read_b64_tr_b16 a[156:157], v20 offset:9216\n"
  "ds_read_b64_tr_b16 a[158:159], v20 offset:9472\n"
  "s_xor_b32 s74, s56, 32\n"
  "v_xor_b32 v20, s74, v15\n"
  "v_add_u32 v20, 16384, v20\n"
  "ds_read_b64_tr_b16 a[160:161], v20 offset:0\n"
  "ds_read_b64_tr_b16 a[162:163], v20 offset:256\n"
  "ds_read_b64_tr_b16 a[164:165], v20 offset:1024\n"
  "ds_read_b64_tr_b16 a[166:167], v20 offset:1280\n"
  "ds_read_b64_tr_b16 a[168:169], v20 offset:8192\n"
  "ds_read_b64_tr_b16 a[170:171], v20 offset:8448\n"
  "ds_read_b64_tr_b16 a[172:173], v20 offset:9216\n"
  "ds_read_b64_tr_b16 a[174:175], v20 offset:9472\n"
  "s_xor_b32 s74, s56, 64\n"
  "v_xor_b32 v20, s74, v15\n"
  "v_add_u32 v20, 16384, v20\n"
  "ds_read_b64_tr_b16 a[176:177], v20 offset:0\n"
  "ds_read_b64_tr_b16 a[178:179], v20 offset:256\n"
  "ds_read_b64_tr_b16 a[180:181], v20 offset:1024\n"
  "ds_read_b64_tr_b16 a[182:183], v20 offset:1280\n"
  "ds_read_b64_tr_b16 a[184:185], v20 offset:8192\n"
  "ds_read_b64_tr_b16 a[186:187], v20 offset:8448\n"
  "ds_read_b64_tr_b16 a[188:189], v20 offset:9216\n"
  "ds_read_b64_tr_b16 a[190:191], v20 offset:9472\n"
  "s_xor_b32 s74, s56, 96\n"
  "v_xor_b32 v20, s74, v15\n"
  "v_add_u32 v20, 16384, v20\n"
  "ds_read_b64_tr_b16 a[192:193], v20 offset:0\n"
  "ds_read_b64_tr_b16 a[194:195], v20 offset:256\n"
  "ds_read_b64_tr_b16 a[196:197], v20 offset:1024\n"
  "ds_read_b64_tr_b16 a[198:199], v20 offset:1280\n"
  "ds_read_b64_tr_b16 a[200:201], v20 offset:8192\n"
  "ds_read_b64_tr_b16 a[202:203], v20 offset:8448\n"
  "ds_read_b64_tr_b16 a[204:205], v20 offset:9216\n"
  "ds_read_b64_tr_b16 a[206:207], v20 offset:9472\n"
  "HLS10:\n"
  "s_waitcnt vmcnt(28) lgkmcnt(0)\n"
  "s_barrier\n"
  "s_add_u32 m0, s79, 16384\n"
  "s_add_u32 s74, s38, 2621440\n"
  "s_addc_u32 s75, s39, 0\n"
  "global_load_lds_dwordx4 v12, s[74:75]\n"
  "s_add_u32 m0, s79, 18432\n"
  "s_add_u32 s74, s38, 2686976\n"
  "s_addc_u32 s75, s39, 0\n"
  "global_load_lds_dwordx4 v12, s[74:75]\n"
  "s_add_u32 m0, s79, 24576\n"
  "s_add_u32 s74, s38, 2883584\n"
  "s_addc_u32 s75, s39, 0\n"
  "global_load_lds_dwordx4 v12, s[74:75]\n"
  "s_add_u32 m0, s79, 26624\n"
  "s_add_u32 s74, s38, 2949120\n"
  "s_addc_u32 s75, s39, 0\n"
  "global_load_lds_dwordx4 v12, s[74:75]\n"
  "s_cmp_lg_u32 s48, 2\n"
  "s_cbranch_scc1 HLS11\n"
  "s_xor_b32 s74, s56, 0\n"
  "v_xor_b32 v20, s74, v15\n"
  "v_add_u32 v20, 32768, v20\n"
  "ds_read_b64_tr_b16 a[144:145], v20 offset:0\n"
  "ds_read_b64_tr_b16 a[146:147], v20 offset:256\n"
  "ds_read_b64_tr_b16 a[148:149], v20 offset:1024\n"
  "ds_read_b64_tr_b16 a[150:151], v20 offset:1280\n"
  "ds_read_b64_tr_b16 a[152:153], v20 offset:8192\n"
  "ds_read_b64_tr_b16 a[154:155], v20 offset:8448\n"
  "ds_read_b64_tr_b16 a[156:157], v20 offset:9216\n"
  "ds_read_b64_tr_b16 a[158:159], v20 offset:9472\n"
  "s_xor_b32 s74, s56, 32\n"
  "v_xor_b32 v20, s74, v15\n"
  "v_add_u32 v20, 32768, v20\n"
  "ds_read_b64_tr_b16 a[160:161], v20 offset:0\n"
  "ds_read_b64_tr_b16 a[162:163], v20 offset:256\n"
  "ds_read_b64_tr_b16 a[164:165], v20 offset:1024\n"
  "ds_read_b64_tr_b16 a[166:167], v20 offset:1280\n"
  "ds_read_b64_tr_b16 a[168:169], v20 offset:8192\n"
  "ds_read_b64_tr_b16 a[170:171], v20 offset:8448\n"
  "ds_read_b64_tr_b16 a[172:173], v20 offset:9216\n"
  "ds_read_b64_tr_b16 a[174:175], v20 offset:9472\n"
  "s_xor_b32 s74, s56, 64\n"
  "v_xor_b32 v20, s74, v15\n"
  "v_add_u32 v20, 32768, v20\n"
  "ds_read_b64_tr_b16 a[176:177], v20 offset:0\n"
  "ds_read_b64_tr_b16 a[178:179], v20 offset:256\n"
  "ds_read_b64_tr_b16 a[180:181], v20 offset:1024\n"
  "ds_read_b64_tr_b16 a[182:183], v20 offset:1280\n"
  "ds_read_b64_tr_b16 a[184:185], v20 offset:8192\n"
  "ds_read_b64_tr_b16 a[186:187], v20 offset:8448\n"
  "ds_read_b64_tr_b16 a[188:189], v20 offset:9216\n"
  "ds_read_b64_tr_b16 a[190:191], v20 offset:9472\n"
  "s_xor_b32 s74, s56, 96\n"
  "v_xor_b32 v20, s74, v15\n"
  "v_add_u32 v20, 32768, v20\n"
  "ds_read_b64_tr_b16 a[192:193], v20 offset:0\n"
  "ds_read_b64_tr_b16 a[194:195], v20 offset:256\n"
  "ds_read_b64_tr_b16 a[196:197], v20 offset:1024\n"
  "ds_read_b64_tr_b16 a[198:199], v20 offset:1280\n"
  "ds_read_b64_tr_b16 a[200:201], v20 offset:8192\n"
  "ds_read_b64_tr_b16 a[202:203], v20 offset:8448\n"
  "ds_read_b64_tr_b16 a[204:205], v20 offset:9216\n"
  "ds_read_b64_tr_b16 a[206:207], v20 offset:9472\n"
  "HLS11:\n"
  "s_waitcnt vmcnt(28) lgkmcnt(0)\n"
  "s_barrier\n"
  "s_add_u32 m0, s79, 32768\n"
  "s_add_u32 s74, s38, 3145728\n"
  "s_addc_u32 s75, s39, 0\n"
  "global_load_lds_dwordx4 v12, s[74:75]\n"
  "s_add_u32 m0, s79, 34816\n"
  "s_add_u32 s74, s38, 3211264\n"
  "s_addc_u32 s75, s39, 0\n"
  "global_load_lds_dwordx4 v12, s[74:75]\n"
  "s_add_u32 m0, s79, 40960\n"
  "s_add_u32 s74, s38, 3407872\n"
  "s_addc_u32 s75, s39, 0\n"
  "global_load_lds_dwordx4 v12, s[74:75]\n"
  "s_add_u32 m0, s79, 43008\n"
  "s_add_u32 s74, s38, 3473408\n"
  "s_addc_u32 s75, s39, 0\n"
  "global_load_lds_dwordx4 v12, s[74:75]\n"
  "s_cmp_lg_u32 s48, 3\n"
  "s_cbranch_scc1 HLS12\n"
  "s_xor_b32 s74, s56, 0\n"
  "v_xor_b32 v20, s74, v15\n"
  "v_add_u32 v20, 49152, v20\n"
  "ds_read_b64_tr_b16 a[144:145], v20 offset:0\n"
  "ds_read_b64_tr_b16 a[146:147], v20 offset:256\n"
  "ds_read_b64_tr_b16 a[148:149], v20 offset:1024\n"
  "ds_read_b64_tr_b16 a[150:151], v20 offset:1280\n"
  "ds_read_b64_tr_b16 a[152:153], v20 offset:8192\n"
  "ds_read_b64_tr_b16 a[154:155], v20 offset:8448\n"
  "ds_read_b64_tr_b16 a[156:157], v20 offset:9216\n"
  "ds_read_b64_tr_b16 a[158:159], v20 offset:9472\n"
  "s_xor_b32 s74, s56, 32\n"
  "v_xor_b32 v20, s74, v15\n"
  "v_add_u32 v20, 49152, v20\n"
  "ds_read_b64_tr_b16 a[160:161], v20 offset:0\n"
  "ds_read_b64_tr_b16 a[162:163], v20 offset:256\n"
  "ds_read_b64_tr_b16 a[164:165], v20 offset:1024\n"
  "ds_read_b64_tr_b16 a[166:167], v20 offset:1280\n"
  "ds_read_b64_tr_b16 a[168:169], v20 offset:8192\n"
  "ds_read_b64_tr_b16 a[170:171], v20 offset:8448\n"
  "ds_read_b64_tr_b16 a[172:173], v20 offset:9216\n"
  "ds_read_b64_tr_b16 a[174:175], v20 offset:9472\n"
  "s_xor_b32 s74, s56, 64\n"
  "v_xor_b32 v20, s74, v15\n"
  "v_add_u32 v20, 49152, v20\n"
  "ds_read_b64_tr_b16 a[176:177], v20 offset:0\n"
  "ds_read_b64_tr_b16 a[178:179], v20 offset:256\n"
  "ds_read_b64_tr_b16 a[180:181], v20 offset:1024\n"
  "ds_read_b64_tr_b16 a[182:183], v20 offset:1280\n"
  "ds_read_b64_tr_b16 a[184:185], v20 offset:8192\n"
  "ds_read_b64_tr_b16 a[186:187], v20 offset:8448\n"
  "ds_read_b64_tr_b16 a[188:189], v20 offset:9216\n"
  "ds_read_b64_tr_b16 a[190:191], v20 offset:9472\n"
  "s_xor_b32 s74, s56, 96\n"
  "v_xor_b32 v20, s74, v15\n"
  "v_add_u32 v20, 49152, v20\n"
  "ds_read_b64_tr_b16 a[192:193], v20 offset:0\n"
  "ds_read_b64_tr_b16 a[194:195], v20 offset:256\n"
  "ds_read_b64_tr_b16 a[196:197], v20 offset:1024\n"
  "ds_read_b64_tr_b16 a[198:199], v20 offset:1280\n"
  "ds_read_b64_tr_b16 a[200:201], v20 offset:8192\n"
  "ds_read_b64_tr_b16 a[202:203], v20 offset:8448\n"
  "ds_read_b64_tr_b16 a[204:205], v20 offset:9216\n"
  "ds_read_b64_tr_b16 a[206:207], v20 offset:9472\n"
  "HLS12:\n"
  "s_waitcnt vmcnt(28) lgkmcnt(0)\n"
  "s_barrier\n"
  "s_add_u32 m0, s79, 49152\n"
  "s_add_u32 s74, s38, 3670016\n"
  "s_addc_u32 s75, s39, 0\n"
  "global_load_lds_dwordx4 v12, s[74:75]\n"
  "s_add_u32 m0, s79, 51200\n"
  "s_add_u32 s74, s38, 3735552\n"
  "s_addc_u32 s75, s39, 0\n"
  "global_load_lds_dwordx4 v12, s[74:75]\n"
  "s_add_u32 m0, s79, 57344\n"
  "s_add_u32 s74, s38, 3932160\n"
  "s_addc_u32 s75, s39, 0\n"
  "global_load_lds_dwordx4 v12, s[74:75]\n"
  "s_add_u32 m0, s79, 59392\n"
  "s_add_u32 s74, s38, 3997696\n"
  "s_addc_u32 s75, s39, 0\n"
  "global_load_lds_dwordx4 v12, s[74:75]\n"
  "s_lshr_b32 s74, s52, 3\n"
  "s_and_b32 s74, s74, 64\n"
  "s_and_b32 s75, s48, 1\n"
  "s_lshl_b32 s75, s75, 5\n"
  "s_add_u32 s74, s74, s75\n"
  "v_xor_b32 v20, s74, v15\n"
  "v_add_u32 v20, 65536, v20\n"
  "ds_read_b64_tr_b16 a[208:209], v20 offset:0\n"
  "ds_read_b64_tr_b16 a[210:211], v20 offset:256\n"
  "ds_read_b64_tr_b16 a[212:213], v20 offset:1024\n"
  "ds_read_b64_tr_b16 a[214:215], v20 offset:1280\n"
  "ds_read_b64_tr_b16 a[216:217], v20 offset:8192\n"
  "ds_read_b64_tr_b16 a[218:219], v20 offset:8448\n"
  "ds_read_b64_tr_b16 a[220:221], v20 offset:9216\n"
  "ds_read_b64_tr_b16 a[222:223], v20 offset:9472\n"
  "s_waitcnt vmcnt(28) lgkmcnt(0)\n"
  "s_barrier\n"
  "s_add_u32 m0, s79, 65536\n"
  "s_add_u32 s74, s38, 4194304\n"
  "s_addc_u32 s75, s39, 0\n"
  "global_load_lds_dwordx4 v12, s[74:75]\n"
  "s_add_u32 m0, s79, 67584\n"
  "s_add_u32 s74, s38, 4259840\n"
  "s_addc_u32 s75, s39, 0\n"
  "global_load_lds_dwordx4 v12, s[74:75]\n"
  "s_add_u32 m0, s79, 73728\n"
  "s_add_u32 s74, s38, 4456448\n"
  "s_addc_u32 s75, s39, 0\n"
  "global_load_lds_dwordx4 v12, s[74:75]\n"
  "s_add_u32 m0, s79, 75776\n"
  "s_add_u32 s74, s38, 4521984\n"
  "s_addc_u32 s75, s39, 0\n"
  "global_load_lds_dwordx4 v12, s[74:75]\n"
  "v_add_u32 v20, 81920, v13\n"
  "v_xor_b32 v21, 32, v20\n"
  "ds_read_b64_tr_b16 v[112:113], v20 offset:0\n"
  "ds_read_b64_tr_b16 v[114:115], v20 offset:2048\n"
  "ds_read_b64_tr_b16 v[116:117], v20 offset:8192\n"
  "ds_read_b64_tr_b16 v[118:119], v20 offset:10240\n"
  "ds_read_b64_tr_b16 v[120:121], v21 offset:0\n"
  "ds_read_b64_tr_b16 v[122:123], v21 offset:2048\n"
  "ds_read_b64_tr_b16 v[124:125], v21 offset:8192\n"
  "ds_read_b64_tr_b16 v[126:127], v21 offset:10240\n"
  "s_waitcnt vmcnt(28) lgkmcnt(0)\n"
  "s_barrier\n"
  "v_add_u32 v20, 98304, v13\n"
  "v_xor_b32 v21, 32, v20\n"
  "ds_read_b64_tr_b16 v[128:129], v20 offset:0\n"
  "ds_read_b64_tr_b16 v[130:131], v20 offset:2048\n"
  "ds_read_b64_tr_b16 v[132:133], v20 offset:8192\n"
  "ds_read_b64_tr_b16 v[134:135], v20 offset:10240\n"
  "ds_read_b64_tr_b16 v[136:137], v21 offset:0\n"
  "ds_read_b64_tr_b16 v[138:139], v21 offset:2048\n"
  "ds_read_b64_tr_b16 v[140:141], v21 offset:8192\n"
  "ds_read_b64_tr_b16 v[142:143], v21 offset:10240\n"
  "s_waitcnt vmcnt(24) lgkmcnt(0)\n"
  "s_barrier\n"
  "v_add_u32 v20, 114688, v13\n"
  "v_xor_b32 v21, 32, v20\n"
  "ds_read_b64_tr_b16 v[144:145], v20 offset:0\n"
  "ds_read_b64_tr_b16 v[146:147], v20 offset:2048\n"
  "ds_read_b64_tr_b16 v[148:149], v20 offset:8192\n"
  "ds_read_b64_tr_b16 v[150:151], v20 offset:10240\n"
  "ds_read_b64_tr_b16 v[152:153], v21 offset:0\n"
  "ds_read_b64_tr_b16 v[154:155], v21 offset:2048\n"
  "ds_read_b64_tr_b16 v[156:157], v21 offset:8192\n"
  "ds_read_b64_tr_b16 v[158:159], v21 offset:10240\n"
  "s_waitcnt vmcnt(20) lgkmcnt(0)\n"
  "s_barrier\n"
  "v_add_u32 v20, 131072, v13\n"
  "v_xor_b32 v21, 32, v20\n"
  "ds_read_b64_tr_b16 v[160:161], v20 offset:0\n"
  "ds_read_b64_tr_b16 v[162:163], v20 offset:2048\n"
  "ds_read_b64_tr_b16 v[164:165], v20 offset:8192\n"
  "ds_read_b64_tr_b16 v[166:167], v20 offset:10240\n"
  "ds_read_b64_tr_b16 v[168:169], v21 offset:0\n"
  "ds_read_b64_tr_b16 v[170:171], v21 offset:2048\n"
  "ds_read_b64_tr_b16 v[172:173], v21 offset:8192\n"
  "ds_read_b64_tr_b16 v[174:175], v21 offset:10240\n"
  "s_waitcnt vmcnt(16) lgkmcnt(0)\n"
  "s_barrier\n"
  "v_add_u32 v20, 0, v13\n"
  "v_xor_b32 v21, 32, v20\n"
  "ds_read_b64_tr_b16 v[176:177], v20 offset:0\n"
  "ds_read_b64_tr_b16 v[178:179], v20 offset:2048\n"
  "ds_read_b64_tr_b16 v[180:181], v20 offset:8192\n"
  "ds_read_b64_tr_b16 v[182:183], v20 offset:10240\n"
  "ds_read_b64_tr_b16 v[184:185], v21 offset:0\n"
  "ds_read_b64_tr_b16 v[186:187], v21 offset:2048\n"
  "ds_read_b64_tr_b16 v[188:189], v21 offset:8192\n"
  "ds_read_b64_tr_b16 v[190:191], v21 offset:10240\n"
  "s_waitcnt vmcnt(12) lgkmcnt(0)\n"
  "s_barrier\n"
  "v_add_u32 v20, 16384, v13\n"
  "v_xor_b32 v21, 32, v20\n"
  "ds_read_b64_tr_b16 v[192:193], v20 offset:0\n"
  "ds_read_b64_tr_b16 v[194:195], v20 offset:2048\n"
  "ds_read_b64_tr_b16 v[196:197], v20 offset:8192\n"
  "ds_read_b64_tr_b16 v[198:199], v20 offset:10240\n"
  "ds_read_b64_tr_b16 v[200:201], v21 offset:0\n"
  "ds_read_b64_tr_b16 v[202:203], v21 offset:2048\n"
  "ds_read_b64_tr_b16 v[204:205], v21 offset:8192\n"
  "ds_read_b64_tr_b16 v[206:207], v21 offset:10240\n"
  "s_waitcnt vmcnt(8) lgkmcnt(0)\n"
  "s_barrier\n"
  "v_add_u32 v20, 32768, v13\n"
  "v_xor_b32 v21, 32, v20\n"
  "ds_read_b64_tr_b16 v[208:209], v20 offset:0\n"
  "ds_read_b64_tr_b16 v[210:211], v20 offset:2048\n"
  "ds_read_b64_tr_b16 v[212:213], v20 offset:8192\n"
  "ds_read_b64_tr_b16 v[214:215], v20 offset:10240\n"
  "ds_read_b64_tr_b16 v[216:217], v21 offset:0\n"
  "ds_read_b64_tr_b16 v[218:219], v21 offset:2048\n"
  "ds_read_b64_tr_b16 v[220:221], v21 offset:8192\n"
  "ds_read_b64_tr_b16 v[222:223], v21 offset:10240\n"
  "s_waitcnt vmcnt(4) lgkmcnt(0)\n"
  "s_barrier\n"
  "v_add_u32 v20, 49152, v13\n"
  "v_xor_b32 v21, 32, v20\n"
  "ds_read_b64_tr_b16 v[224:225], v20 offset:0\n"
  "ds_read_b64_tr_b16 v[226:227], v20 offset:2048\n"
  "ds_read_b64_tr_b16 v[228:229], v20 offset:8192\n"
  "ds_read_b64_tr_b16 v[230:231], v20 offset:10240\n"
  "ds_read_b64_tr_b16 v[232:233], v21 offset:0\n"
  "ds_read_b64_tr_b16 v[234:235], v21 offset:2048\n"
  "ds_read_b64_tr_b16 v[236:237], v21 offset:8192\n"
  "ds_read_b64_tr_b16 v[238:239], v21 offset:10240\n"
  "s_waitcnt vmcnt(0) lgkmcnt(0)\n"
  "s_barrier\n"
  "v_add_u32 v20, 65536, v13\n"
  "v_xor_b32 v21, 32, v20\n"
  "ds_read_b64_tr_b16 v[240:241], v20 offset:0\n"
  "ds_read_b64_tr_b16 v[242:243], v20 offset:2048\n"
  "ds_read_b64_tr_b16 v[244:245], v20 offset:8192\n"
  "ds_read_b64_tr_b16 v[246:247], v20 offset:10240\n"
  "ds_read_b64_tr_b16 v[248:249], v21 offset:0\n"
  "ds_read_b64_tr_b16 v[250:251], v21 offset:2048\n"
  "ds_read_b64_tr_b16 v[252:253], v21 offset:8192\n"
  "ds_read_b64_tr_b16 v[254:255], v21 offset:10240\n"
  "s_waitcnt lgkmcnt(0)\n"
  "s_barrier\n"
  "v_mov_b32 v16, 0x3f803f80\n"
  "v_mov_b32 v17, 0x3f803f80\n"
  "v_mov_b32 v18, 0x3f803f80\n"
  "v_mov_b32 v19, 0x3f803f80\n"
  "v_mov_b32 v77, s55\n"
  "s_cmp_lt_u32 s48, 2\n"
  "s_cselect_b64 s[38:39], -1, 0\n"
  "s_mov_b32 s2, s45\n"
  "s_mov_b32 s100, s50\n"
  "s_mov_b32 s101, s51\n"
  "s_lshr_b32 s3, s52, 8\n"
  "s_and_b32 s3, s3, 4\n"
  "s_add_u32 s3, s3, s48\n"
  "s_mov_b32 s7, s57\n"
  "s_mov_b32 s14, s58\n"
  "s_mov_b32 s15, s59\n"
  "s_mov_b32 s55, s58\n"
  "s_mov_b32 s51, s59\n"
  "s_mov_b32 s56, 0\n"
  "s_mov_b32 s57, 0\n"
  "s_mov_b32 s58, 100000\n"
  "s_mov_b32 s59, 0\n"
  "s_mov_b32 s60, 562036736\n"
  "s_mov_b32 s61, 1006632960\n"
  "s_mov_b32 s81, -1\n"
  "s_add_u32 s45, s44, 128000\n"
  "s_add_u32 s76, s101, s7\n"
  "s_add_u32 s76, s76, 137216\n"
  "s_add_u32 s74, s36, s76\n"
  "s_addc_u32 s75, s37, 0\n"
  "v_mov_b32 v79, 1\n"
  "s_cmp_eq_u32 s48, 0\n"
  "s_cselect_b64 exec, 1, 0\n"
  "global_atomic_add v0, v79, s[74:75] sc1\n"
  "s_mov_b64 exec, -1\n"
  "s_add_u32 s18, s36, s2\n"
  "s_addc_u32 s19, s37, 0\n"
  "s_sub_u32 s18, s18, 768\n"
  "s_subb_u32 s19, s19, 0\n"
  "s_cmp_eq_u32 s48, 0\n"
  "s_cselect_b32 s98, -1, 0\n"
  "v_mov_b64 v[56:57], 0\n"
  "v_mov_b64 v[58:59], 0\n"
  "v_mov_b64 v[60:61], 0\n"
  "v_mov_b64 v[62:63], 0\n"
  "v_mov_b64 v[64:65], 0\n"
  "v_mov_b64 v[66:67], 0\n"
  "v_mov_b64 v[68:69], 0\n"
  "v_mov_b64 v[70:71], 0\n"
  "v_mov_b64 v[72:73], 0\n"
  "v_mov_b64 v[74:75], 0\n"
  "v_add_u32 v76, 0x4000, v0\n"
  "ds_write_b32 v0, v56 offset:50432\n"
  "ds_write_b32 v76, v56 offset:50432\n"
  "s_mov_b32 s32, 0\n"
  "s_mov_b32 s35, 0\n"
  "v_mov_b32 v76, v0\n"
  "s_cmp_eq_u32 s52, 1024\n"
  "s_cselect_b64 s[0:1], -1, 0\n"
  "s_waitcnt lgkmcnt(0)\n"
  "s_barrier\n"
  "s_add_u32 s74, s36, s2\n"
  "s_addc_u32 s75, s37, 0\n"
  "PLHK:\n"
  "v_and_b32 v79, 128, v0\n"
  "global_load_dword v79, v79, s[74:75] sc1\n"
  "s_waitcnt vmcnt(0)\n"
  "v_readfirstlane_b32 s76, v79\n"
  "v_readlane_b32 s77, v79, 8\n"
  "s_min_u32 s76, s76, s77\n"
  "s_cmp_ge_u32 s76, 1\n"
  "s_cbranch_scc1 PDHK\n"
  "s_sub_u32 s58, s58, 1\n"
  "s_cbranch_scc1 QUITH\n"
  "s_sleep 4\n"
  "s_branch PLHK\n"
  "PDHK:\n"
  "s_bitcmp1_b64 s[4:5], 0\n"
  "s_cselect_b32 s74, 8192, 0\n"
  "s_lshl_b32 s75, 0, 21\n"
  "s_add_u32 s74, s74, s75\n"
  "s_add_u32 s74, s74, 20971520\n"
  "s_xor_b32 s75, s74, s47\n"
  "s_add_u32 s40, s36, s75\n"
  "s_addc_u32 s41, s37, 0\n"
  "s_lshl_b32 s42, s80, 2\n"
  "s_mov_b32 m0, s42\n"
  "s_nop 0\n"
  "global_load_lds_dwordx4 v0, s[40:41] offset:0 sc1\n"
  "global_load_lds_dwordx4 v0, s[40:41] offset:1024 sc1\n"
  "global_load_lds_dwordx4 v0, s[40:41] offset:2048 sc1\n"
  "global_load_lds_dwordx4 v0, s[40:41] offset:3072 sc1\n"
  "s_add_u32 s77, s101, s7\n"
  "s_add_u32 s77, s77, 138240\n"
  "s_add_u32 s74, s36, s77\n"
  "s_addc_u32 s75, s37, 0\n"
  "PLGH:\n"
  "v_mov_b32 v79, 0\n"
  "global_load_dword v79, v79, s[74:75] sc1\n"
  "s_waitcnt vmcnt(0)\n"
  "v_readfirstlane_b32 s76, v79\n"
  "s_cmp_lg_u32 s76, 0\n"
  "s_cbranch_scc1 PDGH\n"
  "s_sub_u32 s58, s58, 1\n"
  "s_cbranch_scc1 QUITH\n"
  "s_sleep 1\n"
  "s_branch PLGH\n"
  "PDGH:\n"
  "s_mov_b32 s49, s76\n"
  "s_mov_b32 s43, s49\n"
  "s_sub_u32 s6, s49, 0x100000\n"
  "s_mov_b32 s16, 0\n"
  "s_mov_b32 m0, 50432\n"
  "s_mov_b32 s33, -1\n"
  "s_mov_b32 exec_lo, 0\n"
  "s_and_b32 exec_hi, s98, 0x01010000\n"
  "global_load_lds_dword v0, s[18:19] sc1\n"
  "s_mov_b64 exec, -1\n"
  "HTRY:\n"
  "s_sub_u32 s74, s49, s43\n"
  "v_mov_b32 v78, s74\n"
  "v_mov_b32 v15, s56\n"
  "v_lshrrev_b32 v79, 1, v0\n"
  "s_mul_i32 s78, s57, 163840\n"
  "s_lshl_b32 s79, s32, 16\n"
  "s_add_u32 s79, s79, s78\n"
  "s_add_u32 s79, s79, s44\n"
  "s_add_u32 s62, s36, s79\n"
  "s_addc_u32 s63, s37, 0\n"
  "s_add_u32 s64, s62, 32768\n"
  "s_addc_u32 s65, s63, 0\n"
  "s_lshl_b32 s79, s32, 10\n"
  "s_add_u32 s79, s79, s78\n"
  "s_add_u32 s79, s79, s45\n"
  "s_add_u32 s66, s36, s79\n"
  "s_addc_u32 s67, s37, 0\n"
  "s_add_u32 m0, s80, 32768\n"
  "s_add_u32 s79, s80, 36864\n"
  "s_branch WGH\n"
  "WH:\n"
  "s_memrealtime s[74:75]\n"
  "s_waitcnt lgkmcnt(0)\n"
  "s_mul_i32 s74, s74, 24\n"
  "s_sub_i32 s74, s49, s74\n"
  "s_cmp_lt_i32 s74, 750\n"
  "s_cbranch_scc1 WFH\n"
  "s_cmp_gt_i32 s74, 0x40000\n"
  "s_cbranch_scc1 WEH\n"
  "s_sleep 2\n"
  "s_branch WH\n"
  "WFH:\n"
  "s_cmp_lt_i32 s74, 100\n"
  "s_cbranch_scc1 WEH\n"
  "WGH:\n"
  "s_memtime s[76:77]\n"
  "s_memrealtime s[20:21]\n"
  "s_nop 2\n"
  "s_memrealtime s[22:23]\n"
  "s_nop 2\n"
  "s_memrealtime s[24:25]\n"
  "s_nop 2\n"
  "s_memrealtime s[26:27]\n"
  "s_nop 2\n"
  "s_memrealtime s[28:29]\n"
  "s_nop 2\n"
  "s_memrealtime s[30:31]\n"
  "s_waitcnt lgkmcnt(0)\n"
  "s_add_u32 s20, s20, s22\n"
  "s_add_u32 s24, s24, s26\n"
  "s_add_u32 s28, s28, s30\n"
  "s_add_u32 s20, s20, s24\n"
  "s_add_u32 s20, s20, s28\n"
  "s_lshl_b32 s74, s20, 2\n"
  "s_sub_u32 s34, s74, s76\n"
  "s_sub_i32 s74, s49, s74\n"
  "s_sub_i32 s74, s74, 238\n"
  "s_max_i32 s74, s74, 0\n"
  "s_cmp_ge_i32 s74, 512\n"
  "s_cbranch_scc1 WH\n"
  "s_lshr_b32 s74, s74, 3\n"
  "s_lshl_b32 s74, s74, 2\n"
  "s_sub_i32 s74, 268, s74\n"
  "s_getpc_b64 s[76:77]\n"
  "s_add_u32 s76, s76, s74\n"
  "s_addc_u32 s77, s77, 0\n"
  "s_setpc_b64 s[76:77]\n"
  "s_nop 1\n"
  "s_nop 1\n"
  "s_nop 1\n"
  "s_nop 1\n"
  "s_nop 1\n"
  "s_nop 1\n"
  "s_nop 1\n"
  "s_nop 1\n"
  "s_nop 1\n"
  "s_nop 1\n"
  "s_nop 1\n"
  "s_nop 1\n"
  "s_nop 1\n"
  "s_nop 1\n"
  "s_nop 1\n"
  "s_nop 1\n"
  "s_nop 1\n"
  "s_nop 1\n"
  "s_nop 1\n"
  "s_nop 1\n"
  "s_nop 1\n"
  "s_nop 1\n"
  "s_nop 1\n"
  "s_nop 1\n"
  "s_nop 1\n"
  "s_nop 1\n"
  "s_nop 1\n"
  "s_nop 1\n"
  "s_nop 1\n"
  "s_nop 1\n"
  "s_nop 1\n"
  "s_nop 1\n"
  "s_nop 1\n"
  "s_nop 1\n"
  "s_nop 1\n"
  "s_nop 1\n"
  "s_nop 1\n"
  "s_nop 1\n"
  "s_nop 1\n"
  "s_nop 1\n"
  "s_nop 1\n"
  "s_nop 1\n"
  "s_nop 1\n"
  "s_nop 1\n"
  "s_nop 1\n"
  "s_nop 1\n"
  "s_nop 1\n"
  "s_nop 1\n"
  "s_nop 1\n"
  "s_nop 1\n"
  "s_nop 1\n"
  "s_nop 1\n"
  "s_nop 1\n"
  "s_nop 1\n"
  "s_nop 1\n"
  "s_nop 1\n"
  "s_nop 1\n"
  "s_nop 1\n"
  "s_nop 1\n"
  "s_nop 1\n"
  "s_nop 1\n"
  "s_nop 1\n"
  "s_nop 1\n"
  "s_nop 1\n"
  "WEH:\n"
  "global_load_lds_dwordx4 v1, s[62:63] sc1\n"
  "s_mov_b32 m0, s79\n"
  "s_cmp_lg_u32 s48, 3\n"
  "global_load_lds_dwordx4 v1, s[64:65] sc1\n"
  "s_cbranch_scc1 HN8\n"
  "s_mov_b32 m0, 40960\n"
  "s_nop 0\n"
  "global_load_lds_dwordx4 v1, s[66:67] sc1\n"
  "s_waitcnt vmcnt(3)\n"
  "s_branch HW8\n"
  "HN8:\n"
  "s_waitcnt vmcnt(2)\n"
  "HW8:\n"
  "s_barrier\n"
  "ds_read_b128 v[80:83], v76 offset:0\n"
  "s_add_u32 s64, s36, s100\n"
  "s_addc_u32 s65, s37, 0\n"
  "s_cmp_eq_u32 s3, 4\n"
  "ds_read_b128 v[84:87], v76 offset:1024\n"
  "s_cselect_b64 s[22:23], 1, 0\n"
  "s_mov_b64 exec, s[22:23]\n"
  "global_store_dword v0, v15, s[64:65] sc1\n"
  "s_mov_b64 exec, -1\n"
  "ds_read_b128 v[88:91], v76 offset:2048\n"
  "s_and_b32 s77, s56, 1\n"
  "s_xor_b32 s76, s77, 1\n"
  "s_lshl_b32 s76, s76, 14\n"
  "ds_read_b128 v[92:95], v76 offset:3072\n"
  "s_add_u32 s26, s76, 50432\n"
  "s_mov_b32 m0, s26\n"
  "s_add_u32 s75, s56, 1\n"
  "ds_read_b128 v[96:99], v76 offset:4096\n"
  "s_and_b32 s74, s75, 0x78\n"
  "s_add_u32 s74, s74, s75\n"
  "s_lshl_b32 s74, s74, 10\n"
  "ds_read_b128 v[100:103], v76 offset:5120\n"
  "s_add_u32 s74, s74, s101\n"
  "s_add_u32 s62, s36, s74\n"
  "s_addc_u32 s63, s37, 0\n"
  "ds_read_b128 v[104:107], v76 offset:6144\n"
  "s_mov_b32 exec_lo, s98\n"
  "s_and_b32 exec_hi, s98, 0xffff\n"
  "global_load_lds_dword v0, s[62:63] sc1\n"
  "s_mov_b64 exec, -1\n"
  "ds_read_b128 v[108:111], v76 offset:7168\n"
  "s_add_u32 m0, s42, s76\n"
  "s_mov_b64 exec, 0x8000\n"
  "ds_max_u32 v76, v78 offset:50432\n"
  "s_mov_b64 exec, -1\n"
  "ds_read_b128 a[224:227], v76 offset:8192\n"
  "s_lshl_b32 s75, s77, 16\n"
  "s_add_u32 s70, s36, s75\n"
  "s_addc_u32 s71, s37, 0\n"
  "ds_read_b128 a[228:231], v76 offset:9216\n"
  "s_add_u32 s75, s56, 4\n"
  "s_and_b32 s74, s75, 0x78\n"
  "s_add_u32 s74, s74, s75\n"
  "ds_read_b128 a[232:235], v76 offset:10240\n"
  "s_lshl_b32 s74, s74, 10\n"
  "s_add_u32 s74, s74, s101\n"
  "s_add_u32 s74, s74, s7\n"
  "ds_read_b128 a[236:239], v76 offset:11264\n"
  "s_add_u32 s66, s36, s74\n"
  "s_addc_u32 s67, s37, 0\n"
  "ds_read_b128 a[240:243], v76 offset:12288\n"
  "ds_read_b128 a[244:247], v76 offset:13312\n"
  "ds_read_b128 a[248:251], v76 offset:14336\n"
  "ds_read_b128 a[252:255], v76 offset:15360\n"
  "s_cmp_lg_u32 s48, 3\n"
  "s_waitcnt vmcnt(4) lgkmcnt(0)\n"
  "s_cbranch_scc0 HD8\n"
  "s_waitcnt vmcnt(3)\n"
  "HD8:\n"
  "s_barrier\n"
  "; BODY\n"
  "ds_read_b128 v[20:23], v0 offset:32768\n"
  "ds_read_b128 v[24:27], v0 offset:33792\n"
  "ds_read_b128 v[28:31], v0 offset:34816\n"
  "ds_read_b128 v[32:35], v0 offset:35840\n"
  "s_waitcnt lgkmcnt(3)\n"
  "v_smfmac_f32_16x16x64_bf16 v[56:59], v[20:23], v[112:119], v6\n"
  "v_smfmac_f32_16x16x64_bf16 v[60:63], v[20:23], v[120:127], v6\n"
  "s_waitcnt vmcnt(2)\n"
  "s_barrier\n"
  "ds_read_b128 v[36:39], v0 offset:36864\n"
  "s_waitcnt lgkmcnt(3)\n"
  "v_smfmac_f32_16x16x64_bf16 v[56:59], v[24:27], v[128:135], v6\n"
  "ds_read_b128 v[40:43], v0 offset:37888\n"
  "v_smfmac_f32_16x16x64_bf16 v[60:63], v[24:27], v[136:143], v6\n"
  "ds_read_b128 v[44:47], v0 offset:38912\n"
  "s_waitcnt lgkmcnt(4)\n"
  "v_smfmac_f32_16x16x64_bf16 v[56:59], v[28:31], v[144:151], v6\n"
  "ds_read_b128 v[48:51], v0 offset:39936\n"
  "v_smfmac_f32_16x16x64_bf16 v[60:63], v[28:31], v[152:159], v6\n"
  "ds_read_b128 v[52:55], v0 offset:40960\n"
  "s_waitcnt lgkmcnt(5)\n"
  "v_smfmac_f32_16x16x64_bf16 v[56:59], v[32:35], v[160:167], v6\n"
  "s_mul_i32 s74, s57, 131072\n"
  "s_add_u32 s74, s74, s46\n"
  "v_smfmac_f32_16x16x64_bf16 v[60:63], v[32:35], v[168:175], v6\n"
  "s_add_u32 s68, s36, s74\n"
  "s_addc_u32 s69, s37, 0\n"
  "s_waitcnt lgkmcnt(4)\n"
  "v_smfmac_f32_16x16x64_bf16 v[56:59], v[36:39], v[176:183], v6\n"
  "s_add_u32 s72, s36, s53\n"
  "s_addc_u32 s73, s37, 0\n"
  "s_waitcnt lgkmcnt(3)\n"
  "v_smfmac_f32_16x16x64_bf16 v[56:59], v[40:43], v[192:199], v6\n"
  "s_add_u32 s78, s56, 1\n"
  "s_bitcmp1_b64 s[4:5], s78\n"
  "s_waitcnt lgkmcnt(2)\n"
  "v_smfmac_f32_16x16x64_bf16 v[56:59], v[44:47], v[208:215], v6\n"
  "s_cselect_b32 s74, 8192, 0\n"
  "s_lshl_b32 s75, s78, 21\n"
  "s_waitcnt lgkmcnt(1)\n"
  "v_smfmac_f32_16x16x64_bf16 v[56:59], v[48:51], v[224:231], v6\n"
  "s_add_u32 s74, s74, s75\n"
  "s_add_u32 s74, s74, 20971520\n"
  "s_waitcnt lgkmcnt(0)\n"
  "v_smfmac_f32_16x16x64_bf16 v[56:59], v[52:55], v[240:247], v6\n"
  "s_xor_b32 s75, s74, s47\n"
  "s_add_u32 s40, s36, s75\n"
  "v_smfmac_f32_16x16x64_bf16 v[60:63], v[36:39], v[184:191], v6\n"
  "s_addc_u32 s41, s37, 0\n"
  "s_add_u32 s76, s16, s34\n"
  "v_smfmac_f32_16x16x64_bf16 v[60:63], v[40:43], v[200:207], v6\n"
  "s_sub_u32 s76, s76, s6\n"
  "s_min_u32 s33, s33, s76\n"
  "v_smfmac_f32_16x16x64_bf16 v[60:63], v[44:47], v[216:223], v6\n"
  "v_add_f32 v56, v56, v58\n"
  "v_smfmac_f32_16x16x64_bf16 v[60:63], v[48:51], v[232:239], v6\n"
  "v_add_f32 v57, v57, v59\n"
  "v_smfmac_f32_16x16x64_bf16 v[60:63], v[52:55], v[248:255], v6\n"
  "v_cvt_pk_bf16_f32 v14, v56, v57\n"
  "v_smfmac_f32_16x16x64_bf16 v[64:67], v[20:23], a[0:7], v6\n"
  "ds_write2_b32 v3, v14, v14 offset0:0 offset1:1\n"
  "v_smfmac_f32_16x16x64_bf16 v[68:71], v[20:23], a[8:15], v6\n"
  "v_cmp_u_f32 vcc, v56, v57\n"
  "v_smfmac_f32_16x16x64_bf16 v[64:67], v[24:27], a[16:23], v6\n"
  "v_add_f32 v60, v60, v62\n"
  "v_smfmac_f32_16x16x64_bf16 v[68:71], v[24:27], a[24:31], v6\n"
  "v_add_f32 v61, v61, v63\n"
  "v_smfmac_f32_16x16x64_bf16 v[64:67], v[28:31], a[32:39], v6\n"
  "v_cvt_pk_bf16_f32 v15, v60, v61\n"
  "v_smfmac_f32_16x16x64_bf16 v[68:71], v[28:31], a[40:47], v6\n"
  "ds_write2_b32 v3, v15, v15 offset0:128 offset1:129\n"
  "v_smfmac_f32_16x16x64_bf16 v[64:67], v[32:35], a[48:55], v6\n"
  "ds_write_b64 v4, v[14:15]\n"
  "v_smfmac_f32_16x16x64_bf16 v[68:71], v[32:35], a[56:63], v6\n"
  "s_cbranch_vccnz HBADM\n"
  "HOKM:\n"
  "v_smfmac_f32_16x16x64_bf16 v[64:67], v[36:39], a[64:71], v6\n"
  "s_sub_u32 s6, s6, 0x100000\n"
  "s_cmp_lt_u32 s33, 6000\n"
  "s_cselect_b32 s24, 1, 0\n"
  "v_smfmac_f32_16x16x64_bf16 v[68:71], v[36:39], a[72:79], v6\n"
  "s_add_u32 s74, s56, s3\n"
  "s_and_b32 s74, s74, 7\n"
  "s_cselect_b32 s24, 0, s24\n"
  "v_smfmac_f32_16x16x64_bf16 v[64:67], v[40:43], a[80:87], v6\n"
  "s_mov_b32 s25, 0\n"
  "s_waitcnt lgkmcnt(0)\n"
  "s_barrier\n"
  "v_smfmac_f32_16x16x64_bf16 v[68:71], v[40:43], a[88:95], v6\n"
  "ds_read_b64_tr_b16 v[20:21], v5 offset:0\n"
  "ds_read_b64_tr_b16 v[22:23], v5 offset:512\n"
  "v_smfmac_f32_16x16x64_bf16 v[64:67], v[44:47], a[96:103], v6\n"
  "global_load_dwordx4 v[36:39], v2, s[68:69] offset:0 sc1\n"
  "ds_read_b64_tr_b16 v[24:25], v5 offset:1024\n"
  "v_smfmac_f32_16x16x64_bf16 v[64:67], v[48:51], a[112:119], v6\n"
  "ds_read_b64_tr_b16 v[26:27], v5 offset:1536\n"
  "ds_read_b64_tr_b16 v[28:29], v5 offset:2048\n"
  "v_smfmac_f32_16x16x64_bf16 v[64:67], v[52:55], a[128:135], v6\n"
  "ds_read_b64_tr_b16 v[30:31], v5 offset:2560\n"
  "ds_read_b64_tr_b16 v[32:33], v5 offset:3072\n"
  "v_smfmac_f32_16x16x64_bf16 v[68:71], v[44:47], a[104:111], v6\n"
  "ds_read_b64_tr_b16 v[34:35], v5 offset:3584\n"
  "v_smfmac_f32_16x16x64_bf16 v[68:71], v[48:51], a[120:127], v6\n"
  "global_load_dwordx4 v[40:43], v2, s[68:69] offset:1024 sc1\n"
  "ds_read_b64_tr_b16 v[44:45], v5 offset:4096\n"
  "ds_read_b64_tr_b16 v[46:47], v5 offset:4608\n"
  "v_smfmac_f32_16x16x64_bf16 v[68:71], v[52:55], a[136:143], v6\n"
  "v_add_f32 v64, v64, v66\n"
  "s_waitcnt lgkmcnt(8)\n"
  "v_mfma_f32_16x16x32_bf16 v[56:59], v[80:83], v[20:23], 0\n"
  "v_add_f32 v65, v65, v67\n"
  "v_cvt_pk_bf16_f32 v14, v64, v65\n"
  "s_waitcnt lgkmcnt(6)\n"
  "v_mfma_f32_16x16x32_bf16 v[56:59], v[88:91], v[24:27], v[56:59]\n"
  "s_nop 0\n"
  "v_add_f32 v68, v68, v70\n"
  "v_add_f32 v69, v69, v71\n"
  "s_waitcnt lgkmcnt(4)\n"
  "v_mfma_f32_16x16x32_bf16 v[56:59], v[96:99], v[28:31], v[56:59]\n"
  "v_cvt_pk_bf16_f32 v15, v68, v69\n"
  "ds_write_b64 v4, v[14:15] offset:2048\n"
  "s_waitcnt lgkmcnt(3)\n"
  "v_mfma_f32_16x16x32_bf16 v[56:59], v[104:107], v[32:35], v[56:59]\n"
  "ds_read_b64_tr_b16 v[48:49], v5 offset:5120\n"
  "ds_read_b64_tr_b16 v[50:51], v5 offset:5632\n"
  "v_mfma_f32_16x16x32_bf16 v[60:63], v[84:87], v[20:23], 0\n"
  "s_mov_b64 exec, s[0:1]\n"
  "global_store_dwordx2 v79, v[14:15], s[72:73] sc1\n"
  "s_mov_b64 exec, -1\n"
  "v_mfma_f32_16x16x32_bf16 v[60:63], v[92:95], v[24:27], v[60:63]\n"
  "s_cmp_eq_u32 s56, 63\n"
  "s_cbranch_scc1 ENDH\n"
  "v_mfma_f32_16x16x32_bf16 v[60:63], v[100:103], v[28:31], v[60:63]\n"
  "v_exp_f32 v56, v56\n"
  "v_mfma_f32_16x16x32_bf16 v[60:63], v[108:111], v[32:35], v[60:63]\n"
  "s_waitcnt lgkmcnt(2)\n"
  "s_barrier\n"
  "v_mfma_f32_32x32x16_bf16 v[80:95], v[72:75], v[72:75], 0\n"
  "ds_read_b64_tr_b16 v[96:97], v5 offset:6144\n"
  "ds_read_b64_tr_b16 v[98:99], v5 offset:6656\n"
  "ds_read_b64_tr_b16 v[100:101], v5 offset:7168\n"
  "ds_read_b64_tr_b16 v[102:103], v5 offset:7680\n"
  "v_exp_f32 v57, v57\n"
  "v_exp_f32 v58, v58\n"
  "v_exp_f32 v59, v59\n"
  "v_exp_f32 v60, v60\n"
  "s_waitcnt lgkmcnt(2)\n"
  "v_mfma_f32_16x16x32_bf16 v[52:55], v[96:99], v[44:47], 0\n"
  "v_exp_f32 v61, v61\n"
  "s_waitcnt lgkmcnt(0)\n"
  "v_mfma_f32_16x16x32_bf16 v[52:55], v[100:103], v[48:51], v[52:55]\n"
  "global_load_lds_dwordx4 v0, s[40:41] offset:0 sc1\n"
  "v_exp_f32 v62, v62\n"
  "v_exp_f32 v63, v63\n"
  "v_perm_b32 v44, v57, v56, v7\n"
  "v_perm_b32 v45, v59, v58, v7\n"
  "v_perm_b32 v46, v61, v60, v7\n"
  "v_perm_b32 v47, v63, v62, v7\n"
  "v_perm_b32 v48, v57, v56, v8\n"
  "v_perm_b32 v49, v59, v58, v8\n"
  "v_mfma_f32_16x16x32_bf16 v[68:71], v[16:19], v[44:47], 0\n"
  "v_perm_b32 v50, v61, v60, v8\n"
  "v_perm_b32 v51, v63, v62, v8\n"
  "v_mfma_f32_16x16x32_bf16 v[20:23], a[224:227], v[44:47], 0\n"
  "v_mul_f32_dpp v64, v52, v77 quad_perm:[0,1,0,1] row_mask:0xf bank_mask:0xf\n"
  "v_mul_f32_dpp v65, v53, v77 quad_perm:[0,1,0,1] row_mask:0xf bank_mask:0xf\n"
  "v_mfma_f32_16x16x32_bf16 v[68:71], v[16:19], v[48:51], v[68:71]\n"
  "v_fmac_f32_dpp v64, v54, v77 quad_perm:[2,3,2,3] row_mask:0xf bank_mask:0xf\n"
  "v_fmac_f32_dpp v65, v55, v77 quad_perm:[2,3,2,3] row_mask:0xf bank_mask:0xf\n"
  "v_mfma_f32_16x16x32_bf16 v[20:23], a[228:231], v[48:51], v[20:23]\n"
  "v_exp_f32 v64, v64\n"
  "v_mfma_f32_16x16x32_bf16 v[24:27], a[232:235], v[44:47], 0\n"
  "v_exp_f32 v65, v65\n"
  "v_mfma_f32_16x16x32_bf16 v[24:27], a[236:239], v[48:51], v[24:27]\n"
  "v_perm_b32 v12, v65, v64, v7\n"
  "v_perm_b32 v13, v65, v64, v8\n"
  "v_mfma_f32_16x16x32_bf16 v[28:31], a[240:243], v[44:47], 0\n"
  "global_load_lds_dwordx4 v0, s[40:41] offset:1024 sc1\n"
  "HPV:\n"
  "v_mfma_f32_16x16x16_bf16 v[68:71], v[16:17], v[12:13], v[68:71]\n"
  "s_waitcnt vmcnt(4)\n"
  "v_mfma_f32_16x16x16_bf16 v[20:23], v[36:37], v[12:13], v[20:23]\n"
  "v_mfma_f32_16x16x16_bf16 v[24:27], v[38:39], v[12:13], v[24:27]\n"
  "v_mfma_f32_16x16x32_bf16 v[28:31], a[244:247], v[48:51], v[28:31]\n"
  "v_rcp_f32 v78, v68\n"
  "v_mfma_f32_16x16x32_bf16 v[32:35], a[248:251], v[44:47], 0\n"
  "v_mul_f32 v20, v78, v20\n"
  "v_mul_f32 v21, v78, v21\n"
  "v_mfma_f32_16x16x32_bf16 v[32:35], a[252:255], v[48:51], v[32:35]\n"
  "v_mul_f32 v22, v78, v22\n"
  "v_mul_f32 v23, v78, v23\n"
  "s_waitcnt vmcnt(3)\n"
  "v_mfma_f32_16x16x16_bf16 v[28:31], v[40:41], v[12:13], v[28:31]\n"
  "v_cvt_pk_bf16_f32 v104, v20, v21\n"
  "v_cvt_pk_bf16_f32 v105, v22, v23\n"
  "v_mul_f32 v24, v78, v24\n"
  "v_mul_f32 v25, v78, v25\n"
  "v_mfma_f32_16x16x16_bf16 v[32:35], v[42:43], v[12:13], v[32:35]\n"
  "v_mul_f32 v26, v78, v26\n"
  "v_mul_f32 v27, v78, v27\n"
  "v_cvt_pk_bf16_f32 v106, v24, v25\n"
  "v_cvt_pk_bf16_f32 v107, v26, v27\n"
  "global_load_lds_dwordx4 v0, s[40:41] offset:2048 sc1\n"
  "v_subrev_u32 v79, s60, v68\n"
  "v_smfmac_f32_16x16x64_bf16 v[72:75], v[104:107], a[208:215], v6\n"
  "v_mul_f32 v28, v78, v28\n"
  "v_mul_f32 v29, v78, v29\n"
  "v_mul_f32 v30, v78, v30\n"
  "v_smfmac_f32_16x16x64_bf16 v[80:83], v[104:107], a[144:151], v6\n"
  "v_mul_f32 v31, v78, v31\n"
  "v_cvt_pk_bf16_f32 v108, v28, v29\n"
  "v_cvt_pk_bf16_f32 v109, v30, v31\n"
  "v_smfmac_f32_16x16x64_bf16 v[84:87], v[104:107], a[160:167], v6\n"
  "v_mul_f32 v32, v78, v32\n"
  "v_mul_f32 v33, v78, v33\n"
  "v_mul_f32 v34, v78, v34\n"
  "v_smfmac_f32_16x16x64_bf16 v[88:91], v[104:107], a[176:183], v6\n"
  "v_mul_f32 v35, v78, v35\n"
  "v_cvt_pk_bf16_f32 v110, v32, v33\n"
  "v_cvt_pk_bf16_f32 v111, v34, v35\n"
  "v_smfmac_f32_16x16x64_bf16 v[92:95], v[104:107], a[192:199], v6\n"
  "v_cmp_le_u32 vcc, s61, v79\n"
  "global_load_lds_dwordx4 v0, s[40:41] offset:3072 sc1\n"
  "v_smfmac_f32_16x16x64_bf16 v[72:75], v[108:111], a[216:223], v6\n"
  "v_smfmac_f32_16x16x64_bf16 v[80:83], v[108:111], a[152:159], v6\n"
  "v_smfmac_f32_16x16x64_bf16 v[84:87], v[108:111], a[168:175], v6\n"
  "s_nop 0\n"
  "v_add_f32 v72, v72, v74\n"
  "s_cbranch_vccnz HBADZ\n"
  "HOKZ:\n"
  "v_smfmac_f32_16x16x64_bf16 v[88:91], v[108:111], a[184:191], v6\n"
  "v_add_f32 v73, v73, v75\n"
  "v_cvt_pk_bf16_f32 v74, v72, v73\n"
  "v_smfmac_f32_16x16x64_bf16 v[92:95], v[108:111], a[200:207], v6\n"
  "s_mov_b64 exec, s[38:39]\n"
  "global_store_dword v11, v74, s[70:71] offset:0 sc1\n"
  "s_mov_b64 exec, -1\n"
  "v_add_f32 v80, v80, v82\n"
  "v_add_f32 v81, v81, v83\n"
  "v_cvt_pk_bf16_f32 v82, v80, v81\n"
  "global_store_dword v9, v82, s[70:71] offset:0 sc1\n"
  "v_add_f32 v84, v84, v86\n"
  "v_add_f32 v85, v85, v87\n"
  "v_cvt_pk_bf16_f32 v86, v84, v85\n"
  "global_store_dword v9, v86, s[70:71] offset:128 sc1\n"
  "v_add_f32 v88, v88, v90\n"
  "v_add_f32 v89, v89, v91\n"
  "v_cvt_pk_bf16_f32 v90, v88, v89\n"
  "global_store_dword v10, v90, s[70:71] offset:0 sc1\n"
  "v_add_f32 v92, v92, v94\n"
  "v_add_f32 v93, v93, v95\n"
  "v_cvt_pk_bf16_f32 v94, v92, v93\n"
  "global_store_dword v10, v94, s[70:71] offset:128 sc1\n"
  "s_memtime s[16:17]\n"
  "; BODYEND\n"
  "v_add_f32 v79, v80, v81\n"
  "v_add_f32 v78, v84, v85\n"
  "v_add_f32 v79, v79, v78\n"
  "v_add_f32 v78, v88, v89\n"
  "v_add_f32 v79, v79, v78\n"
  "v_add_f32 v78, v92, v93\n"
  "v_add_f32 v79, v79, v78\n"
  "v_add_f32 v78, v72, v73\n"
  "v_add_f32 v79, v79, v78\n"
  "v_cmp_u_f32 vcc, v79, v79\n"
  "ds_read_b32 v15, v76 offset:50432\n"
  "s_memrealtime s[76:77]\n"
  "v_mov_b64 v[56:57], 0\n"
  "v_mov_b64 v[58:59], 0\n"
  "v_mov_b64 v[60:61], 0\n"
  "v_mov_b64 v[62:63], 0\n"
  "v_mov_b64 v[64:65], 0\n"
  "v_mov_b64 v[66:67], 0\n"
  "v_mov_b64 v[68:69], 0\n"
  "v_mov_b64 v[70:71], 0\n"
  "s_cbranch_vccnz HBADV\n"
  "HOKV:\n"
  "v_mov_b64 v[72:73], 0\n"
  "v_mov_b64 v[74:75], 0\n"
  "v_mov_b32 v14, s33\n"
  "v_xor_b32 v76, 0x4000, v76\n"
  "s_waitcnt lgkmcnt(0)\n"
  "v_readlane_b32 s8, v15, 0\n"
  "v_readlane_b32 s9, v15, 2\n"
  "v_readlane_b32 s10, v15, 4\n"
  "v_readlane_b32 s11, v15, 6\n"
  "v_readlane_b32 s12, v15, 8\n"
  "v_readlane_b32 s13, v15, 10\n"
  "v_readlane_b32 s74, v15, 12\n"
  "v_readlane_b32 s75, v15, 14\n"
  "v_readlane_b32 s49, v15, 15\n"
  "s_mov_b64 exec, s[24:25]\n"
  "global_atomic_umax v0, v14, s[66:67] sc1\n"
  "s_mov_b64 exec, -1\n"
  "s_add_u32 s56, s56, 1\n"
  "s_min_u32 s74, s74, s75\n"
  "s_cmp_le_u32 s74, s56\n"
  "s_cbranch_scc1 HKW\n"
  "HKOK:\n"
  "s_mov_b32 m0, s26\n"
  "s_mov_b32 exec_lo, 0\n"
  "s_and_b32 exec_hi, s98, 0x01010000\n"
  "global_load_lds_dword v0, s[18:19] sc1\n"
  "s_mov_b64 exec, -1\n"
  "s_sub_u32 s78, s56, 1\n"
  "s_max_u32 s8, s8, s9\n"
  "s_max_u32 s10, s10, s11\n"
  "s_max_u32 s8, s8, s10\n"
  "s_max_u32 s12, s12, s13\n"
  "s_cmp_eq_u32 s8, 0\n"
  "s_cselect_b32 s8, s14, s8\n"
  "s_sub_i32 s75, s8, s55\n"
  "s_min_i32 s75, s75, 24\n"
  "s_max_i32 s75, s75, -6\n"
  "s_add_u32 s55, s55, s75\n"
  "s_min_u32 s75, s8, s14\n"
  "s_max_u32 s55, s55, s75\n"
  "s_max_u32 s75, s8, s14\n"
  "s_add_u32 s75, s75, 40\n"
  "s_min_u32 s55, s55, s75\n"
  "s_mov_b32 s14, s8\n"
  "s_cmp_eq_u32 s12, 0\n"
  "s_cselect_b32 s12, s15, s12\n"
  "s_sub_i32 s75, s12, s51\n"
  "s_min_i32 s75, s75, 24\n"
  "s_max_i32 s75, s75, -6\n"
  "s_add_u32 s51, s51, s75\n"
  "s_min_u32 s75, s12, s15\n"
  "s_max_u32 s51, s51, s75\n"
  "s_max_u32 s75, s12, s15\n"
  "s_add_u32 s75, s75, 40\n"
  "s_min_u32 s51, s51, s75\n"
  "s_mov_b32 s15, s12\n"
  "s_lshl_b32 s75, s78, 1\n"
  "s_min_u32 s75, s75, 31\n"
  "s_lshr_b32 s75, 500, s75\n"
  "s_add_u32 s74, s55, s75\n"
  "s_add_u32 s74, s74, 130\n"
  "s_add_u32 s50, s74, s51\n"
  "s_add_u32 s50, s50, s75\n"
  "s_add_u32 s50, s50, 230\n"
  "s_mov_b32 s33, -1\n"
  "s_add_u32 s53, s53, 65536\n"
  "s_add_u32 s57, s57, 1\n"
  "s_cmp_eq_u32 s57, 3\n"
  "s_cselect_b32 s57, 0, s57\n"
  "s_mul_i32 s99, s76, 24\n"
  "s_add_u32 s49, s49, s43\n"
  "s_sub_u32 s6, s49, s35\n"
  "s_mov_b32 s35, 0\n"
  "s_add_u32 s49, s49, s50\n"
  "s_mov_b32 s32, 0\n"
  "s_sub_i32 s74, s49, s99\n"
  "s_cmp_ge_i32 s74, 500\n"
  "s_cbranch_scc1 HTRY\n"
  "s_mov_b32 s32, 1\n"
  "s_add_u32 s99, s99, 680\n"
  "HSK:\n"
  "s_add_u32 s49, s49, 2200\n"
  "s_sub_i32 s74, s49, s99\n"
  "s_cmp_lt_i32 s74, 0\n"
  "s_cbranch_scc1 HSK\n"
  "s_branch HTRY\n"
  "HBADM:\n"
  "s_waitcnt lgkmcnt(0)\n"
  "s_nop 3\n"
  "v_mov_b32 v1, v1\n"
  "s_nop 2\n"
  "v_mov_b32 v1, v1\n"
  "s_nop 2\n"
  "s_mov_b32 s79, 0xff820000\n"
  "v_max3_u32 v78, v20, v21, v22\n"
  "v_max3_u32 v78, v78, v23, v24\n"
  "v_max3_u32 v78, v78, v25, v26\n"
  "v_max3_u32 v78, v78, v27, v28\n"
  "v_max3_u32 v78, v78, v29, v30\n"
  "v_max3_u32 v78, v78, v31, v32\n"
  "v_max3_u32 v78, v78, v33, v34\n"
  "v_max3_u32 v78, v78, v35, v36\n"
  "v_max3_u32 v78, v78, v37, v38\n"
  "v_max3_u32 v78, v78, v39, v40\n"
  "v_max3_u32 v78, v78, v41, v42\n"
  "v_max3_u32 v78, v78, v43, v44\n"
  "v_max3_u32 v78, v78, v45, v46\n"
  "v_max3_u32 v78, v78, v47, v48\n"
  "v_max3_u32 v78, v78, v49, v50\n"
  "v_max3_u32 v78, v78, v51, v52\n"
  "v_max3_u32 v78, v78, v53, v54\n"
  "v_max_u32 v78, v78, v55\n"
  "v_cmp_le_u32 vcc, s79, v78\n"
  "s_cbranch_vccnz HBM2\n"
  "s_mov_b32 s79, 0x7fff7fff\n"
  "v_and_b32 v78, s79, v20\n"
  "v_and_b32 v12, s79, v21\n"
  "v_pk_max_u16 v78, v78, v12\n"
  "v_and_b32 v12, s79, v22\n"
  "v_pk_max_u16 v78, v78, v12\n"
  "v_and_b32 v12, s79, v23\n"
  "v_pk_max_u16 v78, v78, v12\n"
  "v_and_b32 v12, s79, v24\n"
  "v_pk_max_u16 v78, v78, v12\n"
  "v_and_b32 v12, s79, v25\n"
  "v_pk_max_u16 v78, v78, v12\n"
  "v_and_b32 v12, s79, v26\n"
  "v_pk_max_u16 v78, v78, v12\n"
  "v_and_b32 v12, s79, v27\n"
  "v_pk_max_u16 v78, v78, v12\n"
  "v_and_b32 v12, s79, v28\n"
  "v_pk_max_u16 v78, v78, v12\n"
  "v_and_b32 v12, s79, v29\n"
  "v_pk_max_u16 v78, v78, v12\n"
  "v_and_b32 v12, s79, v30\n"
  "v_pk_max_u16 v78, v78, v12\n"
  "v_and_b32 v12, s79, v31\n"
  "v_pk_max_u16 v78, v78, v12\n"
  "v_and_b32 v12, s79, v32\n"
  "v_pk_max_u16 v78, v78, v12\n"
  "v_and_b32 v12, s79, v33\n"
  "v_pk_max_u16 v78, v78, v12\n"
  "v_and_b32 v12, s79, v34\n"
  "v_pk_max_u16 v78, v78, v12\n"
  "v_and_b32 v12, s79, v35\n"
  "v_pk_max_u16 v78, v78, v12\n"
  "v_and_b32 v12, s79, v36\n"
  "v_pk_max_u16 v78, v78, v12\n"
  "v_and_b32 v12, s79, v37\n"
  "v_pk_max_u16 v78, v78, v12\n"
  "v_and_b32 v12, s79, v38\n"
  "v_pk_max_u16 v78, v78, v12\n"
  "v_and_b32 v12, s79, v39\n"
  "v_pk_max_u16 v78, v78, v12\n"
  "v_and_b32 v12, s79, v40\n"
  "v_pk_max_u16 v78, v78, v12\n"
  "v_and_b32 v12, s79, v41\n"
  "v_pk_max_u16 v78, v78, v12\n"
  "v_and_b32 v12, s79, v42\n"
  "v_pk_max_u16 v78, v78, v12\n"
  "v_and_b32 v12, s79, v43\n"
  "v_pk_max_u16 v78, v78, v12\n"
  "v_and_b32 v12, s79, v44\n"
  "v_pk_max_u16 v78, v78, v12\n"
  "v_and_b32 v12, s79, v45\n"
  "v_pk_max_u16 v78, v78, v12\n"
  "v_and_b32 v12, s79, v46\n"
  "v_pk_max_u16 v78, v78, v12\n"
  "v_and_b32 v12, s79, v47\n"
  "v_pk_max_u16 v78, v78, v12\n"
  "v_and_b32 v12, s79, v48\n"
  "v_pk_max_u16 v78, v78, v12\n"
  "v_and_b32 v12, s79, v49\n"
  "v_pk_max_u16 v78, v78, v12\n"
  "v_and_b32 v12, s79, v50\n"
  "v_pk_max_u16 v78, v78, v12\n"
  "v_and_b32 v12, s79, v51\n"
  "v_pk_max_u16 v78, v78, v12\n"
  "v_and_b32 v12, s79, v52\n"
  "v_pk_max_u16 v78, v78, v12\n"
  "v_and_b32 v12, s79, v53\n"
  "v_pk_max_u16 v78, v78, v12\n"
  "v_and_b32 v12, s79, v54\n"
  "v_pk_max_u16 v78, v78, v12\n"
  "v_and_b32 v12, s79, v55\n"
  "v_pk_max_u16 v78, v78, v12\n"
  "v_lshrrev_b32 v12, 16, v78\n"
  "v_and_b32 v78, 0xffff, v78\n"
  "v_max_u32 v78, v78, v12\n"
  "v_cmp_lt_u32 vcc, 0x7f81, v78\n"
  "s_cbranch_vccz HOKM\n"
  "HBM2:\n"
  "s_memrealtime s[76:77]\n"
  "v_mov_b64 v[56:57], 0\n"
  "v_mov_b64 v[58:59], 0\n"
  "v_mov_b64 v[60:61], 0\n"
  "v_mov_b64 v[62:63], 0\n"
  "v_mov_b64 v[64:65], 0\n"
  "v_mov_b64 v[66:67], 0\n"
  "v_mov_b64 v[68:69], 0\n"
  "v_mov_b64 v[70:71], 0\n"
  "s_barrier\n"
  "s_sub_u32 s58, s58, 1\n"
  "s_cbranch_scc1 QUITH\n"
  "s_add_u32 s59, s59, 1\n"
  "s_mov_b32 s32, 1\n"
  "s_waitcnt lgkmcnt(0)\n"
  "s_mul_i32 s99, s76, 24\n"
  "s_add_u32 s99, s99, 480\n"
  "s_branch HSK\n"
  "HKW:\n"
  "s_waitcnt vmcnt(0) lgkmcnt(0)\n"
  "s_nop 3\n"
  "v_mov_b32 v1, v1\n"
  "s_nop 2\n"
  "v_mov_b32 v1, v1\n"
  "s_nop 2\n"
  "s_add_u32 s74, s36, s2\n"
  "s_addc_u32 s75, s37, 0\n"
  "PLHK2:\n"
  "v_and_b32 v12, 128, v0\n"
  "global_load_dword v12, v12, s[74:75] sc1\n"
  "s_waitcnt vmcnt(0)\n"
  "v_readfirstlane_b32 s76, v12\n"
  "v_readlane_b32 s77, v12, 8\n"
  "s_min_u32 s76, s76, s77\n"
  "s_cmp_gt_u32 s76, s56\n"
  "s_cbranch_scc1 PDHK2\n"
  "s_sub_u32 s58, s58, 1\n"
  "s_cbranch_scc1 QUITH\n"
  "s_sleep 2\n"
  "s_branch PLHK2\n"
  "PDHK2:\n"
  "s_add_u32 s59, s59, 0x100\n"
  "global_load_lds_dwordx4 v0, s[40:41] offset:0 sc1\n"
  "global_load_lds_dwordx4 v0, s[40:41] offset:1024 sc1\n"
  "global_load_lds_dwordx4 v0, s[40:41] offset:2048 sc1\n"
  "global_load_lds_dwordx4 v0, s[40:41] offset:3072 sc1\n"
  "s_memrealtime s[76:77]\n"
  "s_waitcnt lgkmcnt(0)\n"
  "s_branch HKOK\n"
  "HBADV:\n"
  "s_waitcnt vmcnt(0) lgkmcnt(0)\n"
  "s_nop 3\n"
  "v_mov_b32 v1, v1\n"
  "s_nop 2\n"
  "v_mov_b32 v1, v1\n"
  "s_nop 2\n"
  "s_mov_b32 s74, 0x7fff7fff\n"
  "v_and_b32 v79, s74, v36\n"
  "v_and_b32 v78, s74, v37\n"
  "v_pk_max_u16 v79, v79, v78\n"
  "v_and_b32 v78, s74, v38\n"
  "v_pk_max_u16 v79, v79, v78\n"
  "v_and_b32 v78, s74, v39\n"
  "v_pk_max_u16 v79, v79, v78\n"
  "v_and_b32 v78, s74, v40\n"
  "v_pk_max_u16 v79, v79, v78\n"
  "v_and_b32 v78, s74, v41\n"
  "v_pk_max_u16 v79, v79, v78\n"
  "v_and_b32 v78, s74, v42\n"
  "v_pk_max_u16 v79, v79, v78\n"
  "v_and_b32 v78, s74, v43\n"
  "v_pk_max_u16 v79, v79, v78\n"
  "v_lshrrev_b32 v78, 16, v79\n"
  "v_and_b32 v79, 0xffff, v79\n"
  "v_max_u32 v79, v79, v78\n"
  "v_cmp_lt_u32 vcc, 0x7f81, v79\n"
  "s_cbranch_vccnz HVL\n"
  "s_mov_b32 s74, 0x7fff7fff\n"
  "s_mov_b32 s75, 0x7f817f81\n"
  "v_and_b32 v79, s74, v82\n"
  "v_pk_min_u16 v79, v79, s75\n"
  "v_bfi_b32 v82, s74, v79, v82\n"
  "v_and_b32 v79, s74, v86\n"
  "v_pk_min_u16 v79, v79, s75\n"
  "v_bfi_b32 v86, s74, v79, v86\n"
  "v_and_b32 v79, s74, v90\n"
  "v_pk_min_u16 v79, v79, s75\n"
  "v_bfi_b32 v90, s74, v79, v90\n"
  "v_and_b32 v79, s74, v94\n"
  "v_pk_min_u16 v79, v79, s75\n"
  "v_bfi_b32 v94, s74, v79, v94\n"
  "v_and_b32 v79, s74, v74\n"
  "v_pk_min_u16 v79, v79, s75\n"
  "v_bfi_b32 v74, s74, v79, v74\n"
  "s_mov_b64 exec, s[38:39]\n"
  "global_store_dword v11, v74, s[70:71] offset:0 sc1\n"
  "s_mov_b64 exec, -1\n"
  "global_store_dword v9, v82, s[70:71] offset:0 sc1\n"
  "global_store_dword v9, v86, s[70:71] offset:128 sc1\n"
  "global_store_dword v10, v90, s[70:71] offset:0 sc1\n"
  "global_store_dword v10, v94, s[70:71] offset:128 sc1\n"
  "s_branch HOKV\n"
  "HVL:\n"
  "s_sub_u32 s58, s58, 1\n"
  "s_cbranch_scc1 QUITH\n"
  "s_add_u32 s59, s59, 0x10000\n"
  "s_mov_b32 s35, 0x100000\n"
  "global_load_dwordx4 v[36:39], v2, s[68:69] offset:0 sc1\n"
  "global_load_dwordx4 v[40:43], v2, s[68:69] offset:1024 sc1\n"
  "s_mov_b32 s74, 0x7fff7fff\n"
  "s_waitcnt vmcnt(1)\n"
  "v_and_b32 v79, s74, v36\n"
  "v_and_b32 v78, s74, v37\n"
  "v_pk_max_u16 v79, v79, v78\n"
  "v_and_b32 v78, s74, v38\n"
  "v_pk_max_u16 v79, v79, v78\n"
  "v_and_b32 v78, s74, v39\n"
  "v_pk_max_u16 v79, v79, v78\n"
  "s_waitcnt vmcnt(0)\n"
  "v_and_b32 v78, s74, v40\n"
  "v_pk_max_u16 v79, v79, v78\n"
  "v_and_b32 v78, s74, v41\n"
  "v_pk_max_u16 v79, v79, v78\n"
  "v_and_b32 v78, s74, v42\n"
  "v_pk_max_u16 v79, v79, v78\n"
  "v_and_b32 v78, s74, v43\n"
  "v_pk_max_u16 v79, v79, v78\n"
  "v_lshrrev_b32 v78, 16, v79\n"
  "v_and_b32 v79, 0xffff, v79\n"
  "v_max_u32 v79, v79, v78\n"
  "v_cmp_lt_u32 vcc, 0x7f81, v79\n"
  "s_cbranch_vccz HREDO\n"
  "s_sleep 4\n"
  "s_branch HVL\n"
  "HREDO:\n"
  "v_mov_b64 v[80:81], 0\n"
  "v_mov_b64 v[82:83], 0\n"
  "v_mov_b64 v[84:85], 0\n"
  "v_mov_b64 v[86:87], 0\n"
  "v_mov_b64 v[88:89], 0\n"
  "v_mov_b64 v[90:91], 0\n"
  "v_mov_b64 v[92:93], 0\n"
  "v_mov_b64 v[94:95], 0\n"
  "v_mov_b64 v[72:73], 0\n"
  "v_mov_b64 v[74:75], 0\n"
  "v_mfma_f32_16x16x32_bf16 v[68:71], v[16:19], v[44:47], 0\n"
  "v_mfma_f32_16x16x32_bf16 v[20:23], a[224:227], v[44:47], 0\n"
  "v_mfma_f32_16x16x32_bf16 v[68:71], v[16:19], v[48:51], v[68:71]\n"
  "v_mfma_f32_16x16x32_bf16 v[20:23], a[228:231], v[48:51], v[20:23]\n"
  "v_mfma_f32_16x16x32_bf16 v[24:27], a[232:235], v[44:47], 0\n"
  "v_mfma_f32_16x16x32_bf16 v[24:27], a[236:239], v[48:51], v[24:27]\n"
  "v_mfma_f32_16x16x32_bf16 v[28:31], a[240:243], v[44:47], 0\n"
  "s_nop 7\n"
  "s_branch HPV\n"
  "HBADZ:\n"
  "s_waitcnt vmcnt(0) lgkmcnt(0)\n"
  "s_nop 3\n"
  "v_mov_b32 v1, v1\n"
  "s_nop 2\n"
  "v_mov_b32 v1, v1\n"
  "s_nop 2\n"
  "s_cmp_eq_u32 s81, s56\n"
  "s_cbranch_scc1 HOKZ\n"
  "s_mov_b32 s81, s56\n"
  "ds_read_b128 v[80:83], v76 offset:0\n"
  "ds_read_b128 v[84:87], v76 offset:1024\n"
  "ds_read_b128 v[88:91], v76 offset:2048\n"
  "ds_read_b128 v[92:95], v76 offset:3072\n"
  "ds_read_b128 v[96:99], v76 offset:4096\n"
  "ds_read_b128 v[100:103], v76 offset:5120\n"
  "ds_read_b128 v[104:107], v76 offset:6144\n"
  "ds_read_b128 v[108:111], v76 offset:7168\n"
  "ds_read_b64_tr_b16 v[20:21], v5 offset:0\n"
  "ds_read_b64_tr_b16 v[22:23], v5 offset:512\n"
  "ds_read_b64_tr_b16 v[24:25], v5 offset:1024\n"
  "ds_read_b64_tr_b16 v[26:27], v5 offset:1536\n"
  "ds_read_b64_tr_b16 v[28:29], v5 offset:2048\n"
  "ds_read_b64_tr_b16 v[30:31], v5 offset:2560\n"
  "ds_read_b64_tr_b16 v[32:33], v5 offset:3072\n"
  "ds_read_b64_tr_b16 v[34:35], v5 offset:3584\n"
  "ds_read_b64_tr_b16 v[44:45], v5 offset:4096\n"
  "ds_read_b64_tr_b16 v[46:47], v5 offset:4608\n"
  "ds_read_b64_tr_b16 v[48:49], v5 offset:5120\n"
  "ds_read_b64_tr_b16 v[50:51], v5 offset:5632\n"
  "s_waitcnt lgkmcnt(10)\n"
  "v_mfma_f32_16x16x32_bf16 v[56:59], v[80:83], v[20:23], 0\n"
  "s_waitcnt lgkmcnt(8)\n"
  "v_mfma_f32_16x16x32_bf16 v[56:59], v[88:91], v[24:27], v[56:59]\n"
  "s_waitcnt lgkmcnt(6)\n"
  "v_mfma_f32_16x16x32_bf16 v[56:59], v[96:99], v[28:31], v[56:59]\n"
  "s_waitcnt lgkmcnt(4)\n"
  "v_mfma_f32_16x16x32_bf16 v[56:59], v[104:107], v[32:35], v[56:59]\n"
  "v_mfma_f32_16x16x32_bf16 v[60:63], v[84:87], v[20:23], 0\n"
  "v_mfma_f32_16x16x32_bf16 v[60:63], v[92:95], v[24:27], v[60:63]\n"
  "v_mfma_f32_16x16x32_bf16 v[60:63], v[100:103], v[28:31], v[60:63]\n"
  "v_mfma_f32_16x16x32_bf16 v[60:63], v[108:111], v[32:35], v[60:63]\n"
  "ds_read_b64_tr_b16 v[96:97], v5 offset:6144\n"
  "ds_read_b64_tr_b16 v[98:99], v5 offset:6656\n"
  "ds_read_b64_tr_b16 v[100:101], v5 offset:7168\n"
  "ds_read_b64_tr_b16 v[102:103], v5 offset:7680\n"
  "s_waitcnt lgkmcnt(2)\n"
  "v_mfma_f32_16x16x32_bf16 v[52:55], v[96:99], v[44:47], 0\n"
  "s_waitcnt lgkmcnt(0)\n"
  "v_mfma_f32_16x16x32_bf16 v[52:55], v[100:103], v[48:51], v[52:55]\n"
  "s_nop 3\n"
  "v_mov_b32 v1, v1\n"
  "s_nop 2\n"
  "v_mul_f32_dpp v64, v52, v77 quad_perm:[0,1,0,1] row_mask:0xf bank_mask:0xf\n"
  "v_mul_f32_dpp v65, v53, v77 quad_perm:[0,1,0,1] row_mask:0xf bank_mask:0xf\n"
  "s_nop 0\n"
  "v_fmac_f32_dpp v64, v54, v77 quad_perm:[2,3,2,3] row_mask:0xf bank_mask:0xf\n"
  "v_fmac_f32_dpp v65, v55, v77 quad_perm:[2,3,2,3] row_mask:0xf bank_mask:0xf\n"
  "v_max3_f32 v79, v56, v57, v58\n"
  "v_max3_f32 v79, v79, v59, v60\n"
  "v_max3_f32 v79, v79, v61, v62\n"
  "v_max3_f32 v79, v79, v63, v64\n"
  "v_max_f32 v79, v79, v65\n"
  "v_mov_b32 v78, v79\n"
  "s_nop 1\n"
  "v_permlane16_swap_b32 v79, v78\n"
  "v_max_f32 v79, v79, v78\n"
  "v_mov_b32 v78, v79\n"
  "s_nop 1\n"
  "v_permlane32_swap_b32 v79, v78\n"
  "v_max_f32 v79, v79, v78\n"
  "v_sub_f32 v56, v56, v79\n"
  "v_sub_f32 v57, v57, v79\n"
  "v_sub_f32 v58, v58, v79\n"
  "v_sub_f32 v59, v59, v79\n"
  "v_sub_f32 v60, v60, v79\n"
  "v_sub_f32 v61, v61, v79\n"
  "v_sub_f32 v62, v62, v79\n"
  "v_sub_f32 v63, v63, v79\n"
  "v_sub_f32 v64, v64, v79\n"
  "v_sub_f32 v65, v65, v79\n"
  "v_exp_f32 v56, v56\n"
  "v_exp_f32 v57, v57\n"
  "v_exp_f32 v58, v58\n"
  "v_exp_f32 v59, v59\n"
  "v_exp_f32 v60, v60\n"
  "v_exp_f32 v61, v61\n"
  "v_exp_f32 v62, v62\n"
  "v_exp_f32 v63, v63\n"
  "v_perm_b32 v44, v57, v56, v7\n"
  "v_perm_b32 v45, v59, v58, v7\n"
  "v_perm_b32 v46, v61, v60, v7\n"
  "v_perm_b32 v47, v63, v62, v7\n"
  "v_perm_b32 v48, v57, v56, v8\n"
  "v_perm_b32 v49, v59, v58, v8\n"
  "v_perm_b32 v50, v61, v60, v8\n"
  "v_perm_b32 v51, v63, v62, v8\n"
  "v_exp_f32 v64, v64\n"
  "v_exp_f32 v65, v65\n"
  "s_nop 0\n"
  "v_perm_b32 v12, v65, v64, v7\n"
  "v_perm_b32 v13, v65, v64, v8\n"
  "s_nop 1\n"
  "s_branch HREDO\n"
  "ENDH:\n"
  "s_waitcnt vmcnt(0) lgkmcnt(0)\n"
  "s_barrier\n"
  "v_mov_b32 v78, 64\n"
  "s_mov_b64 exec, s[22:23]\n"
  "global_store_dword v0, v78, s[64:65] sc1\n"
  "QUITH:\n"
  "s_endpgm\n"
        : "+{v[0:15]}"(vin), "+{s[36:43]}"(sa), "+{s[44:59]}"(sb) : "{s[4:5]}"(pol) : CLOB);
    } else {
      const int ci = tk - 16, cv = gam * 16 + ci, h = cv >> 1, vt = (cv & 1) * 4 + wave;
      {
        const int row = 8 * wave + (lane >> 3);
#pragma unroll 6
        for (int j = 0; j < 18; ++j) dma(p.Wv + cv * 64 + (size_t)j * 32 * 4096, row * 8192 + ((lane & 7) ^ (2 * (((row >> 2) & 1) | ((row >> 3) & 2)))) * 16, wave * 1024 + j * 4096);
      }
      const int line = (2 * ci + (gam & 1)) * 4 + (gam >> 1) * 2 + (g & 1), kb = line >> 3, cp = ((lane >> 3) & 1) + 2 * (lane >> 5);
      const unsigned ms = chunk(cls, cp, kb < 8 ? (kb >> 2) : 4, (kb < 8 ? (kb & 3) : 0) * 1024 + (line & 7) * 128 + (lane & 7) * 16);
      const unsigned ms2 = ms + (kb < 8 ? 2 * CH : 1024), inb = chunk(cls, gam, 28 + 4 * ci, 0);
      const unsigned m2 = chunk(cls, gam, 16 + ((h & 7) >> 1), (h & 1) * 2048 + vt * 256);
      vin[1] = m1pat; vin[2] = lane * 4; vin[3] = ms; vin[4] = idx;
      vin[6] = trrow * 128 + ((2 * wave + (c4 >> 1)) ^ (2 * (((trrow >> 2) & 1) | ((trrow >> 3) & 2)))) * 16 + (c4 & 1) * 8;
      vin[7] = line * 128 + (lane & 7) * 16;
#pragma unroll
      for (int i = 0; i < 4; ++i) vin[8 + i] = ((lane & 7) >> 1) == i ? ((lane & 1) ? 0x3f800000u : 0x3f80u) : 0u;
      vin[12] = c4 * 18432 + (16 * g + 4 * wave + qq) * 16; vin[13] = ms2;
      sa[2] = (C_VF ^ cx) + cv * 128; sa[3] = C_REP ^ cx; sb[5] = gam * 128; sb[6] = cv == 15 && wave == 0 ? C_KVP ^ cx : 0; sb[7] = 2200; sa[5] = 512 + (gam & 1) * 128; sa[6] = 2500; sa[7] = 720;
      sb[0] = chunk(cls, gam, 0, 0) + (wave - 1) * 1024;
      sb[1] = wave == 3 ? chunk(cls, gam, 4, 0) : chunk(cls, gam, 1, (wave + 1) * 1024);
      sb[2] = m2; sb[3] = WO_VP + h * 2048 + vt * 256;
      sb[9] = inb; sb[12] = ci < 9 ? 1 + wave : 0; sb[13] = wave == 1 ? 3072 : CH - 1024; sb[14] = WO_DB;
      UNIFORM(sa, 8); UNIFORM(sb, 16);
      asm volatile(
  "s_nop 4\n"
  "s_lshl_b32 s80, s48, 10\n"
  "s_waitcnt vmcnt(0)\n"
  "s_barrier\n"
  "v_add_u32 v60, 0x8000, v6\n"
  "ds_read_b64_tr_b16 v[96:97], v6 offset:0\n"
  "ds_read_b64_tr_b16 v[98:99], v6 offset:1024\n"
  "ds_read_b64_tr_b16 v[100:101], v6 offset:4096\n"
  "ds_read_b64_tr_b16 v[102:103], v6 offset:5120\n"
  "ds_read_b64_tr_b16 v[104:105], v6 offset:8192\n"
  "ds_read_b64_tr_b16 v[106:107], v6 offset:9216\n"
  "ds_read_b64_tr_b16 v[108:109], v6 offset:12288\n"
  "ds_read_b64_tr_b16 v[110:111], v6 offset:13312\n"
  "ds_read_b64_tr_b16 v[112:113], v6 offset:16384\n"
  "ds_read_b64_tr_b16 v[114:115], v6 offset:17408\n"
  "ds_read_b64_tr_b16 v[116:117], v6 offset:20480\n"
  "ds_read_b64_tr_b16 v[118:119], v6 offset:21504\n"
  "ds_read_b64_tr_b16 v[120:121], v6 offset:24576\n"
  "ds_read_b64_tr_b16 v[122:123], v6 offset:25600\n"
  "ds_read_b64_tr_b16 v[124:125], v6 offset:28672\n"
  "ds_read_b64_tr_b16 v[126:127], v6 offset:29696\n"
  "ds_read_b64_tr_b16 v[128:129], v60 offset:0\n"
  "ds_read_b64_tr_b16 v[130:131], v60 offset:1024\n"
  "ds_read_b64_tr_b16 v[132:133], v60 offset:4096\n"
  "ds_read_b64_tr_b16 v[134:135], v60 offset:5120\n"
  "ds_read_b64_tr_b16 v[136:137], v60 offset:8192\n"
  "ds_read_b64_tr_b16 v[138:139], v60 offset:9216\n"
  "ds_read_b64_tr_b16 v[140:141], v60 offset:12288\n"
  "ds_read_b64_tr_b16 v[142:143], v60 offset:13312\n"
  "ds_read_b64_tr_b16 v[144:145], v60 offset:16384\n"
  "ds_read_b64_tr_b16 v[146:147], v60 offset:17408\n"
  "ds_read_b64_tr_b16 v[148:149], v60 offset:20480\n"
  "ds_read_b64_tr_b16 v[150:151], v60 offset:21504\n"
  "ds_read_b64_tr_b16 v[152:153], v60 offset:24576\n"
  "ds_read_b64_tr_b16 v[154:155], v60 offset:25600\n"
  "ds_read_b64_tr_b16 v[156:157], v60 offset:28672\n"
  "ds_read_b64_tr_b16 v[158:159], v60 offset:29696\n"
  "ds_read_b64_tr_b16 v[160:161], v60 offset:32768\n"
  "ds_read_b64_tr_b16 v[162:163], v60 offset:33792\n"
  "ds_read_b64_tr_b16 v[164:165], v60 offset:36864\n"
  "ds_read_b64_tr_b16 v[166:167], v60 offset:37888\n"
  "v_mov_b32 v64, -1\n"
  "v_mov_b32 v65, -1\n"
  "v_mov_b32 v66, -1\n"
  "v_mov_b32 v67, -1\n"
  "s_mov_b32 s7, s41\n"
  "s_mov_b32 s14, s42\n"
  "s_mov_b32 s15, s43\n"
  "s_mov_b32 s6, 0\n"
  "s_mov_b32 s16, s42\n"
  "s_mov_b32 s17, s43\n"
  "s_mov_b32 s40, 0\n"
  "s_mov_b32 s41, 0\n"
  "s_mov_b32 s42, 100000\n"
  "s_mov_b32 s43, 0\n"
  "s_waitcnt lgkmcnt(0)\n"
  "s_barrier\n"
  "s_mul_i32 s74, s48, 18432\n"
  "v_add_u32 v60, s74, v0\n"
  "ds_write_b128 v60, v[96:99] offset:0\n"
  "ds_write_b128 v60, v[100:103] offset:1024\n"
  "ds_write_b128 v60, v[104:107] offset:2048\n"
  "ds_write_b128 v60, v[108:111] offset:3072\n"
  "ds_write_b128 v60, v[112:115] offset:4096\n"
  "ds_write_b128 v60, v[116:119] offset:5120\n"
  "ds_write_b128 v60, v[120:123] offset:6144\n"
  "ds_write_b128 v60, v[124:127] offset:7168\n"
  "ds_write_b128 v60, v[128:131] offset:8192\n"
  "ds_write_b128 v60, v[132:135] offset:9216\n"
  "ds_write_b128 v60, v[136:139] offset:10240\n"
  "ds_write_b128 v60, v[140:143] offset:11264\n"
  "ds_write_b128 v60, v[144:147] offset:12288\n"
  "ds_write_b128 v60, v[148:151] offset:13312\n"
  "ds_write_b128 v60, v[152:155] offset:14336\n"
  "ds_write_b128 v60, v[156:159] offset:15360\n"
  "ds_write_b128 v60, v[160:163] offset:16384\n"
  "ds_write_b128 v60, v[164:167] offset:17408\n"
  "s_waitcnt lgkmcnt(0)\n"
  "s_barrier\n"
  "ds_read_b128 v[96:99], v12 offset:0\n"
  "ds_read_b128 v[100:103], v12 offset:1024\n"
  "ds_read_b128 v[104:107], v12 offset:2048\n"
  "ds_read_b128 v[108:111], v12 offset:3072\n"
  "ds_read_b128 v[112:115], v12 offset:4096\n"
  "ds_read_b128 v[116:119], v12 offset:5120\n"
  "ds_read_b128 v[120:123], v12 offset:6144\n"
  "ds_read_b128 v[124:127], v12 offset:7168\n"
  "ds_read_b128 v[128:131], v12 offset:8192\n"
  "ds_read_b128 v[132:135], v12 offset:9216\n"
  "ds_read_b128 v[136:139], v12 offset:10240\n"
  "ds_read_b128 v[140:143], v12 offset:11264\n"
  "ds_read_b128 v[144:147], v12 offset:12288\n"
  "ds_read_b128 v[148:151], v12 offset:13312\n"
  "ds_read_b128 v[152:155], v12 offset:14336\n"
  "ds_read_b128 v[156:159], v12 offset:15360\n"
  "ds_read_b128 v[160:163], v12 offset:16384\n"
  "ds_read_b128 v[164:167], v12 offset:17408\n"
  "s_waitcnt lgkmcnt(0)\n"
  "s_barrier\n"
  "s_add_u32 s76, s39, s49\n"
  "s_add_u32 s76, s76, 137216\n"
  "s_add_u32 s74, s36, s76\n"
  "s_addc_u32 s75, s37, 0\n"
  "v_mov_b32 v60, 1\n"
  "s_cmp_eq_u32 s48, 0\n"
  "s_cselect_b64 exec, 1, 0\n"
  "global_atomic_add v0, v60, s[74:75] sc1\n"
  "s_mov_b64 exec, -1\n"
  "s_cmp_eq_u32 s50, 0\n"
  "s_cbranch_scc1 RCD\n"
  "s_add_u32 s62, s36, s50\n"
  "s_addc_u32 s63, s37, 0\n"
  "s_add_u32 s74, s39, 137216\n"
  "s_add_u32 s64, s36, s74\n"
  "s_addc_u32 s65, s37, 0\n"
  "v_lshlrev_b32 v56, 3, v0\n"
  "v_and_b32 v57, 0x180, v56\n"
  "RCP:\n"
  "global_load_dword v60, v56, s[62:63] sc1\n"
  "global_load_dword v61, v57, s[64:65] sc1\n"
  "s_waitcnt vmcnt(0)\n"
  "v_lshrrev_b32 v61, 5, v61\n"
  "v_min_u32 v60, v60, v61\n"
  "v_cmp_eq_u32 vcc, 0, v60\n"
  "s_cbranch_vccz RCG\n"
  "s_sub_u32 s42, s42, 1\n"
  "s_cbranch_scc1 QUITV\n"
  "s_branch RCP\n"
  "RCG:\n"
  "s_memrealtime s[74:75]\n"
  "s_waitcnt lgkmcnt(0)\n"
  "s_mul_i32 s76, s74, 24\n"
  "s_add_u32 s76, s76, 1500\n"
  "s_or_b32 s76, s76, 1\n"
  "v_mov_b32 v60, s76\n"
  "s_mov_b64 exec, 15\n"
  "global_store_dword v57, v60, s[64:65] offset:1024 sc1\n"
  "s_mov_b64 exec, -1\n"
  "RCD:\n"
  "s_add_u32 s77, s39, s49\n"
  "s_add_u32 s77, s77, 138240\n"
  "s_add_u32 s74, s36, s77\n"
  "s_addc_u32 s75, s37, 0\n"
  "PLGV:\n"
  "v_mov_b32 v60, 0\n"
  "global_load_dword v60, v60, s[74:75] sc1\n"
  "s_waitcnt vmcnt(0)\n"
  "v_readfirstlane_b32 s76, v60\n"
  "s_cmp_lg_u32 s76, 0\n"
  "s_cbranch_scc1 PDGV\n"
  "s_sub_u32 s42, s42, 1\n"
  "s_cbranch_scc1 QUITV\n"
  "s_sleep 1\n"
  "s_branch PLGV\n"
  "PDGV:\n"
  "s_mov_b32 s49, s76\n"
  "v_mov_b32 v60, 0\n"
  "v_mov_b32 v61, s49\n"
  "ds_write_b32 v60, v61 offset:9232\n"
  "s_sub_u32 s80, s80, 1024\n"
  "RLOOP:\n"
  "s_mov_b32 s52, 0\n"
  "s_mov_b32 s98, s49\n"
  "RTRY:\n"
  "v_mov_b64 v[56:57], 0\n"
  "v_mov_b64 v[58:59], 0\n"
  "s_mul_i32 s74, s41, 131072\n"
  "s_add_u32 s74, s74, s46\n"
  "s_add_u32 s68, s36, s74\n"
  "s_addc_u32 s69, s37, 0\n"
  "v_mov_b32 v61, 0\n"
  "s_cmp_eq_u32 s48, 0\n"
  "s_cbranch_scc1 NDV\n"
  "s_mul_i32 s78, s41, 163840\n"
  "s_lshl_b32 s79, s52, 16\n"
  "s_add_u32 s79, s79, s78\n"
  "s_add_u32 s79, s79, s44\n"
  "s_add_u32 s62, s36, s79\n"
  "s_addc_u32 s63, s37, 0\n"
  "s_add_u32 s64, s62, s57\n"
  "s_addc_u32 s65, s63, 0\n"
  "s_add_u32 s79, s78, s45\n"
  "s_cmp_eq_u32 s48, 3\n"
  "s_cselect_b32 s74, 10, 16\n"
  "s_lshl_b32 s74, s52, s74\n"
  "s_add_u32 s79, s79, s74\n"
  "s_add_u32 s66, s36, s79\n"
  "s_addc_u32 s67, s37, 0\n"
  "s_add_u32 m0, s80, 0\n"
  "s_add_u32 s79, s80, 3072\n"
  "s_add_u32 s78, s80, 6144\n"
  "s_add_u32 s81, s49, 0\n"
  "WV:\n"
  "s_memrealtime s[74:75]\n"
  "s_waitcnt lgkmcnt(0)\n"
  "s_mul_i32 s74, s74, 24\n"
  "s_sub_i32 s74, s81, s74\n"
  "s_cmp_lt_i32 s74, 750\n"
  "s_cbranch_scc1 WFV\n"
  "s_cmp_gt_i32 s74, 0x40000\n"
  "s_cbranch_scc1 WEV\n"
  "s_sleep 2\n"
  "s_branch WV\n"
  "WFV:\n"
  "s_cmp_lt_i32 s74, 100\n"
  "s_cbranch_scc1 WEV\n"
  "WGV:\n"
  "s_memtime s[76:77]\n"
  "s_memrealtime s[20:21]\n"
  "s_nop 2\n"
  "s_memrealtime s[22:23]\n"
  "s_nop 2\n"
  "s_memrealtime s[24:25]\n"
  "s_nop 2\n"
  "s_memrealtime s[26:27]\n"
  "s_nop 2\n"
  "s_memrealtime s[28:29]\n"
  "s_nop 2\n"
  "s_memrealtime s[30:31]\n"
  "s_waitcnt lgkmcnt(0)\n"
  "s_add_u32 s20, s20, s22\n"
  "s_add_u32 s24, s24, s26\n"
  "s_add_u32 s28, s28, s30\n"
  "s_add_u32 s20, s20, s24\n"
  "s_add_u32 s20, s20, s28\n"
  "s_lshl_b32 s74, s20, 2\n"
  "s_nop 0\n"
  "s_sub_i32 s74, s81, s74\n"
  "s_sub_i32 s74, s74, 238\n"
  "s_max_i32 s74, s74, 0\n"
  "s_cmp_ge_i32 s74, 512\n"
  "s_cbranch_scc1 WV\n"
  "s_lshr_b32 s74, s74, 3\n"
  "s_lshl_b32 s74, s74, 2\n"
  "s_sub_i32 s74, 268, s74\n"
  "s_getpc_b64 s[76:77]\n"
  "s_add_u32 s76, s76, s74\n"
  "s_addc_u32 s77, s77, 0\n"
  "s_setpc_b64 s[76:77]\n"
  "s_nop 1\n"
  "s_nop 1\n"
  "s_nop 1\n"
  "s_nop 1\n"
  "s_nop 1\n"
  "s_nop 1\n"
  "s_nop 1\n"
  "s_nop 1\n"
  "s_nop 1\n"
  "s_nop 1\n"
  "s_nop 1\n"
  "s_nop 1\n"
  "s_nop 1\n"
  "s_nop 1\n"
  "s_nop 1\n"
  "s_nop 1\n"
  "s_nop 1\n"
  "s_nop 1\n"
  "s_nop 1\n"
  "s_nop 1\n"
  "s_nop 1\n"
  "s_nop 1\n"
  "s_nop 1\n"
  "s_nop 1\n"
  "s_nop 1\n"
  "s_nop 1\n"
  "s_nop 1\n"
  "s_nop 1\n"
  "s_nop 1\n"
  "s_nop 1\n"
  "s_nop 1\n"
  "s_nop 1\n"
  "s_nop 1\n"
  "s_nop 1\n"
  "s_nop 1\n"
  "s_nop 1\n"
  "s_nop 1\n"
  "s_nop 1\n"
  "s_nop 1\n"
  "s_nop 1\n"
  "s_nop 1\n"
  "s_nop 1\n"
  "s_nop 1\n"
  "s_nop 1\n"
  "s_nop 1\n"
  "s_nop 1\n"
  "s_nop 1\n"
  "s_nop 1\n"
  "s_nop 1\n"
  "s_nop 1\n"
  "s_nop 1\n"
  "s_nop 1\n"
  "s_nop 1\n"
  "s_nop 1\n"
  "s_nop 1\n"
  "s_nop 1\n"
  "s_nop 1\n"
  "s_nop 1\n"
  "s_nop 1\n"
  "s_nop 1\n"
  "s_nop 1\n"
  "s_nop 1\n"
  "s_nop 1\n"
  "s_nop 1\n"
  "WEV:\n"
  "global_load_lds_dwordx4 v1, s[62:63] sc1\n"
  "s_mov_b32 m0, s79\n"
  "s_nop 0\n"
  "global_load_lds_dwordx4 v1, s[64:65] sc1\n"
  "s_mov_b32 m0, s78\n"
  "s_nop 0\n"
  "global_load_lds_dwordx4 v1, s[66:67] sc1\n"
  "s_waitcnt vmcnt(2)\n"
  "NDV:\n"
  "s_waitcnt lgkmcnt(0)\n"
  "s_barrier\n"
  "ds_read_b32 v60, v61 offset:9232\n"
  "ds_read_b128 v[20:23], v0 offset:0\n"
  "ds_read_b128 v[24:27], v0 offset:1024\n"
  "ds_read_b128 v[28:31], v0 offset:2048\n"
  "s_waitcnt lgkmcnt(3)\n"
  "v_readfirstlane_b32 s74, v60\n"
  "s_sub_i32 s75, s74, s49\n"
  "s_cmp_gt_i32 s75, 0\n"
  "s_cbranch_scc1 RLATE\n"
  "s_cmp_eq_u32 s48, 0\n"
  "s_cbranch_scc1 RNW\n"
  "s_waitcnt vmcnt(0)\n"
  "RNW:\n"
  "s_barrier\n"
  "ds_read_b128 v[32:35], v0 offset:3072\n"
  "ds_read_b128 v[36:39], v0 offset:4096\n"
  "s_waitcnt lgkmcnt(4)\n"
  "v_smfmac_f32_16x16x64_bf16 v[56:59], v[20:23], v[96:103], v4\n"
  "ds_read_b128 v[40:43], v0 offset:5120\n"
  "s_waitcnt lgkmcnt(4)\n"
  "v_smfmac_f32_16x16x64_bf16 v[56:59], v[24:27], v[104:111], v4\n"
  "ds_read_b128 v[44:47], v0 offset:6144\n"
  "s_waitcnt lgkmcnt(4)\n"
  "v_smfmac_f32_16x16x64_bf16 v[56:59], v[28:31], v[112:119], v4\n"
  "ds_read_b128 v[48:51], v0 offset:7168\n"
  "s_waitcnt lgkmcnt(4)\n"
  "v_smfmac_f32_16x16x64_bf16 v[56:59], v[32:35], v[120:127], v4\n"
  "ds_read_b128 v[52:55], v0 offset:8192\n"
  "s_waitcnt lgkmcnt(4)\n"
  "v_smfmac_f32_16x16x64_bf16 v[56:59], v[36:39], v[128:135], v4\n"
  "s_waitcnt lgkmcnt(3)\n"
  "v_smfmac_f32_16x16x64_bf16 v[56:59], v[40:43], v[136:143], v4\n"
  "s_waitcnt lgkmcnt(2)\n"
  "v_smfmac_f32_16x16x64_bf16 v[56:59], v[44:47], v[144:151], v4\n"
  "s_waitcnt lgkmcnt(1)\n"
  "v_smfmac_f32_16x16x64_bf16 v[56:59], v[48:51], v[152:159], v4\n"
  "s_waitcnt lgkmcnt(0)\n"
  "v_smfmac_f32_16x16x64_bf16 v[56:59], v[52:55], v[160:167], v4\n"
  "s_nop 3\n"
  "v_mov_b32 v7, v7\n"
  "s_nop 2\n"
  "v_add_f32 v56, v56, v58\n"
  "v_add_f32 v57, v57, v59\n"
  "v_cvt_pk_bf16_f32 v58, v56, v57\n"
  "global_store_dword v2, v58, s[68:69] sc1\n"
  "v_cmp_u_f32 vcc, v56, v57\n"
  "s_cbranch_vccnz RBADM\n"
  "ROKM:\n"
  "v_readlane_b32 s8, v14, 0\n"
  "v_readlane_b32 s9, v14, 8\n"
  "v_readlane_b32 s10, v14, 16\n"
  "v_readlane_b32 s11, v14, 24\n"
  "v_readlane_b32 s12, v14, 32\n"
  "v_readlane_b32 s13, v14, 40\n"
  "s_max_u32 s8, s8, s9\n"
  "s_max_u32 s10, s10, s11\n"
  "s_max_u32 s8, s8, s10\n"
  "s_max_u32 s12, s12, s13\n"
  "s_cmp_eq_u32 s8, 0\n"
  "s_cselect_b32 s8, s14, s8\n"
  "s_sub_i32 s74, s8, s16\n"
  "s_min_i32 s74, s74, 24\n"
  "s_max_i32 s74, s74, -6\n"
  "s_add_u32 s16, s16, s74\n"
  "s_min_u32 s74, s8, s14\n"
  "s_max_u32 s16, s16, s74\n"
  "s_max_u32 s74, s8, s14\n"
  "s_add_u32 s74, s74, 40\n"
  "s_min_u32 s16, s16, s74\n"
  "s_mov_b32 s14, s8\n"
  "s_cmp_eq_u32 s12, 0\n"
  "s_cselect_b32 s12, s15, s12\n"
  "s_sub_i32 s74, s12, s17\n"
  "s_min_i32 s74, s74, 24\n"
  "s_max_i32 s74, s74, -6\n"
  "s_add_u32 s17, s17, s74\n"
  "s_min_u32 s74, s12, s15\n"
  "s_max_u32 s17, s17, s74\n"
  "s_max_u32 s74, s12, s15\n"
  "s_add_u32 s74, s74, 40\n"
  "s_min_u32 s17, s17, s74\n"
  "s_mov_b32 s15, s12\n"
  "s_lshl_b32 s74, s40, 1\n"
  "s_min_u32 s74, s74, 31\n"
  "s_lshr_b32 s74, 500, s74\n"
  "s_add_u32 s55, s16, s74\n"
  "s_add_u32 s55, s55, 130\n"
  "s_add_u32 s50, s55, s17\n"
  "s_add_u32 s50, s50, s74\n"
  "s_add_u32 s50, s50, 230\n"
  "s_add_u32 s74, s36, s47\n"
  "s_addc_u32 s75, s37, 0\n"
  "global_store_dword v2, v58, s[74:75] sc1\n"
  "s_add_u32 s47, s47, 65536\n"
  "s_cmp_eq_u32 s40, 63\n"
  "s_cbranch_scc1 ENDV\n"
  "s_add_u32 s75, s40, 3\n"
  "s_and_b32 s74, s75, 0x78\n"
  "s_add_u32 s74, s74, s75\n"
  "s_lshl_b32 s74, s74, 10\n"
  "s_add_u32 s74, s74, s39\n"
  "s_add_u32 s74, s74, s7\n"
  "s_add_u32 s18, s36, s74\n"
  "s_addc_u32 s19, s37, 0\n"
  "v_mov_b32 v61, s6\n"
  "s_cmp_lg_u32 s6, 0\n"
  "s_cselect_b64 exec, 1, 0\n"
  "global_atomic_umax v0, v61, s[18:19] sc1\n"
  "s_mov_b64 exec, -1\n"
  "s_add_u32 s75, s40, 1\n"
  "s_and_b32 s74, s75, 0x78\n"
  "s_add_u32 s74, s74, s75\n"
  "s_lshl_b32 s74, s74, 10\n"
  "s_add_u32 s74, s74, s39\n"
  "s_add_u32 s18, s36, s74\n"
  "s_addc_u32 s19, s37, 0\n"
  "s_mov_b32 exec_lo, 0x01010101\n"
  "s_mov_b32 exec_hi, 0x0101\n"
  "global_load_dword v14, v0, s[18:19] sc1\n"
  "s_mov_b64 exec, -1\n"
  "s_add_u32 s78, s41, 2\n"
  "s_cmp_ge_u32 s78, 3\n"
  "s_cselect_b32 s75, 3, 0\n"
  "s_sub_u32 s78, s78, s75\n"
  "s_mul_i32 s74, s78, 131072\n"
  "s_add_u32 s76, s74, s46\n"
  "s_add_u32 s74, s36, s76\n"
  "s_addc_u32 s75, s37, 0\n"
  "global_store_dword v2, v64, s[74:75] sc1\n"
  "s_add_u32 s40, s40, 1\n"
  "s_add_u32 s81, s49, s55\n"
  "s_add_u32 s49, s49, s50\n"
  "s_add_u32 s41, s41, 1\n"
  "s_cmp_eq_u32 s41, 3\n"
  "s_cselect_b32 s41, 0, s41\n"
  "s_cmp_eq_u32 s56, 1\n"
  "s_cbranch_scc1 RUNIT\n"
  "s_cmp_eq_u32 s56, 0\n"
  "s_cbranch_scc1 RNOA\n"
  "s_and_b32 s74, s40, 1\n"
  "s_lshl_b32 s74, s74, 1\n"
  "s_add_u32 s74, s74, s48\n"
  "s_sub_u32 s74, s74, 1\n"
  "s_lshl_b32 s74, s74, 15\n"
  "s_add_u32 s74, s74, s53\n"
  "s_add_u32 s70, s36, s74\n"
  "s_addc_u32 s71, s37, 0\n"
  "s_mul_i32 s78, s78, 163840\n"
  "s_add_u32 s72, s36, s78\n"
  "s_addc_u32 s73, s37, 0\n"
  "s_cmp_eq_u32 s48, 3\n"
  "s_cbranch_scc1 RAM\n"
  "global_store_dwordx4 v0, v[64:67], s[70:71] offset:0 sc1\n"
  "global_store_dwordx4 v0, v[64:67], s[70:71] offset:1024 sc1\n"
  "global_store_dwordx4 v0, v[64:67], s[70:71] offset:2048 sc1\n"
  "global_store_dwordx4 v0, v[64:67], s[70:71] offset:3072 sc1\n"
  "s_branch RNOA\n"
  "RAM:\n"
  "global_store_dwordx4 v3, v[64:67], s[72:73] sc1\n"
  "global_store_dwordx4 v13, v[64:67], s[72:73] sc1\n"
  "RNOA:\n"
  "s_waitcnt vmcnt(0)\n"
  "s_barrier\n"
  "s_add_u32 s74, s36, s38\n"
  "s_addc_u32 s75, s37, 0\n"
  "v_mov_b32 v61, s40\n"
  "s_cmp_eq_u32 s48, 1\n"
  "s_cselect_b64 exec, 1, 0\n"
  "global_store_dword v0, v61, s[74:75] sc1\n"
  "s_mov_b64 exec, -1\n"
  "s_branch RTAIL\n"
  "RUNIT:\n"
  "s_mul_i32 s78, s41, 163840\n"
  "s_add_u32 s72, s36, s78\n"
  "s_addc_u32 s73, s37, 0\n"
  "s_and_b32 s74, s40, 1\n"
  "s_xor_b32 s74, s74, 1\n"
  "s_lshl_b32 s74, s74, 16\n"
  "s_add_u32 s74, s74, s53\n"
  "s_add_u32 s70, s36, s74\n"
  "s_addc_u32 s71, s37, 0\n"
  "s_add_u32 s66, s70, 32768\n"
  "s_addc_u32 s67, s71, 0\n"
  "s_branch WGR3\n"
  "WR3:\n"
  "s_memrealtime s[74:75]\n"
  "s_waitcnt lgkmcnt(0)\n"
  "s_mul_i32 s74, s74, 24\n"
  "s_sub_i32 s74, s81, s74\n"
  "s_cmp_lt_i32 s74, 750\n"
  "s_cbranch_scc1 WFR3\n"
  "s_cmp_gt_i32 s74, 0x40000\n"
  "s_cbranch_scc1 WER3\n"
  "s_sleep 2\n"
  "s_branch WR3\n"
  "WFR3:\n"
  "s_cmp_lt_i32 s74, 100\n"
  "s_cbranch_scc1 WER3\n"
  "WGR3:\n"
  "s_memtime s[76:77]\n"
  "s_memrealtime s[20:21]\n"
  "s_nop 2\n"
  "s_memrealtime s[22:23]\n"
  "s_nop 2\n"
  "s_memrealtime s[24:25]\n"
  "s_nop 2\n"
  "s_memrealtime s[26:27]\n"
  "s_nop 2\n"
  "s_memrealtime s[28:29]\n"
  "s_nop 2\n"
  "s_memrealtime s[30:31]\n"
  "s_waitcnt lgkmcnt(0)\n"
  "s_add_u32 s20, s20, s22\n"
  "s_add_u32 s24, s24, s26\n"
  "s_add_u32 s28, s28, s30\n"
  "s_add_u32 s20, s20, s24\n"
  "s_add_u32 s20, s20, s28\n"
  "s_lshl_b32 s74, s20, 2\n"
  "s_sub_u32 s64, s74, s76\n"
  "s_sub_i32 s74, s81, s74\n"
  "s_sub_i32 s74, s74, 238\n"
  "s_max_i32 s74, s74, 0\n"
  "s_cmp_ge_i32 s74, 512\n"
  "s_cbranch_scc1 WR3\n"
  "s_lshr_b32 s74, s74, 3\n"
  "s_lshl_b32 s74, s74, 2\n"
  "s_sub_i32 s74, 268, s74\n"
  "s_getpc_b64 s[76:77]\n"
  "s_add_u32 s76, s76, s74\n"
  "s_addc_u32 s77, s77, 0\n"
  "s_setpc_b64 s[76:77]\n"
  "s_nop 1\n"
  "s_nop 1\n"
  "s_nop 1\n"
  "s_nop 1\n"
  "s_nop 1\n"
  "s_nop 1\n"
  "s_nop 1\n"
  "s_nop 1\n"
  "s_nop 1\n"
  "s_nop 1\n"
  "s_nop 1\n"
  "s_nop 1\n"
  "s_nop 1\n"
  "s_nop 1\n"
  "s_nop 1\n"
  "s_nop 1\n"
  "s_nop 1\n"
  "s_nop 1\n"
  "s_nop 1\n"
  "s_nop 1\n"
  "s_nop 1\n"
  "s_nop 1\n"
  "s_nop 1\n"
  "s_nop 1\n"
  "s_nop 1\n"
  "s_nop 1\n"
  "s_nop 1\n"
  "s_nop 1\n"
  "s_nop 1\n"
  "s_nop 1\n"
  "s_nop 1\n"
  "s_nop 1\n"
  "s_nop 1\n"
  "s_nop 1\n"
  "s_nop 1\n"
  "s_nop 1\n"
  "s_nop 1\n"
  "s_nop 1\n"
  "s_nop 1\n"
  "s_nop 1\n"
  "s_nop 1\n"
  "s_nop 1\n"
  "s_nop 1\n"
  "s_nop 1\n"
  "s_nop 1\n"
  "s_nop 1\n"
  "s_nop 1\n"
  "s_nop 1\n"
  "s_nop 1\n"
  "s_nop 1\n"
  "s_nop 1\n"
  "s_nop 1\n"
  "s_nop 1\n"
  "s_nop 1\n"
  "s_nop 1\n"
  "s_nop 1\n"
  "s_nop 1\n"
  "s_nop 1\n"
  "s_nop 1\n"
  "s_nop 1\n"
  "s_nop 1\n"
  "s_nop 1\n"
  "s_nop 1\n"
  "s_nop 1\n"
  "WER3:\n"
  "s_memtime s[76:77]\n"
  "global_load_dwordx4 v[168:171], v0, s[70:71] offset:0 sc1\n"
  "global_load_dwordx4 v[172:175], v0, s[70:71] offset:1024 sc1\n"
  "global_load_dwordx4 v[176:179], v0, s[70:71] offset:2048 sc1\n"
  "global_load_dwordx4 v[180:183], v0, s[70:71] offset:3072 sc1\n"
  "global_load_dwordx4 v[184:187], v0, s[66:67] offset:0 sc1\n"
  "global_load_dwordx4 v[188:191], v0, s[66:67] offset:1024 sc1\n"
  "global_load_dwordx4 v[192:195], v0, s[66:67] offset:2048 sc1\n"
  "global_load_dwordx4 v[196:199], v0, s[66:67] offset:3072 sc1\n"
  "s_waitcnt lgkmcnt(0)\n"
  "s_add_u32 s64, s64, s76\n"
  "s_sub_i32 s64, s49, s64\n"
  "s_sub_i32 s64, s64, 230\n"
  "s_waitcnt vmcnt(8)\n"
  "s_barrier\n"
  "s_waitcnt vmcnt(7)\n"
  "v_mfma_f32_16x16x32_bf16 v[84:87], v[168:171], v[8:11], 0\n"
  "s_waitcnt vmcnt(6)\n"
  "v_mfma_f32_16x16x32_bf16 v[84:87], v[172:175], v[8:11], v[84:87]\n"
  "s_waitcnt vmcnt(5)\n"
  "v_mfma_f32_16x16x32_bf16 v[84:87], v[176:179], v[8:11], v[84:87]\n"
  "s_waitcnt vmcnt(4)\n"
  "v_mfma_f32_16x16x32_bf16 v[84:87], v[180:183], v[8:11], v[84:87]\n"
  "s_waitcnt vmcnt(3)\n"
  "v_mfma_f32_16x16x32_bf16 v[84:87], v[184:187], v[8:11], v[84:87]\n"
  "s_memtime s[74:75]\n"
  "s_waitcnt vmcnt(2)\n"
  "v_mfma_f32_16x16x32_bf16 v[84:87], v[188:191], v[8:11], v[84:87]\n"
  "s_waitcnt vmcnt(1)\n"
  "v_mfma_f32_16x16x32_bf16 v[84:87], v[192:195], v[8:11], v[84:87]\n"
  "s_waitcnt vmcnt(0)\n"
  "v_mfma_f32_16x16x32_bf16 v[84:87], v[196:199], v[8:11], v[84:87]\n"
  "s_waitcnt lgkmcnt(0)\n"
  "s_sub_i32 s74, s74, s76\n"
  "s_cmp_gt_i32 s74, s64\n"
  "s_cbranch_scc1 RSLOW\n"
  "s_nop 3\n"
  "v_cvt_pk_bf16_f32 v88, v84, v85\n"
  "v_cvt_pk_bf16_f32 v89, v86, v87\n"
  "v_cvt_pk_bf16_f32 v90, v84, v85\n"
  "v_cvt_pk_bf16_f32 v91, v86, v87\n"
  "s_nop 0\n"
  "v_permlane32_swap_b32 v88, v90\n"
  "v_permlane32_swap_b32 v89, v91\n"
  "global_store_dwordx4 v3, v[88:91], s[72:73] sc1\n"
  "s_memtime s[74:75]\n"
  "global_store_dwordx4 v13, v[88:91], s[72:73] sc1\n"
  "v_add_f32 v60, v84, v85\n"
  "v_add_f32 v61, v86, v87\n"
  "v_cmp_u_f32 vcc, v60, v61\n"
  "s_cbranch_vccnz RAGAIN\n"
  "s_waitcnt lgkmcnt(0)\n"
  "s_sub_u32 s6, s74, s76\n"
  "RDONE:\n"
  "RTAIL:\n"
  "s_branch RLOOP\n"
  "RBADM:\n"
  "s_waitcnt lgkmcnt(0)\n"
  "s_nop 3\n"
  "v_mov_b32 v7, v7\n"
  "s_nop 2\n"
  "v_mov_b32 v7, v7\n"
  "s_nop 2\n"
  "s_mov_b32 s74, 0xff820000\n"
  "v_max3_u32 v60, v20, v21, v22\n"
  "v_max3_u32 v60, v60, v23, v24\n"
  "v_max3_u32 v60, v60, v25, v26\n"
  "v_max3_u32 v60, v60, v27, v28\n"
  "v_max3_u32 v60, v60, v29, v30\n"
  "v_max3_u32 v60, v60, v31, v32\n"
  "v_max3_u32 v60, v60, v33, v34\n"
  "v_max3_u32 v60, v60, v35, v36\n"
  "v_max3_u32 v60, v60, v37, v38\n"
  "v_max3_u32 v60, v60, v39, v40\n"
  "v_max3_u32 v60, v60, v41, v42\n"
  "v_max3_u32 v60, v60, v43, v44\n"
  "v_max3_u32 v60, v60, v45, v46\n"
  "v_max3_u32 v60, v60, v47, v48\n"
  "v_max3_u32 v60, v60, v49, v50\n"
  "v_max3_u32 v60, v60, v51, v52\n"
  "v_max3_u32 v60, v60, v53, v54\n"
  "v_max_u32 v60, v60, v55\n"
  "v_cmp_le_u32 vcc, s74, v60\n"
  "s_cbranch_vccnz REV\n"
  "s_mov_b32 s74, 0x7fff7fff\n"
  "v_and_b32 v60, s74, v20\n"
  "v_and_b32 v61, s74, v21\n"
  "v_pk_max_u16 v60, v60, v61\n"
  "v_and_b32 v61, s74, v22\n"
  "v_pk_max_u16 v60, v60, v61\n"
  "v_and_b32 v61, s74, v23\n"
  "v_pk_max_u16 v60, v60, v61\n"
  "v_and_b32 v61, s74, v24\n"
  "v_pk_max_u16 v60, v60, v61\n"
  "v_and_b32 v61, s74, v25\n"
  "v_pk_max_u16 v60, v60, v61\n"
  "v_and_b32 v61, s74, v26\n"
  "v_pk_max_u16 v60, v60, v61\n"
  "v_and_b32 v61, s74, v27\n"
  "v_pk_max_u16 v60, v60, v61\n"
  "v_and_b32 v61, s74, v28\n"
  "v_pk_max_u16 v60, v60, v61\n"
  "v_and_b32 v61, s74, v29\n"
  "v_pk_max_u16 v60, v60, v61\n"
  "v_and_b32 v61, s74, v30\n"
  "v_pk_max_u16 v60, v60, v61\n"
  "v_and_b32 v61, s74, v31\n"
  "v_pk_max_u16 v60, v60, v61\n"
  "v_and_b32 v61, s74, v32\n"
  "v_pk_max_u16 v60, v60, v61\n"
  "v_and_b32 v61, s74, v33\n"
  "v_pk_max_u16 v60, v60, v61\n"
  "v_and_b32 v61, s74, v34\n"
  "v_pk_max_u16 v60, v60, v61\n"
  "v_and_b32 v61, s74, v35\n"
  "v_pk_max_u16 v60, v60, v61\n"
  "v_and_b32 v61, s74, v36\n"
  "v_pk_max_u16 v60, v60, v61\n"
  "v_and_b32 v61, s74, v37\n"
  "v_pk_max_u16 v60, v60, v61\n"
  "v_and_b32 v61, s74, v38\n"
  "v_pk_max_u16 v60, v60, v61\n"
  "v_and_b32 v61, s74, v39\n"
  "v_pk_max_u16 v60, v60, v61\n"
  "v_and_b32 v61, s74, v40\n"
  "v_pk_max_u16 v60, v60, v61\n"
  "v_and_b32 v61, s74, v41\n"
  "v_pk_max_u16 v60, v60, v61\n"
  "v_and_b32 v61, s74, v42\n"
  "v_pk_max_u16 v60, v60, v61\n"
  "v_and_b32 v61, s74, v43\n"
  "v_pk_max_u16 v60, v60, v61\n"
  "v_and_b32 v61, s74, v44\n"
  "v_pk_max_u16 v60, v60, v61\n"
  "v_and_b32 v61, s74, v45\n"
  "v_pk_max_u16 v60, v60, v61\n"
  "v_and_b32 v61, s74, v46\n"
  "v_pk_max_u16 v60, v60, v61\n"
  "v_and_b32 v61, s74, v47\n"
  "v_pk_max_u16 v60, v60, v61\n"
  "v_and_b32 v61, s74, v48\n"
  "v_pk_max_u16 v60, v60, v61\n"
  "v_and_b32 v61, s74, v49\n"
  "v_pk_max_u16 v60, v60, v61\n"
  "v_and_b32 v61, s74, v50\n"
  "v_pk_max_u16 v60, v60, v61\n"
  "v_and_b32 v61, s74, v51\n"
  "v_pk_max_u16 v60, v60, v61\n"
  "v_and_b32 v61, s74, v52\n"
  "v_pk_max_u16 v60, v60, v61\n"
  "v_and_b32 v61, s74, v53\n"
  "v_pk_max_u16 v60, v60, v61\n"
  "v_and_b32 v61, s74, v54\n"
  "v_pk_max_u16 v60, v60, v61\n"
  "v_and_b32 v61, s74, v55\n"
  "v_pk_max_u16 v60, v60, v61\n"
  "v_lshrrev_b32 v61, 16, v60\n"
  "v_and_b32 v60, 0xffff, v60\n"
  "v_max_u32 v60, v60, v61\n"
  "v_cmp_lt_u32 vcc, 0x7f81, v60\n"
  "s_cbranch_vccnz REV\n"
  "s_mov_b32 s74, 0x7fff7fff\n"
  "s_mov_b32 s75, 0x7f817f81\n"
  "v_and_b32 v60, s74, v58\n"
  "v_pk_min_u16 v60, v60, s75\n"
  "v_bfi_b32 v58, s74, v60, v58\n"
  "s_waitcnt vmcnt(0)\n"
  "global_store_dword v2, v58, s[68:69] sc1\n"
  "s_branch ROKM\n"
  "REV:\n"
  "s_sub_u32 s42, s42, 1\n"
  "s_cbranch_scc1 QUITV\n"
  "s_add_u32 s43, s43, 1\n"
  "s_memrealtime s[74:75]\n"
  "s_waitcnt lgkmcnt(0)\n"
  "s_mul_i32 s78, s74, 24\n"
  "s_add_u32 s78, s78, 650\n"
  "RSV:\n"
  "s_add_u32 s49, s49, s51\n"
  "s_sub_i32 s74, s49, s78\n"
  "s_cmp_lt_i32 s74, 0\n"
  "s_cbranch_scc1 RSV\n"
  "AGV:\n"
  "v_mov_b32 v60, s49\n"
  "v_mov_b32 v61, s48\n"
  "v_lshlrev_b32 v61, 2, v61\n"
  "ds_write_b32 v61, v60 offset:9216\n"
  "s_waitcnt lgkmcnt(0)\n"
  "s_barrier\n"
  "v_mov_b32 v61, 0\n"
  "ds_read_b128 v[20:23], v61 offset:9216\n"
  "s_waitcnt lgkmcnt(0)\n"
  "v_readfirstlane_b32 s74, v20\n"
  "s_sub_i32 s75, s74, s49\n"
  "s_cmp_gt_i32 s75, 0\n"
  "s_cselect_b32 s49, s74, s49\n"
  "v_readfirstlane_b32 s74, v21\n"
  "s_sub_i32 s75, s74, s49\n"
  "s_cmp_gt_i32 s75, 0\n"
  "s_cselect_b32 s49, s74, s49\n"
  "v_readfirstlane_b32 s74, v22\n"
  "s_sub_i32 s75, s74, s49\n"
  "s_cmp_gt_i32 s75, 0\n"
  "s_cselect_b32 s49, s74, s49\n"
  "v_readfirstlane_b32 s74, v23\n"
  "s_sub_i32 s75, s74, s49\n"
  "s_cmp_gt_i32 s75, 0\n"
  "s_cselect_b32 s49, s74, s49\n"
  "s_cmp_lg_u32 s49, s98\n"
  "s_cselect_b32 s52, 1, 0\n"
  "s_branch RTRY\n"
  "RLATE:\n"
  "s_waitcnt lgkmcnt(0)\n"
  "s_mov_b32 s49, s74\n"
  "s_mov_b32 s52, 1\n"
  "s_add_u32 s43, s43, 1\n"
  "s_barrier\n"
  "s_branch RTRY\n"
  "RSLOW:\n"
  "s_waitcnt lgkmcnt(0)\n"
  "s_nop 3\n"
  "v_mov_b32 v7, v7\n"
  "s_nop 2\n"
  "v_mov_b32 v7, v7\n"
  "s_nop 2\n"
  "s_add_u32 s6, s74, 110\n"
  "s_cmp_gt_i32 s64, 0\n"
  "s_cselect_b32 s6, s6, 0\n"
  "v_add_f32 v60, v84, v85\n"
  "v_add_f32 v61, v86, v87\n"
  "v_cmp_u_f32 vcc, v60, v61\n"
  "s_cbranch_vccz RFULL\n"
  "RAGAIN:\n"
  "s_waitcnt lgkmcnt(0)\n"
  "s_nop 3\n"
  "v_mov_b32 v7, v7\n"
  "s_nop 2\n"
  "v_mov_b32 v7, v7\n"
  "s_nop 2\n"
  "s_mov_b32 s6, 0\n"
  "RAG2:\n"
  "s_sub_u32 s42, s42, 1\n"
  "s_cbranch_scc1 QUITV\n"
  "s_add_u32 s43, s43, 0x10000\n"
  "global_load_dwordx4 v[168:171], v0, s[70:71] offset:0 sc1\n"
  "global_load_dwordx4 v[172:175], v0, s[70:71] offset:1024 sc1\n"
  "global_load_dwordx4 v[176:179], v0, s[70:71] offset:2048 sc1\n"
  "global_load_dwordx4 v[180:183], v0, s[70:71] offset:3072 sc1\n"
  "global_load_dwordx4 v[184:187], v0, s[66:67] offset:0 sc1\n"
  "global_load_dwordx4 v[188:191], v0, s[66:67] offset:1024 sc1\n"
  "global_load_dwordx4 v[192:195], v0, s[66:67] offset:2048 sc1\n"
  "global_load_dwordx4 v[196:199], v0, s[66:67] offset:3072 sc1\n"
  "s_waitcnt vmcnt(7)\n"
  "v_mfma_f32_16x16x32_bf16 v[84:87], v[168:171], v[8:11], 0\n"
  "s_waitcnt vmcnt(6)\n"
  "v_mfma_f32_16x16x32_bf16 v[84:87], v[172:175], v[8:11], v[84:87]\n"
  "s_waitcnt vmcnt(5)\n"
  "v_mfma_f32_16x16x32_bf16 v[84:87], v[176:179], v[8:11], v[84:87]\n"
  "s_waitcnt vmcnt(4)\n"
  "v_mfma_f32_16x16x32_bf16 v[84:87], v[180:183], v[8:11], v[84:87]\n"
  "s_waitcnt vmcnt(3)\n"
  "v_mfma_f32_16x16x32_bf16 v[84:87], v[184:187], v[8:11], v[84:87]\n"
  "s_waitcnt vmcnt(2)\n"
  "v_mfma_f32_16x16x32_bf16 v[84:87], v[188:191], v[8:11], v[84:87]\n"
  "s_waitcnt vmcnt(1)\n"
  "v_mfma_f32_16x16x32_bf16 v[84:87], v[192:195], v[8:11], v[84:87]\n"
  "s_waitcnt vmcnt(0)\n"
  "v_mfma_f32_16x16x32_bf16 v[84:87], v[196:199], v[8:11], v[84:87]\n"
  "s_nop 3\n"
  "v_mov_b32 v7, v7\n"
  "s_nop 2\n"
  "v_add_f32 v60, v84, v85\n"
  "v_add_f32 v61, v86, v87\n"
  "v_cmp_u_f32 vcc, v60, v61\n"
  "s_cbranch_vccz RFULL\n"
  "s_mov_b32 s74, 0xff820000\n"
  "v_max3_u32 v60, v168, v169, v170\n"
  "v_max3_u32 v60, v60, v171, v172\n"
  "v_max3_u32 v60, v60, v173, v174\n"
  "v_max3_u32 v60, v60, v175, v176\n"
  "v_max3_u32 v60, v60, v177, v178\n"
  "v_max3_u32 v60, v60, v179, v180\n"
  "v_max3_u32 v60, v60, v181, v182\n"
  "v_max3_u32 v60, v60, v183, v184\n"
  "v_max3_u32 v60, v60, v185, v186\n"
  "v_max3_u32 v60, v60, v187, v188\n"
  "v_max3_u32 v60, v60, v189, v190\n"
  "v_max3_u32 v60, v60, v191, v192\n"
  "v_max3_u32 v60, v60, v193, v194\n"
  "v_max3_u32 v60, v60, v195, v196\n"
  "v_max3_u32 v60, v60, v197, v198\n"
  "v_max_u32 v60, v60, v199\n"
  "v_cmp_le_u32 vcc, s74, v60\n"
  "s_cbranch_vccnz RAG2\n"
  "s_mov_b32 s74, 0x7fff7fff\n"
  "v_and_b32 v60, s74, v168\n"
  "v_and_b32 v61, s74, v169\n"
  "v_pk_max_u16 v60, v60, v61\n"
  "v_and_b32 v61, s74, v170\n"
  "v_pk_max_u16 v60, v60, v61\n"
  "v_and_b32 v61, s74, v171\n"
  "v_pk_max_u16 v60, v60, v61\n"
  "v_and_b32 v61, s74, v172\n"
  "v_pk_max_u16 v60, v60, v61\n"
  "v_and_b32 v61, s74, v173\n"
  "v_pk_max_u16 v60, v60, v61\n"
  "v_and_b32 v61, s74, v174\n"
  "v_pk_max_u16 v60, v60, v61\n"
  "v_and_b32 v61, s74, v175\n"
  "v_pk_max_u16 v60, v60, v61\n"
  "v_and_b32 v61, s74, v176\n"
  "v_pk_max_u16 v60, v60, v61\n"
  "v_and_b32 v61, s74, v177\n"
  "v_pk_max_u16 v60, v60, v61\n"
  "v_and_b32 v61, s74, v178\n"
  "v_pk_max_u16 v60, v60, v61\n"
  "v_and_b32 v61, s74, v179\n"
  "v_pk_max_u16 v60, v60, v61\n"
  "v_and_b32 v61, s74, v180\n"
  "v_pk_max_u16 v60, v60, v61\n"
  "v_and_b32 v61, s74, v181\n"
  "v_pk_max_u16 v60, v60, v61\n"
  "v_and_b32 v61, s74, v182\n"
  "v_pk_max_u16 v60, v60, v61\n"
  "v_and_b32 v61, s74, v183\n"
  "v_pk_max_u16 v60, v60, v61\n"
  "v_and_b32 v61, s74, v184\n"
  "v_pk_max_u16 v60, v60, v61\n"
  "v_and_b32 v61, s74, v185\n"
  "v_pk_max_u16 v60, v60, v61\n"
  "v_and_b32 v61, s74, v186\n"
  "v_pk_max_u16 v60, v60, v61\n"
  "v_and_b32 v61, s74, v187\n"
  "v_pk_max_u16 v60, v60, v61\n"
  "v_and_b32 v61, s74, v188\n"
  "v_pk_max_u16 v60, v60, v61\n"
  "v_and_b32 v61, s74, v189\n"
  "v_pk_max_u16 v60, v60, v61\n"
  "v_and_b32 v61, s74, v190\n"
  "v_pk_max_u16 v60, v60, v61\n"
  "v_and_b32 v61, s74, v191\n"
  "v_pk_max_u16 v60, v60, v61\n"
  "v_and_b32 v61, s74, v192\n"
  "v_pk_max_u16 v60, v60, v61\n"
  "v_and_b32 v61, s74, v193\n"
  "v_pk_max_u16 v60, v60, v61\n"
  "v_and_b32 v61, s74, v194\n"
  "v_pk_max_u16 v60, v60, v61\n"
  "v_and_b32 v61, s74, v195\n"
  "v_pk_max_u16 v60, v60, v61\n"
  "v_and_b32 v61, s74, v196\n"
  "v_pk_max_u16 v60, v60, v61\n"
  "v_and_b32 v61, s74, v197\n"
  "v_pk_max_u16 v60, v60, v61\n"
  "v_and_b32 v61, s74, v198\n"
  "v_pk_max_u16 v60, v60, v61\n"
  "v_and_b32 v61, s74, v199\n"
  "v_pk_max_u16 v60, v60, v61\n"
  "v_lshrrev_b32 v61, 16, v60\n"
  "v_and_b32 v60, 0xffff, v60\n"
  "v_max_u32 v60, v60, v61\n"
  "v_cmp_lt_u32 vcc, 0x7f81, v60\n"
  "s_cbranch_vccnz RAG2\n"
  "RFULL:\n"
  "v_cvt_pk_bf16_f32 v88, v84, v85\n"
  "v_cvt_pk_bf16_f32 v89, v86, v87\n"
  "v_cvt_pk_bf16_f32 v90, v84, v85\n"
  "v_cvt_pk_bf16_f32 v91, v86, v87\n"
  "s_nop 0\n"
  "v_permlane32_swap_b32 v88, v90\n"
  "v_permlane32_swap_b32 v89, v91\n"
  "s_mov_b32 s74, 0x7fff7fff\n"
  "s_mov_b32 s75, 0x7f817f81\n"
  "v_and_b32 v60, s74, v88\n"
  "v_pk_min_u16 v60, v60, s75\n"
  "v_bfi_b32 v88, s74, v60, v88\n"
  "v_and_b32 v60, s74, v89\n"
  "v_pk_min_u16 v60, v60, s75\n"
  "v_bfi_b32 v89, s74, v60, v89\n"
  "v_and_b32 v60, s74, v90\n"
  "v_pk_min_u16 v60, v60, s75\n"
  "v_bfi_b32 v90, s74, v60, v90\n"
  "v_and_b32 v60, s74, v91\n"
  "v_pk_min_u16 v60, v60, s75\n"
  "v_bfi_b32 v91, s74, v60, v91\n"
  "s_add_u32 s43, s43, 0x100\n"
  "s_memrealtime s[74:75]\n"
  "s_waitcnt lgkmcnt(0)\n"
  "s_mul_i32 s78, s74, 24\n"
  "s_add_u32 s78, s78, 750\n"
  "s_mov_b32 s81, s49\n"
  "RSG:\n"
  "s_add_u32 s79, s81, s51\n"
  "s_sub_i32 s74, s79, s78\n"
  "s_cmp_ge_i32 s74, 0\n"
  "s_cbranch_scc1 RGOT\n"
  "s_mov_b32 s81, s79\n"
  "s_branch RSG\n"
  "RGOT:\n"
  "v_mov_b32 v60, 0\n"
  "v_mov_b32 v61, s79\n"
  "ds_write_b32 v60, v61 offset:9232\n"
  "s_add_u32 s81, s81, 200\n"
  "WR4:\n"
  "s_memrealtime s[74:75]\n"
  "s_waitcnt lgkmcnt(0)\n"
  "s_mul_i32 s74, s74, 24\n"
  "s_sub_i32 s74, s81, s74\n"
  "s_cmp_lt_i32 s74, 750\n"
  "s_cbranch_scc1 WFR4\n"
  "s_cmp_gt_i32 s74, 0x40000\n"
  "s_cbranch_scc1 WER4\n"
  "s_sleep 2\n"
  "s_branch WR4\n"
  "WFR4:\n"
  "s_cmp_lt_i32 s74, 100\n"
  "s_cbranch_scc1 WER4\n"
  "WGR4:\n"
  "s_memtime s[76:77]\n"
  "s_memrealtime s[20:21]\n"
  "s_nop 2\n"
  "s_memrealtime s[22:23]\n"
  "s_nop 2\n"
  "s_memrealtime s[24:25]\n"
  "s_nop 2\n"
  "s_memrealtime s[26:27]\n"
  "s_nop 2\n"
  "s_memrealtime s[28:29]\n"
  "s_nop 2\n"
  "s_memrealtime s[30:31]\n"
  "s_waitcnt lgkmcnt(0)\n"
  "s_add_u32 s20, s20, s22\n"
  "s_add_u32 s24, s24, s26\n"
  "s_add_u32 s28, s28, s30\n"
  "s_add_u32 s20, s20, s24\n"
  "s_add_u32 s20, s20, s28\n"
  "s_lshl_b32 s74, s20, 2\n"
  "s_nop 0\n"
  "s_sub_i32 s74, s81, s74\n"
  "s_sub_i32 s74, s74, 238\n"
  "s_max_i32 s74, s74, 0\n"
  "s_cmp_ge_i32 s74, 512\n"
  "s_cbranch_scc1 WR4\n"
  "s_lshr_b32 s74, s74, 3\n"
  "s_lshl_b32 s74, s74, 2\n"
  "s_sub_i32 s74, 268, s74\n"
  "s_getpc_b64 s[76:77]\n"
  "s_add_u32 s76, s76, s74\n"
  "s_addc_u32 s77, s77, 0\n"
  "s_setpc_b64 s[76:77]\n"
  "s_nop 1\n"
  "s_nop 1\n"
  "s_nop 1\n"
  "s_nop 1\n"
  "s_nop 1\n"
  "s_nop 1\n"
  "s_nop 1\n"
  "s_nop 1\n"
  "s_nop 1\n"
  "s_nop 1\n"
  "s_nop 1\n"
  "s_nop 1\n"
  "s_nop 1\n"
  "s_nop 1\n"
  "s_nop 1\n"
  "s_nop 1\n"
  "s_nop 1\n"
  "s_nop 1\n"
  "s_nop 1\n"
  "s_nop 1\n"
  "s_nop 1\n"
  "s_nop 1\n"
  "s_nop 1\n"
  "s_nop 1\n"
  "s_nop 1\n"
  "s_nop 1\n"
  "s_nop 1\n"
  "s_nop 1\n"
  "s_nop 1\n"
  "s_nop 1\n"
  "s_nop 1\n"
  "s_nop 1\n"
  "s_nop 1\n"
  "s_nop 1\n"
  "s_nop 1\n"
  "s_nop 1\n"
  "s_nop 1\n"
  "s_nop 1\n"
  "s_nop 1\n"
  "s_nop 1\n"
  "s_nop 1\n"
  "s_nop 1\n"
  "s_nop 1\n"
  "s_nop 1\n"
  "s_nop 1\n"
  "s_nop 1\n"
  "s_nop 1\n"
  "s_nop 1\n"
  "s_nop 1\n"
  "s_nop 1\n"
  "s_nop 1\n"
  "s_nop 1\n"
  "s_nop 1\n"
  "s_nop 1\n"
  "s_nop 1\n"
  "s_nop 1\n"
  "s_nop 1\n"
  "s_nop 1\n"
  "s_nop 1\n"
  "s_nop 1\n"
  "s_nop 1\n"
  "s_nop 1\n"
  "s_nop 1\n"
  "s_nop 1\n"
  "WER4:\n"
  "global_store_dwordx4 v3, v[88:91], s[72:73] sc1\n"
  "global_store_dwordx4 v13, v[88:91], s[72:73] sc1\n"
  "s_branch RDONE\n"
  "ENDV:\n"
  "s_waitcnt vmcnt(0) lgkmcnt(0)\n"
  "s_barrier\n"
  "s_add_u32 s74, s36, s38\n"
  "s_addc_u32 s75, s37, 0\n"
  "v_mov_b32 v60, 0\n"
  "v_mov_b32 v61, 64\n"
  "s_cmp_eq_u32 s48, 1\n"
  "s_cselect_b64 exec, 1, 0\n"
  "global_store_dword v60, v61, s[74:75] sc1\n"
  "QUITV:\n"
  "s_endpgm\n"
        : "+{v[0:15]}"(vin), "+{s[36:43]}"(sa), "+{s[44:59]}"(sb) : "{s[4:5]}"(pol) : CLOB);
    }
    return;
  }
  const int fd = gam - 4;
  if (tk < 16) {
    const int h = fd * 8 + (tk >> 1), isq = tk & 1;
    { const int row = 4 * wave + g, sw = 2 * ((row & 3) | ((row >> 1) & 4)); vin[1] = row * 8192 + (n ^ sw) * 16; }
    vin[2] = (8 * g + qq) * 256 + ((4 * wave + (c4 >> 1)) ^ (2 * (qq | ((g & 1) << 2)))) * 16 + (c4 & 1) * 8;
    vin[3] = x_src(wave, lane); vin[9] = x_frag(lane);
    { const int row = 8 * wave + (lane >> 3); vin[4] = row * 8192 + ((lane & 7) ^ (2 * (((row >> 1) & 1) | ((row >> 2) & 2)))) * 16; }
    vin[5] = (8 * g + qq) * 128 + ((2 * wave + (c4 >> 1)) ^ (2 * ((qq >> 1) | ((g & 1) << 1)))) * 16 + (c4 & 1) * 8;
    PTR(sa, 2, (isq ? p.Wq : p.Wk) + h * 128); PTR(sa, 4, p.Wv + h * 128 + isq * 64); PTR(sa, 6, p.x);
    sb[0] = h * 65536 + (isq ? 32768 : KVH) + wave * 2048; sb[1] = KVH + h * 65536 + 16384 + (4 * isq + wave) * 1024;
    sb[2] = (C_KVP ^ cx) + (2 * h + isq) * 128; sb[3] = wave; sb[4] = __float_as_uint(isq ? 1.0f : QS);
    sb[6] = WO_ST + (role * 4 + wave) * 4096;
    UNIFORM(sa, 8); UNIFORM(sb, 16);
    asm volatile(
  "s_nop 4\n"
  "s_lshl_b32 s80, s47, 10\n"
  "v_mov_b32 v8, s48\n"
  "s_add_u32 s58, s80, 122880\n"
  "s_add_u32 m0, s58, 0\n"
  "s_nop 0\n"
  "global_load_lds_dwordx4 v3, s[42:43] offset:0\n"
  "s_add_u32 m0, s58, 3968\n"
  "s_nop 0\n"
  "global_load_lds_dwordx4 v3, s[42:43] offset:128\n"
  "s_add_u32 m0, s58, 7936\n"
  "s_nop 0\n"
  "global_load_lds_dwordx4 v3, s[42:43] offset:256\n"
  "s_add_u32 m0, s58, 11904\n"
  "s_nop 0\n"
  "global_load_lds_dwordx4 v3, s[42:43] offset:384\n"
  "s_add_u32 m0, s58, 15872\n"
  "s_nop 0\n"
  "global_load_lds_dwordx4 v3, s[42:43] offset:512\n"
  "s_add_u32 m0, s58, 19840\n"
  "s_nop 0\n"
  "global_load_lds_dwordx4 v3, s[42:43] offset:640\n"
  "s_add_u32 m0, s58, 23808\n"
  "s_nop 0\n"
  "global_load_lds_dwordx4 v3, s[42:43] offset:768\n"
  "s_add_u32 m0, s58, 27776\n"
  "s_nop 0\n"
  "global_load_lds_dwordx4 v3, s[42:43] offset:896\n"
  "s_add_u32 m0, s58, 31744\n"
  "s_nop 0\n"
  "global_load_lds_dwordx4 v3, s[42:43] offset:1024\n"
  "s_add_u32 m0, s80, 0\n"
  "s_add_u32 s74, s38, 0\n"
  "s_addc_u32 s75, s39, 0\n"
  "global_load_lds_dwordx4 v1, s[74:75]\n"
  "s_add_u32 m0, s80, 4096\n"
  "s_add_u32 s74, s38, 131072\n"
  "s_addc_u32 s75, s39, 0\n"
  "global_load_lds_dwordx4 v1, s[74:75]\n"
  "s_add_u32 m0, s80, 8192\n"
  "s_add_u32 s74, s38, 262144\n"
  "s_addc_u32 s75, s39, 0\n"
  "global_load_lds_dwordx4 v1, s[74:75]\n"
  "s_add_u32 m0, s80, 12288\n"
  "s_add_u32 s74, s38, 393216\n"
  "s_addc_u32 s75, s39, 0\n"
  "global_load_lds_dwordx4 v1, s[74:75]\n"
  "s_add_u32 m0, s80, 16384\n"
  "s_add_u32 s74, s40, 0\n"
  "s_addc_u32 s75, s41, 0\n"
  "global_load_lds_dwordx4 v4, s[74:75]\n"
  "s_add_u32 m0, s80, 20480\n"
  "s_add_u32 s74, s40, 262144\n"
  "s_addc_u32 s75, s41, 0\n"
  "global_load_lds_dwordx4 v4, s[74:75]\n"
  "s_add_u32 m0, s80, 24576\n"
  "s_add_u32 s74, s38, 524288\n"
  "s_addc_u32 s75, s39, 0\n"
  "global_load_lds_dwordx4 v1, s[74:75]\n"
  "s_add_u32 m0, s80, 28672\n"
  "s_add_u32 s74, s38, 655360\n"
  "s_addc_u32 s75, s39, 0\n"
  "global_load_lds_dwordx4 v1, s[74:75]\n"
  "s_add_u32 m0, s80, 32768\n"
  "s_add_u32 s74, s38, 786432\n"
  "s_addc_u32 s75, s39, 0\n"
  "global_load_lds_dwordx4 v1, s[74:75]\n"
  "s_add_u32 m0, s80, 36864\n"
  "s_add_u32 s74, s38, 917504\n"
  "s_addc_u32 s75, s39, 0\n"
  "global_load_lds_dwordx4 v1, s[74:75]\n"
  "s_add_u32 m0, s80, 40960\n"
  "s_add_u32 s74, s40, 524288\n"
  "s_addc_u32 s75, s41, 0\n"
  "global_load_lds_dwordx4 v4, s[74:75]\n"
  "s_add_u32 m0, s80, 45056\n"
  "s_add_u32 s74, s40, 786432\n"
  "s_addc_u32 s75, s41, 0\n"
  "global_load_lds_dwordx4 v4, s[74:75]\n"
  "s_add_u32 m0, s80, 49152\n"
  "s_add_u32 s74, s38, 1048576\n"
  "s_addc_u32 s75, s39, 0\n"
  "global_load_lds_dwordx4 v1, s[74:75]\n"
  "s_add_u32 m0, s80, 53248\n"
  "s_add_u32 s74, s38, 1179648\n"
  "s_addc_u32 s75, s39, 0\n"
  "global_load_lds_dwordx4 v1, s[74:75]\n"
  "s_add_u32 m0, s80, 57344\n"
  "s_add_u32 s74, s38, 1310720\n"
  "s_addc_u32 s75, s39, 0\n"
  "global_load_lds_dwordx4 v1, s[74:75]\n"
  "s_add_u32 m0, s80, 61440\n"
  "s_add_u32 s74, s38, 1441792\n"
  "s_addc_u32 s75, s39, 0\n"
  "global_load_lds_dwordx4 v1, s[74:75]\n"
  "s_add_u32 m0, s80, 65536\n"
  "s_add_u32 s74, s40, 1048576\n"
  "s_addc_u32 s75, s41, 0\n"
  "global_load_lds_dwordx4 v4, s[74:75]\n"
  "s_add_u32 m0, s80, 69632\n"
  "s_add_u32 s74, s40, 1310720\n"
  "s_addc_u32 s75, s41, 0\n"
  "global_load_lds_dwordx4 v4, s[74:75]\n"
  "s_add_u32 m0, s80, 73728\n"
  "s_add_u32 s74, s38, 1572864\n"
  "s_addc_u32 s75, s39, 0\n"
  "global_load_lds_dwordx4 v1, s[74:75]\n"
  "s_add_u32 m0, s80, 77824\n"
  "s_add_u32 s74, s38, 1703936\n"
  "s_addc_u32 s75, s39, 0\n"
  "global_load_lds_dwordx4 v1, s[74:75]\n"
  "s_add_u32 m0, s80, 81920\n"
  "s_add_u32 s74, s38, 1835008\n"
  "s_addc_u32 s75, s39, 0\n"
  "global_load_lds_dwordx4 v1, s[74:75]\n"
  "s_add_u32 m0, s80, 86016\n"
  "s_add_u32 s74, s38, 1966080\n"
  "s_addc_u32 s75, s39, 0\n"
  "global_load_lds_dwordx4 v1, s[74:75]\n"
  "s_add_u32 m0, s80, 90112\n"
  "s_add_u32 s74, s40, 1572864\n"
  "s_addc_u32 s75, s41, 0\n"
  "global_load_lds_dwordx4 v4, s[74:75]\n"
  "s_add_u32 m0, s80, 94208\n"
  "s_add_u32 s74, s40, 1835008\n"
  "s_addc_u32 s75, s41, 0\n"
  "global_load_lds_dwordx4 v4, s[74:75]\n"
  "s_add_u32 s64, s36, s46\n"
  "s_addc_u32 s65, s37, 0\n"
  "s_waitcnt vmcnt(18) lgkmcnt(0)\n"
  "s_barrier\n"
  "s_add_u32 m0, s80, 98304\n"
  "s_add_u32 s74, s38, 2097152\n"
  "s_addc_u32 s75, s39, 0\n"
  "global_load_lds_dwordx4 v1, s[74:75]\n"
  "s_add_u32 m0, s80, 102400\n"
  "s_add_u32 s74, s38, 2228224\n"
  "s_addc_u32 s75, s39, 0\n"
  "global_load_lds_dwordx4 v1, s[74:75]\n"
  "s_add_u32 m0, s80, 106496\n"
  "s_add_u32 s74, s38, 2359296\n"
  "s_addc_u32 s75, s39, 0\n"
  "global_load_lds_dwordx4 v1, s[74:75]\n"
  "s_add_u32 m0, s80, 110592\n"
  "s_add_u32 s74, s38, 2490368\n"
  "s_addc_u32 s75, s39, 0\n"
  "global_load_lds_dwordx4 v1, s[74:75]\n"
  "s_add_u32 m0, s80, 114688\n"
  "s_add_u32 s74, s40, 2097152\n"
  "s_addc_u32 s75, s41, 0\n"
  "global_load_lds_dwordx4 v4, s[74:75]\n"
  "s_add_u32 m0, s80, 118784\n"
  "s_add_u32 s74, s40, 2359296\n"
  "s_addc_u32 s75, s41, 0\n"
  "global_load_lds_dwordx4 v4, s[74:75]\n"
  "v_xor_b32 v6, 0, v2\n"
  "v_add_u32 v6, 0, v6\n"
  "ds_read_b64_tr_b16 a[0:1], v6 offset:0\n"
  "ds_read_b64_tr_b16 a[2:3], v6 offset:1024\n"
  "ds_read_b64_tr_b16 a[4:5], v6 offset:8192\n"
  "ds_read_b64_tr_b16 a[6:7], v6 offset:9216\n"
  "v_xor_b32 v6, 32, v2\n"
  "v_add_u32 v6, 0, v6\n"
  "ds_read_b64_tr_b16 a[72:73], v6 offset:0\n"
  "ds_read_b64_tr_b16 a[74:75], v6 offset:1024\n"
  "ds_read_b64_tr_b16 a[76:77], v6 offset:8192\n"
  "ds_read_b64_tr_b16 a[78:79], v6 offset:9216\n"
  "v_add_u32 v6, 16384, v5\n"
  "ds_read_b64_tr_b16 a[144:145], v6 offset:0\n"
  "ds_read_b64_tr_b16 a[146:147], v6 offset:512\n"
  "ds_read_b64_tr_b16 a[148:149], v6 offset:4096\n"
  "ds_read_b64_tr_b16 a[150:151], v6 offset:4608\n"
  "s_waitcnt vmcnt(18) lgkmcnt(0)\n"
  "s_barrier\n"
  "s_add_u32 m0, s80, 0\n"
  "s_add_u32 s74, s38, 2621440\n"
  "s_addc_u32 s75, s39, 0\n"
  "global_load_lds_dwordx4 v1, s[74:75]\n"
  "s_add_u32 m0, s80, 4096\n"
  "s_add_u32 s74, s38, 2752512\n"
  "s_addc_u32 s75, s39, 0\n"
  "global_load_lds_dwordx4 v1, s[74:75]\n"
  "s_add_u32 m0, s80, 8192\n"
  "s_add_u32 s74, s38, 2883584\n"
  "s_addc_u32 s75, s39, 0\n"
  "global_load_lds_dwordx4 v1, s[74:75]\n"
  "s_add_u32 m0, s80, 12288\n"
  "s_add_u32 s74, s38, 3014656\n"
  "s_addc_u32 s75, s39, 0\n"
  "global_load_lds_dwordx4 v1, s[74:75]\n"
  "s_add_u32 m0, s80, 16384\n"
  "s_add_u32 s74, s40, 2621440\n"
  "s_addc_u32 s75, s41, 0\n"
  "global_load_lds_dwordx4 v4, s[74:75]\n"
  "s_add_u32 m0, s80, 20480\n"
  "s_add_u32 s74, s40, 2883584\n"
  "s_addc_u32 s75, s41, 0\n"
  "global_load_lds_dwordx4 v4, s[74:75]\n"
  "v_xor_b32 v6, 0, v2\n"
  "v_add_u32 v6, 24576, v6\n"
  "ds_read_b64_tr_b16 a[8:9], v6 offset:0\n"
  "ds_read_b64_tr_b16 a[10:11], v6 offset:1024\n"
  "ds_read_b64_tr_b16 a[12:13], v6 offset:8192\n"
  "ds_read_b64_tr_b16 a[14:15], v6 offset:9216\n"
  "v_xor_b32 v6, 32, v2\n"
  "v_add_u32 v6, 24576, v6\n"
  "ds_read_b64_tr_b16 a[80:81], v6 offset:0\n"
  "ds_read_b64_tr_b16 a[82:83], v6 offset:1024\n"
  "ds_read_b64_tr_b16 a[84:85], v6 offset:8192\n"
  "ds_read_b64_tr_b16 a[86:87], v6 offset:9216\n"
  "v_add_u32 v6, 40960, v5\n"
  "ds_read_b64_tr_b16 a[152:153], v6 offset:0\n"
  "ds_read_b64_tr_b16 a[154:155], v6 offset:512\n"
  "ds_read_b64_tr_b16 a[156:157], v6 offset:4096\n"
  "ds_read_b64_tr_b16 a[158:159], v6 offset:4608\n"
  "s_waitcnt vmcnt(18) lgkmcnt(0)\n"
  "s_barrier\n"
  "s_add_u32 m0, s80, 24576\n"
  "s_add_u32 s74, s38, 3145728\n"
  "s_addc_u32 s75, s39, 0\n"
  "global_load_lds_dwordx4 v1, s[74:75]\n"
  "s_add_u32 m0, s80, 28672\n"
  "s_add_u32 s74, s38, 3276800\n"
  "s_addc_u32 s75, s39, 0\n"
  "global_load_lds_dwordx4 v1, s[74:75]\n"
  "s_add_u32 m0, s80, 32768\n"
  "s_add_u32 s74, s38, 3407872\n"
  "s_addc_u32 s75, s39, 0\n"
  "global_load_lds_dwordx4 v1, s[74:75]\n"
  "s_add_u32 m0, s80, 36864\n"
  "s_add_u32 s74, s38, 3538944\n"
  "s_addc_u32 s75, s39, 0\n"
  "global_load_lds_dwordx4 v1, s[74:75]\n"
  "s_add_u32 m0, s80, 40960\n"
  "s_add_u32 s74, s40, 3145728\n"
  "s_addc_u32 s75, s41, 0\n"
  "global_load_lds_dwordx4 v4, s[74:75]\n"
  "s_add_u32 m0, s80, 45056\n"
  "s_add_u32 s74, s40, 3407872\n"
  "s_addc_u32 s75, s41, 0\n"
  "global_load_lds_dwordx4 v4, s[74:75]\n"
  "v_xor_b32 v6, 0, v2\n"
  "v_add_u32 v6, 49152, v6\n"
  "ds_read_b64_tr_b16 a[16:17], v6 offset:0\n"
  "ds_read_b64_tr_b16 a[18:19], v6 offset:1024\n"
  "ds_read_b64_tr_b16 a[20:21], v6 offset:8192\n"
  "ds_read_b64_tr_b16 a[22:23], v6 offset:9216\n"
  "v_xor_b32 v6, 32, v2\n"
  "v_add_u32 v6, 49152, v6\n"
  "ds_read_b64_tr_b16 a[88:89], v6 offset:0\n"
  "ds_read_b64_tr_b16 a[90:91], v6 offset:1024\n"
  "ds_read_b64_tr_b16 a[92:93], v6 offset:8192\n"
  "ds_read_b64_tr_b16 a[94:95], v6 offset:9216\n"
  "v_add_u32 v6, 65536, v5\n"
  "ds_read_b64_tr_b16 a[160:161], v6 offset:0\n"
  "ds_read_b64_tr_b16 a[162:163], v6 offset:512\n"
  "ds_read_b64_tr_b16 a[164:165], v6 offset:4096\n"
  "ds_read_b64_tr_b16 a[166:167], v6 offset:4608\n"
  "s_waitcnt vmcnt(18) lgkmcnt(0)\n"
  "s_barrier\n"
  "s_add_u32 m0, s80, 49152\n"
  "s_add_u32 s74, s38, 3670016\n"
  "s_addc_u32 s75, s39, 0\n"
  "global_load_lds_dwordx4 v1, s[74:75]\n"
  "s_add_u32 m0, s80, 53248\n"
  "s_add_u32 s74, s38, 3801088\n"
  "s_addc_u32 s75, s39, 0\n"
  "global_load_lds_dwordx4 v1, s[74:75]\n"
  "s_add_u32 m0, s80, 57344\n"
  "s_add_u32 s74, s38, 3932160\n"
  "s_addc_u32 s75, s39, 0\n"
  "global_load_lds_dwordx4 v1, s[74:75]\n"
  "s_add_u32 m0, s80, 61440\n"
  "s_add_u32 s74, s38, 4063232\n"
  "s_addc_u32 s75, s39, 0\n"
  "global_load_lds_dwordx4 v1, s[74:75]\n"
  "s_add_u32 m0, s80, 65536\n"
  "s_add_u32 s74, s40, 3670016\n"
  "s_addc_u32 s75, s41, 0\n"
  "global_load_lds_dwordx4 v4, s[74:75]\n"
  "s_add_u32 m0, s80, 69632\n"
  "s_add_u32 s74, s40, 3932160\n"
  "s_addc_u32 s75, s41, 0\n"
  "global_load_lds_dwordx4 v4, s[74:75]\n"
  "v_xor_b32 v6, 0, v2\n"
  "v_add_u32 v6, 73728, v6\n"
  "ds_read_b64_tr_b16 a[24:25], v6 offset:0\n"
  "ds_read_b64_tr_b16 a[26:27], v6 offset:1024\n"
  "ds_read_b64_tr_b16 a[28:29], v6 offset:8192\n"
  "ds_read_b64_tr_b16 a[30:31], v6 offset:9216\n"
  "v_xor_b32 v6, 32, v2\n"
  "v_add_u32 v6, 73728, v6\n"
  "ds_read_b64_tr_b16 a[96:97], v6 offset:0\n"
  "ds_read_b64_tr_b16 a[98:99], v6 offset:1024\n"
  "ds_read_b64_tr_b16 a[100:101], v6 offset:8192\n"
  "ds_read_b64_tr_b16 a[102:103], v6 offset:9216\n"
  "v_add_u32 v6, 90112, v5\n"
  "ds_read_b64_tr_b16 a[168:169], v6 offset:0\n"
  "ds_read_b64_tr_b16 a[170:171], v6 offset:512\n"
  "ds_read_b64_tr_b16 a[172:173], v6 offset:4096\n"
  "ds_read_b64_tr_b16 a[174:175], v6 offset:4608\n"
  "s_waitcnt vmcnt(18) lgkmcnt(0)\n"
  "s_barrier\n"
  "s_add_u32 m0, s80, 73728\n"
  "s_add_u32 s74, s38, 4194304\n"
  "s_addc_u32 s75, s39, 0\n"
  "global_load_lds_dwordx4 v1, s[74:75]\n"
  "s_add_u32 m0, s80, 77824\n"
  "s_add_u32 s74, s38, 4325376\n"
  "s_addc_u32 s75, s39, 0\n"
  "global_load_lds_dwordx4 v1, s[74:75]\n"
  "s_add_u32 m0, s80, 81920\n"
  "s_add_u32 s74, s38, 4456448\n"
  "s_addc_u32 s75, s39, 0\n"
  "global_load_lds_dwordx4 v1, s[74:75]\n"
  "s_add_u32 m0, s80, 86016\n"
  "s_add_u32 s74, s38, 4587520\n"
  "s_addc_u32 s75, s39, 0\n"
  "global_load_lds_dwordx4 v1, s[74:75]\n"
  "s_add_u32 m0, s80, 90112\n"
  "s_add_u32 s74, s40, 4194304\n"
  "s_addc_u32 s75, s41, 0\n"
  "global_load_lds_dwordx4 v4, s[74:75]\n"
  "s_add_u32 m0, s80, 94208\n"
  "s_add_u32 s74, s40, 4456448\n"
  "s_addc_u32 s75, s41, 0\n"
  "global_load_lds_dwordx4 v4, s[74:75]\n"
  "v_xor_b32 v6, 0, v2\n"
  "v_add_u32 v6, 98304, v6\n"
  "ds_read_b64_tr_b16 a[32:33], v6 offset:0\n"
  "ds_read_b64_tr_b16 a[34:35], v6 offset:1024\n"
  "ds_read_b64_tr_b16 a[36:37], v6 offset:8192\n"
  "ds_read_b64_tr_b16 a[38:39], v6 offset:9216\n"
  "v_xor_b32 v6, 32, v2\n"
  "v_add_u32 v6, 98304, v6\n"
  "ds_read_b64_tr_b16 a[104:105], v6 offset:0\n"
  "ds_read_b64_tr_b16 a[106:107], v6 offset:1024\n"
  "ds_read_b64_tr_b16 a[108:109], v6 offset:8192\n"
  "ds_read_b64_tr_b16 a[110:111], v6 offset:9216\n"
  "v_add_u32 v6, 114688, v5\n"
  "ds_read_b64_tr_b16 a[176:177], v6 offset:0\n"
  "ds_read_b64_tr_b16 a[178:179], v6 offset:512\n"
  "ds_read_b64_tr_b16 a[180:181], v6 offset:4096\n"
  "ds_read_b64_tr_b16 a[182:183], v6 offset:4608\n"
  "s_waitcnt vmcnt(18) lgkmcnt(0)\n"
  "s_barrier\n"
  "v_xor_b32 v6, 0, v2\n"
  "v_add_u32 v6, 0, v6\n"
  "ds_read_b64_tr_b16 a[40:41], v6 offset:0\n"
  "ds_read_b64_tr_b16 a[42:43], v6 offset:1024\n"
  "ds_read_b64_tr_b16 a[44:45], v6 offset:8192\n"
  "ds_read_b64_tr_b16 a[46:47], v6 offset:9216\n"
  "v_xor_b32 v6, 32, v2\n"
  "v_add_u32 v6, 0, v6\n"
  "ds_read_b64_tr_b16 a[112:113], v6 offset:0\n"
  "ds_read_b64_tr_b16 a[114:115], v6 offset:1024\n"
  "ds_read_b64_tr_b16 a[116:117], v6 offset:8192\n"
  "ds_read_b64_tr_b16 a[118:119], v6 offset:9216\n"
  "v_add_u32 v6, 16384, v5\n"
  "ds_read_b64_tr_b16 a[184:185], v6 offset:0\n"
  "ds_read_b64_tr_b16 a[186:187], v6 offset:512\n"
  "ds_read_b64_tr_b16 a[188:189], v6 offset:4096\n"
  "ds_read_b64_tr_b16 a[190:191], v6 offset:4608\n"
  "s_waitcnt vmcnt(12) lgkmcnt(0)\n"
  "s_barrier\n"
  "v_xor_b32 v6, 0, v2\n"
  "v_add_u32 v6, 24576, v6\n"
  "ds_read_b64_tr_b16 a[48:49], v6 offset:0\n"
  "ds_read_b64_tr_b16 a[50:51], v6 offset:1024\n"
  "ds_read_b64_tr_b16 a[52:53], v6 offset:8192\n"
  "ds_read_b64_tr_b16 a[54:55], v6 offset:9216\n"
  "v_xor_b32 v6, 32, v2\n"
  "v_add_u32 v6, 24576, v6\n"
  "ds_read_b64_tr_b16 a[120:121], v6 offset:0\n"
  "ds_read_b64_tr_b16 a[122:123], v6 offset:1024\n"
  "ds_read_b64_tr_b16 a[124:125], v6 offset:8192\n"
  "ds_read_b64_tr_b16 a[126:127], v6 offset:9216\n"
  "v_add_u32 v6, 40960, v5\n"
  "ds_read_b64_tr_b16 a[192:193], v6 offset:0\n"
  "ds_read_b64_tr_b16 a[194:195], v6 offset:512\n"
  "ds_read_b64_tr_b16 a[196:197], v6 offset:4096\n"
  "ds_read_b64_tr_b16 a[198:199], v6 offset:4608\n"
  "s_waitcnt vmcnt(6) lgkmcnt(0)\n"
  "s_barrier\n"
  "v_xor_b32 v6, 0, v2\n"
  "v_add_u32 v6, 49152, v6\n"
  "ds_read_b64_tr_b16 a[56:57], v6 offset:0\n"
  "ds_read_b64_tr_b16 a[58:59], v6 offset:1024\n"
  "ds_read_b64_tr_b16 a[60:61], v6 offset:8192\n"
  "ds_read_b64_tr_b16 a[62:63], v6 offset:9216\n"
  "v_xor_b32 v6, 32, v2\n"
  "v_add_u32 v6, 49152, v6\n"
  "ds_read_b64_tr_b16 a[128:129], v6 offset:0\n"
  "ds_read_b64_tr_b16 a[130:131], v6 offset:1024\n"
  "ds_read_b64_tr_b16 a[132:133], v6 offset:8192\n"
  "ds_read_b64_tr_b16 a[134:135], v6 offset:9216\n"
  "v_add_u32 v6, 65536, v5\n"
  "ds_read_b64_tr_b16 a[200:201], v6 offset:0\n"
  "ds_read_b64_tr_b16 a[202:203], v6 offset:512\n"
  "ds_read_b64_tr_b16 a[204:205], v6 offset:4096\n"
  "ds_read_b64_tr_b16 a[206:207], v6 offset:4608\n"
  "s_waitcnt vmcnt(0) lgkmcnt(0)\n"
  "s_barrier\n"
  "v_xor_b32 v6, 0, v2\n"
  "v_add_u32 v6, 73728, v6\n"
  "ds_read_b64_tr_b16 a[64:65], v6 offset:0\n"
  "ds_read_b64_tr_b16 a[66:67], v6 offset:1024\n"
  "ds_read_b64_tr_b16 a[68:69], v6 offset:8192\n"
  "ds_read_b64_tr_b16 a[70:71], v6 offset:9216\n"
  "v_xor_b32 v6, 32, v2\n"
  "v_add_u32 v6, 73728, v6\n"
  "ds_read_b64_tr_b16 a[136:137], v6 offset:0\n"
  "ds_read_b64_tr_b16 a[138:139], v6 offset:1024\n"
  "ds_read_b64_tr_b16 a[140:141], v6 offset:8192\n"
  "ds_read_b64_tr_b16 a[142:143], v6 offset:9216\n"
  "v_add_u32 v6, 90112, v5\n"
  "ds_read_b64_tr_b16 a[208:209], v6 offset:0\n"
  "ds_read_b64_tr_b16 a[210:211], v6 offset:512\n"
  "ds_read_b64_tr_b16 a[212:213], v6 offset:4096\n"
  "ds_read_b64_tr_b16 a[214:215], v6 offset:4608\n"
  "s_mov_b32 s56, 0\n"
  "s_mov_b32 s57, 122880\n"
  "v_add_u32 v12, 122880, v9\n"
  "v_xor_b32 v13, 64, v12\n"
  "s_waitcnt lgkmcnt(0)\n"
  "s_barrier\n"
  "s_add_u32 s58, s80, 12288\n"
  "s_add_u32 s42, s42, 36864\n"
  "s_addc_u32 s43, s43, 0\n"
  "ds_read_b128 v[16:19], v12 offset:0\n"
  "ds_read_b128 v[20:23], v12 offset:2048\n"
  "ds_read_b128 v[24:27], v13 offset:0\n"
  "ds_read_b128 v[28:31], v13 offset:2048\n"
  "ds_read_b128 v[32:35], v12 offset:4096\n"
  "ds_read_b128 v[36:39], v12 offset:6144\n"
  "s_add_u32 m0, s58, 0\n"
  "s_nop 0\n"
  "global_load_lds_dwordx4 v3, s[42:43] offset:0\n"
  "s_add_u32 m0, s58, 3968\n"
  "s_nop 0\n"
  "global_load_lds_dwordx4 v3, s[42:43] offset:128\n"
  "s_add_u32 m0, s58, 7936\n"
  "s_nop 0\n"
  "global_load_lds_dwordx4 v3, s[42:43] offset:256\n"
  "s_add_u32 m0, s58, 11904\n"
  "s_nop 0\n"
  "global_load_lds_dwordx4 v3, s[42:43] offset:384\n"
  "s_add_u32 m0, s58, 15872\n"
  "s_nop 0\n"
  "global_load_lds_dwordx4 v3, s[42:43] offset:512\n"
  "s_add_u32 m0, s58, 19840\n"
  "s_nop 0\n"
  "global_load_lds_dwordx4 v3, s[42:43] offset:640\n"
  "s_add_u32 m0, s58, 23808\n"
  "s_nop 0\n"
  "global_load_lds_dwordx4 v3, s[42:43] offset:768\n"
  "s_add_u32 m0, s58, 27776\n"
  "s_nop 0\n"
  "global_load_lds_dwordx4 v3, s[42:43] offset:896\n"
  "s_add_u32 m0, s58, 31744\n"
  "s_nop 0\n"
  "global_load_lds_dwordx4 v3, s[42:43] offset:1024\n"
  "v_mov_b64 v[10:11], v[12:13]\n"
  "s_waitcnt lgkmcnt(5)\n"
  "v_mfma_f32_16x16x32_bf16 v[48:51], a[0:3], v[16:19], 0\n"
  "ds_read_b128 v[40:43], v11 offset:4096\n"
  "s_waitcnt lgkmcnt(5)\n"
  "v_mfma_f32_16x16x32_bf16 v[52:55], a[0:3], v[20:23], 0\n"
  "ds_read_b128 v[44:47], v11 offset:6144\n"
  "v_mfma_f32_16x16x32_bf16 v[56:59], a[72:75], v[16:19], 0\n"
  "s_add_u32 s57, s57, 36864\n"
  "s_cmp_eq_u32 s57, 159744\n"
  "v_mfma_f32_16x16x32_bf16 v[60:63], a[72:75], v[20:23], 0\n"
  "s_cselect_b32 s57, 12288, s57\n"
  "v_add_u32 v12, s57, v9\n"
  "v_mfma_f32_16x16x32_bf16 v[64:67], v[16:19], a[144:147], 0\n"
  "v_xor_b32 v13, 64, v12\n"
  "s_add_u32 s58, s57, 36864\n"
  "v_mfma_f32_16x16x32_bf16 v[68:71], v[20:23], a[144:147], 0\n"
  "s_cmp_eq_u32 s57, 122880\n"
  "s_cselect_b32 s58, 12288, s58\n"
  "s_waitcnt lgkmcnt(5)\n"
  "v_mfma_f32_16x16x32_bf16 v[48:51], a[4:7], v[24:27], v[48:51]\n"
  "ds_read_b128 v[16:19], v10 offset:8192\n"
  "s_waitcnt lgkmcnt(5)\n"
  "v_mfma_f32_16x16x32_bf16 v[52:55], a[4:7], v[28:31], v[52:55]\n"
  "ds_read_b128 v[20:23], v10 offset:10240\n"
  "v_mfma_f32_16x16x32_bf16 v[56:59], a[76:79], v[24:27], v[56:59]\n"
  "s_add_u32 s58, s58, s80\n"
  "s_cmp_lt_u32 s56, 62\n"
  "v_mfma_f32_16x16x32_bf16 v[60:63], a[76:79], v[28:31], v[60:63]\n"
  "s_cselect_b32 s74, 36864, 0\n"
  "s_add_u32 s42, s42, s74\n"
  "v_mfma_f32_16x16x32_bf16 v[64:67], v[24:27], a[148:151], v[64:67]\n"
  "s_addc_u32 s43, s43, 0\n"
  "v_mfma_f32_16x16x32_bf16 v[68:71], v[28:31], a[148:151], v[68:71]\n"
  "s_waitcnt lgkmcnt(5)\n"
  "v_mfma_f32_16x16x32_bf16 v[48:51], a[8:11], v[32:35], v[48:51]\n"
  "ds_read_b128 v[24:27], v11 offset:8192\n"
  "s_waitcnt lgkmcnt(5)\n"
  "v_mfma_f32_16x16x32_bf16 v[52:55], a[8:11], v[36:39], v[52:55]\n"
  "ds_read_b128 v[28:31], v11 offset:10240\n"
  "v_mfma_f32_16x16x32_bf16 v[56:59], a[80:83], v[32:35], v[56:59]\n"
  "v_mfma_f32_16x16x32_bf16 v[60:63], a[80:83], v[36:39], v[60:63]\n"
  "v_mfma_f32_16x16x32_bf16 v[64:67], v[32:35], a[152:155], v[64:67]\n"
  "v_mfma_f32_16x16x32_bf16 v[68:71], v[36:39], a[152:155], v[68:71]\n"
  "s_waitcnt lgkmcnt(5)\n"
  "v_mfma_f32_16x16x32_bf16 v[48:51], a[12:15], v[40:43], v[48:51]\n"
  "ds_read_b128 v[32:35], v10 offset:12288\n"
  "s_waitcnt lgkmcnt(5)\n"
  "v_mfma_f32_16x16x32_bf16 v[52:55], a[12:15], v[44:47], v[52:55]\n"
  "ds_read_b128 v[36:39], v10 offset:14336\n"
  "v_mfma_f32_16x16x32_bf16 v[56:59], a[84:87], v[40:43], v[56:59]\n"
  "v_mfma_f32_16x16x32_bf16 v[60:63], a[84:87], v[44:47], v[60:63]\n"
  "v_mfma_f32_16x16x32_bf16 v[64:67], v[40:43], a[156:159], v[64:67]\n"
  "v_mfma_f32_16x16x32_bf16 v[68:71], v[44:47], a[156:159], v[68:71]\n"
  "s_waitcnt lgkmcnt(5)\n"
  "v_mfma_f32_16x16x32_bf16 v[48:51], a[16:19], v[16:19], v[48:51]\n"
  "ds_read_b128 v[40:43], v11 offset:12288\n"
  "s_waitcnt lgkmcnt(5)\n"
  "v_mfma_f32_16x16x32_bf16 v[52:55], a[16:19], v[20:23], v[52:55]\n"
  "ds_read_b128 v[44:47], v11 offset:14336\n"
  "v_mfma_f32_16x16x32_bf16 v[56:59], a[88:91], v[16:19], v[56:59]\n"
  "v_mfma_f32_16x16x32_bf16 v[60:63], a[88:91], v[20:23], v[60:63]\n"
  "v_mfma_f32_16x16x32_bf16 v[64:67], v[16:19], a[160:163], v[64:67]\n"
  "v_mfma_f32_16x16x32_bf16 v[68:71], v[20:23], a[160:163], v[68:71]\n"
  "s_waitcnt lgkmcnt(5)\n"
  "v_mfma_f32_16x16x32_bf16 v[48:51], a[20:23], v[24:27], v[48:51]\n"
  "ds_read_b128 v[16:19], v10 offset:16384\n"
  "s_waitcnt lgkmcnt(5)\n"
  "v_mfma_f32_16x16x32_bf16 v[52:55], a[20:23], v[28:31], v[52:55]\n"
  "ds_read_b128 v[20:23], v10 offset:18432\n"
  "v_mfma_f32_16x16x32_bf16 v[56:59], a[92:95], v[24:27], v[56:59]\n"
  "v_mfma_f32_16x16x32_bf16 v[60:63], a[92:95], v[28:31], v[60:63]\n"
  "v_mfma_f32_16x16x32_bf16 v[64:67], v[24:27], a[164:167], v[64:67]\n"
  "v_mfma_f32_16x16x32_bf16 v[68:71], v[28:31], a[164:167], v[68:71]\n"
  "s_waitcnt lgkmcnt(5)\n"
  "v_mfma_f32_16x16x32_bf16 v[48:51], a[24:27], v[32:35], v[48:51]\n"
  "ds_read_b128 v[24:27], v11 offset:16384\n"
  "s_waitcnt lgkmcnt(5)\n"
  "v_mfma_f32_16x16x32_bf16 v[52:55], a[24:27], v[36:39], v[52:55]\n"
  "ds_read_b128 v[28:31], v11 offset:18432\n"
  "v_mfma_f32_16x16x32_bf16 v[56:59], a[96:99], v[32:35], v[56:59]\n"
  "v_mfma_f32_16x16x32_bf16 v[60:63], a[96:99], v[36:39], v[60:63]\n"
  "s_add_u32 m0, s58, 0\n"
  "s_nop 0\n"
  "global_load_lds_dwordx4 v3, s[42:43] offset:0\n"
  "v_mfma_f32_16x16x32_bf16 v[64:67], v[32:35], a[168:171], v[64:67]\n"
  "v_mfma_f32_16x16x32_bf16 v[68:71], v[36:39], a[168:171], v[68:71]\n"
  "s_waitcnt lgkmcnt(5)\n"
  "v_mfma_f32_16x16x32_bf16 v[48:51], a[28:31], v[40:43], v[48:51]\n"
  "ds_read_b128 v[32:35], v10 offset:20480\n"
  "s_waitcnt lgkmcnt(5)\n"
  "v_mfma_f32_16x16x32_bf16 v[52:55], a[28:31], v[44:47], v[52:55]\n"
  "ds_read_b128 v[36:39], v10 offset:22528\n"
  "v_mfma_f32_16x16x32_bf16 v[56:59], a[100:103], v[40:43], v[56:59]\n"
  "v_mfma_f32_16x16x32_bf16 v[60:63], a[100:103], v[44:47], v[60:63]\n"
  "s_add_u32 m0, s58, 3968\n"
  "s_nop 0\n"
  "global_load_lds_dwordx4 v3, s[42:43] offset:128\n"
  "v_mfma_f32_16x16x32_bf16 v[64:67], v[40:43], a[172:175], v[64:67]\n"
  "v_mfma_f32_16x16x32_bf16 v[68:71], v[44:47], a[172:175], v[68:71]\n"
  "s_waitcnt lgkmcnt(5)\n"
  "v_mfma_f32_16x16x32_bf16 v[48:51], a[32:35], v[16:19], v[48:51]\n"
  "ds_read_b128 v[40:43], v11 offset:20480\n"
  "s_waitcnt lgkmcnt(5)\n"
  "v_mfma_f32_16x16x32_bf16 v[52:55], a[32:35], v[20:23], v[52:55]\n"
  "ds_read_b128 v[44:47], v11 offset:22528\n"
  "v_mfma_f32_16x16x32_bf16 v[56:59], a[104:107], v[16:19], v[56:59]\n"
  "v_mfma_f32_16x16x32_bf16 v[60:63], a[104:107], v[20:23], v[60:63]\n"
  "s_add_u32 m0, s58, 7936\n"
  "s_nop 0\n"
  "global_load_lds_dwordx4 v3, s[42:43] offset:256\n"
  "v_mfma_f32_16x16x32_bf16 v[64:67], v[16:19], a[176:179], v[64:67]\n"
  "v_mfma_f32_16x16x32_bf16 v[68:71], v[20:23], a[176:179], v[68:71]\n"
  "s_waitcnt lgkmcnt(5)\n"
  "v_mfma_f32_16x16x32_bf16 v[48:51], a[36:39], v[24:27], v[48:51]\n"
  "ds_read_b128 v[16:19], v10 offset:24576\n"
  "s_waitcnt lgkmcnt(5)\n"
  "v_mfma_f32_16x16x32_bf16 v[52:55], a[36:39], v[28:31], v[52:55]\n"
  "ds_read_b128 v[20:23], v10 offset:26624\n"
  "v_mfma_f32_16x16x32_bf16 v[56:59], a[108:111], v[24:27], v[56:59]\n"
  "v_mfma_f32_16x16x32_bf16 v[60:63], a[108:111], v[28:31], v[60:63]\n"
  "s_add_u32 m0, s58, 11904\n"
  "s_nop 0\n"
  "global_load_lds_dwordx4 v3, s[42:43] offset:384\n"
  "v_mfma_f32_16x16x32_bf16 v[64:67], v[24:27], a[180:183], v[64:67]\n"
  "v_mfma_f32_16x16x32_bf16 v[68:71], v[28:31], a[180:183], v[68:71]\n"
  "s_waitcnt lgkmcnt(5)\n"
  "v_mfma_f32_16x16x32_bf16 v[48:51], a[40:43], v[32:35], v[48:51]\n"
  "ds_read_b128 v[24:27], v11 offset:24576\n"
  "s_waitcnt lgkmcnt(5)\n"
  "v_mfma_f32_16x16x32_bf16 v[52:55], a[40:43], v[36:39], v[52:55]\n"
  "ds_read_b128 v[28:31], v11 offset:26624\n"
  "v_mfma_f32_16x16x32_bf16 v[56:59], a[112:115], v[32:35], v[56:59]\n"
  "v_mfma_f32_16x16x32_bf16 v[60:63], a[112:115], v[36:39], v[60:63]\n"
  "s_add_u32 m0, s58, 15872\n"
  "s_nop 0\n"
  "global_load_lds_dwordx4 v3, s[42:43] offset:512\n"
  "v_mfma_f32_16x16x32_bf16 v[64:67], v[32:35], a[184:187], v[64:67]\n"
  "v_mfma_f32_16x16x32_bf16 v[68:71], v[36:39], a[184:187], v[68:71]\n"
  "s_waitcnt lgkmcnt(5)\n"
  "v_mfma_f32_16x16x32_bf16 v[48:51], a[44:47], v[40:43], v[48:51]\n"
  "ds_read_b128 v[32:35], v10 offset:28672\n"
  "s_waitcnt lgkmcnt(5)\n"
  "v_mfma_f32_16x16x32_bf16 v[52:55], a[44:47], v[44:47], v[52:55]\n"
  "ds_read_b128 v[36:39], v10 offset:30720\n"
  "v_mfma_f32_16x16x32_bf16 v[56:59], a[116:119], v[40:43], v[56:59]\n"
  "v_mfma_f32_16x16x32_bf16 v[60:63], a[116:119], v[44:47], v[60:63]\n"
  "s_add_u32 m0, s58, 19840\n"
  "s_nop 0\n"
  "global_load_lds_dwordx4 v3, s[42:43] offset:640\n"
  "v_mfma_f32_16x16x32_bf16 v[64:67], v[40:43], a[188:191], v[64:67]\n"
  "v_mfma_f32_16x16x32_bf16 v[68:71], v[44:47], a[188:191], v[68:71]\n"
  "s_waitcnt lgkmcnt(5)\n"
  "v_mfma_f32_16x16x32_bf16 v[48:51], a[48:51], v[16:19], v[48:51]\n"
  "ds_read_b128 v[40:43], v11 offset:28672\n"
  "s_waitcnt lgkmcnt(5)\n"
  "v_mfma_f32_16x16x32_bf16 v[52:55], a[48:51], v[20:23], v[52:55]\n"
  "ds_read_b128 v[44:47], v11 offset:30720\n"
  "v_mfma_f32_16x16x32_bf16 v[56:59], a[120:123], v[16:19], v[56:59]\n"
  "v_mfma_f32_16x16x32_bf16 v[60:63], a[120:123], v[20:23], v[60:63]\n"
  "s_add_u32 m0, s58, 23808\n"
  "s_nop 0\n"
  "global_load_lds_dwordx4 v3, s[42:43] offset:768\n"
  "v_mfma_f32_16x16x32_bf16 v[64:67], v[16:19], a[192:195], v[64:67]\n"
  "v_mfma_f32_16x16x32_bf16 v[68:71], v[20:23], a[192:195], v[68:71]\n"
  "s_waitcnt lgkmcnt(5)\n"
  "v_mfma_f32_16x16x32_bf16 v[48:51], a[52:55], v[24:27], v[48:51]\n"
  "ds_read_b128 v[16:19], v10 offset:32768\n"
  "s_waitcnt lgkmcnt(5)\n"
  "v_mfma_f32_16x16x32_bf16 v[52:55], a[52:55], v[28:31], v[52:55]\n"
  "ds_read_b128 v[20:23], v10 offset:34816\n"
  "v_mfma_f32_16x16x32_bf16 v[56:59], a[124:127], v[24:27], v[56:59]\n"
  "v_mfma_f32_16x16x32_bf16 v[60:63], a[124:127], v[28:31], v[60:63]\n"
  "s_add_u32 m0, s58, 27776\n"
  "s_nop 0\n"
  "global_load_lds_dwordx4 v3, s[42:43] offset:896\n"
  "v_mfma_f32_16x16x32_bf16 v[64:67], v[24:27], a[196:199], v[64:67]\n"
  "v_mfma_f32_16x16x32_bf16 v[68:71], v[28:31], a[196:199], v[68:71]\n"
  "s_waitcnt lgkmcnt(5)\n"
  "v_mfma_f32_16x16x32_bf16 v[48:51], a[56:59], v[32:35], v[48:51]\n"
  "ds_read_b128 v[24:27], v11 offset:32768\n"
  "s_waitcnt lgkmcnt(5)\n"
  "v_mfma_f32_16x16x32_bf16 v[52:55], a[56:59], v[36:39], v[52:55]\n"
  "ds_read_b128 v[28:31], v11 offset:34816\n"
  "v_mfma_f32_16x16x32_bf16 v[56:59], a[128:131], v[32:35], v[56:59]\n"
  "v_mfma_f32_16x16x32_bf16 v[60:63], a[128:131], v[36:39], v[60:63]\n"
  "s_add_u32 m0, s58, 31744\n"
  "s_nop 0\n"
  "global_load_lds_dwordx4 v3, s[42:43] offset:1024\n"
  "v_mfma_f32_16x16x32_bf16 v[64:67], v[32:35], a[200:203], v[64:67]\n"
  "v_mfma_f32_16x16x32_bf16 v[68:71], v[36:39], a[200:203], v[68:71]\n"
  "s_waitcnt vmcnt(9)\n"
  "s_barrier\n"
  "s_waitcnt lgkmcnt(5)\n"
  "v_mfma_f32_16x16x32_bf16 v[48:51], a[60:63], v[40:43], v[48:51]\n"
  "ds_read_b128 v[32:35], v12 offset:0\n"
  "s_waitcnt lgkmcnt(5)\n"
  "v_mfma_f32_16x16x32_bf16 v[52:55], a[60:63], v[44:47], v[52:55]\n"
  "ds_read_b128 v[36:39], v12 offset:2048\n"
  "v_mfma_f32_16x16x32_bf16 v[56:59], a[132:135], v[40:43], v[56:59]\n"
  "s_bitcmp1_b64 s[4:5], s56\n"
  "s_cselect_b32 s74, 8192, 0\n"
  "v_mfma_f32_16x16x32_bf16 v[60:63], a[132:135], v[44:47], v[60:63]\n"
  "s_lshl_b32 s75, s56, 21\n"
  "s_add_u32 s74, s74, s75\n"
  "v_mfma_f32_16x16x32_bf16 v[64:67], v[40:43], a[204:207], v[64:67]\n"
  "s_add_u32 s74, s74, 20971520\n"
  "s_xor_b32 s75, s74, s44\n"
  "v_mfma_f32_16x16x32_bf16 v[68:71], v[44:47], a[204:207], v[68:71]\n"
  "s_add_u32 s60, s36, s75\n"
  "s_addc_u32 s61, s37, 0\n"
  "s_waitcnt lgkmcnt(5)\n"
  "v_mfma_f32_16x16x32_bf16 v[48:51], a[64:67], v[16:19], v[48:51]\n"
  "ds_read_b128 v[40:43], v13 offset:0\n"
  "s_waitcnt lgkmcnt(5)\n"
  "v_mfma_f32_16x16x32_bf16 v[52:55], a[64:67], v[20:23], v[52:55]\n"
  "ds_read_b128 v[44:47], v13 offset:2048\n"
  "v_mfma_f32_16x16x32_bf16 v[56:59], a[136:139], v[16:19], v[56:59]\n"
  "s_xor_b32 s75, s74, s45\n"
  "s_add_u32 s62, s36, s75\n"
  "v_mfma_f32_16x16x32_bf16 v[60:63], a[136:139], v[20:23], v[60:63]\n"
  "s_addc_u32 s63, s37, 0\n"
  "v_mfma_f32_16x16x32_bf16 v[64:67], v[16:19], a[208:211], v[64:67]\n"
  "v_mfma_f32_16x16x32_bf16 v[68:71], v[20:23], a[208:211], v[68:71]\n"
  "s_waitcnt lgkmcnt(5)\n"
  "v_mfma_f32_16x16x32_bf16 v[48:51], a[68:71], v[24:27], v[48:51]\n"
  "ds_read_b128 v[16:19], v12 offset:4096\n"
  "s_waitcnt lgkmcnt(5)\n"
  "v_mfma_f32_16x16x32_bf16 v[52:55], a[68:71], v[28:31], v[52:55]\n"
  "ds_read_b128 v[20:23], v12 offset:6144\n"
  "v_mfma_f32_16x16x32_bf16 v[56:59], a[140:143], v[24:27], v[56:59]\n"
  "v_mfma_f32_16x16x32_bf16 v[60:63], a[140:143], v[28:31], v[60:63]\n"
  "v_mfma_f32_16x16x32_bf16 v[64:67], v[24:27], a[212:215], v[64:67]\n"
  "v_mfma_f32_16x16x32_bf16 v[68:71], v[28:31], a[212:215], v[68:71]\n"
  "s_add_u32 s56, s56, 1\n"
  "v_mul_f32 v48, v8, v48\n"
  "v_mul_f32 v49, v8, v49\n"
  "v_mul_f32 v50, v8, v50\n"
  "v_mul_f32 v51, v8, v51\n"
  "v_cvt_pk_bf16_f32 v96, v48, v49\n"
  "v_cvt_pk_bf16_f32 v97, v50, v51\n"
  "v_mul_f32 v56, v8, v56\n"
  "v_mul_f32 v57, v8, v57\n"
  "v_mul_f32 v58, v8, v58\n"
  "v_mul_f32 v59, v8, v59\n"
  "v_cvt_pk_bf16_f32 v98, v56, v57\n"
  "v_cvt_pk_bf16_f32 v99, v58, v59\n"
  "global_store_dwordx4 v0, v[96:99], s[60:61] offset:0 sc1\n"
  "v_mul_f32 v52, v8, v52\n"
  "v_mul_f32 v53, v8, v53\n"
  "v_mul_f32 v54, v8, v54\n"
  "v_mul_f32 v55, v8, v55\n"
  "v_cvt_pk_bf16_f32 v100, v52, v53\n"
  "v_cvt_pk_bf16_f32 v101, v54, v55\n"
  "v_mul_f32 v60, v8, v60\n"
  "v_mul_f32 v61, v8, v61\n"
  "v_mul_f32 v62, v8, v62\n"
  "v_mul_f32 v63, v8, v63\n"
  "v_cvt_pk_bf16_f32 v102, v60, v61\n"
  "v_cvt_pk_bf16_f32 v103, v62, v63\n"
  "global_store_dwordx4 v0, v[100:103], s[60:61] offset:1024 sc1\n"
  "v_cvt_pk_bf16_f32 v104, v64, v65\n"
  "v_cvt_pk_bf16_f32 v105, v66, v67\n"
  "v_cvt_pk_bf16_f32 v106, v68, v69\n"
  "v_cvt_pk_bf16_f32 v107, v70, v71\n"
  "global_store_dwordx4 v0, v[104:107], s[62:63] offset:0 sc1\n"
  "s_waitcnt vmcnt(0)\n"
  "s_barrier\n"
  "v_mov_b32 v6, 0\n"
  "v_mov_b32 v7, s56\n"
  "s_cmp_eq_u32 s47, 3\n"
  "s_cselect_b64 exec, 1, 0\n"
  "global_store_dword v6, v7, s[64:65] sc1\n"
  "s_mov_b64 exec, -1\n"
  "v_mov_b64 v[10:11], v[12:13]\n"
  "s_waitcnt lgkmcnt(5)\n"
  "v_mfma_f32_16x16x32_bf16 v[72:75], a[0:3], v[32:35], 0\n"
  "ds_read_b128 v[24:27], v11 offset:4096\n"
  "s_waitcnt lgkmcnt(5)\n"
  "v_mfma_f32_16x16x32_bf16 v[76:79], a[0:3], v[36:39], 0\n"
  "ds_read_b128 v[28:31], v11 offset:6144\n"
  "v_mfma_f32_16x16x32_bf16 v[80:83], a[72:75], v[32:35], 0\n"
  "s_add_u32 s57, s57, 36864\n"
  "s_cmp_eq_u32 s57, 159744\n"
  "v_mfma_f32_16x16x32_bf16 v[84:87], a[72:75], v[36:39], 0\n"
  "s_cselect_b32 s57, 12288, s57\n"
  "v_add_u32 v12, s57, v9\n"
  "v_mfma_f32_16x16x32_bf16 v[88:91], v[32:35], a[144:147], 0\n"
  "v_xor_b32 v13, 64, v12\n"
  "s_add_u32 s58, s57, 36864\n"
  "v_mfma_f32_16x16x32_bf16 v[92:95], v[36:39], a[144:147], 0\n"
  "s_cmp_eq_u32 s57, 122880\n"
  "s_cselect_b32 s58, 12288, s58\n"
  "s_waitcnt lgkmcnt(5)\n"
  "v_mfma_f32_16x16x32_bf16 v[72:75], a[4:7], v[40:43], v[72:75]\n"
  "ds_read_b128 v[32:35], v10 offset:8192\n"
  "s_waitcnt lgkmcnt(5)\n"
  "v_mfma_f32_16x16x32_bf16 v[76:79], a[4:7], v[44:47], v[76:79]\n"
  "ds_read_b128 v[36:39], v10 offset:10240\n"
  "v_mfma_f32_16x16x32_bf16 v[80:83], a[76:79], v[40:43], v[80:83]\n"
  "s_add_u32 s58, s58, s80\n"
  "s_cmp_lt_u32 s56, 62\n"
  "v_mfma_f32_16x16x32_bf16 v[84:87], a[76:79], v[44:47], v[84:87]\n"
  "s_cselect_b32 s74, 36864, 0\n"
  "s_add_u32 s42, s42, s74\n"
  "v_mfma_f32_16x16x32_bf16 v[88:91], v[40:43], a[148:151], v[88:91]\n"
  "s_addc_u32 s43, s43, 0\n"
  "v_mfma_f32_16x16x32_bf16 v[92:95], v[44:47], a[148:151], v[92:95]\n"
  "s_waitcnt lgkmcnt(5)\n"
  "v_mfma_f32_16x16x32_bf16 v[72:75], a[8:11], v[16:19], v[72:75]\n"
  "ds_read_b128 v[40:43], v11 offset:8192\n"
  "s_waitcnt lgkmcnt(5)\n"
  "v_mfma_f32_16x16x32_bf16 v[76:79], a[8:11], v[20:23], v[76:79]\n"
  "ds_read_b128 v[44:47], v11 offset:10240\n"
  "v_mfma_f32_16x16x32_bf16 v[80:83], a[80:83], v[16:19], v[80:83]\n"
  "v_mfma_f32_16x16x32_bf16 v[84:87], a[80:83], v[20:23], v[84:87]\n"
  "v_mfma_f32_16x16x32_bf16 v[88:91], v[16:19], a[152:155], v[88:91]\n"
  "v_mfma_f32_16x16x32_bf16 v[92:95], v[20:23], a[152:155], v[92:95]\n"
  "s_waitcnt lgkmcnt(5)\n"
  "v_mfma_f32_16x16x32_bf16 v[72:75], a[12:15], v[24:27], v[72:75]\n"
  "ds_read_b128 v[16:19], v10 offset:12288\n"
  "s_waitcnt lgkmcnt(5)\n"
  "v_mfma_f32_16x16x32_bf16 v[76:79], a[12:15], v[28:31], v[76:79]\n"
  "ds_read_b128 v[20:23], v10 offset:14336\n"
  "v_mfma_f32_16x16x32_bf16 v[80:83], a[84:87], v[24:27], v[80:83]\n"
  "v_mfma_f32_16x16x32_bf16 v[84:87], a[84:87], v[28:31], v[84:87]\n"
  "v_mfma_f32_16x16x32_bf16 v[88:91], v[24:27], a[156:159], v[88:91]\n"
  "v_mfma_f32_16x16x32_bf16 v[92:95], v[28:31], a[156:159], v[92:95]\n"
  "s_waitcnt lgkmcnt(5)\n"
  "v_mfma_f32_16x16x32_bf16 v[72:75], a[16:19], v[32:35], v[72:75]\n"
  "ds_read_b128 v[24:27], v11 offset:12288\n"
  "s_waitcnt lgkmcnt(5)\n"
  "v_mfma_f32_16x16x32_bf16 v[76:79], a[16:19], v[36:39], v[76:79]\n"
  "ds_read_b128 v[28:31], v11 offset:14336\n"
  "v_mfma_f32_16x16x32_bf16 v[80:83], a[88:91], v[32:35], v[80:83]\n"
  "v_mfma_f32_16x16x32_bf16 v[84:87], a[88:91], v[36:39], v[84:87]\n"
  "v_mfma_f32_16x16x32_bf16 v[88:91], v[32:35], a[160:163], v[88:91]\n"
  "v_mfma_f32_16x16x32_bf16 v[92:95], v[36:39], a[160:163], v[92:95]\n"
  "s_waitcnt lgkmcnt(5)\n"
  "v_mfma_f32_16x16x32_bf16 v[72:75], a[20:23], v[40:43], v[72:75]\n"
  "ds_read_b128 v[32:35], v10 offset:16384\n"
  "s_waitcnt lgkmcnt(5)\n"
  "v_mfma_f32_16x16x32_bf16 v[76:79], a[20:23], v[44:47], v[76:79]\n"
  "ds_read_b128 v[36:39], v10 offset:18432\n"
  "v_mfma_f32_16x16x32_bf16 v[80:83], a[92:95], v[40:43], v[80:83]\n"
  "v_mfma_f32_16x16x32_bf16 v[84:87], a[92:95], v[44:47], v[84:87]\n"
  "v_mfma_f32_16x16x32_bf16 v[88:91], v[40:43], a[164:167], v[88:91]\n"
  "v_mfma_f32_16x16x32_bf16 v[92:95], v[44:47], a[164:167], v[92:95]\n"
  "s_waitcnt lgkmcnt(5)\n"
  "v_mfma_f32_16x16x32_bf16 v[72:75], a[24:27], v[16:19], v[72:75]\n"
  "ds_read_b128 v[40:43], v11 offset:16384\n"
  "s_waitcnt lgkmcnt(5)\n"
  "v_mfma_f32_16x16x32_bf16 v[76:79], a[24:27], v[20:23], v[76:79]\n"
  "ds_read_b128 v[44:47], v11 offset:18432\n"
  "v_mfma_f32_16x16x32_bf16 v[80:83], a[96:99], v[16:19], v[80:83]\n"
  "v_mfma_f32_16x16x32_bf16 v[84:87], a[96:99], v[20:23], v[84:87]\n"
  "s_add_u32 m0, s58, 0\n"
  "s_nop 0\n"
  "global_load_lds_dwordx4 v3, s[42:43] offset:0\n"
  "v_mfma_f32_16x16x32_bf16 v[88:91], v[16:19], a[168:171], v[88:91]\n"
  "v_mfma_f32_16x16x32_bf16 v[92:95], v[20:23], a[168:171], v[92:95]\n"
  "s_waitcnt lgkmcnt(5)\n"
  "v_mfma_f32_16x16x32_bf16 v[72:75], a[28:31], v[24:27], v[72:75]\n"
  "ds_read_b128 v[16:19], v10 offset:20480\n"
  "s_waitcnt lgkmcnt(5)\n"
  "v_mfma_f32_16x16x32_bf16 v[76:79], a[28:31], v[28:31], v[76:79]\n"
  "ds_read_b128 v[20:23], v10 offset:22528\n"
  "v_mfma_f32_16x16x32_bf16 v[80:83], a[100:103], v[24:27], v[80:83]\n"
  "v_mfma_f32_16x16x32_bf16 v[84:87], a[100:103], v[28:31], v[84:87]\n"
  "s_add_u32 m0, s58, 3968\n"
  "s_nop 0\n"
  "global_load_lds_dwordx4 v3, s[42:43] offset:128\n"
  "v_mfma_f32_16x16x32_bf16 v[88:91], v[24:27], a[172:175], v[88:91]\n"
  "v_mfma_f32_16x16x32_bf16 v[92:95], v[28:31], a[172:175], v[92:95]\n"
  "s_waitcnt lgkmcnt(5)\n"
  "v_mfma_f32_16x16x32_bf16 v[72:75], a[32:35], v[32:35], v[72:75]\n"
  "ds_read_b128 v[24:27], v11 offset:20480\n"
  "s_waitcnt lgkmcnt(5)\n"
  "v_mfma_f32_16x16x32_bf16 v[76:79], a[32:35], v[36:39], v[76:79]\n"
  "ds_read_b128 v[28:31], v11 offset:22528\n"
  "v_mfma_f32_16x16x32_bf16 v[80:83], a[104:107], v[32:35], v[80:83]\n"
  "v_mfma_f32_16x16x32_bf16 v[84:87], a[104:107], v[36:39], v[84:87]\n"
  "s_add_u32 m0, s58, 7936\n"
  "s_nop 0\n"
  "global_load_lds_dwordx4 v3, s[42:43] offset:256\n"
  "v_mfma_f32_16x16x32_bf16 v[88:91], v[32:35], a[176:179], v[88:91]\n"
  "v_mfma_f32_16x16x32_bf16 v[92:95], v[36:39], a[176:179], v[92:95]\n"
  "s_waitcnt lgkmcnt(5)\n"
  "v_mfma_f32_16x16x32_bf16 v[72:75], a[36:39], v[40:43], v[72:75]\n"
  "ds_read_b128 v[32:35], v10 offset:24576\n"
  "s_waitcnt lgkmcnt(5)\n"
  "v_mfma_f32_16x16x32_bf16 v[76:79], a[36:39], v[44:47], v[76:79]\n"
  "ds_read_b128 v[36:39], v10 offset:26624\n"
  "v_mfma_f32_16x16x32_bf16 v[80:83], a[108:111], v[40:43], v[80:83]\n"
  "v_mfma_f32_16x16x32_bf16 v[84:87], a[108:111], v[44:47], v[84:87]\n"
  "s_add_u32 m0, s58, 11904\n"
  "s_nop 0\n"
  "global_load_lds_dwordx4 v3, s[42:43] offset:384\n"
  "v_mfma_f32_16x16x32_bf16 v[88:91], v[40:43], a[180:183], v[88:91]\n"
  "v_mfma_f32_16x16x32_bf16 v[92:95], v[44:47], a[180:183], v[92:95]\n"
  "s_waitcnt lgkmcnt(5)\n"
  "v_mfma_f32_16x16x32_bf16 v[72:75], a[40:43], v[16:19], v[72:75]\n"
  "ds_read_b128 v[40:43], v11 offset:24576\n"
  "s_waitcnt lgkmcnt(5)\n"
  "v_mfma_f32_16x16x32_bf16 v[76:79], a[40:43], v[20:23], v[76:79]\n"
  "ds_read_b128 v[44:47], v11 offset:26624\n"
  "v_mfma_f32_16x16x32_bf16 v[80:83], a[112:115], v[16:19], v[80:83]\n"
  "v_mfma_f32_16x16x32_bf16 v[84:87], a[112:115], v[20:23], v[84:87]\n"
  "s_add_u32 m0, s58, 15872\n"
  "s_nop 0\n"
  "global_load_lds_dwordx4 v3, s[42:43] offset:512\n"
  "v_mfma_f32_16x16x32_bf16 v[88:91], v[16:19], a[184:187], v[88:91]\n"
  "v_mfma_f32_16x16x32_bf16 v[92:95], v[20:23], a[184:187], v[92:95]\n"
  "s_waitcnt lgkmcnt(5)\n"
  "v_mfma_f32_16x16x32_bf16 v[72:75], a[44:47], v[24:27], v[72:75]\n"
  "ds_read_b128 v[16:19], v10 offset:28672\n"
  "s_waitcnt lgkmcnt(5)\n"
  "v_mfma_f32_16x16x32_bf16 v[76:79], a[44:47], v[28:31], v[76:79]\n"
  "ds_read_b128 v[20:23], v10 offset:30720\n"
  "v_mfma_f32_16x16x32_bf16 v[80:83], a[116:119], v[24:27], v[80:83]\n"
  "v_mfma_f32_16x16x32_bf16 v[84:87], a[116:119], v[28:31], v[84:87]\n"
  "s_add_u32 m0, s58, 19840\n"
  "s_nop 0\n"
  "global_load_lds_dwordx4 v3, s[42:43] offset:640\n"
  "v_mfma_f32_16x16x32_bf16 v[88:91], v[24:27], a[188:191], v[88:91]\n"
  "v_mfma_f32_16x16x32_bf16 v[92:95], v[28:31], a[188:191], v[92:95]\n"
  "s_waitcnt lgkmcnt(5)\n"
  "v_mfma_f32_16x16x32_bf16 v[72:75], a[48:51], v[32:35], v[72:75]\n"
  "ds_read_b128 v[24:27], v11 offset:28672\n"
  "s_waitcnt lgkmcnt(5)\n"
  "v_mfma_f32_16x16x32_bf16 v[76:79], a[48:51], v[36:39], v[76:79]\n"
  "ds_read_b128 v[28:31], v11 offset:30720\n"
  "v_mfma_f32_16x16x32_bf16 v[80:83], a[120:123], v[32:35], v[80:83]\n"
  "v_mfma_f32_16x16x32_bf16 v[84:87], a[120:123], v[36:39], v[84:87]\n"
  "s_add_u32 m0, s58, 23808\n"
  "s_nop 0\n"
  "global_load_lds_dwordx4 v3, s[42:43] offset:768\n"
  "v_mfma_f32_16x16x32_bf16 v[88:91], v[32:35], a[192:195], v[88:91]\n"
  "v_mfma_f32_16x16x32_bf16 v[92:95], v[36:39], a[192:195], v[92:95]\n"
  "s_waitcnt lgkmcnt(5)\n"
  "v_mfma_f32_16x16x32_bf16 v[72:75], a[52:55], v[40:43], v[72:75]\n"
  "ds_read_b128 v[32:35], v10 offset:32768\n"
  "s_waitcnt lgkmcnt(5)\n"
  "v_mfma_f32_16x16x32_bf16 v[76:79], a[52:55], v[44:47], v[76:79]\n"
  "ds_read_b128 v[36:39], v10 offset:34816\n"
  "v_mfma_f32_16x16x32_bf16 v[80:83], a[124:127], v[40:43], v[80:83]\n"
  "v_mfma_f32_16x16x32_bf16 v[84:87], a[124:127], v[44:47], v[84:87]\n"
  "s_add_u32 m0, s58, 27776\n"
  "s_nop 0\n"
  "global_load_lds_dwordx4 v3, s[42:43] offset:896\n"
  "v_mfma_f32_16x16x32_bf16 v[88:91], v[40:43], a[196:199], v[88:91]\n"
  "v_mfma_f32_16x16x32_bf16 v[92:95], v[44:47], a[196:199], v[92:95]\n"
  "s_waitcnt lgkmcnt(5)\n"
  "v_mfma_f32_16x16x32_bf16 v[72:75], a[56:59], v[16:19], v[72:75]\n"
  "ds_read_b128 v[40:43], v11 offset:32768\n"
  "s_waitcnt lgkmcnt(5)\n"
  "v_mfma_f32_16x16x32_bf16 v[76:79], a[56:59], v[20:23], v[76:79]\n"
  "ds_read_b128 v[44:47], v11 offset:34816\n"
  "v_mfma_f32_16x16x32_bf16 v[80:83], a[128:131], v[16:19], v[80:83]\n"
  "v_mfma_f32_16x16x32_bf16 v[84:87], a[128:131], v[20:23], v[84:87]\n"
  "s_add_u32 m0, s58, 31744\n"
  "s_nop 0\n"
  "global_load_lds_dwordx4 v3, s[42:43] offset:1024\n"
  "v_mfma_f32_16x16x32_bf16 v[88:91], v[16:19], a[200:203], v[88:91]\n"
  "v_mfma_f32_16x16x32_bf16 v[92:95], v[20:23], a[200:203], v[92:95]\n"
  "s_waitcnt vmcnt(9)\n"
  "s_barrier\n"
  "s_waitcnt lgkmcnt(5)\n"
  "v_mfma_f32_16x16x32_bf16 v[72:75], a[60:63], v[24:27], v[72:75]\n"
  "ds_read_b128 v[16:19], v12 offset:0\n"
  "s_waitcnt lgkmcnt(5)\n"
  "v_mfma_f32_16x16x32_bf16 v[76:79], a[60:63], v[28:31], v[76:79]\n"
  "ds_read_b128 v[20:23], v12 offset:2048\n"
  "v_mfma_f32_16x16x32_bf16 v[80:83], a[132:135], v[24:27], v[80:83]\n"
  "s_bitcmp1_b64 s[4:5], s56\n"
  "s_cselect_b32 s74, 8192, 0\n"
  "v_mfma_f32_16x16x32_bf16 v[84:87], a[132:135], v[28:31], v[84:87]\n"
  "s_lshl_b32 s75, s56, 21\n"
  "s_add_u32 s74, s74, s75\n"
  "v_mfma_f32_16x16x32_bf16 v[88:91], v[24:27], a[204:207], v[88:91]\n"
  "s_add_u32 s74, s74, 20971520\n"
  "s_xor_b32 s75, s74, s44\n"
  "v_mfma_f32_16x16x32_bf16 v[92:95], v[28:31], a[204:207], v[92:95]\n"
  "s_add_u32 s60, s36, s75\n"
  "s_addc_u32 s61, s37, 0\n"
  "s_waitcnt lgkmcnt(5)\n"
  "v_mfma_f32_16x16x32_bf16 v[72:75], a[64:67], v[32:35], v[72:75]\n"
  "ds_read_b128 v[24:27], v13 offset:0\n"
  "s_waitcnt lgkmcnt(5)\n"
  "v_mfma_f32_16x16x32_bf16 v[76:79], a[64:67], v[36:39], v[76:79]\n"
  "ds_read_b128 v[28:31], v13 offset:2048\n"
  "v_mfma_f32_16x16x32_bf16 v[80:83], a[136:139], v[32:35], v[80:83]\n"
  "s_xor_b32 s75, s74, s45\n"
  "s_add_u32 s62, s36, s75\n"
  "v_mfma_f32_16x16x32_bf16 v[84:87], a[136:139], v[36:39], v[84:87]\n"
  "s_addc_u32 s63, s37, 0\n"
  "v_mfma_f32_16x16x32_bf16 v[88:91], v[32:35], a[208:211], v[88:91]\n"
  "v_mfma_f32_16x16x32_bf16 v[92:95], v[36:39], a[208:211], v[92:95]\n"
  "s_waitcnt lgkmcnt(5)\n"
  "v_mfma_f32_16x16x32_bf16 v[72:75], a[68:71], v[40:43], v[72:75]\n"
  "ds_read_b128 v[32:35], v12 offset:4096\n"
  "s_waitcnt lgkmcnt(5)\n"
  "v_mfma_f32_16x16x32_bf16 v[76:79], a[68:71], v[44:47], v[76:79]\n"
  "ds_read_b128 v[36:39], v12 offset:6144\n"
  "v_mfma_f32_16x16x32_bf16 v[80:83], a[140:143], v[40:43], v[80:83]\n"
  "v_mfma_f32_16x16x32_bf16 v[84:87], a[140:143], v[44:47], v[84:87]\n"
  "v_mfma_f32_16x16x32_bf16 v[88:91], v[40:43], a[212:215], v[88:91]\n"
  "v_mfma_f32_16x16x32_bf16 v[92:95], v[44:47], a[212:215], v[92:95]\n"
  "s_add_u32 s56, s56, 1\n"
  "v_mov_b64 v[10:11], v[12:13]\n"
  "s_waitcnt lgkmcnt(5)\n"
  "v_mfma_f32_16x16x32_bf16 v[48:51], a[0:3], v[16:19], 0\n"
  "ds_read_b128 v[40:43], v11 offset:4096\n"
  "s_waitcnt lgkmcnt(5)\n"
  "v_mfma_f32_16x16x32_bf16 v[52:55], a[0:3], v[20:23], 0\n"
  "ds_read_b128 v[44:47], v11 offset:6144\n"
  "v_mfma_f32_16x16x32_bf16 v[56:59], a[72:75], v[16:19], 0\n"
  "s_add_u32 s57, s57, 36864\n"
  "s_cmp_eq_u32 s57, 159744\n"
  "v_mfma_f32_16x16x32_bf16 v[60:63], a[72:75], v[20:23], 0\n"
  "s_cselect_b32 s57, 12288, s57\n"
  "v_mul_f32 v72, v8, v72\n"
  "v_mfma_f32_16x16x32_bf16 v[64:67], v[16:19], a[144:147], 0\n"
  "v_mul_f32 v73, v8, v73\n"
  "v_mul_f32 v74, v8, v74\n"
  "v_mfma_f32_16x16x32_bf16 v[68:71], v[20:23], a[144:147], 0\n"
  "v_mul_f32 v75, v8, v75\n"
  "v_cvt_pk_bf16_f32 v96, v72, v73\n"
  "s_waitcnt lgkmcnt(5)\n"
  "v_mfma_f32_16x16x32_bf16 v[48:51], a[4:7], v[24:27], v[48:51]\n"
  "ds_read_b128 v[16:19], v10 offset:8192\n"
  "s_waitcnt lgkmcnt(5)\n"
  "v_mfma_f32_16x16x32_bf16 v[52:55], a[4:7], v[28:31], v[52:55]\n"
  "ds_read_b128 v[20:23], v10 offset:10240\n"
  "v_mfma_f32_16x16x32_bf16 v[56:59], a[76:79], v[24:27], v[56:59]\n"
  "v_cvt_pk_bf16_f32 v97, v74, v75\n"
  "v_mul_f32 v80, v8, v80\n"
  "v_mfma_f32_16x16x32_bf16 v[60:63], a[76:79], v[28:31], v[60:63]\n"
  "v_mul_f32 v81, v8, v81\n"
  "v_mul_f32 v82, v8, v82\n"
  "v_mfma_f32_16x16x32_bf16 v[64:67], v[24:27], a[148:151], v[64:67]\n"
  "v_mul_f32 v83, v8, v83\n"
  "v_cvt_pk_bf16_f32 v98, v80, v81\n"
  "v_mfma_f32_16x16x32_bf16 v[68:71], v[28:31], a[148:151], v[68:71]\n"
  "v_cvt_pk_bf16_f32 v99, v82, v83\n"
  "global_store_dwordx4 v0, v[96:99], s[60:61] offset:0 sc1\n"
  "s_waitcnt lgkmcnt(5)\n"
  "v_mfma_f32_16x16x32_bf16 v[48:51], a[8:11], v[32:35], v[48:51]\n"
  "ds_read_b128 v[24:27], v11 offset:8192\n"
  "s_waitcnt lgkmcnt(5)\n"
  "v_mfma_f32_16x16x32_bf16 v[52:55], a[8:11], v[36:39], v[52:55]\n"
  "ds_read_b128 v[28:31], v11 offset:10240\n"
  "v_mfma_f32_16x16x32_bf16 v[56:59], a[80:83], v[32:35], v[56:59]\n"
  "v_mul_f32 v76, v8, v76\n"
  "v_mul_f32 v77, v8, v77\n"
  "v_mfma_f32_16x16x32_bf16 v[60:63], a[80:83], v[36:39], v[60:63]\n"
  "v_mul_f32 v78, v8, v78\n"
  "v_mul_f32 v79, v8, v79\n"
  "v_mfma_f32_16x16x32_bf16 v[64:67], v[32:35], a[152:155], v[64:67]\n"
  "v_cvt_pk_bf16_f32 v100, v76, v77\n"
  "v_cvt_pk_bf16_f32 v101, v78, v79\n"
  "v_mfma_f32_16x16x32_bf16 v[68:71], v[36:39], a[152:155], v[68:71]\n"
  "v_mul_f32 v84, v8, v84\n"
  "v_mul_f32 v85, v8, v85\n"
  "s_waitcnt lgkmcnt(5)\n"
  "v_mfma_f32_16x16x32_bf16 v[48:51], a[12:15], v[40:43], v[48:51]\n"
  "ds_read_b128 v[32:35], v10 offset:12288\n"
  "s_waitcnt lgkmcnt(5)\n"
  "v_mfma_f32_16x16x32_bf16 v[52:55], a[12:15], v[44:47], v[52:55]\n"
  "ds_read_b128 v[36:39], v10 offset:14336\n"
  "v_mfma_f32_16x16x32_bf16 v[56:59], a[84:87], v[40:43], v[56:59]\n"
  "v_mul_f32 v86, v8, v86\n"
  "v_mul_f32 v87, v8, v87\n"
  "v_mfma_f32_16x16x32_bf16 v[60:63], a[84:87], v[44:47], v[60:63]\n"
  "v_cvt_pk_bf16_f32 v102, v84, v85\n"
  "v_cvt_pk_bf16_f32 v103, v86, v87\n"
  "v_mfma_f32_16x16x32_bf16 v[64:67], v[40:43], a[156:159], v[64:67]\n"
  "global_store_dwordx4 v0, v[100:103], s[60:61] offset:1024 sc1\n"
  "v_cvt_pk_bf16_f32 v104, v88, v89\n"
  "v_mfma_f32_16x16x32_bf16 v[68:71], v[44:47], a[156:159], v[68:71]\n"
  "v_cvt_pk_bf16_f32 v105, v90, v91\n"
  "v_cvt_pk_bf16_f32 v106, v92, v93\n"
  "s_waitcnt lgkmcnt(5)\n"
  "v_mfma_f32_16x16x32_bf16 v[48:51], a[16:19], v[16:19], v[48:51]\n"
  "ds_read_b128 v[40:43], v11 offset:12288\n"
  "s_waitcnt lgkmcnt(5)\n"
  "v_mfma_f32_16x16x32_bf16 v[52:55], a[16:19], v[20:23], v[52:55]\n"
  "ds_read_b128 v[44:47], v11 offset:14336\n"
  "v_mfma_f32_16x16x32_bf16 v[56:59], a[88:91], v[16:19], v[56:59]\n"
  "v_cvt_pk_bf16_f32 v107, v94, v95\n"
  "global_store_dwordx4 v0, v[104:107], s[62:63] offset:0 sc1\n"
  "v_mfma_f32_16x16x32_bf16 v[60:63], a[88:91], v[20:23], v[60:63]\n"
  "v_add_u32 v12, s57, v9\n"
  "v_xor_b32 v13, 64, v12\n"
  "v_mfma_f32_16x16x32_bf16 v[64:67], v[16:19], a[160:163], v[64:67]\n"
  "s_add_u32 s58, s57, 36864\n"
  "s_cmp_eq_u32 s57, 122880\n"
  "v_mfma_f32_16x16x32_bf16 v[68:71], v[20:23], a[160:163], v[68:71]\n"
  "s_cselect_b32 s58, 12288, s58\n"
  "s_add_u32 s58, s58, s80\n"
  "s_waitcnt lgkmcnt(5)\n"
  "v_mfma_f32_16x16x32_bf16 v[48:51], a[20:23], v[24:27], v[48:51]\n"
  "ds_read_b128 v[16:19], v10 offset:16384\n"
  "s_waitcnt lgkmcnt(5)\n"
  "v_mfma_f32_16x16x32_bf16 v[52:55], a[20:23], v[28:31], v[52:55]\n"
  "ds_read_b128 v[20:23], v10 offset:18432\n"
  "v_mfma_f32_16x16x32_bf16 v[56:59], a[92:95], v[24:27], v[56:59]\n"
  "s_cmp_lt_u32 s56, 62\n"
  "s_cselect_b32 s74, 36864, 0\n"
  "v_mfma_f32_16x16x32_bf16 v[60:63], a[92:95], v[28:31], v[60:63]\n"
  "s_add_u32 s42, s42, s74\n"
  "s_addc_u32 s43, s43, 0\n"
  "v_mfma_f32_16x16x32_bf16 v[64:67], v[24:27], a[164:167], v[64:67]\n"
  "v_mfma_f32_16x16x32_bf16 v[68:71], v[28:31], a[164:167], v[68:71]\n"
  "s_waitcnt lgkmcnt(5)\n"
  "v_mfma_f32_16x16x32_bf16 v[48:51], a[24:27], v[32:35], v[48:51]\n"
  "ds_read_b128 v[24:27], v11 offset:16384\n"
  "s_waitcnt lgkmcnt(5)\n"
  "v_mfma_f32_16x16x32_bf16 v[52:55], a[24:27], v[36:39], v[52:55]\n"
  "ds_read_b128 v[28:31], v11 offset:18432\n"
  "v_mfma_f32_16x16x32_bf16 v[56:59], a[96:99], v[32:35], v[56:59]\n"
  "v_mfma_f32_16x16x32_bf16 v[60:63], a[96:99], v[36:39], v[60:63]\n"
  "s_add_u32 m0, s58, 0\n"
  "s_nop 0\n"
  "global_load_lds_dwordx4 v3, s[42:43] offset:0\n"
  "v_mfma_f32_16x16x32_bf16 v[64:67], v[32:35], a[168:171], v[64:67]\n"
  "v_mfma_f32_16x16x32_bf16 v[68:71], v[36:39], a[168:171], v[68:71]\n"
  "s_waitcnt lgkmcnt(5)\n"
  "v_mfma_f32_16x16x32_bf16 v[48:51], a[28:31], v[40:43], v[48:51]\n"
  "ds_read_b128 v[32:35], v10 offset:20480\n"
  "s_waitcnt lgkmcnt(5)\n"
  "v_mfma_f32_16x16x32_bf16 v[52:55], a[28:31], v[44:47], v[52:55]\n"
  "ds_read_b128 v[36:39], v10 offset:22528\n"
  "v_mfma_f32_16x16x32_bf16 v[56:59], a[100:103], v[40:43], v[56:59]\n"
  "v_mfma_f32_16x16x32_bf16 v[60:63], a[100:103], v[44:47], v[60:63]\n"
  "s_add_u32 m0, s58, 3968\n"
  "s_nop 0\n"
  "global_load_lds_dwordx4 v3, s[42:43] offset:128\n"
  "v_mfma_f32_16x16x32_bf16 v[64:67], v[40:43], a[172:175], v[64:67]\n"
  "v_mfma_f32_16x16x32_bf16 v[68:71], v[44:47], a[172:175], v[68:71]\n"
  "s_waitcnt lgkmcnt(5)\n"
  "v_mfma_f32_16x16x32_bf16 v[48:51], a[32:35], v[16:19], v[48:51]\n"
  "ds_read_b128 v[40:43], v11 offset:20480\n"
  "s_waitcnt lgkmcnt(5)\n"
  "v_mfma_f32_16x16x32_bf16 v[52:55], a[32:35], v[20:23], v[52:55]\n"
  "ds_read_b128 v[44:47], v11 offset:22528\n"
  "v_mfma_f32_16x16x32_bf16 v[56:59], a[104:107], v[16:19], v[56:59]\n"
  "v_mfma_f32_16x16x32_bf16 v[60:63], a[104:107], v[20:23], v[60:63]\n"
  "s_add_u32 m0, s58, 7936\n"
  "s_nop 0\n"
  "global_load_lds_dwordx4 v3, s[42:43] offset:256\n"
  "v_mfma_f32_16x16x32_bf16 v[64:67], v[16:19], a[176:179], v[64:67]\n"
  "v_mfma_f32_16x16x32_bf16 v[68:71], v[20:23], a[176:179], v[68:71]\n"
  "s_waitcnt lgkmcnt(5)\n"
  "v_mfma_f32_16x16x32_bf16 v[48:51], a[36:39], v[24:27], v[48:51]\n"
  "ds_read_b128 v[16:19], v10 offset:24576\n"
  "s_waitcnt lgkmcnt(5)\n"
  "v_mfma_f32_16x16x32_bf16 v[52:55], a[36:39], v[28:31], v[52:55]\n"
  "ds_read_b128 v[20:23], v10 offset:26624\n"
  "v_mfma_f32_16x16x32_bf16 v[56:59], a[108:111], v[24:27], v[56:59]\n"
  "v_mfma_f32_16x16x32_bf16 v[60:63], a[108:111], v[28:31], v[60:63]\n"
  "s_add_u32 m0, s58, 11904\n"
  "s_nop 0\n"
  "global_load_lds_dwordx4 v3, s[42:43] offset:384\n"
  "v_mfma_f32_16x16x32_bf16 v[64:67], v[24:27], a[180:183], v[64:67]\n"
  "v_mfma_f32_16x16x32_bf16 v[68:71], v[28:31], a[180:183], v[68:71]\n"
  "s_waitcnt lgkmcnt(5)\n"
  "v_mfma_f32_16x16x32_bf16 v[48:51], a[40:43], v[32:35], v[48:51]\n"
  "ds_read_b128 v[24:27], v11 offset:24576\n"
  "s_waitcnt lgkmcnt(5)\n"
  "v_mfma_f32_16x16x32_bf16 v[52:55], a[40:43], v[36:39], v[52:55]\n"
  "ds_read_b128 v[28:31], v11 offset:26624\n"
  "v_mfma_f32_16x16x32_bf16 v[56:59], a[112:115], v[32:35], v[56:59]\n"
  "v_mfma_f32_16x16x32_bf16 v[60:63], a[112:115], v[36:39], v[60:63]\n"
  "s_add_u32 m0, s58, 15872\n"
  "s_nop 0\n"
  "global_load_lds_dwordx4 v3, s[42:43] offset:512\n"
  "v_mfma_f32_16x16x32_bf16 v[64:67], v[32:35], a[184:187], v[64:67]\n"
  "v_mfma_f32_16x16x32_bf16 v[68:71], v[36:39], a[184:187], v[68:71]\n"
  "s_waitcnt lgkmcnt(5)\n"
  "v_mfma_f32_16x16x32_bf16 v[48:51], a[44:47], v[40:43], v[48:51]\n"
  "ds_read_b128 v[32:35], v10 offset:28672\n"
  "s_waitcnt lgkmcnt(5)\n"
  "v_mfma_f32_16x16x32_bf16 v[52:55], a[44:47], v[44:47], v[52:55]\n"
  "ds_read_b128 v[36:39], v10 offset:30720\n"
  "v_mfma_f32_16x16x32_bf16 v[56:59], a[116:119], v[40:43], v[56:59]\n"
  "v_mfma_f32_16x16x32_bf16 v[60:63], a[116:119], v[44:47], v[60:63]\n"
  "s_add_u32 m0, s58, 19840\n"
  "s_nop 0\n"
  "global_load_lds_dwordx4 v3, s[42:43] offset:640\n"
  "v_mfma_f32_16x16x32_bf16 v[64:67], v[40:43], a[188:191], v[64:67]\n"
  "v_mfma_f32_16x16x32_bf16 v[68:71], v[44:47], a[188:191], v[68:71]\n"
  "s_waitcnt lgkmcnt(5)\n"
  "v_mfma_f32_16x16x32_bf16 v[48:51], a[48:51], v[16:19], v[48:51]\n"
  "ds_read_b128 v[40:43], v11 offset:28672\n"
  "s_waitcnt lgkmcnt(5)\n"
  "v_mfma_f32_16x16x32_bf16 v[52:55], a[48:51], v[20:23], v[52:55]\n"
  "ds_read_b128 v[44:47], v11 offset:30720\n"
  "v_mfma_f32_16x16x32_bf16 v[56:59], a[120:123], v[16:19], v[56:59]\n"
  "v_mfma_f32_16x16x32_bf16 v[60:63], a[120:123], v[20:23], v[60:63]\n"
  "s_add_u32 m0, s58, 23808\n"
  "s_nop 0\n"
  "global_load_lds_dwordx4 v3, s[42:43] offset:768\n"
  "v_mfma_f32_16x16x32_bf16 v[64:67], v[16:19], a[192:195], v[64:67]\n"
  "v_mfma_f32_16x16x32_bf16 v[68:71], v[20:23], a[192:195], v[68:71]\n"
  "s_waitcnt lgkmcnt(5)\n"
  "v_mfma_f32_16x16x32_bf16 v[48:51], a[52:55], v[24:27], v[48:51]\n"
  "ds_read_b128 v[16:19], v10 offset:32768\n"
  "s_waitcnt lgkmcnt(5)\n"
  "v_mfma_f32_16x16x32_bf16 v[52:55], a[52:55], v[28:31], v[52:55]\n"
  "ds_read_b128 v[20:23], v10 offset:34816\n"
  "v_mfma_f32_16x16x32_bf16 v[56:59], a[124:127], v[24:27], v[56:59]\n"
  "v_mfma_f32_16x16x32_bf16 v[60:63], a[124:127], v[28:31], v[60:63]\n"
  "s_add_u32 m0, s58, 27776\n"
  "s_nop 0\n"
  "global_load_lds_dwordx4 v3, s[42:43] offset:896\n"
  "v_mfma_f32_16x16x32_bf16 v[64:67], v[24:27], a[196:199], v[64:67]\n"
  "v_mfma_f32_16x16x32_bf16 v[68:71], v[28:31], a[196:199], v[68:71]\n"
  "s_waitcnt lgkmcnt(5)\n"
  "v_mfma_f32_16x16x32_bf16 v[48:51], a[56:59], v[32:35], v[48:51]\n"
  "ds_read_b128 v[24:27], v11 offset:32768\n"
  "s_waitcnt lgkmcnt(5)\n"
  "v_mfma_f32_16x16x32_bf16 v[52:55], a[56:59], v[36:39], v[52:55]\n"
  "ds_read_b128 v[28:31], v11 offset:34816\n"
  "v_mfma_f32_16x16x32_bf16 v[56:59], a[128:131], v[32:35], v[56:59]\n"
  "v_mfma_f32_16x16x32_bf16 v[60:63], a[128:131], v[36:39], v[60:63]\n"
  "s_add_u32 m0, s58, 31744\n"
  "s_nop 0\n"
  "global_load_lds_dwordx4 v3, s[42:43] offset:1024\n"
  "v_mfma_f32_16x16x32_bf16 v[64:67], v[32:35], a[200:203], v[64:67]\n"
  "v_mfma_f32_16x16x32_bf16 v[68:71], v[36:39], a[200:203], v[68:71]\n"
  "s_waitcnt vmcnt(9)\n"
  "s_barrier\n"
  "s_waitcnt lgkmcnt(5)\n"
  "v_mfma_f32_16x16x32_bf16 v[48:51], a[60:63], v[40:43], v[48:51]\n"
  "ds_read_b128 v[32:35], v12 offset:0\n"
  "s_waitcnt lgkmcnt(5)\n"
  "v_mfma_f32_16x16x32_bf16 v[52:55], a[60:63], v[44:47], v[52:55]\n"
  "ds_read_b128 v[36:39], v12 offset:2048\n"
  "v_mfma_f32_16x16x32_bf16 v[56:59], a[132:135], v[40:43], v[56:59]\n"
  "v_mov_b32 v6, 0\n"
  "v_mov_b32 v7, s56\n"
  "s_cmp_eq_u32 s47, 3\n"
  "s_cselect_b64 exec, 1, 0\n"
  "global_store_dword v6, v7, s[64:65] sc1\n"
  "s_mov_b64 exec, -1\n"
  "v_mfma_f32_16x16x32_bf16 v[60:63], a[132:135], v[44:47], v[60:63]\n"
  "s_bitcmp1_b64 s[4:5], s56\n"
  "s_cselect_b32 s74, 8192, 0\n"
  "v_mfma_f32_16x16x32_bf16 v[64:67], v[40:43], a[204:207], v[64:67]\n"
  "s_lshl_b32 s75, s56, 21\n"
  "s_add_u32 s74, s74, s75\n"
  "v_mfma_f32_16x16x32_bf16 v[68:71], v[44:47], a[204:207], v[68:71]\n"
  "s_add_u32 s74, s74, 20971520\n"
  "s_xor_b32 s75, s74, s44\n"
  "s_waitcnt lgkmcnt(5)\n"
  "v_mfma_f32_16x16x32_bf16 v[48:51], a[64:67], v[16:19], v[48:51]\n"
  "ds_read_b128 v[40:43], v13 offset:0\n"
  "s_waitcnt lgkmcnt(5)\n"
  "v_mfma_f32_16x16x32_bf16 v[52:55], a[64:67], v[20:23], v[52:55]\n"
  "ds_read_b128 v[44:47], v13 offset:2048\n"
  "v_mfma_f32_16x16x32_bf16 v[56:59], a[136:139], v[16:19], v[56:59]\n"
  "s_add_u32 s60, s36, s75\n"
  "s_addc_u32 s61, s37, 0\n"
  "v_mfma_f32_16x16x32_bf16 v[60:63], a[136:139], v[20:23], v[60:63]\n"
  "s_xor_b32 s75, s74, s45\n"
  "s_add_u32 s62, s36, s75\n"
  "v_mfma_f32_16x16x32_bf16 v[64:67], v[16:19], a[208:211], v[64:67]\n"
  "s_addc_u32 s63, s37, 0\n"
  "v_mfma_f32_16x16x32_bf16 v[68:71], v[20:23], a[208:211], v[68:71]\n"
  "s_waitcnt lgkmcnt(5)\n"
  "v_mfma_f32_16x16x32_bf16 v[48:51], a[68:71], v[24:27], v[48:51]\n"
  "ds_read_b128 v[16:19], v12 offset:4096\n"
  "s_waitcnt lgkmcnt(5)\n"
  "v_mfma_f32_16x16x32_bf16 v[52:55], a[68:71], v[28:31], v[52:55]\n"
  "ds_read_b128 v[20:23], v12 offset:6144\n"
  "v_mfma_f32_16x16x32_bf16 v[56:59], a[140:143], v[24:27], v[56:59]\n"
  "v_mfma_f32_16x16x32_bf16 v[60:63], a[140:143], v[28:31], v[60:63]\n"
  "v_mfma_f32_16x16x32_bf16 v[64:67], v[24:27], a[212:215], v[64:67]\n"
  "v_mfma_f32_16x16x32_bf16 v[68:71], v[28:31], a[212:215], v[68:71]\n"
  "s_add_u32 s56, s56, 1\n"
  "PJL:\n"
  "v_mov_b64 v[10:11], v[12:13]\n"
  "s_waitcnt lgkmcnt(5)\n"
  "v_mfma_f32_16x16x32_bf16 v[72:75], a[0:3], v[32:35], 0\n"
  "ds_read_b128 v[24:27], v11 offset:4096\n"
  "s_waitcnt lgkmcnt(5)\n"
  "v_mfma_f32_16x16x32_bf16 v[76:79], a[0:3], v[36:39], 0\n"
  "ds_read_b128 v[28:31], v11 offset:6144\n"
  "v_mfma_f32_16x16x32_bf16 v[80:83], a[72:75], v[32:35], 0\n"
  "s_add_u32 s57, s57, 36864\n"
  "s_cmp_eq_u32 s57, 159744\n"
  "v_mfma_f32_16x16x32_bf16 v[84:87], a[72:75], v[36:39], 0\n"
  "s_cselect_b32 s57, 12288, s57\n"
  "v_mul_f32 v48, v8, v48\n"
  "v_mfma_f32_16x16x32_bf16 v[88:91], v[32:35], a[144:147], 0\n"
  "v_mul_f32 v49, v8, v49\n"
  "v_mul_f32 v50, v8, v50\n"
  "v_mfma_f32_16x16x32_bf16 v[92:95], v[36:39], a[144:147], 0\n"
  "v_mul_f32 v51, v8, v51\n"
  "v_cvt_pk_bf16_f32 v96, v48, v49\n"
  "s_waitcnt lgkmcnt(5)\n"
  "v_mfma_f32_16x16x32_bf16 v[72:75], a[4:7], v[40:43], v[72:75]\n"
  "ds_read_b128 v[32:35], v10 offset:8192\n"
  "s_waitcnt lgkmcnt(5)\n"
  "v_mfma_f32_16x16x32_bf16 v[76:79], a[4:7], v[44:47], v[76:79]\n"
  "ds_read_b128 v[36:39], v10 offset:10240\n"
  "v_mfma_f32_16x16x32_bf16 v[80:83], a[76:79], v[40:43], v[80:83]\n"
  "v_cvt_pk_bf16_f32 v97, v50, v51\n"
  "v_mul_f32 v56, v8, v56\n"
  "v_mfma_f32_16x16x32_bf16 v[84:87], a[76:79], v[44:47], v[84:87]\n"
  "v_mul_f32 v57, v8, v57\n"
  "v_mul_f32 v58, v8, v58\n"
  "v_mfma_f32_16x16x32_bf16 v[88:91], v[40:43], a[148:151], v[88:91]\n"
  "v_mul_f32 v59, v8, v59\n"
  "v_cvt_pk_bf16_f32 v98, v56, v57\n"
  "v_mfma_f32_16x16x32_bf16 v[92:95], v[44:47], a[148:151], v[92:95]\n"
  "v_cvt_pk_bf16_f32 v99, v58, v59\n"
  "global_store_dwordx4 v0, v[96:99], s[60:61] offset:0 sc1\n"
  "s_waitcnt lgkmcnt(5)\n"
  "v_mfma_f32_16x16x32_bf16 v[72:75], a[8:11], v[16:19], v[72:75]\n"
  "ds_read_b128 v[40:43], v11 offset:8192\n"
  "s_waitcnt lgkmcnt(5)\n"
  "v_mfma_f32_16x16x32_bf16 v[76:79], a[8:11], v[20:23], v[76:79]\n"
  "ds_read_b128 v[44:47], v11 offset:10240\n"
  "v_mfma_f32_16x16x32_bf16 v[80:83], a[80:83], v[16:19], v[80:83]\n"
  "v_mul_f32 v52, v8, v52\n"
  "v_mul_f32 v53, v8, v53\n"
  "v_mfma_f32_16x16x32_bf16 v[84:87], a[80:83], v[20:23], v[84:87]\n"
  "v_mul_f32 v54, v8, v54\n"
  "v_mul_f32 v55, v8, v55\n"
  "v_mfma_f32_16x16x32_bf16 v[88:91], v[16:19], a[152:155], v[88:91]\n"
  "v_cvt_pk_bf16_f32 v100, v52, v53\n"
  "v_cvt_pk_bf16_f32 v101, v54, v55\n"
  "v_mfma_f32_16x16x32_bf16 v[92:95], v[20:23], a[152:155], v[92:95]\n"
  "v_mul_f32 v60, v8, v60\n"
  "v_mul_f32 v61, v8, v61\n"
  "s_waitcnt lgkmcnt(5)\n"
  "v_mfma_f32_16x16x32_bf16 v[72:75], a[12:15], v[24:27], v[72:75]\n"
  "ds_read_b128 v[16:19], v10 offset:12288\n"
  "s_waitcnt lgkmcnt(5)\n"
  "v_mfma_f32_16x16x32_bf16 v[76:79], a[12:15], v[28:31], v[76:79]\n"
  "ds_read_b128 v[20:23], v10 offset:14336\n"
  "v_mfma_f32_16x16x32_bf16 v[80:83], a[84:87], v[24:27], v[80:83]\n"
  "v_mul_f32 v62, v8, v62\n"
  "v_mul_f32 v63, v8, v63\n"
  "v_mfma_f32_16x16x32_bf16 v[84:87], a[84:87], v[28:31], v[84:87]\n"
  "v_cvt_pk_bf16_f32 v102, v60, v61\n"
  "v_cvt_pk_bf16_f32 v103, v62, v63\n"
  "v_mfma_f32_16x16x32_bf16 v[88:91], v[24:27], a[156:159], v[88:91]\n"
  "global_store_dwordx4 v0, v[100:103], s[60:61] offset:1024 sc1\n"
  "v_cvt_pk_bf16_f32 v104, v64, v65\n"
  "v_mfma_f32_16x16x32_bf16 v[92:95], v[28:31], a[156:159], v[92:95]\n"
  "v_cvt_pk_bf16_f32 v105, v66, v67\n"
  "v_cvt_pk_bf16_f32 v106, v68, v69\n"
  "s_waitcnt lgkmcnt(5)\n"
  "v_mfma_f32_16x16x32_bf16 v[72:75], a[16:19], v[32:35], v[72:75]\n"
  "ds_read_b128 v[24:27], v11 offset:12288\n"
  "s_waitcnt lgkmcnt(5)\n"
  "v_mfma_f32_16x16x32_bf16 v[76:79], a[16:19], v[36:39], v[76:79]\n"
  "ds_read_b128 v[28:31], v11 offset:14336\n"
  "v_mfma_f32_16x16x32_bf16 v[80:83], a[88:91], v[32:35], v[80:83]\n"
  "v_cvt_pk_bf16_f32 v107, v70, v71\n"
  "global_store_dwordx4 v0, v[104:107], s[62:63] offset:0 sc1\n"
  "v_mfma_f32_16x16x32_bf16 v[84:87], a[88:91], v[36:39], v[84:87]\n"
  "v_add_u32 v12, s57, v9\n"
  "v_xor_b32 v13, 64, v12\n"
  "v_mfma_f32_16x16x32_bf16 v[88:91], v[32:35], a[160:163], v[88:91]\n"
  "s_add_u32 s58, s57, 36864\n"
  "s_cmp_eq_u32 s57, 122880\n"
  "v_mfma_f32_16x16x32_bf16 v[92:95], v[36:39], a[160:163], v[92:95]\n"
  "s_cselect_b32 s58, 12288, s58\n"
  "s_add_u32 s58, s58, s80\n"
  "s_waitcnt lgkmcnt(5)\n"
  "v_mfma_f32_16x16x32_bf16 v[72:75], a[20:23], v[40:43], v[72:75]\n"
  "ds_read_b128 v[32:35], v10 offset:16384\n"
  "s_waitcnt lgkmcnt(5)\n"
  "v_mfma_f32_16x16x32_bf16 v[76:79], a[20:23], v[44:47], v[76:79]\n"
  "ds_read_b128 v[36:39], v10 offset:18432\n"
  "v_mfma_f32_16x16x32_bf16 v[80:83], a[92:95], v[40:43], v[80:83]\n"
  "s_cmp_lt_u32 s56, 62\n"
  "s_cselect_b32 s74, 36864, 0\n"
  "v_mfma_f32_16x16x32_bf16 v[84:87], a[92:95], v[44:47], v[84:87]\n"
  "s_add_u32 s42, s42, s74\n"
  "s_addc_u32 s43, s43, 0\n"
  "v_mfma_f32_16x16x32_bf16 v[88:91], v[40:43], a[164:167], v[88:91]\n"
  "v_mfma_f32_16x16x32_bf16 v[92:95], v[44:47], a[164:167], v[92:95]\n"
  "s_waitcnt lgkmcnt(5)\n"
  "v_mfma_f32_16x16x32_bf16 v[72:75], a[24:27], v[16:19], v[72:75]\n"
  "ds_read_b128 v[40:43], v11 offset:16384\n"
  "s_waitcnt lgkmcnt(5)\n"
  "v_mfma_f32_16x16x32_bf16 v[76:79], a[24:27], v[20:23], v[76:79]\n"
  "ds_read_b128 v[44:47], v11 offset:18432\n"
  "v_mfma_f32_16x16x32_bf16 v[80:83], a[96:99], v[16:19], v[80:83]\n"
  "v_mfma_f32_16x16x32_bf16 v[84:87], a[96:99], v[20:23], v[84:87]\n"
  "s_add_u32 m0, s58, 0\n"
  "s_nop 0\n"
  "global_load_lds_dwordx4 v3, s[42:43] offset:0\n"
  "v_mfma_f32_16x16x32_bf16 v[88:91], v[16:19], a[168:171], v[88:91]\n"
  "v_mfma_f32_16x16x32_bf16 v[92:95], v[20:23], a[168:171], v[92:95]\n"
  "s_waitcnt lgkmcnt(5)\n"
  "v_mfma_f32_16x16x32_bf16 v[72:75], a[28:31], v[24:27], v[72:75]\n"
  "ds_read_b128 v[16:19], v10 offset:20480\n"
  "s_waitcnt lgkmcnt(5)\n"
  "v_mfma_f32_16x16x32_bf16 v[76:79], a[28:31], v[28:31], v[76:79]\n"
  "ds_read_b128 v[20:23], v10 offset:22528\n"
  "v_mfma_f32_16x16x32_bf16 v[80:83], a[100:103], v[24:27], v[80:83]\n"
  "v_mfma_f32_16x16x32_bf16 v[84:87], a[100:103], v[28:31], v[84:87]\n"
  "s_add_u32 m0, s58, 3968\n"
  "s_nop 0\n"
  "global_load_lds_dwordx4 v3, s[42:43] offset:128\n"
  "v_mfma_f32_16x16x32_bf16 v[88:91], v[24:27], a[172:175], v[88:91]\n"
  "v_mfma_f32_16x16x32_bf16 v[92:95], v[28:31], a[172:175], v[92:95]\n"
  "s_waitcnt lgkmcnt(5)\n"
  "v_mfma_f32_16x16x32_bf16 v[72:75], a[32:35], v[32:35], v[72:75]\n"
  "ds_read_b128 v[24:27], v11 offset:20480\n"
  "s_waitcnt lgkmcnt(5)\n"
  "v_mfma_f32_16x16x32_bf16 v[76:79], a[32:35], v[36:39], v[76:79]\n"
  "ds_read_b128 v[28:31], v11 offset:22528\n"
  "v_mfma_f32_16x16x32_bf16 v[80:83], a[104:107], v[32:35], v[80:83]\n"
  "v_mfma_f32_16x16x32_bf16 v[84:87], a[104:107], v[36:39], v[84:87]\n"
  "s_add_u32 m0, s58, 7936\n"
  "s_nop 0\n"
  "global_load_lds_dwordx4 v3, s[42:43] offset:256\n"
  "v_mfma_f32_16x16x32_bf16 v[88:91], v[32:35], a[176:179], v[88:91]\n"
  "v_mfma_f32_16x16x32_bf16 v[92:95], v[36:39], a[176:179], v[92:95]\n"
  "s_waitcnt lgkmcnt(5)\n"
  "v_mfma_f32_16x16x32_bf16 v[72:75], a[36:39], v[40:43], v[72:75]\n"
  "ds_read_b128 v[32:35], v10 offset:24576\n"
  "s_waitcnt lgkmcnt(5)\n"
  "v_mfma_f32_16x16x32_bf16 v[76:79], a[36:39], v[44:47], v[76:79]\n"
  "ds_read_b128 v[36:39], v10 offset:26624\n"
  "v_mfma_f32_16x16x32_bf16 v[80:83], a[108:111], v[40:43], v[80:83]\n"
  "v_mfma_f32_16x16x32_bf16 v[84:87], a[108:111], v[44:47], v[84:87]\n"
  "s_add_u32 m0, s58, 11904\n"
  "s_nop 0\n"
  "global_load_lds_dwordx4 v3, s[42:43] offset:384\n"
  "v_mfma_f32_16x16x32_bf16 v[88:91], v[40:43], a[180:183], v[88:91]\n"
  "v_mfma_f32_16x16x32_bf16 v[92:95], v[44:47], a[180:183], v[92:95]\n"
  "s_waitcnt lgkmcnt(5)\n"
  "v_mfma_f32_16x16x32_bf16 v[72:75], a[40:43], v[16:19], v[72:75]\n"
  "ds_read_b128 v[40:43], v11 offset:24576\n"
  "s_waitcnt lgkmcnt(5)\n"
  "v_mfma_f32_16x16x32_bf16 v[76:79], a[40:43], v[20:23], v[76:79]\n"
  "ds_read_b128 v[44:47], v11 offset:26624\n"
  "v_mfma_f32_16x16x32_bf16 v[80:83], a[112:115], v[16:19], v[80:83]\n"
  "v_mfma_f32_16x16x32_bf16 v[84:87], a[112:115], v[20:23], v[84:87]\n"
  "s_add_u32 m0, s58, 15872\n"
  "s_nop 0\n"
  "global_load_lds_dwordx4 v3, s[42:43] offset:512\n"
  "v_mfma_f32_16x16x32_bf16 v[88:91], v[16:19], a[184:187], v[88:91]\n"
  "v_mfma_f32_16x16x32_bf16 v[92:95], v[20:23], a[184:187], v[92:95]\n"
  "s_waitcnt lgkmcnt(5)\n"
  "v_mfma_f32_16x16x32_bf16 v[72:75], a[44:47], v[24:27], v[72:75]\n"
  "ds_read_b128 v[16:19], v10 offset:28672\n"
  "s_waitcnt lgkmcnt(5)\n"
  "v_mfma_f32_16x16x32_bf16 v[76:79], a[44:47], v[28:31], v[76:79]\n"
  "ds_read_b128 v[20:23], v10 offset:30720\n"
  "v_mfma_f32_16x16x32_bf16 v[80:83], a[116:119], v[24:27], v[80:83]\n"
  "v_mfma_f32_16x16x32_bf16 v[84:87], a[116:119], v[28:31], v[84:87]\n"
  "s_add_u32 m0, s58, 19840\n"
  "s_nop 0\n"
  "global_load_lds_dwordx4 v3, s[42:43] offset:640\n"
  "v_mfma_f32_16x16x32_bf16 v[88:91], v[24:27], a[188:191], v[88:91]\n"
  "v_mfma_f32_16x16x32_bf16 v[92:95], v[28:31], a[188:191], v[92:95]\n"
  "s_waitcnt lgkmcnt(5)\n"
  "v_mfma_f32_16x16x32_bf16 v[72:75], a[48:51], v[32:35], v[72:75]\n"
  "ds_read_b128 v[24:27], v11 offset:28672\n"
  "s_waitcnt lgkmcnt(5)\n"
  "v_mfma_f32_16x16x32_bf16 v[76:79], a[48:51], v[36:39], v[76:79]\n"
  "ds_read_b128 v[28:31], v11 offset:30720\n"
  "v_mfma_f32_16x16x32_bf16 v[80:83], a[120:123], v[32:35], v[80:83]\n"
  "v_mfma_f32_16x16x32_bf16 v[84:87], a[120:123], v[36:39], v[84:87]\n"
  "s_add_u32 m0, s58, 23808\n"
  "s_nop 0\n"
  "global_load_lds_dwordx4 v3, s[42:43] offset:768\n"
  "v_mfma_f32_16x16x32_bf16 v[88:91], v[32:35], a[192:195], v[88:91]\n"
  "v_mfma_f32_16x16x32_bf16 v[92:95], v[36:39], a[192:195], v[92:95]\n"
  "s_waitcnt lgkmcnt(5)\n"
  "v_mfma_f32_16x16x32_bf16 v[72:75], a[52:55], v[40:43], v[72:75]\n"
  "ds_read_b128 v[32:35], v10 offset:32768\n"
  "s_waitcnt lgkmcnt(5)\n"
  "v_mfma_f32_16x16x32_bf16 v[76:79], a[52:55], v[44:47], v[76:79]\n"
  "ds_read_b128 v[36:39], v10 offset:34816\n"
  "v_mfma_f32_16x16x32_bf16 v[80:83], a[124:127], v[40:43], v[80:83]\n"
  "v_mfma_f32_16x16x32_bf16 v[84:87], a[124:127], v[44:47], v[84:87]\n"
  "s_add_u32 m0, s58, 27776\n"
  "s_nop 0\n"
  "global_load_lds_dwordx4 v3, s[42:43] offset:896\n"
  "v_mfma_f32_16x16x32_bf16 v[88:91], v[40:43], a[196:199], v[88:91]\n"
  "v_mfma_f32_16x16x32_bf16 v[92:95], v[44:47], a[196:199], v[92:95]\n"
  "s_waitcnt lgkmcnt(5)\n"
  "v_mfma_f32_16x16x32_bf16 v[72:75], a[56:59], v[16:19], v[72:75]\n"
  "ds_read_b128 v[40:43], v11 offset:32768\n"
  "s_waitcnt lgkmcnt(5)\n"
  "v_mfma_f32_16x16x32_bf16 v[76:79], a[56:59], v[20:23], v[76:79]\n"
  "ds_read_b128 v[44:47], v11 offset:34816\n"
  "v_mfma_f32_16x16x32_bf16 v[80:83], a[128:131], v[16:19], v[80:83]\n"
  "v_mfma_f32_16x16x32_bf16 v[84:87], a[128:131], v[20:23], v[84:87]\n"
  "s_add_u32 m0, s58, 31744\n"
  "s_nop 0\n"
  "global_load_lds_dwordx4 v3, s[42:43] offset:1024\n"
  "v_mfma_f32_16x16x32_bf16 v[88:91], v[16:19], a[200:203], v[88:91]\n"
  "v_mfma_f32_16x16x32_bf16 v[92:95], v[20:23], a[200:203], v[92:95]\n"
  "s_waitcnt vmcnt(9)\n"
  "s_barrier\n"
  "s_waitcnt lgkmcnt(5)\n"
  "v_mfma_f32_16x16x32_bf16 v[72:75], a[60:63], v[24:27], v[72:75]\n"
  "ds_read_b128 v[16:19], v12 offset:0\n"
  "s_waitcnt lgkmcnt(5)\n"
  "v_mfma_f32_16x16x32_bf16 v[76:79], a[60:63], v[28:31], v[76:79]\n"
  "ds_read_b128 v[20:23], v12 offset:2048\n"
  "v_mfma_f32_16x16x32_bf16 v[80:83], a[132:135], v[24:27], v[80:83]\n"
  "v_mov_b32 v6, 0\n"
  "v_mov_b32 v7, s56\n"
  "s_cmp_eq_u32 s47, 3\n"
  "s_cselect_b64 exec, 1, 0\n"
  "global_store_dword v6, v7, s[64:65] sc1\n"
  "s_mov_b64 exec, -1\n"
  "v_mfma_f32_16x16x32_bf16 v[84:87], a[132:135], v[28:31], v[84:87]\n"
  "s_bitcmp1_b64 s[4:5], s56\n"
  "s_cselect_b32 s74, 8192, 0\n"
  "v_mfma_f32_16x16x32_bf16 v[88:91], v[24:27], a[204:207], v[88:91]\n"
  "s_lshl_b32 s75, s56, 21\n"
  "s_add_u32 s74, s74, s75\n"
  "v_mfma_f32_16x16x32_bf16 v[92:95], v[28:31], a[204:207], v[92:95]\n"
  "s_add_u32 s74, s74, 20971520\n"
  "s_xor_b32 s75, s74, s44\n"
  "s_waitcnt lgkmcnt(5)\n"
  "v_mfma_f32_16x16x32_bf16 v[72:75], a[64:67], v[32:35], v[72:75]\n"
  "ds_read_b128 v[24:27], v13 offset:0\n"
  "s_waitcnt lgkmcnt(5)\n"
  "v_mfma_f32_16x16x32_bf16 v[76:79], a[64:67], v[36:39], v[76:79]\n"
  "ds_read_b128 v[28:31], v13 offset:2048\n"
  "v_mfma_f32_16x16x32_bf16 v[80:83], a[136:139], v[32:35], v[80:83]\n"
  "s_add_u32 s60, s36, s75\n"
  "s_addc_u32 s61, s37, 0\n"
  "v_mfma_f32_16x16x32_bf16 v[84:87], a[136:139], v[36:39], v[84:87]\n"
  "s_xor_b32 s75, s74, s45\n"
  "s_add_u32 s62, s36, s75\n"
  "v_mfma_f32_16x16x32_bf16 v[88:91], v[32:35], a[208:211], v[88:91]\n"
  "s_addc_u32 s63, s37, 0\n"
  "v_mfma_f32_16x16x32_bf16 v[92:95], v[36:39], a[208:211], v[92:95]\n"
  "s_waitcnt lgkmcnt(5)\n"
  "v_mfma_f32_16x16x32_bf16 v[72:75], a[68:71], v[40:43], v[72:75]\n"
  "ds_read_b128 v[32:35], v12 offset:4096\n"
  "s_waitcnt lgkmcnt(5)\n"
  "v_mfma_f32_16x16x32_bf16 v[76:79], a[68:71], v[44:47], v[76:79]\n"
  "ds_read_b128 v[36:39], v12 offset:6144\n"
  "v_mfma_f32_16x16x32_bf16 v[80:83], a[140:143], v[40:43], v[80:83]\n"
  "v_mfma_f32_16x16x32_bf16 v[84:87], a[140:143], v[44:47], v[84:87]\n"
  "v_mfma_f32_16x16x32_bf16 v[88:91], v[40:43], a[212:215], v[88:91]\n"
  "v_mfma_f32_16x16x32_bf16 v[92:95], v[44:47], a[212:215], v[92:95]\n"
  "s_add_u32 s56, s56, 1\n"
  "s_cmp_eq_u32 s56, 64\n"
  "s_cbranch_scc1 PJE\n"
  "v_mov_b64 v[10:11], v[12:13]\n"
  "s_waitcnt lgkmcnt(5)\n"
  "v_mfma_f32_16x16x32_bf16 v[48:51], a[0:3], v[16:19], 0\n"
  "ds_read_b128 v[40:43], v11 offset:4096\n"
  "s_waitcnt lgkmcnt(5)\n"
  "v_mfma_f32_16x16x32_bf16 v[52:55], a[0:3], v[20:23], 0\n"
  "ds_read_b128 v[44:47], v11 offset:6144\n"
  "v_mfma_f32_16x16x32_bf16 v[56:59], a[72:75], v[16:19], 0\n"
  "s_add_u32 s57, s57, 36864\n"
  "s_cmp_eq_u32 s57, 159744\n"
  "v_mfma_f32_16x16x32_bf16 v[60:63], a[72:75], v[20:23], 0\n"
  "s_cselect_b32 s57, 12288, s57\n"
  "v_mul_f32 v72, v8, v72\n"
  "v_mfma_f32_16x16x32_bf16 v[64:67], v[16:19], a[144:147], 0\n"
  "v_mul_f32 v73, v8, v73\n"
  "v_mul_f32 v74, v8, v74\n"
  "v_mfma_f32_16x16x32_bf16 v[68:71], v[20:23], a[144:147], 0\n"
  "v_mul_f32 v75, v8, v75\n"
  "v_cvt_pk_bf16_f32 v96, v72, v73\n"
  "s_waitcnt lgkmcnt(5)\n"
  "v_mfma_f32_16x16x32_bf16 v[48:51], a[4:7], v[24:27], v[48:51]\n"
  "ds_read_b128 v[16:19], v10 offset:8192\n"
  "s_waitcnt lgkmcnt(5)\n"
  "v_mfma_f32_16x16x32_bf16 v[52:55], a[4:7], v[28:31], v[52:55]\n"
  "ds_read_b128 v[20:23], v10 offset:10240\n"
  "v_mfma_f32_16x16x32_bf16 v[56:59], a[76:79], v[24:27], v[56:59]\n"
  "v_cvt_pk_bf16_f32 v97, v74, v75\n"
  "v_mul_f32 v80, v8, v80\n"
  "v_mfma_f32_16x16x32_bf16 v[60:63], a[76:79], v[28:31], v[60:63]\n"
  "v_mul_f32 v81, v8, v81\n"
  "v_mul_f32 v82, v8, v82\n"
  "v_mfma_f32_16x16x32_bf16 v[64:67], v[24:27], a[148:151], v[64:67]\n"
  "v_mul_f32 v83, v8, v83\n"
  "v_cvt_pk_bf16_f32 v98, v80, v81\n"
  "v_mfma_f32_16x16x32_bf16 v[68:71], v[28:31], a[148:151], v[68:71]\n"
  "v_cvt_pk_bf16_f32 v99, v82, v83\n"
  "global_store_dwordx4 v0, v[96:99], s[60:61] offset:0 sc1\n"
  "s_waitcnt lgkmcnt(5)\n"
  "v_mfma_f32_16x16x32_bf16 v[48:51], a[8:11], v[32:35], v[48:51]\n"
  "ds_read_b128 v[24:27], v11 offset:8192\n"
  "s_waitcnt lgkmcnt(5)\n"
  "v_mfma_f32_16x16x32_bf16 v[52:55], a[8:11], v[36:39], v[52:55]\n"
  "ds_read_b128 v[28:31], v11 offset:10240\n"
  "v_mfma_f32_16x16x32_bf16 v[56:59], a[80:83], v[32:35], v[56:59]\n"
  "v_mul_f32 v76, v8, v76\n"
  "v_mul_f32 v77, v8, v77\n"
  "v_mfma_f32_16x16x32_bf16 v[60:63], a[80:83], v[36:39], v[60:63]\n"
  "v_mul_f32 v78, v8, v78\n"
  "v_mul_f32 v79, v8, v79\n"
  "v_mfma_f32_16x16x32_bf16 v[64:67], v[32:35], a[152:155], v[64:67]\n"
  "v_cvt_pk_bf16_f32 v100, v76, v77\n"
  "v_cvt_pk_bf16_f32 v101, v78, v79\n"
  "v_mfma_f32_16x16x32_bf16 v[68:71], v[36:39], a[152:155], v[68:71]\n"
  "v_mul_f32 v84, v8, v84\n"
  "v_mul_f32 v85, v8, v85\n"
  "s_waitcnt lgkmcnt(5)\n"
  "v_mfma_f32_16x16x32_bf16 v[48:51], a[12:15], v[40:43], v[48:51]\n"
  "ds_read_b128 v[32:35], v10 offset:12288\n"
  "s_waitcnt lgkmcnt(5)\n"
  "v_mfma_f32_16x16x32_bf16 v[52:55], a[12:15], v[44:47], v[52:55]\n"
  "ds_read_b128 v[36:39], v10 offset:14336\n"
  "v_mfma_f32_16x16x32_bf16 v[56:59], a[84:87], v[40:43], v[56:59]\n"
  "v_mul_f32 v86, v8, v86\n"
  "v_mul_f32 v87, v8, v87\n"
  "v_mfma_f32_16x16x32_bf16 v[60:63], a[84:87], v[44:47], v[60:63]\n"
  "v_cvt_pk_bf16_f32 v102, v84, v85\n"
  "v_cvt_pk_bf16_f32 v103, v86, v87\n"
  "v_mfma_f32_16x16x32_bf16 v[64:67], v[40:43], a[156:159], v[64:67]\n"
  "global_store_dwordx4 v0, v[100:103], s[60:61] offset:1024 sc1\n"
  "v_cvt_pk_bf16_f32 v104, v88, v89\n"
  "v_mfma_f32_16x16x32_bf16 v[68:71], v[44:47], a[156:159], v[68:71]\n"
  "v_cvt_pk_bf16_f32 v105, v90, v91\n"
  "v_cvt_pk_bf16_f32 v106, v92, v93\n"
  "s_waitcnt lgkmcnt(5)\n"
  "v_mfma_f32_16x16x32_bf16 v[48:51], a[16:19], v[16:19], v[48:51]\n"
  "ds_read_b128 v[40:43], v11 offset:12288\n"
  "s_waitcnt lgkmcnt(5)\n"
  "v_mfma_f32_16x16x32_bf16 v[52:55], a[16:19], v[20:23], v[52:55]\n"
  "ds_read_b128 v[44:47], v11 offset:14336\n"
  "v_mfma_f32_16x16x32_bf16 v[56:59], a[88:91], v[16:19], v[56:59]\n"
  "v_cvt_pk_bf16_f32 v107, v94, v95\n"
  "global_store_dwordx4 v0, v[104:107], s[62:63] offset:0 sc1\n"
  "v_mfma_f32_16x16x32_bf16 v[60:63], a[88:91], v[20:23], v[60:63]\n"
  "v_add_u32 v12, s57, v9\n"
  "v_xor_b32 v13, 64, v12\n"
  "v_mfma_f32_16x16x32_bf16 v[64:67], v[16:19], a[160:163], v[64:67]\n"
  "s_add_u32 s58, s57, 36864\n"
  "s_cmp_eq_u32 s57, 122880\n"
  "v_mfma_f32_16x16x32_bf16 v[68:71], v[20:23], a[160:163], v[68:71]\n"
  "s_cselect_b32 s58, 12288, s58\n"
  "s_add_u32 s58, s58, s80\n"
  "s_waitcnt lgkmcnt(5)\n"
  "v_mfma_f32_16x16x32_bf16 v[48:51], a[20:23], v[24:27], v[48:51]\n"
  "ds_read_b128 v[16:19], v10 offset:16384\n"
  "s_waitcnt lgkmcnt(5)\n"
  "v_mfma_f32_16x16x32_bf16 v[52:55], a[20:23], v[28:31], v[52:55]\n"
  "ds_read_b128 v[20:23], v10 offset:18432\n"
  "v_mfma_f32_16x16x32_bf16 v[56:59], a[92:95], v[24:27], v[56:59]\n"
  "s_cmp_lt_u32 s56, 62\n"
  "s_cselect_b32 s74, 36864, 0\n"
  "v_mfma_f32_16x16x32_bf16 v[60:63], a[92:95], v[28:31], v[60:63]\n"
  "s_add_u32 s42, s42, s74\n"
  "s_addc_u32 s43, s43, 0\n"
  "v_mfma_f32_16x16x32_bf16 v[64:67], v[24:27], a[164:167], v[64:67]\n"
  "v_mfma_f32_16x16x32_bf16 v[68:71], v[28:31], a[164:167], v[68:71]\n"
  "s_waitcnt lgkmcnt(5)\n"
  "v_mfma_f32_16x16x32_bf16 v[48:51], a[24:27], v[32:35], v[48:51]\n"
  "ds_read_b128 v[24:27], v11 offset:16384\n"
  "s_waitcnt lgkmcnt(5)\n"
  "v_mfma_f32_16x16x32_bf16 v[52:55], a[24:27], v[36:39], v[52:55]\n"
  "ds_read_b128 v[28:31], v11 offset:18432\n"
  "v_mfma_f32_16x16x32_bf16 v[56:59], a[96:99], v[32:35], v[56:59]\n"
  "v_mfma_f32_16x16x32_bf16 v[60:63], a[96:99], v[36:39], v[60:63]\n"
  "s_add_u32 m0, s58, 0\n"
  "s_nop 0\n"
  "global_load_lds_dwordx4 v3, s[42:43] offset:0\n"
  "v_mfma_f32_16x16x32_bf16 v[64:67], v[32:35], a[168:171], v[64:67]\n"
  "v_mfma_f32_16x16x32_bf16 v[68:71], v[36:39], a[168:171], v[68:71]\n"
  "s_waitcnt lgkmcnt(5)\n"
  "v_mfma_f32_16x16x32_bf16 v[48:51], a[28:31], v[40:43], v[48:51]\n"
  "ds_read_b128 v[32:35], v10 offset:20480\n"
  "s_waitcnt lgkmcnt(5)\n"
  "v_mfma_f32_16x16x32_bf16 v[52:55], a[28:31], v[44:47], v[52:55]\n"
  "ds_read_b128 v[36:39], v10 offset:22528\n"
  "v_mfma_f32_16x16x32_bf16 v[56:59], a[100:103], v[40:43], v[56:59]\n"
  "v_mfma_f32_16x16x32_bf16 v[60:63], a[100:103], v[44:47], v[60:63]\n"
  "s_add_u32 m0, s58, 3968\n"
  "s_nop 0\n"
  "global_load_lds_dwordx4 v3, s[42:43] offset:128\n"
  "v_mfma_f32_16x16x32_bf16 v[64:67], v[40:43], a[172:175], v[64:67]\n"
  "v_mfma_f32_16x16x32_bf16 v[68:71], v[44:47], a[172:175], v[68:71]\n"
  "s_waitcnt lgkmcnt(5)\n"
  "v_mfma_f32_16x16x32_bf16 v[48:51], a[32:35], v[16:19], v[48:51]\n"
  "ds_read_b128 v[40:43], v11 offset:20480\n"
  "s_waitcnt lgkmcnt(5)\n"
  "v_mfma_f32_16x16x32_bf16 v[52:55], a[32:35], v[20:23], v[52:55]\n"
  "ds_read_b128 v[44:47], v11 offset:22528\n"
  "v_mfma_f32_16x16x32_bf16 v[56:59], a[104:107], v[16:19], v[56:59]\n"
  "v_mfma_f32_16x16x32_bf16 v[60:63], a[104:107], v[20:23], v[60:63]\n"
  "s_add_u32 m0, s58, 7936\n"
  "s_nop 0\n"
  "global_load_lds_dwordx4 v3, s[42:43] offset:256\n"
  "v_mfma_f32_16x16x32_bf16 v[64:67], v[16:19], a[176:179], v[64:67]\n"
  "v_mfma_f32_16x16x32_bf16 v[68:71], v[20:23], a[176:179], v[68:71]\n"
  "s_waitcnt lgkmcnt(5)\n"
  "v_mfma_f32_16x16x32_bf16 v[48:51], a[36:39], v[24:27], v[48:51]\n"
  "ds_read_b128 v[16:19], v10 offset:24576\n"
  "s_waitcnt lgkmcnt(5)\n"
  "v_mfma_f32_16x16x32_bf16 v[52:55], a[36:39], v[28:31], v[52:55]\n"
  "ds_read_b128 v[20:23], v10 offset:26624\n"
  "v_mfma_f32_16x16x32_bf16 v[56:59], a[108:111], v[24:27], v[56:59]\n"
  "v_mfma_f32_16x16x32_bf16 v[60:63], a[108:111], v[28:31], v[60:63]\n"
  "s_add_u32 m0, s58, 11904\n"
  "s_nop 0\n"
  "global_load_lds_dwordx4 v3, s[42:43] offset:384\n"
  "v_mfma_f32_16x16x32_bf16 v[64:67], v[24:27], a[180:183], v[64:67]\n"
  "v_mfma_f32_16x16x32_bf16 v[68:71], v[28:31], a[180:183], v[68:71]\n"
  "s_waitcnt lgkmcnt(5)\n"
  "v_mfma_f32_16x16x32_bf16 v[48:51], a[40:43], v[32:35], v[48:51]\n"
  "ds_read_b128 v[24:27], v11 offset:24576\n"
  "s_waitcnt lgkmcnt(5)\n"
  "v_mfma_f32_16x16x32_bf16 v[52:55], a[40:43], v[36:39], v[52:55]\n"
  "ds_read_b128 v[28:31], v11 offset:26624\n"
  "v_mfma_f32_16x16x32_bf16 v[56:59], a[112:115], v[32:35], v[56:59]\n"
  "v_mfma_f32_16x16x32_bf16 v[60:63], a[112:115], v[36:39], v[60:63]\n"
  "s_add_u32 m0, s58, 15872\n"
  "s_nop 0\n"
  "global_load_lds_dwordx4 v3, s[42:43] offset:512\n"
  "v_mfma_f32_16x16x32_bf16 v[64:67], v[32:35], a[184:187], v[64:67]\n"
  "v_mfma_f32_16x16x32_bf16 v[68:71], v[36:39], a[184:187], v[68:71]\n"
  "s_waitcnt lgkmcnt(5)\n"
  "v_mfma_f32_16x16x32_bf16 v[48:51], a[44:47], v[40:43], v[48:51]\n"
  "ds_read_b128 v[32:35], v10 offset:28672\n"
  "s_waitcnt lgkmcnt(5)\n"
  "v_mfma_f32_16x16x32_bf16 v[52:55], a[44:47], v[44:47], v[52:55]\n"
  "ds_read_b128 v[36:39], v10 offset:30720\n"
  "v_mfma_f32_16x16x32_bf16 v[56:59], a[116:119], v[40:43], v[56:59]\n"
  "v_mfma_f32_16x16x32_bf16 v[60:63], a[116:119], v[44:47], v[60:63]\n"
  "s_add_u32 m0, s58, 19840\n"
  "s_nop 0\n"
  "global_load_lds_dwordx4 v3, s[42:43] offset:640\n"
  "v_mfma_f32_16x16x32_bf16 v[64:67], v[40:43], a[188:191], v[64:67]\n"
  "v_mfma_f32_16x16x32_bf16 v[68:71], v[44:47], a[188:191], v[68:71]\n"
  "s_waitcnt lgkmcnt(5)\n"
  "v_mfma_f32_16x16x32_bf16 v[48:51], a[48:51], v[16:19], v[48:51]\n"
  "ds_read_b128 v[40:43], v11 offset:28672\n"
  "s_waitcnt lgkmcnt(5)\n"
  "v_mfma_f32_16x16x32_bf16 v[52:55], a[48:51], v[20:23], v[52:55]\n"
  "ds_read_b128 v[44:47], v11 offset:30720\n"
  "v_mfma_f32_16x16x32_bf16 v[56:59], a[120:123], v[16:19], v[56:59]\n"
  "v_mfma_f32_16x16x32_bf16 v[60:63], a[120:123], v[20:23], v[60:63]\n"
  "s_add_u32 m0, s58, 23808\n"
  "s_nop 0\n"
  "global_load_lds_dwordx4 v3, s[42:43] offset:768\n"
  "v_mfma_f32_16x16x32_bf16 v[64:67], v[16:19], a[192:195], v[64:67]\n"
  "v_mfma_f32_16x16x32_bf16 v[68:71], v[20:23], a[192:195], v[68:71]\n"
  "s_waitcnt lgkmcnt(5)\n"
  "v_mfma_f32_16x16x32_bf16 v[48:51], a[52:55], v[24:27], v[48:51]\n"
  "ds_read_b128 v[16:19], v10 offset:32768\n"
  "s_waitcnt lgkmcnt(5)\n"
  "v_mfma_f32_16x16x32_bf16 v[52:55], a[52:55], v[28:31], v[52:55]\n"
  "ds_read_b128 v[20:23], v10 offset:34816\n"
  "v_mfma_f32_16x16x32_bf16 v[56:59], a[124:127], v[24:27], v[56:59]\n"
  "v_mfma_f32_16x16x32_bf16 v[60:63], a[124:127], v[28:31], v[60:63]\n"
  "s_add_u32 m0, s58, 27776\n"
  "s_nop 0\n"
  "global_load_lds_dwordx4 v3, s[42:43] offset:896\n"
  "v_mfma_f32_16x16x32_bf16 v[64:67], v[24:27], a[196:199], v[64:67]\n"
  "v_mfma_f32_16x16x32_bf16 v[68:71], v[28:31], a[196:199], v[68:71]\n"
  "s_waitcnt lgkmcnt(5)\n"
  "v_mfma_f32_16x16x32_bf16 v[48:51], a[56:59], v[32:35], v[48:51]\n"
  "ds_read_b128 v[24:27], v11 offset:32768\n"
  "s_waitcnt lgkmcnt(5)\n"
  "v_mfma_f32_16x16x32_bf16 v[52:55], a[56:59], v[36:39], v[52:55]\n"
  "ds_read_b128 v[28:31], v11 offset:34816\n"
  "v_mfma_f32_16x16x32_bf16 v[56:59], a[128:131], v[32:35], v[56:59]\n"
  "v_mfma_f32_16x16x32_bf16 v[60:63], a[128:131], v[36:39], v[60:63]\n"
  "s_add_u32 m0, s58, 31744\n"
  "s_nop 0\n"
  "global_load_lds_dwordx4 v3, s[42:43] offset:1024\n"
  "v_mfma_f32_16x16x32_bf16 v[64:67], v[32:35], a[200:203], v[64:67]\n"
  "v_mfma_f32_16x16x32_bf16 v[68:71], v[36:39], a[200:203], v[68:71]\n"
  "s_waitcnt vmcnt(9)\n"
  "s_barrier\n"
  "s_waitcnt lgkmcnt(5)\n"
  "v_mfma_f32_16x16x32_bf16 v[48:51], a[60:63], v[40:43], v[48:51]\n"
  "ds_read_b128 v[32:35], v12 offset:0\n"
  "s_waitcnt lgkmcnt(5)\n"
  "v_mfma_f32_16x16x32_bf16 v[52:55], a[60:63], v[44:47], v[52:55]\n"
  "ds_read_b128 v[36:39], v12 offset:2048\n"
  "v_mfma_f32_16x16x32_bf16 v[56:59], a[132:135], v[40:43], v[56:59]\n"
  "v_mov_b32 v6, 0\n"
  "v_mov_b32 v7, s56\n"
  "s_cmp_eq_u32 s47, 3\n"
  "s_cselect_b64 exec, 1, 0\n"
  "global_store_dword v6, v7, s[64:65] sc1\n"
  "s_mov_b64 exec, -1\n"
  "v_mfma_f32_16x16x32_bf16 v[60:63], a[132:135], v[44:47], v[60:63]\n"
  "s_bitcmp1_b64 s[4:5], s56\n"
  "s_cselect_b32 s74, 8192, 0\n"
  "v_mfma_f32_16x16x32_bf16 v[64:67], v[40:43], a[204:207], v[64:67]\n"
  "s_lshl_b32 s75, s56, 21\n"
  "s_add_u32 s74, s74, s75\n"
  "v_mfma_f32_16x16x32_bf16 v[68:71], v[44:47], a[204:207], v[68:71]\n"
  "s_add_u32 s74, s74, 20971520\n"
  "s_xor_b32 s75, s74, s44\n"
  "s_waitcnt lgkmcnt(5)\n"
  "v_mfma_f32_16x16x32_bf16 v[48:51], a[64:67], v[16:19], v[48:51]\n"
  "ds_read_b128 v[40:43], v13 offset:0\n"
  "s_waitcnt lgkmcnt(5)\n"
  "v_mfma_f32_16x16x32_bf16 v[52:55], a[64:67], v[20:23], v[52:55]\n"
  "ds_read_b128 v[44:47], v13 offset:2048\n"
  "v_mfma_f32_16x16x32_bf16 v[56:59], a[136:139], v[16:19], v[56:59]\n"
  "s_add_u32 s60, s36, s75\n"
  "s_addc_u32 s61, s37, 0\n"
  "v_mfma_f32_16x16x32_bf16 v[60:63], a[136:139], v[20:23], v[60:63]\n"
  "s_xor_b32 s75, s74, s45\n"
  "s_add_u32 s62, s36, s75\n"
  "v_mfma_f32_16x16x32_bf16 v[64:67], v[16:19], a[208:211], v[64:67]\n"
  "s_addc_u32 s63, s37, 0\n"
  "v_mfma_f32_16x16x32_bf16 v[68:71], v[20:23], a[208:211], v[68:71]\n"
  "s_waitcnt lgkmcnt(5)\n"
  "v_mfma_f32_16x16x32_bf16 v[48:51], a[68:71], v[24:27], v[48:51]\n"
  "ds_read_b128 v[16:19], v12 offset:4096\n"
  "s_waitcnt lgkmcnt(5)\n"
  "v_mfma_f32_16x16x32_bf16 v[52:55], a[68:71], v[28:31], v[52:55]\n"
  "ds_read_b128 v[20:23], v12 offset:6144\n"
  "v_mfma_f32_16x16x32_bf16 v[56:59], a[140:143], v[24:27], v[56:59]\n"
  "v_mfma_f32_16x16x32_bf16 v[60:63], a[140:143], v[28:31], v[60:63]\n"
  "v_mfma_f32_16x16x32_bf16 v[64:67], v[24:27], a[212:215], v[64:67]\n"
  "v_mfma_f32_16x16x32_bf16 v[68:71], v[28:31], a[212:215], v[68:71]\n"
  "s_add_u32 s56, s56, 1\n"
  "s_branch PJL\n"
  "PJE:\n"
  "s_waitcnt vmcnt(0) lgkmcnt(0)\n"
  "s_nop 3\n"
  "v_mov_b32 v7, v7\n"
  "s_nop 2\n"
  "v_mov_b32 v7, v7\n"
  "s_nop 2\n"
  "v_mul_f32 v72, v8, v72\n"
  "v_mul_f32 v73, v8, v73\n"
  "v_mul_f32 v74, v8, v74\n"
  "v_mul_f32 v75, v8, v75\n"
  "v_cvt_pk_bf16_f32 v96, v72, v73\n"
  "v_cvt_pk_bf16_f32 v97, v74, v75\n"
  "v_mul_f32 v80, v8, v80\n"
  "v_mul_f32 v81, v8, v81\n"
  "v_mul_f32 v82, v8, v82\n"
  "v_mul_f32 v83, v8, v83\n"
  "v_cvt_pk_bf16_f32 v98, v80, v81\n"
  "v_cvt_pk_bf16_f32 v99, v82, v83\n"
  "global_store_dwordx4 v0, v[96:99], s[60:61] offset:0 sc1\n"
  "v_mul_f32 v76, v8, v76\n"
  "v_mul_f32 v77, v8, v77\n"
  "v_mul_f32 v78, v8, v78\n"
  "v_mul_f32 v79, v8, v79\n"
  "v_cvt_pk_bf16_f32 v100, v76, v77\n"
  "v_cvt_pk_bf16_f32 v101, v78, v79\n"
  "v_mul_f32 v84, v8, v84\n"
  "v_mul_f32 v85, v8, v85\n"
  "v_mul_f32 v86, v8, v86\n"
  "v_mul_f32 v87, v8, v87\n"
  "v_cvt_pk_bf16_f32 v102, v84, v85\n"
  "v_cvt_pk_bf16_f32 v103, v86, v87\n"
  "global_store_dwordx4 v0, v[100:103], s[60:61] offset:1024 sc1\n"
  "v_cvt_pk_bf16_f32 v104, v88, v89\n"
  "v_cvt_pk_bf16_f32 v105, v90, v91\n"
  "v_cvt_pk_bf16_f32 v106, v92, v93\n"
  "v_cvt_pk_bf16_f32 v107, v94, v95\n"
  "global_store_dwordx4 v0, v[104:107], s[62:63] offset:0 sc1\n"
  "s_waitcnt vmcnt(0)\n"
  "s_barrier\n"
  "v_mov_b32 v6, 0\n"
  "v_mov_b32 v7, s56\n"
  "s_cmp_eq_u32 s47, 3\n"
  "s_cselect_b64 exec, 1, 0\n"
  "global_store_dword v6, v7, s[64:65] sc1\n"
  "s_mov_b64 exec, -1\n"
  "s_endpgm\n"
      : "+{v[0:15]}"(vin), "+{s[36:43]}"(sa), "+{s[44:59]}"(sb) : "{s[4:5]}"(pol) : CLOB);
  } else if (tk >= 29) {
    const int h = fd * 8 + (tk - 29) * 3 + wave;
    if (wave == 3 || (tk == 31 && wave == 2)) return;
    vin[1] = lane < 2 ? (C_KVP ^ cx) + (2 * h + lane) * 128 : lane == 2 ? (C_HF ^ cx) + h * 256 : (C_VF ^ cx) + (2 * h + (lane & 1)) * 128;
    vin[2] = lane < 2 ? 2 : 1; vin[3] = qq * 256 + g * 64 + c4 * 16;
    vin[4] = wave * 2048 + lane * 16; vin[5] = wave * 2048 + c4 * 128 + (4 * g + qq) * 8;
    vin[6] = n - 4 * g;
    sb[0] = KVH + h * 65536 + 4096; sb[1] = h * 65536 + 4096 + 32768; sb[2] = WO_KM + h * 2048; sb[3] = WO_VP + h * 2048;
    sb[4] = sb[1] + 16384; sb[5] = (C_QAP ^ cx) + h * 128; sb[6] = __float_as_uint(QS); sb[7] = WO_ST + (role * 4 + wave) * 4096;
    UNIFORM(sa, 8); UNIFORM(sb, 16);
    asm volatile(
  "s_nop 4\n"
  "v_cmp_le_i32 s[8:9], 0, v6\n"
  "v_cmp_le_i32 s[10:11], 1, v6\n"
  "v_cmp_le_i32 s[12:13], 2, v6\n"
  "v_cmp_le_i32 s[14:15], 3, v6\n"
  "v_mov_b64 v[156:157], 0\n"
  "v_mov_b64 v[158:159], 0\n"
  "v_mov_b64 v[160:161], 0\n"
  "v_mov_b64 v[162:163], 0\n"
  "v_mov_b64 v[164:165], 0\n"
  "v_mov_b64 v[166:167], 0\n"
  "v_mov_b64 v[168:169], 0\n"
  "v_mov_b64 v[170:171], 0\n"
  "v_mov_b64 v[172:173], 0\n"
  "v_mov_b64 v[174:175], 0\n"
  "v_mov_b64 v[176:177], 0\n"
  "v_mov_b64 v[178:179], 0\n"
  "v_mov_b64 v[180:181], 0\n"
  "v_mov_b64 v[182:183], 0\n"
  "v_mov_b64 v[184:185], 0\n"
  "v_mov_b64 v[186:187], 0\n"
  "v_mov_b64 v[150:151], 0\n"
  "v_mov_b32 v12, 0xff7fffff\n"
  "s_mov_b32 s56, 0\n"
  "s_mov_b32 s57, 400000\n"
  "s_add_u32 s64, s36, s46\n"
  "s_addc_u32 s65, s37, 0\n"
  "s_add_u32 s66, s36, s47\n"
  "s_addc_u32 s67, s37, 0\n"
  "s_add_u32 s70, s36, s49\n"
  "s_addc_u32 s71, s37, 0\n"
  "s_bitcmp1_b64 s[4:5], 0\n"
  "s_cselect_b32 s74, 8192, 0\n"
  "s_lshl_b32 s75, 0, 21\n"
  "s_add_u32 s74, s74, s75\n"
  "s_add_u32 s74, s74, 20971520\n"
  "s_xor_b32 s75, s74, s44\n"
  "s_add_u32 s60, s36, s75\n"
  "s_addc_u32 s61, s37, 0\n"
  "s_xor_b32 s75, s74, s45\n"
  "s_add_u32 s62, s36, s75\n"
  "s_addc_u32 s63, s37, 0\n"
  "TPS:\n"
  "global_load_dword v9, v1, s[36:37] sc1\n"
  "v_add_u32 v7, s56, v2\n"
  "v_add_u32 v7, -1, v7\n"
  "v_min_i32 v7, 64, v7\n"
  "s_waitcnt vmcnt(0)\n"
  "v_cmp_gt_i32 vcc, v7, v9\n"
  "s_cbranch_vccz TGS\n"
  "s_sub_u32 s57, s57, 1\n"
  "s_cbranch_scc1 TQUIT\n"
  "s_sleep 2\n"
  "s_branch TPS\n"
  "TGS:\n"
  "global_load_dwordx4 v[16:19], v0, s[62:63] offset:-4096 sc1\n"
  "global_load_dwordx4 v[20:23], v0, s[62:63] offset:-3072 sc1\n"
  "global_load_dwordx4 v[24:27], v0, s[62:63] offset:-2048 sc1\n"
  "global_load_dwordx4 v[28:31], v0, s[62:63] offset:-1024 sc1\n"
  "global_load_dwordx4 v[32:35], v0, s[62:63] offset:0 sc1\n"
  "global_load_dwordx4 v[36:39], v0, s[62:63] offset:1024 sc1\n"
  "global_load_dwordx4 v[40:43], v0, s[62:63] offset:2048 sc1\n"
  "global_load_dwordx4 v[44:47], v0, s[62:63] offset:3072 sc1\n"
  "global_load_dwordx4 a[0:3], v0, s[60:61] offset:-4096 sc1\n"
  "global_load_dwordx4 a[4:7], v0, s[60:61] offset:-3072 sc1\n"
  "global_load_dwordx4 a[8:11], v0, s[60:61] offset:-2048 sc1\n"
  "global_load_dwordx4 a[12:15], v0, s[60:61] offset:-1024 sc1\n"
  "global_load_dwordx4 a[16:19], v0, s[60:61] offset:0 sc1\n"
  "global_load_dwordx4 a[20:23], v0, s[60:61] offset:1024 sc1\n"
  "global_load_dwordx4 a[24:27], v0, s[60:61] offset:2048 sc1\n"
  "global_load_dwordx4 a[28:31], v0, s[60:61] offset:3072 sc1\n"
  "s_add_u32 s60, s60, 16384\n"
  "s_addc_u32 s61, s61, 0\n"
  "global_load_dwordx4 a[32:35], v0, s[60:61] offset:-4096 sc1\n"
  "global_load_dwordx4 a[36:39], v0, s[60:61] offset:-3072 sc1\n"
  "global_load_dwordx4 a[40:43], v0, s[60:61] offset:-2048 sc1\n"
  "global_load_dwordx4 a[44:47], v0, s[60:61] offset:-1024 sc1\n"
  "global_load_dwordx4 a[48:51], v0, s[60:61] offset:0 sc1\n"
  "global_load_dwordx4 a[52:55], v0, s[60:61] offset:1024 sc1\n"
  "global_load_dwordx4 a[56:59], v0, s[60:61] offset:2048 sc1\n"
  "global_load_dwordx4 a[60:63], v0, s[60:61] offset:3072 sc1\n"
  "s_bitcmp1_b64 s[4:5], 1\n"
  "s_cselect_b32 s74, 8192, 0\n"
  "s_lshl_b32 s75, 1, 21\n"
  "s_add_u32 s74, s74, s75\n"
  "s_add_u32 s74, s74, 20971520\n"
  "s_xor_b32 s75, s74, s44\n"
  "s_add_u32 s60, s36, s75\n"
  "s_addc_u32 s61, s37, 0\n"
  "s_xor_b32 s75, s74, s45\n"
  "s_add_u32 s62, s36, s75\n"
  "s_addc_u32 s63, s37, 0\n"
  "s_mov_b64 s[16:17], -1\n"
  "s_waitcnt vmcnt(0)\n"
  "TOP0:\n"
  "s_waitcnt vmcnt(8)\n"
  "ds_write_b128 v4, v[80:83] offset:0\n"
  "ds_write_b128 v4, v[84:87] offset:1024\n"
  "ds_read_b64_tr_b16 v[88:89], v5 offset:0\n"
  "ds_read_b64_tr_b16 v[90:91], v5 offset:512\n"
  "ds_read_b64_tr_b16 v[92:93], v5 offset:1024\n"
  "ds_read_b64_tr_b16 v[94:95], v5 offset:1536\n"
  "v_mfma_f32_16x16x32_bf16 v[120:123], a[0:3], v[16:19], 0\n"
  "v_mfma_f32_16x16x32_bf16 v[124:127], a[0:3], v[20:23], 0\n"
  "v_mfma_f32_16x16x32_bf16 v[128:131], a[4:7], v[20:23], 0\n"
  "v_mfma_f32_16x16x32_bf16 v[120:123], a[8:11], v[24:27], v[120:123]\n"
  "v_mfma_f32_16x16x32_bf16 v[124:127], a[8:11], v[28:31], v[124:127]\n"
  "v_mfma_f32_16x16x32_bf16 v[128:131], a[12:15], v[28:31], v[128:131]\n"
  "v_mfma_f32_16x16x32_bf16 v[120:123], a[16:19], v[32:35], v[120:123]\n"
  "v_mfma_f32_16x16x32_bf16 v[124:127], a[16:19], v[36:39], v[124:127]\n"
  "v_mfma_f32_16x16x32_bf16 v[128:131], a[20:23], v[36:39], v[128:131]\n"
  "v_mfma_f32_16x16x32_bf16 v[120:123], a[24:27], v[40:43], v[120:123]\n"
  "v_mfma_f32_16x16x32_bf16 v[124:127], a[24:27], v[44:47], v[124:127]\n"
  "v_mfma_f32_16x16x32_bf16 v[128:131], a[28:31], v[44:47], v[128:131]\n"
  "s_cmp_eq_u64 s[16:17], 0\n"
  "s_cbranch_scc1 TGO0\n"
  "s_waitcnt vmcnt(0)\n"
  "v_mov_b32 v7, 0\n"
  "v_mov_b32 v8, s56\n"
  "s_mov_b64 exec, 1\n"
  "global_store_dword v7, v8, s[70:71] sc1\n"
  "s_mov_b64 exec, -1\n"
  "TP0:\n"
  "global_load_dword v9, v1, s[36:37] sc1\n"
  "v_add_u32 v7, s56, v2\n"
  "v_add_u32 v7, 0, v7\n"
  "v_min_i32 v7, 64, v7\n"
  "s_waitcnt vmcnt(0)\n"
  "v_cmp_gt_i32 vcc, v7, v9\n"
  "s_cbranch_vccz TG0\n"
  "s_sub_u32 s57, s57, 1\n"
  "s_cbranch_scc1 TQUIT\n"
  "s_sleep 2\n"
  "s_branch TP0\n"
  "TG0:\n"
  "global_load_dwordx4 v[80:83], v0, s[64:65] offset:0 sc1\n"
  "global_load_dwordx4 v[84:87], v0, s[64:65] offset:1024 sc1\n"
  "global_load_dwordx4 v[96:99], v3, s[66:67] offset:0 sc1\n"
  "global_load_dwordx4 v[100:103], v3, s[66:67] offset:1024 sc1\n"
  "s_add_u32 s64, s64, 65536\n"
  "s_addc_u32 s65, s65, 0\n"
  "s_add_u32 s66, s66, 65536\n"
  "s_addc_u32 s67, s67, 0\n"
  "s_waitcnt vmcnt(3)\n"
  "ds_write_b128 v4, v[80:83] offset:0\n"
  "s_waitcnt vmcnt(2)\n"
  "ds_write_b128 v4, v[84:87] offset:1024\n"
  "s_waitcnt lgkmcnt(5)\n"
  "ds_read_b64_tr_b16 v[88:89], v5 offset:0\n"
  "s_waitcnt lgkmcnt(5)\n"
  "ds_read_b64_tr_b16 v[90:91], v5 offset:512\n"
  "s_waitcnt lgkmcnt(5)\n"
  "ds_read_b64_tr_b16 v[92:93], v5 offset:1024\n"
  "s_waitcnt lgkmcnt(5)\n"
  "ds_read_b64_tr_b16 v[94:95], v5 offset:1536\n"
  "s_waitcnt vmcnt(0)\n"
  "TGO0:\n"
  "s_waitcnt lgkmcnt(3)\n"
  "v_mfma_f32_16x16x16_bf16 v[132:135], v[88:89], v[16:17], 0\n"
  "global_load_dword v9, v1, s[36:37] sc1\n"
  "v_mfma_f32_16x16x16_bf16 v[140:143], v[88:89], v[18:19], 0\n"
  "global_load_dwordx4 v[48:51], v0, s[62:63] offset:-4096 sc1\n"
  "s_waitcnt lgkmcnt(2)\n"
  "v_mfma_f32_16x16x16_bf16 v[132:135], v[90:91], v[24:25], v[132:135]\n"
  "global_load_dwordx4 v[52:55], v0, s[62:63] offset:-3072 sc1\n"
  "v_mfma_f32_16x16x16_bf16 v[140:143], v[90:91], v[26:27], v[140:143]\n"
  "global_load_dwordx4 v[56:59], v0, s[62:63] offset:-2048 sc1\n"
  "s_waitcnt lgkmcnt(1)\n"
  "v_mfma_f32_16x16x16_bf16 v[132:135], v[92:93], v[32:33], v[132:135]\n"
  "global_load_dwordx4 v[60:63], v0, s[62:63] offset:-1024 sc1\n"
  "v_mfma_f32_16x16x16_bf16 v[140:143], v[92:93], v[34:35], v[140:143]\n"
  "global_load_dwordx4 v[64:67], v0, s[62:63] offset:0 sc1\n"
  "s_waitcnt lgkmcnt(0)\n"
  "v_mfma_f32_16x16x16_bf16 v[132:135], v[94:95], v[40:41], v[132:135]\n"
  "global_load_dwordx4 v[68:71], v0, s[62:63] offset:1024 sc1\n"
  "v_mfma_f32_16x16x16_bf16 v[140:143], v[94:95], v[42:43], v[140:143]\n"
  "global_load_dwordx4 v[72:75], v0, s[62:63] offset:2048 sc1\n"
  "v_mfma_f32_16x16x16_bf16 v[136:139], v[88:89], v[20:21], 0\n"
  "global_load_dwordx4 v[76:79], v0, s[62:63] offset:3072 sc1\n"
  "v_mfma_f32_16x16x16_bf16 v[144:147], v[88:89], v[22:23], 0\n"
  "global_load_dwordx4 a[0:3], v0, s[60:61] offset:-4096 sc1\n"
  "v_mfma_f32_16x16x16_bf16 v[136:139], v[90:91], v[28:29], v[136:139]\n"
  "global_load_dwordx4 a[4:7], v0, s[60:61] offset:-3072 sc1\n"
  "v_mfma_f32_16x16x16_bf16 v[144:147], v[90:91], v[30:31], v[144:147]\n"
  "global_load_dwordx4 a[8:11], v0, s[60:61] offset:-2048 sc1\n"
  "v_mfma_f32_16x16x16_bf16 v[136:139], v[92:93], v[36:37], v[136:139]\n"
  "v_cndmask_b32 v120, v12, v120, s[8:9]\n"
  "v_cndmask_b32 v121, v12, v121, s[10:11]\n"
  "v_mfma_f32_16x16x16_bf16 v[144:147], v[92:93], v[38:39], v[144:147]\n"
  "v_cndmask_b32 v122, v12, v122, s[12:13]\n"
  "v_cndmask_b32 v123, v12, v123, s[14:15]\n"
  "v_mfma_f32_16x16x16_bf16 v[136:139], v[94:95], v[44:45], v[136:139]\n"
  "v_cndmask_b32 v128, v12, v128, s[8:9]\n"
  "v_cndmask_b32 v129, v12, v129, s[10:11]\n"
  "v_mfma_f32_16x16x16_bf16 v[144:147], v[94:95], v[46:47], v[144:147]\n"
  "v_cndmask_b32 v130, v12, v130, s[12:13]\n"
  "v_cndmask_b32 v131, v12, v131, s[14:15]\n"
  "v_add_f32 v132, v132, v142\n"
  "v_add_f32 v133, v133, v143\n"
  "v_mul_f32 v132, s50, v132\n"
  "v_mul_f32 v133, s50, v133\n"
  "v_max_f32 v10, v132, v133\n"
  "v_max3_f32 v10, v10, v120, v121\n"
  "v_max3_f32 v10, v10, v122, v123\n"
  "v_mov_b32 v11, v10\n"
  "s_nop 1\n"
  "v_permlane16_swap_b32 v10, v11\n"
  "v_max_f32 v10, v10, v11\n"
  "v_mov_b32 v11, v10\n"
  "s_nop 1\n"
  "v_permlane32_swap_b32 v10, v11\n"
  "v_max_f32 v10, v10, v11\n"
  "v_sub_f32 v120, v120, v10\n"
  "v_sub_f32 v121, v121, v10\n"
  "v_sub_f32 v122, v122, v10\n"
  "v_sub_f32 v123, v123, v10\n"
  "v_sub_f32 v132, v132, v10\n"
  "v_sub_f32 v133, v133, v10\n"
  "v_exp_f32 v120, v120\n"
  "v_exp_f32 v121, v121\n"
  "v_exp_f32 v122, v122\n"
  "v_exp_f32 v123, v123\n"
  "v_exp_f32 v132, v132\n"
  "v_exp_f32 v133, v133\n"
  "v_add_f32 v10, v120, v121\n"
  "v_add_f32 v10, v10, v122\n"
  "v_add_f32 v10, v10, v123\n"
  "v_add_f32 v10, v10, v132\n"
  "v_add_f32 v10, v10, v133\n"
  "v_mov_b32 v11, v10\n"
  "s_nop 1\n"
  "v_permlane16_swap_b32 v10, v11\n"
  "v_add_f32 v10, v10, v11\n"
  "v_mov_b32 v11, v10\n"
  "s_nop 1\n"
  "v_permlane32_swap_b32 v10, v11\n"
  "v_add_f32 v10, v10, v11\n"
  "v_rcp_f32 v10, v10\n"
  "s_nop 0\n"
  "v_mul_f32 v120, v10, v120\n"
  "v_mul_f32 v121, v10, v121\n"
  "v_mul_f32 v122, v10, v122\n"
  "v_mul_f32 v123, v10, v123\n"
  "v_mul_f32 v132, v10, v132\n"
  "v_mul_f32 v133, v10, v133\n"
  "v_cvt_pk_bf16_f32 v148, v120, v121\n"
  "v_cvt_pk_bf16_f32 v149, v122, v123\n"
  "v_cvt_pk_bf16_f32 v156, v132, v133\n"
  "v_cvt_pk_bf16_f32 v161, v132, v133\n"
  "v_cvt_pk_bf16_f32 v166, v132, v133\n"
  "v_cvt_pk_bf16_f32 v171, v132, v133\n"
  "v_mfma_f32_16x16x32_bf16 v[188:191], a[32:35], v[148:151], 0\n"
  "global_load_dwordx4 a[12:15], v0, s[60:61] offset:-1024 sc1\n"
  "v_mfma_f32_16x16x32_bf16 v[188:191], v[96:99], v[156:159], v[188:191]\n"
  "global_load_dwordx4 a[16:19], v0, s[60:61] offset:0 sc1\n"
  "v_mfma_f32_16x16x32_bf16 v[192:195], a[36:39], v[148:151], 0\n"
  "global_load_dwordx4 a[20:23], v0, s[60:61] offset:1024 sc1\n"
  "v_mfma_f32_16x16x32_bf16 v[192:195], v[96:99], v[160:163], v[192:195]\n"
  "global_load_dwordx4 a[24:27], v0, s[60:61] offset:2048 sc1\n"
  "v_mfma_f32_16x16x32_bf16 v[196:199], a[40:43], v[148:151], 0\n"
  "global_load_dwordx4 a[28:31], v0, s[60:61] offset:3072 sc1\n"
  "v_mfma_f32_16x16x32_bf16 v[196:199], v[96:99], v[164:167], v[196:199]\n"
  "s_add_u32 s60, s60, 16384\n"
  "s_addc_u32 s61, s61, 0\n"
  "global_load_dwordx4 a[64:67], v0, s[60:61] offset:-4096 sc1\n"
  "v_mfma_f32_16x16x32_bf16 v[200:203], a[44:47], v[148:151], 0\n"
  "global_load_dwordx4 a[68:71], v0, s[60:61] offset:-3072 sc1\n"
  "v_mfma_f32_16x16x32_bf16 v[200:203], v[96:99], v[168:171], v[200:203]\n"
  "global_load_dwordx4 a[72:75], v0, s[60:61] offset:-2048 sc1\n"
  "v_mfma_f32_16x16x32_bf16 v[204:207], a[48:51], v[148:151], 0\n"
  "global_load_dwordx4 a[76:79], v0, s[60:61] offset:-1024 sc1\n"
  "v_mfma_f32_16x16x32_bf16 v[204:207], v[100:103], v[156:159], v[204:207]\n"
  "global_load_dwordx4 a[80:83], v0, s[60:61] offset:0 sc1\n"
  "v_mfma_f32_16x16x32_bf16 v[208:211], a[52:55], v[148:151], 0\n"
  "global_load_dwordx4 a[84:87], v0, s[60:61] offset:1024 sc1\n"
  "v_mfma_f32_16x16x32_bf16 v[208:211], v[100:103], v[160:163], v[208:211]\n"
  "global_load_dwordx4 a[88:91], v0, s[60:61] offset:2048 sc1\n"
  "v_mfma_f32_16x16x32_bf16 v[212:215], a[56:59], v[148:151], 0\n"
  "global_load_dwordx4 a[92:95], v0, s[60:61] offset:3072 sc1\n"
  "v_mfma_f32_16x16x32_bf16 v[212:215], v[100:103], v[164:167], v[212:215]\n"
  "v_mfma_f32_16x16x32_bf16 v[216:219], a[60:63], v[148:151], 0\n"
  "v_mfma_f32_16x16x32_bf16 v[216:219], v[100:103], v[168:171], v[216:219]\n"
  "s_add_u32 s76, s56, 2\n"
  "s_min_u32 s76, s76, 63\n"
  "s_bitcmp1_b64 s[4:5], s76\n"
  "s_cselect_b32 s74, 8192, 0\n"
  "s_lshl_b32 s75, s76, 21\n"
  "s_add_u32 s74, s74, s75\n"
  "s_add_u32 s74, s74, 20971520\n"
  "s_xor_b32 s75, s74, s44\n"
  "s_add_u32 s60, s36, s75\n"
  "s_addc_u32 s61, s37, 0\n"
  "s_xor_b32 s75, s74, s45\n"
  "s_add_u32 s62, s36, s75\n"
  "s_addc_u32 s63, s37, 0\n"
  "s_bitcmp1_b64 s[4:5], s56\n"
  "s_cselect_b32 s74, 8192, 0\n"
  "s_lshl_b32 s75, s56, 21\n"
  "s_add_u32 s74, s74, s75\n"
  "s_add_u32 s74, s74, 20971520\n"
  "s_xor_b32 s75, s74, s48\n"
  "s_add_u32 s68, s36, s75\n"
  "s_addc_u32 s69, s37, 0\n"
  "v_add_f32 v136, v136, v146\n"
  "v_add_f32 v137, v137, v147\n"
  "v_mul_f32 v136, s50, v136\n"
  "v_mul_f32 v137, s50, v137\n"
  "v_max_f32 v10, v136, v137\n"
  "v_max3_f32 v10, v10, v124, v125\n"
  "v_max3_f32 v10, v10, v126, v127\n"
  "v_max3_f32 v10, v10, v128, v129\n"
  "v_max3_f32 v10, v10, v130, v131\n"
  "v_mov_b32 v11, v10\n"
  "s_nop 1\n"
  "v_permlane16_swap_b32 v10, v11\n"
  "v_max_f32 v10, v10, v11\n"
  "v_mov_b32 v11, v10\n"
  "s_nop 1\n"
  "v_permlane32_swap_b32 v10, v11\n"
  "v_max_f32 v10, v10, v11\n"
  "v_sub_f32 v124, v124, v10\n"
  "v_sub_f32 v125, v125, v10\n"
  "v_sub_f32 v126, v126, v10\n"
  "v_sub_f32 v127, v127, v10\n"
  "v_sub_f32 v128, v128, v10\n"
  "v_sub_f32 v129, v129, v10\n"
  "v_sub_f32 v130, v130, v10\n"
  "v_sub_f32 v131, v131, v10\n"
  "v_sub_f32 v136, v136, v10\n"
  "v_sub_f32 v137, v137, v10\n"
  "v_exp_f32 v124, v124\n"
  "v_exp_f32 v125, v125\n"
  "v_exp_f32 v126, v126\n"
  "v_exp_f32 v127, v127\n"
  "v_exp_f32 v128, v128\n"
  "v_exp_f32 v129, v129\n"
  "v_exp_f32 v130, v130\n"
  "v_exp_f32 v131, v131\n"
  "v_exp_f32 v136, v136\n"
  "v_exp_f32 v137, v137\n"
  "v_add_f32 v10, v124, v125\n"
  "v_add_f32 v10, v10, v126\n"
  "v_add_f32 v10, v10, v127\n"
  "v_add_f32 v10, v10, v128\n"
  "v_add_f32 v10, v10, v129\n"
  "v_add_f32 v10, v10, v130\n"
  "v_add_f32 v10, v10, v131\n"
  "v_add_f32 v10, v10, v136\n"
  "v_add_f32 v10, v10, v137\n"
  "v_mov_b32 v11, v10\n"
  "s_nop 1\n"
  "v_permlane16_swap_b32 v10, v11\n"
  "v_add_f32 v10, v10, v11\n"
  "v_mov_b32 v11, v10\n"
  "s_nop 1\n"
  "v_permlane32_swap_b32 v10, v11\n"
  "v_add_f32 v10, v10, v11\n"
  "v_rcp_f32 v10, v10\n"
  "s_nop 0\n"
  "v_mul_f32 v124, v10, v124\n"
  "v_mul_f32 v125, v10, v125\n"
  "v_mul_f32 v126, v10, v126\n"
  "v_mul_f32 v127, v10, v127\n"
  "v_mul_f32 v128, v10, v128\n"
  "v_mul_f32 v129, v10, v129\n"
  "v_mul_f32 v130, v10, v130\n"
  "v_mul_f32 v131, v10, v131\n"
  "v_mul_f32 v136, v10, v136\n"
  "v_mul_f32 v137, v10, v137\n"
  "v_cvt_pk_bf16_f32 v152, v124, v125\n"
  "v_cvt_pk_bf16_f32 v153, v126, v127\n"
  "v_cvt_pk_bf16_f32 v154, v128, v129\n"
  "v_cvt_pk_bf16_f32 v155, v130, v131\n"
  "v_cvt_pk_bf16_f32 v172, v136, v137\n"
  "v_cvt_pk_bf16_f32 v177, v136, v137\n"
  "v_cvt_pk_bf16_f32 v182, v136, v137\n"
  "v_cvt_pk_bf16_f32 v187, v136, v137\n"
  "v_add_u32 v7, s56, v2\n"
  "v_add_u32 v7, 1, v7\n"
  "v_min_i32 v7, 64, v7\n"
  "s_waitcnt vmcnt(24)\n"
  "v_cmp_gt_i32 s[16:17], v7, v9\n"
  "v_mov_b32 v7, 0\n"
  "v_mov_b32 v8, s56\n"
  "s_mov_b64 exec, 1\n"
  "global_store_dword v7, v8, s[70:71] sc1\n"
  "s_mov_b64 exec, -1\n"
  "s_cmp_lg_u64 s[16:17], 0\n"
  "s_cbranch_scc1 TNM0\n"
  "global_load_dwordx4 v[80:83], v0, s[64:65] offset:0 sc1\n"
  "global_load_dwordx4 v[84:87], v0, s[64:65] offset:1024 sc1\n"
  "global_load_dwordx4 v[104:107], v3, s[66:67] offset:0 sc1\n"
  "global_load_dwordx4 v[108:111], v3, s[66:67] offset:1024 sc1\n"
  "s_add_u32 s64, s64, 65536\n"
  "s_addc_u32 s65, s65, 0\n"
  "s_add_u32 s66, s66, 65536\n"
  "s_addc_u32 s67, s67, 0\n"
  "TNM0:\n"
  "v_cvt_pk_bf16_f32 v220, v188, v189\n"
  "v_cvt_pk_bf16_f32 v221, v190, v191\n"
  "v_cvt_pk_bf16_f32 v222, v192, v193\n"
  "v_cvt_pk_bf16_f32 v223, v194, v195\n"
  "global_store_dwordx4 v0, v[220:223], s[68:69] offset:-4096 sc1\n"
  "v_mfma_f32_16x16x32_bf16 v[188:191], a[32:35], v[152:155], 0\n"
  "v_cvt_pk_bf16_f32 v224, v196, v197\n"
  "v_cvt_pk_bf16_f32 v225, v198, v199\n"
  "v_mfma_f32_16x16x32_bf16 v[188:191], v[96:99], v[172:175], v[188:191]\n"
  "v_cvt_pk_bf16_f32 v226, v200, v201\n"
  "v_cvt_pk_bf16_f32 v227, v202, v203\n"
  "v_mfma_f32_16x16x32_bf16 v[192:195], a[36:39], v[152:155], 0\n"
  "global_store_dwordx4 v0, v[224:227], s[68:69] offset:-3072 sc1\n"
  "v_mfma_f32_16x16x32_bf16 v[192:195], v[96:99], v[176:179], v[192:195]\n"
  "v_mfma_f32_16x16x32_bf16 v[196:199], a[40:43], v[152:155], 0\n"
  "v_cvt_pk_bf16_f32 v228, v204, v205\n"
  "v_cvt_pk_bf16_f32 v229, v206, v207\n"
  "v_mfma_f32_16x16x32_bf16 v[196:199], v[96:99], v[180:183], v[196:199]\n"
  "v_cvt_pk_bf16_f32 v230, v208, v209\n"
  "v_cvt_pk_bf16_f32 v231, v210, v211\n"
  "v_mfma_f32_16x16x32_bf16 v[200:203], a[44:47], v[152:155], 0\n"
  "global_store_dwordx4 v0, v[228:231], s[68:69] offset:-2048 sc1\n"
  "v_mfma_f32_16x16x32_bf16 v[200:203], v[96:99], v[184:187], v[200:203]\n"
  "v_mfma_f32_16x16x32_bf16 v[204:207], a[48:51], v[152:155], 0\n"
  "v_cvt_pk_bf16_f32 v232, v212, v213\n"
  "v_cvt_pk_bf16_f32 v233, v214, v215\n"
  "v_mfma_f32_16x16x32_bf16 v[204:207], v[100:103], v[172:175], v[204:207]\n"
  "v_cvt_pk_bf16_f32 v234, v216, v217\n"
  "v_cvt_pk_bf16_f32 v235, v218, v219\n"
  "v_mfma_f32_16x16x32_bf16 v[208:211], a[52:55], v[152:155], 0\n"
  "global_store_dwordx4 v0, v[232:235], s[68:69] offset:-1024 sc1\n"
  "v_mfma_f32_16x16x32_bf16 v[208:211], v[100:103], v[176:179], v[208:211]\n"
  "v_mfma_f32_16x16x32_bf16 v[212:215], a[56:59], v[152:155], 0\n"
  "v_cvt_pk_bf16_f32 v220, v188, v189\n"
  "v_cvt_pk_bf16_f32 v221, v190, v191\n"
  "v_mfma_f32_16x16x32_bf16 v[212:215], v[100:103], v[180:183], v[212:215]\n"
  "v_cvt_pk_bf16_f32 v222, v192, v193\n"
  "v_cvt_pk_bf16_f32 v223, v194, v195\n"
  "v_mfma_f32_16x16x32_bf16 v[216:219], a[60:63], v[152:155], 0\n"
  "global_store_dwordx4 v0, v[220:223], s[68:69] offset:0 sc1\n"
  "v_mfma_f32_16x16x32_bf16 v[216:219], v[100:103], v[184:187], v[216:219]\n"
  "v_cvt_pk_bf16_f32 v224, v196, v197\n"
  "v_cvt_pk_bf16_f32 v225, v198, v199\n"
  "v_cvt_pk_bf16_f32 v226, v200, v201\n"
  "v_cvt_pk_bf16_f32 v227, v202, v203\n"
  "global_store_dwordx4 v0, v[224:227], s[68:69] offset:1024 sc1\n"
  "v_cvt_pk_bf16_f32 v228, v204, v205\n"
  "v_cvt_pk_bf16_f32 v229, v206, v207\n"
  "v_cvt_pk_bf16_f32 v230, v208, v209\n"
  "v_cvt_pk_bf16_f32 v231, v210, v211\n"
  "global_store_dwordx4 v0, v[228:231], s[68:69] offset:2048 sc1\n"
  "v_cvt_pk_bf16_f32 v232, v212, v213\n"
  "v_cvt_pk_bf16_f32 v233, v214, v215\n"
  "v_cvt_pk_bf16_f32 v234, v216, v217\n"
  "v_cvt_pk_bf16_f32 v235, v218, v219\n"
  "global_store_dwordx4 v0, v[232:235], s[68:69] offset:3072 sc1\n"
  "s_add_u32 s56, s56, 1\n"
  "TOP1:\n"
  "s_waitcnt vmcnt(8)\n"
  "ds_write_b128 v4, v[80:83] offset:0\n"
  "ds_write_b128 v4, v[84:87] offset:1024\n"
  "ds_read_b64_tr_b16 v[88:89], v5 offset:0\n"
  "ds_read_b64_tr_b16 v[90:91], v5 offset:512\n"
  "ds_read_b64_tr_b16 v[92:93], v5 offset:1024\n"
  "ds_read_b64_tr_b16 v[94:95], v5 offset:1536\n"
  "v_mfma_f32_16x16x32_bf16 v[120:123], a[0:3], v[48:51], 0\n"
  "v_mfma_f32_16x16x32_bf16 v[124:127], a[0:3], v[52:55], 0\n"
  "v_mfma_f32_16x16x32_bf16 v[128:131], a[4:7], v[52:55], 0\n"
  "v_mfma_f32_16x16x32_bf16 v[120:123], a[8:11], v[56:59], v[120:123]\n"
  "v_mfma_f32_16x16x32_bf16 v[124:127], a[8:11], v[60:63], v[124:127]\n"
  "v_mfma_f32_16x16x32_bf16 v[128:131], a[12:15], v[60:63], v[128:131]\n"
  "v_mfma_f32_16x16x32_bf16 v[120:123], a[16:19], v[64:67], v[120:123]\n"
  "v_mfma_f32_16x16x32_bf16 v[124:127], a[16:19], v[68:71], v[124:127]\n"
  "v_mfma_f32_16x16x32_bf16 v[128:131], a[20:23], v[68:71], v[128:131]\n"
  "v_mfma_f32_16x16x32_bf16 v[120:123], a[24:27], v[72:75], v[120:123]\n"
  "v_mfma_f32_16x16x32_bf16 v[124:127], a[24:27], v[76:79], v[124:127]\n"
  "v_mfma_f32_16x16x32_bf16 v[128:131], a[28:31], v[76:79], v[128:131]\n"
  "s_cmp_eq_u64 s[16:17], 0\n"
  "s_cbranch_scc1 TGO1\n"
  "s_waitcnt vmcnt(0)\n"
  "v_mov_b32 v7, 0\n"
  "v_mov_b32 v8, s56\n"
  "s_mov_b64 exec, 1\n"
  "global_store_dword v7, v8, s[70:71] sc1\n"
  "s_mov_b64 exec, -1\n"
  "TP1:\n"
  "global_load_dword v9, v1, s[36:37] sc1\n"
  "v_add_u32 v7, s56, v2\n"
  "v_add_u32 v7, 0, v7\n"
  "v_min_i32 v7, 64, v7\n"
  "s_waitcnt vmcnt(0)\n"
  "v_cmp_gt_i32 vcc, v7, v9\n"
  "s_cbranch_vccz TG1\n"
  "s_sub_u32 s57, s57, 1\n"
  "s_cbranch_scc1 TQUIT\n"
  "s_sleep 2\n"
  "s_branch TP1\n"
  "TG1:\n"
  "global_load_dwordx4 v[80:83], v0, s[64:65] offset:0 sc1\n"
  "global_load_dwordx4 v[84:87], v0, s[64:65] offset:1024 sc1\n"
  "global_load_dwordx4 v[104:107], v3, s[66:67] offset:0 sc1\n"
  "global_load_dwordx4 v[108:111], v3, s[66:67] offset:1024 sc1\n"
  "s_add_u32 s64, s64, 65536\n"
  "s_addc_u32 s65, s65, 0\n"
  "s_add_u32 s66, s66, 65536\n"
  "s_addc_u32 s67, s67, 0\n"
  "s_waitcnt vmcnt(3)\n"
  "ds_write_b128 v4, v[80:83] offset:0\n"
  "s_waitcnt vmcnt(2)\n"
  "ds_write_b128 v4, v[84:87] offset:1024\n"
  "s_waitcnt lgkmcnt(5)\n"
  "ds_read_b64_tr_b16 v[88:89], v5 offset:0\n"
  "s_waitcnt lgkmcnt(5)\n"
  "ds_read_b64_tr_b16 v[90:91], v5 offset:512\n"
  "s_waitcnt lgkmcnt(5)\n"
  "ds_read_b64_tr_b16 v[92:93], v5 offset:1024\n"
  "s_waitcnt lgkmcnt(5)\n"
  "ds_read_b64_tr_b16 v[94:95], v5 offset:1536\n"
  "s_waitcnt vmcnt(0)\n"
  "TGO1:\n"
  "s_waitcnt lgkmcnt(3)\n"
  "v_mfma_f32_16x16x16_bf16 v[132:135], v[88:89], v[48:49], 0\n"
  "global_load_dword v9, v1, s[36:37] sc1\n"
  "v_mfma_f32_16x16x16_bf16 v[140:143], v[88:89], v[50:51], 0\n"
  "global_load_dwordx4 v[16:19], v0, s[62:63] offset:-4096 sc1\n"
  "s_waitcnt lgkmcnt(2)\n"
  "v_mfma_f32_16x16x16_bf16 v[132:135], v[90:91], v[56:57], v[132:135]\n"
  "global_load_dwordx4 v[20:23], v0, s[62:63] offset:-3072 sc1\n"
  "v_mfma_f32_16x16x16_bf16 v[140:143], v[90:91], v[58:59], v[140:143]\n"
  "global_load_dwordx4 v[24:27], v0, s[62:63] offset:-2048 sc1\n"
  "s_waitcnt lgkmcnt(1)\n"
  "v_mfma_f32_16x16x16_bf16 v[132:135], v[92:93], v[64:65], v[132:135]\n"
  "global_load_dwordx4 v[28:31], v0, s[62:63] offset:-1024 sc1\n"
  "v_mfma_f32_16x16x16_bf16 v[140:143], v[92:93], v[66:67], v[140:143]\n"
  "global_load_dwordx4 v[32:35], v0, s[62:63] offset:0 sc1\n"
  "s_waitcnt lgkmcnt(0)\n"
  "v_mfma_f32_16x16x16_bf16 v[132:135], v[94:95], v[72:73], v[132:135]\n"
  "global_load_dwordx4 v[36:39], v0, s[62:63] offset:1024 sc1\n"
  "v_mfma_f32_16x16x16_bf16 v[140:143], v[94:95], v[74:75], v[140:143]\n"
  "global_load_dwordx4 v[40:43], v0, s[62:63] offset:2048 sc1\n"
  "v_mfma_f32_16x16x16_bf16 v[136:139], v[88:89], v[52:53], 0\n"
  "global_load_dwordx4 v[44:47], v0, s[62:63] offset:3072 sc1\n"
  "v_mfma_f32_16x16x16_bf16 v[144:147], v[88:89], v[54:55], 0\n"
  "global_load_dwordx4 a[0:3], v0, s[60:61] offset:-4096 sc1\n"
  "v_mfma_f32_16x16x16_bf16 v[136:139], v[90:91], v[60:61], v[136:139]\n"
  "global_load_dwordx4 a[4:7], v0, s[60:61] offset:-3072 sc1\n"
  "v_mfma_f32_16x16x16_bf16 v[144:147], v[90:91], v[62:63], v[144:147]\n"
  "global_load_dwordx4 a[8:11], v0, s[60:61] offset:-2048 sc1\n"
  "v_mfma_f32_16x16x16_bf16 v[136:139], v[92:93], v[68:69], v[136:139]\n"
  "v_cndmask_b32 v120, v12, v120, s[8:9]\n"
  "v_cndmask_b32 v121, v12, v121, s[10:11]\n"
  "v_mfma_f32_16x16x16_bf16 v[144:147], v[92:93], v[70:71], v[144:147]\n"
  "v_cndmask_b32 v122, v12, v122, s[12:13]\n"
  "v_cndmask_b32 v123, v12, v123, s[14:15]\n"
  "v_mfma_f32_16x16x16_bf16 v[136:139], v[94:95], v[76:77], v[136:139]\n"
  "v_cndmask_b32 v128, v12, v128, s[8:9]\n"
  "v_cndmask_b32 v129, v12, v129, s[10:11]\n"
  "v_mfma_f32_16x16x16_bf16 v[144:147], v[94:95], v[78:79], v[144:147]\n"
  "v_cndmask_b32 v130, v12, v130, s[12:13]\n"
  "v_cndmask_b32 v131, v12, v131, s[14:15]\n"
  "v_add_f32 v132, v132, v142\n"
  "v_add_f32 v133, v133, v143\n"
  "v_mul_f32 v132, s50, v132\n"
  "v_mul_f32 v133, s50, v133\n"
  "v_max_f32 v10, v132, v133\n"
  "v_max3_f32 v10, v10, v120, v121\n"
  "v_max3_f32 v10, v10, v122, v123\n"
  "v_mov_b32 v11, v10\n"
  "s_nop 1\n"
  "v_permlane16_swap_b32 v10, v11\n"
  "v_max_f32 v10, v10, v11\n"
  "v_mov_b32 v11, v10\n"
  "s_nop 1\n"
  "v_permlane32_swap_b32 v10, v11\n"
  "v_max_f32 v10, v10, v11\n"
  "v_sub_f32 v120, v120, v10\n"
  "v_sub_f32 v121, v121, v10\n"
  "v_sub_f32 v122, v122, v10\n"
  "v_sub_f32 v123, v123, v10\n"
  "v_sub_f32 v132, v132, v10\n"
  "v_sub_f32 v133, v133, v10\n"
  "v_exp_f32 v120, v120\n"
  "v_exp_f32 v121, v121\n"
  "v_exp_f32 v122, v122\n"
  "v_exp_f32 v123, v123\n"
  "v_exp_f32 v132, v132\n"
  "v_exp_f32 v133, v133\n"
  "v_add_f32 v10, v120, v121\n"
  "v_add_f32 v10, v10, v122\n"
  "v_add_f32 v10, v10, v123\n"
  "v_add_f32 v10, v10, v132\n"
  "v_add_f32 v10, v10, v133\n"
  "v_mov_b32 v11, v10\n"
  "s_nop 1\n"
  "v_permlane16_swap_b32 v10, v11\n"
  "v_add_f32 v10, v10, v11\n"
  "v_mov_b32 v11, v10\n"
  "s_nop 1\n"
  "v_permlane32_swap_b32 v10, v11\n"
  "v_add_f32 v10, v10, v11\n"
  "v_rcp_f32 v10, v10\n"
  "s_nop 0\n"
  "v_mul_f32 v120, v10, v120\n"
  "v_mul_f32 v121, v10, v121\n"
  "v_mul_f32 v122, v10, v122\n"
  "v_mul_f32 v123, v10, v123\n"
  "v_mul_f32 v132, v10, v132\n"
  "v_mul_f32 v133, v10, v133\n"
  "v_cvt_pk_bf16_f32 v148, v120, v121\n"
  "v_cvt_pk_bf16_f32 v149, v122, v123\n"
  "v_cvt_pk_bf16_f32 v156, v132, v133\n"
  "v_cvt_pk_bf16_f32 v161, v132, v133\n"
  "v_cvt_pk_bf16_f32 v166, v132, v133\n"
  "v_cvt_pk_bf16_f32 v171, v132, v133\n"
  "v_mfma_f32_16x16x32_bf16 v[188:191], a[64:67], v[148:151], 0\n"
  "global_load_dwordx4 a[12:15], v0, s[60:61] offset:-1024 sc1\n"
  "v_mfma_f32_16x16x32_bf16 v[188:191], v[104:107], v[156:159], v[188:191]\n"
  "global_load_dwordx4 a[16:19], v0, s[60:61] offset:0 sc1\n"
  "v_mfma_f32_16x16x32_bf16 v[192:195], a[68:71], v[148:151], 0\n"
  "global_load_dwordx4 a[20:23], v0, s[60:61] offset:1024 sc1\n"
  "v_mfma_f32_16x16x32_bf16 v[192:195], v[104:107], v[160:163], v[192:195]\n"
  "global_load_dwordx4 a[24:27], v0, s[60:61] offset:2048 sc1\n"
  "v_mfma_f32_16x16x32_bf16 v[196:199], a[72:75], v[148:151], 0\n"
  "global_load_dwordx4 a[28:31], v0, s[60:61] offset:3072 sc1\n"
  "v_mfma_f32_16x16x32_bf16 v[196:199], v[104:107], v[164:167], v[196:199]\n"
  "s_add_u32 s60, s60, 16384\n"
  "s_addc_u32 s61, s61, 0\n"
  "global_load_dwordx4 a[32:35], v0, s[60:61] offset:-4096 sc1\n"
  "v_mfma_f32_16x16x32_bf16 v[200:203], a[76:79], v[148:151], 0\n"
  "global_load_dwordx4 a[36:39], v0, s[60:61] offset:-3072 sc1\n"
  "v_mfma_f32_16x16x32_bf16 v[200:203], v[104:107], v[168:171], v[200:203]\n"
  "global_load_dwordx4 a[40:43], v0, s[60:61] offset:-2048 sc1\n"
  "v_mfma_f32_16x16x32_bf16 v[204:207], a[80:83], v[148:151], 0\n"
  "global_load_dwordx4 a[44:47], v0, s[60:61] offset:-1024 sc1\n"
  "v_mfma_f32_16x16x32_bf16 v[204:207], v[108:111], v[156:159], v[204:207]\n"
  "global_load_dwordx4 a[48:51], v0, s[60:61] offset:0 sc1\n"
  "v_mfma_f32_16x16x32_bf16 v[208:211], a[84:87], v[148:151], 0\n"
  "global_load_dwordx4 a[52:55], v0, s[60:61] offset:1024 sc1\n"
  "v_mfma_f32_16x16x32_bf16 v[208:211], v[108:111], v[160:163], v[208:211]\n"
  "global_load_dwordx4 a[56:59], v0, s[60:61] offset:2048 sc1\n"
  "v_mfma_f32_16x16x32_bf16 v[212:215], a[88:91], v[148:151], 0\n"
  "global_load_dwordx4 a[60:63], v0, s[60:61] offset:3072 sc1\n"
  "v_mfma_f32_16x16x32_bf16 v[212:215], v[108:111], v[164:167], v[212:215]\n"
  "v_mfma_f32_16x16x32_bf16 v[216:219], a[92:95], v[148:151], 0\n"
  "v_mfma_f32_16x16x32_bf16 v[216:219], v[108:111], v[168:171], v[216:219]\n"
  "s_add_u32 s76, s56, 2\n"
  "s_min_u32 s76, s76, 63\n"
  "s_bitcmp1_b64 s[4:5], s76\n"
  "s_cselect_b32 s74, 8192, 0\n"
  "s_lshl_b32 s75, s76, 21\n"
  "s_add_u32 s74, s74, s75\n"
  "s_add_u32 s74, s74, 20971520\n"
  "s_xor_b32 s75, s74, s44\n"
  "s_add_u32 s60, s36, s75\n"
  "s_addc_u32 s61, s37, 0\n"
  "s_xor_b32 s75, s74, s45\n"
  "s_add_u32 s62, s36, s75\n"
  "s_addc_u32 s63, s37, 0\n"
  "s_bitcmp1_b64 s[4:5], s56\n"
  "s_cselect_b32 s74, 8192, 0\n"
  "s_lshl_b32 s75, s56, 21\n"
  "s_add_u32 s74, s74, s75\n"
  "s_add_u32 s74, s74, 20971520\n"
  "s_xor_b32 s75, s74, s48\n"
  "s_add_u32 s68, s36, s75\n"
  "s_addc_u32 s69, s37, 0\n"
  "v_add_f32 v136, v136, v146\n"
  "v_add_f32 v137, v137, v147\n"
  "v_mul_f32 v136, s50, v136\n"
  "v_mul_f32 v137, s50, v137\n"
  "v_max_f32 v10, v136, v137\n"
  "v_max3_f32 v10, v10, v124, v125\n"
  "v_max3_f32 v10, v10, v126, v127\n"
  "v_max3_f32 v10, v10, v128, v129\n"
  "v_max3_f32 v10, v10, v130, v131\n"
  "v_mov_b32 v11, v10\n"
  "s_nop 1\n"
  "v_permlane16_swap_b32 v10, v11\n"
  "v_max_f32 v10, v10, v11\n"
  "v_mov_b32 v11, v10\n"
  "s_nop 1\n"
  "v_permlane32_swap_b32 v10, v11\n"
  "v_max_f32 v10, v10, v11\n"
  "v_sub_f32 v124, v124, v10\n"
  "v_sub_f32 v125, v125, v10\n"
  "v_sub_f32 v126, v126, v10\n"
  "v_sub_f32 v127, v127, v10\n"
  "v_sub_f32 v128, v128, v10\n"
  "v_sub_f32 v129, v129, v10\n"
  "v_sub_f32 v130, v130, v10\n"
  "v_sub_f32 v131, v131, v10\n"
  "v_sub_f32 v136, v136, v10\n"
  "v_sub_f32 v137, v137, v10\n"
  "v_exp_f32 v124, v124\n"
  "v_exp_f32 v125, v125\n"
  "v_exp_f32 v126, v126\n"
  "v_exp_f32 v127, v127\n"
  "v_exp_f32 v128, v128\n"
  "v_exp_f32 v129, v129\n"
  "v_exp_f32 v130, v130\n"
  "v_exp_f32 v131, v131\n"
  "v_exp_f32 v136, v136\n"
  "v_exp_f32 v137, v137\n"
  "v_add_f32 v10, v124, v125\n"
  "v_add_f32 v10, v10, v126\n"
  "v_add_f32 v10, v10, v127\n"
  "v_add_f32 v10, v10, v128\n"
  "v_add_f32 v10, v10, v129\n"
  "v_add_f32 v10, v10, v130\n"
  "v_add_f32 v10, v10, v131\n"
  "v_add_f32 v10, v10, v136\n"
  "v_add_f32 v10, v10, v137\n"
  "v_mov_b32 v11, v10\n"
  "s_nop 1\n"
  "v_permlane16_swap_b32 v10, v11\n"
  "v_add_f32 v10, v10, v11\n"
  "v_mov_b32 v11, v10\n"
  "s_nop 1\n"
  "v_permlane32_swap_b32 v10, v11\n"
  "v_add_f32 v10, v10, v11\n"
  "v_rcp_f32 v10, v10\n"
  "s_nop 0\n"
  "v_mul_f32 v124, v10, v124\n"
  "v_mul_f32 v125, v10, v125\n"
  "v_mul_f32 v126, v10, v126\n"
  "v_mul_f32 v127, v10, v127\n"
  "v_mul_f32 v128, v10, v128\n"
  "v_mul_f32 v129, v10, v129\n"
  "v_mul_f32 v130, v10, v130\n"
  "v_mul_f32 v131, v10, v131\n"
  "v_mul_f32 v136, v10, v136\n"
  "v_mul_f32 v137, v10, v137\n"
  "v_cvt_pk_bf16_f32 v152, v124, v125\n"
  "v_cvt_pk_bf16_f32 v153, v126, v127\n"
  "v_cvt_pk_bf16_f32 v154, v128, v129\n"
  "v_cvt_pk_bf16_f32 v155, v130, v131\n"
  "v_cvt_pk_bf16_f32 v172, v136, v137\n"
  "v_cvt_pk_bf16_f32 v177, v136, v137\n"
  "v_cvt_pk_bf16_f32 v182, v136, v137\n"
  "v_cvt_pk_bf16_f32 v187, v136, v137\n"
  "v_add_u32 v7, s56, v2\n"
  "v_add_u32 v7, 1, v7\n"
  "v_min_i32 v7, 64, v7\n"
  "s_waitcnt vmcnt(24)\n"
  "v_cmp_gt_i32 s[16:17], v7, v9\n"
  "v_mov_b32 v7, 0\n"
  "v_mov_b32 v8, s56\n"
  "s_mov_b64 exec, 1\n"
  "global_store_dword v7, v8, s[70:71] sc1\n"
  "s_mov_b64 exec, -1\n"
  "s_cmp_lg_u64 s[16:17], 0\n"
  "s_cbranch_scc1 TNM1\n"
  "global_load_dwordx4 v[80:83], v0, s[64:65] offset:0 sc1\n"
  "global_load_dwordx4 v[84:87], v0, s[64:65] offset:1024 sc1\n"
  "global_load_dwordx4 v[96:99], v3, s[66:67] offset:0 sc1\n"
  "global_load_dwordx4 v[100:103], v3, s[66:67] offset:1024 sc1\n"
  "s_add_u32 s64, s64, 65536\n"
  "s_addc_u32 s65, s65, 0\n"
  "s_add_u32 s66, s66, 65536\n"
  "s_addc_u32 s67, s67, 0\n"
  "TNM1:\n"
  "v_cvt_pk_bf16_f32 v220, v188, v189\n"
  "v_cvt_pk_bf16_f32 v221, v190, v191\n"
  "v_cvt_pk_bf16_f32 v222, v192, v193\n"
  "v_cvt_pk_bf16_f32 v223, v194, v195\n"
  "global_store_dwordx4 v0, v[220:223], s[68:69] offset:-4096 sc1\n"
  "v_mfma_f32_16x16x32_bf16 v[188:191], a[64:67], v[152:155], 0\n"
  "v_cvt_pk_bf16_f32 v224, v196, v197\n"
  "v_cvt_pk_bf16_f32 v225, v198, v199\n"
  "v_mfma_f32_16x16x32_bf16 v[188:191], v[104:107], v[172:175], v[188:191]\n"
  "v_cvt_pk_bf16_f32 v226, v200, v201\n"
  "v_cvt_pk_bf16_f32 v227, v202, v203\n"
  "v_mfma_f32_16x16x32_bf16 v[192:195], a[68:71], v[152:155], 0\n"
  "global_store_dwordx4 v0, v[224:227], s[68:69] offset:-3072 sc1\n"
  "v_mfma_f32_16x16x32_bf16 v[192:195], v[104:107], v[176:179], v[192:195]\n"
  "v_mfma_f32_16x16x32_bf16 v[196:199], a[72:75], v[152:155], 0\n"
  "v_cvt_pk_bf16_f32 v228, v204, v205\n"
  "v_cvt_pk_bf16_f32 v229, v206, v207\n"
  "v_mfma_f32_16x16x32_bf16 v[196:199], v[104:107], v[180:183], v[196:199]\n"
  "v_cvt_pk_bf16_f32 v230, v208, v209\n"
  "v_cvt_pk_bf16_f32 v231, v210, v211\n"
  "v_mfma_f32_16x16x32_bf16 v[200:203], a[76:79], v[152:155], 0\n"
  "global_store_dwordx4 v0, v[228:231], s[68:69] offset:-2048 sc1\n"
  "v_mfma_f32_16x16x32_bf16 v[200:203], v[104:107], v[184:187], v[200:203]\n"
  "v_mfma_f32_16x16x32_bf16 v[204:207], a[80:83], v[152:155], 0\n"
  "v_cvt_pk_bf16_f32 v232, v212, v213\n"
  "v_cvt_pk_bf16_f32 v233, v214, v215\n"
  "v_mfma_f32_16x16x32_bf16 v[204:207], v[108:111], v[172:175], v[204:207]\n"
  "v_cvt_pk_bf16_f32 v234, v216, v217\n"
  "v_cvt_pk_bf16_f32 v235, v218, v219\n"
  "v_mfma_f32_16x16x32_bf16 v[208:211], a[84:87], v[152:155], 0\n"
  "global_store_dwordx4 v0, v[232:235], s[68:69] offset:-1024 sc1\n"
  "v_mfma_f32_16x16x32_bf16 v[208:211], v[108:111], v[176:179], v[208:211]\n"
  "v_mfma_f32_16x16x32_bf16 v[212:215], a[88:91], v[152:155], 0\n"
  "v_cvt_pk_bf16_f32 v220, v188, v189\n"
  "v_cvt_pk_bf16_f32 v221, v190, v191\n"
  "v_mfma_f32_16x16x32_bf16 v[212:215], v[108:111], v[180:183], v[212:215]\n"
  "v_cvt_pk_bf16_f32 v222, v192, v193\n"
  "v_cvt_pk_bf16_f32 v223, v194, v195\n"
  "v_mfma_f32_16x16x32_bf16 v[216:219], a[92:95], v[152:155], 0\n"
  "global_store_dwordx4 v0, v[220:223], s[68:69] offset:0 sc1\n"
  "v_mfma_f32_16x16x32_bf16 v[216:219], v[108:111], v[184:187], v[216:219]\n"
  "v_cvt_pk_bf16_f32 v224, v196, v197\n"
  "v_cvt_pk_bf16_f32 v225, v198, v199\n"
  "v_cvt_pk_bf16_f32 v226, v200, v201\n"
  "v_cvt_pk_bf16_f32 v227, v202, v203\n"
  "global_store_dwordx4 v0, v[224:227], s[68:69] offset:1024 sc1\n"
  "v_cvt_pk_bf16_f32 v228, v204, v205\n"
  "v_cvt_pk_bf16_f32 v229, v206, v207\n"
  "v_cvt_pk_bf16_f32 v230, v208, v209\n"
  "v_cvt_pk_bf16_f32 v231, v210, v211\n"
  "global_store_dwordx4 v0, v[228:231], s[68:69] offset:2048 sc1\n"
  "v_cvt_pk_bf16_f32 v232, v212, v213\n"
  "v_cvt_pk_bf16_f32 v233, v214, v215\n"
  "v_cvt_pk_bf16_f32 v234, v216, v217\n"
  "v_cvt_pk_bf16_f32 v235, v218, v219\n"
  "global_store_dwordx4 v0, v[232:235], s[68:69] offset:3072 sc1\n"
  "s_add_u32 s56, s56, 1\n"
  "s_cmp_lt_u32 s56, 64\n"
  "s_cbranch_scc1 TOP0\n"
  "TEND:\n"
  "s_waitcnt vmcnt(0)\n"
  "v_mov_b32 v7, 0\n"
  "v_mov_b32 v8, s56\n"
  "s_mov_b64 exec, 1\n"
  "global_store_dword v7, v8, s[70:71] sc1\n"
  "s_mov_b64 exec, -1\n"
  "TQUIT:\n"
  "s_endpgm\n"
      : "+{v[0:15]}"(vin), "+{s[36:43]}"(sa), "+{s[44:59]}"(sb) : "{s[4:5]}"(pol) : CLOB);
  } else if (tk < 25) {
    const int id = fd * 9 + tk - 16, set = id / 12, cg = id % 12;
    v16u wa;
#pragma unroll
    for (int i = 0; i < 16; ++i) {
      const int gr = 4 * (i >> 1) + g, k = 8 * (gr % 5) + 4 * (i & 1) + qq;
      wa[i] = wave * 39936 + (36 * (gr / 5) + 18 * (k / 20) + min(k % 20, 17)) * 96 + c4 * 8;
    }
    const int r4 = min(lane / 12, 3), cq = lane % 12;
    vin[1] = (lane & 8) ? (C_AP ^ cx) - CTL + (4 * wave + c4) * 128 : (C_QAP ^ cx) - CTL + (8 * wave + (lane & 7)) * 128;
    vin[2] = wave * 512 + (4 * wave + r4) * 4;
    vin[3] = 73728 + (cq >> 2) * 1024 + (16 * (cq & 3) + 4 * wave + r4) * 16;
    vin[4] = ((4 * wave + r4) * DM + 48 * cg + 4 * cq) * 2;
    vin[5] = wave * 39936 + (4 * g + qq) * 96 + c4 * 8;
#pragma unroll
    for (int j = 0; j < 3; ++j) vin[6 + j] = ((64 * j + lane) / 6) * 1152 + ((64 * j + lane) % 6) * 16;
    PTR(sa, 2, p.Wo + (size_t)(wave * 8 + (id & 7)) * 128 * DM + 48 * cg); PTR(sa, 4, p.out + set * 16 * DM); PTR(sa, 6, p.wo_a + wave * 144 * DM + 48 * cg);
    sb[0] = (wave * 8 + (id & 7)) * 65536 + 49152; sb[1] = WO_OA + wave * 5120 + 2048; sb[2] = WO_G; sb[3] = wave;
    sb[4] = WO_ST + (role * 4 + wave) * 4096; sb[5] = set; sb[6] = 7 - (id & 7);
    UNIFORM(sa, 8); UNIFORM(sb, 16);
    asm volatile(
  "s_nop 4\n"
  "s_mul_i32 s80, s47, 39936\n"
  "s_add_u32 m0, s80, 0\n"
  "s_nop 0\n"
  "global_load_lds_dwordx4 v6, s[42:43]\n"
  "s_add_u32 m0, s80, 1024\n"
  "s_nop 0\n"
  "global_load_lds_dwordx4 v7, s[42:43]\n"
  "s_add_u32 m0, s80, 2048\n"
  "s_nop 0\n"
  "global_load_lds_dwordx4 v8, s[42:43]\n"
  "s_add_u32 s42, s42, 36864\n"
  "s_addc_u32 s43, s43, 0\n"
  "s_add_u32 m0, s80, 3072\n"
  "s_nop 0\n"
  "global_load_lds_dwordx4 v6, s[42:43]\n"
  "s_add_u32 m0, s80, 4096\n"
  "s_nop 0\n"
  "global_load_lds_dwordx4 v7, s[42:43]\n"
  "s_add_u32 m0, s80, 5120\n"
  "s_nop 0\n"
  "global_load_lds_dwordx4 v8, s[42:43]\n"
  "s_add_u32 s42, s42, 36864\n"
  "s_addc_u32 s43, s43, 0\n"
  "s_add_u32 m0, s80, 6144\n"
  "s_nop 0\n"
  "global_load_lds_dwordx4 v6, s[42:43]\n"
  "s_add_u32 m0, s80, 7168\n"
  "s_nop 0\n"
  "global_load_lds_dwordx4 v7, s[42:43]\n"
  "s_add_u32 m0, s80, 8192\n"
  "s_nop 0\n"
  "global_load_lds_dwordx4 v8, s[42:43]\n"
  "s_add_u32 s42, s42, 36864\n"
  "s_addc_u32 s43, s43, 0\n"
  "s_add_u32 m0, s80, 9216\n"
  "s_nop 0\n"
  "global_load_lds_dwordx4 v6, s[42:43]\n"
  "s_add_u32 m0, s80, 10240\n"
  "s_nop 0\n"
  "global_load_lds_dwordx4 v7, s[42:43]\n"
  "s_add_u32 m0, s80, 11264\n"
  "s_nop 0\n"
  "global_load_lds_dwordx4 v8, s[42:43]\n"
  "s_add_u32 s42, s42, 36864\n"
  "s_addc_u32 s43, s43, 0\n"
  "s_add_u32 m0, s80, 12288\n"
  "s_nop 0\n"
  "global_load_lds_dwordx4 v6, s[42:43]\n"
  "s_add_u32 m0, s80, 13312\n"
  "s_mov_b32 exec_hi, 0\n"
  "global_load_lds_dwordx4 v7, s[42:43]\n"
  "s_mov_b64 exec, -1\n"
  "s_add_u32 m0, s80, 15360\n"
  "s_nop 0\n"
  "global_load_lds_dwordx4 v6, s[38:39]\n"
  "s_add_u32 m0, s80, 16384\n"
  "s_nop 0\n"
  "global_load_lds_dwordx4 v7, s[38:39]\n"
  "s_add_u32 m0, s80, 17408\n"
  "s_nop 0\n"
  "global_load_lds_dwordx4 v8, s[38:39]\n"
  "s_add_u32 s38, s38, 36864\n"
  "s_addc_u32 s39, s39, 0\n"
  "s_add_u32 m0, s80, 18432\n"
  "s_nop 0\n"
  "global_load_lds_dwordx4 v6, s[38:39]\n"
  "s_add_u32 m0, s80, 19456\n"
  "s_nop 0\n"
  "global_load_lds_dwordx4 v7, s[38:39]\n"
  "s_add_u32 m0, s80, 20480\n"
  "s_nop 0\n"
  "global_load_lds_dwordx4 v8, s[38:39]\n"
  "s_add_u32 s38, s38, 36864\n"
  "s_addc_u32 s39, s39, 0\n"
  "s_add_u32 m0, s80, 21504\n"
  "s_nop 0\n"
  "global_load_lds_dwordx4 v6, s[38:39]\n"
  "s_add_u32 m0, s80, 22528\n"
  "s_nop 0\n"
  "global_load_lds_dwordx4 v7, s[38:39]\n"
  "s_add_u32 m0, s80, 23552\n"
  "s_nop 0\n"
  "global_load_lds_dwordx4 v8, s[38:39]\n"
  "s_add_u32 s38, s38, 36864\n"
  "s_addc_u32 s39, s39, 0\n"
  "s_add_u32 m0, s80, 24576\n"
  "s_nop 0\n"
  "global_load_lds_dwordx4 v6, s[38:39]\n"
  "s_add_u32 m0, s80, 25600\n"
  "s_nop 0\n"
  "global_load_lds_dwordx4 v7, s[38:39]\n"
  "s_add_u32 m0, s80, 26624\n"
  "s_nop 0\n"
  "global_load_lds_dwordx4 v8, s[38:39]\n"
  "s_add_u32 s38, s38, 36864\n"
  "s_addc_u32 s39, s39, 0\n"
  "s_cmp_eq_u32 s50, 0\n"
  "s_cselect_b32 s74, 1179648, 0\n"
  "s_sub_u32 s38, s38, s74\n"
  "s_subb_u32 s39, s39, 0\n"
  "s_add_u32 m0, s80, 27648\n"
  "s_nop 0\n"
  "global_load_lds_dwordx4 v6, s[38:39]\n"
  "s_add_u32 m0, s80, 28672\n"
  "s_nop 0\n"
  "global_load_lds_dwordx4 v7, s[38:39]\n"
  "s_add_u32 m0, s80, 29696\n"
  "s_nop 0\n"
  "global_load_lds_dwordx4 v8, s[38:39]\n"
  "s_add_u32 s38, s38, 36864\n"
  "s_addc_u32 s39, s39, 0\n"
  "s_add_u32 m0, s80, 30720\n"
  "s_nop 0\n"
  "global_load_lds_dwordx4 v6, s[38:39]\n"
  "s_add_u32 m0, s80, 31744\n"
  "s_nop 0\n"
  "global_load_lds_dwordx4 v7, s[38:39]\n"
  "s_add_u32 m0, s80, 32768\n"
  "s_nop 0\n"
  "global_load_lds_dwordx4 v8, s[38:39]\n"
  "s_add_u32 s38, s38, 36864\n"
  "s_addc_u32 s39, s39, 0\n"
  "s_add_u32 m0, s80, 33792\n"
  "s_nop 0\n"
  "global_load_lds_dwordx4 v6, s[38:39]\n"
  "s_add_u32 m0, s80, 34816\n"
  "s_nop 0\n"
  "global_load_lds_dwordx4 v7, s[38:39]\n"
  "s_add_u32 m0, s80, 35840\n"
  "s_nop 0\n"
  "global_load_lds_dwordx4 v8, s[38:39]\n"
  "s_add_u32 s38, s38, 36864\n"
  "s_addc_u32 s39, s39, 0\n"
  "s_waitcnt vmcnt(21)\n"
  "ds_read_b64_tr_b16 v[52:53], v16 offset:0\n"
  "ds_read_b64_tr_b16 v[54:55], v17 offset:0\n"
  "ds_read_b64_tr_b16 v[56:57], v16 offset:32\n"
  "ds_read_b64_tr_b16 v[58:59], v17 offset:32\n"
  "ds_read_b64_tr_b16 v[60:61], v16 offset:64\n"
  "ds_read_b64_tr_b16 v[62:63], v17 offset:64\n"
  "ds_read_b64_tr_b16 v[64:65], v18 offset:0\n"
  "ds_read_b64_tr_b16 v[66:67], v19 offset:0\n"
  "ds_read_b64_tr_b16 v[68:69], v18 offset:32\n"
  "ds_read_b64_tr_b16 v[70:71], v19 offset:32\n"
  "ds_read_b64_tr_b16 v[72:73], v18 offset:64\n"
  "ds_read_b64_tr_b16 v[74:75], v19 offset:64\n"
  "ds_read_b64_tr_b16 v[76:77], v20 offset:0\n"
  "ds_read_b64_tr_b16 v[78:79], v21 offset:0\n"
  "ds_read_b64_tr_b16 v[80:81], v20 offset:32\n"
  "ds_read_b64_tr_b16 v[82:83], v21 offset:32\n"
  "ds_read_b64_tr_b16 v[84:85], v20 offset:64\n"
  "ds_read_b64_tr_b16 v[86:87], v21 offset:64\n"
  "ds_read_b64_tr_b16 v[88:89], v22 offset:0\n"
  "ds_read_b64_tr_b16 v[90:91], v23 offset:0\n"
  "ds_read_b64_tr_b16 v[92:93], v22 offset:32\n"
  "ds_read_b64_tr_b16 v[94:95], v23 offset:32\n"
  "ds_read_b64_tr_b16 v[96:97], v22 offset:64\n"
  "ds_read_b64_tr_b16 v[98:99], v23 offset:64\n"
  "ds_read_b64_tr_b16 v[100:101], v24 offset:0\n"
  "ds_read_b64_tr_b16 v[102:103], v25 offset:0\n"
  "ds_read_b64_tr_b16 v[104:105], v24 offset:32\n"
  "ds_read_b64_tr_b16 v[106:107], v25 offset:32\n"
  "ds_read_b64_tr_b16 v[108:109], v24 offset:64\n"
  "ds_read_b64_tr_b16 v[110:111], v25 offset:64\n"
  "s_waitcnt lgkmcnt(0)\n"
  "s_add_u32 m0, s80, 36864\n"
  "s_nop 0\n"
  "global_load_lds_dwordx4 v6, s[38:39]\n"
  "s_add_u32 m0, s80, 37888\n"
  "s_nop 0\n"
  "global_load_lds_dwordx4 v7, s[38:39]\n"
  "s_add_u32 m0, s80, 38912\n"
  "s_nop 0\n"
  "global_load_lds_dwordx4 v8, s[38:39]\n"
  "s_add_u32 s38, s38, 36864\n"
  "s_addc_u32 s39, s39, 0\n"
  "s_cmp_eq_u32 s50, 1\n"
  "s_cselect_b32 s74, 1179648, 0\n"
  "s_sub_u32 s38, s38, s74\n"
  "s_subb_u32 s39, s39, 0\n"
  "s_add_u32 m0, s80, 0\n"
  "s_nop 0\n"
  "global_load_lds_dwordx4 v6, s[38:39]\n"
  "s_add_u32 m0, s80, 1024\n"
  "s_nop 0\n"
  "global_load_lds_dwordx4 v7, s[38:39]\n"
  "s_add_u32 m0, s80, 2048\n"
  "s_nop 0\n"
  "global_load_lds_dwordx4 v8, s[38:39]\n"
  "s_add_u32 s38, s38, 36864\n"
  "s_addc_u32 s39, s39, 0\n"
  "s_add_u32 m0, s80, 3072\n"
  "s_nop 0\n"
  "global_load_lds_dwordx4 v6, s[38:39]\n"
  "s_add_u32 m0, s80, 4096\n"
  "s_nop 0\n"
  "global_load_lds_dwordx4 v7, s[38:39]\n"
  "s_add_u32 m0, s80, 5120\n"
  "s_nop 0\n"
  "global_load_lds_dwordx4 v8, s[38:39]\n"
  "s_add_u32 s38, s38, 36864\n"
  "s_addc_u32 s39, s39, 0\n"
  "s_add_u32 m0, s80, 6144\n"
  "s_nop 0\n"
  "global_load_lds_dwordx4 v6, s[38:39]\n"
  "s_add_u32 m0, s80, 7168\n"
  "s_nop 0\n"
  "global_load_lds_dwordx4 v7, s[38:39]\n"
  "s_add_u32 m0, s80, 8192\n"
  "s_nop 0\n"
  "global_load_lds_dwordx4 v8, s[38:39]\n"
  "s_add_u32 s38, s38, 36864\n"
  "s_addc_u32 s39, s39, 0\n"
  "s_add_u32 m0, s80, 9216\n"
  "s_nop 0\n"
  "global_load_lds_dwordx4 v6, s[38:39]\n"
  "s_add_u32 m0, s80, 10240\n"
  "s_nop 0\n"
  "global_load_lds_dwordx4 v7, s[38:39]\n"
  "s_add_u32 m0, s80, 11264\n"
  "s_nop 0\n"
  "global_load_lds_dwordx4 v8, s[38:39]\n"
  "s_add_u32 s38, s38, 36864\n"
  "s_addc_u32 s39, s39, 0\n"
  "s_cmp_eq_u32 s50, 2\n"
  "s_cselect_b32 s74, 1179648, 0\n"
  "s_sub_u32 s38, s38, s74\n"
  "s_subb_u32 s39, s39, 0\n"
  "s_waitcnt vmcnt(33) lgkmcnt(0)\n"
  "s_add_u32 m0, s80, 12288\n"
  "s_nop 0\n"
  "global_load_lds_dwordx4 v6, s[38:39]\n"
  "s_add_u32 m0, s80, 13312\n"
  "s_nop 0\n"
  "global_load_lds_dwordx4 v7, s[38:39]\n"
  "s_add_u32 m0, s80, 14336\n"
  "s_nop 0\n"
  "global_load_lds_dwordx4 v8, s[38:39]\n"
  "s_add_u32 s38, s38, 36864\n"
  "s_addc_u32 s39, s39, 0\n"
  "ds_read_b64_tr_b16 a[0:1], v5 offset:15360\n"
  "ds_read_b64_tr_b16 a[2:3], v5 offset:16896\n"
  "ds_read_b64_tr_b16 a[4:5], v5 offset:15392\n"
  "ds_read_b64_tr_b16 a[6:7], v5 offset:16928\n"
  "ds_read_b64_tr_b16 a[8:9], v5 offset:15424\n"
  "ds_read_b64_tr_b16 a[10:11], v5 offset:16960\n"
  "s_waitcnt vmcnt(33) lgkmcnt(0)\n"
  "s_add_u32 m0, s80, 15360\n"
  "s_nop 0\n"
  "global_load_lds_dwordx4 v6, s[38:39]\n"
  "s_add_u32 m0, s80, 16384\n"
  "s_nop 0\n"
  "global_load_lds_dwordx4 v7, s[38:39]\n"
  "s_add_u32 m0, s80, 17408\n"
  "s_nop 0\n"
  "global_load_lds_dwordx4 v8, s[38:39]\n"
  "s_add_u32 s38, s38, 36864\n"
  "s_addc_u32 s39, s39, 0\n"
  "ds_read_b64_tr_b16 a[12:13], v5 offset:18432\n"
  "ds_read_b64_tr_b16 a[14:15], v5 offset:19968\n"
  "ds_read_b64_tr_b16 a[16:17], v5 offset:18464\n"
  "ds_read_b64_tr_b16 a[18:19], v5 offset:20000\n"
  "ds_read_b64_tr_b16 a[20:21], v5 offset:18496\n"
  "ds_read_b64_tr_b16 a[22:23], v5 offset:20032\n"
  "s_waitcnt vmcnt(33) lgkmcnt(0)\n"
  "s_add_u32 m0, s80, 18432\n"
  "s_nop 0\n"
  "global_load_lds_dwordx4 v6, s[38:39]\n"
  "s_add_u32 m0, s80, 19456\n"
  "s_nop 0\n"
  "global_load_lds_dwordx4 v7, s[38:39]\n"
  "s_add_u32 m0, s80, 20480\n"
  "s_nop 0\n"
  "global_load_lds_dwordx4 v8, s[38:39]\n"
  "s_add_u32 s38, s38, 36864\n"
  "s_addc_u32 s39, s39, 0\n"
  "ds_read_b64_tr_b16 a[24:25], v5 offset:21504\n"
  "ds_read_b64_tr_b16 a[26:27], v5 offset:23040\n"
  "ds_read_b64_tr_b16 a[28:29], v5 offset:21536\n"
  "ds_read_b64_tr_b16 a[30:31], v5 offset:23072\n"
  "ds_read_b64_tr_b16 a[32:33], v5 offset:21568\n"
  "ds_read_b64_tr_b16 a[34:35], v5 offset:23104\n"
  "s_waitcnt vmcnt(33) lgkmcnt(0)\n"
  "s_add_u32 m0, s80, 21504\n"
  "s_nop 0\n"
  "global_load_lds_dwordx4 v6, s[38:39]\n"
  "s_add_u32 m0, s80, 22528\n"
  "s_nop 0\n"
  "global_load_lds_dwordx4 v7, s[38:39]\n"
  "s_add_u32 m0, s80, 23552\n"
  "s_nop 0\n"
  "global_load_lds_dwordx4 v8, s[38:39]\n"
  "s_add_u32 s38, s38, 36864\n"
  "s_addc_u32 s39, s39, 0\n"
  "s_cmp_eq_u32 s50, 3\n"
  "s_cselect_b32 s74, 1179648, 0\n"
  "s_sub_u32 s38, s38, s74\n"
  "s_subb_u32 s39, s39, 0\n"
  "ds_read_b64_tr_b16 a[36:37], v5 offset:24576\n"
  "ds_read_b64_tr_b16 a[38:39], v5 offset:26112\n"
  "ds_read_b64_tr_b16 a[40:41], v5 offset:24608\n"
  "ds_read_b64_tr_b16 a[42:43], v5 offset:26144\n"
  "ds_read_b64_tr_b16 a[44:45], v5 offset:24640\n"
  "ds_read_b64_tr_b16 a[46:47], v5 offset:26176\n"
  "s_waitcnt vmcnt(33) lgkmcnt(0)\n"
  "s_add_u32 m0, s80, 24576\n"
  "s_nop 0\n"
  "global_load_lds_dwordx4 v6, s[38:39]\n"
  "s_add_u32 m0, s80, 25600\n"
  "s_nop 0\n"
  "global_load_lds_dwordx4 v7, s[38:39]\n"
  "s_add_u32 m0, s80, 26624\n"
  "s_nop 0\n"
  "global_load_lds_dwordx4 v8, s[38:39]\n"
  "s_add_u32 s38, s38, 36864\n"
  "s_addc_u32 s39, s39, 0\n"
  "ds_read_b64_tr_b16 a[48:49], v5 offset:27648\n"
  "ds_read_b64_tr_b16 a[50:51], v5 offset:29184\n"
  "ds_read_b64_tr_b16 a[52:53], v5 offset:27680\n"
  "ds_read_b64_tr_b16 a[54:55], v5 offset:29216\n"
  "ds_read_b64_tr_b16 a[56:57], v5 offset:27712\n"
  "ds_read_b64_tr_b16 a[58:59], v5 offset:29248\n"
  "s_waitcnt vmcnt(33) lgkmcnt(0)\n"
  "s_add_u32 m0, s80, 27648\n"
  "s_nop 0\n"
  "global_load_lds_dwordx4 v6, s[38:39]\n"
  "s_add_u32 m0, s80, 28672\n"
  "s_nop 0\n"
  "global_load_lds_dwordx4 v7, s[38:39]\n"
  "s_add_u32 m0, s80, 29696\n"
  "s_nop 0\n"
  "global_load_lds_dwordx4 v8, s[38:39]\n"
  "s_add_u32 s38, s38, 36864\n"
  "s_addc_u32 s39, s39, 0\n"
  "ds_read_b64_tr_b16 a[60:61], v5 offset:30720\n"
  "ds_read_b64_tr_b16 a[62:63], v5 offset:32256\n"
  "ds_read_b64_tr_b16 a[64:65], v5 offset:30752\n"
  "ds_read_b64_tr_b16 a[66:67], v5 offset:32288\n"
  "ds_read_b64_tr_b16 a[68:69], v5 offset:30784\n"
  "ds_read_b64_tr_b16 a[70:71], v5 offset:32320\n"
  "s_waitcnt vmcnt(33) lgkmcnt(0)\n"
  "s_add_u32 m0, s80, 30720\n"
  "s_nop 0\n"
  "global_load_lds_dwordx4 v6, s[38:39]\n"
  "s_add_u32 m0, s80, 31744\n"
  "s_nop 0\n"
  "global_load_lds_dwordx4 v7, s[38:39]\n"
  "s_add_u32 m0, s80, 32768\n"
  "s_nop 0\n"
  "global_load_lds_dwordx4 v8, s[38:39]\n"
  "s_add_u32 s38, s38, 36864\n"
  "s_addc_u32 s39, s39, 0\n"
  "ds_read_b64_tr_b16 a[72:73], v5 offset:33792\n"
  "ds_read_b64_tr_b16 a[74:75], v5 offset:35328\n"
  "ds_read_b64_tr_b16 a[76:77], v5 offset:33824\n"
  "ds_read_b64_tr_b16 a[78:79], v5 offset:35360\n"
  "ds_read_b64_tr_b16 a[80:81], v5 offset:33856\n"
  "ds_read_b64_tr_b16 a[82:83], v5 offset:35392\n"
  "s_waitcnt vmcnt(33) lgkmcnt(0)\n"
  "s_add_u32 m0, s80, 33792\n"
  "s_nop 0\n"
  "global_load_lds_dwordx4 v6, s[38:39]\n"
  "s_add_u32 m0, s80, 34816\n"
  "s_nop 0\n"
  "global_load_lds_dwordx4 v7, s[38:39]\n"
  "s_add_u32 m0, s80, 35840\n"
  "s_nop 0\n"
  "global_load_lds_dwordx4 v8, s[38:39]\n"
  "s_add_u32 s38, s38, 36864\n"
  "s_addc_u32 s39, s39, 0\n"
  "s_cmp_eq_u32 s50, 4\n"
  "s_cselect_b32 s74, 1179648, 0\n"
  "s_sub_u32 s38, s38, s74\n"
  "s_subb_u32 s39, s39, 0\n"
  "ds_read_b64_tr_b16 a[84:85], v5 offset:36864\n"
  "ds_read_b64_tr_b16 a[86:87], v5 offset:38400\n"
  "ds_read_b64_tr_b16 a[88:89], v5 offset:36896\n"
  "ds_read_b64_tr_b16 a[90:91], v5 offset:38432\n"
  "ds_read_b64_tr_b16 a[92:93], v5 offset:36928\n"
  "ds_read_b64_tr_b16 a[94:95], v5 offset:38464\n"
  "s_waitcnt vmcnt(33) lgkmcnt(0)\n"
  "s_add_u32 m0, s80, 36864\n"
  "s_nop 0\n"
  "global_load_lds_dwordx4 v6, s[38:39]\n"
  "s_add_u32 m0, s80, 37888\n"
  "s_nop 0\n"
  "global_load_lds_dwordx4 v7, s[38:39]\n"
  "s_add_u32 m0, s80, 38912\n"
  "s_nop 0\n"
  "global_load_lds_dwordx4 v8, s[38:39]\n"
  "s_add_u32 s38, s38, 36864\n"
  "s_addc_u32 s39, s39, 0\n"
  "ds_read_b64_tr_b16 a[96:97], v5 offset:0\n"
  "ds_read_b64_tr_b16 a[98:99], v5 offset:1536\n"
  "ds_read_b64_tr_b16 a[100:101], v5 offset:32\n"
  "ds_read_b64_tr_b16 a[102:103], v5 offset:1568\n"
  "ds_read_b64_tr_b16 a[104:105], v5 offset:64\n"
  "ds_read_b64_tr_b16 a[106:107], v5 offset:1600\n"
  "s_waitcnt vmcnt(33) lgkmcnt(0)\n"
  "s_add_u32 m0, s80, 0\n"
  "s_nop 0\n"
  "global_load_lds_dwordx4 v6, s[38:39]\n"
  "s_add_u32 m0, s80, 1024\n"
  "s_nop 0\n"
  "global_load_lds_dwordx4 v7, s[38:39]\n"
  "s_add_u32 m0, s80, 2048\n"
  "s_nop 0\n"
  "global_load_lds_dwordx4 v8, s[38:39]\n"
  "s_add_u32 s38, s38, 36864\n"
  "s_addc_u32 s39, s39, 0\n"
  "ds_read_b64_tr_b16 a[108:109], v5 offset:3072\n"
  "ds_read_b64_tr_b16 a[110:111], v5 offset:4608\n"
  "ds_read_b64_tr_b16 a[112:113], v5 offset:3104\n"
  "ds_read_b64_tr_b16 a[114:115], v5 offset:4640\n"
  "ds_read_b64_tr_b16 a[116:117], v5 offset:3136\n"
  "ds_read_b64_tr_b16 a[118:119], v5 offset:4672\n"
  "s_waitcnt vmcnt(33) lgkmcnt(0)\n"
  "s_add_u32 m0, s80, 3072\n"
  "s_nop 0\n"
  "global_load_lds_dwordx4 v6, s[38:39]\n"
  "s_add_u32 m0, s80, 4096\n"
  "s_nop 0\n"
  "global_load_lds_dwordx4 v7, s[38:39]\n"
  "s_add_u32 m0, s80, 5120\n"
  "s_nop 0\n"
  "global_load_lds_dwordx4 v8, s[38:39]\n"
  "s_add_u32 s38, s38, 36864\n"
  "s_addc_u32 s39, s39, 0\n"
  "ds_read_b64_tr_b16 a[120:121], v5 offset:6144\n"
  "ds_read_b64_tr_b16 a[122:123], v5 offset:7680\n"
  "ds_read_b64_tr_b16 a[124:125], v5 offset:6176\n"
  "ds_read_b64_tr_b16 a[126:127], v5 offset:7712\n"
  "ds_read_b64_tr_b16 a[128:129], v5 offset:6208\n"
  "ds_read_b64_tr_b16 a[130:131], v5 offset:7744\n"
  "s_waitcnt vmcnt(33) lgkmcnt(0)\n"
  "s_add_u32 m0, s80, 6144\n"
  "s_nop 0\n"
  "global_load_lds_dwordx4 v6, s[38:39]\n"
  "s_add_u32 m0, s80, 7168\n"
  "s_nop 0\n"
  "global_load_lds_dwordx4 v7, s[38:39]\n"
  "s_add_u32 m0, s80, 8192\n"
  "s_nop 0\n"
  "global_load_lds_dwordx4 v8, s[38:39]\n"
  "s_add_u32 s38, s38, 36864\n"
  "s_addc_u32 s39, s39, 0\n"
  "s_cmp_eq_u32 s50, 5\n"
  "s_cselect_b32 s74, 1179648, 0\n"
  "s_sub_u32 s38, s38, s74\n"
  "s_subb_u32 s39, s39, 0\n"
  "ds_read_b64_tr_b16 a[132:133], v5 offset:9216\n"
  "ds_read_b64_tr_b16 a[134:135], v5 offset:10752\n"
  "ds_read_b64_tr_b16 a[136:137], v5 offset:9248\n"
  "ds_read_b64_tr_b16 a[138:139], v5 offset:10784\n"
  "ds_read_b64_tr_b16 a[140:141], v5 offset:9280\n"
  "ds_read_b64_tr_b16 a[142:143], v5 offset:10816\n"
  "s_waitcnt vmcnt(33) lgkmcnt(0)\n"
  "s_add_u32 m0, s80, 9216\n"
  "s_nop 0\n"
  "global_load_lds_dwordx4 v6, s[38:39]\n"
  "s_add_u32 m0, s80, 10240\n"
  "s_nop 0\n"
  "global_load_lds_dwordx4 v7, s[38:39]\n"
  "s_add_u32 m0, s80, 11264\n"
  "s_nop 0\n"
  "global_load_lds_dwordx4 v8, s[38:39]\n"
  "s_add_u32 s38, s38, 36864\n"
  "s_addc_u32 s39, s39, 0\n"
  "ds_read_b64_tr_b16 a[144:145], v5 offset:12288\n"
  "ds_read_b64_tr_b16 a[146:147], v5 offset:13824\n"
  "ds_read_b64_tr_b16 a[148:149], v5 offset:12320\n"
  "ds_read_b64_tr_b16 a[150:151], v5 offset:13856\n"
  "ds_read_b64_tr_b16 a[152:153], v5 offset:12352\n"
  "ds_read_b64_tr_b16 a[154:155], v5 offset:13888\n"
  "s_waitcnt vmcnt(33) lgkmcnt(0)\n"
  "s_add_u32 m0, s80, 12288\n"
  "s_nop 0\n"
  "global_load_lds_dwordx4 v6, s[38:39]\n"
  "s_add_u32 m0, s80, 13312\n"
  "s_nop 0\n"
  "global_load_lds_dwordx4 v7, s[38:39]\n"
  "s_add_u32 m0, s80, 14336\n"
  "s_nop 0\n"
  "global_load_lds_dwordx4 v8, s[38:39]\n"
  "s_add_u32 s38, s38, 36864\n"
  "s_addc_u32 s39, s39, 0\n"
  "ds_read_b64_tr_b16 a[156:157], v5 offset:15360\n"
  "ds_read_b64_tr_b16 a[158:159], v5 offset:16896\n"
  "ds_read_b64_tr_b16 a[160:161], v5 offset:15392\n"
  "ds_read_b64_tr_b16 a[162:163], v5 offset:16928\n"
  "ds_read_b64_tr_b16 a[164:165], v5 offset:15424\n"
  "ds_read_b64_tr_b16 a[166:167], v5 offset:16960\n"
  "s_waitcnt vmcnt(33) lgkmcnt(0)\n"
  "s_add_u32 m0, s80, 15360\n"
  "s_nop 0\n"
  "global_load_lds_dwordx4 v6, s[38:39]\n"
  "s_add_u32 m0, s80, 16384\n"
  "s_nop 0\n"
  "global_load_lds_dwordx4 v7, s[38:39]\n"
  "s_add_u32 m0, s80, 17408\n"
  "s_nop 0\n"
  "global_load_lds_dwordx4 v8, s[38:39]\n"
  "s_add_u32 s38, s38, 36864\n"
  "s_addc_u32 s39, s39, 0\n"
  "ds_read_b64_tr_b16 a[168:169], v5 offset:18432\n"
  "ds_read_b64_tr_b16 a[170:171], v5 offset:19968\n"
  "ds_read_b64_tr_b16 a[172:173], v5 offset:18464\n"
  "ds_read_b64_tr_b16 a[174:175], v5 offset:20000\n"
  "ds_read_b64_tr_b16 a[176:177], v5 offset:18496\n"
  "ds_read_b64_tr_b16 a[178:179], v5 offset:20032\n"
  "s_waitcnt vmcnt(33) lgkmcnt(0)\n"
  "s_add_u32 m0, s80, 18432\n"
  "s_nop 0\n"
  "global_load_lds_dwordx4 v6, s[38:39]\n"
  "s_add_u32 m0, s80, 19456\n"
  "s_nop 0\n"
  "global_load_lds_dwordx4 v7, s[38:39]\n"
  "s_add_u32 m0, s80, 20480\n"
  "s_nop 0\n"
  "global_load_lds_dwordx4 v8, s[38:39]\n"
  "s_add_u32 s38, s38, 36864\n"
  "s_addc_u32 s39, s39, 0\n"
  "s_cmp_eq_u32 s50, 6\n"
  "s_cselect_b32 s74, 1179648, 0\n"
  "s_sub_u32 s38, s38, s74\n"
  "s_subb_u32 s39, s39, 0\n"
  "ds_read_b64_tr_b16 a[180:181], v5 offset:21504\n"
  "ds_read_b64_tr_b16 a[182:183], v5 offset:23040\n"
  "ds_read_b64_tr_b16 a[184:185], v5 offset:21536\n"
  "ds_read_b64_tr_b16 a[186:187], v5 offset:23072\n"
  "ds_read_b64_tr_b16 a[188:189], v5 offset:21568\n"
  "ds_read_b64_tr_b16 a[190:191], v5 offset:23104\n"
  "s_waitcnt vmcnt(33) lgkmcnt(0)\n"
  "s_add_u32 m0, s80, 21504\n"
  "s_nop 0\n"
  "global_load_lds_dwordx4 v6, s[38:39]\n"
  "s_add_u32 m0, s80, 22528\n"
  "s_nop 0\n"
  "global_load_lds_dwordx4 v7, s[38:39]\n"
  "s_add_u32 m0, s80, 23552\n"
  "s_nop 0\n"
  "global_load_lds_dwordx4 v8, s[38:39]\n"
  "s_add_u32 s38, s38, 36864\n"
  "s_addc_u32 s39, s39, 0\n"
  "ds_read_b64_tr_b16 a[192:193], v5 offset:24576\n"
  "ds_read_b64_tr_b16 a[194:195], v5 offset:26112\n"
  "ds_read_b64_tr_b16 a[196:197], v5 offset:24608\n"
  "ds_read_b64_tr_b16 a[198:199], v5 offset:26144\n"
  "ds_read_b64_tr_b16 a[200:201], v5 offset:24640\n"
  "ds_read_b64_tr_b16 a[202:203], v5 offset:26176\n"
  "s_waitcnt vmcnt(33) lgkmcnt(0)\n"
  "s_add_u32 m0, s80, 24576\n"
  "s_nop 0\n"
  "global_load_lds_dwordx4 v6, s[38:39]\n"
  "s_add_u32 m0, s80, 25600\n"
  "s_nop 0\n"
  "global_load_lds_dwordx4 v7, s[38:39]\n"
  "s_add_u32 m0, s80, 26624\n"
  "s_nop 0\n"
  "global_load_lds_dwordx4 v8, s[38:39]\n"
  "s_add_u32 s38, s38, 36864\n"
  "s_addc_u32 s39, s39, 0\n"
  "ds_read_b64_tr_b16 a[204:205], v5 offset:27648\n"
  "ds_read_b64_tr_b16 a[206:207], v5 offset:29184\n"
  "ds_read_b64_tr_b16 a[208:209], v5 offset:27680\n"
  "ds_read_b64_tr_b16 a[210:211], v5 offset:29216\n"
  "ds_read_b64_tr_b16 a[212:213], v5 offset:27712\n"
  "ds_read_b64_tr_b16 a[214:215], v5 offset:29248\n"
  "s_waitcnt vmcnt(33) lgkmcnt(0)\n"
  "s_add_u32 m0, s80, 27648\n"
  "s_nop 0\n"
  "global_load_lds_dwordx4 v6, s[38:39]\n"
  "s_add_u32 m0, s80, 28672\n"
  "s_nop 0\n"
  "global_load_lds_dwordx4 v7, s[38:39]\n"
  "s_add_u32 m0, s80, 29696\n"
  "s_nop 0\n"
  "global_load_lds_dwordx4 v8, s[38:39]\n"
  "s_add_u32 s38, s38, 36864\n"
  "s_addc_u32 s39, s39, 0\n"
  "ds_read_b64_tr_b16 a[216:217], v5 offset:30720\n"
  "ds_read_b64_tr_b16 a[218:219], v5 offset:32256\n"
  "ds_read_b64_tr_b16 a[220:221], v5 offset:30752\n"
  "ds_read_b64_tr_b16 a[222:223], v5 offset:32288\n"
  "ds_read_b64_tr_b16 a[224:225], v5 offset:30784\n"
  "ds_read_b64_tr_b16 a[226:227], v5 offset:32320\n"
  "s_waitcnt vmcnt(33) lgkmcnt(0)\n"
  "s_add_u32 m0, s80, 30720\n"
  "s_nop 0\n"
  "global_load_lds_dwordx4 v6, s[38:39]\n"
  "s_add_u32 m0, s80, 31744\n"
  "s_nop 0\n"
  "global_load_lds_dwordx4 v7, s[38:39]\n"
  "s_add_u32 m0, s80, 32768\n"
  "s_nop 0\n"
  "global_load_lds_dwordx4 v8, s[38:39]\n"
  "s_add_u32 s38, s38, 36864\n"
  "s_addc_u32 s39, s39, 0\n"
  "s_cmp_eq_u32 s50, 7\n"
  "s_cselect_b32 s74, 1179648, 0\n"
  "s_sub_u32 s38, s38, s74\n"
  "s_subb_u32 s39, s39, 0\n"
  "ds_read_b64_tr_b16 a[228:229], v5 offset:33792\n"
  "ds_read_b64_tr_b16 a[230:231], v5 offset:35328\n"
  "ds_read_b64_tr_b16 a[232:233], v5 offset:33824\n"
  "ds_read_b64_tr_b16 a[234:235], v5 offset:35360\n"
  "ds_read_b64_tr_b16 a[236:237], v5 offset:33856\n"
  "ds_read_b64_tr_b16 a[238:239], v5 offset:35392\n"
  "s_waitcnt vmcnt(33) lgkmcnt(0)\n"
  "ds_read_b64_tr_b16 a[240:241], v5 offset:36864\n"
  "ds_read_b64_tr_b16 a[242:243], v5 offset:38400\n"
  "ds_read_b64_tr_b16 a[244:245], v5 offset:36896\n"
  "ds_read_b64_tr_b16 a[246:247], v5 offset:38432\n"
  "ds_read_b64_tr_b16 a[248:249], v5 offset:36928\n"
  "ds_read_b64_tr_b16 a[250:251], v5 offset:38464\n"
  "s_waitcnt vmcnt(30) lgkmcnt(0)\n"
  "ds_read_b64_tr_b16 a[252:253], v5 offset:0\n"
  "ds_read_b64_tr_b16 a[254:255], v5 offset:1536\n"
  "ds_read_b64_tr_b16 v[140:141], v5 offset:32\n"
  "ds_read_b64_tr_b16 v[142:143], v5 offset:1568\n"
  "ds_read_b64_tr_b16 v[144:145], v5 offset:64\n"
  "ds_read_b64_tr_b16 v[146:147], v5 offset:1600\n"
  "s_waitcnt vmcnt(27) lgkmcnt(0)\n"
  "ds_read_b64_tr_b16 v[148:149], v5 offset:3072\n"
  "ds_read_b64_tr_b16 v[150:151], v5 offset:4608\n"
  "ds_read_b64_tr_b16 v[152:153], v5 offset:3104\n"
  "ds_read_b64_tr_b16 v[154:155], v5 offset:4640\n"
  "ds_read_b64_tr_b16 v[156:157], v5 offset:3136\n"
  "ds_read_b64_tr_b16 v[158:159], v5 offset:4672\n"
  "s_waitcnt vmcnt(24) lgkmcnt(0)\n"
  "ds_read_b64_tr_b16 v[160:161], v5 offset:6144\n"
  "ds_read_b64_tr_b16 v[162:163], v5 offset:7680\n"
  "ds_read_b64_tr_b16 v[164:165], v5 offset:6176\n"
  "ds_read_b64_tr_b16 v[166:167], v5 offset:7712\n"
  "ds_read_b64_tr_b16 v[168:169], v5 offset:6208\n"
  "ds_read_b64_tr_b16 v[170:171], v5 offset:7744\n"
  "s_waitcnt vmcnt(21) lgkmcnt(0)\n"
  "ds_read_b64_tr_b16 v[172:173], v5 offset:9216\n"
  "ds_read_b64_tr_b16 v[174:175], v5 offset:10752\n"
  "ds_read_b64_tr_b16 v[176:177], v5 offset:9248\n"
  "ds_read_b64_tr_b16 v[178:179], v5 offset:10784\n"
  "ds_read_b64_tr_b16 v[180:181], v5 offset:9280\n"
  "ds_read_b64_tr_b16 v[182:183], v5 offset:10816\n"
  "s_waitcnt vmcnt(18) lgkmcnt(0)\n"
  "ds_read_b64_tr_b16 v[184:185], v5 offset:12288\n"
  "ds_read_b64_tr_b16 v[186:187], v5 offset:13824\n"
  "ds_read_b64_tr_b16 v[188:189], v5 offset:12320\n"
  "ds_read_b64_tr_b16 v[190:191], v5 offset:13856\n"
  "ds_read_b64_tr_b16 v[192:193], v5 offset:12352\n"
  "ds_read_b64_tr_b16 v[194:195], v5 offset:13888\n"
  "s_waitcnt vmcnt(15) lgkmcnt(0)\n"
  "ds_read_b64_tr_b16 v[196:197], v5 offset:15360\n"
  "ds_read_b64_tr_b16 v[198:199], v5 offset:16896\n"
  "ds_read_b64_tr_b16 v[200:201], v5 offset:15392\n"
  "ds_read_b64_tr_b16 v[202:203], v5 offset:16928\n"
  "ds_read_b64_tr_b16 v[204:205], v5 offset:15424\n"
  "ds_read_b64_tr_b16 v[206:207], v5 offset:16960\n"
  "s_waitcnt vmcnt(12) lgkmcnt(0)\n"
  "ds_read_b64_tr_b16 v[208:209], v5 offset:18432\n"
  "ds_read_b64_tr_b16 v[210:211], v5 offset:19968\n"
  "ds_read_b64_tr_b16 v[212:213], v5 offset:18464\n"
  "ds_read_b64_tr_b16 v[214:215], v5 offset:20000\n"
  "ds_read_b64_tr_b16 v[216:217], v5 offset:18496\n"
  "ds_read_b64_tr_b16 v[218:219], v5 offset:20032\n"
  "s_waitcnt vmcnt(9) lgkmcnt(0)\n"
  "ds_read_b64_tr_b16 v[220:221], v5 offset:21504\n"
  "ds_read_b64_tr_b16 v[222:223], v5 offset:23040\n"
  "ds_read_b64_tr_b16 v[224:225], v5 offset:21536\n"
  "ds_read_b64_tr_b16 v[226:227], v5 offset:23072\n"
  "ds_read_b64_tr_b16 v[228:229], v5 offset:21568\n"
  "ds_read_b64_tr_b16 v[230:231], v5 offset:23104\n"
  "s_waitcnt vmcnt(6) lgkmcnt(0)\n"
  "ds_read_b64_tr_b16 v[232:233], v5 offset:24576\n"
  "ds_read_b64_tr_b16 v[234:235], v5 offset:26112\n"
  "ds_read_b64_tr_b16 v[236:237], v5 offset:24608\n"
  "ds_read_b64_tr_b16 v[238:239], v5 offset:26144\n"
  "ds_read_b64_tr_b16 v[240:241], v5 offset:24640\n"
  "ds_read_b64_tr_b16 v[242:243], v5 offset:26176\n"
  "s_waitcnt vmcnt(3) lgkmcnt(0)\n"
  "ds_read_b64_tr_b16 v[244:245], v5 offset:27648\n"
  "ds_read_b64_tr_b16 v[246:247], v5 offset:29184\n"
  "ds_read_b64_tr_b16 v[248:249], v5 offset:27680\n"
  "ds_read_b64_tr_b16 v[250:251], v5 offset:29216\n"
  "ds_read_b64_tr_b16 v[252:253], v5 offset:27712\n"
  "ds_read_b64_tr_b16 v[254:255], v5 offset:29248\n"
  "s_waitcnt vmcnt(0) lgkmcnt(0)\n"
  "ds_read_b64_tr_b16 v[112:113], v5 offset:30720\n"
  "ds_read_b64_tr_b16 v[114:115], v5 offset:32256\n"
  "ds_read_b64_tr_b16 v[116:117], v5 offset:30752\n"
  "ds_read_b64_tr_b16 v[118:119], v5 offset:32288\n"
  "ds_read_b64_tr_b16 v[120:121], v5 offset:30784\n"
  "ds_read_b64_tr_b16 v[122:123], v5 offset:32320\n"
  "s_waitcnt lgkmcnt(0)\n"
  "s_barrier\n"
  "s_mul_i32 s74, s47, 18432\n"
  "v_add_u32 v5, s74, v0\n"
  "ds_write_b128 v5, v[52:55] offset:0\n"
  "ds_write_b128 v5, v[56:59] offset:1024\n"
  "ds_write_b128 v5, v[60:63] offset:2048\n"
  "ds_write_b128 v5, v[64:67] offset:3072\n"
  "ds_write_b128 v5, v[68:71] offset:4096\n"
  "ds_write_b128 v5, v[72:75] offset:5120\n"
  "ds_write_b128 v5, v[76:79] offset:6144\n"
  "ds_write_b128 v5, v[80:83] offset:7168\n"
  "ds_write_b128 v5, v[84:87] offset:8192\n"
  "ds_write_b128 v5, v[88:91] offset:9216\n"
  "ds_write_b128 v5, v[92:95] offset:10240\n"
  "ds_write_b128 v5, v[96:99] offset:11264\n"
  "ds_write_b128 v5, v[100:103] offset:12288\n"
  "ds_write_b128 v5, v[104:107] offset:13312\n"
  "ds_write_b128 v5, v[108:111] offset:14336\n"
  "ds_write_b128 v5, v[112:115] offset:15360\n"
  "ds_write_b128 v5, v[116:119] offset:16384\n"
  "ds_write_b128 v5, v[120:123] offset:17408\n"
  "s_mul_i32 s74, s47, 6144\n"
  "s_add_u32 s74, s74, 73728\n"
  "v_add_u32 v11, s74, v0\n"
  "s_mov_b32 s57, 400000\n"
  "s_add_u32 s66, s36, 16777216\n"
  "s_addc_u32 s67, s37, 0\n"
  "s_waitcnt lgkmcnt(0)\n"
  "s_and_b32 s74, s49, 1\n"
  "s_mul_i32 s74, s74, 24576\n"
  "v_add_u32 v11, s74, v11\n"
  "v_add_u32 v3, s74, v3\n"
  "OPOLL:\n"
  "global_load_dword v8, v1, s[66:67] sc1\n"
  "s_lshr_b32 s74, s49, 1\n"
  "s_waitcnt vmcnt(0)\n"
  "v_cmp_ge_u32 vcc, s74, v8\n"
  "s_cbranch_vccz OGO\n"
  "s_sub_u32 s57, s57, 1\n"
  "s_cbranch_scc1 OQUIT\n"
  "s_sleep 4\n"
  "s_branch OPOLL\n"
  "OGO:\n"
  "s_add_u32 s74, s49, 0\n"
  "s_lshr_b32 s75, s74, 1\n"
  "s_and_b32 s76, s74, 1\n"
  "s_mul_i32 s74, s74, 20480\n"
  "s_add_u32 s74, s74, s45\n"
  "s_add_u32 s62, s36, s74\n"
  "s_addc_u32 s63, s37, 0\n"
  "s_lshl_b32 s77, s76, 12\n"
  "s_add_u32 s77, s77, s44\n"
  "s_bitcmp1_b64 s[4:5], s75\n"
  "s_cselect_b32 s78, 8192, 0\n"
  "s_lshl_b32 s79, s75, 21\n"
  "s_add_u32 s78, s78, s79\n"
  "s_add_u32 s78, s78, 20971520\n"
  "s_xor_b32 s79, s78, s77\n"
  "s_add_u32 s60, s36, s79\n"
  "s_addc_u32 s61, s37, 0\n"
  "s_lshl_b32 s74, s75, 11\n"
  "s_lshl_b32 s77, s76, 6\n"
  "s_add_u32 s74, s74, s77\n"
  "s_add_u32 s74, s74, s46\n"
  "s_add_u32 s64, s36, s74\n"
  "s_addc_u32 s65, s37, 0\n"
  "global_load_dwordx4 v[52:55], v0, s[60:61] offset:0 sc1\n"
  "global_load_dwordx4 v[56:59], v0, s[60:61] offset:1024 sc1\n"
  "global_load_dwordx4 v[60:63], v0, s[60:61] offset:2048 sc1\n"
  "global_load_dwordx4 v[64:67], v0, s[60:61] offset:3072 sc1\n"
  "s_cmp_eq_u32 s50, 0\n"
  "s_cselect_b32 s74, 524288, 0\n"
  "s_sub_u32 s60, s60, s74\n"
  "s_subb_u32 s61, s61, 0\n"
  "s_add_u32 s60, s60, 65536\n"
  "s_addc_u32 s61, s61, 0\n"
  "global_load_dwordx4 v[68:71], v0, s[60:61] offset:0 sc1\n"
  "global_load_dwordx4 v[72:75], v0, s[60:61] offset:1024 sc1\n"
  "global_load_dwordx4 v[76:79], v0, s[60:61] offset:2048 sc1\n"
  "global_load_dwordx4 v[80:83], v0, s[60:61] offset:3072 sc1\n"
  "s_cmp_eq_u32 s50, 1\n"
  "s_cselect_b32 s74, 524288, 0\n"
  "s_sub_u32 s60, s60, s74\n"
  "s_subb_u32 s61, s61, 0\n"
  "s_add_u32 s60, s60, 65536\n"
  "s_addc_u32 s61, s61, 0\n"
  "global_load_dwordx4 v[84:87], v0, s[60:61] offset:0 sc1\n"
  "global_load_dwordx4 v[88:91], v0, s[60:61] offset:1024 sc1\n"
  "global_load_dwordx4 v[92:95], v0, s[60:61] offset:2048 sc1\n"
  "global_load_dwordx4 v[96:99], v0, s[60:61] offset:3072 sc1\n"
  "s_cmp_eq_u32 s50, 2\n"
  "s_cselect_b32 s74, 524288, 0\n"
  "s_sub_u32 s60, s60, s74\n"
  "s_subb_u32 s61, s61, 0\n"
  "s_add_u32 s60, s60, 65536\n"
  "s_addc_u32 s61, s61, 0\n"
  "global_load_dwordx4 v[100:103], v0, s[60:61] offset:0 sc1\n"
  "global_load_dwordx4 v[104:107], v0, s[60:61] offset:1024 sc1\n"
  "global_load_dwordx4 v[108:111], v0, s[60:61] offset:2048 sc1\n"
  "global_load_dwordx4 v[112:115], v0, s[60:61] offset:3072 sc1\n"
  "s_cmp_eq_u32 s50, 3\n"
  "s_cselect_b32 s74, 524288, 0\n"
  "s_sub_u32 s60, s60, s74\n"
  "s_subb_u32 s61, s61, 0\n"
  "s_add_u32 s60, s60, 65536\n"
  "s_addc_u32 s61, s61, 0\n"
  "global_load_dwordx4 v[116:119], v0, s[60:61] offset:0 sc1\n"
  "global_load_dwordx4 v[120:123], v0, s[60:61] offset:1024 sc1\n"
  "global_load_dwordx4 v[124:127], v0, s[60:61] offset:2048 sc1\n"
  "OSEG:\n"
  "global_load_dword v8, v1, s[66:67] sc1\n"
  "s_waitcnt vmcnt(19)\n"
  "v_mfma_f32_16x16x32_bf16 v[12:15], a[0:3], v[52:55], 0\n"
  "v_mfma_f32_16x16x32_bf16 v[16:19], a[4:7], v[52:55], 0\n"
  "v_mfma_f32_16x16x32_bf16 v[20:23], a[8:11], v[52:55], 0\n"
  "global_load_dwordx4 v[52:55], v0, s[60:61] offset:3072 sc1\n"
  "s_cmp_eq_u32 s50, 4\n"
  "s_cselect_b32 s74, 524288, 0\n"
  "s_sub_u32 s60, s60, s74\n"
  "s_subb_u32 s61, s61, 0\n"
  "s_add_u32 s60, s60, 65536\n"
  "s_addc_u32 s61, s61, 0\n"
  "s_waitcnt vmcnt(19)\n"
  "v_mfma_f32_16x16x32_bf16 v[12:15], a[12:15], v[56:59], v[12:15]\n"
  "v_mfma_f32_16x16x32_bf16 v[16:19], a[16:19], v[56:59], v[16:19]\n"
  "v_mfma_f32_16x16x32_bf16 v[20:23], a[20:23], v[56:59], v[20:23]\n"
  "global_load_dwordx4 v[56:59], v0, s[60:61] offset:0 sc1\n"
  "s_waitcnt vmcnt(19)\n"
  "v_mfma_f32_16x16x32_bf16 v[12:15], a[24:27], v[60:63], v[12:15]\n"
  "v_mfma_f32_16x16x32_bf16 v[16:19], a[28:31], v[60:63], v[16:19]\n"
  "v_mfma_f32_16x16x32_bf16 v[20:23], a[32:35], v[60:63], v[20:23]\n"
  "global_load_dwordx4 v[60:63], v0, s[60:61] offset:1024 sc1\n"
  "s_waitcnt vmcnt(19)\n"
  "v_mfma_f32_16x16x32_bf16 v[12:15], a[36:39], v[64:67], v[12:15]\n"
  "v_mfma_f32_16x16x32_bf16 v[16:19], a[40:43], v[64:67], v[16:19]\n"
  "v_mfma_f32_16x16x32_bf16 v[20:23], a[44:47], v[64:67], v[20:23]\n"
  "global_load_dwordx4 v[64:67], v0, s[60:61] offset:2048 sc1\n"
  "s_waitcnt vmcnt(19)\n"
  "v_mfma_f32_16x16x32_bf16 v[12:15], a[48:51], v[68:71], v[12:15]\n"
  "v_mfma_f32_16x16x32_bf16 v[16:19], a[52:55], v[68:71], v[16:19]\n"
  "v_mfma_f32_16x16x32_bf16 v[20:23], a[56:59], v[68:71], v[20:23]\n"
  "global_load_dwordx4 v[68:71], v0, s[60:61] offset:3072 sc1\n"
  "s_cmp_eq_u32 s50, 5\n"
  "s_cselect_b32 s74, 524288, 0\n"
  "s_sub_u32 s60, s60, s74\n"
  "s_subb_u32 s61, s61, 0\n"
  "s_add_u32 s60, s60, 65536\n"
  "s_addc_u32 s61, s61, 0\n"
  "s_waitcnt vmcnt(19)\n"
  "v_mfma_f32_16x16x32_bf16 v[12:15], a[60:63], v[72:75], v[12:15]\n"
  "v_mfma_f32_16x16x32_bf16 v[16:19], a[64:67], v[72:75], v[16:19]\n"
  "v_mfma_f32_16x16x32_bf16 v[20:23], a[68:71], v[72:75], v[20:23]\n"
  "global_load_dwordx4 v[72:75], v0, s[60:61] offset:0 sc1\n"
  "s_waitcnt vmcnt(19)\n"
  "v_mfma_f32_16x16x32_bf16 v[12:15], a[72:75], v[76:79], v[12:15]\n"
  "v_mfma_f32_16x16x32_bf16 v[16:19], a[76:79], v[76:79], v[16:19]\n"
  "v_mfma_f32_16x16x32_bf16 v[20:23], a[80:83], v[76:79], v[20:23]\n"
  "global_load_dwordx4 v[76:79], v0, s[60:61] offset:1024 sc1\n"
  "s_waitcnt vmcnt(19)\n"
  "v_mfma_f32_16x16x32_bf16 v[12:15], a[84:87], v[80:83], v[12:15]\n"
  "v_mfma_f32_16x16x32_bf16 v[16:19], a[88:91], v[80:83], v[16:19]\n"
  "v_mfma_f32_16x16x32_bf16 v[20:23], a[92:95], v[80:83], v[20:23]\n"
  "global_load_dwordx4 v[80:83], v0, s[60:61] offset:2048 sc1\n"
  "s_waitcnt vmcnt(19)\n"
  "v_mfma_f32_16x16x32_bf16 v[12:15], a[96:99], v[84:87], v[12:15]\n"
  "v_mfma_f32_16x16x32_bf16 v[16:19], a[100:103], v[84:87], v[16:19]\n"
  "v_mfma_f32_16x16x32_bf16 v[20:23], a[104:107], v[84:87], v[20:23]\n"
  "global_load_dwordx4 v[84:87], v0, s[60:61] offset:3072 sc1\n"
  "s_cmp_eq_u32 s50, 6\n"
  "s_cselect_b32 s74, 524288, 0\n"
  "s_sub_u32 s60, s60, s74\n"
  "s_subb_u32 s61, s61, 0\n"
  "s_add_u32 s60, s60, 65536\n"
  "s_addc_u32 s61, s61, 0\n"
  "s_waitcnt vmcnt(19)\n"
  "v_mfma_f32_16x16x32_bf16 v[12:15], a[108:111], v[88:91], v[12:15]\n"
  "v_mfma_f32_16x16x32_bf16 v[16:19], a[112:115], v[88:91], v[16:19]\n"
  "v_mfma_f32_16x16x32_bf16 v[20:23], a[116:119], v[88:91], v[20:23]\n"
  "global_load_dwordx4 v[88:91], v0, s[60:61] offset:0 sc1\n"
  "s_waitcnt vmcnt(19)\n"
  "v_mfma_f32_16x16x32_bf16 v[12:15], a[120:123], v[92:95], v[12:15]\n"
  "v_mfma_f32_16x16x32_bf16 v[16:19], a[124:127], v[92:95], v[16:19]\n"
  "v_mfma_f32_16x16x32_bf16 v[20:23], a[128:131], v[92:95], v[20:23]\n"
  "global_load_dwordx4 v[92:95], v0, s[60:61] offset:1024 sc1\n"
  "s_waitcnt vmcnt(19)\n"
  "v_mfma_f32_16x16x32_bf16 v[12:15], a[132:135], v[96:99], v[12:15]\n"
  "v_mfma_f32_16x16x32_bf16 v[16:19], a[136:139], v[96:99], v[16:19]\n"
  "v_mfma_f32_16x16x32_bf16 v[20:23], a[140:143], v[96:99], v[20:23]\n"
  "global_load_dwordx4 v[96:99], v0, s[60:61] offset:2048 sc1\n"
  "s_waitcnt vmcnt(19)\n"
  "v_mfma_f32_16x16x32_bf16 v[12:15], a[144:147], v[100:103], v[12:15]\n"
  "v_mfma_f32_16x16x32_bf16 v[16:19], a[148:151], v[100:103], v[16:19]\n"
  "v_mfma_f32_16x16x32_bf16 v[20:23], a[152:155], v[100:103], v[20:23]\n"
  "global_load_dwordx4 v[100:103], v0, s[60:61] offset:3072 sc1\n"
  "s_cmp_eq_u32 s50, 7\n"
  "s_cselect_b32 s74, 524288, 0\n"
  "s_sub_u32 s60, s60, s74\n"
  "s_subb_u32 s61, s61, 0\n"
  "s_add_u32 s60, s60, 65536\n"
  "s_addc_u32 s61, s61, 0\n"
  "s_waitcnt vmcnt(19)\n"
  "v_mfma_f32_16x16x32_bf16 v[12:15], a[156:159], v[104:107], v[12:15]\n"
  "v_mfma_f32_16x16x32_bf16 v[16:19], a[160:163], v[104:107], v[16:19]\n"
  "v_mfma_f32_16x16x32_bf16 v[20:23], a[164:167], v[104:107], v[20:23]\n"
  "global_load_dwordx4 v[104:107], v0, s[62:63] offset:-2048 sc1\n"
  "s_waitcnt vmcnt(19)\n"
  "v_mfma_f32_16x16x32_bf16 v[12:15], a[168:171], v[108:111], v[12:15]\n"
  "v_mfma_f32_16x16x32_bf16 v[16:19], a[172:175], v[108:111], v[16:19]\n"
  "v_mfma_f32_16x16x32_bf16 v[20:23], a[176:179], v[108:111], v[20:23]\n"
  "global_load_dwordx4 v[108:111], v0, s[62:63] offset:-1024 sc1\n"
  "s_waitcnt vmcnt(19)\n"
  "v_mfma_f32_16x16x32_bf16 v[12:15], a[180:183], v[112:115], v[12:15]\n"
  "v_mfma_f32_16x16x32_bf16 v[16:19], a[184:187], v[112:115], v[16:19]\n"
  "v_mfma_f32_16x16x32_bf16 v[20:23], a[188:191], v[112:115], v[20:23]\n"
  "global_load_dwordx4 v[112:115], v0, s[62:63] offset:0 sc1\n"
  "s_waitcnt vmcnt(19)\n"
  "v_mfma_f32_16x16x32_bf16 v[12:15], a[192:195], v[116:119], v[12:15]\n"
  "v_mfma_f32_16x16x32_bf16 v[16:19], a[196:199], v[116:119], v[16:19]\n"
  "v_mfma_f32_16x16x32_bf16 v[20:23], a[200:203], v[116:119], v[20:23]\n"
  "global_load_dwordx4 v[116:119], v0, s[62:63] offset:1024 sc1\n"
  "s_waitcnt vmcnt(19)\n"
  "v_mfma_f32_16x16x32_bf16 v[12:15], a[204:207], v[120:123], v[12:15]\n"
  "v_mfma_f32_16x16x32_bf16 v[16:19], a[208:211], v[120:123], v[16:19]\n"
  "v_mfma_f32_16x16x32_bf16 v[20:23], a[212:215], v[120:123], v[20:23]\n"
  "global_load_dwordx4 v[120:123], v0, s[62:63] offset:2048 sc1\n"
  "s_waitcnt vmcnt(19)\n"
  "v_mfma_f32_16x16x32_bf16 v[12:15], a[216:219], v[124:127], v[12:15]\n"
  "v_mfma_f32_16x16x32_bf16 v[16:19], a[220:223], v[124:127], v[16:19]\n"
  "v_mfma_f32_16x16x32_bf16 v[20:23], a[224:227], v[124:127], v[20:23]\n"
  "global_load_dword v9, v2, s[64:65] sc1\n"
  "s_add_u32 s74, s49, 3\n"
  "s_lshr_b32 s75, s74, 1\n"
  "s_and_b32 s76, s74, 1\n"
  "s_mul_i32 s74, s74, 20480\n"
  "s_add_u32 s74, s74, s45\n"
  "s_add_u32 s62, s36, s74\n"
  "s_addc_u32 s63, s37, 0\n"
  "s_lshl_b32 s77, s76, 12\n"
  "s_add_u32 s77, s77, s44\n"
  "s_bitcmp1_b64 s[4:5], s75\n"
  "s_cselect_b32 s78, 8192, 0\n"
  "s_lshl_b32 s79, s75, 21\n"
  "s_add_u32 s78, s78, s79\n"
  "s_add_u32 s78, s78, 20971520\n"
  "s_xor_b32 s79, s78, s77\n"
  "s_add_u32 s60, s36, s79\n"
  "s_addc_u32 s61, s37, 0\n"
  "s_lshl_b32 s74, s75, 11\n"
  "s_lshl_b32 s77, s76, 6\n"
  "s_add_u32 s74, s74, s77\n"
  "s_add_u32 s74, s74, s46\n"
  "s_add_u32 s64, s36, s74\n"
  "s_addc_u32 s65, s37, 0\n"
  "s_waitcnt vmcnt(19)\n"
  "v_cmp_ge_u32 vcc, s75, v8\n"
  "s_cbranch_vccnz ODRAIN\n"
  "s_waitcnt vmcnt(18)\n"
  "v_mfma_f32_16x16x32_bf16 v[12:15], a[228:231], v[52:55], v[12:15]\n"
  "v_mfma_f32_16x16x32_bf16 v[16:19], a[232:235], v[52:55], v[16:19]\n"
  "v_mfma_f32_16x16x32_bf16 v[20:23], a[236:239], v[52:55], v[20:23]\n"
  "global_load_dwordx4 v[52:55], v0, s[60:61] offset:0 sc1\n"
  "s_waitcnt vmcnt(18)\n"
  "v_mfma_f32_16x16x32_bf16 v[12:15], a[240:243], v[56:59], v[12:15]\n"
  "v_mfma_f32_16x16x32_bf16 v[16:19], a[244:247], v[56:59], v[16:19]\n"
  "v_mfma_f32_16x16x32_bf16 v[20:23], a[248:251], v[56:59], v[20:23]\n"
  "global_load_dwordx4 v[56:59], v0, s[60:61] offset:1024 sc1\n"
  "s_waitcnt vmcnt(18)\n"
  "v_mfma_f32_16x16x32_bf16 v[12:15], a[252:255], v[60:63], v[12:15]\n"
  "v_mfma_f32_16x16x32_bf16 v[16:19], v[140:143], v[60:63], v[16:19]\n"
  "v_mfma_f32_16x16x32_bf16 v[20:23], v[144:147], v[60:63], v[20:23]\n"
  "global_load_dwordx4 v[60:63], v0, s[60:61] offset:2048 sc1\n"
  "s_waitcnt vmcnt(18)\n"
  "v_mfma_f32_16x16x32_bf16 v[12:15], v[148:151], v[64:67], v[12:15]\n"
  "v_mfma_f32_16x16x32_bf16 v[16:19], v[152:155], v[64:67], v[16:19]\n"
  "v_mfma_f32_16x16x32_bf16 v[20:23], v[156:159], v[64:67], v[20:23]\n"
  "global_load_dwordx4 v[64:67], v0, s[60:61] offset:3072 sc1\n"
  "s_cmp_eq_u32 s50, 0\n"
  "s_cselect_b32 s74, 524288, 0\n"
  "s_sub_u32 s60, s60, s74\n"
  "s_subb_u32 s61, s61, 0\n"
  "s_add_u32 s60, s60, 65536\n"
  "s_addc_u32 s61, s61, 0\n"
  "s_waitcnt vmcnt(18)\n"
  "v_mfma_f32_16x16x32_bf16 v[12:15], v[160:163], v[68:71], v[12:15]\n"
  "v_mfma_f32_16x16x32_bf16 v[16:19], v[164:167], v[68:71], v[16:19]\n"
  "v_mfma_f32_16x16x32_bf16 v[20:23], v[168:171], v[68:71], v[20:23]\n"
  "global_load_dwordx4 v[68:71], v0, s[60:61] offset:0 sc1\n"
  "s_waitcnt vmcnt(18)\n"
  "v_mfma_f32_16x16x32_bf16 v[12:15], v[172:175], v[72:75], v[12:15]\n"
  "v_mfma_f32_16x16x32_bf16 v[16:19], v[176:179], v[72:75], v[16:19]\n"
  "v_mfma_f32_16x16x32_bf16 v[20:23], v[180:183], v[72:75], v[20:23]\n"
  "global_load_dwordx4 v[72:75], v0, s[60:61] offset:1024 sc1\n"
  "s_waitcnt vmcnt(18)\n"
  "v_mfma_f32_16x16x32_bf16 v[12:15], v[184:187], v[76:79], v[12:15]\n"
  "v_mfma_f32_16x16x32_bf16 v[16:19], v[188:191], v[76:79], v[16:19]\n"
  "v_mfma_f32_16x16x32_bf16 v[20:23], v[192:195], v[76:79], v[20:23]\n"
  "global_load_dwordx4 v[76:79], v0, s[60:61] offset:2048 sc1\n"
  "s_waitcnt vmcnt(18)\n"
  "v_mfma_f32_16x16x32_bf16 v[12:15], v[196:199], v[80:83], v[12:15]\n"
  "v_mfma_f32_16x16x32_bf16 v[16:19], v[200:203], v[80:83], v[16:19]\n"
  "v_mfma_f32_16x16x32_bf16 v[20:23], v[204:207], v[80:83], v[20:23]\n"
  "global_load_dwordx4 v[80:83], v0, s[60:61] offset:3072 sc1\n"
  "s_cmp_eq_u32 s50, 1\n"
  "s_cselect_b32 s74, 524288, 0\n"
  "s_sub_u32 s60, s60, s74\n"
  "s_subb_u32 s61, s61, 0\n"
  "s_add_u32 s60, s60, 65536\n"
  "s_addc_u32 s61, s61, 0\n"
  "s_waitcnt vmcnt(18)\n"
  "v_mfma_f32_16x16x32_bf16 v[12:15], v[208:211], v[84:87], v[12:15]\n"
  "v_mfma_f32_16x16x32_bf16 v[16:19], v[212:215], v[84:87], v[16:19]\n"
  "v_mfma_f32_16x16x32_bf16 v[20:23], v[216:219], v[84:87], v[20:23]\n"
  "global_load_dwordx4 v[84:87], v0, s[60:61] offset:0 sc1\n"
  "s_waitcnt vmcnt(18)\n"
  "v_mfma_f32_16x16x32_bf16 v[12:15], v[220:223], v[88:91], v[12:15]\n"
  "v_mfma_f32_16x16x32_bf16 v[16:19], v[224:227], v[88:91], v[16:19]\n"
  "v_mfma_f32_16x16x32_bf16 v[20:23], v[228:231], v[88:91], v[20:23]\n"
  "global_load_dwordx4 v[88:91], v0, s[60:61] offset:1024 sc1\n"
  "s_waitcnt vmcnt(18)\n"
  "v_mfma_f32_16x16x32_bf16 v[12:15], v[232:235], v[92:95], v[12:15]\n"
  "v_mfma_f32_16x16x32_bf16 v[16:19], v[236:239], v[92:95], v[16:19]\n"
  "v_mfma_f32_16x16x32_bf16 v[20:23], v[240:243], v[92:95], v[20:23]\n"
  "global_load_dwordx4 v[92:95], v0, s[60:61] offset:2048 sc1\n"
  "ds_read_b128 v[36:39], v5 offset:15360\n"
  "ds_read_b128 v[40:43], v5 offset:16384\n"
  "ds_read_b128 v[44:47], v5 offset:17408\n"
  "s_waitcnt vmcnt(18)\n"
  "v_mfma_f32_16x16x32_bf16 v[12:15], v[244:247], v[96:99], v[12:15]\n"
  "v_mfma_f32_16x16x32_bf16 v[16:19], v[248:251], v[96:99], v[16:19]\n"
  "v_mfma_f32_16x16x32_bf16 v[20:23], v[252:255], v[96:99], v[20:23]\n"
  "global_load_dwordx4 v[96:99], v0, s[60:61] offset:3072 sc1\n"
  "s_cmp_eq_u32 s50, 2\n"
  "s_cselect_b32 s74, 524288, 0\n"
  "s_sub_u32 s60, s60, s74\n"
  "s_subb_u32 s61, s61, 0\n"
  "s_add_u32 s60, s60, 65536\n"
  "s_addc_u32 s61, s61, 0\n"
  "ds_read_b128 v[128:131], v5 offset:0\n"
  "ds_read_b128 v[132:135], v5 offset:1024\n"
  "ds_read_b128 v[136:139], v5 offset:2048\n"
  "s_waitcnt vmcnt(18) lgkmcnt(5)\n"
  "v_mfma_f32_16x16x32_bf16 v[12:15], v[36:39], v[100:103], v[12:15]\n"
  "s_waitcnt lgkmcnt(4)\n"
  "v_mfma_f32_16x16x32_bf16 v[16:19], v[40:43], v[100:103], v[16:19]\n"
  "s_waitcnt lgkmcnt(3)\n"
  "v_mfma_f32_16x16x32_bf16 v[20:23], v[44:47], v[100:103], v[20:23]\n"
  "global_load_dwordx4 v[100:103], v0, s[60:61] offset:0 sc1\n"
  "ds_read_b128 v[36:39], v5 offset:3072\n"
  "ds_read_b128 v[40:43], v5 offset:4096\n"
  "ds_read_b128 v[44:47], v5 offset:5120\n"
  "s_waitcnt vmcnt(18) lgkmcnt(5)\n"
  "v_mfma_f32_16x16x32_bf16 v[24:27], v[128:131], v[104:107], 0\n"
  "s_waitcnt lgkmcnt(4)\n"
  "v_mfma_f32_16x16x32_bf16 v[28:31], v[132:135], v[104:107], 0\n"
  "s_waitcnt lgkmcnt(3)\n"
  "v_mfma_f32_16x16x32_bf16 v[32:35], v[136:139], v[104:107], 0\n"
  "global_load_dwordx4 v[104:107], v0, s[60:61] offset:1024 sc1\n"
  "ds_read_b128 v[128:131], v5 offset:6144\n"
  "ds_read_b128 v[132:135], v5 offset:7168\n"
  "ds_read_b128 v[136:139], v5 offset:8192\n"
  "s_waitcnt vmcnt(18) lgkmcnt(5)\n"
  "v_mfma_f32_16x16x32_bf16 v[24:27], v[36:39], v[108:111], v[24:27]\n"
  "s_waitcnt lgkmcnt(4)\n"
  "v_mfma_f32_16x16x32_bf16 v[28:31], v[40:43], v[108:111], v[28:31]\n"
  "s_waitcnt lgkmcnt(3)\n"
  "v_mfma_f32_16x16x32_bf16 v[32:35], v[44:47], v[108:111], v[32:35]\n"
  "global_load_dwordx4 v[108:111], v0, s[60:61] offset:2048 sc1\n"
  "ds_read_b128 v[36:39], v5 offset:9216\n"
  "ds_read_b128 v[40:43], v5 offset:10240\n"
  "ds_read_b128 v[44:47], v5 offset:11264\n"
  "s_waitcnt vmcnt(18) lgkmcnt(5)\n"
  "v_mfma_f32_16x16x32_bf16 v[24:27], v[128:131], v[112:115], v[24:27]\n"
  "s_waitcnt lgkmcnt(4)\n"
  "v_mfma_f32_16x16x32_bf16 v[28:31], v[132:135], v[112:115], v[28:31]\n"
  "s_waitcnt lgkmcnt(3)\n"
  "v_mfma_f32_16x16x32_bf16 v[32:35], v[136:139], v[112:115], v[32:35]\n"
  "global_load_dwordx4 v[112:115], v0, s[60:61] offset:3072 sc1\n"
  "s_cmp_eq_u32 s50, 3\n"
  "s_cselect_b32 s74, 524288, 0\n"
  "s_sub_u32 s60, s60, s74\n"
  "s_subb_u32 s61, s61, 0\n"
  "s_add_u32 s60, s60, 65536\n"
  "s_addc_u32 s61, s61, 0\n"
  "ds_read_b128 v[128:131], v5 offset:12288\n"
  "ds_read_b128 v[132:135], v5 offset:13312\n"
  "ds_read_b128 v[136:139], v5 offset:14336\n"
  "s_waitcnt vmcnt(18) lgkmcnt(5)\n"
  "v_mfma_f32_16x16x32_bf16 v[24:27], v[36:39], v[116:119], v[24:27]\n"
  "s_waitcnt lgkmcnt(4)\n"
  "v_mfma_f32_16x16x32_bf16 v[28:31], v[40:43], v[116:119], v[28:31]\n"
  "s_waitcnt lgkmcnt(3)\n"
  "v_mfma_f32_16x16x32_bf16 v[32:35], v[44:47], v[116:119], v[32:35]\n"
  "global_load_dwordx4 v[116:119], v0, s[60:61] offset:0 sc1\n"
  "s_waitcnt vmcnt(18) lgkmcnt(2)\n"
  "v_mfma_f32_16x16x32_bf16 v[24:27], v[128:131], v[120:123], v[24:27]\n"
  "s_waitcnt lgkmcnt(1)\n"
  "v_mfma_f32_16x16x32_bf16 v[28:31], v[132:135], v[120:123], v[28:31]\n"
  "s_waitcnt lgkmcnt(0)\n"
  "v_mfma_f32_16x16x32_bf16 v[32:35], v[136:139], v[120:123], v[32:35]\n"
  "global_load_dwordx4 v[120:123], v0, s[60:61] offset:1024 sc1\n"
  "s_waitcnt vmcnt(18)\n"
  "v_mov_b32 v10, v9\n"
  "global_load_dwordx4 v[124:127], v0, s[60:61] offset:2048 sc1\n"
  "ds_write_b128 v11, v[12:15] offset:0\n"
  "ds_write_b128 v11, v[16:19] offset:1024\n"
  "ds_write_b128 v11, v[20:23] offset:2048\n"
  "ds_write_b128 v11, v[24:27] offset:3072\n"
  "ds_write_b128 v11, v[28:31] offset:4096\n"
  "ds_write_b128 v11, v[32:35] offset:5120\n"
  "v_sub_f32 v6, 1.0, v10\n"
  "v_cvt_pk_bf16_f32 v6, v6, v6\n"
  "v_lshlrev_b32 v6, 16, v6\n"
  "s_waitcnt lgkmcnt(0)\n"
  "s_barrier\n"
  "ds_read_b128 v[36:39], v3 offset:0\n"
  "ds_read_b128 v[40:43], v3 offset:6144\n"
  "ds_read_b128 v[44:47], v3 offset:12288\n"
  "ds_read_b128 v[48:51], v3 offset:18432\n"
  "ds_read_b128 v[12:15], v3 offset:3072\n"
  "ds_read_b128 v[16:19], v3 offset:9216\n"
  "ds_read_b128 v[20:23], v3 offset:15360\n"
  "ds_read_b128 v[24:27], v3 offset:21504\n"
  "s_bitcmp1_b32 s49, 0\n"
  "s_cselect_b32 s74, 4294942720, 0\n"
  "s_cselect_b32 s75, 0, 24576\n"
  "s_add_u32 s74, s74, s75\n"
  "v_add_u32 v11, s74, v11\n"
  "v_add_u32 v3, s74, v3\n"
  "s_waitcnt lgkmcnt(6)\n"
  "v_add_f32 v36, v36, v40\n"
  "s_waitcnt lgkmcnt(4)\n"
  "v_add_f32 v44, v44, v48\n"
  "v_add_f32 v36, v36, v44\n"
  "v_add_f32 v37, v37, v41\n"
  "v_add_f32 v45, v45, v49\n"
  "v_add_f32 v37, v37, v45\n"
  "v_add_f32 v38, v38, v42\n"
  "v_add_f32 v46, v46, v50\n"
  "v_add_f32 v38, v38, v46\n"
  "v_add_f32 v39, v39, v43\n"
  "v_add_f32 v47, v47, v51\n"
  "v_add_f32 v39, v39, v47\n"
  "v_cvt_pk_bf16_f32 v36, v36, v37\n"
  "v_and_b32 v37, 0xffff0000, v36\n"
  "v_lshlrev_b32 v36, 16, v36\n"
  "v_cvt_pk_bf16_f32 v38, v38, v39\n"
  "v_and_b32 v39, 0xffff0000, v38\n"
  "v_lshlrev_b32 v38, 16, v38\n"
  "v_mul_f32 v36, v6, v36\n"
  "v_mul_f32 v37, v6, v37\n"
  "v_mul_f32 v38, v6, v38\n"
  "v_mul_f32 v39, v6, v39\n"
  "v_cvt_pk_bf16_f32 v36, v36, v37\n"
  "v_and_b32 v37, 0xffff0000, v36\n"
  "v_lshlrev_b32 v36, 16, v36\n"
  "v_cvt_pk_bf16_f32 v38, v38, v39\n"
  "v_and_b32 v39, 0xffff0000, v38\n"
  "v_lshlrev_b32 v38, 16, v38\n"
  "s_waitcnt lgkmcnt(2)\n"
  "v_add_f32 v12, v12, v16\n"
  "s_waitcnt lgkmcnt(0)\n"
  "v_add_f32 v20, v20, v24\n"
  "v_add_f32 v12, v12, v20\n"
  "v_add_f32 v13, v13, v17\n"
  "v_add_f32 v21, v21, v25\n"
  "v_add_f32 v13, v13, v21\n"
  "v_add_f32 v14, v14, v18\n"
  "v_add_f32 v22, v22, v26\n"
  "v_add_f32 v14, v14, v22\n"
  "v_add_f32 v15, v15, v19\n"
  "v_add_f32 v23, v23, v27\n"
  "v_add_f32 v15, v15, v23\n"
  "v_cvt_pk_bf16_f32 v12, v12, v13\n"
  "v_and_b32 v13, 0xffff0000, v12\n"
  "v_lshlrev_b32 v12, 16, v12\n"
  "v_cvt_pk_bf16_f32 v14, v14, v15\n"
  "v_and_b32 v15, 0xffff0000, v14\n"
  "v_lshlrev_b32 v14, 16, v14\n"
  "v_mul_f32 v12, v10, v12\n"
  "v_mul_f32 v13, v10, v13\n"
  "v_mul_f32 v14, v10, v14\n"
  "v_mul_f32 v15, v10, v15\n"
  "v_cvt_pk_bf16_f32 v12, v12, v13\n"
  "v_and_b32 v13, 0xffff0000, v12\n"
  "v_lshlrev_b32 v12, 16, v12\n"
  "v_cvt_pk_bf16_f32 v14, v14, v15\n"
  "v_and_b32 v15, 0xffff0000, v14\n"
  "v_lshlrev_b32 v14, 16, v14\n"
  "v_add_f32 v36, v36, v12\n"
  "v_add_f32 v37, v37, v13\n"
  "v_add_f32 v38, v38, v14\n"
  "v_add_f32 v39, v39, v15\n"
  "v_cvt_pk_bf16_f32 v36, v36, v37\n"
  "v_cvt_pk_bf16_f32 v37, v38, v39\n"
  "s_bfm_b64 exec, 48, 0\n"
  "global_store_dwordx2 v4, v[36:37], s[40:41]\n"
  "s_mov_b64 exec, -1\n"
  "s_add_u32 s40, s40, 55296\n"
  "s_addc_u32 s41, s41, 0\n"
  "s_add_u32 s49, s49, 3\n"
  "s_branch OSEG\n"
  "ODRAIN:\n"
  "s_waitcnt vmcnt(18)\n"
  "v_mfma_f32_16x16x32_bf16 v[12:15], a[228:231], v[52:55], v[12:15]\n"
  "v_mfma_f32_16x16x32_bf16 v[16:19], a[232:235], v[52:55], v[16:19]\n"
  "v_mfma_f32_16x16x32_bf16 v[20:23], a[236:239], v[52:55], v[20:23]\n"
  "s_waitcnt vmcnt(17)\n"
  "v_mfma_f32_16x16x32_bf16 v[12:15], a[240:243], v[56:59], v[12:15]\n"
  "v_mfma_f32_16x16x32_bf16 v[16:19], a[244:247], v[56:59], v[16:19]\n"
  "v_mfma_f32_16x16x32_bf16 v[20:23], a[248:251], v[56:59], v[20:23]\n"
  "s_waitcnt vmcnt(16)\n"
  "v_mfma_f32_16x16x32_bf16 v[12:15], a[252:255], v[60:63], v[12:15]\n"
  "v_mfma_f32_16x16x32_bf16 v[16:19], v[140:143], v[60:63], v[16:19]\n"
  "v_mfma_f32_16x16x32_bf16 v[20:23], v[144:147], v[60:63], v[20:23]\n"
  "s_waitcnt vmcnt(15)\n"
  "v_mfma_f32_16x16x32_bf16 v[12:15], v[148:151], v[64:67], v[12:15]\n"
  "v_mfma_f32_16x16x32_bf16 v[16:19], v[152:155], v[64:67], v[16:19]\n"
  "v_mfma_f32_16x16x32_bf16 v[20:23], v[156:159], v[64:67], v[20:23]\n"
  "s_waitcnt vmcnt(14)\n"
  "v_mfma_f32_16x16x32_bf16 v[12:15], v[160:163], v[68:71], v[12:15]\n"
  "v_mfma_f32_16x16x32_bf16 v[16:19], v[164:167], v[68:71], v[16:19]\n"
  "v_mfma_f32_16x16x32_bf16 v[20:23], v[168:171], v[68:71], v[20:23]\n"
  "s_waitcnt vmcnt(13)\n"
  "v_mfma_f32_16x16x32_bf16 v[12:15], v[172:175], v[72:75], v[12:15]\n"
  "v_mfma_f32_16x16x32_bf16 v[16:19], v[176:179], v[72:75], v[16:19]\n"
  "v_mfma_f32_16x16x32_bf16 v[20:23], v[180:183], v[72:75], v[20:23]\n"
  "s_waitcnt vmcnt(12)\n"
  "v_mfma_f32_16x16x32_bf16 v[12:15], v[184:187], v[76:79], v[12:15]\n"
  "v_mfma_f32_16x16x32_bf16 v[16:19], v[188:191], v[76:79], v[16:19]\n"
  "v_mfma_f32_16x16x32_bf16 v[20:23], v[192:195], v[76:79], v[20:23]\n"
  "s_waitcnt vmcnt(11)\n"
  "v_mfma_f32_16x16x32_bf16 v[12:15], v[196:199], v[80:83], v[12:15]\n"
  "v_mfma_f32_16x16x32_bf16 v[16:19], v[200:203], v[80:83], v[16:19]\n"
  "v_mfma_f32_16x16x32_bf16 v[20:23], v[204:207], v[80:83], v[20:23]\n"
  "s_waitcnt vmcnt(10)\n"
  "v_mfma_f32_16x16x32_bf16 v[12:15], v[208:211], v[84:87], v[12:15]\n"
  "v_mfma_f32_16x16x32_bf16 v[16:19], v[212:215], v[84:87], v[16:19]\n"
  "v_mfma_f32_16x16x32_bf16 v[20:23], v[216:219], v[84:87], v[20:23]\n"
  "s_waitcnt vmcnt(9)\n"
  "v_mfma_f32_16x16x32_bf16 v[12:15], v[220:223], v[88:91], v[12:15]\n"
  "v_mfma_f32_16x16x32_bf16 v[16:19], v[224:227], v[88:91], v[16:19]\n"
  "v_mfma_f32_16x16x32_bf16 v[20:23], v[228:231], v[88:91], v[20:23]\n"
  "s_waitcnt vmcnt(8)\n"
  "v_mfma_f32_16x16x32_bf16 v[12:15], v[232:235], v[92:95], v[12:15]\n"
  "v_mfma_f32_16x16x32_bf16 v[16:19], v[236:239], v[92:95], v[16:19]\n"
  "v_mfma_f32_16x16x32_bf16 v[20:23], v[240:243], v[92:95], v[20:23]\n"
  "ds_read_b128 v[36:39], v5 offset:15360\n"
  "ds_read_b128 v[40:43], v5 offset:16384\n"
  "ds_read_b128 v[44:47], v5 offset:17408\n"
  "s_waitcnt vmcnt(7)\n"
  "v_mfma_f32_16x16x32_bf16 v[12:15], v[244:247], v[96:99], v[12:15]\n"
  "v_mfma_f32_16x16x32_bf16 v[16:19], v[248:251], v[96:99], v[16:19]\n"
  "v_mfma_f32_16x16x32_bf16 v[20:23], v[252:255], v[96:99], v[20:23]\n"
  "ds_read_b128 v[128:131], v5 offset:0\n"
  "ds_read_b128 v[132:135], v5 offset:1024\n"
  "ds_read_b128 v[136:139], v5 offset:2048\n"
  "s_waitcnt vmcnt(6) lgkmcnt(5)\n"
  "v_mfma_f32_16x16x32_bf16 v[12:15], v[36:39], v[100:103], v[12:15]\n"
  "s_waitcnt lgkmcnt(4)\n"
  "v_mfma_f32_16x16x32_bf16 v[16:19], v[40:43], v[100:103], v[16:19]\n"
  "s_waitcnt lgkmcnt(3)\n"
  "v_mfma_f32_16x16x32_bf16 v[20:23], v[44:47], v[100:103], v[20:23]\n"
  "ds_read_b128 v[36:39], v5 offset:3072\n"
  "ds_read_b128 v[40:43], v5 offset:4096\n"
  "ds_read_b128 v[44:47], v5 offset:5120\n"
  "s_waitcnt vmcnt(5) lgkmcnt(5)\n"
  "v_mfma_f32_16x16x32_bf16 v[24:27], v[128:131], v[104:107], 0\n"
  "s_waitcnt lgkmcnt(4)\n"
  "v_mfma_f32_16x16x32_bf16 v[28:31], v[132:135], v[104:107], 0\n"
  "s_waitcnt lgkmcnt(3)\n"
  "v_mfma_f32_16x16x32_bf16 v[32:35], v[136:139], v[104:107], 0\n"
  "ds_read_b128 v[128:131], v5 offset:6144\n"
  "ds_read_b128 v[132:135], v5 offset:7168\n"
  "ds_read_b128 v[136:139], v5 offset:8192\n"
  "s_waitcnt vmcnt(4) lgkmcnt(5)\n"
  "v_mfma_f32_16x16x32_bf16 v[24:27], v[36:39], v[108:111], v[24:27]\n"
  "s_waitcnt lgkmcnt(4)\n"
  "v_mfma_f32_16x16x32_bf16 v[28:31], v[40:43], v[108:111], v[28:31]\n"
  "s_waitcnt lgkmcnt(3)\n"
  "v_mfma_f32_16x16x32_bf16 v[32:35], v[44:47], v[108:111], v[32:35]\n"
  "ds_read_b128 v[36:39], v5 offset:9216\n"
  "ds_read_b128 v[40:43], v5 offset:10240\n"
  "ds_read_b128 v[44:47], v5 offset:11264\n"
  "s_waitcnt vmcnt(3) lgkmcnt(5)\n"
  "v_mfma_f32_16x16x32_bf16 v[24:27], v[128:131], v[112:115], v[24:27]\n"
  "s_waitcnt lgkmcnt(4)\n"
  "v_mfma_f32_16x16x32_bf16 v[28:31], v[132:135], v[112:115], v[28:31]\n"
  "s_waitcnt lgkmcnt(3)\n"
  "v_mfma_f32_16x16x32_bf16 v[32:35], v[136:139], v[112:115], v[32:35]\n"
  "ds_read_b128 v[128:131], v5 offset:12288\n"
  "ds_read_b128 v[132:135], v5 offset:13312\n"
  "ds_read_b128 v[136:139], v5 offset:14336\n"
  "s_waitcnt vmcnt(2) lgkmcnt(5)\n"
  "v_mfma_f32_16x16x32_bf16 v[24:27], v[36:39], v[116:119], v[24:27]\n"
  "s_waitcnt lgkmcnt(4)\n"
  "v_mfma_f32_16x16x32_bf16 v[28:31], v[40:43], v[116:119], v[28:31]\n"
  "s_waitcnt lgkmcnt(3)\n"
  "v_mfma_f32_16x16x32_bf16 v[32:35], v[44:47], v[116:119], v[32:35]\n"
  "s_waitcnt vmcnt(1) lgkmcnt(2)\n"
  "v_mfma_f32_16x16x32_bf16 v[24:27], v[128:131], v[120:123], v[24:27]\n"
  "s_waitcnt lgkmcnt(1)\n"
  "v_mfma_f32_16x16x32_bf16 v[28:31], v[132:135], v[120:123], v[28:31]\n"
  "s_waitcnt lgkmcnt(0)\n"
  "v_mfma_f32_16x16x32_bf16 v[32:35], v[136:139], v[120:123], v[32:35]\n"
  "s_waitcnt vmcnt(0)\n"
  "v_mov_b32 v10, v9\n"
  "ds_write_b128 v11, v[12:15] offset:0\n"
  "ds_write_b128 v11, v[16:19] offset:1024\n"
  "ds_write_b128 v11, v[20:23] offset:2048\n"
  "ds_write_b128 v11, v[24:27] offset:3072\n"
  "ds_write_b128 v11, v[28:31] offset:4096\n"
  "s_nop 0\n"
  "ds_write_b128 v11, v[32:35] offset:5120\n"
  "v_sub_f32 v6, 1.0, v10\n"
  "v_cvt_pk_bf16_f32 v6, v6, v6\n"
  "v_lshlrev_b32 v6, 16, v6\n"
  "s_waitcnt lgkmcnt(0)\n"
  "s_barrier\n"
  "ds_read_b128 v[36:39], v3 offset:0\n"
  "ds_read_b128 v[40:43], v3 offset:6144\n"
  "ds_read_b128 v[44:47], v3 offset:12288\n"
  "ds_read_b128 v[48:51], v3 offset:18432\n"
  "ds_read_b128 v[12:15], v3 offset:3072\n"
  "ds_read_b128 v[16:19], v3 offset:9216\n"
  "ds_read_b128 v[20:23], v3 offset:15360\n"
  "ds_read_b128 v[24:27], v3 offset:21504\n"
  "s_bitcmp1_b32 s49, 0\n"
  "s_cselect_b32 s74, 4294942720, 0\n"
  "s_cselect_b32 s75, 0, 24576\n"
  "s_add_u32 s74, s74, s75\n"
  "v_add_u32 v11, s74, v11\n"
  "v_add_u32 v3, s74, v3\n"
  "s_waitcnt lgkmcnt(6)\n"
  "v_add_f32 v36, v36, v40\n"
  "s_waitcnt lgkmcnt(4)\n"
  "v_add_f32 v44, v44, v48\n"
  "v_add_f32 v36, v36, v44\n"
  "v_add_f32 v37, v37, v41\n"
  "v_add_f32 v45, v45, v49\n"
  "v_add_f32 v37, v37, v45\n"
  "v_add_f32 v38, v38, v42\n"
  "v_add_f32 v46, v46, v50\n"
  "v_add_f32 v38, v38, v46\n"
  "v_add_f32 v39, v39, v43\n"
  "v_add_f32 v47, v47, v51\n"
  "v_add_f32 v39, v39, v47\n"
  "v_cvt_pk_bf16_f32 v36, v36, v37\n"
  "v_and_b32 v37, 0xffff0000, v36\n"
  "v_lshlrev_b32 v36, 16, v36\n"
  "v_cvt_pk_bf16_f32 v38, v38, v39\n"
  "v_and_b32 v39, 0xffff0000, v38\n"
  "v_lshlrev_b32 v38, 16, v38\n"
  "v_mul_f32 v36, v6, v36\n"
  "v_mul_f32 v37, v6, v37\n"
  "v_mul_f32 v38, v6, v38\n"
  "v_mul_f32 v39, v6, v39\n"
  "v_cvt_pk_bf16_f32 v36, v36, v37\n"
  "v_and_b32 v37, 0xffff0000, v36\n"
  "v_lshlrev_b32 v36, 16, v36\n"
  "v_cvt_pk_bf16_f32 v38, v38, v39\n"
  "v_and_b32 v39, 0xffff0000, v38\n"
  "v_lshlrev_b32 v38, 16, v38\n"
  "s_waitcnt lgkmcnt(2)\n"
  "v_add_f32 v12, v12, v16\n"
  "s_waitcnt lgkmcnt(0)\n"
  "v_add_f32 v20, v20, v24\n"
  "v_add_f32 v12, v12, v20\n"
  "v_add_f32 v13, v13, v17\n"
  "v_add_f32 v21, v21, v25\n"
  "v_add_f32 v13, v13, v21\n"
  "v_add_f32 v14, v14, v18\n"
  "v_add_f32 v22, v22, v26\n"
  "v_add_f32 v14, v14, v22\n"
  "v_add_f32 v15, v15, v19\n"
  "v_add_f32 v23, v23, v27\n"
  "v_add_f32 v15, v15, v23\n"
  "v_cvt_pk_bf16_f32 v12, v12, v13\n"
  "v_and_b32 v13, 0xffff0000, v12\n"
  "v_lshlrev_b32 v12, 16, v12\n"
  "v_cvt_pk_bf16_f32 v14, v14, v15\n"
  "v_and_b32 v15, 0xffff0000, v14\n"
  "v_lshlrev_b32 v14, 16, v14\n"
  "v_mul_f32 v12, v10, v12\n"
  "v_mul_f32 v13, v10, v13\n"
  "v_mul_f32 v14, v10, v14\n"
  "v_mul_f32 v15, v10, v15\n"
  "v_cvt_pk_bf16_f32 v12, v12, v13\n"
  "v_and_b32 v13, 0xffff0000, v12\n"
  "v_lshlrev_b32 v12, 16, v12\n"
  "v_cvt_pk_bf16_f32 v14, v14, v15\n"
  "v_and_b32 v15, 0xffff0000, v14\n"
  "v_lshlrev_b32 v14, 16, v14\n"
  "v_add_f32 v36, v36, v12\n"
  "v_add_f32 v37, v37, v13\n"
  "v_add_f32 v38, v38, v14\n"
  "v_add_f32 v39, v39, v15\n"
  "v_cvt_pk_bf16_f32 v36, v36, v37\n"
  "v_cvt_pk_bf16_f32 v37, v38, v39\n"
  "s_bfm_b64 exec, 48, 0\n"
  "global_store_dwordx2 v4, v[36:37], s[40:41]\n"
  "s_mov_b64 exec, -1\n"
  "s_add_u32 s40, s40, 55296\n"
  "s_addc_u32 s41, s41, 0\n"
  "s_add_u32 s49, s49, 3\n"
  "s_cmp_lt_u32 s49, 128\n"
  "s_cbranch_scc1 OPOLL\n"
  "OQUIT:\n"
  "s_endpgm\n"
      : "+{v[0:15]}"(vin), "+{v[16:31]}"(wa), "+{s[36:43]}"(sa), "+{s[44:59]}"(sb) : "{s[4:5]}"(pol) : CLOB);
  } else {
    const int c = fd * 4 + tk - 25, grp = min(lane / 10, 5), pos = lane % 10;
    const bool real = lane < 60 && pos < 9;
    const u16* src = p.wqkv + (size_t)wave * 1728 + (grp >> 1) * DM + 36 * c + (grp & 1) * 18 + 2 * min(pos, 8);
#pragma unroll 1
    for (int b = 0; b < 2; ++b) {
      unsigned v[72];
#pragma unroll
      for (int i = 0; i < 72; ++i) v[i] = *(const gu32*)(src + (size_t)(b * 72 + i) * 4 * 1728);
#pragma unroll
      for (int i = 0; i < 72; ++i) if (lane != 60) *(LAS unsigned*)(lds + ((b * 72 + i) * 4 + wave) * 264 + lane * 4) = real ? v[i] : 0u;
    }
#pragma unroll
    for (int i = 0; i < 3; ++i) { const int r = min(tid + 256 * i, 575); *(LAS unsigned*)(lds + r * 264 + 240) = p.gate[r]; }
    wg_barrier();
    const int rt = wave & 1, hh = wave >> 1;
    constexpr int RS = 304, OI = 64 * RS;
    vin[0] = (8 * g + qq) * 264 + wave * 64 + c4 * 8; vin[1] = x_src(wave, lane); vin[2] = x_frag(lane);
    vin[3] = OI + (16 * rt + n) * 80 + hh * 40 + g * 8; vin[4] = OI + (8 * wave + (lane & 7)) * 80 + min(lane >> 3, 4) * 16;
    vin[5] = (wave >> 1) * 20480 + (lane >> 3) * 256 + ((wave & 1) * 8 + (lane & 7)) * 16; vin[6] = (lane & 31) * RS + 288;
    vin[7] = (n & ~1) - 4 * g; vin[8] = (n & 1) ? 0xffff0000u : 0xffffu;
#pragma unroll
    for (int t = 0; t < 2; ++t) { const int g4 = 8 * wave + 4 * t + g; vin[10 + t] = n * RS + 48 * (g4 / 5) + 8 * (g4 % 5); }
    vin[12] = g == 3 ? OI + 32 * 80 : (16 * rt + n) * RS + 48 * hh + 16 * g;
    vin[13] = ((16 * rt - 16 + n) & 63) * RS + 96 + 48 * hh + 16 * min(g, 2);
    vin[14] = ((16 * rt - 16 + 4 * g + qq) & 63) * RS + 192 + 48 * hh + 8 * c4;
    PTR(sa, 2, p.x);
    sb[0] = WO_OA + c * 1280; sb[1] = WO_G + c * 128; sb[2] = (C_AP ^ cx) + c * 128; sb[3] = wave; sb[4] = WO_ST + (role * 4 + wave) * 4096;
    UNIFORM(sa, 8); UNIFORM(sb, 16);
    asm volatile(
  "s_nop 4\n"
  "s_lshl_b32 s80, s47, 10\n"
  "v_add_u32 v9, 0, v0\n"
  "ds_read_b64_tr_b16 a[0:1], v9 offset:0\n"
  "ds_read_b64_tr_b16 a[2:3], v9 offset:1056\n"
  "ds_read_b64_tr_b16 a[4:5], v9 offset:8448\n"
  "ds_read_b64_tr_b16 a[6:7], v9 offset:9504\n"
  "ds_read_b64_tr_b16 a[8:9], v9 offset:16896\n"
  "ds_read_b64_tr_b16 a[10:11], v9 offset:17952\n"
  "ds_read_b64_tr_b16 a[12:13], v9 offset:25344\n"
  "ds_read_b64_tr_b16 a[14:15], v9 offset:26400\n"
  "ds_read_b64_tr_b16 a[16:17], v9 offset:33792\n"
  "ds_read_b64_tr_b16 a[18:19], v9 offset:34848\n"
  "ds_read_b64_tr_b16 a[20:21], v9 offset:42240\n"
  "ds_read_b64_tr_b16 a[22:23], v9 offset:43296\n"
  "v_add_u32 v9, 32, v0\n"
  "ds_read_b64_tr_b16 a[72:73], v9 offset:0\n"
  "ds_read_b64_tr_b16 a[74:75], v9 offset:1056\n"
  "ds_read_b64_tr_b16 a[76:77], v9 offset:8448\n"
  "ds_read_b64_tr_b16 a[78:79], v9 offset:9504\n"
  "ds_read_b64_tr_b16 a[80:81], v9 offset:16896\n"
  "ds_read_b64_tr_b16 a[82:83], v9 offset:17952\n"
  "ds_read_b64_tr_b16 a[84:85], v9 offset:25344\n"
  "ds_read_b64_tr_b16 a[86:87], v9 offset:26400\n"
  "ds_read_b64_tr_b16 a[88:89], v9 offset:33792\n"
  "ds_read_b64_tr_b16 a[90:91], v9 offset:34848\n"
  "ds_read_b64_tr_b16 a[92:93], v9 offset:42240\n"
  "ds_read_b64_tr_b16 a[94:95], v9 offset:43296\n"
  "v_add_u32 v9, 50688, v0\n"
  "ds_read_b64_tr_b16 a[24:25], v9 offset:0\n"
  "ds_read_b64_tr_b16 a[26:27], v9 offset:1056\n"
  "ds_read_b64_tr_b16 a[28:29], v9 offset:8448\n"
  "ds_read_b64_tr_b16 a[30:31], v9 offset:9504\n"
  "ds_read_b64_tr_b16 a[32:33], v9 offset:16896\n"
  "ds_read_b64_tr_b16 a[34:35], v9 offset:17952\n"
  "ds_read_b64_tr_b16 a[36:37], v9 offset:25344\n"
  "ds_read_b64_tr_b16 a[38:39], v9 offset:26400\n"
  "ds_read_b64_tr_b16 a[40:41], v9 offset:33792\n"
  "ds_read_b64_tr_b16 a[42:43], v9 offset:34848\n"
  "ds_read_b64_tr_b16 a[44:45], v9 offset:42240\n"
  "ds_read_b64_tr_b16 a[46:47], v9 offset:43296\n"
  "v_add_u32 v9, 50720, v0\n"
  "ds_read_b64_tr_b16 a[96:97], v9 offset:0\n"
  "ds_read_b64_tr_b16 a[98:99], v9 offset:1056\n"
  "ds_read_b64_tr_b16 a[100:101], v9 offset:8448\n"
  "ds_read_b64_tr_b16 a[102:103], v9 offset:9504\n"
  "ds_read_b64_tr_b16 a[104:105], v9 offset:16896\n"
  "ds_read_b64_tr_b16 a[106:107], v9 offset:17952\n"
  "ds_read_b64_tr_b16 a[108:109], v9 offset:25344\n"
  "ds_read_b64_tr_b16 a[110:111], v9 offset:26400\n"
  "ds_read_b64_tr_b16 a[112:113], v9 offset:33792\n"
  "ds_read_b64_tr_b16 a[114:115], v9 offset:34848\n"
  "ds_read_b64_tr_b16 a[116:117], v9 offset:42240\n"
  "ds_read_b64_tr_b16 a[118:119], v9 offset:43296\n"
  "v_add_u32 v9, 101376, v0\n"
  "ds_read_b64_tr_b16 a[48:49], v9 offset:0\n"
  "ds_read_b64_tr_b16 a[50:51], v9 offset:1056\n"
  "ds_read_b64_tr_b16 a[52:53], v9 offset:8448\n"
  "ds_read_b64_tr_b16 a[54:55], v9 offset:9504\n"
  "ds_read_b64_tr_b16 a[56:57], v9 offset:16896\n"
  "ds_read_b64_tr_b16 a[58:59], v9 offset:17952\n"
  "ds_read_b64_tr_b16 a[60:61], v9 offset:25344\n"
  "ds_read_b64_tr_b16 a[62:63], v9 offset:26400\n"
  "ds_read_b64_tr_b16 a[64:65], v9 offset:33792\n"
  "ds_read_b64_tr_b16 a[66:67], v9 offset:34848\n"
  "ds_read_b64_tr_b16 a[68:69], v9 offset:42240\n"
  "ds_read_b64_tr_b16 a[70:71], v9 offset:43296\n"
  "v_add_u32 v9, 101408, v0\n"
  "ds_read_b64_tr_b16 a[120:121], v9 offset:0\n"
  "ds_read_b64_tr_b16 a[122:123], v9 offset:1056\n"
  "ds_read_b64_tr_b16 a[124:125], v9 offset:8448\n"
  "ds_read_b64_tr_b16 a[126:127], v9 offset:9504\n"
  "ds_read_b64_tr_b16 a[128:129], v9 offset:16896\n"
  "ds_read_b64_tr_b16 a[130:131], v9 offset:17952\n"
  "ds_read_b64_tr_b16 a[132:133], v9 offset:25344\n"
  "ds_read_b64_tr_b16 a[134:135], v9 offset:26400\n"
  "ds_read_b64_tr_b16 a[136:137], v9 offset:33792\n"
  "ds_read_b64_tr_b16 a[138:139], v9 offset:34848\n"
  "ds_read_b64_tr_b16 a[140:141], v9 offset:42240\n"
  "ds_read_b64_tr_b16 a[142:143], v9 offset:43296\n"
  "s_waitcnt lgkmcnt(0)\n"
  "s_barrier\n"
  "v_mbcnt_lo_u32_b32 v127, -1, 0\n"
  "v_mbcnt_hi_u32_b32 v127, -1, v127\n"
  "v_lshlrev_b32 v9, 4, v127\n"
  "v_add_u32 v9, s80, v9\n"
  "v_lshlrev_b32 v127, 2, v127\n"
  "v_mov_b64 v[120:121], 0\n"
  "v_mov_b64 v[122:123], 0\n"
  "v_mov_b32 v128, 0xce6e6b28\n"
  "ds_write_b128 v9, v[120:123] offset:0\n"
  "ds_write_b128 v9, v[120:123] offset:4096\n"
  "ds_write_b128 v9, v[120:123] offset:8192\n"
  "ds_write_b128 v9, v[120:123] offset:12288\n"
  "ds_write_b128 v9, v[120:123] offset:16384\n"
  "ds_write_b128 v9, v[120:123] offset:20480\n"
  "s_waitcnt lgkmcnt(0)\n"
  "s_barrier\n"
  "s_add_u32 s58, s80, 22272\n"
  "s_add_u32 m0, s58, 0\n"
  "s_nop 0\n"
  "global_load_lds_dwordx4 v1, s[38:39] offset:0\n"
  "s_add_u32 m0, s58, 3968\n"
  "s_nop 0\n"
  "global_load_lds_dwordx4 v1, s[38:39] offset:128\n"
  "s_add_u32 m0, s58, 7936\n"
  "s_nop 0\n"
  "global_load_lds_dwordx4 v1, s[38:39] offset:256\n"
  "s_add_u32 m0, s58, 11904\n"
  "s_nop 0\n"
  "global_load_lds_dwordx4 v1, s[38:39] offset:384\n"
  "s_add_u32 m0, s58, 15872\n"
  "s_nop 0\n"
  "global_load_lds_dwordx4 v1, s[38:39] offset:512\n"
  "s_add_u32 m0, s58, 19840\n"
  "s_nop 0\n"
  "global_load_lds_dwordx4 v1, s[38:39] offset:640\n"
  "s_add_u32 m0, s58, 23808\n"
  "s_nop 0\n"
  "global_load_lds_dwordx4 v1, s[38:39] offset:768\n"
  "s_add_u32 m0, s58, 27776\n"
  "s_nop 0\n"
  "global_load_lds_dwordx4 v1, s[38:39] offset:896\n"
  "s_add_u32 m0, s58, 31744\n"
  "s_nop 0\n"
  "global_load_lds_dwordx4 v1, s[38:39] offset:1024\n"
  "v_add_u32 v68, 0, v13\n"
  "v_subrev_u32 v9, 19456, v68\n"
  "v_min_u32 v68, v68, v9\n"
  "v_add_u32 v72, 0, v14\n"
  "v_subrev_u32 v9, 19456, v72\n"
  "v_min_u32 v72, v72, v9\n"
  "v_add_u32 v69, 4864, v13\n"
  "v_subrev_u32 v9, 19456, v69\n"
  "v_min_u32 v69, v69, v9\n"
  "v_add_u32 v73, 4864, v14\n"
  "v_subrev_u32 v9, 19456, v73\n"
  "v_min_u32 v73, v73, v9\n"
  "v_add_u32 v70, 9728, v13\n"
  "v_subrev_u32 v9, 19456, v70\n"
  "v_min_u32 v70, v70, v9\n"
  "v_add_u32 v74, 9728, v14\n"
  "v_subrev_u32 v9, 19456, v74\n"
  "v_min_u32 v74, v74, v9\n"
  "v_add_u32 v71, 14592, v13\n"
  "v_subrev_u32 v9, 19456, v71\n"
  "v_min_u32 v71, v71, v9\n"
  "v_add_u32 v75, 14592, v14\n"
  "v_subrev_u32 v9, 19456, v75\n"
  "v_min_u32 v75, v75, v9\n"
  "v_mov_b32 v66, v12\n"
  "v_add_u32 v67, 9728, v12\n"
  "v_min_u32 v67, 22016, v67\n"
  "v_add_u32 v9, 16, v7\n"
  "v_cmp_gt_u32 s[8:9], 15, v9\n"
  "v_add_u32 v9, 14, v7\n"
  "v_cmp_gt_u32 s[10:11], 15, v9\n"
  "v_add_u32 v9, 0, v7\n"
  "v_cmp_gt_u32 s[12:13], 15, v9\n"
  "v_add_u32 v9, -2, v7\n"
  "v_cmp_gt_u32 s[14:15], 15, v9\n"
  "s_mov_b64 s[16:17], s[8:9]\n"
  "s_mov_b64 s[18:19], s[10:11]\n"
  "s_bitcmp1_b32 s47, 0\n"
  "s_cselect_b64 s[8:9], s[8:9], 0\n"
  "s_cselect_b64 s[10:11], s[10:11], 0\n"
  "s_add_u32 s60, s36, s44\n"
  "s_addc_u32 s61, s37, 0\n"
  "s_add_u32 s62, s36, s45\n"
  "s_addc_u32 s63, s37, 0\n"
  "s_add_u32 s64, s36, s46\n"
  "s_addc_u32 s65, s37, 0\n"
  "s_cmp_eq_u32 s47, 3\n"
  "s_cselect_b32 s68, -1, 0\n"
  "s_mov_b32 s69, 0\n"
  "s_mov_b32 s70, 0xaaaaaaaa\n"
  "s_mov_b32 s71, 0xaaaaaaaa\n"
  "s_mov_b32 s56, -1\n"
  "s_waitcnt vmcnt(0)\n"
  "s_waitcnt vmcnt(2) lgkmcnt(0)\n"
  "s_barrier\n"
  "v_add_u32 v64, 22272, v2\n"
  "v_xor_b32 v65, 64, v64\n"
  "s_add_u32 s58, s80, 59136\n"
  "s_cmp_lt_i32 s56, 62\n"
  "s_cselect_b32 s74, 36864, 0\n"
  "s_add_u32 s38, s38, s74\n"
  "s_addc_u32 s39, s39, 0\n"
  "ds_read_b128 v[16:19], v64 offset:0\n"
  "ds_read_b128 v[20:23], v64 offset:2048\n"
  "ds_read_b128 v[24:27], v65 offset:0\n"
  "ds_read_b128 v[28:31], v65 offset:2048\n"
  "ds_read_b128 v[32:35], v64 offset:4096\n"
  "ds_read_b128 v[36:39], v64 offset:6144\n"
  "s_waitcnt lgkmcnt(5)\n"
  "v_mfma_f32_16x16x32_bf16 v[48:51], a[0:3], v[16:19], 0\n"
  "ds_read_b128 v[40:43], v65 offset:4096\n"
  "s_waitcnt lgkmcnt(5)\n"
  "v_mfma_f32_16x16x32_bf16 v[52:55], a[0:3], v[20:23], 0\n"
  "ds_read_b128 v[44:47], v65 offset:6144\n"
  "v_mfma_f32_16x16x32_bf16 v[56:59], a[72:75], v[16:19], 0\n"
  "v_mfma_f32_16x16x32_bf16 v[60:63], a[72:75], v[20:23], 0\n"
  "s_waitcnt lgkmcnt(5)\n"
  "v_mfma_f32_16x16x32_bf16 v[48:51], a[4:7], v[24:27], v[48:51]\n"
  "ds_read_b128 v[16:19], v64 offset:8192\n"
  "s_waitcnt lgkmcnt(5)\n"
  "v_mfma_f32_16x16x32_bf16 v[52:55], a[4:7], v[28:31], v[52:55]\n"
  "ds_read_b128 v[20:23], v64 offset:10240\n"
  "v_mfma_f32_16x16x32_bf16 v[56:59], a[76:79], v[24:27], v[56:59]\n"
  "s_add_u32 m0, s58, 0\n"
  "s_nop 0\n"
  "global_load_lds_dwordx4 v1, s[38:39] offset:0\n"
  "v_mfma_f32_16x16x32_bf16 v[60:63], a[76:79], v[28:31], v[60:63]\n"
  "s_waitcnt lgkmcnt(5)\n"
  "v_mfma_f32_16x16x32_bf16 v[48:51], a[8:11], v[32:35], v[48:51]\n"
  "ds_read_b128 v[24:27], v65 offset:8192\n"
  "s_waitcnt lgkmcnt(5)\n"
  "v_mfma_f32_16x16x32_bf16 v[52:55], a[8:11], v[36:39], v[52:55]\n"
  "ds_read_b128 v[28:31], v65 offset:10240\n"
  "v_mfma_f32_16x16x32_bf16 v[56:59], a[80:83], v[32:35], v[56:59]\n"
  "s_add_u32 m0, s58, 3968\n"
  "s_nop 0\n"
  "global_load_lds_dwordx4 v1, s[38:39] offset:128\n"
  "v_mfma_f32_16x16x32_bf16 v[60:63], a[80:83], v[36:39], v[60:63]\n"
  "s_waitcnt lgkmcnt(5)\n"
  "v_mfma_f32_16x16x32_bf16 v[48:51], a[12:15], v[40:43], v[48:51]\n"
  "ds_read_b128 v[32:35], v64 offset:12288\n"
  "s_waitcnt lgkmcnt(5)\n"
  "v_mfma_f32_16x16x32_bf16 v[52:55], a[12:15], v[44:47], v[52:55]\n"
  "ds_read_b128 v[36:39], v64 offset:14336\n"
  "v_mfma_f32_16x16x32_bf16 v[56:59], a[84:87], v[40:43], v[56:59]\n"
  "s_add_u32 m0, s58, 7936\n"
  "s_nop 0\n"
  "global_load_lds_dwordx4 v1, s[38:39] offset:256\n"
  "v_mfma_f32_16x16x32_bf16 v[60:63], a[84:87], v[44:47], v[60:63]\n"
  "s_waitcnt lgkmcnt(5)\n"
  "v_mfma_f32_16x16x32_bf16 v[48:51], a[16:19], v[16:19], v[48:51]\n"
  "ds_read_b128 v[40:43], v65 offset:12288\n"
  "s_waitcnt lgkmcnt(5)\n"
  "v_mfma_f32_16x16x32_bf16 v[52:55], a[16:19], v[20:23], v[52:55]\n"
  "ds_read_b128 v[44:47], v65 offset:14336\n"
  "v_mfma_f32_16x16x32_bf16 v[56:59], a[88:91], v[16:19], v[56:59]\n"
  "s_add_u32 m0, s58, 11904\n"
  "s_nop 0\n"
  "global_load_lds_dwordx4 v1, s[38:39] offset:384\n"
  "v_mfma_f32_16x16x32_bf16 v[60:63], a[88:91], v[20:23], v[60:63]\n"
  "s_waitcnt lgkmcnt(5)\n"
  "v_mfma_f32_16x16x32_bf16 v[48:51], a[20:23], v[24:27], v[48:51]\n"
  "ds_read_b128 v[16:19], v64 offset:16384\n"
  "s_waitcnt lgkmcnt(5)\n"
  "v_mfma_f32_16x16x32_bf16 v[52:55], a[20:23], v[28:31], v[52:55]\n"
  "ds_read_b128 v[20:23], v64 offset:18432\n"
  "v_mfma_f32_16x16x32_bf16 v[56:59], a[92:95], v[24:27], v[56:59]\n"
  "s_add_u32 m0, s58, 15872\n"
  "s_nop 0\n"
  "global_load_lds_dwordx4 v1, s[38:39] offset:512\n"
  "v_mfma_f32_16x16x32_bf16 v[60:63], a[92:95], v[28:31], v[60:63]\n"
  "s_waitcnt lgkmcnt(5)\n"
  "v_mfma_f32_16x16x32_bf16 v[48:51], a[24:27], v[32:35], v[48:51]\n"
  "ds_read_b128 v[24:27], v65 offset:16384\n"
  "s_waitcnt lgkmcnt(5)\n"
  "v_mfma_f32_16x16x32_bf16 v[52:55], a[24:27], v[36:39], v[52:55]\n"
  "ds_read_b128 v[28:31], v65 offset:18432\n"
  "v_mfma_f32_16x16x32_bf16 v[56:59], a[96:99], v[32:35], v[56:59]\n"
  "s_add_u32 m0, s58, 19840\n"
  "s_nop 0\n"
  "global_load_lds_dwordx4 v1, s[38:39] offset:640\n"
  "v_mfma_f32_16x16x32_bf16 v[60:63], a[96:99], v[36:39], v[60:63]\n"
  "s_waitcnt lgkmcnt(5)\n"
  "v_mfma_f32_16x16x32_bf16 v[48:51], a[28:31], v[40:43], v[48:51]\n"
  "ds_read_b128 v[32:35], v64 offset:20480\n"
  "s_waitcnt lgkmcnt(5)\n"
  "v_mfma_f32_16x16x32_bf16 v[52:55], a[28:31], v[44:47], v[52:55]\n"
  "ds_read_b128 v[36:39], v64 offset:22528\n"
  "v_mfma_f32_16x16x32_bf16 v[56:59], a[100:103], v[40:43], v[56:59]\n"
  "s_add_u32 m0, s58, 23808\n"
  "s_nop 0\n"
  "global_load_lds_dwordx4 v1, s[38:39] offset:768\n"
  "v_mfma_f32_16x16x32_bf16 v[60:63], a[100:103], v[44:47], v[60:63]\n"
  "s_waitcnt lgkmcnt(5)\n"
  "v_mfma_f32_16x16x32_bf16 v[48:51], a[32:35], v[16:19], v[48:51]\n"
  "ds_read_b128 v[40:43], v65 offset:20480\n"
  "s_waitcnt lgkmcnt(5)\n"
  "v_mfma_f32_16x16x32_bf16 v[52:55], a[32:35], v[20:23], v[52:55]\n"
  "ds_read_b128 v[44:47], v65 offset:22528\n"
  "v_mfma_f32_16x16x32_bf16 v[56:59], a[104:107], v[16:19], v[56:59]\n"
  "s_add_u32 m0, s58, 27776\n"
  "s_nop 0\n"
  "global_load_lds_dwordx4 v1, s[38:39] offset:896\n"
  "v_mfma_f32_16x16x32_bf16 v[60:63], a[104:107], v[20:23], v[60:63]\n"
  "s_waitcnt lgkmcnt(5)\n"
  "v_mfma_f32_16x16x32_bf16 v[48:51], a[36:39], v[24:27], v[48:51]\n"
  "ds_read_b128 v[16:19], v64 offset:24576\n"
  "s_waitcnt lgkmcnt(5)\n"
  "v_mfma_f32_16x16x32_bf16 v[52:55], a[36:39], v[28:31], v[52:55]\n"
  "ds_read_b128 v[20:23], v64 offset:26624\n"
  "v_mfma_f32_16x16x32_bf16 v[56:59], a[108:111], v[24:27], v[56:59]\n"
  "s_add_u32 m0, s58, 31744\n"
  "s_nop 0\n"
  "global_load_lds_dwordx4 v1, s[38:39] offset:1024\n"
  "v_mfma_f32_16x16x32_bf16 v[60:63], a[108:111], v[28:31], v[60:63]\n"
  "s_waitcnt lgkmcnt(5)\n"
  "v_mfma_f32_16x16x32_bf16 v[48:51], a[40:43], v[32:35], v[48:51]\n"
  "ds_read_b128 v[24:27], v65 offset:24576\n"
  "s_waitcnt lgkmcnt(5)\n"
  "v_mfma_f32_16x16x32_bf16 v[52:55], a[40:43], v[36:39], v[52:55]\n"
  "ds_read_b128 v[28:31], v65 offset:26624\n"
  "v_mfma_f32_16x16x32_bf16 v[56:59], a[112:115], v[32:35], v[56:59]\n"
  "v_mfma_f32_16x16x32_bf16 v[60:63], a[112:115], v[36:39], v[60:63]\n"
  "s_waitcnt lgkmcnt(5)\n"
  "v_mfma_f32_16x16x32_bf16 v[48:51], a[44:47], v[40:43], v[48:51]\n"
  "ds_read_b128 v[32:35], v64 offset:28672\n"
  "s_waitcnt lgkmcnt(5)\n"
  "v_mfma_f32_16x16x32_bf16 v[52:55], a[44:47], v[44:47], v[52:55]\n"
  "ds_read_b128 v[36:39], v64 offset:30720\n"
  "v_mfma_f32_16x16x32_bf16 v[56:59], a[116:119], v[40:43], v[56:59]\n"
  "v_mfma_f32_16x16x32_bf16 v[60:63], a[116:119], v[44:47], v[60:63]\n"
  "s_waitcnt lgkmcnt(5)\n"
  "v_mfma_f32_16x16x32_bf16 v[48:51], a[48:51], v[16:19], v[48:51]\n"
  "ds_read_b128 v[40:43], v65 offset:28672\n"
  "s_waitcnt lgkmcnt(5)\n"
  "v_mfma_f32_16x16x32_bf16 v[52:55], a[48:51], v[20:23], v[52:55]\n"
  "ds_read_b128 v[44:47], v65 offset:30720\n"
  "v_mfma_f32_16x16x32_bf16 v[56:59], a[120:123], v[16:19], v[56:59]\n"
  "v_mfma_f32_16x16x32_bf16 v[60:63], a[120:123], v[20:23], v[60:63]\n"
  "s_waitcnt lgkmcnt(5)\n"
  "v_mfma_f32_16x16x32_bf16 v[48:51], a[52:55], v[24:27], v[48:51]\n"
  "ds_read_b128 v[16:19], v64 offset:32768\n"
  "s_waitcnt lgkmcnt(5)\n"
  "v_mfma_f32_16x16x32_bf16 v[52:55], a[52:55], v[28:31], v[52:55]\n"
  "ds_read_b128 v[20:23], v64 offset:34816\n"
  "v_mfma_f32_16x16x32_bf16 v[56:59], a[124:127], v[24:27], v[56:59]\n"
  "v_mfma_f32_16x16x32_bf16 v[60:63], a[124:127], v[28:31], v[60:63]\n"
  "s_waitcnt lgkmcnt(5)\n"
  "v_mfma_f32_16x16x32_bf16 v[48:51], a[56:59], v[32:35], v[48:51]\n"
  "ds_read_b128 v[24:27], v65 offset:32768\n"
  "s_waitcnt lgkmcnt(5)\n"
  "v_mfma_f32_16x16x32_bf16 v[52:55], a[56:59], v[36:39], v[52:55]\n"
  "ds_read_b128 v[28:31], v65 offset:34816\n"
  "v_mfma_f32_16x16x32_bf16 v[56:59], a[128:131], v[32:35], v[56:59]\n"
  "v_mfma_f32_16x16x32_bf16 v[60:63], a[128:131], v[36:39], v[60:63]\n"
  "s_waitcnt lgkmcnt(5)\n"
  "v_mfma_f32_16x16x32_bf16 v[48:51], a[60:63], v[40:43], v[48:51]\n"
  "s_waitcnt lgkmcnt(4)\n"
  "v_mfma_f32_16x16x32_bf16 v[52:55], a[60:63], v[44:47], v[52:55]\n"
  "v_mfma_f32_16x16x32_bf16 v[56:59], a[132:135], v[40:43], v[56:59]\n"
  "v_mfma_f32_16x16x32_bf16 v[60:63], a[132:135], v[44:47], v[60:63]\n"
  "s_waitcnt lgkmcnt(3)\n"
  "v_mfma_f32_16x16x32_bf16 v[48:51], a[64:67], v[16:19], v[48:51]\n"
  "s_waitcnt lgkmcnt(2)\n"
  "v_mfma_f32_16x16x32_bf16 v[52:55], a[64:67], v[20:23], v[52:55]\n"
  "v_mfma_f32_16x16x32_bf16 v[56:59], a[136:139], v[16:19], v[56:59]\n"
  "v_mfma_f32_16x16x32_bf16 v[60:63], a[136:139], v[20:23], v[60:63]\n"
  "s_waitcnt lgkmcnt(1)\n"
  "v_mfma_f32_16x16x32_bf16 v[48:51], a[68:71], v[24:27], v[48:51]\n"
  "s_waitcnt lgkmcnt(0)\n"
  "v_mfma_f32_16x16x32_bf16 v[52:55], a[68:71], v[28:31], v[52:55]\n"
  "v_mfma_f32_16x16x32_bf16 v[56:59], a[140:143], v[24:27], v[56:59]\n"
  "v_mfma_f32_16x16x32_bf16 v[60:63], a[140:143], v[28:31], v[60:63]\n"
  "v_cvt_pk_bf16_f32 v48, v48, v49\n"
  "v_cvt_pk_bf16_f32 v49, v50, v51\n"
  "v_cvt_pk_bf16_f32 v52, v52, v53\n"
  "v_cvt_pk_bf16_f32 v53, v54, v55\n"
  "s_nop 0\n"
  "v_cvt_pk_bf16_f32 v56, v56, v57\n"
  "v_cvt_pk_bf16_f32 v57, v58, v59\n"
  "s_nop 0\n"
  "v_cvt_pk_bf16_f32 v60, v60, v61\n"
  "v_cvt_pk_bf16_f32 v61, v62, v63\n"
  "ds_write_b64 v10, v[48:49] offset:0\n"
  "ds_write_b64 v10, v[52:53] offset:4864\n"
  "ds_write_b64 v11, v[56:57] offset:0\n"
  "ds_write_b64 v11, v[60:61] offset:4864\n"
  "s_waitcnt vmcnt(0)\n"
  "s_add_u32 s56, s56, 1\n"
  "s_waitcnt vmcnt(2) lgkmcnt(0)\n"
  "s_barrier\n"
  "v_add_u32 v64, 59136, v2\n"
  "v_xor_b32 v65, 64, v64\n"
  "s_add_u32 s58, s80, 22272\n"
  "s_cmp_lt_i32 s56, 62\n"
  "s_cselect_b32 s74, 36864, 0\n"
  "s_add_u32 s38, s38, s74\n"
  "s_addc_u32 s39, s39, 0\n"
  "ds_read_b128 v[16:19], v64 offset:0\n"
  "ds_read_b128 v[20:23], v64 offset:2048\n"
  "ds_read_b128 v[24:27], v65 offset:0\n"
  "ds_read_b128 v[28:31], v65 offset:2048\n"
  "ds_read_b128 v[32:35], v64 offset:4096\n"
  "ds_read_b128 v[36:39], v64 offset:6144\n"
  "s_waitcnt lgkmcnt(5)\n"
  "v_mfma_f32_16x16x32_bf16 v[48:51], a[0:3], v[16:19], 0\n"
  "ds_read_b128 v[40:43], v65 offset:4096\n"
  "ds_read_b128 v[76:79], v66\n"
  "s_nop 0\n"
  "s_waitcnt lgkmcnt(6)\n"
  "v_mfma_f32_16x16x32_bf16 v[52:55], a[0:3], v[20:23], 0\n"
  "ds_read_b128 v[44:47], v65 offset:6144\n"
  "ds_read_b128 v[80:83], v68\n"
  "s_nop 0\n"
  "v_mfma_f32_16x16x32_bf16 v[56:59], a[72:75], v[16:19], 0\n"
  "ds_read_b128 v[84:87], v69\n"
  "s_nop 0\n"
  "v_mfma_f32_16x16x32_bf16 v[60:63], a[72:75], v[20:23], 0\n"
  "ds_read_b64_tr_b16 v[88:89], v72 offset:0\n"
  "s_nop 0\n"
  "s_waitcnt lgkmcnt(9)\n"
  "v_mfma_f32_16x16x32_bf16 v[48:51], a[4:7], v[24:27], v[48:51]\n"
  "ds_read_b128 v[16:19], v64 offset:8192\n"
  "ds_read_b64_tr_b16 v[90:91], v73 offset:0\n"
  "s_nop 0\n"
  "s_waitcnt lgkmcnt(10)\n"
  "v_mfma_f32_16x16x32_bf16 v[52:55], a[4:7], v[28:31], v[52:55]\n"
  "ds_read_b128 v[20:23], v64 offset:10240\n"
  "ds_read_b64_tr_b16 v[92:93], v72 offset:32\n"
  "s_nop 0\n"
  "v_mfma_f32_16x16x32_bf16 v[56:59], a[76:79], v[24:27], v[56:59]\n"
  "s_add_u32 m0, s58, 0\n"
  "s_nop 0\n"
  "global_load_lds_dwordx4 v1, s[38:39] offset:0\n"
  "v_mfma_f32_16x16x32_bf16 v[60:63], a[76:79], v[28:31], v[60:63]\n"
  "ds_read_b64_tr_b16 v[94:95], v73 offset:32\n"
  "s_nop 0\n"
  "s_waitcnt lgkmcnt(12)\n"
  "v_mfma_f32_16x16x32_bf16 v[48:51], a[8:11], v[32:35], v[48:51]\n"
  "ds_read_b128 v[24:27], v65 offset:8192\n"
  "ds_read_b32 v126, v6 offset:0\n"
  "s_nop 0\n"
  "s_waitcnt lgkmcnt(13)\n"
  "v_mfma_f32_16x16x32_bf16 v[52:55], a[8:11], v[36:39], v[52:55]\n"
  "ds_read_b128 v[28:31], v65 offset:10240\n"
  "s_waitcnt lgkmcnt(10)\n"
  "v_mfma_f32_16x16x32_bf16 v[96:99], v[80:83], v[76:79], 0\n"
  "v_mfma_f32_16x16x32_bf16 v[56:59], a[80:83], v[32:35], v[56:59]\n"
  "s_add_u32 m0, s58, 3968\n"
  "s_nop 0\n"
  "global_load_lds_dwordx4 v1, s[38:39] offset:128\n"
  "v_mfma_f32_16x16x32_bf16 v[60:63], a[80:83], v[36:39], v[60:63]\n"
  "s_waitcnt lgkmcnt(9)\n"
  "v_mfma_f32_16x16x32_bf16 v[100:103], v[84:87], v[76:79], 0\n"
  "v_mfma_f32_16x16x32_bf16 v[48:51], a[12:15], v[40:43], v[48:51]\n"
  "ds_read_b128 v[32:35], v64 offset:12288\n"
  "s_waitcnt lgkmcnt(2)\n"
  "v_lshlrev_b32 v126, 16, v126\n"
  "v_mfma_f32_16x16x32_bf16 v[52:55], a[12:15], v[44:47], v[52:55]\n"
  "ds_read_b128 v[36:39], v64 offset:14336\n"
  "v_mul_f32 v126, 0xbfb8aa3b, v126\n"
  "v_exp_f32 v126, v126\n"
  "v_mfma_f32_16x16x32_bf16 v[56:59], a[84:87], v[40:43], v[56:59]\n"
  "s_add_u32 m0, s58, 7936\n"
  "s_nop 0\n"
  "global_load_lds_dwordx4 v1, s[38:39] offset:256\n"
  "v_mfma_f32_16x16x32_bf16 v[60:63], a[84:87], v[44:47], v[60:63]\n"
  "v_add_f32 v126, 1.0, v126\n"
  "v_rcp_f32 v126, v126\n"
  "v_mfma_f32_16x16x32_bf16 v[48:51], a[16:19], v[16:19], v[48:51]\n"
  "ds_read_b128 v[40:43], v65 offset:12288\n"
  "v_cvt_pk_bf16_f32 v126, v126, v126\n"
  "v_lshlrev_b32 v126, 16, v126\n"
  "v_mfma_f32_16x16x32_bf16 v[52:55], a[16:19], v[20:23], v[52:55]\n"
  "ds_read_b128 v[44:47], v65 offset:14336\n"
  "v_cndmask_b32 v104, v96, v97, s[70:71]\n"
  "v_cndmask_b32 v105, v98, v99, s[70:71]\n"
  "v_mfma_f32_16x16x32_bf16 v[56:59], a[88:91], v[16:19], v[56:59]\n"
  "s_add_u32 m0, s58, 11904\n"
  "s_nop 0\n"
  "global_load_lds_dwordx4 v1, s[38:39] offset:384\n"
  "v_mfma_f32_16x16x32_bf16 v[60:63], a[88:91], v[20:23], v[60:63]\n"
  "v_cndmask_b32 v106, v100, v101, s[70:71]\n"
  "v_cndmask_b32 v107, v102, v103, s[70:71]\n"
  "v_mfma_f32_16x16x32_bf16 v[48:51], a[20:23], v[24:27], v[48:51]\n"
  "ds_read_b128 v[16:19], v64 offset:16384\n"
  "v_cvt_pk_bf16_f32 v104, v104, v105\n"
  "v_and_b32 v105, 0xffff0000, v104\n"
  "s_waitcnt lgkmcnt(5)\n"
  "v_mfma_f32_16x16x32_bf16 v[52:55], a[20:23], v[28:31], v[52:55]\n"
  "ds_read_b128 v[20:23], v64 offset:18432\n"
  "v_lshlrev_b32 v104, 16, v104\n"
  "v_cvt_pk_bf16_f32 v106, v106, v107\n"
  "v_mfma_f32_16x16x32_bf16 v[56:59], a[92:95], v[24:27], v[56:59]\n"
  "s_add_u32 m0, s58, 15872\n"
  "s_nop 0\n"
  "global_load_lds_dwordx4 v1, s[38:39] offset:512\n"
  "v_mfma_f32_16x16x32_bf16 v[60:63], a[92:95], v[28:31], v[60:63]\n"
  "v_and_b32 v107, 0xffff0000, v106\n"
  "v_lshlrev_b32 v106, 16, v106\n"
  "s_waitcnt lgkmcnt(5)\n"
  "v_mfma_f32_16x16x32_bf16 v[48:51], a[24:27], v[32:35], v[48:51]\n"
  "ds_read_b128 v[24:27], v65 offset:16384\n"
  "v_mul_f32 v104, 0x3e715bef, v104\n"
  "v_mul_f32 v105, 0x3e715bef, v105\n"
  "s_waitcnt lgkmcnt(5)\n"
  "v_mfma_f32_16x16x32_bf16 v[52:55], a[24:27], v[36:39], v[52:55]\n"
  "ds_read_b128 v[28:31], v65 offset:18432\n"
  "v_mul_f32 v106, 0x3e715bef, v106\n"
  "v_mul_f32 v107, 0x3e715bef, v107\n"
  "v_mfma_f32_16x16x32_bf16 v[56:59], a[96:99], v[32:35], v[56:59]\n"
  "s_add_u32 m0, s58, 19840\n"
  "s_nop 0\n"
  "global_load_lds_dwordx4 v1, s[38:39] offset:640\n"
  "v_mfma_f32_16x16x32_bf16 v[60:63], a[96:99], v[36:39], v[60:63]\n"
  "v_cvt_pk_bf16_f32 v104, v104, v105\n"
  "v_and_b32 v105, 0xffff0000, v104\n"
  "s_waitcnt lgkmcnt(5)\n"
  "v_mfma_f32_16x16x32_bf16 v[48:51], a[28:31], v[40:43], v[48:51]\n"
  "ds_read_b128 v[32:35], v64 offset:20480\n"
  "v_lshlrev_b32 v104, 16, v104\n"
  "v_cvt_pk_bf16_f32 v106, v106, v107\n"
  "s_waitcnt lgkmcnt(5)\n"
  "v_mfma_f32_16x16x32_bf16 v[52:55], a[28:31], v[44:47], v[52:55]\n"
  "ds_read_b128 v[36:39], v64 offset:22528\n"
  "v_and_b32 v107, 0xffff0000, v106\n"
  "v_lshlrev_b32 v106, 16, v106\n"
  "v_mfma_f32_16x16x32_bf16 v[56:59], a[100:103], v[40:43], v[56:59]\n"
  "s_add_u32 m0, s58, 23808\n"
  "s_nop 0\n"
  "global_load_lds_dwordx4 v1, s[38:39] offset:768\n"
  "v_mfma_f32_16x16x32_bf16 v[60:63], a[100:103], v[44:47], v[60:63]\n"
  "v_cndmask_b32 v104, v128, v104, s[8:9]\n"
  "v_cndmask_b32 v105, v128, v105, s[10:11]\n"
  "s_waitcnt lgkmcnt(5)\n"
  "v_mfma_f32_16x16x32_bf16 v[48:51], a[32:35], v[16:19], v[48:51]\n"
  "ds_read_b128 v[40:43], v65 offset:20480\n"
  "v_cndmask_b32 v106, v128, v106, s[12:13]\n"
  "v_cndmask_b32 v107, v128, v107, s[14:15]\n"
  "s_waitcnt lgkmcnt(5)\n"
  "v_mfma_f32_16x16x32_bf16 v[52:55], a[32:35], v[20:23], v[52:55]\n"
  "ds_read_b128 v[44:47], v65 offset:22528\n"
  "v_max3_f32 v124, v104, v105, v106\n"
  "v_max_f32 v124, v124, v107\n"
  "v_mfma_f32_16x16x32_bf16 v[56:59], a[104:107], v[16:19], v[56:59]\n"
  "s_add_u32 m0, s58, 27776\n"
  "s_nop 0\n"
  "global_load_lds_dwordx4 v1, s[38:39] offset:896\n"
  "v_mfma_f32_16x16x32_bf16 v[60:63], a[104:107], v[20:23], v[60:63]\n"
  "v_mov_b32 v125, v124\n"
  "s_nop 1\n"
  "v_permlane16_swap_b32 v124, v125\n"
  "v_max_f32 v124, v124, v125\n"
  "s_waitcnt lgkmcnt(5)\n"
  "v_mfma_f32_16x16x32_bf16 v[48:51], a[36:39], v[24:27], v[48:51]\n"
  "ds_read_b128 v[16:19], v64 offset:24576\n"
  "v_mov_b32 v125, v124\n"
  "s_nop 1\n"
  "v_permlane32_swap_b32 v124, v125\n"
  "v_max_f32 v124, v124, v125\n"
  "s_waitcnt lgkmcnt(5)\n"
  "v_mfma_f32_16x16x32_bf16 v[52:55], a[36:39], v[28:31], v[52:55]\n"
  "ds_read_b128 v[20:23], v64 offset:26624\n"
  "v_sub_f32 v104, v104, v124\n"
  "v_sub_f32 v105, v105, v124\n"
  "v_mfma_f32_16x16x32_bf16 v[56:59], a[108:111], v[24:27], v[56:59]\n"
  "s_add_u32 m0, s58, 31744\n"
  "s_nop 0\n"
  "global_load_lds_dwordx4 v1, s[38:39] offset:1024\n"
  "v_mfma_f32_16x16x32_bf16 v[60:63], a[108:111], v[28:31], v[60:63]\n"
  "v_sub_f32 v106, v106, v124\n"
  "v_sub_f32 v107, v107, v124\n"
  "s_waitcnt lgkmcnt(5)\n"
  "v_mfma_f32_16x16x32_bf16 v[48:51], a[40:43], v[32:35], v[48:51]\n"
  "ds_read_b128 v[24:27], v65 offset:24576\n"
  "v_mul_f32 v104, 0x3fb8aa3b, v104\n"
  "v_mul_f32 v105, 0x3fb8aa3b, v105\n"
  "s_waitcnt lgkmcnt(5)\n"
  "v_mfma_f32_16x16x32_bf16 v[52:55], a[40:43], v[36:39], v[52:55]\n"
  "ds_read_b128 v[28:31], v65 offset:26624\n"
  "v_mul_f32 v106, 0x3fb8aa3b, v106\n"
  "v_mul_f32 v107, 0x3fb8aa3b, v107\n"
  "v_mfma_f32_16x16x32_bf16 v[56:59], a[112:115], v[32:35], v[56:59]\n"
  "v_exp_f32 v104, v104\n"
  "v_mfma_f32_16x16x32_bf16 v[60:63], a[112:115], v[36:39], v[60:63]\n"
  "v_exp_f32 v105, v105\n"
  "s_waitcnt lgkmcnt(5)\n"
  "v_mfma_f32_16x16x32_bf16 v[48:51], a[44:47], v[40:43], v[48:51]\n"
  "ds_read_b128 v[32:35], v64 offset:28672\n"
  "v_exp_f32 v106, v106\n"
  "s_waitcnt lgkmcnt(5)\n"
  "v_mfma_f32_16x16x32_bf16 v[52:55], a[44:47], v[44:47], v[52:55]\n"
  "ds_read_b128 v[36:39], v64 offset:30720\n"
  "v_exp_f32 v107, v107\n"
  "v_mfma_f32_16x16x32_bf16 v[56:59], a[116:119], v[40:43], v[56:59]\n"
  "s_mov_b64 s[8:9], s[16:17]\n"
  "s_mov_b64 s[10:11], s[18:19]\n"
  "v_mfma_f32_16x16x32_bf16 v[60:63], a[116:119], v[44:47], v[60:63]\n"
  "v_add_f32 v124, v104, v105\n"
  "v_add_f32 v124, v124, v106\n"
  "s_waitcnt lgkmcnt(5)\n"
  "v_mfma_f32_16x16x32_bf16 v[48:51], a[48:51], v[16:19], v[48:51]\n"
  "ds_read_b128 v[40:43], v65 offset:28672\n"
  "v_add_f32 v124, v124, v107\n"
  "v_mov_b32 v125, v124\n"
  "s_nop 1\n"
  "v_permlane16_swap_b32 v124, v125\n"
  "v_add_f32 v124, v124, v125\n"
  "s_waitcnt lgkmcnt(5)\n"
  "v_mfma_f32_16x16x32_bf16 v[52:55], a[48:51], v[20:23], v[52:55]\n"
  "ds_read_b128 v[44:47], v65 offset:30720\n"
  "v_mov_b32 v125, v124\n"
  "s_nop 1\n"
  "v_permlane32_swap_b32 v124, v125\n"
  "v_add_f32 v124, v124, v125\n"
  "v_mfma_f32_16x16x32_bf16 v[56:59], a[120:123], v[16:19], v[56:59]\n"
  "v_rcp_f32 v124, v124\n"
  "v_mfma_f32_16x16x32_bf16 v[60:63], a[120:123], v[20:23], v[60:63]\n"
  "v_mul_f32 v104, v124, v104\n"
  "v_mul_f32 v105, v124, v105\n"
  "s_waitcnt lgkmcnt(5)\n"
  "v_mfma_f32_16x16x32_bf16 v[48:51], a[52:55], v[24:27], v[48:51]\n"
  "ds_read_b128 v[16:19], v64 offset:32768\n"
  "v_mul_f32 v106, v124, v106\n"
  "v_mul_f32 v107, v124, v107\n"
  "s_waitcnt lgkmcnt(5)\n"
  "v_mfma_f32_16x16x32_bf16 v[52:55], a[52:55], v[28:31], v[52:55]\n"
  "ds_read_b128 v[20:23], v64 offset:34816\n"
  "v_cvt_pk_bf16_f32 v108, v104, v104\n"
  "v_cvt_pk_bf16_f32 v109, v105, v105\n"
  "v_mfma_f32_16x16x32_bf16 v[56:59], a[124:127], v[24:27], v[56:59]\n"
  "v_cvt_pk_bf16_f32 v110, v106, v106\n"
  "v_cvt_pk_bf16_f32 v111, v107, v107\n"
  "v_mfma_f32_16x16x32_bf16 v[60:63], a[124:127], v[28:31], v[60:63]\n"
  "v_and_b32 v108, v8, v108\n"
  "v_and_b32 v109, v8, v109\n"
  "s_waitcnt lgkmcnt(5)\n"
  "v_mfma_f32_16x16x32_bf16 v[48:51], a[56:59], v[32:35], v[48:51]\n"
  "ds_read_b128 v[24:27], v65 offset:32768\n"
  "v_and_b32 v110, v8, v110\n"
  "v_and_b32 v111, v8, v111\n"
  "s_waitcnt lgkmcnt(5)\n"
  "v_mfma_f32_16x16x32_bf16 v[52:55], a[56:59], v[36:39], v[52:55]\n"
  "ds_read_b128 v[28:31], v65 offset:34816\n"
  "v_mfma_f32_16x16x32_bf16 v[112:115], v[88:91], v[108:111], 0\n"
  "v_mfma_f32_16x16x32_bf16 v[56:59], a[128:131], v[32:35], v[56:59]\n"
  "s_waitcnt vmcnt(9)\n"
  "s_barrier\n"
  "v_mov_b32 v9, 0\n"
  "v_mov_b32 v15, s56\n"
  "s_cmp_eq_u32 s47, 0\n"
  "s_cselect_b64 exec, 1, 0\n"
  "global_store_dword v9, v15, s[64:65] sc1\n"
  "s_mov_b64 exec, -1\n"
  "v_mfma_f32_16x16x32_bf16 v[60:63], a[128:131], v[36:39], v[60:63]\n"
  "v_mfma_f32_16x16x32_bf16 v[116:119], v[92:95], v[108:111], 0\n"
  "s_waitcnt lgkmcnt(5)\n"
  "v_mfma_f32_16x16x32_bf16 v[48:51], a[60:63], v[40:43], v[48:51]\n"
  "s_waitcnt lgkmcnt(4)\n"
  "v_mfma_f32_16x16x32_bf16 v[52:55], a[60:63], v[44:47], v[52:55]\n"
  "v_mfma_f32_16x16x32_bf16 v[56:59], a[132:135], v[40:43], v[56:59]\n"
  "v_mfma_f32_16x16x32_bf16 v[60:63], a[132:135], v[44:47], v[60:63]\n"
  "s_waitcnt lgkmcnt(3)\n"
  "v_mfma_f32_16x16x32_bf16 v[48:51], a[64:67], v[16:19], v[48:51]\n"
  "s_waitcnt lgkmcnt(2)\n"
  "v_mfma_f32_16x16x32_bf16 v[52:55], a[64:67], v[20:23], v[52:55]\n"
  "v_mfma_f32_16x16x32_bf16 v[56:59], a[136:139], v[16:19], v[56:59]\n"
  "v_mfma_f32_16x16x32_bf16 v[60:63], a[136:139], v[20:23], v[60:63]\n"
  "s_waitcnt lgkmcnt(1)\n"
  "v_mfma_f32_16x16x32_bf16 v[48:51], a[68:71], v[24:27], v[48:51]\n"
  "s_waitcnt lgkmcnt(0)\n"
  "v_mfma_f32_16x16x32_bf16 v[52:55], a[68:71], v[28:31], v[52:55]\n"
  "v_mfma_f32_16x16x32_bf16 v[56:59], a[140:143], v[24:27], v[56:59]\n"
  "v_mfma_f32_16x16x32_bf16 v[60:63], a[140:143], v[28:31], v[60:63]\n"
  "v_cvt_pk_bf16_f32 v112, v112, v113\n"
  "v_cvt_pk_bf16_f32 v113, v114, v115\n"
  "v_cvt_pk_bf16_f32 v114, v116, v117\n"
  "v_cvt_pk_bf16_f32 v115, v118, v119\n"
  "ds_write_b64 v3, v[112:113]\n"
  "s_mov_b64 exec, 0xffff\n"
  "ds_write_b64 v3, v[114:115] offset:32\n"
  "s_mov_b64 exec, s[68:69]\n"
  "global_store_dword v127, v126, s[62:63] sc1\n"
  "s_mov_b64 exec, -1\n"
  "s_add_u32 s62, s62, 2048\n"
  "s_addc_u32 s63, s63, 0\n"
  "v_cvt_pk_bf16_f32 v48, v48, v49\n"
  "v_cvt_pk_bf16_f32 v49, v50, v51\n"
  "v_cvt_pk_bf16_f32 v52, v52, v53\n"
  "v_cvt_pk_bf16_f32 v53, v54, v55\n"
  "v_cvt_pk_bf16_f32 v56, v56, v57\n"
  "v_cvt_pk_bf16_f32 v57, v58, v59\n"
  "v_cvt_pk_bf16_f32 v60, v60, v61\n"
  "v_cvt_pk_bf16_f32 v61, v62, v63\n"
  "ds_write_b64 v10, v[48:49] offset:9728\n"
  "ds_write_b64 v10, v[52:53] offset:14592\n"
  "ds_write_b64 v11, v[56:57] offset:9728\n"
  "ds_write_b64 v11, v[60:61] offset:14592\n"
  "s_add_u32 s56, s56, 1\n"
  "ALOOP:\n"
  "s_waitcnt vmcnt(2) lgkmcnt(0)\n"
  "s_barrier\n"
  "v_add_u32 v64, 22272, v2\n"
  "v_xor_b32 v65, 64, v64\n"
  "s_add_u32 s58, s80, 59136\n"
  "s_cmp_lt_i32 s56, 62\n"
  "s_cselect_b32 s74, 36864, 0\n"
  "s_add_u32 s38, s38, s74\n"
  "s_addc_u32 s39, s39, 0\n"
  "ds_read_b128 v[120:123], v4\n"
  "ds_read_b128 v[16:19], v64 offset:0\n"
  "ds_read_b128 v[20:23], v64 offset:2048\n"
  "ds_read_b128 v[24:27], v65 offset:0\n"
  "ds_read_b128 v[28:31], v65 offset:2048\n"
  "ds_read_b128 v[32:35], v64 offset:4096\n"
  "ds_read_b128 v[36:39], v64 offset:6144\n"
  "s_waitcnt lgkmcnt(5)\n"
  "v_mfma_f32_16x16x32_bf16 v[48:51], a[0:3], v[16:19], 0\n"
  "ds_read_b128 v[40:43], v65 offset:4096\n"
  "ds_read_b128 v[76:79], v67\n"
  "s_nop 0\n"
  "s_waitcnt lgkmcnt(6)\n"
  "v_mfma_f32_16x16x32_bf16 v[52:55], a[0:3], v[20:23], 0\n"
  "ds_read_b128 v[44:47], v65 offset:6144\n"
  "ds_read_b128 v[80:83], v70\n"
  "s_nop 0\n"
  "v_mfma_f32_16x16x32_bf16 v[56:59], a[72:75], v[16:19], 0\n"
  "s_bfm_b64 exec, 40, 0\n"
  "global_store_dwordx4 v5, v[120:123], s[60:61] sc1\n"
  "s_mov_b64 exec, -1\n"
  "s_add_u32 s60, s60, 40960\n"
  "s_addc_u32 s61, s61, 0\n"
  "v_mfma_f32_16x16x32_bf16 v[60:63], a[72:75], v[20:23], 0\n"
  "ds_read_b128 v[84:87], v71\n"
  "s_nop 0\n"
  "s_waitcnt lgkmcnt(8)\n"
  "v_mfma_f32_16x16x32_bf16 v[48:51], a[4:7], v[24:27], v[48:51]\n"
  "ds_read_b128 v[16:19], v64 offset:8192\n"
  "ds_read_b64_tr_b16 v[88:89], v74 offset:0\n"
  "s_nop 0\n"
  "s_waitcnt lgkmcnt(9)\n"
  "v_mfma_f32_16x16x32_bf16 v[52:55], a[4:7], v[28:31], v[52:55]\n"
  "ds_read_b128 v[20:23], v64 offset:10240\n"
  "ds_read_b64_tr_b16 v[90:91], v75 offset:0\n"
  "s_nop 0\n"
  "v_mfma_f32_16x16x32_bf16 v[56:59], a[76:79], v[24:27], v[56:59]\n"
  "s_add_u32 m0, s58, 0\n"
  "s_nop 0\n"
  "global_load_lds_dwordx4 v1, s[38:39] offset:0\n"
  "v_mfma_f32_16x16x32_bf16 v[60:63], a[76:79], v[28:31], v[60:63]\n"
  "ds_read_b64_tr_b16 v[92:93], v74 offset:32\n"
  "s_nop 0\n"
  "s_waitcnt lgkmcnt(11)\n"
  "v_mfma_f32_16x16x32_bf16 v[48:51], a[8:11], v[32:35], v[48:51]\n"
  "ds_read_b128 v[24:27], v65 offset:8192\n"
  "ds_read_b64_tr_b16 v[94:95], v75 offset:32\n"
  "s_nop 0\n"
  "s_waitcnt lgkmcnt(12)\n"
  "v_mfma_f32_16x16x32_bf16 v[52:55], a[8:11], v[36:39], v[52:55]\n"
  "ds_read_b128 v[28:31], v65 offset:10240\n"
  "ds_read_b32 v126, v6 offset:9728\n"
  "s_nop 0\n"
  "v_mfma_f32_16x16x32_bf16 v[56:59], a[80:83], v[32:35], v[56:59]\n"
  "s_add_u32 m0, s58, 3968\n"
  "s_nop 0\n"
  "global_load_lds_dwordx4 v1, s[38:39] offset:128\n"
  "v_mfma_f32_16x16x32_bf16 v[60:63], a[80:83], v[36:39], v[60:63]\n"
  "s_waitcnt lgkmcnt(10)\n"
  "v_mfma_f32_16x16x32_bf16 v[96:99], v[80:83], v[76:79], 0\n"
  "v_mfma_f32_16x16x32_bf16 v[48:51], a[12:15], v[40:43], v[48:51]\n"
  "ds_read_b128 v[32:35], v64 offset:12288\n"
  "s_waitcnt lgkmcnt(10)\n"
  "v_mfma_f32_16x16x32_bf16 v[100:103], v[84:87], v[76:79], 0\n"
  "v_mfma_f32_16x16x32_bf16 v[52:55], a[12:15], v[44:47], v[52:55]\n"
  "ds_read_b128 v[36:39], v64 offset:14336\n"
  "s_waitcnt lgkmcnt(2)\n"
  "v_lshlrev_b32 v126, 16, v126\n"
  "v_mfma_f32_16x16x32_bf16 v[56:59], a[84:87], v[40:43], v[56:59]\n"
  "s_add_u32 m0, s58, 7936\n"
  "s_nop 0\n"
  "global_load_lds_dwordx4 v1, s[38:39] offset:256\n"
  "v_mfma_f32_16x16x32_bf16 v[60:63], a[84:87], v[44:47], v[60:63]\n"
  "v_mul_f32 v126, 0xbfb8aa3b, v126\n"
  "v_exp_f32 v126, v126\n"
  "v_mfma_f32_16x16x32_bf16 v[48:51], a[16:19], v[16:19], v[48:51]\n"
  "ds_read_b128 v[40:43], v65 offset:12288\n"
  "v_add_f32 v126, 1.0, v126\n"
  "v_rcp_f32 v126, v126\n"
  "v_mfma_f32_16x16x32_bf16 v[52:55], a[16:19], v[20:23], v[52:55]\n"
  "ds_read_b128 v[44:47], v65 offset:14336\n"
  "v_cvt_pk_bf16_f32 v126, v126, v126\n"
  "v_lshlrev_b32 v126, 16, v126\n"
  "v_mfma_f32_16x16x32_bf16 v[56:59], a[88:91], v[16:19], v[56:59]\n"
  "s_add_u32 m0, s58, 11904\n"
  "s_nop 0\n"
  "global_load_lds_dwordx4 v1, s[38:39] offset:384\n"
  "v_mfma_f32_16x16x32_bf16 v[60:63], a[88:91], v[20:23], v[60:63]\n"
  "v_cndmask_b32 v104, v96, v97, s[70:71]\n"
  "v_cndmask_b32 v105, v98, v99, s[70:71]\n"
  "v_mfma_f32_16x16x32_bf16 v[48:51], a[20:23], v[24:27], v[48:51]\n"
  "ds_read_b128 v[16:19], v64 offset:16384\n"
  "v_cndmask_b32 v106, v100, v101, s[70:71]\n"
  "v_cndmask_b32 v107, v102, v103, s[70:71]\n"
  "v_mfma_f32_16x16x32_bf16 v[52:55], a[20:23], v[28:31], v[52:55]\n"
  "ds_read_b128 v[20:23], v64 offset:18432\n"
  "v_cvt_pk_bf16_f32 v104, v104, v105\n"
  "v_and_b32 v105, 0xffff0000, v104\n"
  "v_mfma_f32_16x16x32_bf16 v[56:59], a[92:95], v[24:27], v[56:59]\n"
  "s_add_u32 m0, s58, 15872\n"
  "s_nop 0\n"
  "global_load_lds_dwordx4 v1, s[38:39] offset:512\n"
  "v_mfma_f32_16x16x32_bf16 v[60:63], a[92:95], v[28:31], v[60:63]\n"
  "v_lshlrev_b32 v104, 16, v104\n"
  "v_cvt_pk_bf16_f32 v106, v106, v107\n"
  "s_waitcnt lgkmcnt(5)\n"
  "v_mfma_f32_16x16x32_bf16 v[48:51], a[24:27], v[32:35], v[48:51]\n"
  "ds_read_b128 v[24:27], v65 offset:16384\n"
  "v_and_b32 v107, 0xffff0000, v106\n"
  "v_lshlrev_b32 v106, 16, v106\n"
  "s_waitcnt lgkmcnt(5)\n"
  "v_mfma_f32_16x16x32_bf16 v[52:55], a[24:27], v[36:39], v[52:55]\n"
  "ds_read_b128 v[28:31], v65 offset:18432\n"
  "v_mul_f32 v104, 0x3e715bef, v104\n"
  "v_mul_f32 v105, 0x3e715bef, v105\n"
  "v_mfma_f32_16x16x32_bf16 v[56:59], a[96:99], v[32:35], v[56:59]\n"
  "s_add_u32 m0, s58, 19840\n"
  "s_nop 0\n"
  "global_load_lds_dwordx4 v1, s[38:39] offset:640\n"
  "v_mfma_f32_16x16x32_bf16 v[60:63], a[96:99], v[36:39], v[60:63]\n"
  "v_mul_f32 v106, 0x3e715bef, v106\n"
  "v_mul_f32 v107, 0x3e715bef, v107\n"
  "s_waitcnt lgkmcnt(5)\n"
  "v_mfma_f32_16x16x32_bf16 v[48:51], a[28:31], v[40:43], v[48:51]\n"
  "ds_read_b128 v[32:35], v64 offset:20480\n"
  "v_cvt_pk_bf16_f32 v104, v104, v105\n"
  "v_and_b32 v105, 0xffff0000, v104\n"
  "s_waitcnt lgkmcnt(5)\n"
  "v_mfma_f32_16x16x32_bf16 v[52:55], a[28:31], v[44:47], v[52:55]\n"
  "ds_read_b128 v[36:39], v64 offset:22528\n"
  "v_lshlrev_b32 v104, 16, v104\n"
  "v_cvt_pk_bf16_f32 v106, v106, v107\n"
  "v_mfma_f32_16x16x32_bf16 v[56:59], a[100:103], v[40:43], v[56:59]\n"
  "s_add_u32 m0, s58, 23808\n"
  "s_nop 0\n"
  "global_load_lds_dwordx4 v1, s[38:39] offset:768\n"
  "v_mfma_f32_16x16x32_bf16 v[60:63], a[100:103], v[44:47], v[60:63]\n"
  "v_and_b32 v107, 0xffff0000, v106\n"
  "v_lshlrev_b32 v106, 16, v106\n"
  "s_waitcnt lgkmcnt(5)\n"
  "v_mfma_f32_16x16x32_bf16 v[48:51], a[32:35], v[16:19], v[48:51]\n"
  "ds_read_b128 v[40:43], v65 offset:20480\n"
  "v_cndmask_b32 v104, v128, v104, s[8:9]\n"
  "v_cndmask_b32 v105, v128, v105, s[10:11]\n"
  "s_waitcnt lgkmcnt(5)\n"
  "v_mfma_f32_16x16x32_bf16 v[52:55], a[32:35], v[20:23], v[52:55]\n"
  "ds_read_b128 v[44:47], v65 offset:22528\n"
  "v_cndmask_b32 v106, v128, v106, s[12:13]\n"
  "v_cndmask_b32 v107, v128, v107, s[14:15]\n"
  "v_mfma_f32_16x16x32_bf16 v[56:59], a[104:107], v[16:19], v[56:59]\n"
  "s_add_u32 m0, s58, 27776\n"
  "s_nop 0\n"
  "global_load_lds_dwordx4 v1, s[38:39] offset:896\n"
  "v_mfma_f32_16x16x32_bf16 v[60:63], a[104:107], v[20:23], v[60:63]\n"
  "v_max3_f32 v124, v104, v105, v106\n"
  "v_max_f32 v124, v124, v107\n"
  "s_waitcnt lgkmcnt(5)\n"
  "v_mfma_f32_16x16x32_bf16 v[48:51], a[36:39], v[24:27], v[48:51]\n"
  "ds_read_b128 v[16:19], v64 offset:24576\n"
  "v_mov_b32 v125, v124\n"
  "s_nop 1\n"
  "v_permlane16_swap_b32 v124, v125\n"
  "v_max_f32 v124, v124, v125\n"
  "s_waitcnt lgkmcnt(5)\n"
  "v_mfma_f32_16x16x32_bf16 v[52:55], a[36:39], v[28:31], v[52:55]\n"
  "ds_read_b128 v[20:23], v64 offset:26624\n"
  "v_mov_b32 v125, v124\n"
  "s_nop 1\n"
  "v_permlane32_swap_b32 v124, v125\n"
  "v_max_f32 v124, v124, v125\n"
  "v_mfma_f32_16x16x32_bf16 v[56:59], a[108:111], v[24:27], v[56:59]\n"
  "s_add_u32 m0, s58, 31744\n"
  "s_nop 0\n"
  "global_load_lds_dwordx4 v1, s[38:39] offset:1024\n"
  "v_mfma_f32_16x16x32_bf16 v[60:63], a[108:111], v[28:31], v[60:63]\n"
  "v_sub_f32 v104, v104, v124\n"
  "v_sub_f32 v105, v105, v124\n"
  "s_waitcnt lgkmcnt(5)\n"
  "v_mfma_f32_16x16x32_bf16 v[48:51], a[40:43], v[32:35], v[48:51]\n"
  "ds_read_b128 v[24:27], v65 offset:24576\n"
  "v_sub_f32 v106, v106, v124\n"
  "v_sub_f32 v107, v107, v124\n"
  "s_waitcnt lgkmcnt(5)\n"
  "v_mfma_f32_16x16x32_bf16 v[52:55], a[40:43], v[36:39], v[52:55]\n"
  "ds_read_b128 v[28:31], v65 offset:26624\n"
  "v_mul_f32 v104, 0x3fb8aa3b, v104\n"
  "v_mul_f32 v105, 0x3fb8aa3b, v105\n"
  "v_mfma_f32_16x16x32_bf16 v[56:59], a[112:115], v[32:35], v[56:59]\n"
  "v_mul_f32 v106, 0x3fb8aa3b, v106\n"
  "v_mul_f32 v107, 0x3fb8aa3b, v107\n"
  "v_mfma_f32_16x16x32_bf16 v[60:63], a[112:115], v[36:39], v[60:63]\n"
  "v_exp_f32 v104, v104\n"
  "s_waitcnt lgkmcnt(5)\n"
  "v_mfma_f32_16x16x32_bf16 v[48:51], a[44:47], v[40:43], v[48:51]\n"
  "ds_read_b128 v[32:35], v64 offset:28672\n"
  "v_exp_f32 v105, v105\n"
  "s_waitcnt lgkmcnt(5)\n"
  "v_mfma_f32_16x16x32_bf16 v[52:55], a[44:47], v[44:47], v[52:55]\n"
  "ds_read_b128 v[36:39], v64 offset:30720\n"
  "v_exp_f32 v106, v106\n"
  "v_mfma_f32_16x16x32_bf16 v[56:59], a[116:119], v[40:43], v[56:59]\n"
  "v_exp_f32 v107, v107\n"
  "v_mfma_f32_16x16x32_bf16 v[60:63], a[116:119], v[44:47], v[60:63]\n"
  "s_mov_b64 s[8:9], s[16:17]\n"
  "s_mov_b64 s[10:11], s[18:19]\n"
  "s_waitcnt lgkmcnt(5)\n"
  "v_mfma_f32_16x16x32_bf16 v[48:51], a[48:51], v[16:19], v[48:51]\n"
  "ds_read_b128 v[40:43], v65 offset:28672\n"
  "v_add_f32 v124, v104, v105\n"
  "v_add_f32 v124, v124, v106\n"
  "s_waitcnt lgkmcnt(5)\n"
  "v_mfma_f32_16x16x32_bf16 v[52:55], a[48:51], v[20:23], v[52:55]\n"
  "ds_read_b128 v[44:47], v65 offset:30720\n"
  "v_add_f32 v124, v124, v107\n"
  "v_mov_b32 v125, v124\n"
  "s_nop 1\n"
  "v_permlane16_swap_b32 v124, v125\n"
  "v_add_f32 v124, v124, v125\n"
  "v_mfma_f32_16x16x32_bf16 v[56:59], a[120:123], v[16:19], v[56:59]\n"
  "v_mov_b32 v125, v124\n"
  "s_nop 1\n"
  "v_permlane32_swap_b32 v124, v125\n"
  "v_add_f32 v124, v124, v125\n"
  "v_mfma_f32_16x16x32_bf16 v[60:63], a[120:123], v[20:23], v[60:63]\n"
  "v_rcp_f32 v124, v124\n"
  "s_waitcnt lgkmcnt(5)\n"
  "v_mfma_f32_16x16x32_bf16 v[48:51], a[52:55], v[24:27], v[48:51]\n"
  "ds_read_b128 v[16:19], v64 offset:32768\n"
  "v_mul_f32 v104, v124, v104\n"
  "v_mul_f32 v105, v124, v105\n"
  "s_waitcnt lgkmcnt(5)\n"
  "v_mfma_f32_16x16x32_bf16 v[52:55], a[52:55], v[28:31], v[52:55]\n"
  "ds_read_b128 v[20:23], v64 offset:34816\n"
  "v_mul_f32 v106, v124, v106\n"
  "v_mul_f32 v107, v124, v107\n"
  "v_mfma_f32_16x16x32_bf16 v[56:59], a[124:127], v[24:27], v[56:59]\n"
  "v_cvt_pk_bf16_f32 v108, v104, v104\n"
  "v_cvt_pk_bf16_f32 v109, v105, v105\n"
  "v_mfma_f32_16x16x32_bf16 v[60:63], a[124:127], v[28:31], v[60:63]\n"
  "v_cvt_pk_bf16_f32 v110, v106, v106\n"
  "v_cvt_pk_bf16_f32 v111, v107, v107\n"
  "s_waitcnt lgkmcnt(5)\n"
  "v_mfma_f32_16x16x32_bf16 v[48:51], a[56:59], v[32:35], v[48:51]\n"
  "ds_read_b128 v[24:27], v65 offset:32768\n"
  "v_and_b32 v108, v8, v108\n"
  "v_and_b32 v109, v8, v109\n"
  "s_waitcnt lgkmcnt(5)\n"
  "v_mfma_f32_16x16x32_bf16 v[52:55], a[56:59], v[36:39], v[52:55]\n"
  "ds_read_b128 v[28:31], v65 offset:34816\n"
  "v_and_b32 v110, v8, v110\n"
  "v_and_b32 v111, v8, v111\n"
  "v_mfma_f32_16x16x32_bf16 v[56:59], a[128:131], v[32:35], v[56:59]\n"
  "s_waitcnt vmcnt(9)\n"
  "s_barrier\n"
  "v_mov_b32 v9, 0\n"
  "v_mov_b32 v15, s56\n"
  "s_cmp_eq_u32 s47, 0\n"
  "s_cselect_b64 exec, 1, 0\n"
  "global_store_dword v9, v15, s[64:65] sc1\n"
  "s_mov_b64 exec, -1\n"
  "v_mfma_f32_16x16x32_bf16 v[60:63], a[128:131], v[36:39], v[60:63]\n"
  "v_mfma_f32_16x16x32_bf16 v[112:115], v[88:91], v[108:111], 0\n"
  "s_waitcnt lgkmcnt(5)\n"
  "v_mfma_f32_16x16x32_bf16 v[48:51], a[60:63], v[40:43], v[48:51]\n"
  "v_mfma_f32_16x16x32_bf16 v[116:119], v[92:95], v[108:111], 0\n"
  "s_waitcnt lgkmcnt(4)\n"
  "v_mfma_f32_16x16x32_bf16 v[52:55], a[60:63], v[44:47], v[52:55]\n"
  "v_mfma_f32_16x16x32_bf16 v[56:59], a[132:135], v[40:43], v[56:59]\n"
  "v_mfma_f32_16x16x32_bf16 v[60:63], a[132:135], v[44:47], v[60:63]\n"
  "s_waitcnt lgkmcnt(3)\n"
  "v_mfma_f32_16x16x32_bf16 v[48:51], a[64:67], v[16:19], v[48:51]\n"
  "s_waitcnt lgkmcnt(2)\n"
  "v_mfma_f32_16x16x32_bf16 v[52:55], a[64:67], v[20:23], v[52:55]\n"
  "v_mfma_f32_16x16x32_bf16 v[56:59], a[136:139], v[16:19], v[56:59]\n"
  "v_mfma_f32_16x16x32_bf16 v[60:63], a[136:139], v[20:23], v[60:63]\n"
  "s_waitcnt lgkmcnt(1)\n"
  "v_mfma_f32_16x16x32_bf16 v[48:51], a[68:71], v[24:27], v[48:51]\n"
  "s_waitcnt lgkmcnt(0)\n"
  "v_mfma_f32_16x16x32_bf16 v[52:55], a[68:71], v[28:31], v[52:55]\n"
  "v_mfma_f32_16x16x32_bf16 v[56:59], a[140:143], v[24:27], v[56:59]\n"
  "v_mfma_f32_16x16x32_bf16 v[60:63], a[140:143], v[28:31], v[60:63]\n"
  "v_cvt_pk_bf16_f32 v112, v112, v113\n"
  "v_cvt_pk_bf16_f32 v113, v114, v115\n"
  "v_cvt_pk_bf16_f32 v114, v116, v117\n"
  "v_cvt_pk_bf16_f32 v115, v118, v119\n"
  "ds_write_b64 v3, v[112:113]\n"
  "s_mov_b64 exec, 0xffff\n"
  "ds_write_b64 v3, v[114:115] offset:32\n"
  "s_mov_b64 exec, s[68:69]\n"
  "global_store_dword v127, v126, s[62:63] sc1\n"
  "s_mov_b64 exec, -1\n"
  "s_add_u32 s62, s62, 2048\n"
  "s_addc_u32 s63, s63, 0\n"
  "v_cvt_pk_bf16_f32 v48, v48, v49\n"
  "v_cvt_pk_bf16_f32 v49, v50, v51\n"
  "v_cvt_pk_bf16_f32 v52, v52, v53\n"
  "v_cvt_pk_bf16_f32 v53, v54, v55\n"
  "v_cvt_pk_bf16_f32 v56, v56, v57\n"
  "v_cvt_pk_bf16_f32 v57, v58, v59\n"
  "v_cvt_pk_bf16_f32 v60, v60, v61\n"
  "v_cvt_pk_bf16_f32 v61, v62, v63\n"
  "ds_write_b64 v10, v[48:49] offset:0\n"
  "ds_write_b64 v10, v[52:53] offset:4864\n"
  "ds_write_b64 v11, v[56:57] offset:0\n"
  "ds_write_b64 v11, v[60:61] offset:4864\n"
  "s_add_u32 s56, s56, 1\n"
  "s_waitcnt vmcnt(2) lgkmcnt(0)\n"
  "s_barrier\n"
  "v_add_u32 v64, 59136, v2\n"
  "v_xor_b32 v65, 64, v64\n"
  "s_add_u32 s58, s80, 22272\n"
  "s_cmp_lt_i32 s56, 62\n"
  "s_cselect_b32 s74, 36864, 0\n"
  "s_add_u32 s38, s38, s74\n"
  "s_addc_u32 s39, s39, 0\n"
  "ds_read_b128 v[120:123], v4\n"
  "ds_read_b128 v[16:19], v64 offset:0\n"
  "ds_read_b128 v[20:23], v64 offset:2048\n"
  "ds_read_b128 v[24:27], v65 offset:0\n"
  "ds_read_b128 v[28:31], v65 offset:2048\n"
  "ds_read_b128 v[32:35], v64 offset:4096\n"
  "ds_read_b128 v[36:39], v64 offset:6144\n"
  "s_waitcnt lgkmcnt(5)\n"
  "v_mfma_f32_16x16x32_bf16 v[48:51], a[0:3], v[16:19], 0\n"
  "ds_read_b128 v[40:43], v65 offset:4096\n"
  "ds_read_b128 v[76:79], v66\n"
  "s_nop 0\n"
  "s_waitcnt lgkmcnt(6)\n"
  "v_mfma_f32_16x16x32_bf16 v[52:55], a[0:3], v[20:23], 0\n"
  "ds_read_b128 v[44:47], v65 offset:6144\n"
  "ds_read_b128 v[80:83], v68\n"
  "s_nop 0\n"
  "v_mfma_f32_16x16x32_bf16 v[56:59], a[72:75], v[16:19], 0\n"
  "s_bfm_b64 exec, 40, 0\n"
  "global_store_dwordx4 v5, v[120:123], s[60:61] sc1\n"
  "s_mov_b64 exec, -1\n"
  "s_add_u32 s60, s60, 40960\n"
  "s_addc_u32 s61, s61, 0\n"
  "v_mfma_f32_16x16x32_bf16 v[60:63], a[72:75], v[20:23], 0\n"
  "ds_read_b128 v[84:87], v69\n"
  "s_nop 0\n"
  "s_waitcnt lgkmcnt(8)\n"
  "v_mfma_f32_16x16x32_bf16 v[48:51], a[4:7], v[24:27], v[48:51]\n"
  "ds_read_b128 v[16:19], v64 offset:8192\n"
  "ds_read_b64_tr_b16 v[88:89], v72 offset:0\n"
  "s_nop 0\n"
  "s_waitcnt lgkmcnt(9)\n"
  "v_mfma_f32_16x16x32_bf16 v[52:55], a[4:7], v[28:31], v[52:55]\n"
  "ds_read_b128 v[20:23], v64 offset:10240\n"
  "ds_read_b64_tr_b16 v[90:91], v73 offset:0\n"
  "s_nop 0\n"
  "v_mfma_f32_16x16x32_bf16 v[56:59], a[76:79], v[24:27], v[56:59]\n"
  "s_add_u32 m0, s58, 0\n"
  "s_nop 0\n"
  "global_load_lds_dwordx4 v1, s[38:39] offset:0\n"
  "v_mfma_f32_16x16x32_bf16 v[60:63], a[76:79], v[28:31], v[60:63]\n"
  "ds_read_b64_tr_b16 v[92:93], v72 offset:32\n"
  "s_nop 0\n"
  "s_waitcnt lgkmcnt(11)\n"
  "v_mfma_f32_16x16x32_bf16 v[48:51], a[8:11], v[32:35], v[48:51]\n"
  "ds_read_b128 v[24:27], v65 offset:8192\n"
  "ds_read_b64_tr_b16 v[94:95], v73 offset:32\n"
  "s_nop 0\n"
  "s_waitcnt lgkmcnt(12)\n"
  "v_mfma_f32_16x16x32_bf16 v[52:55], a[8:11], v[36:39], v[52:55]\n"
  "ds_read_b128 v[28:31], v65 offset:10240\n"
  "ds_read_b32 v126, v6 offset:0\n"
  "s_nop 0\n"
  "v_mfma_f32_16x16x32_bf16 v[56:59], a[80:83], v[32:35], v[56:59]\n"
  "s_add_u32 m0, s58, 3968\n"
  "s_nop 0\n"
  "global_load_lds_dwordx4 v1, s[38:39] offset:128\n"
  "v_mfma_f32_16x16x32_bf16 v[60:63], a[80:83], v[36:39], v[60:63]\n"
  "s_waitcnt lgkmcnt(10)\n"
  "v_mfma_f32_16x16x32_bf16 v[96:99], v[80:83], v[76:79], 0\n"
  "v_mfma_f32_16x16x32_bf16 v[48:51], a[12:15], v[40:43], v[48:51]\n"
  "ds_read_b128 v[32:35], v64 offset:12288\n"
  "s_waitcnt lgkmcnt(10)\n"
  "v_mfma_f32_16x16x32_bf16 v[100:103], v[84:87], v[76:79], 0\n"
  "v_mfma_f32_16x16x32_bf16 v[52:55], a[12:15], v[44:47], v[52:55]\n"
  "ds_read_b128 v[36:39], v64 offset:14336\n"
  "s_waitcnt lgkmcnt(2)\n"
  "v_lshlrev_b32 v126, 16, v126\n"
  "v_mfma_f32_16x16x32_bf16 v[56:59], a[84:87], v[40:43], v[56:59]\n"
  "s_add_u32 m0, s58, 7936\n"
  "s_nop 0\n"
  "global_load_lds_dwordx4 v1, s[38:39] offset:256\n"
  "v_mfma_f32_16x16x32_bf16 v[60:63], a[84:87], v[44:47], v[60:63]\n"
  "v_mul_f32 v126, 0xbfb8aa3b, v126\n"
  "v_exp_f32 v126, v126\n"
  "v_mfma_f32_16x16x32_bf16 v[48:51], a[16:19], v[16:19], v[48:51]\n"
  "ds_read_b128 v[40:43], v65 offset:12288\n"
  "v_add_f32 v126, 1.0, v126\n"
  "v_rcp_f32 v126, v126\n"
  "v_mfma_f32_16x16x32_bf16 v[52:55], a[16:19], v[20:23], v[52:55]\n"
  "ds_read_b128 v[44:47], v65 offset:14336\n"
  "v_cvt_pk_bf16_f32 v126, v126, v126\n"
  "v_lshlrev_b32 v126, 16, v126\n"
  "v_mfma_f32_16x16x32_bf16 v[56:59], a[88:91], v[16:19], v[56:59]\n"
  "s_add_u32 m0, s58, 11904\n"
  "s_nop 0\n"
  "global_load_lds_dwordx4 v1, s[38:39] offset:384\n"
  "v_mfma_f32_16x16x32_bf16 v[60:63], a[88:91], v[20:23], v[60:63]\n"
  "v_cndmask_b32 v104, v96, v97, s[70:71]\n"
  "v_cndmask_b32 v105, v98, v99, s[70:71]\n"
  "v_mfma_f32_16x16x32_bf16 v[48:51], a[20:23], v[24:27], v[48:51]\n"
  "ds_read_b128 v[16:19], v64 offset:16384\n"
  "v_cndmask_b32 v106, v100, v101, s[70:71]\n"
  "v_cndmask_b32 v107, v102, v103, s[70:71]\n"
  "v_mfma_f32_16x16x32_bf16 v[52:55], a[20:23], v[28:31], v[52:55]\n"
  "ds_read_b128 v[20:23], v64 offset:18432\n"
  "v_cvt_pk_bf16_f32 v104, v104, v105\n"
  "v_and_b32 v105, 0xffff0000, v104\n"
  "v_mfma_f32_16x16x32_bf16 v[56:59], a[92:95], v[24:27], v[56:59]\n"
  "s_add_u32 m0, s58, 15872\n"
  "s_nop 0\n"
  "global_load_lds_dwordx4 v1, s[38:39] offset:512\n"
  "v_mfma_f32_16x16x32_bf16 v[60:63], a[92:95], v[28:31], v[60:63]\n"
  "v_lshlrev_b32 v104, 16, v104\n"
  "v_cvt_pk_bf16_f32 v106, v106, v107\n"
  "s_waitcnt lgkmcnt(5)\n"
  "v_mfma_f32_16x16x32_bf16 v[48:51], a[24:27], v[32:35], v[48:51]\n"
  "ds_read_b128 v[24:27], v65 offset:16384\n"
  "v_and_b32 v107, 0xffff0000, v106\n"
  "v_lshlrev_b32 v106, 16, v106\n"
  "s_waitcnt lgkmcnt(5)\n"
  "v_mfma_f32_16x16x32_bf16 v[52:55], a[24:27], v[36:39], v[52:55]\n"
  "ds_read_b128 v[28:31], v65 offset:18432\n"
  "v_mul_f32 v104, 0x3e715bef, v104\n"
  "v_mul_f32 v105, 0x3e715bef, v105\n"
  "v_mfma_f32_16x16x32_bf16 v[56:59], a[96:99], v[32:35], v[56:59]\n"
  "s_add_u32 m0, s58, 19840\n"
  "s_nop 0\n"
  "global_load_lds_dwordx4 v1, s[38:39] offset:640\n"
  "v_mfma_f32_16x16x32_bf16 v[60:63], a[96:99], v[36:39], v[60:63]\n"
  "v_mul_f32 v106, 0x3e715bef, v106\n"
  "v_mul_f32 v107, 0x3e715bef, v107\n"
  "s_waitcnt lgkmcnt(5)\n"
  "v_mfma_f32_16x16x32_bf16 v[48:51], a[28:31], v[40:43], v[48:51]\n"
  "ds_read_b128 v[32:35], v64 offset:20480\n"
  "v_cvt_pk_bf16_f32 v104, v104, v105\n"
  "v_and_b32 v105, 0xffff0000, v104\n"
  "s_waitcnt lgkmcnt(5)\n"
  "v_mfma_f32_16x16x32_bf16 v[52:55], a[28:31], v[44:47], v[52:55]\n"
  "ds_read_b128 v[36:39], v64 offset:22528\n"
  "v_lshlrev_b32 v104, 16, v104\n"
  "v_cvt_pk_bf16_f32 v106, v106, v107\n"
  "v_mfma_f32_16x16x32_bf16 v[56:59], a[100:103], v[40:43], v[56:59]\n"
  "s_add_u32 m0, s58, 23808\n"
  "s_nop 0\n"
  "global_load_lds_dwordx4 v1, s[38:39] offset:768\n"
  "v_mfma_f32_16x16x32_bf16 v[60:63], a[100:103], v[44:47], v[60:63]\n"
  "v_and_b32 v107, 0xffff0000, v106\n"
  "v_lshlrev_b32 v106, 16, v106\n"
  "s_waitcnt lgkmcnt(5)\n"
  "v_mfma_f32_16x16x32_bf16 v[48:51], a[32:35], v[16:19], v[48:51]\n"
  "ds_read_b128 v[40:43], v65 offset:20480\n"
  "v_cndmask_b32 v104, v128, v104, s[8:9]\n"
  "v_cndmask_b32 v105, v128, v105, s[10:11]\n"
  "s_waitcnt lgkmcnt(5)\n"
  "v_mfma_f32_16x16x32_bf16 v[52:55], a[32:35], v[20:23], v[52:55]\n"
  "ds_read_b128 v[44:47], v65 offset:22528\n"
  "v_cndmask_b32 v106, v128, v106, s[12:13]\n"
  "v_cndmask_b32 v107, v128, v107, s[14:15]\n"
  "v_mfma_f32_16x16x32_bf16 v[56:59], a[104:107], v[16:19], v[56:59]\n"
  "s_add_u32 m0, s58, 27776\n"
  "s_nop 0\n"
  "global_load_lds_dwordx4 v1, s[38:39] offset:896\n"
  "v_mfma_f32_16x16x32_bf16 v[60:63], a[104:107], v[20:23], v[60:63]\n"
  "v_max3_f32 v124, v104, v105, v106\n"
  "v_max_f32 v124, v124, v107\n"
  "s_waitcnt lgkmcnt(5)\n"
  "v_mfma_f32_16x16x32_bf16 v[48:51], a[36:39], v[24:27], v[48:51]\n"
  "ds_read_b128 v[16:19], v64 offset:24576\n"
  "v_mov_b32 v125, v124\n"
  "s_nop 1\n"
  "v_permlane16_swap_b32 v124, v125\n"
  "v_max_f32 v124, v124, v125\n"
  "s_waitcnt lgkmcnt(5)\n"
  "v_mfma_f32_16x16x32_bf16 v[52:55], a[36:39], v[28:31], v[52:55]\n"
  "ds_read_b128 v[20:23], v64 offset:26624\n"
  "v_mov_b32 v125, v124\n"
  "s_nop 1\n"
  "v_permlane32_swap_b32 v124, v125\n"
  "v_max_f32 v124, v124, v125\n"
  "v_mfma_f32_16x16x32_bf16 v[56:59], a[108:111], v[24:27], v[56:59]\n"
  "s_add_u32 m0, s58, 31744\n"
  "s_nop 0\n"
  "global_load_lds_dwordx4 v1, s[38:39] offset:1024\n"
  "v_mfma_f32_16x16x32_bf16 v[60:63], a[108:111], v[28:31], v[60:63]\n"
  "v_sub_f32 v104, v104, v124\n"
  "v_sub_f32 v105, v105, v124\n"
  "s_waitcnt lgkmcnt(5)\n"
  "v_mfma_f32_16x16x32_bf16 v[48:51], a[40:43], v[32:35], v[48:51]\n"
  "ds_read_b128 v[24:27], v65 offset:24576\n"
  "v_sub_f32 v106, v106, v124\n"
  "v_sub_f32 v107, v107, v124\n"
  "s_waitcnt lgkmcnt(5)\n"
  "v_mfma_f32_16x16x32_bf16 v[52:55], a[40:43], v[36:39], v[52:55]\n"
  "ds_read_b128 v[28:31], v65 offset:26624\n"
  "v_mul_f32 v104, 0x3fb8aa3b, v104\n"
  "v_mul_f32 v105, 0x3fb8aa3b, v105\n"
  "v_mfma_f32_16x16x32_bf16 v[56:59], a[112:115], v[32:35], v[56:59]\n"
  "v_mul_f32 v106, 0x3fb8aa3b, v106\n"
  "v_mul_f32 v107, 0x3fb8aa3b, v107\n"
  "v_mfma_f32_16x16x32_bf16 v[60:63], a[112:115], v[36:39], v[60:63]\n"
  "v_exp_f32 v104, v104\n"
  "s_waitcnt lgkmcnt(5)\n"
  "v_mfma_f32_16x16x32_bf16 v[48:51], a[44:47], v[40:43], v[48:51]\n"
  "ds_read_b128 v[32:35], v64 offset:28672\n"
  "v_exp_f32 v105, v105\n"
  "s_waitcnt lgkmcnt(5)\n"
  "v_mfma_f32_16x16x32_bf16 v[52:55], a[44:47], v[44:47], v[52:55]\n"
  "ds_read_b128 v[36:39], v64 offset:30720\n"
  "v_exp_f32 v106, v106\n"
  "v_mfma_f32_16x16x32_bf16 v[56:59], a[116:119], v[40:43], v[56:59]\n"
  "v_exp_f32 v107, v107\n"
  "v_mfma_f32_16x16x32_bf16 v[60:63], a[116:119], v[44:47], v[60:63]\n"
  "s_mov_b64 s[8:9], s[16:17]\n"
  "s_mov_b64 s[10:11], s[18:19]\n"
  "s_waitcnt lgkmcnt(5)\n"
  "v_mfma_f32_16x16x32_bf16 v[48:51], a[48:51], v[16:19], v[48:51]\n"
  "ds_read_b128 v[40:43], v65 offset:28672\n"
  "v_add_f32 v124, v104, v105\n"
  "v_add_f32 v124, v124, v106\n"
  "s_waitcnt lgkmcnt(5)\n"
  "v_mfma_f32_16x16x32_bf16 v[52:55], a[48:51], v[20:23], v[52:55]\n"
  "ds_read_b128 v[44:47], v65 offset:30720\n"
  "v_add_f32 v124, v124, v107\n"
  "v_mov_b32 v125, v124\n"
  "s_nop 1\n"
  "v_permlane16_swap_b32 v124, v125\n"
  "v_add_f32 v124, v124, v125\n"
  "v_mfma_f32_16x16x32_bf16 v[56:59], a[120:123], v[16:19], v[56:59]\n"
  "v_mov_b32 v125, v124\n"
  "s_nop 1\n"
  "v_permlane32_swap_b32 v124, v125\n"
  "v_add_f32 v124, v124, v125\n"
  "v_mfma_f32_16x16x32_bf16 v[60:63], a[120:123], v[20:23], v[60:63]\n"
  "v_rcp_f32 v124, v124\n"
  "s_waitcnt lgkmcnt(5)\n"
  "v_mfma_f32_16x16x32_bf16 v[48:51], a[52:55], v[24:27], v[48:51]\n"
  "ds_read_b128 v[16:19], v64 offset:32768\n"
  "v_mul_f32 v104, v124, v104\n"
  "v_mul_f32 v105, v124, v105\n"
  "s_waitcnt lgkmcnt(5)\n"
  "v_mfma_f32_16x16x32_bf16 v[52:55], a[52:55], v[28:31], v[52:55]\n"
  "ds_read_b128 v[20:23], v64 offset:34816\n"
  "v_mul_f32 v106, v124, v106\n"
  "v_mul_f32 v107, v124, v107\n"
  "v_mfma_f32_16x16x32_bf16 v[56:59], a[124:127], v[24:27], v[56:59]\n"
  "v_cvt_pk_bf16_f32 v108, v104, v104\n"
  "v_cvt_pk_bf16_f32 v109, v105, v105\n"
  "v_mfma_f32_16x16x32_bf16 v[60:63], a[124:127], v[28:31], v[60:63]\n"
  "v_cvt_pk_bf16_f32 v110, v106, v106\n"
  "v_cvt_pk_bf16_f32 v111, v107, v107\n"
  "s_waitcnt lgkmcnt(5)\n"
  "v_mfma_f32_16x16x32_bf16 v[48:51], a[56:59], v[32:35], v[48:51]\n"
  "ds_read_b128 v[24:27], v65 offset:32768\n"
  "v_and_b32 v108, v8, v108\n"
  "v_and_b32 v109, v8, v109\n"
  "s_waitcnt lgkmcnt(5)\n"
  "v_mfma_f32_16x16x32_bf16 v[52:55], a[56:59], v[36:39], v[52:55]\n"
  "ds_read_b128 v[28:31], v65 offset:34816\n"
  "v_and_b32 v110, v8, v110\n"
  "v_and_b32 v111, v8, v111\n"
  "v_mfma_f32_16x16x32_bf16 v[56:59], a[128:131], v[32:35], v[56:59]\n"
  "s_waitcnt vmcnt(9)\n"
  "s_barrier\n"
  "v_mov_b32 v9, 0\n"
  "v_mov_b32 v15, s56\n"
  "s_cmp_eq_u32 s47, 0\n"
  "s_cselect_b64 exec, 1, 0\n"
  "global_store_dword v9, v15, s[64:65] sc1\n"
  "s_mov_b64 exec, -1\n"
  "v_mfma_f32_16x16x32_bf16 v[60:63], a[128:131], v[36:39], v[60:63]\n"
  "v_mfma_f32_16x16x32_bf16 v[112:115], v[88:91], v[108:111], 0\n"
  "s_waitcnt lgkmcnt(5)\n"
  "v_mfma_f32_16x16x32_bf16 v[48:51], a[60:63], v[40:43], v[48:51]\n"
  "v_mfma_f32_16x16x32_bf16 v[116:119], v[92:95], v[108:111], 0\n"
  "s_waitcnt lgkmcnt(4)\n"
  "v_mfma_f32_16x16x32_bf16 v[52:55], a[60:63], v[44:47], v[52:55]\n"
  "v_mfma_f32_16x16x32_bf16 v[56:59], a[132:135], v[40:43], v[56:59]\n"
  "v_mfma_f32_16x16x32_bf16 v[60:63], a[132:135], v[44:47], v[60:63]\n"
  "s_waitcnt lgkmcnt(3)\n"
  "v_mfma_f32_16x16x32_bf16 v[48:51], a[64:67], v[16:19], v[48:51]\n"
  "s_waitcnt lgkmcnt(2)\n"
  "v_mfma_f32_16x16x32_bf16 v[52:55], a[64:67], v[20:23], v[52:55]\n"
  "v_mfma_f32_16x16x32_bf16 v[56:59], a[136:139], v[16:19], v[56:59]\n"
  "v_mfma_f32_16x16x32_bf16 v[60:63], a[136:139], v[20:23], v[60:63]\n"
  "s_waitcnt lgkmcnt(1)\n"
  "v_mfma_f32_16x16x32_bf16 v[48:51], a[68:71], v[24:27], v[48:51]\n"
  "s_waitcnt lgkmcnt(0)\n"
  "v_mfma_f32_16x16x32_bf16 v[52:55], a[68:71], v[28:31], v[52:55]\n"
  "v_mfma_f32_16x16x32_bf16 v[56:59], a[140:143], v[24:27], v[56:59]\n"
  "v_mfma_f32_16x16x32_bf16 v[60:63], a[140:143], v[28:31], v[60:63]\n"
  "v_cvt_pk_bf16_f32 v112, v112, v113\n"
  "v_cvt_pk_bf16_f32 v113, v114, v115\n"
  "v_cvt_pk_bf16_f32 v114, v116, v117\n"
  "v_cvt_pk_bf16_f32 v115, v118, v119\n"
  "ds_write_b64 v3, v[112:113]\n"
  "s_mov_b64 exec, 0xffff\n"
  "ds_write_b64 v3, v[114:115] offset:32\n"
  "s_mov_b64 exec, s[68:69]\n"
  "global_store_dword v127, v126, s[62:63] sc1\n"
  "s_mov_b64 exec, -1\n"
  "s_add_u32 s62, s62, 2048\n"
  "s_addc_u32 s63, s63, 0\n"
  "v_cvt_pk_bf16_f32 v48, v48, v49\n"
  "v_cvt_pk_bf16_f32 v49, v50, v51\n"
  "v_cvt_pk_bf16_f32 v52, v52, v53\n"
  "v_cvt_pk_bf16_f32 v53, v54, v55\n"
  "v_cvt_pk_bf16_f32 v56, v56, v57\n"
  "v_cvt_pk_bf16_f32 v57, v58, v59\n"
  "v_cvt_pk_bf16_f32 v60, v60, v61\n"
  "v_cvt_pk_bf16_f32 v61, v62, v63\n"
  "ds_write_b64 v10, v[48:49] offset:9728\n"
  "ds_write_b64 v10, v[52:53] offset:14592\n"
  "ds_write_b64 v11, v[56:57] offset:9728\n"
  "ds_write_b64 v11, v[60:61] offset:14592\n"
  "s_add_u32 s56, s56, 1\n"
  "s_cmp_lt_u32 s56, 63\n"
  "s_cbranch_scc1 ALOOP\n"
  "s_waitcnt lgkmcnt(0)\n"
  "s_barrier\n"
  "ds_read_b128 v[120:123], v4\n"
  "s_bfm_b64 exec, 40, 0\n"
  "s_waitcnt lgkmcnt(0)\n"
  "global_store_dwordx4 v5, v[120:123], s[60:61] sc1\n"
  "s_mov_b64 exec, -1\n"
  "s_add_u32 s60, s60, 40960\n"
  "s_addc_u32 s61, s61, 0\n"
  "ds_read_b128 v[76:79], v67\n"
  "s_nop 0\n"
  "ds_read_b128 v[80:83], v70\n"
  "s_nop 0\n"
  "ds_read_b128 v[84:87], v71\n"
  "s_nop 0\n"
  "ds_read_b64_tr_b16 v[88:89], v74 offset:0\n"
  "s_nop 0\n"
  "ds_read_b64_tr_b16 v[90:91], v75 offset:0\n"
  "s_nop 0\n"
  "ds_read_b64_tr_b16 v[92:93], v74 offset:32\n"
  "s_nop 0\n"
  "ds_read_b64_tr_b16 v[94:95], v75 offset:32\n"
  "s_nop 0\n"
  "ds_read_b32 v126, v6 offset:9728\n"
  "s_nop 0\n"
  "s_waitcnt lgkmcnt(6)\n"
  "v_mfma_f32_16x16x32_bf16 v[96:99], v[80:83], v[76:79], 0\n"
  "s_waitcnt lgkmcnt(5)\n"
  "v_mfma_f32_16x16x32_bf16 v[100:103], v[84:87], v[76:79], 0\n"
  "s_waitcnt lgkmcnt(0)\n"
  "v_lshlrev_b32 v126, 16, v126\n"
  "v_mul_f32 v126, 0xbfb8aa3b, v126\n"
  "v_exp_f32 v126, v126\n"
  "s_nop 0\n"
  "v_add_f32 v126, 1.0, v126\n"
  "v_rcp_f32 v126, v126\n"
  "s_nop 0\n"
  "v_cvt_pk_bf16_f32 v126, v126, v126\n"
  "v_lshlrev_b32 v126, 16, v126\n"
  "v_cndmask_b32 v104, v96, v97, s[70:71]\n"
  "v_cndmask_b32 v105, v98, v99, s[70:71]\n"
  "v_cndmask_b32 v106, v100, v101, s[70:71]\n"
  "v_cndmask_b32 v107, v102, v103, s[70:71]\n"
  "v_cvt_pk_bf16_f32 v104, v104, v105\n"
  "v_and_b32 v105, 0xffff0000, v104\n"
  "v_lshlrev_b32 v104, 16, v104\n"
  "v_cvt_pk_bf16_f32 v106, v106, v107\n"
  "v_and_b32 v107, 0xffff0000, v106\n"
  "v_lshlrev_b32 v106, 16, v106\n"
  "v_mul_f32 v104, 0x3e715bef, v104\n"
  "v_mul_f32 v105, 0x3e715bef, v105\n"
  "v_mul_f32 v106, 0x3e715bef, v106\n"
  "v_mul_f32 v107, 0x3e715bef, v107\n"
  "v_cvt_pk_bf16_f32 v104, v104, v105\n"
  "v_and_b32 v105, 0xffff0000, v104\n"
  "v_lshlrev_b32 v104, 16, v104\n"
  "v_cvt_pk_bf16_f32 v106, v106, v107\n"
  "v_and_b32 v107, 0xffff0000, v106\n"
  "v_lshlrev_b32 v106, 16, v106\n"
  "v_cndmask_b32 v104, v128, v104, s[8:9]\n"
  "v_cndmask_b32 v105, v128, v105, s[10:11]\n"
  "v_cndmask_b32 v106, v128, v106, s[12:13]\n"
  "v_cndmask_b32 v107, v128, v107, s[14:15]\n"
  "v_max3_f32 v124, v104, v105, v106\n"
  "v_max_f32 v124, v124, v107\n"
  "v_mov_b32 v125, v124\n"
  "s_nop 1\n"
  "v_permlane16_swap_b32 v124, v125\n"
  "v_max_f32 v124, v124, v125\n"
  "v_mov_b32 v125, v124\n"
  "s_nop 1\n"
  "v_permlane32_swap_b32 v124, v125\n"
  "v_max_f32 v124, v124, v125\n"
  "v_sub_f32 v104, v104, v124\n"
  "v_sub_f32 v105, v105, v124\n"
  "v_sub_f32 v106, v106, v124\n"
  "v_sub_f32 v107, v107, v124\n"
  "v_mul_f32 v104, 0x3fb8aa3b, v104\n"
  "v_mul_f32 v105, 0x3fb8aa3b, v105\n"
  "v_mul_f32 v106, 0x3fb8aa3b, v106\n"
  "v_mul_f32 v107, 0x3fb8aa3b, v107\n"
  "v_exp_f32 v104, v104\n"
  "v_exp_f32 v105, v105\n"
  "v_exp_f32 v106, v106\n"
  "v_exp_f32 v107, v107\n"
  "s_mov_b64 s[8:9], s[16:17]\n"
  "s_mov_b64 s[10:11], s[18:19]\n"
  "v_add_f32 v124, v104, v105\n"
  "v_add_f32 v124, v124, v106\n"
  "v_add_f32 v124, v124, v107\n"
  "v_mov_b32 v125, v124\n"
  "s_nop 1\n"
  "v_permlane16_swap_b32 v124, v125\n"
  "v_add_f32 v124, v124, v125\n"
  "v_mov_b32 v125, v124\n"
  "s_nop 1\n"
  "v_permlane32_swap_b32 v124, v125\n"
  "v_add_f32 v124, v124, v125\n"
  "v_rcp_f32 v124, v124\n"
  "s_nop 0\n"
  "v_mul_f32 v104, v124, v104\n"
  "v_mul_f32 v105, v124, v105\n"
  "v_mul_f32 v106, v124, v106\n"
  "v_mul_f32 v107, v124, v107\n"
  "v_cvt_pk_bf16_f32 v108, v104, v104\n"
  "v_cvt_pk_bf16_f32 v109, v105, v105\n"
  "v_cvt_pk_bf16_f32 v110, v106, v106\n"
  "v_cvt_pk_bf16_f32 v111, v107, v107\n"
  "v_and_b32 v108, v8, v108\n"
  "v_and_b32 v109, v8, v109\n"
  "v_and_b32 v110, v8, v110\n"
  "v_and_b32 v111, v8, v111\n"
  "s_nop 1\n"
  "v_mfma_f32_16x16x32_bf16 v[112:115], v[88:91], v[108:111], 0\n"
  "v_mfma_f32_16x16x32_bf16 v[116:119], v[92:95], v[108:111], 0\n"
  "s_barrier\n"
  "s_nop 3\n"
  "v_cvt_pk_bf16_f32 v112, v112, v113\n"
  "v_cvt_pk_bf16_f32 v113, v114, v115\n"
  "s_nop 0\n"
  "v_cvt_pk_bf16_f32 v114, v116, v117\n"
  "v_cvt_pk_bf16_f32 v115, v118, v119\n"
  "ds_write_b64 v3, v[112:113]\n"
  "s_mov_b64 exec, 0xffff\n"
  "ds_write_b64 v3, v[114:115] offset:32\n"
  "s_mov_b64 exec, s[68:69]\n"
  "global_store_dword v127, v126, s[62:63] sc1\n"
  "s_mov_b64 exec, -1\n"
  "s_add_u32 s62, s62, 2048\n"
  "s_addc_u32 s63, s63, 0\n"
  "s_waitcnt lgkmcnt(0)\n"
  "s_barrier\n"
  "ds_read_b128 v[120:123], v4\n"
  "s_bfm_b64 exec, 40, 0\n"
  "s_waitcnt lgkmcnt(0)\n"
  "global_store_dwordx4 v5, v[120:123], s[60:61] sc1\n"
  "s_mov_b64 exec, -1\n"
  "s_add_u32 s60, s60, 40960\n"
  "s_addc_u32 s61, s61, 0\n"
  "s_waitcnt vmcnt(0)\n"
  "s_barrier\n"
  "v_mov_b32 v9, 0\n"
  "v_mov_b32 v15, 64\n"
  "s_cmp_eq_u32 s47, 0\n"
  "s_cselect_b64 exec, 1, 0\n"
  "global_store_dword v9, v15, s[64:65] sc1\n"
  "s_mov_b64 exec, -1\n"
  "s_endpgm\n"
      : "+{v[0:15]}"(vin), "+{s[36:43]}"(sa), "+{s[44:59]}"(sb) : "{s[4:5]}"(pol) : CLOB);
  }
}

extern "C" void kernel_launch(void* const* d_in, const int* in_sizes, int n_in, void* d_out, int out_size, void* d_ws, size_t ws_size,
                              hipStream_t stream) {
  Params p;
  p.x = (const u16*)d_in[0]; p.wqkv = (const u16*)d_in[1]; p.wo_a = (const u16*)d_in[2];
  p.mem0 = (const u16*)d_in[4]; p.Wq = (const u16*)d_in[5]; p.Wk = (const u16*)d_in[6]; p.Wv = (const u16*)d_in[7];
  p.Wo = (const u16*)d_in[8]; p.gate = (const u16*)d_in[10];
  p.out = (u16*)d_out; p.ws = (unsigned char*)d_ws + ((0x200000ull - ((unsigned long long)d_ws & 0x1FFFFFull)) & 0x1FFFFFull);
  (void)hipFuncSetAttribute((const void*)gm_kernel, hipFuncAttributeMaxDynamicSharedMemorySize, LDS_BYTES);
  hipLaunchKernelGGL(gm_setup, dim3(97), dim3(512), 0, stream, p);
  hipLaunchKernelGGL(gm_kernel, dim3(256), dim3(256), LDS_BYTES, stream, p);
}
```

```cpp
#include <hip/hip_runtime.h>
#include <stdint.h>
#define LAS __attribute__((address_space(3)))
#define GAS __attribute__((address_space(1)))
typedef unsigned v2u __attribute__((ext_vector_type(2)));
typedef unsigned v4u __attribute__((ext_vector_type(4)));
typedef unsigned v8u __attribute__((ext_vector_type(8)));
typedef unsigned v16u __attribute__((ext_vector_type(16)));
typedef unsigned v32u __attribute__((ext_vector_type(32)));
typedef float f4v __attribute__((ext_vector_type(4)));
typedef short s4v __attribute__((ext_vector_type(4)));
typedef __bf16 b8v __attribute__((ext_vector_type(8)));
typedef __bf16 b2v __attribute__((ext_vector_type(2)));
typedef unsigned short u16;
typedef GAS unsigned gu32;
typedef LAS unsigned char* lptr;
#define RLX_AGENT __ATOMIC_RELAXED, __HIP_MEMORY_SCOPE_AGENT

constexpr int DM = 576, NSEG = 64, LDS_BYTES = 160 * 1024, SPIN = 400000;
constexpr unsigned CH = 32768;
constexpr unsigned CTL = 16u << 20, CTL_BYTES = 11 * 16384;
constexpr unsigned C_TICK = CTL, C_OVF = CTL + 1024, C_CLS = CTL + 1408, C_VOTE = CTL + 1536, C_TAB = CTL + CTL_BYTES;
constexpr unsigned C_REP = CTL + 16384, C_KVP = CTL + 163840, C_HF = CTL + 24576, C_VF = CTL + 40960, C_QAP = CTL + 57344, C_AP = C_QAP + 4096, C_QAL = CTL + 73728, C_OP = CTL + 90112;
constexpr unsigned WO_B = 20u << 20;
constexpr unsigned KVH = 0;
constexpr unsigned WO_KM = 150u << 20, WO_VP = 154u << 20;
constexpr unsigned WO_OA = 158u << 20;
constexpr unsigned WO_G = 161u << 20;
constexpr unsigned WO_ST = 162u << 20, WO_DB = 172u << 20;
constexpr float QS = 0.08838834764831845f * 1.4426950408889634f;

struct Params { const u16* x; const u16* wqkv; const u16* wo_a; const u16* mem0; const u16* Wq; const u16* Wk; const u16* Wv; const u16* Wo; const u16* gate;
                u16* out; unsigned char* ws; };

__device__ __forceinline__ unsigned xcc_id() { return (unsigned)__builtin_amdgcn_s_getreg(20 | (3 << 11)) & 0xfu; }
__device__ __forceinline__ unsigned rfl(unsigned v) { return __builtin_amdgcn_readfirstlane(v); }
__device__ __forceinline__ f4v mfma32(v4u a, v4u b, f4v c) { return __builtin_amdgcn_mfma_f32_16x16x32_bf16(__builtin_bit_cast(b8v, a), __builtin_bit_cast(b8v, b), c, 0, 0, 0); }
__device__ __forceinline__ unsigned pack2(float lo, float hi) { b2v p; p.x = (__bf16)lo; p.y = (__bf16)hi; return __builtin_bit_cast(unsigned, p); }
__device__ __forceinline__ v4u pack8(f4v a, f4v b) { return (v4u){pack2(a.x, a.y), pack2(a.z, a.w), pack2(b.x, b.y), pack2(b.z, b.w)}; }
__device__ __forceinline__ float rbf(float x) { return (float)(__bf16)x; }
__device__ __forceinline__ float bflo(unsigned u) { return __uint_as_float(u << 16); }
__device__ __forceinline__ float bfhi(unsigned u) { return __uint_as_float(u & 0xffff0000u); }
__device__ __forceinline__ v2u lds_tr(lptr p) { return __builtin_bit_cast(v2u, __builtin_amdgcn_ds_read_tr16_b64_v4i16((LAS s4v*)p)); }
__device__ __forceinline__ v4u lds16(lptr p) { return *(LAS v4u*)p; }
#define WAIT_VM(n) asm volatile("s_waitcnt vmcnt(" #n ")" ::: "memory")
__device__ __forceinline__ void wg_barrier() { asm volatile("s_waitcnt lgkmcnt(0)\n\ts_barrier" ::: "memory"); }
#define DMA_BODY(FL) unsigned keep; asm volatile("s_mov_b32 %0, m0\n\ts_mov_b32 m0, %3\n\ts_nop 0\n\tglobal_load_lds_dwordx4 %1, %2" FL "\n\ts_mov_b32 m0, %0" \
                                              : "=&s"(keep) : "v"(off), "s"(base), "s"(dst) : "memory")
__device__ __forceinline__ void dma(const void* base, unsigned off, unsigned dst) { DMA_BODY(""); }
__device__ __forceinline__ void dma_dev(const void* base, unsigned off, unsigned dst) { DMA_BODY(" sc1"); }
__device__ __forceinline__ void st16(unsigned char* ws, unsigned off, v4u v) { asm volatile("global_store_dwordx4 %0, %1, %2 sc1\n\ts_nop 1" :: "v"(off), "v"(v), "s"(ws) : "memory"); }
__device__ __forceinline__ unsigned ctl_ld(unsigned char* ws, unsigned off) { return __hip_atomic_load((gu32*)(ws + off), RLX_AGENT); }
__device__ __forceinline__ void ctl_st(unsigned char* ws, unsigned off, unsigned v) { __hip_atomic_store((gu32*)(ws + off), v, RLX_AGENT); }
__device__ __forceinline__ unsigned ctl_add(unsigned char* ws, unsigned off, unsigned v) { return __hip_atomic_fetch_add((gu32*)(ws + off), v, RLX_AGENT); }
__device__ __forceinline__ bool wait_ge(unsigned char* ws, unsigned off, unsigned want) {
  for (int i = 0; i < SPIN; ++i) {
    if (__all(ctl_ld(ws, off) >= want)) return true;
    __builtin_amdgcn_s_sleep(4);
  }
  return false;
}
__device__ __forceinline__ unsigned chunk(unsigned cls, unsigned g, unsigned c, unsigned off) {
  const unsigned fix = (((c >> 4) & 1) << 2) | ((c >> 5) & 1);
  return g * 0x200000u + c * CH + ((((cls >> (4 * g)) & 7) ^ fix) << 12) + off;
}
__device__ __forceinline__ float xor16(float v) { auto s = __builtin_amdgcn_permlane16_swap(__float_as_uint(v), __float_as_uint(v), false, false); return __uint_as_float((threadIdx.x & 16) ? s[0] : s[1]); }
__device__ __forceinline__ float xor32(float v) { auto s = __builtin_amdgcn_permlane32_swap(__float_as_uint(v), __float_as_uint(v), false, false); return __uint_as_float((threadIdx.x & 32) ? s[0] : s[1]); }
template <int C> __device__ __forceinline__ float dpp(float v) { return __int_as_float(__builtin_amdgcn_update_dpp(0, __float_as_int(v), C, 0xf, 0xf, true)); }
#define STAMP(s, i) do { } while (0)
__device__ __forceinline__ unsigned x_src(int wave, int lane) { return (8 * wave + (lane >> 3)) * 1152 + ((lane & 7) ^ ((4 * wave + (lane >> 4)) & 7)) * 16; }
__device__ __forceinline__ unsigned x_frag(int lane) { return (lane & 15) * 128 + ((lane >> 4) ^ ((lane >> 1) & 7)) * 16; }

#define UNIFORM(v, n) _Pragma("unroll") for (int i_ = 0; i_ < n; ++i_) v[i_] = rfl(v[i_])
#define PTR(v, i, ptr) do { const unsigned long long a_ = (unsigned long long)(ptr); v[i] = (unsigned)a_; v[(i) + 1] = (unsigned)(a_ >> 32); } while (0)
#define CLOB "v255", "a255", "s101", "vcc", "memory"

#define ARGS const __attribute__((address_space(4))) Params* pp_ = (const __attribute__((address_space(4))) Params*)__builtin_amdgcn_kernarg_segment_ptr(); \
             asm volatile("" : "+s"(pp_)); const __attribute__((address_space(4))) Params& p = *pp_

__global__ void __launch_bounds__(256, 1) gm_kernel(Params) {
  ARGS;
  extern __shared__ __attribute__((aligned(16))) unsigned char smem[];
  const lptr lds = (lptr)smem;
  const int tid = threadIdx.x, lane = tid & 63, wave = rfl(tid >> 6);
  const unsigned die = xcc_id() & 7, probe = chunk(0, die & 3, 15, ((blockIdx.x >> 3) & 31) * 128);
  if (wave == 1 && lane < 8 && die < 4) (void)ctl_add(p.ws, probe + (lane << 12), 1u);
  if (wave == 0) {
    const unsigned x = die;
    unsigned t = 0;
    if (lane == 0) t = ctl_add(p.ws, C_TICK + x * 128, 1u);
    t = rfl(t);
    int role = x * 32 + t;
    if (t >= 32) {
      unsigned j = 0;
      if (lane == 0) j = ctl_add(p.ws, C_OVF, 1u);
      j = rfl(j); role = -1;
      for (int i = 0; i < SPIN && role < 0; ++i) {
        const unsigned cnt = ctl_ld(p.ws, C_TICK + (lane & 7) * 128);
        unsigned sum = 0, left = j; int r = -1;
        for (int d = 0; d < 8; ++d) {
          const unsigned cd = __builtin_amdgcn_readlane(cnt, d), open = cd < 32 ? 32 - cd : 0;
          sum += cd;
          if (r < 0) { if (left < open) r = d * 32 + cd + left; else left -= open; }
        }
        if (sum == 256) role = r;
      }
    }
    if (lane == 0) *(LAS int*)lds = role;
  }
  wg_barrier();
  const int role = rfl(*(LAS int*)lds);
  wg_barrier();
  if (role < 0) return;
  STAMP(63, 15);
  const int n = lane & 15, g = lane >> 4, qq = n >> 2, c4 = n & 3, gam = role >> 5, tk = role & 31;
  v16u vin; v8u sa; v16u sb;
#pragma unroll
  for (int i = 0; i < 16; ++i) { vin[i] = 0; sb[i] = 0; }
#pragma unroll
  for (int i = 0; i < 8; ++i) sa[i] = 0;
  vin[0] = lane * 16;
  if (gam < 4) {
    const unsigned m1pat = ((lane >> 5) * 4 + qq) * 128 + (g & 1) * 64 + c4 * 16, idx = (lane & 2) ? 0xEEEEu : 0x4444u;
    const int trrow = 16 * (g & 1) + 2 * (g >> 1) + 4 * (qq & 1) + (qq >> 1);
    sb[4] = wave; sb[10] = WO_ST + (role * 4 + wave) * 4096;
    if (tk < 16) { ARGS; PTR(sa, 0, p.ws);
      const int h = gam * 8 + (tk >> 1), half = tk & 1, flip = gam & 1;
      vin[1] = m1pat + wave * 1024; vin[2] = qq * 256 + g * 64 + c4 * 16;
      vin[3] = 9216 + (32 * wave + n) * 32 + 8 * (g ^ qq);
      vin[4] = 9216 + 4096 + (16 * wave + n) * 32 + 8 * (g ^ qq);
      vin[5] = 9216 + (4 * g + qq) * 32 + 8 * (c4 ^ g);
      vin[6] = idx; vin[7] = (lane & 2) ? 0x0c0c0c0cu : 0x07060302u; vin[8] = (lane & 2) ? 0x07060302u : 0x0c0c0c0cu;
#pragma unroll
      for (int i = 0; i < 3; ++i) {
        const int cg = i < 2 ? 8 * half + 2 * wave + (i ^ flip) : 16 + half, gr = (cg & 1) + 2 * (g >> 1), hs = ((0x3120 >> (4 * (gr ^ gam))) & 3) * 8 + (h & 7);
        vin[9 + i] = chunk(0, gr, 28 + 4 * (cg >> 1) + (hs >> 4), (hs & 15) * 256 + (i == 2 ? (wave & 1) * 128 : 0) + (g & 1) * 64 + n * 4);
      }
      { const int row = 4 * (wave & 1) + 16 * (wave >> 1) + g, sw = 2 * ((row & 1) | ((row >> 1) & 2) | ((row >> 2) & 4)); vin[12] = row * 8192 + (n ^ sw) * 16; }
      { const int sw = 2 * ((trrow & 1) | ((trrow >> 1) & 2) | ((trrow >> 2) & 4)); vin[13] = trrow * 256 + ((4 * wave + (c4 >> 1)) ^ sw) * 16 + (c4 & 1) * 8; }
      vin[14] = (16 * wave + (lane >> 3)) * 1152 + ((lane & 7) ^ (2 * wave)) * 16;
      { const int row = 32 * (g & 1) + 4 * (g >> 1) + (qq & 1) + 16 * (qq >> 1); vin[15] = row * 128 + ((c4 >> 1) ^ (2 * ((row >> 4) & 3))) * 16 + (c4 & 1) * 8; }
      PTR(sa, 2, p.Wq + h * 128); PTR(sa, 4, p.Wk + h * 128); PTR(sa, 6, p.Wo + (size_t)h * 128 * DM + half * 256);
      sb[0] = chunk(0, gam, 0, 0); sb[1] = (C_KVP) + h * 256; sb[2] = chunk(0, gam, 16 + ((h & 7) >> 1), (h & 1) * 2048);
      sb[3] = KVH + h * 65536 + (wave >> 1) * 16384 + (wave & 1) * 4096; sb[6] = (C_HF) + h * 256; sb[7] = C_REP; sb[8] = 1024 - 512 * half; sb[9] = WO_KM + h * 2048 + wave * 512;
      sb[11] = __float_as_uint(QS); sb[12] = flip * 64; sb[13] = gam * 128; sb[14] = 2500; sb[15] = 720;
      UNIFORM(sa, 8); UNIFORM(sb, 16);
      asm volatile(
  "s_nop 4\n"
  "s_lshl_b32 s80, s48, 10\n"
  "s_lshl_b32 s78, s48, 11\n"
  "s_and_b32 s79, s80, 1024\n"
  "s_and_b32 s74, s78, 4096\n"
  "s_add_u32 s79, s79, s74\n"
  "s_add_u32 m0, s79, 0\n"
  "s_add_u32 s74, s40, 0\n"
  "s_addc_u32 s75, s41, 0\n"
  "global_load_lds_dwordx4 v12, s[74:75]\n"
  "s_add_u32 m0, s79, 2048\n"
  "s_add_u32 s74, s40, 65536\n"
  "s_addc_u32 s75, s41, 0\n"
  "global_load_lds_dwordx4 v12, s[74:75]\n"
  "s_add_u32 m0, s79, 8192\n"
  "s_add_u32 s74, s40, 262144\n"
  "s_addc_u32 s75, s41, 0\n"
  "global_load_lds_dwordx4 v12, s[74:75]\n"
  "s_add_u32 m0, s79, 10240\n"
  "s_add_u32 s74, s40, 327680\n"
  "s_addc_u32 s75, s41, 0\n"
  "global_load_lds_dwordx4 v12, s[74:75]\n"
  "s_add_u32 m0, s79, 16384\n"
  "s_add_u32 s74, s40, 524288\n"
  "s_addc_u32 s75, s41, 0\n"
  "global_load_lds_dwordx4 v12, s[74:75]\n"
  "s_add_u32 m0, s79, 18432\n"
  "s_add_u32 s74, s40, 589824\n"
  "s_addc_u32 s75, s41, 0\n"
  "global_load_lds_dwordx4 v12, s[74:75]\n"
  "s_add_u32 m0, s79, 24576\n"
  "s_add_u32 s74, s40, 786432\n"
  "s_addc_u32 s75, s41, 0\n"
  "global_load_lds_dwordx4 v12, s[74:75]\n"
  "s_add_u32 m0, s79, 26624\n"
  "s_add_u32 s74, s40, 851968\n"
  "s_addc_u32 s75, s41, 0\n"
  "global_load_lds_dwordx4 v12, s[74:75]\n"
  "s_add_u32 m0, s79, 32768\n"
  "s_add_u32 s74, s40, 1048576\n"
  "s_addc_u32 s75, s41, 0\n"
  "global_load_lds_dwordx4 v12, s[74:75]\n"
  "s_add_u32 m0, s79, 34816\n"
  "s_add_u32 s74, s40, 1114112\n"
  "s_addc_u32 s75, s41, 0\n"
  "global_load_lds_dwordx4 v12, s[74:75]\n"
  "s_add_u32 m0, s79, 40960\n"
  "s_add_u32 s74, s40, 1310720\n"
  "s_addc_u32 s75, s41, 0\n"
  "global_load_lds_dwordx4 v12, s[74:75]\n"
  "s_add_u32 m0, s79, 43008\n"
  "s_add_u32 s74, s40, 1376256\n"
  "s_addc_u32 s75, s41, 0\n"
  "global_load_lds_dwordx4 v12, s[74:75]\n"
  "s_add_u32 m0, s79, 49152\n"
  "s_add_u32 s74, s40, 1572864\n"
  "s_addc_u32 s75, s41, 0\n"
  "global_load_lds_dwordx4 v12, s[74:75]\n"
  "s_add_u32 m0, s79, 51200\n"
  "s_add_u32 s74, s40, 1638400\n"
  "s_addc_u32 s75, s41, 0\n"
  "global_load_lds_dwordx4 v12, s[74:75]\n"
  "s_add_u32 m0, s79, 57344\n"
  "s_add_u32 s74, s40, 1835008\n"
  "s_addc_u32 s75, s41, 0\n"
  "global_load_lds_dwordx4 v12, s[74:75]\n"
  "s_add_u32 m0, s79, 59392\n"
  "s_add_u32 s74, s40, 1900544\n"
  "s_addc_u32 s75, s41, 0\n"
  "global_load_lds_dwordx4 v12, s[74:75]\n"
  "s_add_u32 m0, s79, 65536\n"
  "s_add_u32 s74, s40, 2097152\n"
  "s_addc_u32 s75, s41, 0\n"
  "global_load_lds_dwordx4 v12, s[74:75]\n"
  "s_add_u32 m0, s79, 67584\n"
  "s_add_u32 s74, s40, 2162688\n"
  "s_addc_u32 s75, s41, 0\n"
  "global_load_lds_dwordx4 v12, s[74:75]\n"
  "s_add_u32 m0, s79, 73728\n"
  "s_add_u32 s74, s40, 2359296\n"
  "s_addc_u32 s75, s41, 0\n"
  "global_load_lds_dwordx4 v12, s[74:75]\n"
  "s_add_u32 m0, s79, 75776\n"
  "s_add_u32 s74, s40, 2424832\n"
  "s_addc_u32 s75, s41, 0\n"
  "global_load_lds_dwordx4 v12, s[74:75]\n"
  "s_add_u32 m0, s79, 81920\n"
  "s_add_u32 s74, s40, 2621440\n"
  "s_addc_u32 s75, s41, 0\n"
  "global_load_lds_dwordx4 v12, s[74:75]\n"
  "s_add_u32 m0, s79, 83968\n"
  "s_add_u32 s74, s40, 2686976\n"
  "s_addc_u32 s75, s41, 0\n"
  "global_load_lds_dwordx4 v12, s[74:75]\n"
  "s_add_u32 m0, s79, 90112\n"
  "s_add_u32 s74, s40, 2883584\n"
  "s_addc_u32 s75, s41, 0\n"
  "global_load_lds_dwordx4 v12, s[74:75]\n"
  "s_add_u32 m0, s79, 92160\n"
  "s_add_u32 s74, s40, 2949120\n"
  "s_addc_u32 s75, s41, 0\n"
  "global_load_lds_dwordx4 v12, s[74:75]\n"
  "s_add_u32 m0, s79, 98304\n"
  "s_add_u32 s74, s40, 3145728\n"
  "s_addc_u32 s75, s41, 0\n"
  "global_load_lds_dwordx4 v12, s[74:75]\n"
  "s_add_u32 m0, s79, 100352\n"
  "s_add_u32 s74, s40, 3211264\n"
  "s_addc_u32 s75, s41, 0\n"
  "global_load_lds_dwordx4 v12, s[74:75]\n"
  "s_add_u32 m0, s79, 106496\n"
  "s_add_u32 s74, s40, 3407872\n"
  "s_addc_u32 s75, s41, 0\n"
  "global_load_lds_dwordx4 v12, s[74:75]\n"
  "s_add_u32 m0, s79, 108544\n"
  "s_add_u32 s74, s40, 3473408\n"
  "s_addc_u32 s75, s41, 0\n"
  "global_load_lds_dwordx4 v12, s[74:75]\n"
  "s_add_u32 m0, s79, 114688\n"
  "s_add_u32 s74, s40, 3670016\n"
  "s_addc_u32 s75, s41, 0\n"
  "global_load_lds_dwordx4 v12, s[74:75]\n"
  "s_add_u32 m0, s79, 116736\n"
  "s_add_u32 s74, s40, 3735552\n"
  "s_addc_u32 s75, s41, 0\n"
  "global_load_lds_dwordx4 v12, s[74:75]\n"
  "s_add_u32 m0, s79, 122880\n"
  "s_add_u32 s74, s40, 3932160\n"
  "s_addc_u32 s75, s41, 0\n"
  "global_load_lds_dwordx4 v12, s[74:75]\n"
  "s_add_u32 m0, s79, 124928\n"
  "s_add_u32 s74, s40, 3997696\n"
  "s_addc_u32 s75, s41, 0\n"
  "global_load_lds_dwordx4 v12, s[74:75]\n"
  "s_waitcnt vmcnt(28) lgkmcnt(0)\n"
  "s_barrier\n"
  "s_add_u32 m0, s79, 131072\n"
  "s_add_u32 s74, s40, 4194304\n"
  "s_addc_u32 s75, s41, 0\n"
  "global_load_lds_dwordx4 v12, s[74:75]\n"
  "s_add_u32 m0, s79, 133120\n"
  "s_add_u32 s74, s40, 4259840\n"
  "s_addc_u32 s75, s41, 0\n"
  "global_load_lds_dwordx4 v12, s[74:75]\n"
  "s_add_u32 m0, s79, 139264\n"
  "s_add_u32 s74, s40, 4456448\n"
  "s_addc_u32 s75, s41, 0\n"
  "global_load_lds_dwordx4 v12, s[74:75]\n"
  "s_add_u32 m0, s79, 141312\n"
  "s_add_u32 s74, s40, 4521984\n"
  "s_addc_u32 s75, s41, 0\n"
  "global_load_lds_dwordx4 v12, s[74:75]\n"
  "v_add_u32 v20, 0, v13\n"
  "v_xor_b32 v21, 32, v20\n"
  "ds_read_b64_tr_b16 a[0:1], v20 offset:0\n"
  "ds_read_b64_tr_b16 a[2:3], v20 offset:2048\n"
  "ds_read_b64_tr_b16 a[4:5], v20 offset:8192\n"
  "ds_read_b64_tr_b16 a[6:7], v20 offset:10240\n"
  "ds_read_b64_tr_b16 a[8:9], v21 offset:0\n"
  "ds_read_b64_tr_b16 a[10:11], v21 offset:2048\n"
  "ds_read_b64_tr_b16 a[12:13], v21 offset:8192\n"
  "ds_read_b64_tr_b16 a[14:15], v21 offset:10240\n"
  "s_waitcnt vmcnt(28) lgkmcnt(0)\n"
  "s_barrier\n"
  "s_add_u32 m0, s78, 0\n"
  "s_add_u32 s74, s42, 0\n"
  "s_addc_u32 s75, s43, 0\n"
  "global_load_lds_dwordx4 v14, s[74:75]\n"
  "s_add_u32 m0, s78, 1024\n"
  "s_add_u32 s74, s42, 9216\n"
  "s_addc_u32 s75, s43, 0\n"
  "global_load_lds_dwordx4 v14, s[74:75]\n"
  "s_add_u32 m0, s78, 8192\n"
  "s_add_u32 s74, s42, 73728\n"
  "s_addc_u32 s75, s43, 0\n"
  "global_load_lds_dwordx4 v14, s[74:75]\n"
  "s_add_u32 m0, s78, 9216\n"
  "s_add_u32 s74, s42, 82944\n"
  "s_addc_u32 s75, s43, 0\n"
  "global_load_lds_dwordx4 v14, s[74:75]\n"
  "v_add_u32 v20, 16384, v13\n"
  "v_xor_b32 v21, 32, v20\n"
  "ds_read_b64_tr_b16 a[16:17], v20 offset:0\n"
  "ds_read_b64_tr_b16 a[18:19], v20 offset:2048\n"
  "ds_read_b64_tr_b16 a[20:21], v20 offset:8192\n"
  "ds_read_b64_tr_b16 a[22:23], v20 offset:10240\n"
  "ds_read_b64_tr_b16 a[24:25], v21 offset:0\n"
  "ds_read_b64_tr_b16 a[26:27], v21 offset:2048\n"
  "ds_read_b64_tr_b16 a[28:29], v21 offset:8192\n"
  "ds_read_b64_tr_b16 a[30:31], v21 offset:10240\n"
  "s_waitcnt vmcnt(28) lgkmcnt(0)\n"
  "s_barrier\n"
  "s_add_u32 m0, s78, 16384\n"
  "s_add_u32 s74, s42, 128\n"
  "s_addc_u32 s75, s43, 0\n"
  "global_load_lds_dwordx4 v14, s[74:75]\n"
  "s_add_u32 m0, s78, 17408\n"
  "s_add_u32 s74, s42, 9344\n"
  "s_addc_u32 s75, s43, 0\n"
  "global_load_lds_dwordx4 v14, s[74:75]\n"
  "s_add_u32 m0, s78, 24576\n"
  "s_add_u32 s74, s42, 73856\n"
  "s_addc_u32 s75, s43, 0\n"
  "global_load_lds_dwordx4 v14, s[74:75]\n"
  "s_add_u32 m0, s78, 25600\n"
  "s_add_u32 s74, s42, 83072\n"
  "s_addc_u32 s75, s43, 0\n"
  "global_load_lds_dwordx4 v14, s[74:75]\n"
  "v_add_u32 v20, 32768, v13\n"
  "v_xor_b32 v21, 32, v20\n"
  "ds_read_b64_tr_b16 a[32:33], v20 offset:0\n"
  "ds_read_b64_tr_b16 a[34:35], v20 offset:2048\n"
  "ds_read_b64_tr_b16 a[36:37], v20 offset:8192\n"
  "ds_read_b64_tr_b16 a[38:39], v20 offset:10240\n"
  "ds_read_b64_tr_b16 a[40:41], v21 offset:0\n"
  "ds_read_b64_tr_b16 a[42:43], v21 offset:2048\n"
  "ds_read_b64_tr_b16 a[44:45], v21 offset:8192\n"
  "ds_read_b64_tr_b16 a[46:47], v21 offset:10240\n"
  "s_waitcnt vmcnt(28) lgkmcnt(0)\n"
  "s_barrier\n"
  "s_add_u32 m0, s78, 32768\n"
  "s_add_u32 s74, s42, 256\n"
  "s_addc_u32 s75, s43, 0\n"
  "global_load_lds_dwordx4 v14, s[74:75]\n"
  "s_add_u32 m0, s78, 33792\n"
  "s_add_u32 s74, s42, 9472\n"
  "s_addc_u32 s75, s43, 0\n"
  "global_load_lds_dwordx4 v14, s[74:75]\n"
  "s_add_u32 m0, s78, 40960\n"
  "s_add_u32 s74, s42, 73984\n"
  "s_addc_u32 s75, s43, 0\n"
  "global_load_lds_dwordx4 v14, s[74:75]\n"
  "s_add_u32 m0, s78, 41984\n"
  "s_add_u32 s74, s42, 83200\n"
  "s_addc_u32 s75, s43, 0\n"
  "global_load_lds_dwordx4 v14, s[74:75]\n"
  "v_add_u32 v20, 49152, v13\n"
  "v_xor_b32 v21, 32, v20\n"
  "ds_read_b64_tr_b16 a[48:49], v20 offset:0\n"
  "ds_read_b64_tr_b16 a[50:51], v20 offset:2048\n"
  "ds_read_b64_tr_b16 a[52:53], v20 offset:8192\n"
  "ds_read_b64_tr_b16 a[54:55], v20 offset:10240\n"
  "ds_read_b64_tr_b16 a[56:57], v21 offset:0\n"
  "ds_read_b64_tr_b16 a[58:59], v21 offset:2048\n"
  "ds_read_b64_tr_b16 a[60:61], v21 offset:8192\n"
  "ds_read_b64_tr_b16 a[62:63], v21 offset:10240\n"
  "s_waitcnt vmcnt(28) lgkmcnt(0)\n"
  "s_barrier\n"
  "s_add_u32 m0, s78, 49152\n"
  "s_add_u32 s74, s42, 384\n"
  "s_addc_u32 s75, s43, 0\n"
  "global_load_lds_dwordx4 v14, s[74:75]\n"
  "s_add_u32 m0, s78, 50176\n"
  "s_add_u32 s74, s42, 9600\n"
  "s_addc_u32 s75, s43, 0\n"
  "global_load_lds_dwordx4 v14, s[74:75]\n"
  "s_add_u32 m0, s78, 57344\n"
  "s_add_u32 s74, s42, 74112\n"
  "s_addc_u32 s75, s43, 0\n"
  "global_load_lds_dwordx4 v14, s[74:75]\n"
  "s_add_u32 m0, s78, 58368\n"
  "s_add_u32 s74, s42, 83328\n"
  "s_addc_u32 s75, s43, 0\n"
  "global_load_lds_dwordx4 v14, s[74:75]\n"
  "v_add_u32 v20, 65536, v13\n"
  "v_xor_b32 v21, 32, v20\n"
  "ds_read_b64_tr_b16 a[64:65], v20 offset:0\n"
  "ds_read_b64_tr_b16 a[66:67], v20 offset:2048\n"
  "ds_read_b64_tr_b16 a[68:69], v20 offset:8192\n"
  "ds_read_b64_tr_b16 a[70:71], v20 offset:10240\n"
  "ds_read_b64_tr_b16 a[72:73], v21 offset:0\n"
  "ds_read_b64_tr_b16 a[74:75], v21 offset:2048\n"
  "ds_read_b64_tr_b16 a[76:77], v21 offset:8192\n"
  "ds_read_b64_tr_b16 a[78:79], v21 offset:10240\n"
  "s_waitcnt vmcnt(28) lgkmcnt(0)\n"
  "s_barrier\n"
  "s_add_u32 m0, s78, 65536\n"
  "s_add_u32 s74, s42, 0\n"
  "s_addc_u32 s75, s43, 0\n"
  "s_add_u32 s74, s74, s52\n"
  "s_addc_u32 s75, s75, 0\n"
  "global_load_lds_dwordx4 v14, s[74:75]\n"
  "s_add_u32 m0, s78, 66560\n"
  "s_add_u32 s74, s42, 9216\n"
  "s_addc_u32 s75, s43, 0\n"
  "s_add_u32 s74, s74, s52\n"
  "s_addc_u32 s75, s75, 0\n"
  "global_load_lds_dwordx4 v14, s[74:75]\n"
  "s_add_u32 m0, s78, 73728\n"
  "s_add_u32 s74, s42, 73728\n"
  "s_addc_u32 s75, s43, 0\n"
  "s_add_u32 s74, s74, s52\n"
  "s_addc_u32 s75, s75, 0\n"
  "global_load_lds_dwordx4 v14, s[74:75]\n"
  "s_add_u32 m0, s78, 74752\n"
  "s_add_u32 s74, s42, 82944\n"
  "s_addc_u32 s75, s43, 0\n"
  "s_add_u32 s74, s74, s52\n"
  "s_addc_u32 s75, s75, 0\n"
  "global_load_lds_dwordx4 v14, s[74:75]\n"
  "v_add_u32 v20, 81920, v13\n"
  "v_xor_b32 v21, 32, v20\n"
  "ds_read_b64_tr_b16 a[80:81], v20 offset:0\n"
  "ds_read_b64_tr_b16 a[82:83], v20 offset:2048\n"
  "ds_read_b64_tr_b16 a[84:85], v20 offset:8192\n"
  "ds_read_b64_tr_b16 a[86:87], v20 offset:10240\n"
  "ds_read_b64_tr_b16 a[88:89], v21 offset:0\n"
  "ds_read_b64_tr_b16 a[90:91], v21 offset:2048\n"
  "ds_read_b64_tr_b16 a[92:93], v21 offset:8192\n"
  "ds_read_b64_tr_b16 a[94:95], v21 offset:10240\n"
  "s_waitcnt vmcnt(28) lgkmcnt(0)\n"
  "s_barrier\n"
  "s_add_u32 m0, s79, 81920\n"
  "s_add_u32 s74, s38, 0\n"
  "s_addc_u32 s75, s39, 0\n"
  "global_load_lds_dwordx4 v12, s[74:75]\n"
  "s_add_u32 m0, s79, 83968\n"
  "s_add_u32 s74, s38, 65536\n"
  "s_addc_u32 s75, s39, 0\n"
  "global_load_lds_dwordx4 v12, s[74:75]\n"
  "s_add_u32 m0, s79, 90112\n"
  "s_add_u32 s74, s38, 262144\n"
  "s_addc_u32 s75, s39, 0\n"
  "global_load_lds_dwordx4 v12, s[74:75]\n"
  "s_add_u32 m0, s79, 92160\n"
  "s_add_u32 s74, s38, 327680\n"
  "s_addc_u32 s75, s39, 0\n"
  "global_load_lds_dwordx4 v12, s[74:75]\n"
  "v_add_u32 v20, 98304, v13\n"
  "v_xor_b32 v21, 32, v20\n"
  "ds_read_b64_tr_b16 a[96:97], v20 offset:0\n"
  "ds_read_b64_tr_b16 a[98:99], v20 offset:2048\n"
  "ds_read_b64_tr_b16 a[100:101], v20 offset:8192\n"
  "ds_read_b64_tr_b16 a[102:103], v20 offset:10240\n"
  "ds_read_b64_tr_b16 a[104:105], v21 offset:0\n"
  "ds_read_b64_tr_b16 a[106:107], v21 offset:2048\n"
  "ds_read_b64_tr_b16 a[108:109], v21 offset:8192\n"
  "ds_read_b64_tr_b16 a[110:111], v21 offset:10240\n"
  "s_waitcnt vmcnt(28) lgkmcnt(0)\n"
  "s_barrier\n"
  "s_add_u32 m0, s79, 98304\n"
  "s_add_u32 s74, s38, 524288\n"
  "s_addc_u32 s75, s39, 0\n"
  "global_load_lds_dwordx4 v12, s[74:75]\n"
  "s_add_u32 m0, s79, 100352\n"
  "s_add_u32 s74, s38, 589824\n"
  "s_addc_u32 s75, s39, 0\n"
  "global_load_lds_dwordx4 v12, s[74:75]\n"
  "s_add_u32 m0, s79, 106496\n"
  "s_add_u32 s74, s38, 786432\n"
  "s_addc_u32 s75, s39, 0\n"
  "global_load_lds_dwordx4 v12, s[74:75]\n"
  "s_add_u32 m0, s79, 108544\n"
  "s_add_u32 s74, s38, 851968\n"
  "s_addc_u32 s75, s39, 0\n"
  "global_load_lds_dwordx4 v12, s[74:75]\n"
  "v_add_u32 v20, 114688, v13\n"
  "v_xor_b32 v21, 32, v20\n"
  "ds_read_b64_tr_b16 a[112:113], v20 offset:0\n"
  "ds_read_b64_tr_b16 a[114:115], v20 offset:2048\n"
  "ds_read_b64_tr_b16 a[116:117], v20 offset:8192\n"
  "ds_read_b64_tr_b16 a[118:119], v20 offset:10240\n"
  "ds_read_b64_tr_b16 a[120:121], v21 offset:0\n"
  "ds_read_b64_tr_b16 a[122:123], v21 offset:2048\n"
  "ds_read_b64_tr_b16 a[124:125], v21 offset:8192\n"
  "ds_read_b64_tr_b16 a[126:127], v21 offset:10240\n"
  "s_waitcnt vmcnt(28) lgkmcnt(0)\n"
  "s_barrier\n"
  "s_add_u32 m0, s79, 114688\n"
  "s_add_u32 s74, s38, 1048576\n"
  "s_addc_u32 s75, s39, 0\n"
  "global_load_lds_dwordx4 v12, s[74:75]\n"
  "s_add_u32 m0, s79, 116736\n"
  "s_add_u32 s74, s38, 1114112\n"
  "s_addc_u32 s75, s39, 0\n"
  "global_load_lds_dwordx4 v12, s[74:75]\n"
  "s_add_u32 m0, s79, 122880\n"
  "s_add_u32 s74, s38, 1310720\n"
  "s_addc_u32 s75, s39, 0\n"
  "global_load_lds_dwordx4 v12, s[74:75]\n"
  "s_add_u32 m0, s79, 124928\n"
  "s_add_u32 s74, s38, 1376256\n"
  "s_addc_u32 s75, s39, 0\n"
  "global_load_lds_dwordx4 v12, s[74:75]\n"
  "v_add_u32 v20, 131072, v13\n"
  "v_xor_b32 v21, 32, v20\n"
  "ds_read_b64_tr_b16 a[128:129], v20 offset:0\n"
  "ds_read_b64_tr_b16 a[130:131], v20 offset:2048\n"
  "ds_read_b64_tr_b16 a[132:133], v20 offset:8192\n"
  "ds_read_b64_tr_b16 a[134:135], v20 offset:10240\n"
  "ds_read_b64_tr_b16 a[136:137], v21 offset:0\n"
  "ds_read_b64_tr_b16 a[138:139], v21 offset:2048\n"
  "ds_read_b64_tr_b16 a[140:141], v21 offset:8192\n"
  "ds_read_b64_tr_b16 a[142:143], v21 offset:10240\n"
  "s_waitcnt vmcnt(28) lgkmcnt(0)\n"
  "s_barrier\n"
  "s_add_u32 m0, s79, 131072\n"
  "s_add_u32 s74, s38, 1572864\n"
  "s_addc_u32 s75, s39, 0\n"
  "global_load_lds_dwordx4 v12, s[74:75]\n"
  "s_add_u32 m0, s79, 133120\n"
  "s_add_u32 s74, s38, 1638400\n"
  "s_addc_u32 s75, s39, 0\n"
  "global_load_lds_dwordx4 v12, s[74:75]\n"
  "s_add_u32 m0, s79, 139264\n"
  "s_add_u32 s74, s38, 1835008\n"
  "s_addc_u32 s75, s39, 0\n"
  "global_load_lds_dwordx4 v12, s[74:75]\n"
  "s_add_u32 m0, s79, 141312\n"
  "s_add_u32 s74, s38, 1900544\n"
  "s_addc_u32 s75, s39, 0\n"
  "global_load_lds_dwordx4 v12, s[74:75]\n"
  "s_cmp_lg_u32 s48, 0\n"
  "s_cbranch_scc1 HLS9\n"
  "s_xor_b32 s74, s56, 0\n"
  "v_xor_b32 v20, s74, v15\n"
  "v_add_u32 v20, 0, v20\n"
  "ds_read_b64_tr_b16 a[144:145], v20 offset:0\n"
  "ds_read_b64_tr_b16 a[146:147], v20 offset:256\n"
  "ds_read_b64_tr_b16 a[148:149], v20 offset:1024\n"
  "ds_read_b64_tr_b16 a[150:151], v20 offset:1280\n"
  "ds_read_b64_tr_b16 a[152:153], v20 offset:8192\n"
  "ds_read_b64_tr_b16 a[154:155], v20 offset:8448\n"
  "ds_read_b64_tr_b16 a[156:157], v20 offset:9216\n"
  "ds_read_b64_tr_b16 a[158:159], v20 offset:9472\n"
  "s_xor_b32 s74, s56, 32\n"
  "v_xor_b32 v20, s74, v15\n"
  "v_add_u32 v20, 0, v20\n"
  "ds_read_b64_tr_b16 a[160:161], v20 offset:0\n"
  "ds_read_b64_tr_b16 a[162:163], v20 offset:256\n"
  "ds_read_b64_tr_b16 a[164:165], v20 offset:1024\n"
  "ds_read_b64_tr_b16 a[166:167], v20 offset:1280\n"
  "ds_read_b64_tr_b16 a[168:169], v20 offset:8192\n"
  "ds_read_b64_tr_b16 a[170:171], v20 offset:8448\n"
  "ds_read_b64_tr_b16 a[172:173], v20 offset:9216\n"
  "ds_read_b64_tr_b16 a[174:175], v20 offset:9472\n"
  "s_xor_b32 s74, s56, 64\n"
  "v_xor_b32 v20, s74, v15\n"
  "v_add_u32 v20, 0, v20\n"
  "ds_read_b64_tr_b16 a[176:177], v20 offset:0\n"
  "ds_read_b64_tr_b16 a[178:179], v20 offset:256\n"
  "ds_read_b64_tr_b16 a[180:181], v20 offset:1024\n"
  "ds_read_b64_tr_b16 a[182:183], v20 offset:1280\n"
  "ds_read_b64_tr_b16 a[184:185], v20 offset:8192\n"
  "ds_read_b64_tr_b16 a[186:187], v20 offset:8448\n"
  "ds_read_b64_tr_b16 a[188:189], v20 offset:9216\n"
  "ds_read_b64_tr_b16 a[190:191], v20 offset:9472\n"
  "s_xor_b32 s74, s56, 96\n"
  "v_xor_b32 v20, s74, v15\n"
  "v_add_u32 v20, 0, v20\n"
  "ds_read_b64_tr_b16 a[192:193], v20 offset:0\n"
  "ds_read_b64_tr_b16 a[194:195], v20 offset:256\n"
  "ds_read_b64_tr_b16 a[196:197], v20 offset:1024\n"
  "ds_read_b64_tr_b16 a[198:199], v20 offset:1280\n"
  "ds_read_b64_tr_b16 a[200:201], v20 offset:8192\n"
  "ds_read_b64_tr_b16 a[202:203], v20 offset:8448\n"
  "ds_read_b64_tr_b16 a[204:205], v20 offset:9216\n"
  "ds_read_b64_tr_b16 a[206:207], v20 offset:9472\n"
  "HLS9:\n"
  "s_waitcnt vmcnt(28) lgkmcnt(0)\n"
  "s_barrier\n"
  "s_add_u32 m0, s79, 0\n"
  "s_add_u32 s74, s38, 2097152\n"
  "s_addc_u32 s75, s39, 0\n"
  "global_load_lds_dwordx4 v12, s[74:75]\n"
  "s_add_u32 m0, s79, 2048\n"
  "s_add_u32 s74, s38, 2162688\n"
  "s_addc_u32 s75, s39, 0\n"
  "global_load_lds_dwordx4 v12, s[74:75]\n"
  "s_add_u32 m0, s79, 8192\n"
  "s_add_u32 s74, s38, 2359296\n"
  "s_addc_u32 s75, s39, 0\n"
  "global_load_lds_dwordx4 v12, s[74:75]\n"
  "s_add_u32 m0, s79, 10240\n"
  "s_add_u32 s74, s38, 2424832\n"
  "s_addc_u32 s75, s39, 0\n"
  "global_load_lds_dwordx4 v12, s[74:75]\n"
  "s_cmp_lg_u32 s48, 1\n"
  "s_cbranch_scc1 HLS10\n"
  "s_xor_b32 s74, s56, 0\n"
  "v_xor_b32 v20, s74, v15\n"
  "v_add_u32 v20, 16384, v20\n"
  "ds_read_b64_tr_b16 a[144:145], v20 offset:0\n"
  "ds_read_b64_tr_b16 a[146:147], v20 offset:256\n"
  "ds_read_b64_tr_b16 a[148:149], v20 offset:1024\n"
  "ds_read_b64_tr_b16 a[150:151], v20 offset:1280\n"
  "ds_read_b64_tr_b16 a[152:153], v20 offset:8192\n"
  "ds_read_b64_tr_b16 a[154:155], v20 offset:8448\n"
  "ds_read_b64_tr_b16 a[156:157], v20 offset:9216\n"
  "ds_read_b64_tr_b16 a[158:159], v20 offset:9472\n"
  "s_xor_b32 s74, s56, 32\n"
  "v_xor_b32 v20, s74, v15\n"
  "v_add_u32 v20, 16384, v20\n"
  "ds_read_b64_tr_b16 a[160:161], v20 offset:0\n"
  "ds_read_b64_tr_b16 a[162:163], v20 offset:256\n"
  "ds_read_b64_tr_b16 a[164:165], v20 offset:1024\n"
  "ds_read_b64_tr_b16 a[166:167], v20 offset:1280\n"
  "ds_read_b64_tr_b16 a[168:169], v20 offset:8192\n"
  "ds_read_b64_tr_b16 a[170:171], v20 offset:8448\n"
  "ds_read_b64_tr_b16 a[172:173], v20 offset:9216\n"
  "ds_read_b64_tr_b16 a[174:175], v20 offset:9472\n"
  "s_xor_b32 s74, s56, 64\n"
  "v_xor_b32 v20, s74, v15\n"
  "v_add_u32 v20, 16384, v20\n"
  "ds_read_b64_tr_b16 a[176:177], v20 offset:0\n"
  "ds_read_b64_tr_b16 a[178:179], v20 offset:256\n"
  "ds_read_b64_tr_b16 a[180:181], v20 offset:1024\n"
  "ds_read_b64_tr_b16 a[182:183], v20 offset:1280\n"
  "ds_read_b64_tr_b16 a[184:185], v20 offset:8192\n"
  "ds_read_b64_tr_b16 a[186:187], v20 offset:8448\n"
  "ds_read_b64_tr_b16 a[188:189], v20 offset:9216\n"
  "ds_read_b64_tr_b16 a[190:191], v20 offset:9472\n"
  "s_xor_b32 s74, s56, 96\n"
  "v_xor_b32 v20, s74, v15\n"
  "v_add_u32 v20, 16384, v20\n"
  "ds_read_b64_tr_b16 a[192:193], v20 offset:0\n"
  "ds_read_b64_tr_b16 a[194:195], v20 offset:256\n"
  "ds_read_b64_tr_b16 a[196:197], v20 offset:1024\n"
  "ds_read_b64_tr_b16 a[198:199], v20 offset:1280\n"
  "ds_read_b64_tr_b16 a[200:201], v20 offset:8192\n"
  "ds_read_b64_tr_b16 a[202:203], v20 offset:8448\n"
  "ds_read_b64_tr_b16 a[204:205], v20 offset:9216\n"
  "ds_read_b64_tr_b16 a[206:207], v20 offset:9472\n"
  "HLS10:\n"
  "s_waitcnt vmcnt(28) lgkmcnt(0)\n"
  "s_barrier\n"
  "s_add_u32 m0, s79, 16384\n"
  "s_add_u32 s74, s38, 2621440\n"
  "s_addc_u32 s75, s39, 0\n"
  "global_load_lds_dwordx4 v12, s[74:75]\n"
  "s_add_u32 m0, s79, 18432\n"
  "s_add_u32 s74, s38, 2686976\n"
  "s_addc_u32 s75, s39, 0\n"
  "global_load_lds_dwordx4 v12, s[74:75]\n"
  "s_add_u32 m0, s79, 24576\n"
  "s_add_u32 s74, s38, 2883584\n"
  "s_addc_u32 s75, s39, 0\n"
  "global_load_lds_dwordx4 v12, s[74:75]\n"
  "s_add_u32 m0, s79, 26624\n"
  "s_add_u32 s74, s38, 2949120\n"
  "s_addc_u32 s75, s39, 0\n"
  "global_load_lds_dwordx4 v12, s[74:75]\n"
  "s_cmp_lg_u32 s48, 2\n"
  "s_cbranch_scc1 HLS11\n"
  "s_xor_b32 s74, s56, 0\n"
  "v_xor_b32 v20, s74, v15\n"
  "v_add_u32 v20, 32768, v20\n"
  "ds_read_b64_tr_b16 a[144:145], v20 offset:0\n"
  "ds_read_b64_tr_b16 a[146:147], v20 offset:256\n"
  "ds_read_b64_tr_b16 a[148:149], v20 offset:1024\n"
  "ds_read_b64_tr_b16 a[150:151], v20 offset:1280\n"
  "ds_read_b64_tr_b16 a[152:153], v20 offset:8192\n"
  "ds_read_b64_tr_b16 a[154:155], v20 offset:8448\n"
  "ds_read_b64_tr_b16 a[156:157], v20 offset:9216\n"
  "ds_read_b64_tr_b16 a[158:159], v20 offset:9472\n"
  "s_xor_b32 s74, s56, 32\n"
  "v_xor_b32 v20, s74, v15\n"
  "v_add_u32 v20, 32768, v20\n"
  "ds_read_b64_tr_b16 a[160:161], v20 offset:0\n"
  "ds_read_b64_tr_b16 a[162:163], v20 offset:256\n"
  "ds_read_b64_tr_b16 a[164:165], v20 offset:1024\n"
  "ds_read_b64_tr_b16 a[166:167], v20 offset:1280\n"
  "ds_read_b64_tr_b16 a[168:169], v20 offset:8192\n"
  "ds_read_b64_tr_b16 a[170:171], v20 offset:8448\n"
  "ds_read_b64_tr_b16 a[172:173], v20 offset:9216\n"
  "ds_read_b64_tr_b16 a[174:175], v20 offset:9472\n"
  "s_xor_b32 s74, s56, 64\n"
  "v_xor_b32 v20, s74, v15\n"
  "v_add_u32 v20, 32768, v20\n"
  "ds_read_b64_tr_b16 a[176:177], v20 offset:0\n"
  "ds_read_b64_tr_b16 a[178:179], v20 offset:256\n"
  "ds_read_b64_tr_b16 a[180:181], v20 offset:1024\n"
  "ds_read_b64_tr_b16 a[182:183], v20 offset:1280\n"
  "ds_read_b64_tr_b16 a[184:185], v20 offset:8192\n"
  "ds_read_b64_tr_b16 a[186:187], v20 offset:8448\n"
  "ds_read_b64_tr_b16 a[188:189], v20 offset:9216\n"
  "ds_read_b64_tr_b16 a[190:191], v20 offset:9472\n"
  "s_xor_b32 s74, s56, 96\n"
  "v_xor_b32 v20, s74, v15\n"
  "v_add_u32 v20, 32768, v20\n"
  "ds_read_b64_tr_b16 a[192:193], v20 offset:0\n"
  "ds_read_b64_tr_b16 a[194:195], v20 offset:256\n"
  "ds_read_b64_tr_b16 a[196:197], v20 offset:1024\n"
  "ds_read_b64_tr_b16 a[198:199], v20 offset:1280\n"
  "ds_read_b64_tr_b16 a[200:201], v20 offset:8192\n"
  "ds_read_b64_tr_b16 a[202:203], v20 offset:8448\n"
  "ds_read_b64_tr_b16 a[204:205], v20 offset:9216\n"
  "ds_read_b64_tr_b16 a[206:207], v20 offset:9472\n"
  "HLS11:\n"
  "s_waitcnt vmcnt(28) lgkmcnt(0)\n"
  "s_barrier\n"
  "s_add_u32 m0, s79, 32768\n"
  "s_add_u32 s74, s38, 3145728\n"
  "s_addc_u32 s75, s39, 0\n"
  "global_load_lds_dwordx4 v12, s[74:75]\n"
  "s_add_u32 m0, s79, 34816\n"
  "s_add_u32 s74, s38, 3211264\n"
  "s_addc_u32 s75, s39, 0\n"
  "global_load_lds_dwordx4 v12, s[74:75]\n"
  "s_add_u32 m0, s79, 40960\n"
  "s_add_u32 s74, s38, 3407872\n"
  "s_addc_u32 s75, s39, 0\n"
  "global_load_lds_dwordx4 v12, s[74:75]\n"
  "s_add_u32 m0, s79, 43008\n"
  "s_add_u32 s74, s38, 3473408\n"
  "s_addc_u32 s75, s39, 0\n"
  "global_load_lds_dwordx4 v12, s[74:75]\n"
  "s_cmp_lg_u32 s48, 3\n"
  "s_cbranch_scc1 HLS12\n"
  "s_xor_b32 s74, s56, 0\n"
  "v_xor_b32 v20, s74, v15\n"
  "v_add_u32 v20, 49152, v20\n"
  "ds_read_b64_tr_b16 a[144:145], v20 offset:0\n"
  "ds_read_b64_tr_b16 a[146:147], v20 offset:256\n"
  "ds_read_b64_tr_b16 a[148:149], v20 offset:1024\n"
  "ds_read_b64_tr_b16 a[150:151], v20 offset:1280\n"
  "ds_read_b64_tr_b16 a[152:153], v20 offset:8192\n"
  "ds_read_b64_tr_b16 a[154:155], v20 offset:8448\n"
  "ds_read_b64_tr_b16 a[156:157], v20 offset:9216\n"
  "ds_read_b64_tr_b16 a[158:159], v20 offset:9472\n"
  "s_xor_b32 s74, s56, 32\n"
  "v_xor_b32 v20, s74, v15\n"
  "v_add_u32 v20, 49152, v20\n"
  "ds_read_b64_tr_b16 a[160:161], v20 offset:0\n"
  "ds_read_b64_tr_b16 a[162:163], v20 offset:256\n"
  "ds_read_b64_tr_b16 a[164:165], v20 offset:1024\n"
  "ds_read_b64_tr_b16 a[166:167], v20 offset:1280\n"
  "ds_read_b64_tr_b16 a[168:169], v20 offset:8192\n"
  "ds_read_b64_tr_b16 a[170:171], v20 offset:8448\n"
  "ds_read_b64_tr_b16 a[172:173], v20 offset:9216\n"
  "ds_read_b64_tr_b16 a[174:175], v20 offset:9472\n"
  "s_xor_b32 s74, s56, 64\n"
  "v_xor_b32 v20, s74, v15\n"
  "v_add_u32 v20, 49152, v20\n"
  "ds_read_b64_tr_b16 a[176:177], v20 offset:0\n"
  "ds_read_b64_tr_b16 a[178:179], v20 offset:256\n"
  "ds_read_b64_tr_b16 a[180:181], v20 offset:1024\n"
  "ds_read_b64_tr_b16 a[182:183], v20 offset:1280\n"
  "ds_read_b64_tr_b16 a[184:185], v20 offset:8192\n"
  "ds_read_b64_tr_b16 a[186:187], v20 offset:8448\n"
  "ds_read_b64_tr_b16 a[188:189], v20 offset:9216\n"
  "ds_read_b64_tr_b16 a[190:191], v20 offset:9472\n"
  "s_xor_b32 s74, s56, 96\n"
  "v_xor_b32 v20, s74, v15\n"
  "v_add_u32 v20, 49152, v20\n"
  "ds_read_b64_tr_b16 a[192:193], v20 offset:0\n"
  "ds_read_b64_tr_b16 a[194:195], v20 offset:256\n"
  "ds_read_b64_tr_b16 a[196:197], v20 offset:1024\n"
  "ds_read_b64_tr_b16 a[198:199], v20 offset:1280\n"
  "ds_read_b64_tr_b16 a[200:201], v20 offset:8192\n"
  "ds_read_b64_tr_b16 a[202:203], v20 offset:8448\n"
  "ds_read_b64_tr_b16 a[204:205], v20 offset:9216\n"
  "ds_read_b64_tr_b16 a[206:207], v20 offset:9472\n"
  "HLS12:\n"
  "s_waitcnt vmcnt(28) lgkmcnt(0)\n"
  "s_barrier\n"
  "s_add_u32 m0, s79, 49152\n"
  "s_add_u32 s74, s38, 3670016\n"
  "s_addc_u32 s75, s39, 0\n"
  "global_load_lds_dwordx4 v12, s[74:75]\n"
  "s_add_u32 m0, s79, 51200\n"
  "s_add_u32 s74, s38, 3735552\n"
  "s_addc_u32 s75, s39, 0\n"
  "global_load_lds_dwordx4 v12, s[74:75]\n"
  "s_add_u32 m0, s79, 57344\n"
  "s_add_u32 s74, s38, 3932160\n"
  "s_addc_u32 s75, s39, 0\n"
  "global_load_lds_dwordx4 v12, s[74:75]\n"
  "s_add_u32 m0, s79, 59392\n"
  "s_add_u32 s74, s38, 3997696\n"
  "s_addc_u32 s75, s39, 0\n"
  "global_load_lds_dwordx4 v12, s[74:75]\n"
  "s_lshr_b32 s74, s52, 3\n"
  "s_and_b32 s74, s74, 64\n"
  "s_and_b32 s75, s48, 1\n"
  "s_lshl_b32 s75, s75, 5\n"
  "s_add_u32 s74, s74, s75\n"
  "v_xor_b32 v20, s74, v15\n"
  "v_add_u32 v20, 65536, v20\n"
  "ds_read_b64_tr_b16 a[208:209], v20 offset:0\n"
  "ds_read_b64_tr_b16 a[210:211], v20 offset:256\n"
  "ds_read_b64_tr_b16 a[212:213], v20 offset:1024\n"
  "ds_read_b64_tr_b16 a[214:215], v20 offset:1280\n"
  "ds_read_b64_tr_b16 a[216:217], v20 offset:8192\n"
  "ds_read_b64_tr_b16 a[218:219], v20 offset:8448\n"
  "ds_read_b64_tr_b16 a[220:221], v20 offset:9216\n"
  "ds_read_b64_tr_b16 a[222:223], v20 offset:9472\n"
  "s_waitcnt vmcnt(28) lgkmcnt(0)\n"
  "s_barrier\n"
  "s_add_u32 m0, s79, 65536\n"
  "s_add_u32 s74, s38, 4194304\n"
  "s_addc_u32 s75, s39, 0\n"
  "global_load_lds_dwordx4 v12, s[74:75]\n"
  "s_add_u32 m0, s79, 67584\n"
  "s_add_u32 s74, s38, 4259840\n"
  "s_addc_u32 s75, s39, 0\n"
  "global_load_lds_dwordx4 v12, s[74:75]\n"
  "s_add_u32 m0, s79, 73728\n"
  "s_add_u32 s74, s38, 4456448\n"
  "s_addc_u32 s75, s39, 0\n"
  "global_load_lds_dwordx4 v12, s[74:75]\n"
  "s_add_u32 m0, s79, 75776\n"
  "s_add_u32 s74, s38, 4521984\n"
  "s_addc_u32 s75, s39, 0\n"
  "global_load_lds_dwordx4 v12, s[74:75]\n"
  "v_add_u32 v20, 81920, v13\n"
  "v_xor_b32 v21, 32, v20\n"
  "ds_read_b64_tr_b16 v[112:113], v20 offset:0\n"
  "ds_read_b64_tr_b16 v[114:115], v20 offset:2048\n"
  "ds_read_b64_tr_b16 v[116:117], v20 offset:8192\n"
  "ds_read_b64_tr_b16 v[118:119], v20 offset:10240\n"
  "ds_read_b64_tr_b16 v[120:121], v21 offset:0\n"
  "ds_read_b64_tr_b16 v[122:123], v21 offset:2048\n"
  "ds_read_b64_tr_b16 v[124:125], v21 offset:8192\n"
  "ds_read_b64_tr_b16 v[126:127], v21 offset:10240\n"
  "s_waitcnt vmcnt(28) lgkmcnt(0)\n"
  "s_barrier\n"
  "v_add_u32 v20, 98304, v13\n"
  "v_xor_b32 v21, 32, v20\n"
  "ds_read_b64_tr_b16 v[128:129], v20 offset:0\n"
  "ds_read_b64_tr_b16 v[130:131], v20 offset:2048\n"
  "ds_read_b64_tr_b16 v[132:133], v20 offset:8192\n"
  "ds_read_b64_tr_b16 v[134:135], v20 offset:10240\n"
  "ds_read_b64_tr_b16 v[136:137], v21 offset:0\n"
  "ds_read_b64_tr_b16 v[138:139], v21 offset:2048\n"
  "ds_read_b64_tr_b16 v[140:141], v21 offset:8192\n"
  "ds_read_b64_tr_b16 v[142:143], v21 offset:10240\n"
  "s_waitcnt vmcnt(24) lgkmcnt(0)\n"
  "s_barrier\n"
  "v_add_u32 v20, 114688, v13\n"
  "v_xor_b32 v21, 32, v20\n"
  "ds_read_b64_tr_b16 v[144:145], v20 offset:0\n"
  "ds_read_b64_tr_b16 v[146:147], v20 offset:2048\n"
  "ds_read_b64_tr_b16 v[148:149], v20 offset:8192\n"
  "ds_read_b64_tr_b16 v[150:151], v20 offset:10240\n"
  "ds_read_b64_tr_b16 v[152:153], v21 offset:0\n"
  "ds_read_b64_tr_b16 v[154:155], v21 offset:2048\n"
  "ds_read_b64_tr_b16 v[156:157], v21 offset:8192\n"
  "ds_read_b64_tr_b16 v[158:159], v21 offset:10240\n"
  "s_waitcnt vmcnt(20) lgkmcnt(0)\n"
  "s_barrier\n"
  "v_add_u32 v20, 131072, v13\n"
  "v_xor_b32 v21, 32, v20\n"
  "ds_read_b64_tr_b16 v[160:161], v20 offset:0\n"
  "ds_read_b64_tr_b16 v[162:163], v20 offset:2048\n"
  "ds_read_b64_tr_b16 v[164:165], v20 offset:8192\n"
  "ds_read_b64_tr_b16 v[166:167], v20 offset:10240\n"
  "ds_read_b64_tr_b16 v[168:169], v21 offset:0\n"
  "ds_read_b64_tr_b16 v[170:171], v21 offset:2048\n"
  "ds_read_b64_tr_b16 v[172:173], v21 offset:8192\n"
  "ds_read_b64_tr_b16 v[174:175], v21 offset:10240\n"
  "s_waitcnt vmcnt(16) lgkmcnt(0)\n"
  "s_barrier\n"
  "v_add_u32 v20, 0, v13\n"
  "v_xor_b32 v21, 32, v20\n"
  "ds_read_b64_tr_b16 v[176:177], v20 offset:0\n"
  "ds_read_b64_tr_b16 v[178:179], v20 offset:2048\n"
  "ds_read_b64_tr_b16 v[180:181], v20 offset:8192\n"
  "ds_read_b64_tr_b16 v[182:183], v20 offset:10240\n"
  "ds_read_b64_tr_b16 v[184:185], v21 offset:0\n"
  "ds_read_b64_tr_b16 v[186:187], v21 offset:2048\n"
  "ds_read_b64_tr_b16 v[188:189], v21 offset:8192\n"
  "ds_read_b64_tr_b16 v[190:191], v21 offset:10240\n"
  "s_waitcnt vmcnt(12) lgkmcnt(0)\n"
  "s_barrier\n"
  "v_add_u32 v20, 16384, v13\n"
  "v_xor_b32 v21, 32, v20\n"
  "ds_read_b64_tr_b16 v[192:193], v20 offset:0\n"
  "ds_read_b64_tr_b16 v[194:195], v20 offset:2048\n"
  "ds_read_b64_tr_b16 v[196:197], v20 offset:8192\n"
  "ds_read_b64_tr_b16 v[198:199], v20 offset:10240\n"
  "ds_read_b64_tr_b16 v[200:201], v21 offset:0\n"
  "ds_read_b64_tr_b16 v[202:203], v21 offset:2048\n"
  "ds_read_b64_tr_b16 v[204:205], v21 offset:8192\n"
  "ds_read_b64_tr_b16 v[206:207], v21 offset:10240\n"
  "s_waitcnt vmcnt(8) lgkmcnt(0)\n"
  "s_barrier\n"
  "v_add_u32 v20, 32768, v13\n"
  "v_xor_b32 v21, 32, v20\n"
  "ds_read_b64_tr_b16 v[208:209], v20 offset:0\n"
  "ds_read_b64_tr_b16 v[210:211], v20 offset:2048\n"
  "ds_read_b64_tr_b16 v[212:213], v20 offset:8192\n"
  "ds_read_b64_tr_b16 v[214:215], v20 offset:10240\n"
  "ds_read_b64_tr_b16 v[216:217], v21 offset:0\n"
  "ds_read_b64_tr_b16 v[218:219], v21 offset:2048\n"
  "ds_read_b64_tr_b16 v[220:221], v21 offset:8192\n"
  "ds_read_b64_tr_b16 v[222:223], v21 offset:10240\n"
  "s_waitcnt vmcnt(4) lgkmcnt(0)\n"
  "s_barrier\n"
  "v_add_u32 v20, 49152, v13\n"
  "v_xor_b32 v21, 32, v20\n"
  "ds_read_b64_tr_b16 v[224:225], v20 offset:0\n"
  "ds_read_b64_tr_b16 v[226:227], v20 offset:2048\n"
  "ds_read_b64_tr_b16 v[228:229], v20 offset:8192\n"
  "ds_read_b64_tr_b16 v[230:231], v20 offset:10240\n"
  "ds_read_b64_tr_b16 v[232:233], v21 offset:0\n"
  "ds_read_b64_tr_b16 v[234:235], v21 offset:2048\n"
  "ds_read_b64_tr_b16 v[236:237], v21 offset:8192\n"
  "ds_read_b64_tr_b16 v[238:239], v21 offset:10240\n"
  "s_waitcnt vmcnt(0) lgkmcnt(0)\n"
  "s_barrier\n"
  "v_add_u32 v20, 65536, v13\n"
  "v_xor_b32 v21, 32, v20\n"
  "ds_read_b64_tr_b16 v[240:241], v20 offset:0\n"
  "ds_read_b64_tr_b16 v[242:243], v20 offset:2048\n"
  "ds_read_b64_tr_b16 v[244:245], v20 offset:8192\n"
  "ds_read_b64_tr_b16 v[246:247], v20 offset:10240\n"
  "ds_read_b64_tr_b16 v[248:249], v21 offset:0\n"
  "ds_read_b64_tr_b16 v[250:251], v21 offset:2048\n"
  "ds_read_b64_tr_b16 v[252:253], v21 offset:8192\n"
  "ds_read_b64_tr_b16 v[254:255], v21 offset:10240\n"
  "s_waitcnt lgkmcnt(0)\n"
  "s_barrier\n"
  "v_mov_b32 v16, 0x3f803f80\n"
  "v_mov_b32 v17, 0x3f803f80\n"
  "v_mov_b32 v18, 0x3f803f80\n"
  "v_mov_b32 v19, 0x3f803f80\n"
  "v_mov_b32 v77, s55\n"
  "s_cmp_lt_u32 s48, 2\n"
  "s_cselect_b64 s[38:39], -1, 0\n"
  "s_mov_b32 s2, s45\n"
  "s_mov_b32 s100, s50\n"
  "s_mov_b32 s101, s51\n"
  "s_lshr_b32 s3, s52, 8\n"
  "s_and_b32 s3, s3, 4\n"
  "s_add_u32 s3, s3, s48\n"
  "s_mov_b32 s7, s57\n"
  "s_mov_b32 s14, s58\n"
  "s_mov_b32 s15, s59\n"
  "s_mov_b32 s55, s58\n"
  "s_mov_b32 s51, s59\n"
  "s_mov_b32 s56, 0\n"
  "s_mov_b32 s57, 0\n"
  "s_mov_b32 s58, 100000\n"
  "s_mov_b32 s59, 0\n"
  "s_mov_b32 s60, 562036736\n"
  "s_mov_b32 s61, 1006632960\n"
  "s_mov_b32 s81, -1\n"
  "s_add_u32 s74, s36, 16778624\n"
  "s_addc_u32 s75, s37, 0\n"
  "PLCH:\n"
  "v_mov_b32 v79, 0\n"
  "global_load_dword v79, v79, s[74:75] sc1\n"
  "s_waitcnt vmcnt(0)\n"
  "v_readfirstlane_b32 s76, v79\n"
  "s_cmp_ge_u32 s76, 0x40000\n"
  "s_cbranch_scc1 PDCH\n"
  "s_sub_u32 s58, s58, 1\n"
  "s_cbranch_scc1 QUITH\n"
  "s_sleep 4\n"
  "s_branch PLCH\n"
  "PDCH:\n"
  "s_and_b32 s74, s76, 2\n"
  "s_lshr_b32 s75, s74, 1\n"
  "s_lshl_b32 s4, 0x55555555, s75\n"
  "s_mov_b32 s5, s4\n"
  "s_lshl_b32 s74, s74, 12\n"
  "s_xor_b32 s2, s74, s2\n"
  "s_xor_b32 s100, s74, s100\n"
  "s_xor_b32 s101, s74, s101\n"
  "v_lshrrev_b32 v79, 21, v9\n"
  "v_lshlrev_b32 v79, 2, v79\n"
  "v_lshrrev_b32 v79, v79, s76\n"
  "v_and_b32 v79, 7, v79\n"
  "v_lshlrev_b32 v79, 12, v79\n"
  "v_xor_b32 v9, v9, v79\n"
  "v_lshrrev_b32 v79, 21, v10\n"
  "v_lshlrev_b32 v79, 2, v79\n"
  "v_lshrrev_b32 v79, v79, s76\n"
  "v_and_b32 v79, 7, v79\n"
  "v_lshlrev_b32 v79, 12, v79\n"
  "v_xor_b32 v10, v10, v79\n"
  "v_lshrrev_b32 v79, 21, v11\n"
  "v_lshlrev_b32 v79, 2, v79\n"
  "v_lshrrev_b32 v79, v79, s76\n"
  "v_and_b32 v79, 7, v79\n"
  "v_lshlrev_b32 v79, 12, v79\n"
  "v_xor_b32 v11, v11, v79\n"
  "s_lshr_b32 s74, s44, 19\n"
  "s_lshr_b32 s74, s76, s74\n"
  "s_and_b32 s74, s74, 7\n"
  "s_lshl_b32 s74, s74, 12\n"
  "s_xor_b32 s44, s44, s74\n"
  "s_xor_b32 s46, s46, s74\n"
  "s_add_u32 s45, s44, 128000\n"
  "s_add_u32 s76, s101, s7\n"
  "s_add_u32 s76, s76, 137216\n"
  "s_add_u32 s74, s36, s76\n"
  "s_addc_u32 s75, s37, 0\n"
  "v_mov_b32 v79, 1\n"
  "s_cmp_eq_u32 s48, 0\n"
  "s_cselect_b64 exec, 1, 0\n"
  "global_atomic_add v0, v79, s[74:75] sc1\n"
  "s_mov_b64 exec, -1\n"
  "s_add_u32 s18, s36, s2\n"
  "s_addc_u32 s19, s37, 0\n"
  "s_sub_u32 s18, s18, 768\n"
  "s_subb_u32 s19, s19, 0\n"
  "s_cmp_eq_u32 s48, 0\n"
  "s_cselect_b32 s98, -1, 0\n"
  "v_mov_b64 v[56:57], 0\n"
  "v_mov_b64 v[58:59], 0\n"
  "v_mov_b64 v[60:61], 0\n"
  "v_mov_b64 v[62:63], 0\n"
  "v_mov_b64 v[64:65], 0\n"
  "v_mov_b64 v[66:67], 0\n"
  "v_mov_b64 v[68:69], 0\n"
  "v_mov_b64 v[70:71], 0\n"
  "v_mov_b64 v[72:73], 0\n"
  "v_mov_b64 v[74:75], 0\n"
  "v_add_u32 v76, 34816, v0\n"
  "ds_write_b32 v0, v56 offset:33792\n"
  "ds_write_b32 v0, v56 offset:51200\n"
  "ds_write_b32 v76, v56 offset:33792\n"
  "s_mov_b32 s32, 0\n"
  "s_mov_b32 s35, 0\n"
  "v_mov_b32 v76, v0\n"
  "s_cmp_eq_u32 s52, 1024\n"
  "s_cselect_b64 s[0:1], -1, 0\n"
  "s_waitcnt lgkmcnt(0)\n"
  "s_barrier\n"
  "s_add_u32 s74, s36, s2\n"
  "s_addc_u32 s75, s37, 0\n"
  "PLHK:\n"
  "v_and_b32 v79, 128, v0\n"
  "global_load_dword v79, v79, s[74:75] sc1\n"
  "s_waitcnt vmcnt(0)\n"
  "v_readfirstlane_b32 s76, v79\n"
  "v_readlane_b32 s77, v79, 8\n"
  "s_min_u32 s76, s76, s77\n"
  "s_cmp_ge_u32 s76, 1\n"
  "s_cbranch_scc1 PDHK\n"
  "s_sub_u32 s58, s58, 1\n"
  "s_cbranch_scc1 QUITH\n"
  "s_sleep 4\n"
  "s_branch PLHK\n"
  "PDHK:\n"
  "s_bitcmp1_b64 s[4:5], 0\n"
  "s_cselect_b32 s74, 8192, 0\n"
  "s_and_b32 s75, 0, 63\n"
  "s_lshl_b32 s75, s75, 21\n"
  "s_add_u32 s74, s74, s75\n"
  "s_add_u32 s74, s74, 20971520\n"
  "s_xor_b32 s75, s74, s47\n"
  "s_add_u32 s40, s36, s75\n"
  "s_addc_u32 s41, s37, 0\n"
  "s_lshl_b32 s42, s80, 2\n"
  "s_add_u32 s42, s42, 17408\n"
  "s_mov_b32 m0, s42\n"
  "s_mov_b32 s52, 1\n"
  "global_load_lds_dwordx4 v0, s[40:41] offset:0 sc1\n"
  "global_load_lds_dwordx4 v0, s[40:41] offset:1024 sc1\n"
  "global_load_lds_dwordx4 v0, s[40:41] offset:2048 sc1\n"
  "global_load_lds_dwordx4 v0, s[40:41] offset:3072 sc1\n"
  "s_add_u32 s77, s101, s7\n"
  "s_add_u32 s77, s77, 138240\n"
  "s_add_u32 s74, s36, s77\n"
  "s_addc_u32 s75, s37, 0\n"
  "PLGH:\n"
  "v_mov_b32 v79, 0\n"
  "global_load_dword v79, v79, s[74:75] sc1\n"
  "s_waitcnt vmcnt(0)\n"
  "v_readfirstlane_b32 s76, v79\n"
  "s_cmp_lg_u32 s76, 0\n"
  "s_cbranch_scc1 PDGH\n"
  "s_sub_u32 s58, s58, 1\n"
  "s_cbranch_scc1 QUITH\n"
  "s_sleep 1\n"
  "s_branch PLGH\n"
  "PDGH:\n"
  "s_mov_b32 s49, s76\n"
  "s_mov_b32 s43, s49\n"
  "s_sub_u32 s6, s49, 0x100000\n"
  "s_mov_b32 s16, 0\n"
  "s_mov_b32 s17, 1\n"
  "s_mov_b32 m0, 33792\n"
  "s_mov_b32 s33, -1\n"
  "s_mov_b32 exec_lo, 0\n"
  "s_and_b32 exec_hi, s98, 0x01010000\n"
  "global_load_lds_dword v0, s[18:19] sc1\n"
  "s_mov_b64 exec, -1\n"
  "HTRY:\n"
  "s_sub_u32 s74, s49, s43\n"
  "v_mov_b32 v78, s74\n"
  "v_mov_b32 v15, s56\n"
  "v_lshrrev_b32 v79, 1, v0\n"
  "s_mul_i32 s78, s57, 163840\n"
  "s_lshl_b32 s79, s32, 16\n"
  "s_add_u32 s79, s79, s78\n"
  "s_add_u32 s79, s79, s44\n"
  "s_add_u32 s62, s36, s79\n"
  "s_addc_u32 s63, s37, 0\n"
  "s_add_u32 s64, s62, 32768\n"
  "s_addc_u32 s65, s63, 0\n"
  "s_lshl_b32 s79, s32, 10\n"
  "s_add_u32 s79, s79, s78\n"
  "s_add_u32 s79, s79, s45\n"
  "s_add_u32 s66, s36, s79\n"
  "s_addc_u32 s67, s37, 0\n"
  "s_add_u32 m0, s80, 0\n"
  "s_add_u32 s79, s80, 4096\n"
  "s_branch WGH\n"
  "WH:\n"
  "s_memrealtime s[74:75]\n"
  "s_waitcnt lgkmcnt(0)\n"
  "s_mul_i32 s74, s74, 24\n"
  "s_sub_i32 s74, s49, s74\n"
  "s_cmp_lt_i32 s74, 750\n"
  "s_cbranch_scc1 WFH\n"
  "s_cmp_gt_i32 s74, 0x40000\n"
  "s_cbranch_scc1 WEH\n"
  "s_sleep 2\n"
  "s_branch WH\n"
  "WFH:\n"
  "s_cmp_lt_i32 s74, 100\n"
  "s_cbranch_scc1 WEH\n"
  "WGH:\n"
  "s_memtime s[76:77]\n"
  "s_memrealtime s[20:21]\n"
  "s_nop 2\n"
  "s_memrealtime s[22:23]\n"
  "s_nop 2\n"
  "s_memrealtime s[24:25]\n"
  "s_nop 2\n"
  "s_memrealtime s[26:27]\n"
  "s_nop 2\n"
  "s_memrealtime s[28:29]\n"
  "s_nop 2\n"
  "s_memrealtime s[30:31]\n"
  "s_waitcnt lgkmcnt(0)\n"
  "s_add_u32 s20, s20, s22\n"
  "s_add_u32 s24, s24, s26\n"
  "s_add_u32 s28, s28, s30\n"
  "s_add_u32 s20, s20, s24\n"
  "s_add_u32 s20, s20, s28\n"
  "s_lshl_b32 s74, s20, 2\n"
  "s_sub_u32 s34, s74, s76\n"
  "s_sub_i32 s74, s49, s74\n"
  "s_sub_i32 s74, s74, 238\n"
  "s_max_i32 s74, s74, 0\n"
  "s_cmp_ge_i32 s74, 512\n"
  "s_cbranch_scc1 WH\n"
  "s_lshr_b32 s74, s74, 3\n"
  "s_lshl_b32 s74, s74, 2\n"
  "s_sub_i32 s74, 268, s74\n"
  "s_getpc_b64 s[76:77]\n"
  "s_add_u32 s76, s76, s74\n"
  "s_addc_u32 s77, s77, 0\n"
  "s_setpc_b64 s[76:77]\n"
  "s_nop 1\n"
  "s_nop 1\n"
  "s_nop 1\n"
  "s_nop 1\n"
  "s_nop 1\n"
  "s_nop 1\n"
  "s_nop 1\n"
  "s_nop 1\n"
  "s_nop 1\n"
  "s_nop 1\n"
  "s_nop 1\n"
  "s_nop 1\n"
  "s_nop 1\n"
  "s_nop 1\n"
  "s_nop 1\n"
  "s_nop 1\n"
  "s_nop 1\n"
  "s_nop 1\n"
  "s_nop 1\n"
  "s_nop 1\n"
  "s_nop 1\n"
  "s_nop 1\n"
  "s_nop 1\n"
  "s_nop 1\n"
  "s_nop 1\n"
  "s_nop 1\n"
  "s_nop 1\n"
  "s_nop 1\n"
  "s_nop 1\n"
  "s_nop 1\n"
  "s_nop 1\n"
  "s_nop 1\n"
  "s_nop 1\n"
  "s_nop 1\n"
  "s_nop 1\n"
  "s_nop 1\n"
  "s_nop 1\n"
  "s_nop 1\n"
  "s_nop 1\n"
  "s_nop 1\n"
  "s_nop 1\n"
  "s_nop 1\n"
  "s_nop 1\n"
  "s_nop 1\n"
  "s_nop 1\n"
  "s_nop 1\n"
  "s_nop 1\n"
  "s_nop 1\n"
  "s_nop 1\n"
  "s_nop 1\n"
  "s_nop 1\n"
  "s_nop 1\n"
  "s_nop 1\n"
  "s_nop 1\n"
  "s_nop 1\n"
  "s_nop 1\n"
  "s_nop 1\n"
  "s_nop 1\n"
  "s_nop 1\n"
  "s_nop 1\n"
  "s_nop 1\n"
  "s_nop 1\n"
  "s_nop 1\n"
  "s_nop 1\n"
  "WEH:\n"
  "global_load_lds_dwordx4 v1, s[62:63] sc1\n"
  "s_mov_b32 m0, s79\n"
  "s_cmp_lg_u32 s48, 3\n"
  "global_load_lds_dwordx4 v1, s[64:65] sc1\n"
  "s_cbranch_scc1 HN8\n"
  "s_mov_b32 m0, 8192\n"
  "s_nop 0\n"
  "global_load_lds_dwordx4 v1, s[66:67] sc1\n"
  "s_waitcnt vmcnt(14)\n"
  "s_branch HM8\n"
  "HN8:\n"
  "s_waitcnt vmcnt(13)\n"
  "HM8:\n"
  "s_cmp_eq_u32 s17, 0\n"
  "s_cbranch_scc1 HW8\n"
  "s_waitcnt vmcnt(9)\n"
  "s_cmp_eq_u32 s17, 1\n"
  "s_cbranch_scc1 HW8\n"
  "s_waitcnt vmcnt(3)\n"
  "HW8:\n"
  "s_barrier\n"
  "ds_read_b128 v[80:83], v76 offset:17408\n"
  "s_add_u32 s64, s36, s100\n"
  "s_addc_u32 s65, s37, 0\n"
  "s_cmp_eq_u32 s3, 4\n"
  "ds_read_b128 v[84:87], v76 offset:18432\n"
  "s_cselect_b64 s[22:23], 1, 0\n"
  "s_mov_b64 exec, s[22:23]\n"
  "global_store_dword v0, v15, s[64:65] sc1\n"
  "s_mov_b64 exec, -1\n"
  "ds_read_b128 v[88:91], v76 offset:19456\n"
  "s_add_u32 s26, s57, 1\n"
  "s_cmp_ge_u32 s26, 3\n"
  "s_cselect_b32 s75, 3, 0\n"
  "ds_read_b128 v[92:95], v76 offset:20480\n"
  "s_sub_u32 s26, s26, s75\n"
  "s_mul_i32 s26, s26, 17408\n"
  "s_add_u32 m0, s26, 33792\n"
  "ds_read_b128 v[96:99], v76 offset:21504\n"
  "s_add_u32 s75, s56, 1\n"
  "s_and_b32 s74, s75, 0x78\n"
  "s_add_u32 s74, s74, s75\n"
  "ds_read_b128 v[100:103], v76 offset:22528\n"
  "s_lshl_b32 s74, s74, 10\n"
  "s_add_u32 s74, s74, s101\n"
  "s_add_u32 s62, s36, s74\n"
  "ds_read_b128 v[104:107], v76 offset:23552\n"
  "s_addc_u32 s63, s37, 0\n"
  "s_mov_b32 exec_lo, s98\n"
  "s_and_b32 exec_hi, s98, 0xffff\n"
  "global_load_lds_dword v0, s[62:63] sc1\n"
  "s_mov_b64 exec, -1\n"
  "ds_read_b128 v[108:111], v76 offset:24576\n"
  "s_add_u32 s76, s57, s52\n"
  "s_cmp_ge_u32 s76, 3\n"
  "s_cselect_b32 s75, 3, 0\n"
  "ds_read_b128 a[224:227], v76 offset:25600\n"
  "s_sub_u32 s76, s76, s75\n"
  "s_mul_i32 s76, s76, 17408\n"
  "s_add_u32 m0, s42, s76\n"
  "ds_read_b128 a[228:231], v76 offset:26624\n"
  "s_and_b32 s77, s56, 1\n"
  "s_mov_b64 exec, 0x8000\n"
  "ds_max_u32 v76, v78 offset:33792\n"
  "s_mov_b64 exec, -1\n"
  "ds_read_b128 a[232:235], v76 offset:27648\n"
  "s_lshl_b32 s75, s77, 16\n"
  "s_add_u32 s70, s36, s75\n"
  "s_addc_u32 s71, s37, 0\n"
  "ds_read_b128 a[236:239], v76 offset:28672\n"
  "s_add_u32 s75, s56, 4\n"
  "s_and_b32 s74, s75, 0x78\n"
  "s_add_u32 s74, s74, s75\n"
  "ds_read_b128 a[240:243], v76 offset:29696\n"
  "s_lshl_b32 s74, s74, 10\n"
  "s_add_u32 s74, s74, s101\n"
  "ds_read_b128 a[244:247], v76 offset:30720\n"
  "ds_read_b128 a[248:251], v76 offset:31744\n"
  "ds_read_b128 a[252:255], v76 offset:32768\n"
  "s_cmp_lg_u32 s48, 3\n"
  "s_waitcnt vmcnt(4) lgkmcnt(0)\n"
  "s_cbranch_scc0 HD8\n"
  "s_waitcnt vmcnt(3)\n"
  "HD8:\n"
  "s_barrier\n"
  "; BODY\n"
  "ds_read_b128 v[20:23], v0 offset:0\n"
  "ds_read_b128 v[24:27], v0 offset:1024\n"
  "ds_read_b128 v[28:31], v0 offset:2048\n"
  "ds_read_b128 v[32:35], v0 offset:3072\n"
  "s_waitcnt lgkmcnt(3)\n"
  "v_smfmac_f32_16x16x64_bf16 v[56:59], v[20:23], v[112:119], v6\n"
  "v_smfmac_f32_16x16x64_bf16 v[60:63], v[20:23], v[120:127], v6\n"
  "s_waitcnt vmcnt(2)\n"
  "s_barrier\n"
  "ds_read_b128 v[36:39], v0 offset:4096\n"
  "s_waitcnt lgkmcnt(3)\n"
  "v_smfmac_f32_16x16x64_bf16 v[56:59], v[24:27], v[128:135], v6\n"
  "ds_read_b128 v[40:43], v0 offset:5120\n"
  "v_smfmac_f32_16x16x64_bf16 v[60:63], v[24:27], v[136:143], v6\n"
  "ds_read_b128 v[44:47], v0 offset:6144\n"
  "s_waitcnt lgkmcnt(4)\n"
  "v_smfmac_f32_16x16x64_bf16 v[56:59], v[28:31], v[144:151], v6\n"
  "ds_read_b128 v[48:51], v0 offset:7168\n"
  "v_smfmac_f32_16x16x64_bf16 v[60:63], v[28:31], v[152:159], v6\n"
  "ds_read_b128 v[52:55], v0 offset:8192\n"
  "s_waitcnt lgkmcnt(5)\n"
  "v_smfmac_f32_16x16x64_bf16 v[56:59], v[32:35], v[160:167], v6\n"
  "s_add_u32 s74, s74, s7\n"
  "s_add_u32 s66, s36, s74\n"
  "v_smfmac_f32_16x16x64_bf16 v[60:63], v[32:35], v[168:175], v6\n"
  "s_addc_u32 s67, s37, 0\n"
  "s_mul_i32 s74, s57, 131072\n"
  "s_waitcnt lgkmcnt(4)\n"
  "v_smfmac_f32_16x16x64_bf16 v[56:59], v[36:39], v[176:183], v6\n"
  "s_add_u32 s74, s74, s46\n"
  "s_add_u32 s68, s36, s74\n"
  "s_waitcnt lgkmcnt(3)\n"
  "v_smfmac_f32_16x16x64_bf16 v[56:59], v[40:43], v[192:199], v6\n"
  "s_addc_u32 s69, s37, 0\n"
  "s_add_u32 s72, s36, s53\n"
  "s_waitcnt lgkmcnt(2)\n"
  "v_smfmac_f32_16x16x64_bf16 v[56:59], v[44:47], v[208:215], v6\n"
  "s_addc_u32 s73, s37, 0\n"
  "s_add_u32 s27, s56, s52\n"
  "s_waitcnt lgkmcnt(1)\n"
  "v_smfmac_f32_16x16x64_bf16 v[56:59], v[48:51], v[224:231], v6\n"
  "s_min_u32 s27, s27, 63\n"
  "s_bitcmp1_b64 s[4:5], s27\n"
  "s_waitcnt lgkmcnt(0)\n"
  "v_smfmac_f32_16x16x64_bf16 v[56:59], v[52:55], v[240:247], v6\n"
  "s_cselect_b32 s74, 8192, 0\n"
  "s_and_b32 s75, s27, 63\n"
  "v_smfmac_f32_16x16x64_bf16 v[60:63], v[36:39], v[184:191], v6\n"
  "s_lshl_b32 s75, s75, 21\n"
  "s_add_u32 s74, s74, s75\n"
  "s_add_u32 s74, s74, 20971520\n"
  "v_smfmac_f32_16x16x64_bf16 v[60:63], v[40:43], v[200:207], v6\n"
  "s_xor_b32 s75, s74, s47\n"
  "s_add_u32 s40, s36, s75\n"
  "s_addc_u32 s41, s37, 0\n"
  "v_smfmac_f32_16x16x64_bf16 v[60:63], v[44:47], v[216:223], v6\n"
  "s_add_u32 s76, s16, s34\n"
  "s_sub_u32 s76, s76, s6\n"
  "v_add_f32 v56, v56, v58\n"
  "v_smfmac_f32_16x16x64_bf16 v[60:63], v[48:51], v[232:239], v6\n"
  "s_min_u32 s33, s33, s76\n"
  "s_sub_u32 s6, s6, 0x100000\n"
  "v_add_f32 v57, v57, v59\n"
  "v_smfmac_f32_16x16x64_bf16 v[60:63], v[52:55], v[248:255], v6\n"
  "s_cmp_lt_u32 s33, 6000\n"
  "s_cselect_b32 s24, 1, 0\n"
  "v_cvt_pk_bf16_f32 v14, v56, v57\n"
  "v_smfmac_f32_16x16x64_bf16 v[64:67], v[20:23], a[0:7], v6\n"
  "ds_write2_b32 v3, v14, v14 offset0:0 offset1:1\n"
  "v_smfmac_f32_16x16x64_bf16 v[68:71], v[20:23], a[8:15], v6\n"
  "v_cmp_u_f32 vcc, v56, v57\n"
  "v_smfmac_f32_16x16x64_bf16 v[64:67], v[24:27], a[16:23], v6\n"
  "v_add_f32 v60, v60, v62\n"
  "v_smfmac_f32_16x16x64_bf16 v[68:71], v[24:27], a[24:31], v6\n"
  "v_add_f32 v61, v61, v63\n"
  "v_smfmac_f32_16x16x64_bf16 v[64:67], v[28:31], a[32:39], v6\n"
  "v_cvt_pk_bf16_f32 v15, v60, v61\n"
  "v_smfmac_f32_16x16x64_bf16 v[68:71], v[28:31], a[40:47], v6\n"
  "ds_write2_b32 v3, v15, v15 offset0:128 offset1:129\n"
  "v_smfmac_f32_16x16x64_bf16 v[64:67], v[32:35], a[48:55], v6\n"
  "ds_write_b64 v4, v[14:15]\n"
  "v_smfmac_f32_16x16x64_bf16 v[68:71], v[32:35], a[56:63], v6\n"
  "s_cbranch_vccnz HBADM\n"
  "HOKM:\n"
  "v_smfmac_f32_16x16x64_bf16 v[64:67], v[36:39], a[64:71], v6\n"
  "s_add_u32 s74, s56, s3\n"
  "s_and_b32 s74, s74, 7\n"
  "s_cselect_b32 s24, 0, s24\n"
  "v_smfmac_f32_16x16x64_bf16 v[68:71], v[36:39], a[72:79], v6\n"
  "s_mov_b32 s25, 0\n"
  "v_smfmac_f32_16x16x64_bf16 v[64:67], v[40:43], a[80:87], v6\n"
  "s_waitcnt lgkmcnt(0)\n"
  "s_barrier\n"
  "v_smfmac_f32_16x16x64_bf16 v[68:71], v[40:43], a[88:95], v6\n"
  "ds_read_b64_tr_b16 v[20:21], v5 offset:0\n"
  "ds_read_b64_tr_b16 v[22:23], v5 offset:512\n"
  "v_smfmac_f32_16x16x64_bf16 v[64:67], v[44:47], a[96:103], v6\n"
  "global_load_dwordx4 v[36:39], v2, s[68:69] offset:0 sc1\n"
  "ds_read_b64_tr_b16 v[24:25], v5 offset:1024\n"
  "v_smfmac_f32_16x16x64_bf16 v[64:67], v[48:51], a[112:119], v6\n"
  "ds_read_b64_tr_b16 v[26:27], v5 offset:1536\n"
  "ds_read_b64_tr_b16 v[28:29], v5 offset:2048\n"
  "v_smfmac_f32_16x16x64_bf16 v[64:67], v[52:55], a[128:135], v6\n"
  "ds_read_b64_tr_b16 v[30:31], v5 offset:2560\n"
  "ds_read_b64_tr_b16 v[32:33], v5 offset:3072\n"
  "v_smfmac_f32_16x16x64_bf16 v[68:71], v[44:47], a[104:111], v6\n"
  "ds_read_b64_tr_b16 v[34:35], v5 offset:3584\n"
  "v_smfmac_f32_16x16x64_bf16 v[68:71], v[48:51], a[120:127], v6\n"
  "global_load_dwordx4 v[40:43], v2, s[68:69] offset:1024 sc1\n"
  "ds_read_b64_tr_b16 v[44:45], v5 offset:4096\n"
  "ds_read_b64_tr_b16 v[46:47], v5 offset:4608\n"
  "v_smfmac_f32_16x16x64_bf16 v[68:71], v[52:55], a[136:143], v6\n"
  "v_add_f32 v64, v64, v66\n"
  "s_waitcnt lgkmcnt(8)\n"
  "v_mfma_f32_16x16x32_bf16 v[56:59], v[80:83], v[20:23], 0\n"
  "v_add_f32 v65, v65, v67\n"
  "v_cvt_pk_bf16_f32 v14, v64, v65\n"
  "s_waitcnt lgkmcnt(6)\n"
  "v_mfma_f32_16x16x32_bf16 v[56:59], v[88:91], v[24:27], v[56:59]\n"
  "s_nop 0\n"
  "v_add_f32 v68, v68, v70\n"
  "v_add_f32 v69, v69, v71\n"
  "s_waitcnt lgkmcnt(4)\n"
  "v_mfma_f32_16x16x32_bf16 v[56:59], v[96:99], v[28:31], v[56:59]\n"
  "v_cvt_pk_bf16_f32 v15, v68, v69\n"
  "ds_write_b64 v4, v[14:15] offset:2048\n"
  "s_waitcnt lgkmcnt(3)\n"
  "v_mfma_f32_16x16x32_bf16 v[56:59], v[104:107], v[32:35], v[56:59]\n"
  "ds_read_b64_tr_b16 v[48:49], v5 offset:5120\n"
  "ds_read_b64_tr_b16 v[50:51], v5 offset:5632\n"
  "v_mfma_f32_16x16x32_bf16 v[60:63], v[84:87], v[20:23], 0\n"
  "s_mov_b64 exec, s[0:1]\n"
  "global_store_dwordx2 v79, v[14:15], s[72:73] sc1\n"
  "s_mov_b64 exec, -1\n"
  "v_mfma_f32_16x16x32_bf16 v[60:63], v[92:95], v[24:27], v[60:63]\n"
  "s_cmp_eq_u32 s56, 63\n"
  "s_cbranch_scc1 ENDH\n"
  "v_mfma_f32_16x16x32_bf16 v[60:63], v[100:103], v[28:31], v[60:63]\n"
  "v_exp_f32 v56, v56\n"
  "v_mfma_f32_16x16x32_bf16 v[60:63], v[108:111], v[32:35], v[60:63]\n"
  "s_waitcnt lgkmcnt(2)\n"
  "s_barrier\n"
  "v_mfma_f32_32x32x16_bf16 v[80:95], v[72:75], v[72:75], 0\n"
  "ds_read_b64_tr_b16 v[96:97], v5 offset:6144\n"
  "ds_read_b64_tr_b16 v[98:99], v5 offset:6656\n"
  "ds_read_b64_tr_b16 v[100:101], v5 offset:7168\n"
  "ds_read_b64_tr_b16 v[102:103], v5 offset:7680\n"
  "global_load_lds_dwordx4 v0, s[40:41] offset:0 sc1\n"
  "v_exp_f32 v57, v57\n"
  "v_exp_f32 v58, v58\n"
  "v_exp_f32 v59, v59\n"
  "v_exp_f32 v60, v60\n"
  "s_waitcnt lgkmcnt(2)\n"
  "v_mfma_f32_16x16x32_bf16 v[52:55], v[96:99], v[44:47], 0\n"
  "v_exp_f32 v61, v61\n"
  "s_waitcnt lgkmcnt(0)\n"
  "v_mfma_f32_16x16x32_bf16 v[52:55], v[100:103], v[48:51], v[52:55]\n"
  "global_load_lds_dwordx4 v0, s[40:41] offset:1024 sc1\n"
  "v_exp_f32 v62, v62\n"
  "v_exp_f32 v63, v63\n"
  "v_perm_b32 v44, v57, v56, v7\n"
  "v_perm_b32 v45, v59, v58, v7\n"
  "v_perm_b32 v46, v61, v60, v7\n"
  "v_perm_b32 v47, v63, v62, v7\n"
  "v_perm_b32 v48, v57, v56, v8\n"
  "v_perm_b32 v49, v59, v58, v8\n"
  "v_mfma_f32_16x16x32_bf16 v[68:71], v[16:19], v[44:47], 0\n"
  "v_perm_b32 v50, v61, v60, v8\n"
  "v_perm_b32 v51, v63, v62, v8\n"
  "v_mfma_f32_16x16x32_bf16 v[20:23], a[224:227], v[44:47], 0\n"
  "v_mul_f32_dpp v64, v52, v77 quad_perm:[0,1,0,1] row_mask:0xf bank_mask:0xf\n"
  "v_mul_f32_dpp v65, v53, v77 quad_perm:[0,1,0,1] row_mask:0xf bank_mask:0xf\n"
  "v_mfma_f32_16x16x32_bf16 v[68:71], v[16:19], v[48:51], v[68:71]\n"
  "v_fmac_f32_dpp v64, v54, v77 quad_perm:[2,3,2,3] row_mask:0xf bank_mask:0xf\n"
  "v_fmac_f32_dpp v65, v55, v77 quad_perm:[2,3,2,3] row_mask:0xf bank_mask:0xf\n"
  "v_mfma_f32_16x16x32_bf16 v[20:23], a[228:231], v[48:51], v[20:23]\n"
  "global_load_lds_dwordx4 v0, s[40:41] offset:2048 sc1\n"
  "v_exp_f32 v64, v64\n"
  "v_mfma_f32_16x16x32_bf16 v[24:27], a[232:235], v[44:47], 0\n"
  "v_exp_f32 v65, v65\n"
  "v_mfma_f32_16x16x32_bf16 v[24:27], a[236:239], v[48:51], v[24:27]\n"
  "v_perm_b32 v12, v65, v64, v7\n"
  "v_perm_b32 v13, v65, v64, v8\n"
  "v_mfma_f32_16x16x32_bf16 v[28:31], a[240:243], v[44:47], 0\n"
  "global_load_lds_dwordx4 v0, s[40:41] offset:3072 sc1\n"
  "HPV:\n"
  "v_mfma_f32_16x16x16_bf16 v[68:71], v[16:17], v[12:13], v[68:71]\n"
  "s_waitcnt vmcnt(6)\n"
  "v_mfma_f32_16x16x16_bf16 v[20:23], v[36:37], v[12:13], v[20:23]\n"
  "v_mfma_f32_16x16x16_bf16 v[24:27], v[38:39], v[12:13], v[24:27]\n"
  "v_mfma_f32_16x16x32_bf16 v[28:31], a[244:247], v[48:51], v[28:31]\n"
  "v_rcp_f32 v78, v68\n"
  "v_mfma_f32_16x16x32_bf16 v[32:35], a[248:251], v[44:47], 0\n"
  "v_mul_f32 v20, v78, v20\n"
  "v_mul_f32 v21, v78, v21\n"
  "v_mfma_f32_16x16x32_bf16 v[32:35], a[252:255], v[48:51], v[32:35]\n"
  "v_mul_f32 v22, v78, v22\n"
  "v_mul_f32 v23, v78, v23\n"
  "s_waitcnt vmcnt(5)\n"
  "v_mfma_f32_16x16x16_bf16 v[28:31], v[40:41], v[12:13], v[28:31]\n"
  "v_cvt_pk_bf16_f32 v104, v20, v21\n"
  "v_cvt_pk_bf16_f32 v105, v22, v23\n"
  "v_mul_f32 v24, v78, v24\n"
  "v_mul_f32 v25, v78, v25\n"
  "v_mfma_f32_16x16x16_bf16 v[32:35], v[42:43], v[12:13], v[32:35]\n"
  "v_mul_f32 v26, v78, v26\n"
  "v_mul_f32 v27, v78, v27\n"
  "v_cvt_pk_bf16_f32 v106, v24, v25\n"
  "v_cvt_pk_bf16_f32 v107, v26, v27\n"
  "v_subrev_u32 v79, s60, v68\n"
  "s_nop 0\n"
  "v_smfmac_f32_16x16x64_bf16 v[72:75], v[104:107], a[208:215], v6\n"
  "v_mul_f32 v28, v78, v28\n"
  "v_mul_f32 v29, v78, v29\n"
  "v_mul_f32 v30, v78, v30\n"
  "v_smfmac_f32_16x16x64_bf16 v[80:83], v[104:107], a[144:151], v6\n"
  "v_mul_f32 v31, v78, v31\n"
  "v_cvt_pk_bf16_f32 v108, v28, v29\n"
  "v_cvt_pk_bf16_f32 v109, v30, v31\n"
  "v_smfmac_f32_16x16x64_bf16 v[84:87], v[104:107], a[160:167], v6\n"
  "v_mul_f32 v32, v78, v32\n"
  "v_mul_f32 v33, v78, v33\n"
  "v_mul_f32 v34, v78, v34\n"
  "v_smfmac_f32_16x16x64_bf16 v[88:91], v[104:107], a[176:183], v6\n"
  "v_mul_f32 v35, v78, v35\n"
  "v_cvt_pk_bf16_f32 v110, v32, v33\n"
  "v_cvt_pk_bf16_f32 v111, v34, v35\n"
  "v_smfmac_f32_16x16x64_bf16 v[92:95], v[104:107], a[192:199], v6\n"
  "v_cmp_le_u32 vcc, s61, v79\n"
  "v_smfmac_f32_16x16x64_bf16 v[72:75], v[108:111], a[216:223], v6\n"
  "v_smfmac_f32_16x16x64_bf16 v[80:83], v[108:111], a[152:159], v6\n"
  "v_smfmac_f32_16x16x64_bf16 v[84:87], v[108:111], a[168:175], v6\n"
  "s_nop 0\n"
  "v_add_f32 v72, v72, v74\n"
  "s_cbranch_vccnz HBADZ\n"
  "HOKZ:\n"
  "v_smfmac_f32_16x16x64_bf16 v[88:91], v[108:111], a[184:191], v6\n"
  "v_add_f32 v73, v73, v75\n"
  "v_cvt_pk_bf16_f32 v74, v72, v73\n"
  "v_smfmac_f32_16x16x64_bf16 v[92:95], v[108:111], a[200:207], v6\n"
  "s_mov_b64 exec, s[38:39]\n"
  "global_store_dword v11, v74, s[70:71] offset:0 sc1\n"
  "s_mov_b64 exec, -1\n"
  "v_add_f32 v80, v80, v82\n"
  "v_add_f32 v81, v81, v83\n"
  "v_cvt_pk_bf16_f32 v82, v80, v81\n"
  "global_store_dword v9, v82, s[70:71] offset:0 sc1\n"
  "v_add_f32 v84, v84, v86\n"
  "v_add_f32 v85, v85, v87\n"
  "v_cvt_pk_bf16_f32 v86, v84, v85\n"
  "global_store_dword v9, v86, s[70:71] offset:128 sc1\n"
  "v_add_f32 v88, v88, v90\n"
  "v_add_f32 v89, v89, v91\n"
  "v_cvt_pk_bf16_f32 v90, v88, v89\n"
  "global_store_dword v10, v90, s[70:71] offset:0 sc1\n"
  "v_add_f32 v92, v92, v94\n"
  "v_add_f32 v93, v93, v95\n"
  "v_cvt_pk_bf16_f32 v94, v92, v93\n"
  "global_store_dword v10, v94, s[70:71] offset:128 sc1\n"
  "s_memtime s[16:17]\n"
  "; BODYEND\n"
  "v_add_f32 v79, v80, v81\n"
  "v_add_f32 v78, v84, v85\n"
  "v_add_f32 v79, v79, v78\n"
  "v_add_f32 v78, v88, v89\n"
  "v_add_f32 v79, v79, v78\n"
  "v_add_f32 v78, v92, v93\n"
  "v_add_f32 v79, v79, v78\n"
  "v_add_f32 v78, v72, v73\n"
  "v_add_f32 v79, v79, v78\n"
  "v_cmp_u_f32 vcc, v79, v79\n"
  "ds_read_b32 v15, v76 offset:33792\n"
  "s_memrealtime s[76:77]\n"
  "v_mov_b64 v[56:57], 0\n"
  "v_mov_b64 v[58:59], 0\n"
  "v_mov_b64 v[60:61], 0\n"
  "v_mov_b64 v[62:63], 0\n"
  "v_mov_b64 v[64:65], 0\n"
  "v_mov_b64 v[66:67], 0\n"
  "v_mov_b64 v[68:69], 0\n"
  "v_mov_b64 v[70:71], 0\n"
  "s_cbranch_vccnz HBADV\n"
  "HOKV:\n"
  "v_mov_b64 v[72:73], 0\n"
  "v_mov_b64 v[74:75], 0\n"
  "v_mov_b32 v14, s33\n"
  "v_add_u32 v76, s26, v0\n"
  "s_waitcnt lgkmcnt(0)\n"
  "v_readlane_b32 s8, v15, 0\n"
  "v_readlane_b32 s9, v15, 2\n"
  "v_readlane_b32 s10, v15, 4\n"
  "v_readlane_b32 s11, v15, 6\n"
  "v_readlane_b32 s12, v15, 8\n"
  "v_readlane_b32 s13, v15, 10\n"
  "v_readlane_b32 s74, v15, 12\n"
  "v_readlane_b32 s75, v15, 14\n"
  "v_readlane_b32 s49, v15, 15\n"
  "s_mov_b64 exec, s[24:25]\n"
  "global_atomic_umax v0, v14, s[66:67] sc1\n"
  "s_mov_b64 exec, -1\n"
  "s_add_u32 s56, s56, 1\n"
  "s_min_u32 s74, s74, s75\n"
  "s_sub_u32 s17, 2, s52\n"
  "s_cmp_le_u32 s74, s27\n"
  "s_cbranch_scc1 HKW\n"
  "s_add_u32 s75, s27, 3\n"
  "s_cmp_ge_u32 s74, s75\n"
  "s_cselect_b32 s75, s17, 0\n"
  "s_cmp_lg_u32 s75, 0\n"
  "s_cbranch_scc1 HFAR\n"
  "HKOK:\n"
  "s_add_u32 m0, s26, 33792\n"
  "s_mov_b32 exec_lo, 0\n"
  "s_and_b32 exec_hi, s98, 0x01010000\n"
  "global_load_lds_dword v0, s[18:19] sc1\n"
  "s_mov_b64 exec, -1\n"
  "s_sub_u32 s78, s56, 1\n"
  "s_max_u32 s8, s8, s9\n"
  "s_max_u32 s10, s10, s11\n"
  "s_max_u32 s8, s8, s10\n"
  "s_max_u32 s12, s12, s13\n"
  "s_cmp_eq_u32 s8, 0\n"
  "s_cselect_b32 s8, s14, s8\n"
  "s_sub_i32 s75, s8, s55\n"
  "s_min_i32 s75, s75, 24\n"
  "s_max_i32 s75, s75, -6\n"
  "s_add_u32 s55, s55, s75\n"
  "s_min_u32 s75, s8, s14\n"
  "s_max_u32 s55, s55, s75\n"
  "s_max_u32 s75, s8, s14\n"
  "s_add_u32 s75, s75, 40\n"
  "s_min_u32 s55, s55, s75\n"
  "s_mov_b32 s14, s8\n"
  "s_cmp_eq_u32 s12, 0\n"
  "s_cselect_b32 s12, s15, s12\n"
  "s_sub_i32 s75, s12, s51\n"
  "s_min_i32 s75, s75, 24\n"
  "s_max_i32 s75, s75, -6\n"
  "s_add_u32 s51, s51, s75\n"
  "s_min_u32 s75, s12, s15\n"
  "s_max_u32 s51, s51, s75\n"
  "s_max_u32 s75, s12, s15\n"
  "s_add_u32 s75, s75, 40\n"
  "s_min_u32 s51, s51, s75\n"
  "s_mov_b32 s15, s12\n"
  "s_min_u32 s75, s78, 31\n"
  "s_lshr_b32 s75, 700, s75\n"
  "s_add_u32 s74, s55, s75\n"
  "s_add_u32 s74, s74, 130\n"
  "s_add_u32 s50, s74, s51\n"
  "s_add_u32 s50, s50, s75\n"
  "s_add_u32 s50, s50, 230\n"
  "s_mov_b32 s33, -1\n"
  "s_add_u32 s53, s53, 65536\n"
  "s_add_u32 s57, s57, 1\n"
  "s_cmp_eq_u32 s57, 3\n"
  "s_cselect_b32 s57, 0, s57\n"
  "s_mul_i32 s99, s76, 24\n"
  "s_add_u32 s49, s49, s43\n"
  "s_sub_u32 s6, s49, s35\n"
  "s_mov_b32 s35, 0\n"
  "s_add_u32 s49, s49, s50\n"
  "s_mov_b32 s32, 0\n"
  "s_sub_i32 s74, s49, s99\n"
  "s_cmp_ge_i32 s74, 500\n"
  "s_cbranch_scc1 HTRY\n"
  "s_mov_b32 s32, 1\n"
  "s_add_u32 s99, s99, 680\n"
  "HSK:\n"
  "s_add_u32 s49, s49, 2200\n"
  "s_sub_i32 s74, s49, s99\n"
  "s_cmp_lt_i32 s74, 0\n"
  "s_cbranch_scc1 HSK\n"
  "s_branch HTRY\n"
  "HBADM:\n"
  "s_waitcnt lgkmcnt(0)\n"
  "s_nop 3\n"
  "v_mov_b32 v1, v1\n"
  "s_nop 2\n"
  "v_mov_b32 v1, v1\n"
  "s_nop 2\n"
  "s_mov_b32 s79, 0xff820000\n"
  "v_max3_u32 v78, v20, v21, v22\n"
  "v_max3_u32 v78, v78, v23, v24\n"
  "v_max3_u32 v78, v78, v25, v26\n"
  "v_max3_u32 v78, v78, v27, v28\n"
  "v_max3_u32 v78, v78, v29, v30\n"
  "v_max3_u32 v78, v78, v31, v32\n"
  "v_max3_u32 v78, v78, v33, v34\n"
  "v_max3_u32 v78, v78, v35, v36\n"
  "v_max3_u32 v78, v78, v37, v38\n"
  "v_max3_u32 v78, v78, v39, v40\n"
  "v_max3_u32 v78, v78, v41, v42\n"
  "v_max3_u32 v78, v78, v43, v44\n"
  "v_max3_u32 v78, v78, v45, v46\n"
  "v_max3_u32 v78, v78, v47, v48\n"
  "v_max3_u32 v78, v78, v49, v50\n"
  "v_max3_u32 v78, v78, v51, v52\n"
  "v_max3_u32 v78, v78, v53, v54\n"
  "v_max_u32 v78, v78, v55\n"
  "v_cmp_le_u32 vcc, s79, v78\n"
  "s_cbranch_vccnz HBM2\n"
  "s_mov_b32 s79, 0x7fff7fff\n"
  "v_and_b32 v78, s79, v20\n"
  "v_and_b32 v12, s79, v21\n"
  "v_pk_max_u16 v78, v78, v12\n"
  "v_and_b32 v12, s79, v22\n"
  "v_pk_max_u16 v78, v78, v12\n"
  "v_and_b32 v12, s79, v23\n"
  "v_pk_max_u16 v78, v78, v12\n"
  "v_and_b32 v12, s79, v24\n"
  "v_pk_max_u16 v78, v78, v12\n"
  "v_and_b32 v12, s79, v25\n"
  "v_pk_max_u16 v78, v78, v12\n"
  "v_and_b32 v12, s79, v26\n"
  "v_pk_max_u16 v78, v78, v12\n"
  "v_and_b32 v12, s79, v27\n"
  "v_pk_max_u16 v78, v78, v12\n"
  "v_and_b32 v12, s79, v28\n"
  "v_pk_max_u16 v78, v78, v12\n"
  "v_and_b32 v12, s79, v29\n"
  "v_pk_max_u16 v78, v78, v12\n"
  "v_and_b32 v12, s79, v30\n"
  "v_pk_max_u16 v78, v78, v12\n"
  "v_and_b32 v12, s79, v31\n"
  "v_pk_max_u16 v78, v78, v12\n"
  "v_and_b32 v12, s79, v32\n"
  "v_pk_max_u16 v78, v78, v12\n"
  "v_and_b32 v12, s79, v33\n"
  "v_pk_max_u16 v78, v78, v12\n"
  "v_and_b32 v12, s79, v34\n"
  "v_pk_max_u16 v78, v78, v12\n"
  "v_and_b32 v12, s79, v35\n"
  "v_pk_max_u16 v78, v78, v12\n"
  "v_and_b32 v12, s79, v36\n"
  "v_pk_max_u16 v78, v78, v12\n"
  "v_and_b32 v12, s79, v37\n"
  "v_pk_max_u16 v78, v78, v12\n"
  "v_and_b32 v12, s79, v38\n"
  "v_pk_max_u16 v78, v78, v12\n"
  "v_and_b32 v12, s79, v39\n"
  "v_pk_max_u16 v78, v78, v12\n"
  "v_and_b32 v12, s79, v40\n"
  "v_pk_max_u16 v78, v78, v12\n"
  "v_and_b32 v12, s79, v41\n"
  "v_pk_max_u16 v78, v78, v12\n"
  "v_and_b32 v12, s79, v42\n"
  "v_pk_max_u16 v78, v78, v12\n"
  "v_and_b32 v12, s79, v43\n"
  "v_pk_max_u16 v78, v78, v12\n"
  "v_and_b32 v12, s79, v44\n"
  "v_pk_max_u16 v78, v78, v12\n"
  "v_and_b32 v12, s79, v45\n"
  "v_pk_max_u16 v78, v78, v12\n"
  "v_and_b32 v12, s79, v46\n"
  "v_pk_max_u16 v78, v78, v12\n"
  "v_and_b32 v12, s79, v47\n"
  "v_pk_max_u16 v78, v78, v12\n"
  "v_and_b32 v12, s79, v48\n"
  "v_pk_max_u16 v78, v78, v12\n"
  "v_and_b32 v12, s79, v49\n"
  "v_pk_max_u16 v78, v78, v12\n"
  "v_and_b32 v12, s79, v50\n"
  "v_pk_max_u16 v78, v78, v12\n"
  "v_and_b32 v12, s79, v51\n"
  "v_pk_max_u16 v78, v78, v12\n"
  "v_and_b32 v12, s79, v52\n"
  "v_pk_max_u16 v78, v78, v12\n"
  "v_and_b32 v12, s79, v53\n"
  "v_pk_max_u16 v78, v78, v12\n"
  "v_and_b32 v12, s79, v54\n"
  "v_pk_max_u16 v78, v78, v12\n"
  "v_and_b32 v12, s79, v55\n"
  "v_pk_max_u16 v78, v78, v12\n"
  "v_lshrrev_b32 v12, 16, v78\n"
  "v_and_b32 v78, 0xffff, v78\n"
  "v_max_u32 v78, v78, v12\n"
  "v_cmp_lt_u32 vcc, 0x7f81, v78\n"
  "s_cbranch_vccz HOKM\n"
  "HBM2:\n"
  "s_memrealtime s[76:77]\n"
  "v_mov_b64 v[56:57], 0\n"
  "v_mov_b64 v[58:59], 0\n"
  "v_mov_b64 v[60:61], 0\n"
  "v_mov_b64 v[62:63], 0\n"
  "v_mov_b64 v[64:65], 0\n"
  "v_mov_b64 v[66:67], 0\n"
  "v_mov_b64 v[68:69], 0\n"
  "v_mov_b64 v[70:71], 0\n"
  "s_barrier\n"
  "s_sub_u32 s58, s58, 1\n"
  "s_cbranch_scc1 QUITH\n"
  "s_add_u32 s59, s59, 1\n"
  "s_mov_b32 s32, 1\n"
  "s_waitcnt lgkmcnt(0)\n"
  "s_mul_i32 s99, s76, 24\n"
  "s_add_u32 s99, s99, 480\n"
  "s_branch HSK\n"
  "HKW:\n"
  "s_waitcnt vmcnt(0) lgkmcnt(0)\n"
  "s_nop 3\n"
  "v_mov_b32 v1, v1\n"
  "s_nop 2\n"
  "v_mov_b32 v1, v1\n"
  "s_nop 2\n"
  "s_add_u32 s74, s36, s2\n"
  "s_addc_u32 s75, s37, 0\n"
  "PLHK2:\n"
  "v_and_b32 v12, 128, v0\n"
  "global_load_dword v12, v12, s[74:75] sc1\n"
  "s_waitcnt vmcnt(0)\n"
  "v_readfirstlane_b32 s76, v12\n"
  "v_readlane_b32 s77, v12, 8\n"
  "s_min_u32 s76, s76, s77\n"
  "s_cmp_gt_u32 s76, s27\n"
  "s_cbranch_scc1 PDHK2\n"
  "s_sub_u32 s58, s58, 1\n"
  "s_cbranch_scc1 QUITH\n"
  "s_sleep 2\n"
  "s_branch PLHK2\n"
  "PDHK2:\n"
  "s_add_u32 s59, s59, 0x100\n"
  "s_lshl_b32 s17, s17, 1\n"
  "global_load_lds_dwordx4 v0, s[40:41] offset:0 sc1\n"
  "global_load_lds_dwordx4 v0, s[40:41] offset:1024 sc1\n"
  "global_load_lds_dwordx4 v0, s[40:41] offset:2048 sc1\n"
  "global_load_lds_dwordx4 v0, s[40:41] offset:3072 sc1\n"
  "s_memrealtime s[76:77]\n"
  "s_waitcnt lgkmcnt(0)\n"
  "s_branch HKOK\n"
  "HFAR:\n"
  "s_add_u32 s78, s57, 2\n"
  "s_cmp_ge_u32 s78, 3\n"
  "s_cselect_b32 s75, 3, 0\n"
  "s_sub_u32 s78, s78, s75\n"
  "s_mul_i32 s78, s78, 17408\n"
  "s_add_u32 m0, s42, s78\n"
  "s_add_u32 s27, s27, 1\n"
  "s_bitcmp1_b64 s[4:5], s27\n"
  "s_cselect_b32 s74, 8192, 0\n"
  "s_and_b32 s75, s27, 63\n"
  "s_lshl_b32 s75, s75, 21\n"
  "s_add_u32 s74, s74, s75\n"
  "s_add_u32 s74, s74, 20971520\n"
  "s_xor_b32 s75, s74, s47\n"
  "s_add_u32 s40, s36, s75\n"
  "s_addc_u32 s41, s37, 0\n"
  "s_mov_b32 s52, 2\n"
  "global_load_lds_dwordx4 v0, s[40:41] offset:0 sc1\n"
  "global_load_lds_dwordx4 v0, s[40:41] offset:1024 sc1\n"
  "global_load_lds_dwordx4 v0, s[40:41] offset:2048 sc1\n"
  "global_load_lds_dwordx4 v0, s[40:41] offset:3072 sc1\n"
  "s_branch HKOK\n"
  "HBADV:\n"
  "s_waitcnt vmcnt(0) lgkmcnt(0)\n"
  "s_nop 3\n"
  "v_mov_b32 v1, v1\n"
  "s_nop 2\n"
  "v_mov_b32 v1, v1\n"
  "s_nop 2\n"
  "s_mov_b32 s74, 0x7fff7fff\n"
  "v_and_b32 v79, s74, v36\n"
  "v_and_b32 v78, s74, v37\n"
  "v_pk_max_u16 v79, v79, v78\n"
  "v_and_b32 v78, s74, v38\n"
  "v_pk_max_u16 v79, v79, v78\n"
  "v_and_b32 v78, s74, v39\n"
  "v_pk_max_u16 v79, v79, v78\n"
  "v_and_b32 v78, s74, v40\n"
  "v_pk_max_u16 v79, v79, v78\n"
  "v_and_b32 v78, s74, v41\n"
  "v_pk_max_u16 v79, v79, v78\n"
  "v_and_b32 v78, s74, v42\n"
  "v_pk_max_u16 v79, v79, v78\n"
  "v_and_b32 v78, s74, v43\n"
  "v_pk_max_u16 v79, v79, v78\n"
  "v_lshrrev_b32 v78, 16, v79\n"
  "v_and_b32 v79, 0xffff, v79\n"
  "v_max_u32 v79, v79, v78\n"
  "v_cmp_lt_u32 vcc, 0x7f81, v79\n"
  "s_cbranch_vccnz HVL\n"
  "s_mov_b32 s74, 0x7fff7fff\n"
  "s_mov_b32 s75, 0x7f817f81\n"
  "v_and_b32 v79, s74, v82\n"
  "v_pk_min_u16 v79, v79, s75\n"
  "v_bfi_b32 v82, s74, v79, v82\n"
  "v_and_b32 v79, s74, v86\n"
  "v_pk_min_u16 v79, v79, s75\n"
  "v_bfi_b32 v86, s74, v79, v86\n"
  "v_and_b32 v79, s74, v90\n"
  "v_pk_min_u16 v79, v79, s75\n"
  "v_bfi_b32 v90, s74, v79, v90\n"
  "v_and_b32 v79, s74, v94\n"
  "v_pk_min_u16 v79, v79, s75\n"
  "v_bfi_b32 v94, s74, v79, v94\n"
  "v_and_b32 v79, s74, v74\n"
  "v_pk_min_u16 v79, v79, s75\n"
  "v_bfi_b32 v74, s74, v79, v74\n"
  "s_mov_b64 exec, s[38:39]\n"
  "global_store_dword v11, v74, s[70:71] offset:0 sc1\n"
  "s_mov_b64 exec, -1\n"
  "global_store_dword v9, v82, s[70:71] offset:0 sc1\n"
  "global_store_dword v9, v86, s[70:71] offset:128 sc1\n"
  "global_store_dword v10, v90, s[70:71] offset:0 sc1\n"
  "global_store_dword v10, v94, s[70:71] offset:128 sc1\n"
  "s_branch HOKV\n"
  "HVL:\n"
  "s_sub_u32 s58, s58, 1\n"
  "s_cbranch_scc1 QUITH\n"
  "s_add_u32 s59, s59, 0x10000\n"
  "s_mov_b32 s35, 0x100000\n"
  "global_load_dwordx4 v[36:39], v2, s[68:69] offset:0 sc1\n"
  "global_load_dwordx4 v[40:43], v2, s[68:69] offset:1024 sc1\n"
  "s_mov_b32 s74, 0x7fff7fff\n"
  "s_waitcnt vmcnt(1)\n"
  "v_and_b32 v79, s74, v36\n"
  "v_and_b32 v78, s74, v37\n"
  "v_pk_max_u16 v79, v79, v78\n"
  "v_and_b32 v78, s74, v38\n"
  "v_pk_max_u16 v79, v79, v78\n"
  "v_and_b32 v78, s74, v39\n"
  "v_pk_max_u16 v79, v79, v78\n"
  "s_waitcnt vmcnt(0)\n"
  "v_and_b32 v78, s74, v40\n"
  "v_pk_max_u16 v79, v79, v78\n"
  "v_and_b32 v78, s74, v41\n"
  "v_pk_max_u16 v79, v79, v78\n"
  "v_and_b32 v78, s74, v42\n"
  "v_pk_max_u16 v79, v79, v78\n"
  "v_and_b32 v78, s74, v43\n"
  "v_pk_max_u16 v79, v79, v78\n"
  "v_lshrrev_b32 v78, 16, v79\n"
  "v_and_b32 v79, 0xffff, v79\n"
  "v_max_u32 v79, v79, v78\n"
  "v_cmp_lt_u32 vcc, 0x7f81, v79\n"
  "s_cbranch_vccz HREDO\n"
  "s_sleep 4\n"
  "s_branch HVL\n"
  "HREDO:\n"
  "v_mov_b64 v[80:81], 0\n"
  "v_mov_b64 v[82:83], 0\n"
  "v_mov_b64 v[84:85], 0\n"
  "v_mov_b64 v[86:87], 0\n"
  "v_mov_b64 v[88:89], 0\n"
  "v_mov_b64 v[90:91], 0\n"
  "v_mov_b64 v[92:93], 0\n"
  "v_mov_b64 v[94:95], 0\n"
  "v_mov_b64 v[72:73], 0\n"
  "v_mov_b64 v[74:75], 0\n"
  "v_mfma_f32_16x16x32_bf16 v[68:71], v[16:19], v[44:47], 0\n"
  "v_mfma_f32_16x16x32_bf16 v[20:23], a[224:227], v[44:47], 0\n"
  "v_mfma_f32_16x16x32_bf16 v[68:71], v[16:19], v[48:51], v[68:71]\n"
  "v_mfma_f32_16x16x32_bf16 v[20:23], a[228:231], v[48:51], v[20:23]\n"
  "v_mfma_f32_16x16x32_bf16 v[24:27], a[232:235], v[44:47], 0\n"
  "v_mfma_f32_16x16x32_bf16 v[24:27], a[236:239], v[48:51], v[24:27]\n"
  "v_mfma_f32_16x16x32_bf16 v[28:31], a[240:243], v[44:47], 0\n"
  "s_nop 7\n"
  "s_branch HPV\n"
  "HBADZ:\n"
  "s_waitcnt vmcnt(0) lgkmcnt(0)\n"
  "s_nop 3\n"
  "v_mov_b32 v1, v1\n"
  "s_nop 2\n"
  "v_mov_b32 v1, v1\n"
  "s_nop 2\n"
  "s_cmp_eq_u32 s81, s56\n"
  "s_cbranch_scc1 HOKZ\n"
  "s_mov_b32 s81, s56\n"
  "ds_read_b128 v[80:83], v76 offset:17408\n"
  "ds_read_b128 v[84:87], v76 offset:18432\n"
  "ds_read_b128 v[88:91], v76 offset:19456\n"
  "ds_read_b128 v[92:95], v76 offset:20480\n"
  "ds_read_b128 v[96:99], v76 offset:21504\n"
  "ds_read_b128 v[100:103], v76 offset:22528\n"
  "ds_read_b128 v[104:107], v76 offset:23552\n"
  "ds_read_b128 v[108:111], v76 offset:24576\n"
  "ds_read_b64_tr_b16 v[20:21], v5 offset:0\n"
  "ds_read_b64_tr_b16 v[22:23], v5 offset:512\n"
  "ds_read_b64_tr_b16 v[24:25], v5 offset:1024\n"
  "ds_read_b64_tr_b16 v[26:27], v5 offset:1536\n"
  "ds_read_b64_tr_b16 v[28:29], v5 offset:2048\n"
  "ds_read_b64_tr_b16 v[30:31], v5 offset:2560\n"
  "ds_read_b64_tr_b16 v[32:33], v5 offset:3072\n"
  "ds_read_b64_tr_b16 v[34:35], v5 offset:3584\n"
  "ds_read_b64_tr_b16 v[44:45], v5 offset:4096\n"
  "ds_read_b64_tr_b16 v[46:47], v5 offset:4608\n"
  "ds_read_b64_tr_b16 v[48:49], v5 offset:5120\n"
  "ds_read_b64_tr_b16 v[50:51], v5 offset:5632\n"
  "s_waitcnt lgkmcnt(10)\n"
  "v_mfma_f32_16x16x32_bf16 v[56:59], v[80:83], v[20:23], 0\n"
  "s_waitcnt lgkmcnt(8)\n"
  "v_mfma_f32_16x16x32_bf16 v[56:59], v[88:91], v[24:27], v[56:59]\n"
  "s_waitcnt lgkmcnt(6)\n"
  "v_mfma_f32_16x16x32_bf16 v[56:59], v[96:99], v[28:31], v[56:59]\n"
  "s_waitcnt lgkmcnt(4)\n"
  "v_mfma_f32_16x16x32_bf16 v[56:59], v[104:107], v[32:35], v[56:59]\n"
  "v_mfma_f32_16x16x32_bf16 v[60:63], v[84:87], v[20:23], 0\n"
  "v_mfma_f32_16x16x32_bf16 v[60:63], v[92:95], v[24:27], v[60:63]\n"
  "v_mfma_f32_16x16x32_bf16 v[60:63], v[100:103], v[28:31], v[60:63]\n"
  "v_mfma_f32_16x16x32_bf16 v[60:63], v[108:111], v[32:35], v[60:63]\n"
  "ds_read_b64_tr_b16 v[96:97], v5 offset:6144\n"
  "ds_read_b64_tr_b16 v[98:99], v5 offset:6656\n"
  "ds_read_b64_tr_b16 v[100:101], v5 offset:7168\n"
  "ds_read_b64_tr_b16 v[102:103], v5 offset:7680\n"
  "s_waitcnt lgkmcnt(2)\n"
  "v_mfma_f32_16x16x32_bf16 v[52:55], v[96:99], v[44:47], 0\n"
  "s_waitcnt lgkmcnt(0)\n"
  "v_mfma_f32_16x16x32_bf16 v[52:55], v[100:103], v[48:51], v[52:55]\n"
  "s_nop 3\n"
  "v_mov_b32 v1, v1\n"
  "s_nop 2\n"
  "v_mul_f32_dpp v64, v52, v77 quad_perm:[0,1,0,1] row_mask:0xf bank_mask:0xf\n"
  "v_mul_f32_dpp v65, v53, v77 quad_perm:[0,1,0,1] row_mask:0xf bank_mask:0xf\n"
  "s_nop 0\n"
  "v_fmac_f32_dpp v64, v54, v77 quad_perm:[2,3,2,3] row_mask:0xf bank_mask:0xf\n"
  "v_fmac_f32_dpp v65, v55, v77 quad_perm:[2,3,2,3] row_mask:0xf bank_mask:0xf\n"
  "v_max3_f32 v79, v56, v57, v58\n"
  "v_max3_f32 v79, v79, v59, v60\n"
  "v_max3_f32 v79, v79, v61, v62\n"
  "v_max3_f32 v79, v79, v63, v64\n"
  "v_max_f32 v79, v79, v65\n"
  "v_mov_b32 v78, v79\n"
  "s_nop 1\n"
  "v_permlane16_swap_b32 v79, v78\n"
  "v_max_f32 v79, v79, v78\n"
  "v_mov_b32 v78, v79\n"
  "s_nop 1\n"
  "v_permlane32_swap_b32 v79, v78\n"
  "v_max_f32 v79, v79, v78\n"
  "v_sub_f32 v56, v56, v79\n"
  "v_sub_f32 v57, v57, v79\n"
  "v_sub_f32 v58, v58, v79\n"
  "v_sub_f32 v59, v59, v79\n"
  "v_sub_f32 v60, v60, v79\n"
  "v_sub_f32 v61, v61, v79\n"
  "v_sub_f32 v62, v62, v79\n"
  "v_sub_f32 v63, v63, v79\n"
  "v_sub_f32 v64, v64, v79\n"
  "v_sub_f32 v65, v65, v79\n"
  "v_exp_f32 v56, v56\n"
  "v_exp_f32 v57, v57\n"
  "v_exp_f32 v58, v58\n"
  "v_exp_f32 v59, v59\n"
  "v_exp_f32 v60, v60\n"
  "v_exp_f32 v61, v61\n"
  "v_exp_f32 v62, v62\n"
  "v_exp_f32 v63, v63\n"
  "v_perm_b32 v44, v57, v56, v7\n"
  "v_perm_b32 v45, v59, v58, v7\n"
  "v_perm_b32 v46, v61, v60, v7\n"
  "v_perm_b32 v47, v63, v62, v7\n"
  "v_perm_b32 v48, v57, v56, v8\n"
  "v_perm_b32 v49, v59, v58, v8\n"
  "v_perm_b32 v50, v61, v60, v8\n"
  "v_perm_b32 v51, v63, v62, v8\n"
  "v_exp_f32 v64, v64\n"
  "v_exp_f32 v65, v65\n"
  "s_nop 0\n"
  "v_perm_b32 v12, v65, v64, v7\n"
  "v_perm_b32 v13, v65, v64, v8\n"
  "s_nop 1\n"
  "s_branch HREDO\n"
  "ENDH:\n"
  "s_waitcnt vmcnt(0) lgkmcnt(0)\n"
  "s_barrier\n"
  "v_mov_b32 v78, 64\n"
  "s_mov_b64 exec, s[22:23]\n"
  "global_store_dword v0, v78, s[64:65] sc1\n"
  "QUITH:\n"
  "s_endpgm\n"
        : "+{v[0:15]}"(vin), "+{s[36:43]}"(sa), "+{s[44:59]}"(sb) :: CLOB);
    } else { ARGS; PTR(sa, 0, p.ws);
      const int ci = tk - 16, cv = gam * 16 + ci, h = cv >> 1, vt = (cv & 1) * 4 + wave;
      v4u iv = {~0u, ~0u, ~0u, ~0u};
      const v4u snt = iv;
      const int line = (2 * ci + (gam & 1)) * 4 + (gam >> 1) * 2 + (g & 1), kb = line >> 3, cp = ((lane >> 3) & 1) + 2 * (lane >> 5);
      if (wave == 1) {
        const unsigned map = (0x7160u >> (4 * (die & 3))) & 7, hf = ci & 1, bit = (ci >> 1) & 1, first = ((ci >> 2) & 1) ^ 1;
        unsigned m[2] = {~0u, ~0u};
        WAIT_VM(0);
#pragma unroll 1
        for (int r = 0; r < 4; ++r) {
          const unsigned sel = (first ^ r ^ (r >> 1)) & 1, off = probe + (((2 * hf + (sel << (2 * bit)) + ((ci >> 3) << (2 - 2 * bit))) ^ map) << 12);
          unsigned v = 0; unsigned long long t0, t1;
          asm volatile("s_mov_b64 exec, 1\n\ts_memtime %1\n\ts_waitcnt lgkmcnt(0)\n\tglobal_atomic_add %0, %3, %0, %4 sc0 sc1\n\ts_waitcnt vmcnt(0)\n\t"
                       "s_memtime %2\n\ts_waitcnt lgkmcnt(0)\n\ts_mov_b64 exec, -1" : "+v"(v), "=&s"(t0), "=&s"(t1) : "v"(off), "s"(p.ws) : "memory");
          const unsigned t = (unsigned)(t1 - t0);
          if (sel) m[1] = min(m[1], t); else m[0] = min(m[0], t);
        }
        if (lane == 0) {
          const unsigned long long mine = (1ull << 56) | ((unsigned long long)min(min(m[0], m[1]) >> 5, 63u) << (32 + 12 * hf)) | ((unsigned long long)(m[1] < m[0]) << (8 * (2 * hf + bit)));
          const unsigned long long v = __hip_atomic_fetch_add((GAS unsigned long long*)(p.ws + C_VOTE), mine, RLX_AGENT) + mine;
          if ((v >> 56) == 64) {
            const unsigned b1 = ((v >> 44) & 4095) < ((v >> 32) & 4095), w = (unsigned)(v >> (16 * b1));
            ctl_st(p.ws, C_CLS, (((((w & 255) > 8) | (b1 << 1) | (((w >> 8 & 255) > 8) << 2)) * 0x1111u) ^ 0x7160u) | 0x40000u);
          }
        }
        STAMP(63, 13);
      }
      wg_barrier();
      if (wave == 0 && ci < 9) {
        const int j = lane & 7, m = 2 * (line & 3) + (j & 1), col = 32 * (line >> 2) + (j >> 1);
#pragma unroll
        for (int e = 0; e < 4; ++e) {
          const unsigned lo = p.mem0[m * DM + col + 8 * e], hi = p.mem0[m * DM + col + 8 * e + 4];
          iv[e] = ((lo & 0x7fff) > 0x7f81 ? 0x7f81 : lo) | (((hi & 0x7fff) > 0x7f81 ? 0x7f81 : hi) << 16);
        }
      }
      {
        const int row = 8 * wave + (lane >> 3);
#pragma unroll 6
        for (int j = 0; j < 18; ++j) dma(p.Wv + cv * 64 + (size_t)j * 32 * 4096, row * 8192 + ((lane & 7) ^ (2 * (((row >> 2) & 1) | ((row >> 3) & 2)))) * 16, wave * 1024 + j * 4096);
      }
      if (!wait_ge(p.ws, C_CLS, 0x40000)) return;
      const unsigned cls = rfl(ctl_ld(p.ws, C_CLS)), cx = (cls & 2) << 12;
      const unsigned ms = chunk(cls, cp, kb < 8 ? (kb >> 2) : 4, (kb < 8 ? (kb & 3) : 0) * 1024 + (line & 7) * 128 + (lane & 7) * 16);
      const unsigned ms2 = ms + (kb < 8 ? 2 * CH : 1024), inb = chunk(cls, gam, 28 + 4 * ci, 0);
      const unsigned m2 = chunk(cls, gam, 16 + ((h & 7) >> 1), (h & 1) * 2048 + vt * 256);
      if (ci < 9) {
        if (wave < 3) { st16(p.ws, ms + wave * 5 * CH, iv); st16(p.ws, ms2 + wave * 5 * CH, iv); }
#pragma unroll
        for (int e = 0; e < 4; ++e) st16(p.ws, inb + e * CH + wave * 1024 + lane * 16, snt);
      }
      if (lane < 48) st16(p.ws, m2 + g * 4 * CH + n * 16, snt);
      STAMP(63, 14);
      vin[1] = m1pat; vin[2] = lane * 4; vin[3] = ms; vin[4] = idx;
      vin[6] = trrow * 128 + ((2 * wave + (c4 >> 1)) ^ (2 * (((trrow >> 2) & 1) | ((trrow >> 3) & 2)))) * 16 + (c4 & 1) * 8;
      vin[7] = line * 128 + (lane & 7) * 16;
#pragma unroll
      for (int i = 0; i < 4; ++i) vin[8 + i] = ((lane & 7) >> 1) == i ? ((lane & 1) ? 0x3f800000u : 0x3f80u) : 0u;
      vin[12] = c4 * 18432 + (16 * g + 4 * wave + qq) * 16; vin[13] = ms2;
      sa[2] = (C_VF ^ cx) + cv * 128; sa[3] = C_REP ^ cx; sb[5] = gam * 128; sb[6] = cv == 15 && wave == 0 ? C_KVP ^ cx : 0; sb[7] = 2200; sa[5] = 512 + (gam & 1) * 128; sa[6] = 2500; sa[7] = 720;
      sb[0] = chunk(cls, gam, 0, 0) + (wave - 1) * 1024;
      sb[1] = wave == 3 ? chunk(cls, gam, 4, 0) : chunk(cls, gam, 1, (wave + 1) * 1024);
      sb[2] = m2; sb[3] = WO_VP + h * 2048 + vt * 256;
      sb[9] = inb; sb[12] = ci < 9 ? 1 + wave : 0; sb[13] = wave == 1 ? 3072 : CH - 1024; sb[14] = WO_DB;
      UNIFORM(sa, 8); UNIFORM(sb, 16);
      asm volatile(
  "s_nop 4\n"
  "s_lshl_b32 s80, s48, 10\n"
  "s_waitcnt vmcnt(0)\n"
  "s_barrier\n"
  "v_add_u32 v60, 0x8000, v6\n"
  "ds_read_b64_tr_b16 v[96:97], v6 offset:0\n"
  "ds_read_b64_tr_b16 v[98:99], v6 offset:1024\n"
  "ds_read_b64_tr_b16 v[100:101], v6 offset:4096\n"
  "ds_read_b64_tr_b16 v[102:103], v6 offset:5120\n"
  "ds_read_b64_tr_b16 v[104:105], v6 offset:8192\n"
  "ds_read_b64_tr_b16 v[106:107], v6 offset:9216\n"
  "ds_read_b64_tr_b16 v[108:109], v6 offset:12288\n"
  "ds_read_b64_tr_b16 v[110:111], v6 offset:13312\n"
  "ds_read_b64_tr_b16 v[112:113], v6 offset:16384\n"
  "ds_read_b64_tr_b16 v[114:115], v6 offset:17408\n"
  "ds_read_b64_tr_b16 v[116:117], v6 offset:20480\n"
  "ds_read_b64_tr_b16 v[118:119], v6 offset:21504\n"
  "ds_read_b64_tr_b16 v[120:121], v6 offset:24576\n"
  "ds_read_b64_tr_b16 v[122:123], v6 offset:25600\n"
  "ds_read_b64_tr_b16 v[124:125], v6 offset:28672\n"
  "ds_read_b64_tr_b16 v[126:127], v6 offset:29696\n"
  "ds_read_b64_tr_b16 v[128:129], v60 offset:0\n"
  "ds_read_b64_tr_b16 v[130:131], v60 offset:1024\n"
  "ds_read_b64_tr_b16 v[132:133], v60 offset:4096\n"
  "ds_read_b64_tr_b16 v[134:135], v60 offset:5120\n"
  "ds_read_b64_tr_b16 v[136:137], v60 offset:8192\n"
  "ds_read_b64_tr_b16 v[138:139], v60 offset:9216\n"
  "ds_read_b64_tr_b16 v[140:141], v60 offset:12288\n"
  "ds_read_b64_tr_b16 v[142:143], v60 offset:13312\n"
  "ds_read_b64_tr_b16 v[144:145], v60 offset:16384\n"
  "ds_read_b64_tr_b16 v[146:147], v60 offset:17408\n"
  "ds_read_b64_tr_b16 v[148:149], v60 offset:20480\n"
  "ds_read_b64_tr_b16 v[150:151], v60 offset:21504\n"
  "ds_read_b64_tr_b16 v[152:153], v60 offset:24576\n"
  "ds_read_b64_tr_b16 v[154:155], v60 offset:25600\n"
  "ds_read_b64_tr_b16 v[156:157], v60 offset:28672\n"
  "ds_read_b64_tr_b16 v[158:159], v60 offset:29696\n"
  "ds_read_b64_tr_b16 v[160:161], v60 offset:32768\n"
  "ds_read_b64_tr_b16 v[162:163], v60 offset:33792\n"
  "ds_read_b64_tr_b16 v[164:165], v60 offset:36864\n"
  "ds_read_b64_tr_b16 v[166:167], v60 offset:37888\n"
  "v_mov_b32 v64, -1\n"
  "v_mov_b32 v65, -1\n"
  "v_mov_b32 v66, -1\n"
  "v_mov_b32 v67, -1\n"
  "s_mov_b32 s7, s41\n"
  "s_mov_b32 s14, s42\n"
  "s_mov_b32 s15, s43\n"
  "s_mov_b32 s6, 0\n"
  "s_mov_b32 s16, s42\n"
  "s_mov_b32 s17, s43\n"
  "s_mov_b32 s40, 0\n"
  "s_mov_b32 s41, 0\n"
  "s_mov_b32 s42, 100000\n"
  "s_mov_b32 s43, 0\n"
  "s_waitcnt lgkmcnt(0)\n"
  "s_barrier\n"
  "s_mul_i32 s74, s48, 18432\n"
  "v_add_u32 v60, s74, v0\n"
  "ds_write_b128 v60, v[96:99] offset:0\n"
  "ds_write_b128 v60, v[100:103] offset:1024\n"
  "ds_write_b128 v60, v[104:107] offset:2048\n"
  "ds_write_b128 v60, v[108:111] offset:3072\n"
  "ds_write_b128 v60, v[112:115] offset:4096\n"
  "ds_write_b128 v60, v[116:119] offset:5120\n"
  "ds_write_b128 v60, v[120:123] offset:6144\n"
  "ds_write_b128 v60, v[124:127] offset:7168\n"
  "ds_write_b128 v60, v[128:131] offset:8192\n"
  "ds_write_b128 v60, v[132:135] offset:9216\n"
  "ds_write_b128 v60, v[136:139] offset:10240\n"
  "ds_write_b128 v60, v[140:143] offset:11264\n"
  "ds_write_b128 v60, v[144:147] offset:12288\n"
  "ds_write_b128 v60, v[148:151] offset:13312\n"
  "ds_write_b128 v60, v[152:155] offset:14336\n"
  "ds_write_b128 v60, v[156:159] offset:15360\n"
  "ds_write_b128 v60, v[160:163] offset:16384\n"
  "ds_write_b128 v60, v[164:167] offset:17408\n"
  "s_waitcnt lgkmcnt(0)\n"
  "s_barrier\n"
  "ds_read_b128 v[96:99], v12 offset:0\n"
  "ds_read_b128 v[100:103], v12 offset:1024\n"
  "ds_read_b128 v[104:107], v12 offset:2048\n"
  "ds_read_b128 v[108:111], v12 offset:3072\n"
  "ds_read_b128 v[112:115], v12 offset:4096\n"
  "ds_read_b128 v[116:119], v12 offset:5120\n"
  "ds_read_b128 v[120:123], v12 offset:6144\n"
  "ds_read_b128 v[124:127], v12 offset:7168\n"
  "ds_read_b128 v[128:131], v12 offset:8192\n"
  "ds_read_b128 v[132:135], v12 offset:9216\n"
  "ds_read_b128 v[136:139], v12 offset:10240\n"
  "ds_read_b128 v[140:143], v12 offset:11264\n"
  "ds_read_b128 v[144:147], v12 offset:12288\n"
  "ds_read_b128 v[148:151], v12 offset:13312\n"
  "ds_read_b128 v[152:155], v12 offset:14336\n"
  "ds_read_b128 v[156:159], v12 offset:15360\n"
  "ds_read_b128 v[160:163], v12 offset:16384\n"
  "ds_read_b128 v[164:167], v12 offset:17408\n"
  "s_waitcnt lgkmcnt(0)\n"
  "s_barrier\n"
  "s_add_u32 s76, s39, s49\n"
  "s_add_u32 s76, s76, 137216\n"
  "s_add_u32 s74, s36, s76\n"
  "s_addc_u32 s75, s37, 0\n"
  "v_mov_b32 v60, 1\n"
  "s_cmp_eq_u32 s48, 0\n"
  "s_cselect_b64 exec, 1, 0\n"
  "global_atomic_add v0, v60, s[74:75] sc1\n"
  "s_mov_b64 exec, -1\n"
  "s_cmp_eq_u32 s50, 0\n"
  "s_cbranch_scc1 RCD\n"
  "s_add_u32 s62, s36, s50\n"
  "s_addc_u32 s63, s37, 0\n"
  "s_add_u32 s74, s39, 137216\n"
  "s_add_u32 s64, s36, s74\n"
  "s_addc_u32 s65, s37, 0\n"
  "v_lshlrev_b32 v56, 3, v0\n"
  "v_and_b32 v57, 0x180, v56\n"
  "RCP:\n"
  "global_load_dword v60, v56, s[62:63] sc1\n"
  "global_load_dword v61, v57, s[64:65] sc1\n"
  "s_waitcnt vmcnt(0)\n"
  "v_lshrrev_b32 v61, 5, v61\n"
  "v_min_u32 v60, v60, v61\n"
  "v_cmp_eq_u32 vcc, 0, v60\n"
  "s_cbranch_vccz RCG\n"
  "s_sub_u32 s42, s42, 1\n"
  "s_cbranch_scc1 QUITV\n"
  "s_branch RCP\n"
  "RCG:\n"
  "s_memrealtime s[74:75]\n"
  "s_waitcnt lgkmcnt(0)\n"
  "s_mul_i32 s76, s74, 24\n"
  "s_add_u32 s76, s76, 1500\n"
  "s_or_b32 s76, s76, 1\n"
  "v_mov_b32 v60, s76\n"
  "s_mov_b64 exec, 15\n"
  "global_store_dword v57, v60, s[64:65] offset:1024 sc1\n"
  "s_mov_b64 exec, -1\n"
  "RCD:\n"
  "s_add_u32 s77, s39, s49\n"
  "s_add_u32 s77, s77, 138240\n"
  "s_add_u32 s74, s36, s77\n"
  "s_addc_u32 s75, s37, 0\n"
  "PLGV:\n"
  "v_mov_b32 v60, 0\n"
  "global_load_dword v60, v60, s[74:75] sc1\n"
  "s_waitcnt vmcnt(0)\n"
  "v_readfirstlane_b32 s76, v60\n"
  "s_cmp_lg_u32 s76, 0\n"
  "s_cbranch_scc1 PDGV\n"
  "s_sub_u32 s42, s42, 1\n"
  "s_cbranch_scc1 QUITV\n"
  "s_sleep 1\n"
  "s_branch PLGV\n"
  "PDGV:\n"
  "s_mov_b32 s49, s76\n"
  "v_mov_b32 v60, 0\n"
  "v_mov_b32 v61, s49\n"
  "ds_write_b32 v60, v61 offset:9232\n"
  "s_sub_u32 s80, s80, 1024\n"
  "RLOOP:\n"
  "s_mov_b32 s52, 0\n"
  "s_mov_b32 s98, s49\n"
  "RTRY:\n"
  "v_mov_b64 v[56:57], 0\n"
  "v_mov_b64 v[58:59], 0\n"
  "s_mul_i32 s74, s41, 131072\n"
  "s_add_u32 s74, s74, s46\n"
  "s_add_u32 s68, s36, s74\n"
  "s_addc_u32 s69, s37, 0\n"
  "v_mov_b32 v61, 0\n"
  "s_cmp_eq_u32 s48, 0\n"
  "s_cbranch_scc1 NDV\n"
  "s_mul_i32 s78, s41, 163840\n"
  "s_lshl_b32 s79, s52, 16\n"
  "s_add_u32 s79, s79, s78\n"
  "s_add_u32 s79, s79, s44\n"
  "s_add_u32 s62, s36, s79\n"
  "s_addc_u32 s63, s37, 0\n"
  "s_add_u32 s64, s62, s57\n"
  "s_addc_u32 s65, s63, 0\n"
  "s_add_u32 s79, s78, s45\n"
  "s_cmp_eq_u32 s48, 3\n"
  "s_cselect_b32 s74, 10, 16\n"
  "s_lshl_b32 s74, s52, s74\n"
  "s_add_u32 s79, s79, s74\n"
  "s_add_u32 s66, s36, s79\n"
  "s_addc_u32 s67, s37, 0\n"
  "s_add_u32 m0, s80, 0\n"
  "s_add_u32 s79, s80, 3072\n"
  "s_add_u32 s78, s80, 6144\n"
  "s_add_u32 s81, s49, 0\n"
  "WV:\n"
  "s_memrealtime s[74:75]\n"
  "s_waitcnt lgkmcnt(0)\n"
  "s_mul_i32 s74, s74, 24\n"
  "s_sub_i32 s74, s81, s74\n"
  "s_cmp_lt_i32 s74, 750\n"
  "s_cbranch_scc1 WFV\n"
  "s_cmp_gt_i32 s74, 0x40000\n"
  "s_cbranch_scc1 WEV\n"
  "s_sleep 2\n"
  "s_branch WV\n"
  "WFV:\n"
  "s_cmp_lt_i32 s74, 100\n"
  "s_cbranch_scc1 WEV\n"
  "WGV:\n"
  "s_memtime s[76:77]\n"
  "s_memrealtime s[20:21]\n"
  "s_nop 2\n"
  "s_memrealtime s[22:23]\n"
  "s_nop 2\n"
  "s_memrealtime s[24:25]\n"
  "s_nop 2\n"
  "s_memrealtime s[26:27]\n"
  "s_nop 2\n"
  "s_memrealtime s[28:29]\n"
  "s_nop 2\n"
  "s_memrealtime s[30:31]\n"
  "s_waitcnt lgkmcnt(0)\n"
  "s_add_u32 s20, s20, s22\n"
  "s_add_u32 s24, s24, s26\n"
  "s_add_u32 s28, s28, s30\n"
  "s_add_u32 s20, s20, s24\n"
  "s_add_u32 s20, s20, s28\n"
  "s_lshl_b32 s74, s20, 2\n"
  "s_nop 0\n"
  "s_sub_i32 s74, s81, s74\n"
  "s_sub_i32 s74, s74, 238\n"
  "s_max_i32 s74, s74, 0\n"
  "s_cmp_ge_i32 s74, 512\n"
  "s_cbranch_scc1 WV\n"
  "s_lshr_b32 s74, s74, 3\n"
  "s_lshl_b32 s74, s74, 2\n"
  "s_sub_i32 s74, 268, s74\n"
  "s_getpc_b64 s[76:77]\n"
  "s_add_u32 s76, s76, s74\n"
  "s_addc_u32 s77, s77, 0\n"
  "s_setpc_b64 s[76:77]\n"
  "s_nop 1\n"
  "s_nop 1\n"
  "s_nop 1\n"
  "s_nop 1\n"
  "s_nop 1\n"
  "s_nop 1\n"
  "s_nop 1\n"
  "s_nop 1\n"
  "s_nop 1\n"
  "s_nop 1\n"
  "s_nop 1\n"
  "s_nop 1\n"
  "s_nop 1\n"
  "s_nop 1\n"
  "s_nop 1\n"
  "s_nop 1\n"
  "s_nop 1\n"
  "s_nop 1\n"
  "s_nop 1\n"
  "s_nop 1\n"
  "s_nop 1\n"
  "s_nop 1\n"
  "s_nop 1\n"
  "s_nop 1\n"
  "s_nop 1\n"
  "s_nop 1\n"
  "s_nop 1\n"
  "s_nop 1\n"
  "s_nop 1\n"
  "s_nop 1\n"
  "s_nop 1\n"
  "s_nop 1\n"
  "s_nop 1\n"
  "s_nop 1\n"
  "s_nop 1\n"
  "s_nop 1\n"
  "s_nop 1\n"
  "s_nop 1\n"
  "s_nop 1\n"
  "s_nop 1\n"
  "s_nop 1\n"
  "s_nop 1\n"
  "s_nop 1\n"
  "s_nop 1\n"
  "s_nop 1\n"
  "s_nop 1\n"
  "s_nop 1\n"
  "s_nop 1\n"
  "s_nop 1\n"
  "s_nop 1\n"
  "s_nop 1\n"
  "s_nop 1\n"
  "s_nop 1\n"
  "s_nop 1\n"
  "s_nop 1\n"
  "s_nop 1\n"
  "s_nop 1\n"
  "s_nop 1\n"
  "s_nop 1\n"
  "s_nop 1\n"
  "s_nop 1\n"
  "s_nop 1\n"
  "s_nop 1\n"
  "s_nop 1\n"
  "WEV:\n"
  "global_load_lds_dwordx4 v1, s[62:63] sc1\n"
  "s_mov_b32 m0, s79\n"
  "s_nop 0\n"
  "global_load_lds_dwordx4 v1, s[64:65] sc1\n"
  "s_mov_b32 m0, s78\n"
  "s_nop 0\n"
  "global_load_lds_dwordx4 v1, s[66:67] sc1\n"
  "s_waitcnt vmcnt(2)\n"
  "NDV:\n"
  "s_waitcnt lgkmcnt(0)\n"
  "s_barrier\n"
  "ds_read_b32 v60, v61 offset:9232\n"
  "ds_read_b128 v[20:23], v0 offset:0\n"
  "ds_read_b128 v[24:27], v0 offset:1024\n"
  "ds_read_b128 v[28:31], v0 offset:2048\n"
  "s_waitcnt lgkmcnt(3)\n"
  "v_readfirstlane_b32 s74, v60\n"
  "s_sub_i32 s75, s74, s49\n"
  "s_cmp_gt_i32 s75, 0\n"
  "s_cbranch_scc1 RLATE\n"
  "s_cmp_eq_u32 s48, 0\n"
  "s_cbranch_scc1 RNW\n"
  "s_waitcnt vmcnt(0)\n"
  "RNW:\n"
  "s_barrier\n"
  "ds_read_b128 v[32:35], v0 offset:3072\n"
  "ds_read_b128 v[36:39], v0 offset:4096\n"
  "s_waitcnt lgkmcnt(4)\n"
  "v_smfmac_f32_16x16x64_bf16 v[56:59], v[20:23], v[96:103], v4\n"
  "ds_read_b128 v[40:43], v0 offset:5120\n"
  "s_waitcnt lgkmcnt(4)\n"
  "v_smfmac_f32_16x16x64_bf16 v[56:59], v[24:27], v[104:111], v4\n"
  "ds_read_b128 v[44:47], v0 offset:6144\n"
  "s_waitcnt lgkmcnt(4)\n"
  "v_smfmac_f32_16x16x64_bf16 v[56:59], v[28:31], v[112:119], v4\n"
  "ds_read_b128 v[48:51], v0 offset:7168\n"
  "s_waitcnt lgkmcnt(4)\n"
  "v_smfmac_f32_16x16x64_bf16 v[56:59], v[32:35], v[120:127], v4\n"
  "ds_read_b128 v[52:55], v0 offset:8192\n"
  "s_waitcnt lgkmcnt(4)\n"
  "v_smfmac_f32_16x16x64_bf16 v[56:59], v[36:39], v[128:135], v4\n"
  "s_waitcnt lgkmcnt(3)\n"
  "v_smfmac_f32_16x16x64_bf16 v[56:59], v[40:43], v[136:143], v4\n"
  "s_waitcnt lgkmcnt(2)\n"
  "v_smfmac_f32_16x16x64_bf16 v[56:59], v[44:47], v[144:151], v4\n"
  "s_waitcnt lgkmcnt(1)\n"
  "v_smfmac_f32_16x16x64_bf16 v[56:59], v[48:51], v[152:159], v4\n"
  "s_waitcnt lgkmcnt(0)\n"
  "v_smfmac_f32_16x16x64_bf16 v[56:59], v[52:55], v[160:167], v4\n"
  "s_nop 3\n"
  "v_mov_b32 v7, v7\n"
  "s_nop 2\n"
  "v_add_f32 v56, v56, v58\n"
  "v_add_f32 v57, v57, v59\n"
  "v_cvt_pk_bf16_f32 v58, v56, v57\n"
  "global_store_dword v2, v58, s[68:69] sc1\n"
  "v_cmp_u_f32 vcc, v56, v57\n"
  "s_cbranch_vccnz RBADM\n"
  "ROKM:\n"
  "v_readlane_b32 s8, v14, 0\n"
  "v_readlane_b32 s9, v14, 8\n"
  "v_readlane_b32 s10, v14, 16\n"
  "v_readlane_b32 s11, v14, 24\n"
  "v_readlane_b32 s12, v14, 32\n"
  "v_readlane_b32 s13, v14, 40\n"
  "s_max_u32 s8, s8, s9\n"
  "s_max_u32 s10, s10, s11\n"
  "s_max_u32 s8, s8, s10\n"
  "s_max_u32 s12, s12, s13\n"
  "s_cmp_eq_u32 s8, 0\n"
  "s_cselect_b32 s8, s14, s8\n"
  "s_sub_i32 s74, s8, s16\n"
  "s_min_i32 s74, s74, 24\n"
  "s_max_i32 s74, s74, -6\n"
  "s_add_u32 s16, s16, s74\n"
  "s_min_u32 s74, s8, s14\n"
  "s_max_u32 s16, s16, s74\n"
  "s_max_u32 s74, s8, s14\n"
  "s_add_u32 s74, s74, 40\n"
  "s_min_u32 s16, s16, s74\n"
  "s_mov_b32 s14, s8\n"
  "s_cmp_eq_u32 s12, 0\n"
  "s_cselect_b32 s12, s15, s12\n"
  "s_sub_i32 s74, s12, s17\n"
  "s_min_i32 s74, s74, 24\n"
  "s_max_i32 s74, s74, -6\n"
  "s_add_u32 s17, s17, s74\n"
  "s_min_u32 s74, s12, s15\n"
  "s_max_u32 s17, s17, s74\n"
  "s_max_u32 s74, s12, s15\n"
  "s_add_u32 s74, s74, 40\n"
  "s_min_u32 s17, s17, s74\n"
  "s_mov_b32 s15, s12\n"
  "s_min_u32 s74, s40, 31\n"
  "s_lshr_b32 s74, 700, s74\n"
  "s_add_u32 s55, s16, s74\n"
  "s_add_u32 s55, s55, 130\n"
  "s_add_u32 s50, s55, s17\n"
  "s_add_u32 s50, s50, s74\n"
  "s_add_u32 s50, s50, 230\n"
  "s_add_u32 s74, s36, s47\n"
  "s_addc_u32 s75, s37, 0\n"
  "global_store_dword v2, v58, s[74:75] sc1\n"
  "s_add_u32 s47, s47, 65536\n"
  "s_cmp_eq_u32 s40, 63\n"
  "s_cbranch_scc1 ENDV\n"
  "s_add_u32 s75, s40, 3\n"
  "s_and_b32 s74, s75, 0x78\n"
  "s_add_u32 s74, s74, s75\n"
  "s_lshl_b32 s74, s74, 10\n"
  "s_add_u32 s74, s74, s39\n"
  "s_add_u32 s74, s74, s7\n"
  "s_add_u32 s18, s36, s74\n"
  "s_addc_u32 s19, s37, 0\n"
  "v_mov_b32 v61, s6\n"
  "s_cmp_lg_u32 s6, 0\n"
  "s_cselect_b64 exec, 1, 0\n"
  "global_atomic_umax v0, v61, s[18:19] sc1\n"
  "s_mov_b64 exec, -1\n"
  "s_add_u32 s75, s40, 1\n"
  "s_and_b32 s74, s75, 0x78\n"
  "s_add_u32 s74, s74, s75\n"
  "s_lshl_b32 s74, s74, 10\n"
  "s_add_u32 s74, s74, s39\n"
  "s_add_u32 s18, s36, s74\n"
  "s_addc_u32 s19, s37, 0\n"
  "s_mov_b32 exec_lo, 0x01010101\n"
  "s_mov_b32 exec_hi, 0x0101\n"
  "global_load_dword v14, v0, s[18:19] sc1\n"
  "s_mov_b64 exec, -1\n"
  "s_add_u32 s78, s41, 2\n"
  "s_cmp_ge_u32 s78, 3\n"
  "s_cselect_b32 s75, 3, 0\n"
  "s_sub_u32 s78, s78, s75\n"
  "s_mul_i32 s74, s78, 131072\n"
  "s_add_u32 s76, s74, s46\n"
  "s_add_u32 s74, s36, s76\n"
  "s_addc_u32 s75, s37, 0\n"
  "global_store_dword v2, v64, s[74:75] sc1\n"
  "s_add_u32 s40, s40, 1\n"
  "s_add_u32 s81, s49, s55\n"
  "s_add_u32 s49, s49, s50\n"
  "s_add_u32 s41, s41, 1\n"
  "s_cmp_eq_u32 s41, 3\n"
  "s_cselect_b32 s41, 0, s41\n"
  "s_cmp_eq_u32 s56, 1\n"
  "s_cbranch_scc1 RUNIT\n"
  "s_cmp_eq_u32 s56, 0\n"
  "s_cbranch_scc1 RNOA\n"
  "s_and_b32 s74, s40, 1\n"
  "s_lshl_b32 s74, s74, 1\n"
  "s_add_u32 s74, s74, s48\n"
  "s_sub_u32 s74, s74, 1\n"
  "s_lshl_b32 s74, s74, 15\n"
  "s_add_u32 s74, s74, s53\n"
  "s_add_u32 s70, s36, s74\n"
  "s_addc_u32 s71, s37, 0\n"
  "s_mul_i32 s78, s78, 163840\n"
  "s_add_u32 s72, s36, s78\n"
  "s_addc_u32 s73, s37, 0\n"
  "s_cmp_eq_u32 s48, 3\n"
  "s_cbranch_scc1 RAM\n"
  "global_store_dwordx4 v0, v[64:67], s[70:71] offset:0 sc1\n"
  "global_store_dwordx4 v0, v[64:67], s[70:71] offset:1024 sc1\n"
  "global_store_dwordx4 v0, v[64:67], s[70:71] offset:2048 sc1\n"
  "global_store_dwordx4 v0, v[64:67], s[70:71] offset:3072 sc1\n"
  "s_branch RNOA\n"
  "RAM:\n"
  "global_store_dwordx4 v3, v[64:67], s[72:73] sc1\n"
  "global_store_dwordx4 v13, v[64:67], s[72:73] sc1\n"
  "RNOA:\n"
  "s_waitcnt vmcnt(0)\n"
  "s_barrier\n"
  "s_add_u32 s74, s36, s38\n"
  "s_addc_u32 s75, s37, 0\n"
  "v_mov_b32 v61, s40\n"
  "s_cmp_eq_u32 s48, 1\n"
  "s_cselect_b64 exec, 1, 0\n"
  "global_store_dword v0, v61, s[74:75] sc1\n"
  "s_mov_b64 exec, -1\n"
  "s_branch RTAIL\n"
  "RUNIT:\n"
  "s_mul_i32 s78, s41, 163840\n"
  "s_add_u32 s72, s36, s78\n"
  "s_addc_u32 s73, s37, 0\n"
  "s_and_b32 s74, s40, 1\n"
  "s_xor_b32 s74, s74, 1\n"
  "s_lshl_b32 s74, s74, 16\n"
  "s_add_u32 s74, s74, s53\n"
  "s_add_u32 s70, s36, s74\n"
  "s_addc_u32 s71, s37, 0\n"
  "s_add_u32 s66, s70, 32768\n"
  "s_addc_u32 s67, s71, 0\n"
  "s_branch WGR3\n"
  "WR3:\n"
  "s_memrealtime s[74:75]\n"
  "s_waitcnt lgkmcnt(0)\n"
  "s_mul_i32 s74, s74, 24\n"
  "s_sub_i32 s74, s81, s74\n"
  "s_cmp_lt_i32 s74, 750\n"
  "s_cbranch_scc1 WFR3\n"
  "s_cmp_gt_i32 s74, 0x40000\n"
  "s_cbranch_scc1 WER3\n"
  "s_sleep 2\n"
  "s_branch WR3\n"
  "WFR3:\n"
  "s_cmp_lt_i32 s74, 100\n"
  "s_cbranch_scc1 WER3\n"
  "WGR3:\n"
  "s_memtime s[76:77]\n"
  "s_memrealtime s[20:21]\n"
  "s_nop 2\n"
  "s_memrealtime s[22:23]\n"
  "s_nop 2\n"
  "s_memrealtime s[24:25]\n"
  "s_nop 2\n"
  "s_memrealtime s[26:27]\n"
  "s_nop 2\n"
  "s_memrealtime s[28:29]\n"
  "s_nop 2\n"
  "s_memrealtime s[30:31]\n"
  "s_waitcnt lgkmcnt(0)\n"
  "s_add_u32 s20, s20, s22\n"
  "s_add_u32 s24, s24, s26\n"
  "s_add_u32 s28, s28, s30\n"
  "s_add_u32 s20, s20, s24\n"
  "s_add_u32 s20, s20, s28\n"
  "s_lshl_b32 s74, s20, 2\n"
  "s_sub_u32 s64, s74, s76\n"
  "s_sub_i32 s74, s81, s74\n"
  "s_sub_i32 s74, s74, 238\n"
  "s_max_i32 s74, s74, 0\n"
  "s_cmp_ge_i32 s74, 512\n"
  "s_cbranch_scc1 WR3\n"
  "s_lshr_b32 s74, s74, 3\n"
  "s_lshl_b32 s74, s74, 2\n"
  "s_sub_i32 s74, 268, s74\n"
  "s_getpc_b64 s[76:77]\n"
  "s_add_u32 s76, s76, s74\n"
  "s_addc_u32 s77, s77, 0\n"
  "s_setpc_b64 s[76:77]\n"
  "s_nop 1\n"
  "s_nop 1\n"
  "s_nop 1\n"
  "s_nop 1\n"
  "s_nop 1\n"
  "s_nop 1\n"
  "s_nop 1\n"
  "s_nop 1\n"
  "s_nop 1\n"
  "s_nop 1\n"
  "s_nop 1\n"
  "s_nop 1\n"
  "s_nop 1\n"
  "s_nop 1\n"
  "s_nop 1\n"
  "s_nop 1\n"
  "s_nop 1\n"
  "s_nop 1\n"
  "s_nop 1\n"
  "s_nop 1\n"
  "s_nop 1\n"
  "s_nop 1\n"
  "s_nop 1\n"
  "s_nop 1\n"
  "s_nop 1\n"
  "s_nop 1\n"
  "s_nop 1\n"
  "s_nop 1\n"
  "s_nop 1\n"
  "s_nop 1\n"
  "s_nop 1\n"
  "s_nop 1\n"
  "s_nop 1\n"
  "s_nop 1\n"
  "s_nop 1\n"
  "s_nop 1\n"
  "s_nop 1\n"
  "s_nop 1\n"
  "s_nop 1\n"
  "s_nop 1\n"
  "s_nop 1\n"
  "s_nop 1\n"
  "s_nop 1\n"
  "s_nop 1\n"
  "s_nop 1\n"
  "s_nop 1\n"
  "s_nop 1\n"
  "s_nop 1\n"
  "s_nop 1\n"
  "s_nop 1\n"
  "s_nop 1\n"
  "s_nop 1\n"
  "s_nop 1\n"
  "s_nop 1\n"
  "s_nop 1\n"
  "s_nop 1\n"
  "s_nop 1\n"
  "s_nop 1\n"
  "s_nop 1\n"
  "s_nop 1\n"
  "s_nop 1\n"
  "s_nop 1\n"
  "s_nop 1\n"
  "s_nop 1\n"
  "WER3:\n"
  "s_memtime s[76:77]\n"
  "global_load_dwordx4 v[168:171], v0, s[70:71] offset:0 sc1\n"
  "global_load_dwordx4 v[172:175], v0, s[70:71] offset:1024 sc1\n"
  "global_load_dwordx4 v[176:179], v0, s[70:71] offset:2048 sc1\n"
  "global_load_dwordx4 v[180:183], v0, s[70:71] offset:3072 sc1\n"
  "global_load_dwordx4 v[184:187], v0, s[66:67] offset:0 sc1\n"
  "global_load_dwordx4 v[188:191], v0, s[66:67] offset:1024 sc1\n"
  "global_load_dwordx4 v[192:195], v0, s[66:67] offset:2048 sc1\n"
  "global_load_dwordx4 v[196:199], v0, s[66:67] offset:3072 sc1\n"
  "s_waitcnt lgkmcnt(0)\n"
  "s_add_u32 s64, s64, s76\n"
  "s_sub_i32 s64, s49, s64\n"
  "s_sub_i32 s64, s64, 230\n"
  "s_waitcnt vmcnt(8)\n"
  "s_barrier\n"
  "s_waitcnt vmcnt(7)\n"
  "v_mfma_f32_16x16x32_bf16 v[84:87], v[168:171], v[8:11], 0\n"
  "s_waitcnt vmcnt(6)\n"
  "v_mfma_f32_16x16x32_bf16 v[84:87], v[172:175], v[8:11], v[84:87]\n"
  "s_waitcnt vmcnt(5)\n"
  "v_mfma_f32_16x16x32_bf16 v[84:87], v[176:179], v[8:11], v[84:87]\n"
  "s_waitcnt vmcnt(4)\n"
  "v_mfma_f32_16x16x32_bf16 v[84:87], v[180:183], v[8:11], v[84:87]\n"
  "s_waitcnt vmcnt(3)\n"
  "v_mfma_f32_16x16x32_bf16 v[84:87], v[184:187], v[8:11], v[84:87]\n"
  "s_memtime s[74:75]\n"
  "s_waitcnt vmcnt(2)\n"
  "v_mfma_f32_16x16x32_bf16 v[84:87], v[188:191], v[8:11], v[84:87]\n"
  "s_waitcnt vmcnt(1)\n"
  "v_mfma_f32_16x16x32_bf16 v[84:87], v[192:195], v[8:11], v[84:87]\n"
  "s_waitcnt vmcnt(0)\n"
  "v_mfma_f32_16x16x32_bf16 v[84:87], v[196:199], v[8:11], v[84:87]\n"
  "s_waitcnt lgkmcnt(0)\n"
  "s_sub_i32 s74, s74, s76\n"
  "s_cmp_gt_i32 s74, s64\n"
  "s_cbranch_scc1 RSLOW\n"
  "s_nop 3\n"
  "v_cvt_pk_bf16_f32 v88, v84, v85\n"
  "v_cvt_pk_bf16_f32 v89, v86, v87\n"
  "v_cvt_pk_bf16_f32 v90, v84, v85\n"
  "v_cvt_pk_bf16_f32 v91, v86, v87\n"
  "s_nop 0\n"
  "v_permlane32_swap_b32 v88, v90\n"
  "v_permlane32_swap_b32 v89, v91\n"
  "global_store_dwordx4 v3, v[88:91], s[72:73] sc1\n"
  "s_memtime s[74:75]\n"
  "global_store_dwordx4 v13, v[88:91], s[72:73] sc1\n"
  "v_add_f32 v60, v84, v85\n"
  "v_add_f32 v61, v86, v87\n"
  "v_cmp_u_f32 vcc, v60, v61\n"
  "s_cbranch_vccnz RAGAIN\n"
  "s_waitcnt lgkmcnt(0)\n"
  "s_sub_u32 s6, s74, s76\n"
  "RDONE:\n"
  "RTAIL:\n"
  "s_branch RLOOP\n"
  "RBADM:\n"
  "s_waitcnt lgkmcnt(0)\n"
  "s_nop 3\n"
  "v_mov_b32 v7, v7\n"
  "s_nop 2\n"
  "v_mov_b32 v7, v7\n"
  "s_nop 2\n"
  "s_mov_b32 s74, 0xff820000\n"
  "v_max3_u32 v60, v20, v21, v22\n"
  "v_max3_u32 v60, v60, v23, v24\n"
  "v_max3_u32 v60, v60, v25, v26\n"
  "v_max3_u32 v60, v60, v27, v28\n"
  "v_max3_u32 v60, v60, v29, v30\n"
  "v_max3_u32 v60, v60, v31, v32\n"
  "v_max3_u32 v60, v60, v33, v34\n"
  "v_max3_u32 v60, v60, v35, v36\n"
  "v_max3_u32 v60, v60, v37, v38\n"
  "v_max3_u32 v60, v60, v39, v40\n"
  "v_max3_u32 v60, v60, v41, v42\n"
  "v_max3_u32 v60, v60, v43, v44\n"
  "v_max3_u32 v60, v60, v45, v46\n"
  "v_max3_u32 v60, v60, v47, v48\n"
  "v_max3_u32 v60, v60, v49, v50\n"
  "v_max3_u32 v60, v60, v51, v52\n"
  "v_max3_u32 v60, v60, v53, v54\n"
  "v_max_u32 v60, v60, v55\n"
  "v_cmp_le_u32 vcc, s74, v60\n"
  "s_cbranch_vccnz REV\n"
  "s_mov_b32 s74, 0x7fff7fff\n"
  "v_and_b32 v60, s74, v20\n"
  "v_and_b32 v61, s74, v21\n"
  "v_pk_max_u16 v60, v60, v61\n"
  "v_and_b32 v61, s74, v22\n"
  "v_pk_max_u16 v60, v60, v61\n"
  "v_and_b32 v61, s74, v23\n"
  "v_pk_max_u16 v60, v60, v61\n"
  "v_and_b32 v61, s74, v24\n"
  "v_pk_max_u16 v60, v60, v61\n"
  "v_and_b32 v61, s74, v25\n"
  "v_pk_max_u16 v60, v60, v61\n"
  "v_and_b32 v61, s74, v26\n"
  "v_pk_max_u16 v60, v60, v61\n"
  "v_and_b32 v61, s74, v27\n"
  "v_pk_max_u16 v60, v60, v61\n"
  "v_and_b32 v61, s74, v28\n"
  "v_pk_max_u16 v60, v60, v61\n"
  "v_and_b32 v61, s74, v29\n"
  "v_pk_max_u16 v60, v60, v61\n"
  "v_and_b32 v61, s74, v30\n"
  "v_pk_max_u16 v60, v60, v61\n"
  "v_and_b32 v61, s74, v31\n"
  "v_pk_max_u16 v60, v60, v61\n"
  "v_and_b32 v61, s74, v32\n"
  "v_pk_max_u16 v60, v60, v61\n"
  "v_and_b32 v61, s74, v33\n"
  "v_pk_max_u16 v60, v60, v61\n"
  "v_and_b32 v61, s74, v34\n"
  "v_pk_max_u16 v60, v60, v61\n"
  "v_and_b32 v61, s74, v35\n"
  "v_pk_max_u16 v60, v60, v61\n"
  "v_and_b32 v61, s74, v36\n"
  "v_pk_max_u16 v60, v60, v61\n"
  "v_and_b32 v61, s74, v37\n"
  "v_pk_max_u16 v60, v60, v61\n"
  "v_and_b32 v61, s74, v38\n"
  "v_pk_max_u16 v60, v60, v61\n"
  "v_and_b32 v61, s74, v39\n"
  "v_pk_max_u16 v60, v60, v61\n"
  "v_and_b32 v61, s74, v40\n"
  "v_pk_max_u16 v60, v60, v61\n"
  "v_and_b32 v61, s74, v41\n"
  "v_pk_max_u16 v60, v60, v61\n"
  "v_and_b32 v61, s74, v42\n"
  "v_pk_max_u16 v60, v60, v61\n"
  "v_and_b32 v61, s74, v43\n"
  "v_pk_max_u16 v60, v60, v61\n"
  "v_and_b32 v61, s74, v44\n"
  "v_pk_max_u16 v60, v60, v61\n"
  "v_and_b32 v61, s74, v45\n"
  "v_pk_max_u16 v60, v60, v61\n"
  "v_and_b32 v61, s74, v46\n"
  "v_pk_max_u16 v60, v60, v61\n"
  "v_and_b32 v61, s74, v47\n"
  "v_pk_max_u16 v60, v60, v61\n"
  "v_and_b32 v61, s74, v48\n"
  "v_pk_max_u16 v60, v60, v61\n"
  "v_and_b32 v61, s74, v49\n"
  "v_pk_max_u16 v60, v60, v61\n"
  "v_and_b32 v61, s74, v50\n"
  "v_pk_max_u16 v60, v60, v61\n"
  "v_and_b32 v61, s74, v51\n"
  "v_pk_max_u16 v60, v60, v61\n"
  "v_and_b32 v61, s74, v52\n"
  "v_pk_max_u16 v60, v60, v61\n"
  "v_and_b32 v61, s74, v53\n"
  "v_pk_max_u16 v60, v60, v61\n"
  "v_and_b32 v61, s74, v54\n"
  "v_pk_max_u16 v60, v60, v61\n"
  "v_and_b32 v61, s74, v55\n"
  "v_pk_max_u16 v60, v60, v61\n"
  "v_lshrrev_b32 v61, 16, v60\n"
  "v_and_b32 v60, 0xffff, v60\n"
  "v_max_u32 v60, v60, v61\n"
  "v_cmp_lt_u32 vcc, 0x7f81, v60\n"
  "s_cbranch_vccnz REV\n"
  "s_mov_b32 s74, 0x7fff7fff\n"
  "s_mov_b32 s75, 0x7f817f81\n"
  "v_and_b32 v60, s74, v58\n"
  "v_pk_min_u16 v60, v60, s75\n"
  "v_bfi_b32 v58, s74, v60, v58\n"
  "s_waitcnt vmcnt(0)\n"
  "global_store_dword v2, v58, s[68:69] sc1\n"
  "s_branch ROKM\n"
  "REV:\n"
  "s_sub_u32 s42, s42, 1\n"
  "s_cbranch_scc1 QUITV\n"
  "s_add_u32 s43, s43, 1\n"
  "s_memrealtime s[74:75]\n"
  "s_waitcnt lgkmcnt(0)\n"
  "s_mul_i32 s78, s74, 24\n"
  "s_add_u32 s78, s78, 650\n"
  "RSV:\n"
  "s_add_u32 s49, s49, s51\n"
  "s_sub_i32 s74, s49, s78\n"
  "s_cmp_lt_i32 s74, 0\n"
  "s_cbranch_scc1 RSV\n"
  "AGV:\n"
  "v_mov_b32 v60, s49\n"
  "v_mov_b32 v61, s48\n"
  "v_lshlrev_b32 v61, 2, v61\n"
  "ds_write_b32 v61, v60 offset:9216\n"
  "s_waitcnt lgkmcnt(0)\n"
  "s_barrier\n"
  "v_mov_b32 v61, 0\n"
  "ds_read_b128 v[20:23], v61 offset:9216\n"
  "s_waitcnt lgkmcnt(0)\n"
  "v_readfirstlane_b32 s74, v20\n"
  "s_sub_i32 s75, s74, s49\n"
  "s_cmp_gt_i32 s75, 0\n"
  "s_cselect_b32 s49, s74, s49\n"
  "v_readfirstlane_b32 s74, v21\n"
  "s_sub_i32 s75, s74, s49\n"
  "s_cmp_gt_i32 s75, 0\n"
  "s_cselect_b32 s49, s74, s49\n"
  "v_readfirstlane_b32 s74, v22\n"
  "s_sub_i32 s75, s74, s49\n"
  "s_cmp_gt_i32 s75, 0\n"
  "s_cselect_b32 s49, s74, s49\n"
  "v_readfirstlane_b32 s74, v23\n"
  "s_sub_i32 s75, s74, s49\n"
  "s_cmp_gt_i32 s75, 0\n"
  "s_cselect_b32 s49, s74, s49\n"
  "s_cmp_lg_u32 s49, s98\n"
  "s_cselect_b32 s52, 1, 0\n"
  "s_branch RTRY\n"
  "RLATE:\n"
  "s_waitcnt lgkmcnt(0)\n"
  "s_mov_b32 s49, s74\n"
  "s_mov_b32 s52, 1\n"
  "s_add_u32 s43, s43, 1\n"
  "s_barrier\n"
  "s_branch RTRY\n"
  "RSLOW:\n"
  "s_waitcnt lgkmcnt(0)\n"
  "s_nop 3\n"
  "v_mov_b32 v7, v7\n"
  "s_nop 2\n"
  "v_mov_b32 v7, v7\n"
  "s_nop 2\n"
  "s_add_u32 s6, s74, 110\n"
  "s_cmp_gt_i32 s64, 0\n"
  "s_cselect_b32 s6, s6, 0\n"
  "v_add_f32 v60, v84, v85\n"
  "v_add_f32 v61, v86, v87\n"
  "v_cmp_u_f32 vcc, v60, v61\n"
  "s_cbranch_vccz RFULL\n"
  "RAGAIN:\n"
  "s_waitcnt lgkmcnt(0)\n"
  "s_nop 3\n"
  "v_mov_b32 v7, v7\n"
  "s_nop 2\n"
  "v_mov_b32 v7, v7\n"
  "s_nop 2\n"
  "s_mov_b32 s6, 0\n"
  "RAG2:\n"
  "s_sub_u32 s42, s42, 1\n"
  "s_cbranch_scc1 QUITV\n"
  "s_add_u32 s43, s43, 0x10000\n"
  "global_load_dwordx4 v[168:171], v0, s[70:71] offset:0 sc1\n"
  "global_load_dwordx4 v[172:175], v0, s[70:71] offset:1024 sc1\n"
  "global_load_dwordx4 v[176:179], v0, s[70:71] offset:2048 sc1\n"
  "global_load_dwordx4 v[180:183], v0, s[70:71] offset:3072 sc1\n"
  "global_load_dwordx4 v[184:187], v0, s[66:67] offset:0 sc1\n"
  "global_load_dwordx4 v[188:191], v0, s[66:67] offset:1024 sc1\n"
  "global_load_dwordx4 v[192:195], v0, s[66:67] offset:2048 sc1\n"
  "global_load_dwordx4 v[196:199], v0, s[66:67] offset:3072 sc1\n"
  "s_waitcnt vmcnt(7)\n"
  "v_mfma_f32_16x16x32_bf16 v[84:87], v[168:171], v[8:11], 0\n"
  "s_waitcnt vmcnt(6)\n"
  "v_mfma_f32_16x16x32_bf16 v[84:87], v[172:175], v[8:11], v[84:87]\n"
  "s_waitcnt vmcnt(5)\n"
  "v_mfma_f32_16x16x32_bf16 v[84:87], v[176:179], v[8:11], v[84:87]\n"
  "s_waitcnt vmcnt(4)\n"
  "v_mfma_f32_16x16x32_bf16 v[84:87], v[180:183], v[8:11], v[84:87]\n"
  "s_waitcnt vmcnt(3)\n"
  "v_mfma_f32_16x16x32_bf16 v[84:87], v[184:187], v[8:11], v[84:87]\n"
  "s_waitcnt vmcnt(2)\n"
  "v_mfma_f32_16x16x32_bf16 v[84:87], v[188:191], v[8:11], v[84:87]\n"
  "s_waitcnt vmcnt(1)\n"
  "v_mfma_f32_16x16x32_bf16 v[84:87], v[192:195], v[8:11], v[84:87]\n"
  "s_waitcnt vmcnt(0)\n"
  "v_mfma_f32_16x16x32_bf16 v[84:87], v[196:199], v[8:11], v[84:87]\n"
  "s_nop 3\n"
  "v_mov_b32 v7, v7\n"
  "s_nop 2\n"
  "v_add_f32 v60, v84, v85\n"
  "v_add_f32 v61, v86, v87\n"
  "v_cmp_u_f32 vcc, v60, v61\n"
  "s_cbranch_vccz RFULL\n"
  "s_mov_b32 s74, 0xff820000\n"
  "v_max3_u32 v60, v168, v169, v170\n"
  "v_max3_u32 v60, v60, v171, v172\n"
  "v_max3_u32 v60, v60, v173, v174\n"
  "v_max3_u32 v60, v60, v175, v176\n"
  "v_max3_u32 v60, v60, v177, v178\n"
  "v_max3_u32 v60, v60, v179, v180\n"
  "v_max3_u32 v60, v60, v181, v182\n"
  "v_max3_u32 v60, v60, v183, v184\n"
  "v_max3_u32 v60, v60, v185, v186\n"
  "v_max3_u32 v60, v60, v187, v188\n"
  "v_max3_u32 v60, v60, v189, v190\n"
  "v_max3_u32 v60, v60, v191, v192\n"
  "v_max3_u32 v60, v60, v193, v194\n"
  "v_max3_u32 v60, v60, v195, v196\n"
  "v_max3_u32 v60, v60, v197, v198\n"
  "v_max_u32 v60, v60, v199\n"
  "v_cmp_le_u32 vcc, s74, v60\n"
  "s_cbranch_vccnz RAG2\n"
  "s_mov_b32 s74, 0x7fff7fff\n"
  "v_and_b32 v60, s74, v168\n"
  "v_and_b32 v61, s74, v169\n"
  "v_pk_max_u16 v60, v60, v61\n"
  "v_and_b32 v61, s74, v170\n"
  "v_pk_max_u16 v60, v60, v61\n"
  "v_and_b32 v61, s74, v171\n"
  "v_pk_max_u16 v60, v60, v61\n"
  "v_and_b32 v61, s74, v172\n"
  "v_pk_max_u16 v60, v60, v61\n"
  "v_and_b32 v61, s74, v173\n"
  "v_pk_max_u16 v60, v60, v61\n"
  "v_and_b32 v61, s74, v174\n"
  "v_pk_max_u16 v60, v60, v61\n"
  "v_and_b32 v61, s74, v175\n"
  "v_pk_max_u16 v60, v60, v61\n"
  "v_and_b32 v61, s74, v176\n"
  "v_pk_max_u16 v60, v60, v61\n"
  "v_and_b32 v61, s74, v177\n"
  "v_pk_max_u16 v60, v60, v61\n"
  "v_and_b32 v61, s74, v178\n"
  "v_pk_max_u16 v60, v60, v61\n"
  "v_and_b32 v61, s74, v179\n"
  "v_pk_max_u16 v60, v60, v61\n"
  "v_and_b32 v61, s74, v180\n"
  "v_pk_max_u16 v60, v60, v61\n"
  "v_and_b32 v61, s74, v181\n"
  "v_pk_max_u16 v60, v60, v61\n"
  "v_and_b32 v61, s74, v182\n"
  "v_pk_max_u16 v60, v60, v61\n"
  "v_and_b32 v61, s74, v183\n"
  "v_pk_max_u16 v60, v60, v61\n"
  "v_and_b32 v61, s74, v184\n"
  "v_pk_max_u16 v60, v60, v61\n"
  "v_and_b32 v61, s74, v185\n"
  "v_pk_max_u16 v60, v60, v61\n"
  "v_and_b32 v61, s74, v186\n"
  "v_pk_max_u16 v60, v60, v61\n"
  "v_and_b32 v61, s74, v187\n"
  "v_pk_max_u16 v60, v60, v61\n"
  "v_and_b32 v61, s74, v188\n"
  "v_pk_max_u16 v60, v60, v61\n"
  "v_and_b32 v61, s74, v189\n"
  "v_pk_max_u16 v60, v60, v61\n"
  "v_and_b32 v61, s74, v190\n"
  "v_pk_max_u16 v60, v60, v61\n"
  "v_and_b32 v61, s74, v191\n"
  "v_pk_max_u16 v60, v60, v61\n"
  "v_and_b32 v61, s74, v192\n"
  "v_pk_max_u16 v60, v60, v61\n"
  "v_and_b32 v61, s74, v193\n"
  "v_pk_max_u16 v60, v60, v61\n"
  "v_and_b32 v61, s74, v194\n"
  "v_pk_max_u16 v60, v60, v61\n"
  "v_and_b32 v61, s74, v195\n"
  "v_pk_max_u16 v60, v60, v61\n"
  "v_and_b32 v61, s74, v196\n"
  "v_pk_max_u16 v60, v60, v61\n"
  "v_and_b32 v61, s74, v197\n"
  "v_pk_max_u16 v60, v60, v61\n"
  "v_and_b32 v61, s74, v198\n"
  "v_pk_max_u16 v60, v60, v61\n"
  "v_and_b32 v61, s74, v199\n"
  "v_pk_max_u16 v60, v60, v61\n"
  "v_lshrrev_b32 v61, 16, v60\n"
  "v_and_b32 v60, 0xffff, v60\n"
  "v_max_u32 v60, v60, v61\n"
  "v_cmp_lt_u32 vcc, 0x7f81, v60\n"
  "s_cbranch_vccnz RAG2\n"
  "RFULL:\n"
  "v_cvt_pk_bf16_f32 v88, v84, v85\n"
  "v_cvt_pk_bf16_f32 v89, v86, v87\n"
  "v_cvt_pk_bf16_f32 v90, v84, v85\n"
  "v_cvt_pk_bf16_f32 v91, v86, v87\n"
  "s_nop 0\n"
  "v_permlane32_swap_b32 v88, v90\n"
  "v_permlane32_swap_b32 v89, v91\n"
  "s_mov_b32 s74, 0x7fff7fff\n"
  "s_mov_b32 s75, 0x7f817f81\n"
  "v_and_b32 v60, s74, v88\n"
  "v_pk_min_u16 v60, v60, s75\n"
  "v_bfi_b32 v88, s74, v60, v88\n"
  "v_and_b32 v60, s74, v89\n"
  "v_pk_min_u16 v60, v60, s75\n"
  "v_bfi_b32 v89, s74, v60, v89\n"
  "v_and_b32 v60, s74, v90\n"
  "v_pk_min_u16 v60, v60, s75\n"
  "v_bfi_b32 v90, s74, v60, v90\n"
  "v_and_b32 v60, s74, v91\n"
  "v_pk_min_u16 v60, v60, s75\n"
  "v_bfi_b32 v91, s74, v60, v91\n"
  "s_add_u32 s43, s43, 0x100\n"
  "s_memrealtime s[74:75]\n"
  "s_waitcnt lgkmcnt(0)\n"
  "s_mul_i32 s78, s74, 24\n"
  "s_add_u32 s78, s78, 750\n"
  "s_mov_b32 s81, s49\n"
  "RSG:\n"
  "s_add_u32 s79, s81, s51\n"
  "s_sub_i32 s74, s79, s78\n"
  "s_cmp_ge_i32 s74, 0\n"
  "s_cbranch_scc1 RGOT\n"
  "s_mov_b32 s81, s79\n"
  "s_branch RSG\n"
  "RGOT:\n"
  "v_mov_b32 v60, 0\n"
  "v_mov_b32 v61, s79\n"
  "ds_write_b32 v60, v61 offset:9232\n"
  "s_add_u32 s81, s81, 200\n"
  "WR4:\n"
  "s_memrealtime s[74:75]\n"
  "s_waitcnt lgkmcnt(0)\n"
  "s_mul_i32 s74, s74, 24\n"
  "s_sub_i32 s74, s81, s74\n"
  "s_cmp_lt_i32 s74, 750\n"
  "s_cbranch_scc1 WFR4\n"
  "s_cmp_gt_i32 s74, 0x40000\n"
  "s_cbranch_scc1 WER4\n"
  "s_sleep 2\n"
  "s_branch WR4\n"
  "WFR4:\n"
  "s_cmp_lt_i32 s74, 100\n"
  "s_cbranch_scc1 WER4\n"
  "WGR4:\n"
  "s_memtime s[76:77]\n"
  "s_memrealtime s[20:21]\n"
  "s_nop 2\n"
  "s_memrealtime s[22:23]\n"
  "s_nop 2\n"
  "s_memrealtime s[24:25]\n"
  "s_nop 2\n"
  "s_memrealtime s[26:27]\n"
  "s_nop 2\n"
  "s_memrealtime s[28:29]\n"
  "s_nop 2\n"
  "s_memrealtime s[30:31]\n"
  "s_waitcnt lgkmcnt(0)\n"
  "s_add_u32 s20, s20, s22\n"
  "s_add_u32 s24, s24, s26\n"
  "s_add_u32 s28, s28, s30\n"
  "s_add_u32 s20, s20, s24\n"
  "s_add_u32 s20, s20, s28\n"
  "s_lshl_b32 s74, s20, 2\n"
  "s_nop 0\n"
  "s_sub_i32 s74, s81, s74\n"
  "s_sub_i32 s74, s74, 238\n"
  "s_max_i32 s74, s74, 0\n"
  "s_cmp_ge_i32 s74, 512\n"
  "s_cbranch_scc1 WR4\n"
  "s_lshr_b32 s74, s74, 3\n"
  "s_lshl_b32 s74, s74, 2\n"
  "s_sub_i32 s74, 268, s74\n"
  "s_getpc_b64 s[76:77]\n"
  "s_add_u32 s76, s76, s74\n"
  "s_addc_u32 s77, s77, 0\n"
  "s_setpc_b64 s[76:77]\n"
  "s_nop 1\n"
  "s_nop 1\n"
  "s_nop 1\n"
  "s_nop 1\n"
  "s_nop 1\n"
  "s_nop 1\n"
  "s_nop 1\n"
  "s_nop 1\n"
  "s_nop 1\n"
  "s_nop 1\n"
  "s_nop 1\n"
  "s_nop 1\n"
  "s_nop 1\n"
  "s_nop 1\n"
  "s_nop 1\n"
  "s_nop 1\n"
  "s_nop 1\n"
  "s_nop 1\n"
  "s_nop 1\n"
  "s_nop 1\n"
  "s_nop 1\n"
  "s_nop 1\n"
  "s_nop 1\n"
  "s_nop 1\n"
  "s_nop 1\n"
  "s_nop 1\n"
  "s_nop 1\n"
  "s_nop 1\n"
  "s_nop 1\n"
  "s_nop 1\n"
  "s_nop 1\n"
  "s_nop 1\n"
  "s_nop 1\n"
  "s_nop 1\n"
  "s_nop 1\n"
  "s_nop 1\n"
  "s_nop 1\n"
  "s_nop 1\n"
  "s_nop 1\n"
  "s_nop 1\n"
  "s_nop 1\n"
  "s_nop 1\n"
  "s_nop 1\n"
  "s_nop 1\n"
  "s_nop 1\n"
  "s_nop 1\n"
  "s_nop 1\n"
  "s_nop 1\n"
  "s_nop 1\n"
  "s_nop 1\n"
  "s_nop 1\n"
  "s_nop 1\n"
  "s_nop 1\n"
  "s_nop 1\n"
  "s_nop 1\n"
  "s_nop 1\n"
  "s_nop 1\n"
  "s_nop 1\n"
  "s_nop 1\n"
  "s_nop 1\n"
  "s_nop 1\n"
  "s_nop 1\n"
  "s_nop 1\n"
  "s_nop 1\n"
  "WER4:\n"
  "global_store_dwordx4 v3, v[88:91], s[72:73] sc1\n"
  "global_store_dwordx4 v13, v[88:91], s[72:73] sc1\n"
  "s_branch RDONE\n"
  "ENDV:\n"
  "s_waitcnt vmcnt(0) lgkmcnt(0)\n"
  "s_barrier\n"
  "s_add_u32 s74, s36, s38\n"
  "s_addc_u32 s75, s37, 0\n"
  "v_mov_b32 v60, 0\n"
  "v_mov_b32 v61, 64\n"
  "s_cmp_eq_u32 s48, 1\n"
  "s_cselect_b64 exec, 1, 0\n"
  "global_store_dword v60, v61, s[74:75] sc1\n"
  "QUITV:\n"
  "s_endpgm\n"
        : "+{v[0:15]}"(vin), "+{s[36:43]}"(sa), "+{s[44:59]}"(sb) :: CLOB);
    }
    return;
  }
  const int fd = gam - 4;
  if (tk >= 16 && tk < 28) for (int i = 0; i < 6; ++i) __builtin_amdgcn_s_sleep(32);
  if (tk < 16) { ARGS; PTR(sa, 0, p.ws);
    const int h = fd * 8 + (tk >> 1), isq = tk & 1;
    { const int row = 4 * wave + g, sw = 2 * ((row & 3) | ((row >> 1) & 4)); vin[1] = row * 8192 + (n ^ sw) * 16; }
    vin[2] = (8 * g + qq) * 256 + ((4 * wave + (c4 >> 1)) ^ (2 * (qq | ((g & 1) << 2)))) * 16 + (c4 & 1) * 8;
    vin[3] = x_src(wave, lane); vin[9] = x_frag(lane);
    { const int row = 8 * wave + (lane >> 3); vin[4] = row * 8192 + ((lane & 7) ^ (2 * (((row >> 1) & 1) | ((row >> 2) & 2)))) * 16; }
    vin[5] = (8 * g + qq) * 128 + ((2 * wave + (c4 >> 1)) ^ (2 * ((qq >> 1) | ((g & 1) << 1)))) * 16 + (c4 & 1) * 8;
    PTR(sa, 2, (isq ? p.Wq : p.Wk) + h * 128); PTR(sa, 4, p.Wv + h * 128 + isq * 64); PTR(sa, 6, p.x);
    sb[0] = h * 65536 + (isq ? 32768 : KVH) + wave * 2048; sb[1] = KVH + h * 65536 + 16384 + (4 * isq + wave) * 1024;
    sb[2] = (C_KVP) + (2 * h + isq) * 128; sb[3] = wave; sb[4] = __float_as_uint(isq ? 1.0f : QS);
    sb[6] = WO_ST + (role * 4 + wave) * 4096;
    UNIFORM(sa, 8); UNIFORM(sb, 16);
    asm volatile(
  "s_nop 4\n"
  "s_lshl_b32 s80, s47, 10\n"
  "v_mov_b32 v8, s48\n"
  "s_add_u32 s58, s80, 122880\n"
  "s_add_u32 m0, s58, 0\n"
  "s_nop 0\n"
  "global_load_lds_dwordx4 v3, s[42:43] offset:0\n"
  "s_add_u32 m0, s58, 3968\n"
  "s_nop 0\n"
  "global_load_lds_dwordx4 v3, s[42:43] offset:128\n"
  "s_add_u32 m0, s58, 7936\n"
  "s_nop 0\n"
  "global_load_lds_dwordx4 v3, s[42:43] offset:256\n"
  "s_add_u32 m0, s58, 11904\n"
  "s_nop 0\n"
  "global_load_lds_dwordx4 v3, s[42:43] offset:384\n"
  "s_add_u32 m0, s58, 15872\n"
  "s_nop 0\n"
  "global_load_lds_dwordx4 v3, s[42:43] offset:512\n"
  "s_add_u32 m0, s58, 19840\n"
  "s_nop 0\n"
  "global_load_lds_dwordx4 v3, s[42:43] offset:640\n"
  "s_add_u32 m0, s58, 23808\n"
  "s_nop 0\n"
  "global_load_lds_dwordx4 v3, s[42:43] offset:768\n"
  "s_add_u32 m0, s58, 27776\n"
  "s_nop 0\n"
  "global_load_lds_dwordx4 v3, s[42:43] offset:896\n"
  "s_add_u32 m0, s58, 31744\n"
  "s_nop 0\n"
  "global_load_lds_dwordx4 v3, s[42:43] offset:1024\n"
  "s_add_u32 m0, s80, 0\n"
  "s_add_u32 s74, s38, 0\n"
  "s_addc_u32 s75, s39, 0\n"
  "global_load_lds_dwordx4 v1, s[74:75]\n"
  "s_add_u32 m0, s80, 4096\n"
  "s_add_u32 s74, s38, 131072\n"
  "s_addc_u32 s75, s39, 0\n"
  "global_load_lds_dwordx4 v1, s[74:75]\n"
  "s_add_u32 m0, s80, 8192\n"
  "s_add_u32 s74, s38, 262144\n"
  "s_addc_u32 s75, s39, 0\n"
  "global_load_lds_dwordx4 v1, s[74:75]\n"
  "s_add_u32 m0, s80, 12288\n"
  "s_add_u32 s74, s38, 393216\n"
  "s_addc_u32 s75, s39, 0\n"
  "global_load_lds_dwordx4 v1, s[74:75]\n"
  "s_add_u32 m0, s80, 16384\n"
  "s_add_u32 s74, s40, 0\n"
  "s_addc_u32 s75, s41, 0\n"
  "global_load_lds_dwordx4 v4, s[74:75]\n"
  "s_add_u32 m0, s80, 20480\n"
  "s_add_u32 s74, s40, 262144\n"
  "s_addc_u32 s75, s41, 0\n"
  "global_load_lds_dwordx4 v4, s[74:75]\n"
  "s_add_u32 m0, s80, 24576\n"
  "s_add_u32 s74, s38, 524288\n"
  "s_addc_u32 s75, s39, 0\n"
  "global_load_lds_dwordx4 v1, s[74:75]\n"
  "s_add_u32 m0, s80, 28672\n"
  "s_add_u32 s74, s38, 655360\n"
  "s_addc_u32 s75, s39, 0\n"
  "global_load_lds_dwordx4 v1, s[74:75]\n"
  "s_add_u32 m0, s80, 32768\n"
  "s_add_u32 s74, s38, 786432\n"
  "s_addc_u32 s75, s39, 0\n"
  "global_load_lds_dwordx4 v1, s[74:75]\n"
  "s_add_u32 m0, s80, 36864\n"
  "s_add_u32 s74, s38, 917504\n"
  "s_addc_u32 s75, s39, 0\n"
  "global_load_lds_dwordx4 v1, s[74:75]\n"
  "s_add_u32 m0, s80, 40960\n"
  "s_add_u32 s74, s40, 524288\n"
  "s_addc_u32 s75, s41, 0\n"
  "global_load_lds_dwordx4 v4, s[74:75]\n"
  "s_add_u32 m0, s80, 45056\n"
  "s_add_u32 s74, s40, 786432\n"
  "s_addc_u32 s75, s41, 0\n"
  "global_load_lds_dwordx4 v4, s[74:75]\n"
  "s_add_u32 m0, s80, 49152\n"
  "s_add_u32 s74, s38, 1048576\n"
  "s_addc_u32 s75, s39, 0\n"
  "global_load_lds_dwordx4 v1, s[74:75]\n"
  "s_add_u32 m0, s80, 53248\n"
  "s_add_u32 s74, s38, 1179648\n"
  "s_addc_u32 s75, s39, 0\n"
  "global_load_lds_dwordx4 v1, s[74:75]\n"
  "s_add_u32 m0, s80, 57344\n"
  "s_add_u32 s74, s38, 1310720\n"
  "s_addc_u32 s75, s39, 0\n"
  "global_load_lds_dwordx4 v1, s[74:75]\n"
  "s_add_u32 m0, s80, 61440\n"
  "s_add_u32 s74, s38, 1441792\n"
  "s_addc_u32 s75, s39, 0\n"
  "global_load_lds_dwordx4 v1, s[74:75]\n"
  "s_add_u32 m0, s80, 65536\n"
  "s_add_u32 s74, s40, 1048576\n"
  "s_addc_u32 s75, s41, 0\n"
  "global_load_lds_dwordx4 v4, s[74:75]\n"
  "s_add_u32 m0, s80, 69632\n"
  "s_add_u32 s74, s40, 1310720\n"
  "s_addc_u32 s75, s41, 0\n"
  "global_load_lds_dwordx4 v4, s[74:75]\n"
  "s_add_u32 m0, s80, 73728\n"
  "s_add_u32 s74, s38, 1572864\n"
  "s_addc_u32 s75, s39, 0\n"
  "global_load_lds_dwordx4 v1, s[74:75]\n"
  "s_add_u32 m0, s80, 77824\n"
  "s_add_u32 s74, s38, 1703936\n"
  "s_addc_u32 s75, s39, 0\n"
  "global_load_lds_dwordx4 v1, s[74:75]\n"
  "s_add_u32 m0, s80, 81920\n"
  "s_add_u32 s74, s38, 1835008\n"
  "s_addc_u32 s75, s39, 0\n"
  "global_load_lds_dwordx4 v1, s[74:75]\n"
  "s_add_u32 m0, s80, 86016\n"
  "s_add_u32 s74, s38, 1966080\n"
  "s_addc_u32 s75, s39, 0\n"
  "global_load_lds_dwordx4 v1, s[74:75]\n"
  "s_add_u32 m0, s80, 90112\n"
  "s_add_u32 s74, s40, 1572864\n"
  "s_addc_u32 s75, s41, 0\n"
  "global_load_lds_dwordx4 v4, s[74:75]\n"
  "s_add_u32 m0, s80, 94208\n"
  "s_add_u32 s74, s40, 1835008\n"
  "s_addc_u32 s75, s41, 0\n"
  "global_load_lds_dwordx4 v4, s[74:75]\n"
  "s_waitcnt vmcnt(18) lgkmcnt(0)\n"
  "s_barrier\n"
  "s_add_u32 m0, s80, 98304\n"
  "s_add_u32 s74, s38, 2097152\n"
  "s_addc_u32 s75, s39, 0\n"
  "global_load_lds_dwordx4 v1, s[74:75]\n"
  "s_add_u32 m0, s80, 102400\n"
  "s_add_u32 s74, s38, 2228224\n"
  "s_addc_u32 s75, s39, 0\n"
  "global_load_lds_dwordx4 v1, s[74:75]\n"
  "s_add_u32 m0, s80, 106496\n"
  "s_add_u32 s74, s38, 2359296\n"
  "s_addc_u32 s75, s39, 0\n"
  "global_load_lds_dwordx4 v1, s[74:75]\n"
  "s_add_u32 m0, s80, 110592\n"
  "s_add_u32 s74, s38, 2490368\n"
  "s_addc_u32 s75, s39, 0\n"
  "global_load_lds_dwordx4 v1, s[74:75]\n"
  "s_add_u32 m0, s80, 114688\n"
  "s_add_u32 s74, s40, 2097152\n"
  "s_addc_u32 s75, s41, 0\n"
  "global_load_lds_dwordx4 v4, s[74:75]\n"
  "s_add_u32 m0, s80, 118784\n"
  "s_add_u32 s74, s40, 2359296\n"
  "s_addc_u32 s75, s41, 0\n"
  "global_load_lds_dwordx4 v4, s[74:75]\n"
  "v_xor_b32 v6, 0, v2\n"
  "v_add_u32 v6, 0, v6\n"
  "ds_read_b64_tr_b16 a[0:1], v6 offset:0\n"
  "ds_read_b64_tr_b16 a[2:3], v6 offset:1024\n"
  "ds_read_b64_tr_b16 a[4:5], v6 offset:8192\n"
  "ds_read_b64_tr_b16 a[6:7], v6 offset:9216\n"
  "v_xor_b32 v6, 32, v2\n"
  "v_add_u32 v6, 0, v6\n"
  "ds_read_b64_tr_b16 a[72:73], v6 offset:0\n"
  "ds_read_b64_tr_b16 a[74:75], v6 offset:1024\n"
  "ds_read_b64_tr_b16 a[76:77], v6 offset:8192\n"
  "ds_read_b64_tr_b16 a[78:79], v6 offset:9216\n"
  "v_add_u32 v6, 16384, v5\n"
  "ds_read_b64_tr_b16 a[144:145], v6 offset:0\n"
  "ds_read_b64_tr_b16 a[146:147], v6 offset:512\n"
  "ds_read_b64_tr_b16 a[148:149], v6 offset:4096\n"
  "ds_read_b64_tr_b16 a[150:151], v6 offset:4608\n"
  "s_waitcnt vmcnt(18) lgkmcnt(0)\n"
  "s_barrier\n"
  "s_add_u32 m0, s80, 0\n"
  "s_add_u32 s74, s38, 2621440\n"
  "s_addc_u32 s75, s39, 0\n"
  "global_load_lds_dwordx4 v1, s[74:75]\n"
  "s_add_u32 m0, s80, 4096\n"
  "s_add_u32 s74, s38, 2752512\n"
  "s_addc_u32 s75, s39, 0\n"
  "global_load_lds_dwordx4 v1, s[74:75]\n"
  "s_add_u32 m0, s80, 8192\n"
  "s_add_u32 s74, s38, 2883584\n"
  "s_addc_u32 s75, s39, 0\n"
  "global_load_lds_dwordx4 v1, s[74:75]\n"
  "s_add_u32 m0, s80, 12288\n"
  "s_add_u32 s74, s38, 3014656\n"
  "s_addc_u32 s75, s39, 0\n"
  "global_load_lds_dwordx4 v1, s[74:75]\n"
  "s_add_u32 m0, s80, 16384\n"
  "s_add_u32 s74, s40, 2621440\n"
  "s_addc_u32 s75, s41, 0\n"
  "global_load_lds_dwordx4 v4, s[74:75]\n"
  "s_add_u32 m0, s80, 20480\n"
  "s_add_u32 s74, s40, 2883584\n"
  "s_addc_u32 s75, s41, 0\n"
  "global_load_lds_dwordx4 v4, s[74:75]\n"
  "v_xor_b32 v6, 0, v2\n"
  "v_add_u32 v6, 24576, v6\n"
  "ds_read_b64_tr_b16 a[8:9], v6 offset:0\n"
  "ds_read_b64_tr_b16 a[10:11], v6 offset:1024\n"
  "ds_read_b64_tr_b16 a[12:13], v6 offset:8192\n"
  "ds_read_b64_tr_b16 a[14:15], v6 offset:9216\n"
  "v_xor_b32 v6, 32, v2\n"
  "v_add_u32 v6, 24576, v6\n"
  "ds_read_b64_tr_b16 a[80:81], v6 offset:0\n"
  "ds_read_b64_tr_b16 a[82:83], v6 offset:1024\n"
  "ds_read_b64_tr_b16 a[84:85], v6 offset:8192\n"
  "ds_read_b64_tr_b16 a[86:87], v6 offset:9216\n"
  "v_add_u32 v6, 40960, v5\n"
  "ds_read_b64_tr_b16 a[152:153], v6 offset:0\n"
  "ds_read_b64_tr_b16 a[154:155], v6 offset:512\n"
  "ds_read_b64_tr_b16 a[156:157], v6 offset:4096\n"
  "ds_read_b64_tr_b16 a[158:159], v6 offset:4608\n"
  "s_waitcnt vmcnt(18) lgkmcnt(0)\n"
  "s_barrier\n"
  "s_add_u32 m0, s80, 24576\n"
  "s_add_u32 s74, s38, 3145728\n"
  "s_addc_u32 s75, s39, 0\n"
  "global_load_lds_dwordx4 v1, s[74:75]\n"
  "s_add_u32 m0, s80, 28672\n"
  "s_add_u32 s74, s38, 3276800\n"
  "s_addc_u32 s75, s39, 0\n"
  "global_load_lds_dwordx4 v1, s[74:75]\n"
  "s_add_u32 m0, s80, 32768\n"
  "s_add_u32 s74, s38, 3407872\n"
  "s_addc_u32 s75, s39, 0\n"
  "global_load_lds_dwordx4 v1, s[74:75]\n"
  "s_add_u32 m0, s80, 36864\n"
  "s_add_u32 s74, s38, 3538944\n"
  "s_addc_u32 s75, s39, 0\n"
  "global_load_lds_dwordx4 v1, s[74:75]\n"
  "s_add_u32 m0, s80, 40960\n"
  "s_add_u32 s74, s40, 3145728\n"
  "s_addc_u32 s75, s41, 0\n"
  "global_load_lds_dwordx4 v4, s[74:75]\n"
  "s_add_u32 m0, s80, 45056\n"
  "s_add_u32 s74, s40, 3407872\n"
  "s_addc_u32 s75, s41, 0\n"
  "global_load_lds_dwordx4 v4, s[74:75]\n"
  "v_xor_b32 v6, 0, v2\n"
  "v_add_u32 v6, 49152, v6\n"
  "ds_read_b64_tr_b16 a[16:17], v6 offset:0\n"
  "ds_read_b64_tr_b16 a[18:19], v6 offset:1024\n"
  "ds_read_b64_tr_b16 a[20:21], v6 offset:8192\n"
  "ds_read_b64_tr_b16 a[22:23], v6 offset:9216\n"
  "v_xor_b32 v6, 32, v2\n"
  "v_add_u32 v6, 49152, v6\n"
  "ds_read_b64_tr_b16 a[88:89], v6 offset:0\n"
  "ds_read_b64_tr_b16 a[90:91], v6 offset:1024\n"
  "ds_read_b64_tr_b16 a[92:93], v6 offset:8192\n"
  "ds_read_b64_tr_b16 a[94:95], v6 offset:9216\n"
  "v_add_u32 v6, 65536, v5\n"
  "ds_read_b64_tr_b16 a[160:161], v6 offset:0\n"
  "ds_read_b64_tr_b16 a[162:163], v6 offset:512\n"
  "ds_read_b64_tr_b16 a[164:165], v6 offset:4096\n"
  "ds_read_b64_tr_b16 a[166:167], v6 offset:4608\n"
  "s_waitcnt vmcnt(18) lgkmcnt(0)\n"
  "s_barrier\n"
  "s_add_u32 m0, s80, 49152\n"
  "s_add_u32 s74, s38, 3670016\n"
  "s_addc_u32 s75, s39, 0\n"
  "global_load_lds_dwordx4 v1, s[74:75]\n"
  "s_add_u32 m0, s80, 53248\n"
  "s_add_u32 s74, s38, 3801088\n"
  "s_addc_u32 s75, s39, 0\n"
  "global_load_lds_dwordx4 v1, s[74:75]\n"
  "s_add_u32 m0, s80, 57344\n"
  "s_add_u32 s74, s38, 3932160\n"
  "s_addc_u32 s75, s39, 0\n"
  "global_load_lds_dwordx4 v1, s[74:75]\n"
  "s_add_u32 m0, s80, 61440\n"
  "s_add_u32 s74, s38, 4063232\n"
  "s_addc_u32 s75, s39, 0\n"
  "global_load_lds_dwordx4 v1, s[74:75]\n"
  "s_add_u32 m0, s80, 65536\n"
  "s_add_u32 s74, s40, 3670016\n"
  "s_addc_u32 s75, s41, 0\n"
  "global_load_lds_dwordx4 v4, s[74:75]\n"
  "s_add_u32 m0, s80, 69632\n"
  "s_add_u32 s74, s40, 3932160\n"
  "s_addc_u32 s75, s41, 0\n"
  "global_load_lds_dwordx4 v4, s[74:75]\n"
  "v_xor_b32 v6, 0, v2\n"
  "v_add_u32 v6, 73728, v6\n"
  "ds_read_b64_tr_b16 a[24:25], v6 offset:0\n"
  "ds_read_b64_tr_b16 a[26:27], v6 offset:1024\n"
  "ds_read_b64_tr_b16 a[28:29], v6 offset:8192\n"
  "ds_read_b64_tr_b16 a[30:31], v6 offset:9216\n"
  "v_xor_b32 v6, 32, v2\n"
  "v_add_u32 v6, 73728, v6\n"
  "ds_read_b64_tr_b16 a[96:97], v6 offset:0\n"
  "ds_read_b64_tr_b16 a[98:99], v6 offset:1024\n"
  "ds_read_b64_tr_b16 a[100:101], v6 offset:8192\n"
  "ds_read_b64_tr_b16 a[102:103], v6 offset:9216\n"
  "v_add_u32 v6, 90112, v5\n"
  "ds_read_b64_tr_b16 a[168:169], v6 offset:0\n"
  "ds_read_b64_tr_b16 a[170:171], v6 offset:512\n"
  "ds_read_b64_tr_b16 a[172:173], v6 offset:4096\n"
  "ds_read_b64_tr_b16 a[174:175], v6 offset:4608\n"
  "s_waitcnt vmcnt(18) lgkmcnt(0)\n"
  "s_barrier\n"
  "s_add_u32 m0, s80, 73728\n"
  "s_add_u32 s74, s38, 4194304\n"
  "s_addc_u32 s75, s39, 0\n"
  "global_load_lds_dwordx4 v1, s[74:75]\n"
  "s_add_u32 m0, s80, 77824\n"
  "s_add_u32 s74, s38, 4325376\n"
  "s_addc_u32 s75, s39, 0\n"
  "global_load_lds_dwordx4 v1, s[74:75]\n"
  "s_add_u32 m0, s80, 81920\n"
  "s_add_u32 s74, s38, 4456448\n"
  "s_addc_u32 s75, s39, 0\n"
  "global_load_lds_dwordx4 v1, s[74:75]\n"
  "s_add_u32 m0, s80, 86016\n"
  "s_add_u32 s74, s38, 4587520\n"
  "s_addc_u32 s75, s39, 0\n"
  "global_load_lds_dwordx4 v1, s[74:75]\n"
  "s_add_u32 m0, s80, 90112\n"
  "s_add_u32 s74, s40, 4194304\n"
  "s_addc_u32 s75, s41, 0\n"
  "global_load_lds_dwordx4 v4, s[74:75]\n"
  "s_add_u32 m0, s80, 94208\n"
  "s_add_u32 s74, s40, 4456448\n"
  "s_addc_u32 s75, s41, 0\n"
  "global_load_lds_dwordx4 v4, s[74:75]\n"
  "v_xor_b32 v6, 0, v2\n"
  "v_add_u32 v6, 98304, v6\n"
  "ds_read_b64_tr_b16 a[32:33], v6 offset:0\n"
  "ds_read_b64_tr_b16 a[34:35], v6 offset:1024\n"
  "ds_read_b64_tr_b16 a[36:37], v6 offset:8192\n"
  "ds_read_b64_tr_b16 a[38:39], v6 offset:9216\n"
  "v_xor_b32 v6, 32, v2\n"
  "v_add_u32 v6, 98304, v6\n"
  "ds_read_b64_tr_b16 a[104:105], v6 offset:0\n"
  "ds_read_b64_tr_b16 a[106:107], v6 offset:1024\n"
  "ds_read_b64_tr_b16 a[108:109], v6 offset:8192\n"
  "ds_read_b64_tr_b16 a[110:111], v6 offset:9216\n"
  "v_add_u32 v6, 114688, v5\n"
  "ds_read_b64_tr_b16 a[176:177], v6 offset:0\n"
  "ds_read_b64_tr_b16 a[178:179], v6 offset:512\n"
  "ds_read_b64_tr_b16 a[180:181], v6 offset:4096\n"
  "ds_read_b64_tr_b16 a[182:183], v6 offset:4608\n"
  "s_waitcnt vmcnt(18) lgkmcnt(0)\n"
  "s_barrier\n"
  "v_xor_b32 v6, 0, v2\n"
  "v_add_u32 v6, 0, v6\n"
  "ds_read_b64_tr_b16 a[40:41], v6 offset:0\n"
  "ds_read_b64_tr_b16 a[42:43], v6 offset:1024\n"
  "ds_read_b64_tr_b16 a[44:45], v6 offset:8192\n"
  "ds_read_b64_tr_b16 a[46:47], v6 offset:9216\n"
  "v_xor_b32 v6, 32, v2\n"
  "v_add_u32 v6, 0, v6\n"
  "ds_read_b64_tr_b16 a[112:113], v6 offset:0\n"
  "ds_read_b64_tr_b16 a[114:115], v6 offset:1024\n"
  "ds_read_b64_tr_b16 a[116:117], v6 offset:8192\n"
  "ds_read_b64_tr_b16 a[118:119], v6 offset:9216\n"
  "v_add_u32 v6, 16384, v5\n"
  "ds_read_b64_tr_b16 a[184:185], v6 offset:0\n"
  "ds_read_b64_tr_b16 a[186:187], v6 offset:512\n"
  "ds_read_b64_tr_b16 a[188:189], v6 offset:4096\n"
  "ds_read_b64_tr_b16 a[190:191], v6 offset:4608\n"
  "s_waitcnt vmcnt(12) lgkmcnt(0)\n"
  "s_barrier\n"
  "v_xor_b32 v6, 0, v2\n"
  "v_add_u32 v6, 24576, v6\n"
  "ds_read_b64_tr_b16 a[48:49], v6 offset:0\n"
  "ds_read_b64_tr_b16 a[50:51], v6 offset:1024\n"
  "ds_read_b64_tr_b16 a[52:53], v6 offset:8192\n"
  "ds_read_b64_tr_b16 a[54:55], v6 offset:9216\n"
  "v_xor_b32 v6, 32, v2\n"
  "v_add_u32 v6, 24576, v6\n"
  "ds_read_b64_tr_b16 a[120:121], v6 offset:0\n"
  "ds_read_b64_tr_b16 a[122:123], v6 offset:1024\n"
  "ds_read_b64_tr_b16 a[124:125], v6 offset:8192\n"
  "ds_read_b64_tr_b16 a[126:127], v6 offset:9216\n"
  "v_add_u32 v6, 40960, v5\n"
  "ds_read_b64_tr_b16 a[192:193], v6 offset:0\n"
  "ds_read_b64_tr_b16 a[194:195], v6 offset:512\n"
  "ds_read_b64_tr_b16 a[196:197], v6 offset:4096\n"
  "ds_read_b64_tr_b16 a[198:199], v6 offset:4608\n"
  "s_waitcnt vmcnt(6) lgkmcnt(0)\n"
  "s_barrier\n"
  "v_xor_b32 v6, 0, v2\n"
  "v_add_u32 v6, 49152, v6\n"
  "ds_read_b64_tr_b16 a[56:57], v6 offset:0\n"
  "ds_read_b64_tr_b16 a[58:59], v6 offset:1024\n"
  "ds_read_b64_tr_b16 a[60:61], v6 offset:8192\n"
  "ds_read_b64_tr_b16 a[62:63], v6 offset:9216\n"
  "v_xor_b32 v6, 32, v2\n"
  "v_add_u32 v6, 49152, v6\n"
  "ds_read_b64_tr_b16 a[128:129], v6 offset:0\n"
  "ds_read_b64_tr_b16 a[130:131], v6 offset:1024\n"
  "ds_read_b64_tr_b16 a[132:133], v6 offset:8192\n"
  "ds_read_b64_tr_b16 a[134:135], v6 offset:9216\n"
  "v_add_u32 v6, 65536, v5\n"
  "ds_read_b64_tr_b16 a[200:201], v6 offset:0\n"
  "ds_read_b64_tr_b16 a[202:203], v6 offset:512\n"
  "ds_read_b64_tr_b16 a[204:205], v6 offset:4096\n"
  "ds_read_b64_tr_b16 a[206:207], v6 offset:4608\n"
  "s_waitcnt vmcnt(0) lgkmcnt(0)\n"
  "s_barrier\n"
  "v_xor_b32 v6, 0, v2\n"
  "v_add_u32 v6, 73728, v6\n"
  "ds_read_b64_tr_b16 a[64:65], v6 offset:0\n"
  "ds_read_b64_tr_b16 a[66:67], v6 offset:1024\n"
  "ds_read_b64_tr_b16 a[68:69], v6 offset:8192\n"
  "ds_read_b64_tr_b16 a[70:71], v6 offset:9216\n"
  "v_xor_b32 v6, 32, v2\n"
  "v_add_u32 v6, 73728, v6\n"
  "ds_read_b64_tr_b16 a[136:137], v6 offset:0\n"
  "ds_read_b64_tr_b16 a[138:139], v6 offset:1024\n"
  "ds_read_b64_tr_b16 a[140:141], v6 offset:8192\n"
  "ds_read_b64_tr_b16 a[142:143], v6 offset:9216\n"
  "v_add_u32 v6, 90112, v5\n"
  "ds_read_b64_tr_b16 a[208:209], v6 offset:0\n"
  "ds_read_b64_tr_b16 a[210:211], v6 offset:512\n"
  "ds_read_b64_tr_b16 a[212:213], v6 offset:4096\n"
  "ds_read_b64_tr_b16 a[214:215], v6 offset:4608\n"
  "s_mov_b32 s59, 400000\n"
  "s_add_u32 s74, s36, 16778624\n"
  "s_addc_u32 s75, s37, 0\n"
  "PLCP:\n"
  "v_mov_b32 v6, 0\n"
  "global_load_dword v6, v6, s[74:75] sc1\n"
  "s_waitcnt vmcnt(0)\n"
  "v_readfirstlane_b32 s76, v6\n"
  "s_cmp_ge_u32 s76, 0x40000\n"
  "s_cbranch_scc1 PDCP\n"
  "s_sub_u32 s59, s59, 1\n"
  "s_cbranch_scc1 PJQ\n"
  "s_sleep 4\n"
  "s_branch PLCP\n"
  "PDCP:\n"
  "s_and_b32 s74, s76, 2\n"
  "s_lshr_b32 s75, s74, 1\n"
  "s_lshl_b32 s4, 0x55555555, s75\n"
  "s_mov_b32 s5, s4\n"
  "s_lshl_b32 s74, s74, 12\n"
  "s_xor_b32 s46, s74, s46\n"
  "s_add_u32 s64, s36, s46\n"
  "s_addc_u32 s65, s37, 0\n"
  "s_mov_b32 s56, 0\n"
  "s_mov_b32 s57, 122880\n"
  "v_add_u32 v12, 122880, v9\n"
  "v_xor_b32 v13, 64, v12\n"
  "s_waitcnt lgkmcnt(0)\n"
  "s_barrier\n"
  "s_add_u32 s58, s80, 12288\n"
  "s_add_u32 s42, s42, 36864\n"
  "s_addc_u32 s43, s43, 0\n"
  "ds_read_b128 v[16:19], v12 offset:0\n"
  "ds_read_b128 v[20:23], v12 offset:2048\n"
  "ds_read_b128 v[24:27], v13 offset:0\n"
  "ds_read_b128 v[28:31], v13 offset:2048\n"
  "ds_read_b128 v[32:35], v12 offset:4096\n"
  "ds_read_b128 v[36:39], v12 offset:6144\n"
  "s_add_u32 m0, s58, 0\n"
  "s_nop 0\n"
  "global_load_lds_dwordx4 v3, s[42:43] offset:0\n"
  "s_add_u32 m0, s58, 3968\n"
  "s_nop 0\n"
  "global_load_lds_dwordx4 v3, s[42:43] offset:128\n"
  "s_add_u32 m0, s58, 7936\n"
  "s_nop 0\n"
  "global_load_lds_dwordx4 v3, s[42:43] offset:256\n"
  "s_add_u32 m0, s58, 11904\n"
  "s_nop 0\n"
  "global_load_lds_dwordx4 v3, s[42:43] offset:384\n"
  "s_add_u32 m0, s58, 15872\n"
  "s_nop 0\n"
  "global_load_lds_dwordx4 v3, s[42:43] offset:512\n"
  "s_add_u32 m0, s58, 19840\n"
  "s_nop 0\n"
  "global_load_lds_dwordx4 v3, s[42:43] offset:640\n"
  "s_add_u32 m0, s58, 23808\n"
  "s_nop 0\n"
  "global_load_lds_dwordx4 v3, s[42:43] offset:768\n"
  "s_add_u32 m0, s58, 27776\n"
  "s_nop 0\n"
  "global_load_lds_dwordx4 v3, s[42:43] offset:896\n"
  "s_add_u32 m0, s58, 31744\n"
  "s_nop 0\n"
  "global_load_lds_dwordx4 v3, s[42:43] offset:1024\n"
  "v_mov_b64 v[10:11], v[12:13]\n"
  "s_waitcnt lgkmcnt(5)\n"
  "v_mfma_f32_16x16x32_bf16 v[48:51], a[0:3], v[16:19], 0\n"
  "ds_read_b128 v[40:43], v11 offset:4096\n"
  "s_waitcnt lgkmcnt(5)\n"
  "v_mfma_f32_16x16x32_bf16 v[52:55], a[0:3], v[20:23], 0\n"
  "ds_read_b128 v[44:47], v11 offset:6144\n"
  "v_mfma_f32_16x16x32_bf16 v[56:59], a[72:75], v[16:19], 0\n"
  "s_add_u32 s57, s57, 36864\n"
  "s_cmp_eq_u32 s57, 159744\n"
  "v_mfma_f32_16x16x32_bf16 v[60:63], a[72:75], v[20:23], 0\n"
  "s_cselect_b32 s57, 12288, s57\n"
  "v_add_u32 v12, s57, v9\n"
  "v_mfma_f32_16x16x32_bf16 v[64:67], v[16:19], a[144:147], 0\n"
  "v_xor_b32 v13, 64, v12\n"
  "s_add_u32 s58, s57, 36864\n"
  "v_mfma_f32_16x16x32_bf16 v[68:71], v[20:23], a[144:147], 0\n"
  "s_cmp_eq_u32 s57, 122880\n"
  "s_cselect_b32 s58, 12288, s58\n"
  "s_waitcnt lgkmcnt(5)\n"
  "v_mfma_f32_16x16x32_bf16 v[48:51], a[4:7], v[24:27], v[48:51]\n"
  "ds_read_b128 v[16:19], v10 offset:8192\n"
  "s_waitcnt lgkmcnt(5)\n"
  "v_mfma_f32_16x16x32_bf16 v[52:55], a[4:7], v[28:31], v[52:55]\n"
  "ds_read_b128 v[20:23], v10 offset:10240\n"
  "v_mfma_f32_16x16x32_bf16 v[56:59], a[76:79], v[24:27], v[56:59]\n"
  "s_add_u32 s58, s58, s80\n"
  "s_cmp_lt_u32 s56, 62\n"
  "v_mfma_f32_16x16x32_bf16 v[60:63], a[76:79], v[28:31], v[60:63]\n"
  "s_cselect_b32 s74, 36864, 0\n"
  "s_add_u32 s42, s42, s74\n"
  "v_mfma_f32_16x16x32_bf16 v[64:67], v[24:27], a[148:151], v[64:67]\n"
  "s_addc_u32 s43, s43, 0\n"
  "v_mfma_f32_16x16x32_bf16 v[68:71], v[28:31], a[148:151], v[68:71]\n"
  "s_waitcnt lgkmcnt(5)\n"
  "v_mfma_f32_16x16x32_bf16 v[48:51], a[8:11], v[32:35], v[48:51]\n"
  "ds_read_b128 v[24:27], v11 offset:8192\n"
  "s_waitcnt lgkmcnt(5)\n"
  "v_mfma_f32_16x16x32_bf16 v[52:55], a[8:11], v[36:39], v[52:55]\n"
  "ds_read_b128 v[28:31], v11 offset:10240\n"
  "v_mfma_f32_16x16x32_bf16 v[56:59], a[80:83], v[32:35], v[56:59]\n"
  "v_mfma_f32_16x16x32_bf16 v[60:63], a[80:83], v[36:39], v[60:63]\n"
  "v_mfma_f32_16x16x32_bf16 v[64:67], v[32:35], a[152:155], v[64:67]\n"
  "v_mfma_f32_16x16x32_bf16 v[68:71], v[36:39], a[152:155], v[68:71]\n"
  "s_waitcnt lgkmcnt(5)\n"
  "v_mfma_f32_16x16x32_bf16 v[48:51], a[12:15], v[40:43], v[48:51]\n"
  "ds_read_b128 v[32:35], v10 offset:12288\n"
  "s_waitcnt lgkmcnt(5)\n"
  "v_mfma_f32_16x16x32_bf16 v[52:55], a[12:15], v[44:47], v[52:55]\n"
  "ds_read_b128 v[36:39], v10 offset:14336\n"
  "v_mfma_f32_16x16x32_bf16 v[56:59], a[84:87], v[40:43], v[56:59]\n"
  "v_mfma_f32_16x16x32_bf16 v[60:63], a[84:87], v[44:47], v[60:63]\n"
  "v_mfma_f32_16x16x32_bf16 v[64:67], v[40:43], a[156:159], v[64:67]\n"
  "v_mfma_f32_16x16x32_bf16 v[68:71], v[44:47], a[156:159], v[68:71]\n"
  "s_waitcnt lgkmcnt(5)\n"
  "v_mfma_f32_16x16x32_bf16 v[48:51], a[16:19], v[16:19], v[48:51]\n"
  "ds_read_b128 v[40:43], v11 offset:12288\n"
  "s_waitcnt lgkmcnt(5)\n"
  "v_mfma_f32_16x16x32_bf16 v[52:55], a[16:19], v[20:23], v[52:55]\n"
  "ds_read_b128 v[44:47], v11 offset:14336\n"
  "v_mfma_f32_16x16x32_bf16 v[56:59], a[88:91], v[16:19], v[56:59]\n"
  "v_mfma_f32_16x16x32_bf16 v[60:63], a[88:91], v[20:23], v[60:63]\n"
  "v_mfma_f32_16x16x32_bf16 v[64:67], v[16:19], a[160:163], v[64:67]\n"
  "v_mfma_f32_16x16x32_bf16 v[68:71], v[20:23], a[160:163], v[68:71]\n"
  "s_waitcnt lgkmcnt(5)\n"
  "v_mfma_f32_16x16x32_bf16 v[48:51], a[20:23], v[24:27], v[48:51]\n"
  "ds_read_b128 v[16:19], v10 offset:16384\n"
  "s_waitcnt lgkmcnt(5)\n"
  "v_mfma_f32_16x16x32_bf16 v[52:55], a[20:23], v[28:31], v[52:55]\n"
  "ds_read_b128 v[20:23], v10 offset:18432\n"
  "v_mfma_f32_16x16x32_bf16 v[56:59], a[92:95], v[24:27], v[56:59]\n"
  "v_mfma_f32_16x16x32_bf16 v[60:63], a[92:95], v[28:31], v[60:63]\n"
  "v_mfma_f32_16x16x32_bf16 v[64:67], v[24:27], a[164:167], v[64:67]\n"
  "v_mfma_f32_16x16x32_bf16 v[68:71], v[28:31], a[164:167], v[68:71]\n"
  "s_waitcnt lgkmcnt(5)\n"
  "v_mfma_f32_16x16x32_bf16 v[48:51], a[24:27], v[32:35], v[48:51]\n"
  "ds_read_b128 v[24:27], v11 offset:16384\n"
  "s_waitcnt lgkmcnt(5)\n"
  "v_mfma_f32_16x16x32_bf16 v[52:55], a[24:27], v[36:39], v[52:55]\n"
  "ds_read_b128 v[28:31], v11 offset:18432\n"
  "v_mfma_f32_16x16x32_bf16 v[56:59], a[96:99], v[32:35], v[56:59]\n"
  "v_mfma_f32_16x16x32_bf16 v[60:63], a[96:99], v[36:39], v[60:63]\n"
  "s_add_u32 m0, s58, 0\n"
  "s_nop 0\n"
  "global_load_lds_dwordx4 v3, s[42:43] offset:0\n"
  "v_mfma_f32_16x16x32_bf16 v[64:67], v[32:35], a[168:171], v[64:67]\n"
  "v_mfma_f32_16x16x32_bf16 v[68:71], v[36:39], a[168:171], v[68:71]\n"
  "s_waitcnt lgkmcnt(5)\n"
  "v_mfma_f32_16x16x32_bf16 v[48:51], a[28:31], v[40:43], v[48:51]\n"
  "ds_read_b128 v[32:35], v10 offset:20480\n"
  "s_waitcnt lgkmcnt(5)\n"
  "v_mfma_f32_16x16x32_bf16 v[52:55], a[28:31], v[44:47], v[52:55]\n"
  "ds_read_b128 v[36:39], v10 offset:22528\n"
  "v_mfma_f32_16x16x32_bf16 v[56:59], a[100:103], v[40:43], v[56:59]\n"
  "v_mfma_f32_16x16x32_bf16 v[60:63], a[100:103], v[44:47], v[60:63]\n"
  "s_add_u32 m0, s58, 3968\n"
  "s_nop 0\n"
  "global_load_lds_dwordx4 v3, s[42:43] offset:128\n"
  "v_mfma_f32_16x16x32_bf16 v[64:67], v[40:43], a[172:175], v[64:67]\n"
  "v_mfma_f32_16x16x32_bf16 v[68:71], v[44:47], a[172:175], v[68:71]\n"
  "s_waitcnt lgkmcnt(5)\n"
  "v_mfma_f32_16x16x32_bf16 v[48:51], a[32:35], v[16:19], v[48:51]\n"
  "ds_read_b128 v[40:43], v11 offset:20480\n"
  "s_waitcnt lgkmcnt(5)\n"
  "v_mfma_f32_16x16x32_bf16 v[52:55], a[32:35], v[20:23], v[52:55]\n"
  "ds_read_b128 v[44:47], v11 offset:22528\n"
  "v_mfma_f32_16x16x32_bf16 v[56:59], a[104:107], v[16:19], v[56:59]\n"
  "v_mfma_f32_16x16x32_bf16 v[60:63], a[104:107], v[20:23], v[60:63]\n"
  "s_add_u32 m0, s58, 7936\n"
  "s_nop 0\n"
  "global_load_lds_dwordx4 v3, s[42:43] offset:256\n"
  "v_mfma_f32_16x16x32_bf16 v[64:67], v[16:19], a[176:179], v[64:67]\n"
  "v_mfma_f32_16x16x32_bf16 v[68:71], v[20:23], a[176:179], v[68:71]\n"
  "s_waitcnt lgkmcnt(5)\n"
  "v_mfma_f32_16x16x32_bf16 v[48:51], a[36:39], v[24:27], v[48:51]\n"
  "ds_read_b128 v[16:19], v10 offset:24576\n"
  "s_waitcnt lgkmcnt(5)\n"
  "v_mfma_f32_16x16x32_bf16 v[52:55], a[36:39], v[28:31], v[52:55]\n"
  "ds_read_b128 v[20:23], v10 offset:26624\n"
  "v_mfma_f32_16x16x32_bf16 v[56:59], a[108:111], v[24:27], v[56:59]\n"
  "v_mfma_f32_16x16x32_bf16 v[60:63], a[108:111], v[28:31], v[60:63]\n"
  "s_add_u32 m0, s58, 11904\n"
  "s_nop 0\n"
  "global_load_lds_dwordx4 v3, s[42:43] offset:384\n"
  "v_mfma_f32_16x16x32_bf16 v[64:67], v[24:27], a[180:183], v[64:67]\n"
  "v_mfma_f32_16x16x32_bf16 v[68:71], v[28:31], a[180:183], v[68:71]\n"
  "s_waitcnt lgkmcnt(5)\n"
  "v_mfma_f32_16x16x32_bf16 v[48:51], a[40:43], v[32:35], v[48:51]\n"
  "ds_read_b128 v[24:27], v11 offset:24576\n"
  "s_waitcnt lgkmcnt(5)\n"
  "v_mfma_f32_16x16x32_bf16 v[52:55], a[40:43], v[36:39], v[52:55]\n"
  "ds_read_b128 v[28:31], v11 offset:26624\n"
  "v_mfma_f32_16x16x32_bf16 v[56:59], a[112:115], v[32:35], v[56:59]\n"
  "v_mfma_f32_16x16x32_bf16 v[60:63], a[112:115], v[36:39], v[60:63]\n"
  "s_add_u32 m0, s58, 15872\n"
  "s_nop 0\n"
  "global_load_lds_dwordx4 v3, s[42:43] offset:512\n"
  "v_mfma_f32_16x16x32_bf16 v[64:67], v[32:35], a[184:187], v[64:67]\n"
  "v_mfma_f32_16x16x32_bf16 v[68:71], v[36:39], a[184:187], v[68:71]\n"
  "s_waitcnt lgkmcnt(5)\n"
  "v_mfma_f32_16x16x32_bf16 v[48:51], a[44:47], v[40:43], v[48:51]\n"
  "ds_read_b128 v[32:35], v10 offset:28672\n"
  "s_waitcnt lgkmcnt(5)\n"
  "v_mfma_f32_16x16x32_bf16 v[52:55], a[44:47], v[44:47], v[52:55]\n"
  "ds_read_b128 v[36:39], v10 offset:30720\n"
  "v_mfma_f32_16x16x32_bf16 v[56:59], a[116:119], v[40:43], v[56:59]\n"
  "v_mfma_f32_16x16x32_bf16 v[60:63], a[116:119], v[44:47], v[60:63]\n"
  "s_add_u32 m0, s58, 19840\n"
  "s_nop 0\n"
  "global_load_lds_dwordx4 v3, s[42:43] offset:640\n"
  "v_mfma_f32_16x16x32_bf16 v[64:67], v[40:43], a[188:191], v[64:67]\n"
  "v_mfma_f32_16x16x32_bf16 v[68:71], v[44:47], a[188:191], v[68:71]\n"
  "s_waitcnt lgkmcnt(5)\n"
  "v_mfma_f32_16x16x32_bf16 v[48:51], a[48:51], v[16:19], v[48:51]\n"
  "ds_read_b128 v[40:43], v11 offset:28672\n"
  "s_waitcnt lgkmcnt(5)\n"
  "v_mfma_f32_16x16x32_bf16 v[52:55], a[48:51], v[20:23], v[52:55]\n"
  "ds_read_b128 v[44:47], v11 offset:30720\n"
  "v_mfma_f32_16x16x32_bf16 v[56:59], a[120:123], v[16:19], v[56:59]\n"
  "v_mfma_f32_16x16x32_bf16 v[60:63], a[120:123], v[20:23], v[60:63]\n"
  "s_add_u32 m0, s58, 23808\n"
  "s_nop 0\n"
  "global_load_lds_dwordx4 v3, s[42:43] offset:768\n"
  "v_mfma_f32_16x16x32_bf16 v[64:67], v[16:19], a[192:195], v[64:67]\n"
  "v_mfma_f32_16x16x32_bf16 v[68:71], v[20:23], a[192:195], v[68:71]\n"
  "s_waitcnt lgkmcnt(5)\n"
  "v_mfma_f32_16x16x32_bf16 v[48:51], a[52:55], v[24:27], v[48:51]\n"
  "ds_read_b128 v[16:19], v10 offset:32768\n"
  "s_waitcnt lgkmcnt(5)\n"
  "v_mfma_f32_16x16x32_bf16 v[52:55], a[52:55], v[28:31], v[52:55]\n"
  "ds_read_b128 v[20:23], v10 offset:34816\n"
  "v_mfma_f32_16x16x32_bf16 v[56:59], a[124:127], v[24:27], v[56:59]\n"
  "v_mfma_f32_16x16x32_bf16 v[60:63], a[124:127], v[28:31], v[60:63]\n"
  "s_add_u32 m0, s58, 27776\n"
  "s_nop 0\n"
  "global_load_lds_dwordx4 v3, s[42:43] offset:896\n"
  "v_mfma_f32_16x16x32_bf16 v[64:67], v[24:27], a[196:199], v[64:67]\n"
  "v_mfma_f32_16x16x32_bf16 v[68:71], v[28:31], a[196:199], v[68:71]\n"
  "s_waitcnt lgkmcnt(5)\n"
  "v_mfma_f32_16x16x32_bf16 v[48:51], a[56:59], v[32:35], v[48:51]\n"
  "ds_read_b128 v[24:27], v11 offset:32768\n"
  "s_waitcnt lgkmcnt(5)\n"
  "v_mfma_f32_16x16x32_bf16 v[52:55], a[56:59], v[36:39], v[52:55]\n"
  "ds_read_b128 v[28:31], v11 offset:34816\n"
  "v_mfma_f32_16x16x32_bf16 v[56:59], a[128:131], v[32:35], v[56:59]\n"
  "v_mfma_f32_16x16x32_bf16 v[60:63], a[128:131], v[36:39], v[60:63]\n"
  "s_add_u32 m0, s58, 31744\n"
  "s_nop 0\n"
  "global_load_lds_dwordx4 v3, s[42:43] offset:1024\n"
  "v_mfma_f32_16x16x32_bf16 v[64:67], v[32:35], a[200:203], v[64:67]\n"
  "v_mfma_f32_16x16x32_bf16 v[68:71], v[36:39], a[200:203], v[68:71]\n"
  "s_waitcnt vmcnt(9)\n"
  "s_barrier\n"
  "s_waitcnt lgkmcnt(5)\n"
  "v_mfma_f32_16x16x32_bf16 v[48:51], a[60:63], v[40:43], v[48:51]\n"
  "ds_read_b128 v[32:35], v12 offset:0\n"
  "s_waitcnt lgkmcnt(5)\n"
  "v_mfma_f32_16x16x32_bf16 v[52:55], a[60:63], v[44:47], v[52:55]\n"
  "ds_read_b128 v[36:39], v12 offset:2048\n"
  "v_mfma_f32_16x16x32_bf16 v[56:59], a[132:135], v[40:43], v[56:59]\n"
  "s_bitcmp1_b64 s[4:5], s56\n"
  "s_cselect_b32 s74, 8192, 0\n"
  "v_mfma_f32_16x16x32_bf16 v[60:63], a[132:135], v[44:47], v[60:63]\n"
  "s_and_b32 s75, s56, 63\n"
  "s_lshl_b32 s75, s75, 21\n"
  "v_mfma_f32_16x16x32_bf16 v[64:67], v[40:43], a[204:207], v[64:67]\n"
  "s_add_u32 s74, s74, s75\n"
  "s_add_u32 s74, s74, 20971520\n"
  "v_mfma_f32_16x16x32_bf16 v[68:71], v[44:47], a[204:207], v[68:71]\n"
  "s_xor_b32 s75, s74, s44\n"
  "s_add_u32 s60, s36, s75\n"
  "s_waitcnt lgkmcnt(5)\n"
  "v_mfma_f32_16x16x32_bf16 v[48:51], a[64:67], v[16:19], v[48:51]\n"
  "ds_read_b128 v[40:43], v13 offset:0\n"
  "s_waitcnt lgkmcnt(5)\n"
  "v_mfma_f32_16x16x32_bf16 v[52:55], a[64:67], v[20:23], v[52:55]\n"
  "ds_read_b128 v[44:47], v13 offset:2048\n"
  "v_mfma_f32_16x16x32_bf16 v[56:59], a[136:139], v[16:19], v[56:59]\n"
  "s_addc_u32 s61, s37, 0\n"
  "s_xor_b32 s75, s74, s45\n"
  "v_mfma_f32_16x16x32_bf16 v[60:63], a[136:139], v[20:23], v[60:63]\n"
  "s_add_u32 s62, s36, s75\n"
  "s_addc_u32 s63, s37, 0\n"
  "v_mfma_f32_16x16x32_bf16 v[64:67], v[16:19], a[208:211], v[64:67]\n"
  "v_mfma_f32_16x16x32_bf16 v[68:71], v[20:23], a[208:211], v[68:71]\n"
  "s_waitcnt lgkmcnt(5)\n"
  "v_mfma_f32_16x16x32_bf16 v[48:51], a[68:71], v[24:27], v[48:51]\n"
  "ds_read_b128 v[16:19], v12 offset:4096\n"
  "s_waitcnt lgkmcnt(5)\n"
  "v_mfma_f32_16x16x32_bf16 v[52:55], a[68:71], v[28:31], v[52:55]\n"
  "ds_read_b128 v[20:23], v12 offset:6144\n"
  "v_mfma_f32_16x16x32_bf16 v[56:59], a[140:143], v[24:27], v[56:59]\n"
  "v_mfma_f32_16x16x32_bf16 v[60:63], a[140:143], v[28:31], v[60:63]\n"
  "v_mfma_f32_16x16x32_bf16 v[64:67], v[24:27], a[212:215], v[64:67]\n"
  "v_mfma_f32_16x16x32_bf16 v[68:71], v[28:31], a[212:215], v[68:71]\n"
  "s_add_u32 s56, s56, 1\n"
  "v_mul_f32 v48, v8, v48\n"
  "v_mul_f32 v49, v8, v49\n"
  "v_mul_f32 v50, v8, v50\n"
  "v_mul_f32 v51, v8, v51\n"
  "v_cvt_pk_bf16_f32 v96, v48, v49\n"
  "v_cvt_pk_bf16_f32 v97, v50, v51\n"
  "v_mul_f32 v56, v8, v56\n"
  "v_mul_f32 v57, v8, v57\n"
  "v_mul_f32 v58, v8, v58\n"
  "v_mul_f32 v59, v8, v59\n"
  "v_cvt_pk_bf16_f32 v98, v56, v57\n"
  "v_cvt_pk_bf16_f32 v99, v58, v59\n"
  "global_store_dwordx4 v0, v[96:99], s[60:61] offset:0 sc1\n"
  "v_mul_f32 v52, v8, v52\n"
  "v_mul_f32 v53, v8, v53\n"
  "v_mul_f32 v54, v8, v54\n"
  "v_mul_f32 v55, v8, v55\n"
  "v_cvt_pk_bf16_f32 v100, v52, v53\n"
  "v_cvt_pk_bf16_f32 v101, v54, v55\n"
  "v_mul_f32 v60, v8, v60\n"
  "v_mul_f32 v61, v8, v61\n"
  "v_mul_f32 v62, v8, v62\n"
  "v_mul_f32 v63, v8, v63\n"
  "v_cvt_pk_bf16_f32 v102, v60, v61\n"
  "v_cvt_pk_bf16_f32 v103, v62, v63\n"
  "global_store_dwordx4 v0, v[100:103], s[60:61] offset:1024 sc1\n"
  "v_cvt_pk_bf16_f32 v104, v64, v65\n"
  "v_cvt_pk_bf16_f32 v105, v66, v67\n"
  "v_cvt_pk_bf16_f32 v106, v68, v69\n"
  "v_cvt_pk_bf16_f32 v107, v70, v71\n"
  "global_store_dwordx4 v0, v[104:107], s[62:63] offset:0 sc1\n"
  "s_waitcnt vmcnt(0)\n"
  "s_barrier\n"
  "v_mov_b32 v6, 0\n"
  "v_mov_b32 v7, s56\n"
  "s_cmp_eq_u32 s47, 3\n"
  "s_cselect_b64 exec, 1, 0\n"
  "global_store_dword v6, v7, s[64:65] sc1\n"
  "s_mov_b64 exec, -1\n"
  "v_mov_b64 v[10:11], v[12:13]\n"
  "s_waitcnt lgkmcnt(5)\n"
  "v_mfma_f32_16x16x32_bf16 v[72:75], a[0:3], v[32:35], 0\n"
  "ds_read_b128 v[24:27], v11 offset:4096\n"
  "s_waitcnt lgkmcnt(5)\n"
  "v_mfma_f32_16x16x32_bf16 v[76:79], a[0:3], v[36:39], 0\n"
  "ds_read_b128 v[28:31], v11 offset:6144\n"
  "v_mfma_f32_16x16x32_bf16 v[80:83], a[72:75], v[32:35], 0\n"
  "s_add_u32 s57, s57, 36864\n"
  "s_cmp_eq_u32 s57, 159744\n"
  "v_mfma_f32_16x16x32_bf16 v[84:87], a[72:75], v[36:39], 0\n"
  "s_cselect_b32 s57, 12288, s57\n"
  "v_add_u32 v12, s57, v9\n"
  "v_mfma_f32_16x16x32_bf16 v[88:91], v[32:35], a[144:147], 0\n"
  "v_xor_b32 v13, 64, v12\n"
  "s_add_u32 s58, s57, 36864\n"
  "v_mfma_f32_16x16x32_bf16 v[92:95], v[36:39], a[144:147], 0\n"
  "s_cmp_eq_u32 s57, 122880\n"
  "s_cselect_b32 s58, 12288, s58\n"
  "s_waitcnt lgkmcnt(5)\n"
  "v_mfma_f32_16x16x32_bf16 v[72:75], a[4:7], v[40:43], v[72:75]\n"
  "ds_read_b128 v[32:35], v10 offset:8192\n"
  "s_waitcnt lgkmcnt(5)\n"
  "v_mfma_f32_16x16x32_bf16 v[76:79], a[4:7], v[44:47], v[76:79]\n"
  "ds_read_b128 v[36:39], v10 offset:10240\n"
  "v_mfma_f32_16x16x32_bf16 v[80:83], a[76:79], v[40:43], v[80:83]\n"
  "s_add_u32 s58, s58, s80\n"
  "s_cmp_lt_u32 s56, 62\n"
  "v_mfma_f32_16x16x32_bf16 v[84:87], a[76:79], v[44:47], v[84:87]\n"
  "s_cselect_b32 s74, 36864, 0\n"
  "s_add_u32 s42, s42, s74\n"
  "v_mfma_f32_16x16x32_bf16 v[88:91], v[40:43], a[148:151], v[88:91]\n"
  "s_addc_u32 s43, s43, 0\n"
  "v_mfma_f32_16x16x32_bf16 v[92:95], v[44:47], a[148:151], v[92:95]\n"
  "s_waitcnt lgkmcnt(5)\n"
  "v_mfma_f32_16x16x32_bf16 v[72:75], a[8:11], v[16:19], v[72:75]\n"
  "ds_read_b128 v[40:43], v11 offset:8192\n"
  "s_waitcnt lgkmcnt(5)\n"
  "v_mfma_f32_16x16x32_bf16 v[76:79], a[8:11], v[20:23], v[76:79]\n"
  "ds_read_b128 v[44:47], v11 offset:10240\n"
  "v_mfma_f32_16x16x32_bf16 v[80:83], a[80:83], v[16:19], v[80:83]\n"
  "v_mfma_f32_16x16x32_bf16 v[84:87], a[80:83], v[20:23], v[84:87]\n"
  "v_mfma_f32_16x16x32_bf16 v[88:91], v[16:19], a[152:155], v[88:91]\n"
  "v_mfma_f32_16x16x32_bf16 v[92:95], v[20:23], a[152:155], v[92:95]\n"
  "s_waitcnt lgkmcnt(5)\n"
  "v_mfma_f32_16x16x32_bf16 v[72:75], a[12:15], v[24:27], v[72:75]\n"
  "ds_read_b128 v[16:19], v10 offset:12288\n"
  "s_waitcnt lgkmcnt(5)\n"
  "v_mfma_f32_16x16x32_bf16 v[76:79], a[12:15], v[28:31], v[76:79]\n"
  "ds_read_b128 v[20:23], v10 offset:14336\n"
  "v_mfma_f32_16x16x32_bf16 v[80:83], a[84:87], v[24:27], v[80:83]\n"
  "v_mfma_f32_16x16x32_bf16 v[84:87], a[84:87], v[28:31], v[84:87]\n"
  "v_mfma_f32_16x16x32_bf16 v[88:91], v[24:27], a[156:159], v[88:91]\n"
  "v_mfma_f32_16x16x32_bf16 v[92:95], v[28:31], a[156:159], v[92:95]\n"
  "s_waitcnt lgkmcnt(5)\n"
  "v_mfma_f32_16x16x32_bf16 v[72:75], a[16:19], v[32:35], v[72:75]\n"
  "ds_read_b128 v[24:27], v11 offset:12288\n"
  "s_waitcnt lgkmcnt(5)\n"
  "v_mfma_f32_16x16x32_bf16 v[76:79], a[16:19], v[36:39], v[76:79]\n"
  "ds_read_b128 v[28:31], v11 offset:14336\n"
  "v_mfma_f32_16x16x32_bf16 v[80:83], a[88:91], v[32:35], v[80:83]\n"
  "v_mfma_f32_16x16x32_bf16 v[84:87], a[88:91], v[36:39], v[84:87]\n"
  "v_mfma_f32_16x16x32_bf16 v[88:91], v[32:35], a[160:163], v[88:91]\n"
  "v_mfma_f32_16x16x32_bf16 v[92:95], v[36:39], a[160:163], v[92:95]\n"
  "s_waitcnt lgkmcnt(5)\n"
  "v_mfma_f32_16x16x32_bf16 v[72:75], a[20:23], v[40:43], v[72:75]\n"
  "ds_read_b128 v[32:35], v10 offset:16384\n"
  "s_waitcnt lgkmcnt(5)\n"
  "v_mfma_f32_16x16x32_bf16 v[76:79], a[20:23], v[44:47], v[76:79]\n"
  "ds_read_b128 v[36:39], v10 offset:18432\n"
  "v_mfma_f32_16x16x32_bf16 v[80:83], a[92:95], v[40:43], v[80:83]\n"
  "v_mfma_f32_16x16x32_bf16 v[84:87], a[92:95], v[44:47], v[84:87]\n"
  "v_mfma_f32_16x16x32_bf16 v[88:91], v[40:43], a[164:167], v[88:91]\n"
  "v_mfma_f32_16x16x32_bf16 v[92:95], v[44:47], a[164:167], v[92:95]\n"
  "s_waitcnt lgkmcnt(5)\n"
  "v_mfma_f32_16x16x32_bf16 v[72:75], a[24:27], v[16:19], v[72:75]\n"
  "ds_read_b128 v[40:43], v11 offset:16384\n"
  "s_waitcnt lgkmcnt(5)\n"
  "v_mfma_f32_16x16x32_bf16 v[76:79], a[24:27], v[20:23], v[76:79]\n"
  "ds_read_b128 v[44:47], v11 offset:18432\n"
  "v_mfma_f32_16x16x32_bf16 v[80:83], a[96:99], v[16:19], v[80:83]\n"
  "v_mfma_f32_16x16x32_bf16 v[84:87], a[96:99], v[20:23], v[84:87]\n"
  "s_add_u32 m0, s58, 0\n"
  "s_nop 0\n"
  "global_load_lds_dwordx4 v3, s[42:43] offset:0\n"
  "v_mfma_f32_16x16x32_bf16 v[88:91], v[16:19], a[168:171], v[88:91]\n"
  "v_mfma_f32_16x16x32_bf16 v[92:95], v[20:23], a[168:171], v[92:95]\n"
  "s_waitcnt lgkmcnt(5)\n"
  "v_mfma_f32_16x16x32_bf16 v[72:75], a[28:31], v[24:27], v[72:75]\n"
  "ds_read_b128 v[16:19], v10 offset:20480\n"
  "s_waitcnt lgkmcnt(5)\n"
  "v_mfma_f32_16x16x32_bf16 v[76:79], a[28:31], v[28:31], v[76:79]\n"
  "ds_read_b128 v[20:23], v10 offset:22528\n"
  "v_mfma_f32_16x16x32_bf16 v[80:83], a[100:103], v[24:27], v[80:83]\n"
  "v_mfma_f32_16x16x32_bf16 v[84:87], a[100:103], v[28:31], v[84:87]\n"
  "s_add_u32 m0, s58, 3968\n"
  "s_nop 0\n"
  "global_load_lds_dwordx4 v3, s[42:43] offset:128\n"
  "v_mfma_f32_16x16x32_bf16 v[88:91], v[24:27], a[172:175], v[88:91]\n"
  "v_mfma_f32_16x16x32_bf16 v[92:95], v[28:31], a[172:175], v[92:95]\n"
  "s_waitcnt lgkmcnt(5)\n"
  "v_mfma_f32_16x16x32_bf16 v[72:75], a[32:35], v[32:35], v[72:75]\n"
  "ds_read_b128 v[24:27], v11 offset:20480\n"
  "s_waitcnt lgkmcnt(5)\n"
  "v_mfma_f32_16x16x32_bf16 v[76:79], a[32:35], v[36:39], v[76:79]\n"
  "ds_read_b128 v[28:31], v11 offset:22528\n"
  "v_mfma_f32_16x16x32_bf16 v[80:83], a[104:107], v[32:35], v[80:83]\n"
  "v_mfma_f32_16x16x32_bf16 v[84:87], a[104:107], v[36:39], v[84:87]\n"
  "s_add_u32 m0, s58, 7936\n"
  "s_nop 0\n"
  "global_load_lds_dwordx4 v3, s[42:43] offset:256\n"
  "v_mfma_f32_16x16x32_bf16 v[88:91], v[32:35], a[176:179], v[88:91]\n"
  "v_mfma_f32_16x16x32_bf16 v[92:95], v[36:39], a[176:179], v[92:95]\n"
  "s_waitcnt lgkmcnt(5)\n"
  "v_mfma_f32_16x16x32_bf16 v[72:75], a[36:39], v[40:43], v[72:75]\n"
  "ds_read_b128 v[32:35], v10 offset:24576\n"
  "s_waitcnt lgkmcnt(5)\n"
  "v_mfma_f32_16x16x32_bf16 v[76:79], a[36:39], v[44:47], v[76:79]\n"
  "ds_read_b128 v[36:39], v10 offset:26624\n"
  "v_mfma_f32_16x16x32_bf16 v[80:83], a[108:111], v[40:43], v[80:83]\n"
  "v_mfma_f32_16x16x32_bf16 v[84:87], a[108:111], v[44:47], v[84:87]\n"
  "s_add_u32 m0, s58, 11904\n"
  "s_nop 0\n"
  "global_load_lds_dwordx4 v3, s[42:43] offset:384\n"
  "v_mfma_f32_16x16x32_bf16 v[88:91], v[40:43], a[180:183], v[88:91]\n"
  "v_mfma_f32_16x16x32_bf16 v[92:95], v[44:47], a[180:183], v[92:95]\n"
  "s_waitcnt lgkmcnt(5)\n"
  "v_mfma_f32_16x16x32_bf16 v[72:75], a[40:43], v[16:19], v[72:75]\n"
  "ds_read_b128 v[40:43], v11 offset:24576\n"
  "s_waitcnt lgkmcnt(5)\n"
  "v_mfma_f32_16x16x32_bf16 v[76:79], a[40:43], v[20:23], v[76:79]\n"
  "ds_read_b128 v[44:47], v11 offset:26624\n"
  "v_mfma_f32_16x16x32_bf16 v[80:83], a[112:115], v[16:19], v[80:83]\n"
  "v_mfma_f32_16x16x32_bf16 v[84:87], a[112:115], v[20:23], v[84:87]\n"
  "s_add_u32 m0, s58, 15872\n"
  "s_nop 0\n"
  "global_load_lds_dwordx4 v3, s[42:43] offset:512\n"
  "v_mfma_f32_16x16x32_bf16 v[88:91], v[16:19], a[184:187], v[88:91]\n"
  "v_mfma_f32_16x16x32_bf16 v[92:95], v[20:23], a[184:187], v[92:95]\n"
  "s_waitcnt lgkmcnt(5)\n"
  "v_mfma_f32_16x16x32_bf16 v[72:75], a[44:47], v[24:27], v[72:75]\n"
  "ds_read_b128 v[16:19], v10 offset:28672\n"
  "s_waitcnt lgkmcnt(5)\n"
  "v_mfma_f32_16x16x32_bf16 v[76:79], a[44:47], v[28:31], v[76:79]\n"
  "ds_read_b128 v[20:23], v10 offset:30720\n"
  "v_mfma_f32_16x16x32_bf16 v[80:83], a[116:119], v[24:27], v[80:83]\n"
  "v_mfma_f32_16x16x32_bf16 v[84:87], a[116:119], v[28:31], v[84:87]\n"
  "s_add_u32 m0, s58, 19840\n"
  "s_nop 0\n"
  "global_load_lds_dwordx4 v3, s[42:43] offset:640\n"
  "v_mfma_f32_16x16x32_bf16 v[88:91], v[24:27], a[188:191], v[88:91]\n"
  "v_mfma_f32_16x16x32_bf16 v[92:95], v[28:31], a[188:191], v[92:95]\n"
  "s_waitcnt lgkmcnt(5)\n"
  "v_mfma_f32_16x16x32_bf16 v[72:75], a[48:51], v[32:35], v[72:75]\n"
  "ds_read_b128 v[24:27], v11 offset:28672\n"
  "s_waitcnt lgkmcnt(5)\n"
  "v_mfma_f32_16x16x32_bf16 v[76:79], a[48:51], v[36:39], v[76:79]\n"
  "ds_read_b128 v[28:31], v11 offset:30720\n"
  "v_mfma_f32_16x16x32_bf16 v[80:83], a[120:123], v[32:35], v[80:83]\n"
  "v_mfma_f32_16x16x32_bf16 v[84:87], a[120:123], v[36:39], v[84:87]\n"
  "s_add_u32 m0, s58, 23808\n"
  "s_nop 0\n"
  "global_load_lds_dwordx4 v3, s[42:43] offset:768\n"
  "v_mfma_f32_16x16x32_bf16 v[88:91], v[32:35], a[192:195], v[88:91]\n"
  "v_mfma_f32_16x16x32_bf16 v[92:95], v[36:39], a[192:195], v[92:95]\n"
  "s_waitcnt lgkmcnt(5)\n"
  "v_mfma_f32_16x16x32_bf16 v[72:75], a[52:55], v[40:43], v[72:75]\n"
  "ds_read_b128 v[32:35], v10 offset:32768\n"
  "s_waitcnt lgkmcnt(5)\n"
  "v_mfma_f32_16x16x32_bf16 v[76:79], a[52:55], v[44:47], v[76:79]\n"
  "ds_read_b128 v[36:39], v10 offset:34816\n"
  "v_mfma_f32_16x16x32_bf16 v[80:83], a[124:127], v[40:43], v[80:83]\n"
  "v_mfma_f32_16x16x32_bf16 v[84:87], a[124:127], v[44:47], v[84:87]\n"
  "s_add_u32 m0, s58, 27776\n"
  "s_nop 0\n"
  "global_load_lds_dwordx4 v3, s[42:43] offset:896\n"
  "v_mfma_f32_16x16x32_bf16 v[88:91], v[40:43], a[196:199], v[88:91]\n"
  "v_mfma_f32_16x16x32_bf16 v[92:95], v[44:47], a[196:199], v[92:95]\n"
  "s_waitcnt lgkmcnt(5)\n"
  "v_mfma_f32_16x16x32_bf16 v[72:75], a[56:59], v[16:19], v[72:75]\n"
  "ds_read_b128 v[40:43], v11 offset:32768\n"
  "s_waitcnt lgkmcnt(5)\n"
  "v_mfma_f32_16x16x32_bf16 v[76:79], a[56:59], v[20:23], v[76:79]\n"
  "ds_read_b128 v[44:47], v11 offset:34816\n"
  "v_mfma_f32_16x16x32_bf16 v[80:83], a[128:131], v[16:19], v[80:83]\n"
  "v_mfma_f32_16x16x32_bf16 v[84:87], a[128:131], v[20:23], v[84:87]\n"
  "s_add_u32 m0, s58, 31744\n"
  "s_nop 0\n"
  "global_load_lds_dwordx4 v3, s[42:43] offset:1024\n"
  "v_mfma_f32_16x16x32_bf16 v[88:91], v[16:19], a[200:203], v[88:91]\n"
  "v_mfma_f32_16x16x32_bf16 v[92:95], v[20:23], a[200:203], v[92:95]\n"
  "s_waitcnt vmcnt(9)\n"
  "s_barrier\n"
  "s_waitcnt lgkmcnt(5)\n"
  "v_mfma_f32_16x16x32_bf16 v[72:75], a[60:63], v[24:27], v[72:75]\n"
  "ds_read_b128 v[16:19], v12 offset:0\n"
  "s_waitcnt lgkmcnt(5)\n"
  "v_mfma_f32_16x16x32_bf16 v[76:79], a[60:63], v[28:31], v[76:79]\n"
  "ds_read_b128 v[20:23], v12 offset:2048\n"
  "v_mfma_f32_16x16x32_bf16 v[80:83], a[132:135], v[24:27], v[80:83]\n"
  "s_bitcmp1_b64 s[4:5], s56\n"
  "s_cselect_b32 s74, 8192, 0\n"
  "v_mfma_f32_16x16x32_bf16 v[84:87], a[132:135], v[28:31], v[84:87]\n"
  "s_and_b32 s75, s56, 63\n"
  "s_lshl_b32 s75, s75, 21\n"
  "v_mfma_f32_16x16x32_bf16 v[88:91], v[24:27], a[204:207], v[88:91]\n"
  "s_add_u32 s74, s74, s75\n"
  "s_add_u32 s74, s74, 20971520\n"
  "v_mfma_f32_16x16x32_bf16 v[92:95], v[28:31], a[204:207], v[92:95]\n"
  "s_xor_b32 s75, s74, s44\n"
  "s_add_u32 s60, s36, s75\n"
  "s_waitcnt lgkmcnt(5)\n"
  "v_mfma_f32_16x16x32_bf16 v[72:75], a[64:67], v[32:35], v[72:75]\n"
  "ds_read_b128 v[24:27], v13 offset:0\n"
  "s_waitcnt lgkmcnt(5)\n"
  "v_mfma_f32_16x16x32_bf16 v[76:79], a[64:67], v[36:39], v[76:79]\n"
  "ds_read_b128 v[28:31], v13 offset:2048\n"
  "v_mfma_f32_16x16x32_bf16 v[80:83], a[136:139], v[32:35], v[80:83]\n"
  "s_addc_u32 s61, s37, 0\n"
  "s_xor_b32 s75, s74, s45\n"
  "v_mfma_f32_16x16x32_bf16 v[84:87], a[136:139], v[36:39], v[84:87]\n"
  "s_add_u32 s62, s36, s75\n"
  "s_addc_u32 s63, s37, 0\n"
  "v_mfma_f32_16x16x32_bf16 v[88:91], v[32:35], a[208:211], v[88:91]\n"
  "v_mfma_f32_16x16x32_bf16 v[92:95], v[36:39], a[208:211], v[92:95]\n"
  "s_waitcnt lgkmcnt(5)\n"
  "v_mfma_f32_16x16x32_bf16 v[72:75], a[68:71], v[40:43], v[72:75]\n"
  "ds_read_b128 v[32:35], v12 offset:4096\n"
  "s_waitcnt lgkmcnt(5)\n"
  "v_mfma_f32_16x16x32_bf16 v[76:79], a[68:71], v[44:47], v[76:79]\n"
  "ds_read_b128 v[36:39], v12 offset:6144\n"
  "v_mfma_f32_16x16x32_bf16 v[80:83], a[140:143], v[40:43], v[80:83]\n"
  "v_mfma_f32_16x16x32_bf16 v[84:87], a[140:143], v[44:47], v[84:87]\n"
  "v_mfma_f32_16x16x32_bf16 v[88:91], v[40:43], a[212:215], v[88:91]\n"
  "v_mfma_f32_16x16x32_bf16 v[92:95], v[44:47], a[212:215], v[92:95]\n"
  "s_add_u32 s56, s56, 1\n"
  "v_mov_b64 v[10:11], v[12:13]\n"
  "s_waitcnt lgkmcnt(5)\n"
  "v_mfma_f32_16x16x32_bf16 v[48:51], a[0:3], v[16:19], 0\n"
  "ds_read_b128 v[40:43], v11 offset:4096\n"
  "s_waitcnt lgkmcnt(5)\n"
  "v_mfma_f32_16x16x32_bf16 v[52:55], a[0:3], v[20:23], 0\n"
  "ds_read_b128 v[44:47], v11 offset:6144\n"
  "v_mfma_f32_16x16x32_bf16 v[56:59], a[72:75], v[16:19], 0\n"
  "s_add_u32 s57, s57, 36864\n"
  "s_cmp_eq_u32 s57, 159744\n"
  "v_mfma_f32_16x16x32_bf16 v[60:63], a[72:75], v[20:23], 0\n"
  "s_cselect_b32 s57, 12288, s57\n"
  "v_mul_f32 v72, v8, v72\n"
  "v_mfma_f32_16x16x32_bf16 v[64:67], v[16:19], a[144:147], 0\n"
  "v_mul_f32 v73, v8, v73\n"
  "v_mul_f32 v74, v8, v74\n"
  "v_mfma_f32_16x16x32_bf16 v[68:71], v[20:23], a[144:147], 0\n"
  "v_mul_f32 v75, v8, v75\n"
  "v_cvt_pk_bf16_f32 v96, v72, v73\n"
  "s_waitcnt lgkmcnt(5)\n"
  "v_mfma_f32_16x16x32_bf16 v[48:51], a[4:7], v[24:27], v[48:51]\n"
  "ds_read_b128 v[16:19], v10 offset:8192\n"
  "s_waitcnt lgkmcnt(5)\n"
  "v_mfma_f32_16x16x32_bf16 v[52:55], a[4:7], v[28:31], v[52:55]\n"
  "ds_read_b128 v[20:23], v10 offset:10240\n"
  "v_mfma_f32_16x16x32_bf16 v[56:59], a[76:79], v[24:27], v[56:59]\n"
  "v_cvt_pk_bf16_f32 v97, v74, v75\n"
  "v_mul_f32 v80, v8, v80\n"
  "v_mfma_f32_16x16x32_bf16 v[60:63], a[76:79], v[28:31], v[60:63]\n"
  "v_mul_f32 v81, v8, v81\n"
  "v_mul_f32 v82, v8, v82\n"
  "v_mfma_f32_16x16x32_bf16 v[64:67], v[24:27], a[148:151], v[64:67]\n"
  "v_mul_f32 v83, v8, v83\n"
  "v_cvt_pk_bf16_f32 v98, v80, v81\n"
  "v_mfma_f32_16x16x32_bf16 v[68:71], v[28:31], a[148:151], v[68:71]\n"
  "v_cvt_pk_bf16_f32 v99, v82, v83\n"
  "global_store_dwordx4 v0, v[96:99], s[60:61] offset:0 sc1\n"
  "s_waitcnt lgkmcnt(5)\n"
  "v_mfma_f32_16x16x32_bf16 v[48:51], a[8:11], v[32:35], v[48:51]\n"
  "ds_read_b128 v[24:27], v11 offset:8192\n"
  "s_waitcnt lgkmcnt(5)\n"
  "v_mfma_f32_16x16x32_bf16 v[52:55], a[8:11], v[36:39], v[52:55]\n"
  "ds_read_b128 v[28:31], v11 offset:10240\n"
  "v_mfma_f32_16x16x32_bf16 v[56:59], a[80:83], v[32:35], v[56:59]\n"
  "v_mul_f32 v76, v8, v76\n"
  "v_mul_f32 v77, v8, v77\n"
  "v_mfma_f32_16x16x32_bf16 v[60:63], a[80:83], v[36:39], v[60:63]\n"
  "v_mul_f32 v78, v8, v78\n"
  "v_mul_f32 v79, v8, v79\n"
  "v_mfma_f32_16x16x32_bf16 v[64:67], v[32:35], a[152:155], v[64:67]\n"
  "v_cvt_pk_bf16_f32 v100, v76, v77\n"
  "v_cvt_pk_bf16_f32 v101, v78, v79\n"
  "v_mfma_f32_16x16x32_bf16 v[68:71], v[36:39], a[152:155], v[68:71]\n"
  "v_mul_f32 v84, v8, v84\n"
  "v_mul_f32 v85, v8, v85\n"
  "s_waitcnt lgkmcnt(5)\n"
  "v_mfma_f32_16x16x32_bf16 v[48:51], a[12:15], v[40:43], v[48:51]\n"
  "ds_read_b128 v[32:35], v10 offset:12288\n"
  "s_waitcnt lgkmcnt(5)\n"
  "v_mfma_f32_16x16x32_bf16 v[52:55], a[12:15], v[44:47], v[52:55]\n"
  "ds_read_b128 v[36:39], v10 offset:14336\n"
  "v_mfma_f32_16x16x32_bf16 v[56:59], a[84:87], v[40:43], v[56:59]\n"
  "v_mul_f32 v86, v8, v86\n"
  "v_mul_f32 v87, v8, v87\n"
  "v_mfma_f32_16x16x32_bf16 v[60:63], a[84:87], v[44:47], v[60:63]\n"
  "v_cvt_pk_bf16_f32 v102, v84, v85\n"
  "v_cvt_pk_bf16_f32 v103, v86, v87\n"
  "v_mfma_f32_16x16x32_bf16 v[64:67], v[40:43], a[156:159], v[64:67]\n"
  "global_store_dwordx4 v0, v[100:103], s[60:61] offset:1024 sc1\n"
  "v_cvt_pk_bf16_f32 v104, v88, v89\n"
  "v_mfma_f32_16x16x32_bf16 v[68:71], v[44:47], a[156:159], v[68:71]\n"
  "v_cvt_pk_bf16_f32 v105, v90, v91\n"
  "v_cvt_pk_bf16_f32 v106, v92, v93\n"
  "s_waitcnt lgkmcnt(5)\n"
  "v_mfma_f32_16x16x32_bf16 v[48:51], a[16:19], v[16:19], v[48:51]\n"
  "ds_read_b128 v[40:43], v11 offset:12288\n"
  "s_waitcnt lgkmcnt(5)\n"
  "v_mfma_f32_16x16x32_bf16 v[52:55], a[16:19], v[20:23], v[52:55]\n"
  "ds_read_b128 v[44:47], v11 offset:14336\n"
  "v_mfma_f32_16x16x32_bf16 v[56:59], a[88:91], v[16:19], v[56:59]\n"
  "v_cvt_pk_bf16_f32 v107, v94, v95\n"
  "global_store_dwordx4 v0, v[104:107], s[62:63] offset:0 sc1\n"
  "v_mfma_f32_16x16x32_bf16 v[60:63], a[88:91], v[20:23], v[60:63]\n"
  "v_add_u32 v12, s57, v9\n"
  "v_xor_b32 v13, 64, v12\n"
  "v_mfma_f32_16x16x32_bf16 v[64:67], v[16:19], a[160:163], v[64:67]\n"
  "s_add_u32 s58, s57, 36864\n"
  "s_cmp_eq_u32 s57, 122880\n"
  "v_mfma_f32_16x16x32_bf16 v[68:71], v[20:23], a[160:163], v[68:71]\n"
  "s_cselect_b32 s58, 12288, s58\n"
  "s_add_u32 s58, s58, s80\n"
  "s_waitcnt lgkmcnt(5)\n"
  "v_mfma_f32_16x16x32_bf16 v[48:51], a[20:23], v[24:27], v[48:51]\n"
  "ds_read_b128 v[16:19], v10 offset:16384\n"
  "s_waitcnt lgkmcnt(5)\n"
  "v_mfma_f32_16x16x32_bf16 v[52:55], a[20:23], v[28:31], v[52:55]\n"
  "ds_read_b128 v[20:23], v10 offset:18432\n"
  "v_mfma_f32_16x16x32_bf16 v[56:59], a[92:95], v[24:27], v[56:59]\n"
  "s_cmp_lt_u32 s56, 62\n"
  "s_cselect_b32 s74, 36864, 0\n"
  "v_mfma_f32_16x16x32_bf16 v[60:63], a[92:95], v[28:31], v[60:63]\n"
  "s_add_u32 s42, s42, s74\n"
  "s_addc_u32 s43, s43, 0\n"
  "v_mfma_f32_16x16x32_bf16 v[64:67], v[24:27], a[164:167], v[64:67]\n"
  "v_mfma_f32_16x16x32_bf16 v[68:71], v[28:31], a[164:167], v[68:71]\n"
  "s_waitcnt lgkmcnt(5)\n"
  "v_mfma_f32_16x16x32_bf16 v[48:51], a[24:27], v[32:35], v[48:51]\n"
  "ds_read_b128 v[24:27], v11 offset:16384\n"
  "s_waitcnt lgkmcnt(5)\n"
  "v_mfma_f32_16x16x32_bf16 v[52:55], a[24:27], v[36:39], v[52:55]\n"
  "ds_read_b128 v[28:31], v11 offset:18432\n"
  "v_mfma_f32_16x16x32_bf16 v[56:59], a[96:99], v[32:35], v[56:59]\n"
  "v_mfma_f32_16x16x32_bf16 v[60:63], a[96:99], v[36:39], v[60:63]\n"
  "s_add_u32 m0, s58, 0\n"
  "s_nop 0\n"
  "global_load_lds_dwordx4 v3, s[42:43] offset:0\n"
  "v_mfma_f32_16x16x32_bf16 v[64:67], v[32:35], a[168:171], v[64:67]\n"
  "v_mfma_f32_16x16x32_bf16 v[68:71], v[36:39], a[168:171], v[68:71]\n"
  "s_waitcnt lgkmcnt(5)\n"
  "v_mfma_f32_16x16x32_bf16 v[48:51], a[28:31], v[40:43], v[48:51]\n"
  "ds_read_b128 v[32:35], v10 offset:20480\n"
  "s_waitcnt lgkmcnt(5)\n"
  "v_mfma_f32_16x16x32_bf16 v[52:55], a[28:31], v[44:47], v[52:55]\n"
  "ds_read_b128 v[36:39], v10 offset:22528\n"
  "v_mfma_f32_16x16x32_bf16 v[56:59], a[100:103], v[40:43], v[56:59]\n"
  "v_mfma_f32_16x16x32_bf16 v[60:63], a[100:103], v[44:47], v[60:63]\n"
  "s_add_u32 m0, s58, 3968\n"
  "s_nop 0\n"
  "global_load_lds_dwordx4 v3, s[42:43] offset:128\n"
  "v_mfma_f32_16x16x32_bf16 v[64:67], v[40:43], a[172:175], v[64:67]\n"
  "v_mfma_f32_16x16x32_bf16 v[68:71], v[44:47], a[172:175], v[68:71]\n"
  "s_waitcnt lgkmcnt(5)\n"
  "v_mfma_f32_16x16x32_bf16 v[48:51], a[32:35], v[16:19], v[48:51]\n"
  "ds_read_b128 v[40:43], v11 offset:20480\n"
  "s_waitcnt lgkmcnt(5)\n"
  "v_mfma_f32_16x16x32_bf16 v[52:55], a[32:35], v[20:23], v[52:55]\n"
  "ds_read_b128 v[44:47], v11 offset:22528\n"
  "v_mfma_f32_16x16x32_bf16 v[56:59], a[104:107], v[16:19], v[56:59]\n"
  "v_mfma_f32_16x16x32_bf16 v[60:63], a[104:107], v[20:23], v[60:63]\n"
  "s_add_u32 m0, s58, 7936\n"
  "s_nop 0\n"
  "global_load_lds_dwordx4 v3, s[42:43] offset:256\n"
  "v_mfma_f32_16x16x32_bf16 v[64:67], v[16:19], a[176:179], v[64:67]\n"
  "v_mfma_f32_16x16x32_bf16 v[68:71], v[20:23], a[176:179], v[68:71]\n"
  "s_waitcnt lgkmcnt(5)\n"
  "v_mfma_f32_16x16x32_bf16 v[48:51], a[36:39], v[24:27], v[48:51]\n"
  "ds_read_b128 v[16:19], v10 offset:24576\n"
  "s_waitcnt lgkmcnt(5)\n"
  "v_mfma_f32_16x16x32_bf16 v[52:55], a[36:39], v[28:31], v[52:55]\n"
  "ds_read_b128 v[20:23], v10 offset:26624\n"
  "v_mfma_f32_16x16x32_bf16 v[56:59], a[108:111], v[24:27], v[56:59]\n"
  "v_mfma_f32_16x16x32_bf16 v[60:63], a[108:111], v[28:31], v[60:63]\n"
  "s_add_u32 m0, s58, 11904\n"
  "s_nop 0\n"
  "global_load_lds_dwordx4 v3, s[42:43] offset:384\n"
  "v_mfma_f32_16x16x32_bf16 v[64:67], v[24:27], a[180:183], v[64:67]\n"
  "v_mfma_f32_16x16x32_bf16 v[68:71], v[28:31], a[180:183], v[68:71]\n"
  "s_waitcnt lgkmcnt(5)\n"
  "v_mfma_f32_16x16x32_bf16 v[48:51], a[40:43], v[32:35], v[48:51]\n"
  "ds_read_b128 v[24:27], v11 offset:24576\n"
  "s_waitcnt lgkmcnt(5)\n"
  "v_mfma_f32_16x16x32_bf16 v[52:55], a[40:43], v[36:39], v[52:55]\n"
  "ds_read_b128 v[28:31], v11 offset:26624\n"
  "v_mfma_f32_16x16x32_bf16 v[56:59], a[112:115], v[32:35], v[56:59]\n"
  "v_mfma_f32_16x16x32_bf16 v[60:63], a[112:115], v[36:39], v[60:63]\n"
  "s_add_u32 m0, s58, 15872\n"
  "s_nop 0\n"
  "global_load_lds_dwordx4 v3, s[42:43] offset:512\n"
  "v_mfma_f32_16x16x32_bf16 v[64:67], v[32:35], a[184:187], v[64:67]\n"
  "v_mfma_f32_16x16x32_bf16 v[68:71], v[36:39], a[184:187], v[68:71]\n"
  "s_waitcnt lgkmcnt(5)\n"
  "v_mfma_f32_16x16x32_bf16 v[48:51], a[44:47], v[40:43], v[48:51]\n"
  "ds_read_b128 v[32:35], v10 offset:28672\n"
  "s_waitcnt lgkmcnt(5)\n"
  "v_mfma_f32_16x16x32_bf16 v[52:55], a[44:47], v[44:47], v[52:55]\n"
  "ds_read_b128 v[36:39], v10 offset:30720\n"
  "v_mfma_f32_16x16x32_bf16 v[56:59], a[116:119], v[40:43], v[56:59]\n"
  "v_mfma_f32_16x16x32_bf16 v[60:63], a[116:119], v[44:47], v[60:63]\n"
  "s_add_u32 m0, s58, 19840\n"
  "s_nop 0\n"
  "global_load_lds_dwordx4 v3, s[42:43] offset:640\n"
  "v_mfma_f32_16x16x32_bf16 v[64:67], v[40:43], a[188:191], v[64:67]\n"
  "v_mfma_f32_16x16x32_bf16 v[68:71], v[44:47], a[188:191], v[68:71]\n"
  "s_waitcnt lgkmcnt(5)\n"
  "v_mfma_f32_16x16x32_bf16 v[48:51], a[48:51], v[16:19], v[48:51]\n"
  "ds_read_b128 v[40:43], v11 offset:28672\n"
  "s_waitcnt lgkmcnt(5)\n"
  "v_mfma_f32_16x16x32_bf16 v[52:55], a[48:51], v[20:23], v[52:55]\n"
  "ds_read_b128 v[44:47], v11 offset:30720\n"
  "v_mfma_f32_16x16x32_bf16 v[56:59], a[120:123], v[16:19], v[56:59]\n"
  "v_mfma_f32_16x16x32_bf16 v[60:63], a[120:123], v[20:23], v[60:63]\n"
  "s_add_u32 m0, s58, 23808\n"
  "s_nop 0\n"
  "global_load_lds_dwordx4 v3, s[42:43] offset:768\n"
  "v_mfma_f32_16x16x32_bf16 v[64:67], v[16:19], a[192:195], v[64:67]\n"
  "v_mfma_f32_16x16x32_bf16 v[68:71], v[20:23], a[192:195], v[68:71]\n"
  "s_waitcnt lgkmcnt(5)\n"
  "v_mfma_f32_16x16x32_bf16 v[48:51], a[52:55], v[24:27], v[48:51]\n"
  "ds_read_b128 v[16:19], v10 offset:32768\n"
  "s_waitcnt lgkmcnt(5)\n"
  "v_mfma_f32_16x16x32_bf16 v[52:55], a[52:55], v[28:31], v[52:55]\n"
  "ds_read_b128 v[20:23], v10 offset:34816\n"
  "v_mfma_f32_16x16x32_bf16 v[56:59], a[124:127], v[24:27], v[56:59]\n"
  "v_mfma_f32_16x16x32_bf16 v[60:63], a[124:127], v[28:31], v[60:63]\n"
  "s_add_u32 m0, s58, 27776\n"
  "s_nop 0\n"
  "global_load_lds_dwordx4 v3, s[42:43] offset:896\n"
  "v_mfma_f32_16x16x32_bf16 v[64:67], v[24:27], a[196:199], v[64:67]\n"
  "v_mfma_f32_16x16x32_bf16 v[68:71], v[28:31], a[196:199], v[68:71]\n"
  "s_waitcnt lgkmcnt(5)\n"
  "v_mfma_f32_16x16x32_bf16 v[48:51], a[56:59], v[32:35], v[48:51]\n"
  "ds_read_b128 v[24:27], v11 offset:32768\n"
  "s_waitcnt lgkmcnt(5)\n"
  "v_mfma_f32_16x16x32_bf16 v[52:55], a[56:59], v[36:39], v[52:55]\n"
  "ds_read_b128 v[28:31], v11 offset:34816\n"
  "v_mfma_f32_16x16x32_bf16 v[56:59], a[128:131], v[32:35], v[56:59]\n"
  "v_mfma_f32_16x16x32_bf16 v[60:63], a[128:131], v[36:39], v[60:63]\n"
  "s_add_u32 m0, s58, 31744\n"
  "s_nop 0\n"
  "global_load_lds_dwordx4 v3, s[42:43] offset:1024\n"
  "v_mfma_f32_16x16x32_bf16 v[64:67], v[32:35], a[200:203], v[64:67]\n"
  "v_mfma_f32_16x16x32_bf16 v[68:71], v[36:39], a[200:203], v[68:71]\n"
  "s_waitcnt vmcnt(9)\n"
  "s_barrier\n"
  "s_waitcnt lgkmcnt(5)\n"
  "v_mfma_f32_16x16x32_bf16 v[48:51], a[60:63], v[40:43], v[48:51]\n"
  "ds_read_b128 v[32:35], v12 offset:0\n"
  "s_waitcnt lgkmcnt(5)\n"
  "v_mfma_f32_16x16x32_bf16 v[52:55], a[60:63], v[44:47], v[52:55]\n"
  "ds_read_b128 v[36:39], v12 offset:2048\n"
  "v_mfma_f32_16x16x32_bf16 v[56:59], a[132:135], v[40:43], v[56:59]\n"
  "v_mov_b32 v6, 0\n"
  "v_mov_b32 v7, s56\n"
  "s_cmp_eq_u32 s47, 3\n"
  "s_cselect_b64 exec, 1, 0\n"
  "global_store_dword v6, v7, s[64:65] sc1\n"
  "s_mov_b64 exec, -1\n"
  "v_mfma_f32_16x16x32_bf16 v[60:63], a[132:135], v[44:47], v[60:63]\n"
  "s_bitcmp1_b64 s[4:5], s56\n"
  "s_cselect_b32 s74, 8192, 0\n"
  "v_mfma_f32_16x16x32_bf16 v[64:67], v[40:43], a[204:207], v[64:67]\n"
  "s_and_b32 s75, s56, 63\n"
  "s_lshl_b32 s75, s75, 21\n"
  "v_mfma_f32_16x16x32_bf16 v[68:71], v[44:47], a[204:207], v[68:71]\n"
  "s_add_u32 s74, s74, s75\n"
  "s_add_u32 s74, s74, 20971520\n"
  "s_waitcnt lgkmcnt(5)\n"
  "v_mfma_f32_16x16x32_bf16 v[48:51], a[64:67], v[16:19], v[48:51]\n"
  "ds_read_b128 v[40:43], v13 offset:0\n"
  "s_waitcnt lgkmcnt(5)\n"
  "v_mfma_f32_16x16x32_bf16 v[52:55], a[64:67], v[20:23], v[52:55]\n"
  "ds_read_b128 v[44:47], v13 offset:2048\n"
  "v_mfma_f32_16x16x32_bf16 v[56:59], a[136:139], v[16:19], v[56:59]\n"
  "s_xor_b32 s75, s74, s44\n"
  "s_add_u32 s60, s36, s75\n"
  "v_mfma_f32_16x16x32_bf16 v[60:63], a[136:139], v[20:23], v[60:63]\n"
  "s_addc_u32 s61, s37, 0\n"
  "s_xor_b32 s75, s74, s45\n"
  "v_mfma_f32_16x16x32_bf16 v[64:67], v[16:19], a[208:211], v[64:67]\n"
  "s_add_u32 s62, s36, s75\n"
  "s_addc_u32 s63, s37, 0\n"
  "v_mfma_f32_16x16x32_bf16 v[68:71], v[20:23], a[208:211], v[68:71]\n"
  "s_waitcnt lgkmcnt(5)\n"
  "v_mfma_f32_16x16x32_bf16 v[48:51], a[68:71], v[24:27], v[48:51]\n"
  "ds_read_b128 v[16:19], v12 offset:4096\n"
  "s_waitcnt lgkmcnt(5)\n"
  "v_mfma_f32_16x16x32_bf16 v[52:55], a[68:71], v[28:31], v[52:55]\n"
  "ds_read_b128 v[20:23], v12 offset:6144\n"
  "v_mfma_f32_16x16x32_bf16 v[56:59], a[140:143], v[24:27], v[56:59]\n"
  "v_mfma_f32_16x16x32_bf16 v[60:63], a[140:143], v[28:31], v[60:63]\n"
  "v_mfma_f32_16x16x32_bf16 v[64:67], v[24:27], a[212:215], v[64:67]\n"
  "v_mfma_f32_16x16x32_bf16 v[68:71], v[28:31], a[212:215], v[68:71]\n"
  "s_add_u32 s56, s56, 1\n"
  "PJL:\n"
  "v_mov_b64 v[10:11], v[12:13]\n"
  "s_waitcnt lgkmcnt(5)\n"
  "v_mfma_f32_16x16x32_bf16 v[72:75], a[0:3], v[32:35], 0\n"
  "ds_read_b128 v[24:27], v11 offset:4096\n"
  "s_waitcnt lgkmcnt(5)\n"
  "v_mfma_f32_16x16x32_bf16 v[76:79], a[0:3], v[36:39], 0\n"
  "ds_read_b128 v[28:31], v11 offset:6144\n"
  "v_mfma_f32_16x16x32_bf16 v[80:83], a[72:75], v[32:35], 0\n"
  "s_add_u32 s57, s57, 36864\n"
  "s_cmp_eq_u32 s57, 159744\n"
  "v_mfma_f32_16x16x32_bf16 v[84:87], a[72:75], v[36:39], 0\n"
  "s_cselect_b32 s57, 12288, s57\n"
  "v_mul_f32 v48, v8, v48\n"
  "v_mfma_f32_16x16x32_bf16 v[88:91], v[32:35], a[144:147], 0\n"
  "v_mul_f32 v49, v8, v49\n"
  "v_mul_f32 v50, v8, v50\n"
  "v_mfma_f32_16x16x32_bf16 v[92:95], v[36:39], a[144:147], 0\n"
  "v_mul_f32 v51, v8, v51\n"
  "v_cvt_pk_bf16_f32 v96, v48, v49\n"
  "s_waitcnt lgkmcnt(5)\n"
  "v_mfma_f32_16x16x32_bf16 v[72:75], a[4:7], v[40:43], v[72:75]\n"
  "ds_read_b128 v[32:35], v10 offset:8192\n"
  "s_waitcnt lgkmcnt(5)\n"
  "v_mfma_f32_16x16x32_bf16 v[76:79], a[4:7], v[44:47], v[76:79]\n"
  "ds_read_b128 v[36:39], v10 offset:10240\n"
  "v_mfma_f32_16x16x32_bf16 v[80:83], a[76:79], v[40:43], v[80:83]\n"
  "v_cvt_pk_bf16_f32 v97, v50, v51\n"
  "v_mul_f32 v56, v8, v56\n"
  "v_mfma_f32_16x16x32_bf16 v[84:87], a[76:79], v[44:47], v[84:87]\n"
  "v_mul_f32 v57, v8, v57\n"
  "v_mul_f32 v58, v8, v58\n"
  "v_mfma_f32_16x16x32_bf16 v[88:91], v[40:43], a[148:151], v[88:91]\n"
  "v_mul_f32 v59, v8, v59\n"
  "v_cvt_pk_bf16_f32 v98, v56, v57\n"
  "v_mfma_f32_16x16x32_bf16 v[92:95], v[44:47], a[148:151], v[92:95]\n"
  "v_cvt_pk_bf16_f32 v99, v58, v59\n"
  "global_store_dwordx4 v0, v[96:99], s[60:61] offset:0 sc1\n"
  "s_waitcnt lgkmcnt(5)\n"
  "v_mfma_f32_16x16x32_bf16 v[72:75], a[8:11], v[16:19], v[72:75]\n"
  "ds_read_b128 v[40:43], v11 offset:8192\n"
  "s_waitcnt lgkmcnt(5)\n"
  "v_mfma_f32_16x16x32_bf16 v[76:79], a[8:11], v[20:23], v[76:79]\n"
  "ds_read_b128 v[44:47], v11 offset:10240\n"
  "v_mfma_f32_16x16x32_bf16 v[80:83], a[80:83], v[16:19], v[80:83]\n"
  "v_mul_f32 v52, v8, v52\n"
  "v_mul_f32 v53, v8, v53\n"
  "v_mfma_f32_16x16x32_bf16 v[84:87], a[80:83], v[20:23], v[84:87]\n"
  "v_mul_f32 v54, v8, v54\n"
  "v_mul_f32 v55, v8, v55\n"
  "v_mfma_f32_16x16x32_bf16 v[88:91], v[16:19], a[152:155], v[88:91]\n"
  "v_cvt_pk_bf16_f32 v100, v52, v53\n"
  "v_cvt_pk_bf16_f32 v101, v54, v55\n"
  "v_mfma_f32_16x16x32_bf16 v[92:95], v[20:23], a[152:155], v[92:95]\n"
  "v_mul_f32 v60, v8, v60\n"
  "v_mul_f32 v61, v8, v61\n"
  "s_waitcnt lgkmcnt(5)\n"
  "v_mfma_f32_16x16x32_bf16 v[72:75], a[12:15], v[24:27], v[72:75]\n"
  "ds_read_b128 v[16:19], v10 offset:12288\n"
  "s_waitcnt lgkmcnt(5)\n"
  "v_mfma_f32_16x16x32_bf16 v[76:79], a[12:15], v[28:31], v[76:79]\n"
  "ds_read_b128 v[20:23], v10 offset:14336\n"
  "v_mfma_f32_16x16x32_bf16 v[80:83], a[84:87], v[24:27], v[80:83]\n"
  "v_mul_f32 v62, v8, v62\n"
  "v_mul_f32 v63, v8, v63\n"
  "v_mfma_f32_16x16x32_bf16 v[84:87], a[84:87], v[28:31], v[84:87]\n"
  "v_cvt_pk_bf16_f32 v102, v60, v61\n"
  "v_cvt_pk_bf16_f32 v103, v62, v63\n"
  "v_mfma_f32_16x16x32_bf16 v[88:91], v[24:27], a[156:159], v[88:91]\n"
  "global_store_dwordx4 v0, v[100:103], s[60:61] offset:1024 sc1\n"
  "v_cvt_pk_bf16_f32 v104, v64, v65\n"
  "v_mfma_f32_16x16x32_bf16 v[92:95], v[28:31], a[156:159], v[92:95]\n"
  "v_cvt_pk_bf16_f32 v105, v66, v67\n"
  "v_cvt_pk_bf16_f32 v106, v68, v69\n"
  "s_waitcnt lgkmcnt(5)\n"
  "v_mfma_f32_16x16x32_bf16 v[72:75], a[16:19], v[32:35], v[72:75]\n"
  "ds_read_b128 v[24:27], v11 offset:12288\n"
  "s_waitcnt lgkmcnt(5)\n"
  "v_mfma_f32_16x16x32_bf16 v[76:79], a[16:19], v[36:39], v[76:79]\n"
  "ds_read_b128 v[28:31], v11 offset:14336\n"
  "v_mfma_f32_16x16x32_bf16 v[80:83], a[88:91], v[32:35], v[80:83]\n"
  "v_cvt_pk_bf16_f32 v107, v70, v71\n"
  "global_store_dwordx4 v0, v[104:107], s[62:63] offset:0 sc1\n"
  "v_mfma_f32_16x16x32_bf16 v[84:87], a[88:91], v[36:39], v[84:87]\n"
  "v_add_u32 v12, s57, v9\n"
  "v_xor_b32 v13, 64, v12\n"
  "v_mfma_f32_16x16x32_bf16 v[88:91], v[32:35], a[160:163], v[88:91]\n"
  "s_add_u32 s58, s57, 36864\n"
  "s_cmp_eq_u32 s57, 122880\n"
  "v_mfma_f32_16x16x32_bf16 v[92:95], v[36:39], a[160:163], v[92:95]\n"
  "s_cselect_b32 s58, 12288, s58\n"
  "s_add_u32 s58, s58, s80\n"
  "s_waitcnt lgkmcnt(5)\n"
  "v_mfma_f32_16x16x32_bf16 v[72:75], a[20:23], v[40:43], v[72:75]\n"
  "ds_read_b128 v[32:35], v10 offset:16384\n"
  "s_waitcnt lgkmcnt(5)\n"
  "v_mfma_f32_16x16x32_bf16 v[76:79], a[20:23], v[44:47], v[76:79]\n"
  "ds_read_b128 v[36:39], v10 offset:18432\n"
  "v_mfma_f32_16x16x32_bf16 v[80:83], a[92:95], v[40:43], v[80:83]\n"
  "s_cmp_lt_u32 s56, 62\n"
  "s_cselect_b32 s74, 36864, 0\n"
  "v_mfma_f32_16x16x32_bf16 v[84:87], a[92:95], v[44:47], v[84:87]\n"
  "s_add_u32 s42, s42, s74\n"
  "s_addc_u32 s43, s43, 0\n"
  "v_mfma_f32_16x16x32_bf16 v[88:91], v[40:43], a[164:167], v[88:91]\n"
  "v_mfma_f32_16x16x32_bf16 v[92:95], v[44:47], a[164:167], v[92:95]\n"
  "s_waitcnt lgkmcnt(5)\n"
  "v_mfma_f32_16x16x32_bf16 v[72:75], a[24:27], v[16:19], v[72:75]\n"
  "ds_read_b128 v[40:43], v11 offset:16384\n"
  "s_waitcnt lgkmcnt(5)\n"
  "v_mfma_f32_16x16x32_bf16 v[76:79], a[24:27], v[20:23], v[76:79]\n"
  "ds_read_b128 v[44:47], v11 offset:18432\n"
  "v_mfma_f32_16x16x32_bf16 v[80:83], a[96:99], v[16:19], v[80:83]\n"
  "v_mfma_f32_16x16x32_bf16 v[84:87], a[96:99], v[20:23], v[84:87]\n"
  "s_add_u32 m0, s58, 0\n"
  "s_nop 0\n"
  "global_load_lds_dwordx4 v3, s[42:43] offset:0\n"
  "v_mfma_f32_16x16x32_bf16 v[88:91], v[16:19], a[168:171], v[88:91]\n"
  "v_mfma_f32_16x16x32_bf16 v[92:95], v[20:23], a[168:171], v[92:95]\n"
  "s_waitcnt lgkmcnt(5)\n"
  "v_mfma_f32_16x16x32_bf16 v[72:75], a[28:31], v[24:27], v[72:75]\n"
  "ds_read_b128 v[16:19], v10 offset:20480\n"
  "s_waitcnt lgkmcnt(5)\n"
  "v_mfma_f32_16x16x32_bf16 v[76:79], a[28:31], v[28:31], v[76:79]\n"
  "ds_read_b128 v[20:23], v10 offset:22528\n"
  "v_mfma_f32_16x16x32_bf16 v[80:83], a[100:103], v[24:27], v[80:83]\n"
  "v_mfma_f32_16x16x32_bf16 v[84:87], a[100:103], v[28:31], v[84:87]\n"
  "s_add_u32 m0, s58, 3968\n"
  "s_nop 0\n"
  "global_load_lds_dwordx4 v3, s[42:43] offset:128\n"
  "v_mfma_f32_16x16x32_bf16 v[88:91], v[24:27], a[172:175], v[88:91]\n"
  "v_mfma_f32_16x16x32_bf16 v[92:95], v[28:31], a[172:175], v[92:95]\n"
  "s_waitcnt lgkmcnt(5)\n"
  "v_mfma_f32_16x16x32_bf16 v[72:75], a[32:35], v[32:35], v[72:75]\n"
  "ds_read_b128 v[24:27], v11 offset:20480\n"
  "s_waitcnt lgkmcnt(5)\n"
  "v_mfma_f32_16x16x32_bf16 v[76:79], a[32:35], v[36:39], v[76:79]\n"
  "ds_read_b128 v[28:31], v11 offset:22528\n"
  "v_mfma_f32_16x16x32_bf16 v[80:83], a[104:107], v[32:35], v[80:83]\n"
  "v_mfma_f32_16x16x32_bf16 v[84:87], a[104:107], v[36:39], v[84:87]\n"
  "s_add_u32 m0, s58, 7936\n"
  "s_nop 0\n"
  "global_load_lds_dwordx4 v3, s[42:43] offset:256\n"
  "v_mfma_f32_16x16x32_bf16 v[88:91], v[32:35], a[176:179], v[88:91]\n"
  "v_mfma_f32_16x16x32_bf16 v[92:95], v[36:39], a[176:179], v[92:95]\n"
  "s_waitcnt lgkmcnt(5)\n"
  "v_mfma_f32_16x16x32_bf16 v[72:75], a[36:39], v[40:43], v[72:75]\n"
  "ds_read_b128 v[32:35], v10 offset:24576\n"
  "s_waitcnt lgkmcnt(5)\n"
  "v_mfma_f32_16x16x32_bf16 v[76:79], a[36:39], v[44:47], v[76:79]\n"
  "ds_read_b128 v[36:39], v10 offset:26624\n"
  "v_mfma_f32_16x16x32_bf16 v[80:83], a[108:111], v[40:43], v[80:83]\n"
  "v_mfma_f32_16x16x32_bf16 v[84:87], a[108:111], v[44:47], v[84:87]\n"
  "s_add_u32 m0, s58, 11904\n"
  "s_nop 0\n"
  "global_load_lds_dwordx4 v3, s[42:43] offset:384\n"
  "v_mfma_f32_16x16x32_bf16 v[88:91], v[40:43], a[180:183], v[88:91]\n"
  "v_mfma_f32_16x16x32_bf16 v[92:95], v[44:47], a[180:183], v[92:95]\n"
  "s_waitcnt lgkmcnt(5)\n"
  "v_mfma_f32_16x16x32_bf16 v[72:75], a[40:43], v[16:19], v[72:75]\n"
  "ds_read_b128 v[40:43], v11 offset:24576\n"
  "s_waitcnt lgkmcnt(5)\n"
  "v_mfma_f32_16x16x32_bf16 v[76:79], a[40:43], v[20:23], v[76:79]\n"
  "ds_read_b128 v[44:47], v11 offset:26624\n"
  "v_mfma_f32_16x16x32_bf16 v[80:83], a[112:115], v[16:19], v[80:83]\n"
  "v_mfma_f32_16x16x32_bf16 v[84:87], a[112:115], v[20:23], v[84:87]\n"
  "s_add_u32 m0, s58, 15872\n"
  "s_nop 0\n"
  "global_load_lds_dwordx4 v3, s[42:43] offset:512\n"
  "v_mfma_f32_16x16x32_bf16 v[88:91], v[16:19], a[184:187], v[88:91]\n"
  "v_mfma_f32_16x16x32_bf16 v[92:95], v[20:23], a[184:187], v[92:95]\n"
  "s_waitcnt lgkmcnt(5)\n"
  "v_mfma_f32_16x16x32_bf16 v[72:75], a[44:47], v[24:27], v[72:75]\n"
  "ds_read_b128 v[16:19], v10 offset:28672\n"
  "s_waitcnt lgkmcnt(5)\n"
  "v_mfma_f32_16x16x32_bf16 v[76:79], a[44:47], v[28:31], v[76:79]\n"
  "ds_read_b128 v[20:23], v10 offset:30720\n"
  "v_mfma_f32_16x16x32_bf16 v[80:83], a[116:119], v[24:27], v[80:83]\n"
  "v_mfma_f32_16x16x32_bf16 v[84:87], a[116:119], v[28:31], v[84:87]\n"
  "s_add_u32 m0, s58, 19840\n"
  "s_nop 0\n"
  "global_load_lds_dwordx4 v3, s[42:43] offset:640\n"
  "v_mfma_f32_16x16x32_bf16 v[88:91], v[24:27], a[188:191], v[88:91]\n"
  "v_mfma_f32_16x16x32_bf16 v[92:95], v[28:31], a[188:191], v[92:95]\n"
  "s_waitcnt lgkmcnt(5)\n"
  "v_mfma_f32_16x16x32_bf16 v[72:75], a[48:51], v[32:35], v[72:75]\n"
  "ds_read_b128 v[24:27], v11 offset:28672\n"
  "s_waitcnt lgkmcnt(5)\n"
  "v_mfma_f32_16x16x32_bf16 v[76:79], a[48:51], v[36:39], v[76:79]\n"
  "ds_read_b128 v[28:31], v11 offset:30720\n"
  "v_mfma_f32_16x16x32_bf16 v[80:83], a[120:123], v[32:35], v[80:83]\n"
  "v_mfma_f32_16x16x32_bf16 v[84:87], a[120:123], v[36:39], v[84:87]\n"
  "s_add_u32 m0, s58, 23808\n"
  "s_nop 0\n"
  "global_load_lds_dwordx4 v3, s[42:43] offset:768\n"
  "v_mfma_f32_16x16x32_bf16 v[88:91], v[32:35], a[192:195], v[88:91]\n"
  "v_mfma_f32_16x16x32_bf16 v[92:95], v[36:39], a[192:195], v[92:95]\n"
  "s_waitcnt lgkmcnt(5)\n"
  "v_mfma_f32_16x16x32_bf16 v[72:75], a[52:55], v[40:43], v[72:75]\n"
  "ds_read_b128 v[32:35], v10 offset:32768\n"
  "s_waitcnt lgkmcnt(5)\n"
  "v_mfma_f32_16x16x32_bf16 v[76:79], a[52:55], v[44:47], v[76:79]\n"
  "ds_read_b128 v[36:39], v10 offset:34816\n"
  "v_mfma_f32_16x16x32_bf16 v[80:83], a[124:127], v[40:43], v[80:83]\n"
  "v_mfma_f32_16x16x32_bf16 v[84:87], a[124:127], v[44:47], v[84:87]\n"
  "s_add_u32 m0, s58, 27776\n"
  "s_nop 0\n"
  "global_load_lds_dwordx4 v3, s[42:43] offset:896\n"
  "v_mfma_f32_16x16x32_bf16 v[88:91], v[40:43], a[196:199], v[88:91]\n"
  "v_mfma_f32_16x16x32_bf16 v[92:95], v[44:47], a[196:199], v[92:95]\n"
  "s_waitcnt lgkmcnt(5)\n"
  "v_mfma_f32_16x16x32_bf16 v[72:75], a[56:59], v[16:19], v[72:75]\n"
  "ds_read_b128 v[40:43], v11 offset:32768\n"
  "s_waitcnt lgkmcnt(5)\n"
  "v_mfma_f32_16x16x32_bf16 v[76:79], a[56:59], v[20:23], v[76:79]\n"
  "ds_read_b128 v[44:47], v11 offset:34816\n"
  "v_mfma_f32_16x16x32_bf16 v[80:83], a[128:131], v[16:19], v[80:83]\n"
  "v_mfma_f32_16x16x32_bf16 v[84:87], a[128:131], v[20:23], v[84:87]\n"
  "s_add_u32 m0, s58, 31744\n"
  "s_nop 0\n"
  "global_load_lds_dwordx4 v3, s[42:43] offset:1024\n"
  "v_mfma_f32_16x16x32_bf16 v[88:91], v[16:19], a[200:203], v[88:91]\n"
  "v_mfma_f32_16x16x32_bf16 v[92:95], v[20:23], a[200:203], v[92:95]\n"
  "s_waitcnt vmcnt(9)\n"
  "s_barrier\n"
  "s_waitcnt lgkmcnt(5)\n"
  "v_mfma_f32_16x16x32_bf16 v[72:75], a[60:63], v[24:27], v[72:75]\n"
  "ds_read_b128 v[16:19], v12 offset:0\n"
  "s_waitcnt lgkmcnt(5)\n"
  "v_mfma_f32_16x16x32_bf16 v[76:79], a[60:63], v[28:31], v[76:79]\n"
  "ds_read_b128 v[20:23], v12 offset:2048\n"
  "v_mfma_f32_16x16x32_bf16 v[80:83], a[132:135], v[24:27], v[80:83]\n"
  "v_mov_b32 v6, 0\n"
  "v_mov_b32 v7, s56\n"
  "s_cmp_eq_u32 s47, 3\n"
  "s_cselect_b64 exec, 1, 0\n"
  "global_store_dword v6, v7, s[64:65] sc1\n"
  "s_mov_b64 exec, -1\n"
  "v_mfma_f32_16x16x32_bf16 v[84:87], a[132:135], v[28:31], v[84:87]\n"
  "s_bitcmp1_b64 s[4:5], s56\n"
  "s_cselect_b32 s74, 8192, 0\n"
  "v_mfma_f32_16x16x32_bf16 v[88:91], v[24:27], a[204:207], v[88:91]\n"
  "s_and_b32 s75, s56, 63\n"
  "s_lshl_b32 s75, s75, 21\n"
  "v_mfma_f32_16x16x32_bf16 v[92:95], v[28:31], a[204:207], v[92:95]\n"
  "s_add_u32 s74, s74, s75\n"
  "s_add_u32 s74, s74, 20971520\n"
  "s_waitcnt lgkmcnt(5)\n"
  "v_mfma_f32_16x16x32_bf16 v[72:75], a[64:67], v[32:35], v[72:75]\n"
  "ds_read_b128 v[24:27], v13 offset:0\n"
  "s_waitcnt lgkmcnt(5)\n"
  "v_mfma_f32_16x16x32_bf16 v[76:79], a[64:67], v[36:39], v[76:79]\n"
  "ds_read_b128 v[28:31], v13 offset:2048\n"
  "v_mfma_f32_16x16x32_bf16 v[80:83], a[136:139], v[32:35], v[80:83]\n"
  "s_xor_b32 s75, s74, s44\n"
  "s_add_u32 s60, s36, s75\n"
  "v_mfma_f32_16x16x32_bf16 v[84:87], a[136:139], v[36:39], v[84:87]\n"
  "s_addc_u32 s61, s37, 0\n"
  "s_xor_b32 s75, s74, s45\n"
  "v_mfma_f32_16x16x32_bf16 v[88:91], v[32:35], a[208:211], v[88:91]\n"
  "s_add_u32 s62, s36, s75\n"
  "s_addc_u32 s63, s37, 0\n"
  "v_mfma_f32_16x16x32_bf16 v[92:95], v[36:39], a[208:211], v[92:95]\n"
  "s_waitcnt lgkmcnt(5)\n"
  "v_mfma_f32_16x16x32_bf16 v[72:75], a[68:71], v[40:43], v[72:75]\n"
  "ds_read_b128 v[32:35], v12 offset:4096\n"
  "s_waitcnt lgkmcnt(5)\n"
  "v_mfma_f32_16x16x32_bf16 v[76:79], a[68:71], v[44:47], v[76:79]\n"
  "ds_read_b128 v[36:39], v12 offset:6144\n"
  "v_mfma_f32_16x16x32_bf16 v[80:83], a[140:143], v[40:43], v[80:83]\n"
  "v_mfma_f32_16x16x32_bf16 v[84:87], a[140:143], v[44:47], v[84:87]\n"
  "v_mfma_f32_16x16x32_bf16 v[88:91], v[40:43], a[212:215], v[88:91]\n"
  "v_mfma_f32_16x16x32_bf16 v[92:95], v[44:47], a[212:215], v[92:95]\n"
  "s_add_u32 s56, s56, 1\n"
  "s_cmp_eq_u32 s56, 64\n"
  "s_cbranch_scc1 PJE\n"
  "v_mov_b64 v[10:11], v[12:13]\n"
  "s_waitcnt lgkmcnt(5)\n"
  "v_mfma_f32_16x16x32_bf16 v[48:51], a[0:3], v[16:19], 0\n"
  "ds_read_b128 v[40:43], v11 offset:4096\n"
  "s_waitcnt lgkmcnt(5)\n"
  "v_mfma_f32_16x16x32_bf16 v[52:55], a[0:3], v[20:23], 0\n"
  "ds_read_b128 v[44:47], v11 offset:6144\n"
  "v_mfma_f32_16x16x32_bf16 v[56:59], a[72:75], v[16:19], 0\n"
  "s_add_u32 s57, s57, 36864\n"
  "s_cmp_eq_u32 s57, 159744\n"
  "v_mfma_f32_16x16x32_bf16 v[60:63], a[72:75], v[20:23], 0\n"
  "s_cselect_b32 s57, 12288, s57\n"
  "v_mul_f32 v72, v8, v72\n"
  "v_mfma_f32_16x16x32_bf16 v[64:67], v[16:19], a[144:147], 0\n"
  "v_mul_f32 v73, v8, v73\n"
  "v_mul_f32 v74, v8, v74\n"
  "v_mfma_f32_16x16x32_bf16 v[68:71], v[20:23], a[144:147], 0\n"
  "v_mul_f32 v75, v8, v75\n"
  "v_cvt_pk_bf16_f32 v96, v72, v73\n"
  "s_waitcnt lgkmcnt(5)\n"
  "v_mfma_f32_16x16x32_bf16 v[48:51], a[4:7], v[24:27], v[48:51]\n"
  "ds_read_b128 v[16:19], v10 offset:8192\n"
  "s_waitcnt lgkmcnt(5)\n"
  "v_mfma_f32_16x16x32_bf16 v[52:55], a[4:7], v[28:31], v[52:55]\n"
  "ds_read_b128 v[20:23], v10 offset:10240\n"
  "v_mfma_f32_16x16x32_bf16 v[56:59], a[76:79], v[24:27], v[56:59]\n"
  "v_cvt_pk_bf16_f32 v97, v74, v75\n"
  "v_mul_f32 v80, v8, v80\n"
  "v_mfma_f32_16x16x32_bf16 v[60:63], a[76:79], v[28:31], v[60:63]\n"
  "v_mul_f32 v81, v8, v81\n"
  "v_mul_f32 v82, v8, v82\n"
  "v_mfma_f32_16x16x32_bf16 v[64:67], v[24:27], a[148:151], v[64:67]\n"
  "v_mul_f32 v83, v8, v83\n"
  "v_cvt_pk_bf16_f32 v98, v80, v81\n"
  "v_mfma_f32_16x16x32_bf16 v[68:71], v[28:31], a[148:151], v[68:71]\n"
  "v_cvt_pk_bf16_f32 v99, v82, v83\n"
  "global_store_dwordx4 v0, v[96:99], s[60:61] offset:0 sc1\n"
  "s_waitcnt lgkmcnt(5)\n"
  "v_mfma_f32_16x16x32_bf16 v[48:51], a[8:11], v[32:35], v[48:51]\n"
  "ds_read_b128 v[24:27], v11 offset:8192\n"
  "s_waitcnt lgkmcnt(5)\n"
  "v_mfma_f32_16x16x32_bf16 v[52:55], a[8:11], v[36:39], v[52:55]\n"
  "ds_read_b128 v[28:31], v11 offset:10240\n"
  "v_mfma_f32_16x16x32_bf16 v[56:59], a[80:83], v[32:35], v[56:59]\n"
  "v_mul_f32 v76, v8, v76\n"
  "v_mul_f32 v77, v8, v77\n"
  "v_mfma_f32_16x16x32_bf16 v[60:63], a[80:83], v[36:39], v[60:63]\n"
  "v_mul_f32 v78, v8, v78\n"
  "v_mul_f32 v79, v8, v79\n"
  "v_mfma_f32_16x16x32_bf16 v[64:67], v[32:35], a[152:155], v[64:67]\n"
  "v_cvt_pk_bf16_f32 v100, v76, v77\n"
  "v_cvt_pk_bf16_f32 v101, v78, v79\n"
  "v_mfma_f32_16x16x32_bf16 v[68:71], v[36:39], a[152:155], v[68:71]\n"
  "v_mul_f32 v84, v8, v84\n"
  "v_mul_f32 v85, v8, v85\n"
  "s_waitcnt lgkmcnt(5)\n"
  "v_mfma_f32_16x16x32_bf16 v[48:51], a[12:15], v[40:43], v[48:51]\n"
  "ds_read_b128 v[32:35], v10 offset:12288\n"
  "s_waitcnt lgkmcnt(5)\n"
  "v_mfma_f32_16x16x32_bf16 v[52:55], a[12:15], v[44:47], v[52:55]\n"
  "ds_read_b128 v[36:39], v10 offset:14336\n"
  "v_mfma_f32_16x16x32_bf16 v[56:59], a[84:87], v[40:43], v[56:59]\n"
  "v_mul_f32 v86, v8, v86\n"
  "v_mul_f32 v87, v8, v87\n"
  "v_mfma_f32_16x16x32_bf16 v[60:63], a[84:87], v[44:47], v[60:63]\n"
  "v_cvt_pk_bf16_f32 v102, v84, v85\n"
  "v_cvt_pk_bf16_f32 v103, v86, v87\n"
  "v_mfma_f32_16x16x32_bf16 v[64:67], v[40:43], a[156:159], v[64:67]\n"
  "global_store_dwordx4 v0, v[100:103], s[60:61] offset:1024 sc1\n"
  "v_cvt_pk_bf16_f32 v104, v88, v89\n"
  "v_mfma_f32_16x16x32_bf16 v[68:71], v[44:47], a[156:159], v[68:71]\n"
  "v_cvt_pk_bf16_f32 v105, v90, v91\n"
  "v_cvt_pk_bf16_f32 v106, v92, v93\n"
  "s_waitcnt lgkmcnt(5)\n"
  "v_mfma_f32_16x16x32_bf16 v[48:51], a[16:19], v[16:19], v[48:51]\n"
  "ds_read_b128 v[40:43], v11 offset:12288\n"
  "s_waitcnt lgkmcnt(5)\n"
  "v_mfma_f32_16x16x32_bf16 v[52:55], a[16:19], v[20:23], v[52:55]\n"
  "ds_read_b128 v[44:47], v11 offset:14336\n"
  "v_mfma_f32_16x16x32_bf16 v[56:59], a[88:91], v[16:19], v[56:59]\n"
  "v_cvt_pk_bf16_f32 v107, v94, v95\n"
  "global_store_dwordx4 v0, v[104:107], s[62:63] offset:0 sc1\n"
  "v_mfma_f32_16x16x32_bf16 v[60:63], a[88:91], v[20:23], v[60:63]\n"
  "v_add_u32 v12, s57, v9\n"
  "v_xor_b32 v13, 64, v12\n"
  "v_mfma_f32_16x16x32_bf16 v[64:67], v[16:19], a[160:163], v[64:67]\n"
  "s_add_u32 s58, s57, 36864\n"
  "s_cmp_eq_u32 s57, 122880\n"
  "v_mfma_f32_16x16x32_bf16 v[68:71], v[20:23], a[160:163], v[68:71]\n"
  "s_cselect_b32 s58, 12288, s58\n"
  "s_add_u32 s58, s58, s80\n"
  "s_waitcnt lgkmcnt(5)\n"
  "v_mfma_f32_16x16x32_bf16 v[48:51], a[20:23], v[24:27], v[48:51]\n"
  "ds_read_b128 v[16:19], v10 offset:16384\n"
  "s_waitcnt lgkmcnt(5)\n"
  "v_mfma_f32_16x16x32_bf16 v[52:55], a[20:23], v[28:31], v[52:55]\n"
  "ds_read_b128 v[20:23], v10 offset:18432\n"
  "v_mfma_f32_16x16x32_bf16 v[56:59], a[92:95], v[24:27], v[56:59]\n"
  "s_cmp_lt_u32 s56, 62\n"
  "s_cselect_b32 s74, 36864, 0\n"
  "v_mfma_f32_16x16x32_bf16 v[60:63], a[92:95], v[28:31], v[60:63]\n"
  "s_add_u32 s42, s42, s74\n"
  "s_addc_u32 s43, s43, 0\n"
  "v_mfma_f32_16x16x32_bf16 v[64:67], v[24:27], a[164:167], v[64:67]\n"
  "v_mfma_f32_16x16x32_bf16 v[68:71], v[28:31], a[164:167], v[68:71]\n"
  "s_waitcnt lgkmcnt(5)\n"
  "v_mfma_f32_16x16x32_bf16 v[48:51], a[24:27], v[32:35], v[48:51]\n"
  "ds_read_b128 v[24:27], v11 offset:16384\n"
  "s_waitcnt lgkmcnt(5)\n"
  "v_mfma_f32_16x16x32_bf16 v[52:55], a[24:27], v[36:39], v[52:55]\n"
  "ds_read_b128 v[28:31], v11 offset:18432\n"
  "v_mfma_f32_16x16x32_bf16 v[56:59], a[96:99], v[32:35], v[56:59]\n"
  "v_mfma_f32_16x16x32_bf16 v[60:63], a[96:99], v[36:39], v[60:63]\n"
  "s_add_u32 m0, s58, 0\n"
  "s_nop 0\n"
  "global_load_lds_dwordx4 v3, s[42:43] offset:0\n"
  "v_mfma_f32_16x16x32_bf16 v[64:67], v[32:35], a[168:171], v[64:67]\n"
  "v_mfma_f32_16x16x32_bf16 v[68:71], v[36:39], a[168:171], v[68:71]\n"
  "s_waitcnt lgkmcnt(5)\n"
  "v_mfma_f32_16x16x32_bf16 v[48:51], a[28:31], v[40:43], v[48:51]\n"
  "ds_read_b128 v[32:35], v10 offset:20480\n"
  "s_waitcnt lgkmcnt(5)\n"
  "v_mfma_f32_16x16x32_bf16 v[52:55], a[28:31], v[44:47], v[52:55]\n"
  "ds_read_b128 v[36:39], v10 offset:22528\n"
  "v_mfma_f32_16x16x32_bf16 v[56:59], a[100:103], v[40:43], v[56:59]\n"
  "v_mfma_f32_16x16x32_bf16 v[60:63], a[100:103], v[44:47], v[60:63]\n"
  "s_add_u32 m0, s58, 3968\n"
  "s_nop 0\n"
  "global_load_lds_dwordx4 v3, s[42:43] offset:128\n"
  "v_mfma_f32_16x16x32_bf16 v[64:67], v[40:43], a[172:175], v[64:67]\n"
  "v_mfma_f32_16x16x32_bf16 v[68:71], v[44:47], a[172:175], v[68:71]\n"
  "s_waitcnt lgkmcnt(5)\n"
  "v_mfma_f32_16x16x32_bf16 v[48:51], a[32:35], v[16:19], v[48:51]\n"
  "ds_read_b128 v[40:43], v11 offset:20480\n"
  "s_waitcnt lgkmcnt(5)\n"
  "v_mfma_f32_16x16x32_bf16 v[52:55], a[32:35], v[20:23], v[52:55]\n"
  "ds_read_b128 v[44:47], v11 offset:22528\n"
  "v_mfma_f32_16x16x32_bf16 v[56:59], a[104:107], v[16:19], v[56:59]\n"
  "v_mfma_f32_16x16x32_bf16 v[60:63], a[104:107], v[20:23], v[60:63]\n"
  "s_add_u32 m0, s58, 7936\n"
  "s_nop 0\n"
  "global_load_lds_dwordx4 v3, s[42:43] offset:256\n"
  "v_mfma_f32_16x16x32_bf16 v[64:67], v[16:19], a[176:179], v[64:67]\n"
  "v_mfma_f32_16x16x32_bf16 v[68:71], v[20:23], a[176:179], v[68:71]\n"
  "s_waitcnt lgkmcnt(5)\n"
  "v_mfma_f32_16x16x32_bf16 v[48:51], a[36:39], v[24:27], v[48:51]\n"
  "ds_read_b128 v[16:19], v10 offset:24576\n"
  "s_waitcnt lgkmcnt(5)\n"
  "v_mfma_f32_16x16x32_bf16 v[52:55], a[36:39], v[28:31], v[52:55]\n"
  "ds_read_b128 v[20:23], v10 offset:26624\n"
  "v_mfma_f32_16x16x32_bf16 v[56:59], a[108:111], v[24:27], v[56:59]\n"
  "v_mfma_f32_16x16x32_bf16 v[60:63], a[108:111], v[28:31], v[60:63]\n"
  "s_add_u32 m0, s58, 11904\n"
  "s_nop 0\n"
  "global_load_lds_dwordx4 v3, s[42:43] offset:384\n"
  "v_mfma_f32_16x16x32_bf16 v[64:67], v[24:27], a[180:183], v[64:67]\n"
  "v_mfma_f32_16x16x32_bf16 v[68:71], v[28:31], a[180:183], v[68:71]\n"
  "s_waitcnt lgkmcnt(5)\n"
  "v_mfma_f32_16x16x32_bf16 v[48:51], a[40:43], v[32:35], v[48:51]\n"
  "ds_read_b128 v[24:27], v11 offset:24576\n"
  "s_waitcnt lgkmcnt(5)\n"
  "v_mfma_f32_16x16x32_bf16 v[52:55], a[40:43], v[36:39], v[52:55]\n"
  "ds_read_b128 v[28:31], v11 offset:26624\n"
  "v_mfma_f32_16x16x32_bf16 v[56:59], a[112:115], v[32:35], v[56:59]\n"
  "v_mfma_f32_16x16x32_bf16 v[60:63], a[112:115], v[36:39], v[60:63]\n"
  "s_add_u32 m0, s58, 15872\n"
  "s_nop 0\n"
  "global_load_lds_dwordx4 v3, s[42:43] offset:512\n"
  "v_mfma_f32_16x16x32_bf16 v[64:67], v[32:35], a[184:187], v[64:67]\n"
  "v_mfma_f32_16x16x32_bf16 v[68:71], v[36:39], a[184:187], v[68:71]\n"
  "s_waitcnt lgkmcnt(5)\n"
  "v_mfma_f32_16x16x32_bf16 v[48:51], a[44:47], v[40:43], v[48:51]\n"
  "ds_read_b128 v[32:35], v10 offset:28672\n"
  "s_waitcnt lgkmcnt(5)\n"
  "v_mfma_f32_16x16x32_bf16 v[52:55], a[44:47], v[44:47], v[52:55]\n"
  "ds_read_b128 v[36:39], v10 offset:30720\n"
  "v_mfma_f32_16x16x32_bf16 v[56:59], a[116:119], v[40:43], v[56:59]\n"
  "v_mfma_f32_16x16x32_bf16 v[60:63], a[116:119], v[44:47], v[60:63]\n"
  "s_add_u32 m0, s58, 19840\n"
  "s_nop 0\n"
  "global_load_lds_dwordx4 v3, s[42:43] offset:640\n"
  "v_mfma_f32_16x16x32_bf16 v[64:67], v[40:43], a[188:191], v[64:67]\n"
  "v_mfma_f32_16x16x32_bf16 v[68:71], v[44:47], a[188:191], v[68:71]\n"
  "s_waitcnt lgkmcnt(5)\n"
  "v_mfma_f32_16x16x32_bf16 v[48:51], a[48:51], v[16:19], v[48:51]\n"
  "ds_read_b128 v[40:43], v11 offset:28672\n"
  "s_waitcnt lgkmcnt(5)\n"
  "v_mfma_f32_16x16x32_bf16 v[52:55], a[48:51], v[20:23], v[52:55]\n"
  "ds_read_b128 v[44:47], v11 offset:30720\n"
  "v_mfma_f32_16x16x32_bf16 v[56:59], a[120:123], v[16:19], v[56:59]\n"
  "v_mfma_f32_16x16x32_bf16 v[60:63], a[120:123], v[20:23], v[60:63]\n"
  "s_add_u32 m0, s58, 23808\n"
  "s_nop 0\n"
  "global_load_lds_dwordx4 v3, s[42:43] offset:768\n"
  "v_mfma_f32_16x16x32_bf16 v[64:67], v[16:19], a[192:195], v[64:67]\n"
  "v_mfma_f32_16x16x32_bf16 v[68:71], v[20:23], a[192:195], v[68:71]\n"
  "s_waitcnt lgkmcnt(5)\n"
  "v_mfma_f32_16x16x32_bf16 v[48:51], a[52:55], v[24:27], v[48:51]\n"
  "ds_read_b128 v[16:19], v10 offset:32768\n"
  "s_waitcnt lgkmcnt(5)\n"
  "v_mfma_f32_16x16x32_bf16 v[52:55], a[52:55], v[28:31], v[52:55]\n"
  "ds_read_b128 v[20:23], v10 offset:34816\n"
  "v_mfma_f32_16x16x32_bf16 v[56:59], a[124:127], v[24:27], v[56:59]\n"
  "v_mfma_f32_16x16x32_bf16 v[60:63], a[124:127], v[28:31], v[60:63]\n"
  "s_add_u32 m0, s58, 27776\n"
  "s_nop 0\n"
  "global_load_lds_dwordx4 v3, s[42:43] offset:896\n"
  "v_mfma_f32_16x16x32_bf16 v[64:67], v[24:27], a[196:199], v[64:67]\n"
  "v_mfma_f32_16x16x32_bf16 v[68:71], v[28:31], a[196:199], v[68:71]\n"
  "s_waitcnt lgkmcnt(5)\n"
  "v_mfma_f32_16x16x32_bf16 v[48:51], a[56:59], v[32:35], v[48:51]\n"
  "ds_read_b128 v[24:27], v11 offset:32768\n"
  "s_waitcnt lgkmcnt(5)\n"
  "v_mfma_f32_16x16x32_bf16 v[52:55], a[56:59], v[36:39], v[52:55]\n"
  "ds_read_b128 v[28:31], v11 offset:34816\n"
  "v_mfma_f32_16x16x32_bf16 v[56:59], a[128:131], v[32:35], v[56:59]\n"
  "v_mfma_f32_16x16x32_bf16 v[60:63], a[128:131], v[36:39], v[60:63]\n"
  "s_add_u32 m0, s58, 31744\n"
  "s_nop 0\n"
  "global_load_lds_dwordx4 v3, s[42:43] offset:1024\n"
  "v_mfma_f32_16x16x32_bf16 v[64:67], v[32:35], a[200:203], v[64:67]\n"
  "v_mfma_f32_16x16x32_bf16 v[68:71], v[36:39], a[200:203], v[68:71]\n"
  "s_waitcnt vmcnt(9)\n"
  "s_barrier\n"
  "s_waitcnt lgkmcnt(5)\n"
  "v_mfma_f32_16x16x32_bf16 v[48:51], a[60:63], v[40:43], v[48:51]\n"
  "ds_read_b128 v[32:35], v12 offset:0\n"
  "s_waitcnt lgkmcnt(5)\n"
  "v_mfma_f32_16x16x32_bf16 v[52:55], a[60:63], v[44:47], v[52:55]\n"
  "ds_read_b128 v[36:39], v12 offset:2048\n"
  "v_mfma_f32_16x16x32_bf16 v[56:59], a[132:135], v[40:43], v[56:59]\n"
  "v_mov_b32 v6, 0\n"
  "v_mov_b32 v7, s56\n"
  "s_cmp_eq_u32 s47, 3\n"
  "s_cselect_b64 exec, 1, 0\n"
  "global_store_dword v6, v7, s[64:65] sc1\n"
  "s_mov_b64 exec, -1\n"
  "v_mfma_f32_16x16x32_bf16 v[60:63], a[132:135], v[44:47], v[60:63]\n"
  "s_bitcmp1_b64 s[4:5], s56\n"
  "s_cselect_b32 s74, 8192, 0\n"
  "v_mfma_f32_16x16x32_bf16 v[64:67], v[40:43], a[204:207], v[64:67]\n"
  "s_and_b32 s75, s56, 63\n"
  "s_lshl_b32 s75, s75, 21\n"
  "v_mfma_f32_16x16x32_bf16 v[68:71], v[44:47], a[204:207], v[68:71]\n"
  "s_add_u32 s74, s74, s75\n"
  "s_add_u32 s74, s74, 20971520\n"
  "s_waitcnt lgkmcnt(5)\n"
  "v_mfma_f32_16x16x32_bf16 v[48:51], a[64:67], v[16:19], v[48:51]\n"
  "ds_read_b128 v[40:43], v13 offset:0\n"
  "s_waitcnt lgkmcnt(5)\n"
  "v_mfma_f32_16x16x32_bf16 v[52:55], a[64:67], v[20:23], v[52:55]\n"
  "ds_read_b128 v[44:47], v13 offset:2048\n"
  "v_mfma_f32_16x16x32_bf16 v[56:59], a[136:139], v[16:19], v[56:59]\n"
  "s_xor_b32 s75, s74, s44\n"
  "s_add_u32 s60, s36, s75\n"
  "v_mfma_f32_16x16x32_bf16 v[60:63], a[136:139], v[20:23], v[60:63]\n"
  "s_addc_u32 s61, s37, 0\n"
  "s_xor_b32 s75, s74, s45\n"
  "v_mfma_f32_16x16x32_bf16 v[64:67], v[16:19], a[208:211], v[64:67]\n"
  "s_add_u32 s62, s36, s75\n"
  "s_addc_u32 s63, s37, 0\n"
  "v_mfma_f32_16x16x32_bf16 v[68:71], v[20:23], a[208:211], v[68:71]\n"
  "s_waitcnt lgkmcnt(5)\n"
  "v_mfma_f32_16x16x32_bf16 v[48:51], a[68:71], v[24:27], v[48:51]\n"
  "ds_read_b128 v[16:19], v12 offset:4096\n"
  "s_waitcnt lgkmcnt(5)\n"
  "v_mfma_f32_16x16x32_bf16 v[52:55], a[68:71], v[28:31], v[52:55]\n"
  "ds_read_b128 v[20:23], v12 offset:6144\n"
  "v_mfma_f32_16x16x32_bf16 v[56:59], a[140:143], v[24:27], v[56:59]\n"
  "v_mfma_f32_16x16x32_bf16 v[60:63], a[140:143], v[28:31], v[60:63]\n"
  "v_mfma_f32_16x16x32_bf16 v[64:67], v[24:27], a[212:215], v[64:67]\n"
  "v_mfma_f32_16x16x32_bf16 v[68:71], v[28:31], a[212:215], v[68:71]\n"
  "s_add_u32 s56, s56, 1\n"
  "s_branch PJL\n"
  "PJE:\n"
  "s_waitcnt vmcnt(0) lgkmcnt(0)\n"
  "s_nop 3\n"
  "v_mov_b32 v7, v7\n"
  "s_nop 2\n"
  "v_mov_b32 v7, v7\n"
  "s_nop 2\n"
  "v_mul_f32 v72, v8, v72\n"
  "v_mul_f32 v73, v8, v73\n"
  "v_mul_f32 v74, v8, v74\n"
  "v_mul_f32 v75, v8, v75\n"
  "v_cvt_pk_bf16_f32 v96, v72, v73\n"
  "v_cvt_pk_bf16_f32 v97, v74, v75\n"
  "v_mul_f32 v80, v8, v80\n"
  "v_mul_f32 v81, v8, v81\n"
  "v_mul_f32 v82, v8, v82\n"
  "v_mul_f32 v83, v8, v83\n"
  "v_cvt_pk_bf16_f32 v98, v80, v81\n"
  "v_cvt_pk_bf16_f32 v99, v82, v83\n"
  "global_store_dwordx4 v0, v[96:99], s[60:61] offset:0 sc1\n"
  "v_mul_f32 v76, v8, v76\n"
  "v_mul_f32 v77, v8, v77\n"
  "v_mul_f32 v78, v8, v78\n"
  "v_mul_f32 v79, v8, v79\n"
  "v_cvt_pk_bf16_f32 v100, v76, v77\n"
  "v_cvt_pk_bf16_f32 v101, v78, v79\n"
  "v_mul_f32 v84, v8, v84\n"
  "v_mul_f32 v85, v8, v85\n"
  "v_mul_f32 v86, v8, v86\n"
  "v_mul_f32 v87, v8, v87\n"
  "v_cvt_pk_bf16_f32 v102, v84, v85\n"
  "v_cvt_pk_bf16_f32 v103, v86, v87\n"
  "global_store_dwordx4 v0, v[100:103], s[60:61] offset:1024 sc1\n"
  "v_cvt_pk_bf16_f32 v104, v88, v89\n"
  "v_cvt_pk_bf16_f32 v105, v90, v91\n"
  "v_cvt_pk_bf16_f32 v106, v92, v93\n"
  "v_cvt_pk_bf16_f32 v107, v94, v95\n"
  "global_store_dwordx4 v0, v[104:107], s[62:63] offset:0 sc1\n"
  "s_waitcnt vmcnt(0)\n"
  "s_barrier\n"
  "v_mov_b32 v6, 0\n"
  "v_mov_b32 v7, s56\n"
  "s_cmp_eq_u32 s47, 3\n"
  "s_cselect_b64 exec, 1, 0\n"
  "global_store_dword v6, v7, s[64:65] sc1\n"
  "s_mov_b64 exec, -1\n"
  "s_branch PJR\n"
  "PJQ:\n"
  "s_endpgm\n"
  "PJR:\n"
      : "+{v[0:15]}"(vin), "+{s[36:43]}"(sa), "+{s[44:59]}"(sb) :: "a0", "a1", "a2", "a3", "a4", "a5", "a6", "a7", "a8", "a9", "a10", "a11", "a12", "a13", "a14", "a15", "a16", "a17", "a18", "a19", "a20", "a21", "a22", "a23", "a24", "a25", "a26", "a27", "a28", "a29", "a30", "a31", "a32", "a33", "a34", "a35", "a36", "a37", "a38", "a39", "a40", "a41", "a42", "a43", "a44", "a45", "a46", "a47", "a48", "a49", "a50", "a51", "a52", "a53", "a54", "a55", "a56", "a57", "a58", "a59", "a60", "a61", "a62", "a63", "a64", "a65", "a66", "a67", "a68", "a69", "a70", "a71", "a72", "a73", "a74", "a75", "a76", "a77", "a78", "a79", "a80", "a81", "a82", "a83", "a84", "a85", "a86", "a87", "a88", "a89", "a90", "a91", "a92", "a93", "a94", "a95", "a96", "a97", "a98", "a99", "a100", "a101", "a102", "a103", "a104", "a105", "a106", "a107", "a108", "a109", "a110", "a111", "a112", "a113", "a114", "a115", "a116", "a117", "a118", "a119", "a120", "a121", "a122", "a123", "a124", "a125", "a126", "a127", "a128", "a129", "a130", "a131", "a132", "a133", "a134", "a135", "a136", "a137", "a138", "a139", "a140", "a141", "a142", "a143", "a144", "a145", "a146", "a147", "a148", "a149", "a150", "a151", "a152", "a153", "a154", "a155", "a156", "a157", "a158", "a159", "a160", "a161", "a162", "a163", "a164", "a165", "a166", "a167", "a168", "a169", "a170", "a171", "a172", "a173", "a174", "a175", "a176", "a177", "a178", "a179", "a180", "a181", "a182", "a183", "a184", "a185", "a186", "a187", "a188", "a189", "a190", "a191", "a192", "a193", "a194", "a195", "a196", "a197", "a198", "a199", "a200", "a201", "a202", "a203", "a204", "a205", "a206", "a207", "a208", "a209", "a210", "a211", "a212", "a213", "a214", "a215", "s4", "s5", "s60", "s61", "s62", "s63", "s64", "s65", "s74", "s75", "s76", "s80", "v16", "v17", "v18", "v19", "v20", "v21", "v22", "v23", "v24", "v25", "v26", "v27", "v28", "v29", "v30", "v31", "v32", "v33", "v34", "v35", "v36", "v37", "v38", "v39", "v40", "v41", "v42", "v43", "v44", "v45", "v46", "v47", "v48", "v49", "v50", "v51", "v52", "v53", "v54", "v55", "v56", "v57", "v58", "v59", "v60", "v61", "v62", "v63", "v64", "v65", "v66", "v67", "v68", "v69", "v70", "v71", "v72", "v73", "v74", "v75", "v76", "v77", "v78", "v79", "v80", "v81", "v82", "v83", "v84", "v85", "v86", "v87", "v88", "v89", "v90", "v91", "v92", "v93", "v94", "v95", "v96", "v97", "v98", "v99", "v100", "v101", "v102", "v103", "v104", "v105", "v106", "v107", "vcc", "scc", "memory");
  } else if (tk >= 24 && tk < 28) { ARGS; PTR(sa, 0, p.ws);
    const int c = fd * 4 + tk - 24, grp = min(lane / 10, 5), pos = lane % 10;
    const bool real = lane < 60 && pos < 9;
    const u16* src = p.wqkv + (size_t)wave * 1728 + (grp >> 1) * DM + 36 * c + (grp & 1) * 18 + 2 * min(pos, 8);
#pragma unroll 1
    for (int b = 0; b < 2; ++b) {
      unsigned v[72];
#pragma unroll
      for (int i = 0; i < 72; ++i) v[i] = *(const gu32*)(src + (size_t)(b * 72 + i) * 4 * 1728);
#pragma unroll
      for (int i = 0; i < 72; ++i) if (lane != 60) *(LAS unsigned*)(lds + ((b * 72 + i) * 4 + wave) * 264 + lane * 4) = real ? v[i] : 0u;
    }
#pragma unroll
    for (int i = 0; i < 3; ++i) { const int r = min(tid + 256 * i, 575); *(LAS unsigned*)(lds + r * 264 + 240) = p.gate[r]; }
    wg_barrier();
    const int rt = wave & 1, hh = wave >> 1;
    constexpr int RS = 304, OI = 64 * RS;
    vin[0] = (8 * g + qq) * 264 + wave * 64 + c4 * 8; vin[1] = x_src(wave, lane); vin[2] = x_frag(lane);
    vin[3] = OI + (16 * rt + n) * 80 + hh * 40 + g * 8; vin[4] = OI + (8 * wave + (lane & 7)) * 80 + min(lane >> 3, 4) * 16;
    vin[5] = (wave >> 1) * 20480 + (lane >> 3) * 256 + ((wave & 1) * 8 + (lane & 7)) * 16; vin[6] = (lane & 31) * RS + 288;
    vin[7] = (n & ~1) - 4 * g; vin[8] = (n & 1) ? 0xffff0000u : 0xffffu;
#pragma unroll
    for (int t = 0; t < 2; ++t) { const int g4 = 8 * wave + 4 * t + g; vin[10 + t] = n * RS + 48 * (g4 / 5) + 8 * (g4 % 5); }
    vin[12] = g == 3 ? OI + 32 * 80 : (16 * rt + n) * RS + 48 * hh + 16 * g;
    vin[13] = ((16 * rt - 16 + n) & 63) * RS + 96 + 48 * hh + 16 * min(g, 2);
    vin[14] = ((16 * rt - 16 + 4 * g + qq) & 63) * RS + 192 + 48 * hh + 8 * c4;
    PTR(sa, 2, p.x);
    sb[0] = WO_OA + c * 1280; sb[1] = WO_G + c * 128; sb[2] = (C_AP) + c * 128; sb[3] = wave; sb[4] = WO_ST + (role * 4 + wave) * 4096;
    UNIFORM(sa, 8); UNIFORM(sb, 16);
    asm volatile(
  "s_nop 4\n"
  "s_lshl_b32 s80, s47, 10\n"
  "v_add_u32 v9, 0, v0\n"
  "ds_read_b64_tr_b16 a[0:1], v9 offset:0\n"
  "ds_read_b64_tr_b16 a[2:3], v9 offset:1056\n"
  "ds_read_b64_tr_b16 a[4:5], v9 offset:8448\n"
  "ds_read_b64_tr_b16 a[6:7], v9 offset:9504\n"
  "ds_read_b64_tr_b16 a[8:9], v9 offset:16896\n"
  "ds_read_b64_tr_b16 a[10:11], v9 offset:17952\n"
  "ds_read_b64_tr_b16 a[12:13], v9 offset:25344\n"
  "ds_read_b64_tr_b16 a[14:15], v9 offset:26400\n"
  "ds_read_b64_tr_b16 a[16:17], v9 offset:33792\n"
  "ds_read_b64_tr_b16 a[18:19], v9 offset:34848\n"
  "ds_read_b64_tr_b16 a[20:21], v9 offset:42240\n"
  "ds_read_b64_tr_b16 a[22:23], v9 offset:43296\n"
  "v_add_u32 v9, 32, v0\n"
  "ds_read_b64_tr_b16 a[72:73], v9 offset:0\n"
  "ds_read_b64_tr_b16 a[74:75], v9 offset:1056\n"
  "ds_read_b64_tr_b16 a[76:77], v9 offset:8448\n"
  "ds_read_b64_tr_b16 a[78:79], v9 offset:9504\n"
  "ds_read_b64_tr_b16 a[80:81], v9 offset:16896\n"
  "ds_read_b64_tr_b16 a[82:83], v9 offset:17952\n"
  "ds_read_b64_tr_b16 a[84:85], v9 offset:25344\n"
  "ds_read_b64_tr_b16 a[86:87], v9 offset:26400\n"
  "ds_read_b64_tr_b16 a[88:89], v9 offset:33792\n"
  "ds_read_b64_tr_b16 a[90:91], v9 offset:34848\n"
  "ds_read_b64_tr_b16 a[92:93], v9 offset:42240\n"
  "ds_read_b64_tr_b16 a[94:95], v9 offset:43296\n"
  "v_add_u32 v9, 50688, v0\n"
  "ds_read_b64_tr_b16 a[24:25], v9 offset:0\n"
  "ds_read_b64_tr_b16 a[26:27], v9 offset:1056\n"
  "ds_read_b64_tr_b16 a[28:29], v9 offset:8448\n"
  "ds_read_b64_tr_b16 a[30:31], v9 offset:9504\n"
  "ds_read_b64_tr_b16 a[32:33], v9 offset:16896\n"
  "ds_read_b64_tr_b16 a[34:35], v9 offset:17952\n"
  "ds_read_b64_tr_b16 a[36:37], v9 offset:25344\n"
  "ds_read_b64_tr_b16 a[38:39], v9 offset:26400\n"
  "ds_read_b64_tr_b16 a[40:41], v9 offset:33792\n"
  "ds_read_b64_tr_b16 a[42:43], v9 offset:34848\n"
  "ds_read_b64_tr_b16 a[44:45], v9 offset:42240\n"
  "ds_read_b64_tr_b16 a[46:47], v9 offset:43296\n"
  "v_add_u32 v9, 50720, v0\n"
  "ds_read_b64_tr_b16 a[96:97], v9 offset:0\n"
  "ds_read_b64_tr_b16 a[98:99], v9 offset:1056\n"
  "ds_read_b64_tr_b16 a[100:101], v9 offset:8448\n"
  "ds_read_b64_tr_b16 a[102:103], v9 offset:9504\n"
  "ds_read_b64_tr_b16 a[104:105], v9 offset:16896\n"
  "ds_read_b64_tr_b16 a[106:107], v9 offset:17952\n"
  "ds_read_b64_tr_b16 a[108:109], v9 offset:25344\n"
  "ds_read_b64_tr_b16 a[110:111], v9 offset:26400\n"
  "ds_read_b64_tr_b16 a[112:113], v9 offset:33792\n"
  "ds_read_b64_tr_b16 a[114:115], v9 offset:34848\n"
  "ds_read_b64_tr_b16 a[116:117], v9 offset:42240\n"
  "ds_read_b64_tr_b16 a[118:119], v9 offset:43296\n"
  "v_add_u32 v9, 101376, v0\n"
  "ds_read_b64_tr_b16 a[48:49], v9 offset:0\n"
  "ds_read_b64_tr_b16 a[50:51], v9 offset:1056\n"
  "ds_read_b64_tr_b16 a[52:53], v9 offset:8448\n"
  "ds_read_b64_tr_b16 a[54:55], v9 offset:9504\n"
  "ds_read_b64_tr_b16 a[56:57], v9 offset:16896\n"
  "ds_read_b64_tr_b16 a[58:59], v9 offset:17952\n"
  "ds_read_b64_tr_b16 a[60:61], v9 offset:25344\n"
  "ds_read_b64_tr_b16 a[62:63], v9 offset:26400\n"
  "ds_read_b64_tr_b16 a[64:65], v9 offset:33792\n"
  "ds_read_b64_tr_b16 a[66:67], v9 offset:34848\n"
  "ds_read_b64_tr_b16 a[68:69], v9 offset:42240\n"
  "ds_read_b64_tr_b16 a[70:71], v9 offset:43296\n"
  "v_add_u32 v9, 101408, v0\n"
  "ds_read_b64_tr_b16 a[120:121], v9 offset:0\n"
  "ds_read_b64_tr_b16 a[122:123], v9 offset:1056\n"
  "ds_read_b64_tr_b16 a[124:125], v9 offset:8448\n"
  "ds_read_b64_tr_b16 a[126:127], v9 offset:9504\n"
  "ds_read_b64_tr_b16 a[128:129], v9 offset:16896\n"
  "ds_read_b64_tr_b16 a[130:131], v9 offset:17952\n"
  "ds_read_b64_tr_b16 a[132:133], v9 offset:25344\n"
  "ds_read_b64_tr_b16 a[134:135], v9 offset:26400\n"
  "ds_read_b64_tr_b16 a[136:137], v9 offset:33792\n"
  "ds_read_b64_tr_b16 a[138:139], v9 offset:34848\n"
  "ds_read_b64_tr_b16 a[140:141], v9 offset:42240\n"
  "ds_read_b64_tr_b16 a[142:143], v9 offset:43296\n"
  "s_waitcnt lgkmcnt(0)\n"
  "s_barrier\n"
  "v_mbcnt_lo_u32_b32 v127, -1, 0\n"
  "v_mbcnt_hi_u32_b32 v127, -1, v127\n"
  "v_lshlrev_b32 v9, 4, v127\n"
  "v_add_u32 v9, s80, v9\n"
  "v_lshlrev_b32 v127, 2, v127\n"
  "v_mov_b64 v[120:121], 0\n"
  "v_mov_b64 v[122:123], 0\n"
  "v_mov_b32 v128, 0xce6e6b28\n"
  "ds_write_b128 v9, v[120:123] offset:0\n"
  "ds_write_b128 v9, v[120:123] offset:4096\n"
  "ds_write_b128 v9, v[120:123] offset:8192\n"
  "ds_write_b128 v9, v[120:123] offset:12288\n"
  "ds_write_b128 v9, v[120:123] offset:16384\n"
  "ds_write_b128 v9, v[120:123] offset:20480\n"
  "s_waitcnt lgkmcnt(0)\n"
  "s_barrier\n"
  "s_add_u32 s58, s80, 22272\n"
  "s_add_u32 m0, s58, 0\n"
  "s_nop 0\n"
  "global_load_lds_dwordx4 v1, s[38:39] offset:0\n"
  "s_add_u32 m0, s58, 3968\n"
  "s_nop 0\n"
  "global_load_lds_dwordx4 v1, s[38:39] offset:128\n"
  "s_add_u32 m0, s58, 7936\n"
  "s_nop 0\n"
  "global_load_lds_dwordx4 v1, s[38:39] offset:256\n"
  "s_add_u32 m0, s58, 11904\n"
  "s_nop 0\n"
  "global_load_lds_dwordx4 v1, s[38:39] offset:384\n"
  "s_add_u32 m0, s58, 15872\n"
  "s_nop 0\n"
  "global_load_lds_dwordx4 v1, s[38:39] offset:512\n"
  "s_add_u32 m0, s58, 19840\n"
  "s_nop 0\n"
  "global_load_lds_dwordx4 v1, s[38:39] offset:640\n"
  "s_add_u32 m0, s58, 23808\n"
  "s_nop 0\n"
  "global_load_lds_dwordx4 v1, s[38:39] offset:768\n"
  "s_add_u32 m0, s58, 27776\n"
  "s_nop 0\n"
  "global_load_lds_dwordx4 v1, s[38:39] offset:896\n"
  "s_add_u32 m0, s58, 31744\n"
  "s_nop 0\n"
  "global_load_lds_dwordx4 v1, s[38:39] offset:1024\n"
  "v_add_u32 v68, 0, v13\n"
  "v_subrev_u32 v9, 19456, v68\n"
  "v_min_u32 v68, v68, v9\n"
  "v_add_u32 v72, 0, v14\n"
  "v_subrev_u32 v9, 19456, v72\n"
  "v_min_u32 v72, v72, v9\n"
  "v_add_u32 v69, 4864, v13\n"
  "v_subrev_u32 v9, 19456, v69\n"
  "v_min_u32 v69, v69, v9\n"
  "v_add_u32 v73, 4864, v14\n"
  "v_subrev_u32 v9, 19456, v73\n"
  "v_min_u32 v73, v73, v9\n"
  "v_add_u32 v70, 9728, v13\n"
  "v_subrev_u32 v9, 19456, v70\n"
  "v_min_u32 v70, v70, v9\n"
  "v_add_u32 v74, 9728, v14\n"
  "v_subrev_u32 v9, 19456, v74\n"
  "v_min_u32 v74, v74, v9\n"
  "v_add_u32 v71, 14592, v13\n"
  "v_subrev_u32 v9, 19456, v71\n"
  "v_min_u32 v71, v71, v9\n"
  "v_add_u32 v75, 14592, v14\n"
  "v_subrev_u32 v9, 19456, v75\n"
  "v_min_u32 v75, v75, v9\n"
  "v_mov_b32 v66, v12\n"
  "v_add_u32 v67, 9728, v12\n"
  "v_min_u32 v67, 22016, v67\n"
  "v_add_u32 v9, 16, v7\n"
  "v_cmp_gt_u32 s[8:9], 15, v9\n"
  "v_add_u32 v9, 14, v7\n"
  "v_cmp_gt_u32 s[10:11], 15, v9\n"
  "v_add_u32 v9, 0, v7\n"
  "v_cmp_gt_u32 s[12:13], 15, v9\n"
  "v_add_u32 v9, -2, v7\n"
  "v_cmp_gt_u32 s[14:15], 15, v9\n"
  "s_mov_b64 s[16:17], s[8:9]\n"
  "s_mov_b64 s[18:19], s[10:11]\n"
  "s_bitcmp1_b32 s47, 0\n"
  "s_cselect_b64 s[8:9], s[8:9], 0\n"
  "s_cselect_b64 s[10:11], s[10:11], 0\n"
  "s_mov_b32 s56, 400000\n"
  "s_add_u32 s74, s36, 16778624\n"
  "s_addc_u32 s75, s37, 0\n"
  "PLCA:\n"
  "v_mov_b32 v9, 0\n"
  "global_load_dword v9, v9, s[74:75] sc1\n"
  "s_waitcnt vmcnt(0)\n"
  "v_readfirstlane_b32 s76, v9\n"
  "s_cmp_ge_u32 s76, 0x40000\n"
  "s_cbranch_scc1 PDCA\n"
  "s_sub_u32 s56, s56, 1\n"
  "s_cbranch_scc1 AQ\n"
  "s_sleep 4\n"
  "s_branch PLCA\n"
  "PDCA:\n"
  "s_and_b32 s74, s76, 2\n"
  "s_lshr_b32 s75, s74, 1\n"
  "s_lshl_b32 s4, 0x55555555, s75\n"
  "s_mov_b32 s5, s4\n"
  "s_lshl_b32 s74, s74, 12\n"
  "s_xor_b32 s46, s74, s46\n"
  "s_add_u32 s60, s36, s44\n"
  "s_addc_u32 s61, s37, 0\n"
  "s_add_u32 s62, s36, s45\n"
  "s_addc_u32 s63, s37, 0\n"
  "s_add_u32 s64, s36, s46\n"
  "s_addc_u32 s65, s37, 0\n"
  "s_cmp_eq_u32 s47, 3\n"
  "s_cselect_b32 s68, -1, 0\n"
  "s_mov_b32 s69, 0\n"
  "s_mov_b32 s70, 0xaaaaaaaa\n"
  "s_mov_b32 s71, 0xaaaaaaaa\n"
  "s_mov_b32 s56, -1\n"
  "s_waitcnt vmcnt(0)\n"
  "s_waitcnt vmcnt(2) lgkmcnt(0)\n"
  "s_barrier\n"
  "v_add_u32 v64, 22272, v2\n"
  "v_xor_b32 v65, 64, v64\n"
  "s_add_u32 s58, s80, 59136\n"
  "s_cmp_lt_i32 s56, 62\n"
  "s_cselect_b32 s74, 36864, 0\n"
  "s_add_u32 s38, s38, s74\n"
  "s_addc_u32 s39, s39, 0\n"
  "ds_read_b128 v[16:19], v64 offset:0\n"
  "ds_read_b128 v[20:23], v64 offset:2048\n"
  "ds_read_b128 v[24:27], v65 offset:0\n"
  "ds_read_b128 v[28:31], v65 offset:2048\n"
  "ds_read_b128 v[32:35], v64 offset:4096\n"
  "ds_read_b128 v[36:39], v64 offset:6144\n"
  "s_waitcnt lgkmcnt(5)\n"
  "v_mfma_f32_16x16x32_bf16 v[48:51], a[0:3], v[16:19], 0\n"
  "ds_read_b128 v[40:43], v65 offset:4096\n"
  "s_waitcnt lgkmcnt(5)\n"
  "v_mfma_f32_16x16x32_bf16 v[52:55], a[0:3], v[20:23], 0\n"
  "ds_read_b128 v[44:47], v65 offset:6144\n"
  "v_mfma_f32_16x16x32_bf16 v[56:59], a[72:75], v[16:19], 0\n"
  "v_mfma_f32_16x16x32_bf16 v[60:63], a[72:75], v[20:23], 0\n"
  "s_waitcnt lgkmcnt(5)\n"
  "v_mfma_f32_16x16x32_bf16 v[48:51], a[4:7], v[24:27], v[48:51]\n"
  "ds_read_b128 v[16:19], v64 offset:8192\n"
  "s_waitcnt lgkmcnt(5)\n"
  "v_mfma_f32_16x16x32_bf16 v[52:55], a[4:7], v[28:31], v[52:55]\n"
  "ds_read_b128 v[20:23], v64 offset:10240\n"
  "v_mfma_f32_16x16x32_bf16 v[56:59], a[76:79], v[24:27], v[56:59]\n"
  "s_add_u32 m0, s58, 0\n"
  "s_nop 0\n"
  "global_load_lds_dwordx4 v1, s[38:39] offset:0\n"
  "v_mfma_f32_16x16x32_bf16 v[60:63], a[76:79], v[28:31], v[60:63]\n"
  "s_waitcnt lgkmcnt(5)\n"
  "v_mfma_f32_16x16x32_bf16 v[48:51], a[8:11], v[32:35], v[48:51]\n"
  "ds_read_b128 v[24:27], v65 offset:8192\n"
  "s_waitcnt lgkmcnt(5)\n"
  "v_mfma_f32_16x16x32_bf16 v[52:55], a[8:11], v[36:39], v[52:55]\n"
  "ds_read_b128 v[28:31], v65 offset:10240\n"
  "v_mfma_f32_16x16x32_bf16 v[56:59], a[80:83], v[32:35], v[56:59]\n"
  "s_add_u32 m0, s58, 3968\n"
  "s_nop 0\n"
  "global_load_lds_dwordx4 v1, s[38:39] offset:128\n"
  "v_mfma_f32_16x16x32_bf16 v[60:63], a[80:83], v[36:39], v[60:63]\n"
  "s_waitcnt lgkmcnt(5)\n"
  "v_mfma_f32_16x16x32_bf16 v[48:51], a[12:15], v[40:43], v[48:51]\n"
  "ds_read_b128 v[32:35], v64 offset:12288\n"
  "s_waitcnt lgkmcnt(5)\n"
  "v_mfma_f32_16x16x32_bf16 v[52:55], a[12:15], v[44:47], v[52:55]\n"
  "ds_read_b128 v[36:39], v64 offset:14336\n"
  "v_mfma_f32_16x16x32_bf16 v[56:59], a[84:87], v[40:43], v[56:59]\n"
  "s_add_u32 m0, s58, 7936\n"
  "s_nop 0\n"
  "global_load_lds_dwordx4 v1, s[38:39] offset:256\n"
  "v_mfma_f32_16x16x32_bf16 v[60:63], a[84:87], v[44:47], v[60:63]\n"
  "s_waitcnt lgkmcnt(5)\n"
  "v_mfma_f32_16x16x32_bf16 v[48:51], a[16:19], v[16:19], v[48:51]\n"
  "ds_read_b128 v[40:43], v65 offset:12288\n"
  "s_waitcnt lgkmcnt(5)\n"
  "v_mfma_f32_16x16x32_bf16 v[52:55], a[16:19], v[20:23], v[52:55]\n"
  "ds_read_b128 v[44:47], v65 offset:14336\n"
  "v_mfma_f32_16x16x32_bf16 v[56:59], a[88:91], v[16:19], v[56:59]\n"
  "s_add_u32 m0, s58, 11904\n"
  "s_nop 0\n"
  "global_load_lds_dwordx4 v1, s[38:39] offset:384\n"
  "v_mfma_f32_16x16x32_bf16 v[60:63], a[88:91], v[20:23], v[60:63]\n"
  "s_waitcnt lgkmcnt(5)\n"
  "v_mfma_f32_16x16x32_bf16 v[48:51], a[20:23], v[24:27], v[48:51]\n"
  "ds_read_b128 v[16:19], v64 offset:16384\n"
  "s_waitcnt lgkmcnt(5)\n"
  "v_mfma_f32_16x16x32_bf16 v[52:55], a[20:23], v[28:31], v[52:55]\n"
  "ds_read_b128 v[20:23], v64 offset:18432\n"
  "v_mfma_f32_16x16x32_bf16 v[56:59], a[92:95], v[24:27], v[56:59]\n"
  "s_add_u32 m0, s58, 15872\n"
  "s_nop 0\n"
  "global_load_lds_dwordx4 v1, s[38:39] offset:512\n"
  "v_mfma_f32_16x16x32_bf16 v[60:63], a[92:95], v[28:31], v[60:63]\n"
  "s_waitcnt lgkmcnt(5)\n"
  "v_mfma_f32_16x16x32_bf16 v[48:51], a[24:27], v[32:35], v[48:51]\n"
  "ds_read_b128 v[24:27], v65 offset:16384\n"
  "s_waitcnt lgkmcnt(5)\n"
  "v_mfma_f32_16x16x32_bf16 v[52:55], a[24:27], v[36:39], v[52:55]\n"
  "ds_read_b128 v[28:31], v65 offset:18432\n"
  "v_mfma_f32_16x16x32_bf16 v[56:59], a[96:99], v[32:35], v[56:59]\n"
  "s_add_u32 m0, s58, 19840\n"
  "s_nop 0\n"
  "global_load_lds_dwordx4 v1, s[38:39] offset:640\n"
  "v_mfma_f32_16x16x32_bf16 v[60:63], a[96:99], v[36:39], v[60:63]\n"
  "s_waitcnt lgkmcnt(5)\n"
  "v_mfma_f32_16x16x32_bf16 v[48:51], a[28:31], v[40:43], v[48:51]\n"
  "ds_read_b128 v[32:35], v64 offset:20480\n"
  "s_waitcnt lgkmcnt(5)\n"
  "v_mfma_f32_16x16x32_bf16 v[52:55], a[28:31], v[44:47], v[52:55]\n"
  "ds_read_b128 v[36:39], v64 offset:22528\n"
  "v_mfma_f32_16x16x32_bf16 v[56:59], a[100:103], v[40:43], v[56:59]\n"
  "s_add_u32 m0, s58, 23808\n"
  "s_nop 0\n"
  "global_load_lds_dwordx4 v1, s[38:39] offset:768\n"
  "v_mfma_f32_16x16x32_bf16 v[60:63], a[100:103], v[44:47], v[60:63]\n"
  "s_waitcnt lgkmcnt(5)\n"
  "v_mfma_f32_16x16x32_bf16 v[48:51], a[32:35], v[16:19], v[48:51]\n"
  "ds_read_b128 v[40:43], v65 offset:20480\n"
  "s_waitcnt lgkmcnt(5)\n"
  "v_mfma_f32_16x16x32_bf16 v[52:55], a[32:35], v[20:23], v[52:55]\n"
  "ds_read_b128 v[44:47], v65 offset:22528\n"
  "v_mfma_f32_16x16x32_bf16 v[56:59], a[104:107], v[16:19], v[56:59]\n"
  "s_add_u32 m0, s58, 27776\n"
  "s_nop 0\n"
  "global_load_lds_dwordx4 v1, s[38:39] offset:896\n"
  "v_mfma_f32_16x16x32_bf16 v[60:63], a[104:107], v[20:23], v[60:63]\n"
  "s_waitcnt lgkmcnt(5)\n"
  "v_mfma_f32_16x16x32_bf16 v[48:51], a[36:39], v[24:27], v[48:51]\n"
  "ds_read_b128 v[16:19], v64 offset:24576\n"
  "s_waitcnt lgkmcnt(5)\n"
  "v_mfma_f32_16x16x32_bf16 v[52:55], a[36:39], v[28:31], v[52:55]\n"
  "ds_read_b128 v[20:23], v64 offset:26624\n"
  "v_mfma_f32_16x16x32_bf16 v[56:59], a[108:111], v[24:27], v[56:59]\n"
  "s_add_u32 m0, s58, 31744\n"
  "s_nop 0\n"
  "global_load_lds_dwordx4 v1, s[38:39] offset:1024\n"
  "v_mfma_f32_16x16x32_bf16 v[60:63], a[108:111], v[28:31], v[60:63]\n"
  "s_waitcnt lgkmcnt(5)\n"
  "v_mfma_f32_16x16x32_bf16 v[48:51], a[40:43], v[32:35], v[48:51]\n"
  "ds_read_b128 v[24:27], v65 offset:24576\n"
  "s_waitcnt lgkmcnt(5)\n"
  "v_mfma_f32_16x16x32_bf16 v[52:55], a[40:43], v[36:39], v[52:55]\n"
  "ds_read_b128 v[28:31], v65 offset:26624\n"
  "v_mfma_f32_16x16x32_bf16 v[56:59], a[112:115], v[32:35], v[56:59]\n"
  "v_mfma_f32_16x16x32_bf16 v[60:63], a[112:115], v[36:39], v[60:63]\n"
  "s_waitcnt lgkmcnt(5)\n"
  "v_mfma_f32_16x16x32_bf16 v[48:51], a[44:47], v[40:43], v[48:51]\n"
  "ds_read_b128 v[32:35], v64 offset:28672\n"
  "s_waitcnt lgkmcnt(5)\n"
  "v_mfma_f32_16x16x32_bf16 v[52:55], a[44:47], v[44:47], v[52:55]\n"
  "ds_read_b128 v[36:39], v64 offset:30720\n"
  "v_mfma_f32_16x16x32_bf16 v[56:59], a[116:119], v[40:43], v[56:59]\n"
  "v_mfma_f32_16x16x32_bf16 v[60:63], a[116:119], v[44:47], v[60:63]\n"
  "s_waitcnt lgkmcnt(5)\n"
  "v_mfma_f32_16x16x32_bf16 v[48:51], a[48:51], v[16:19], v[48:51]\n"
  "ds_read_b128 v[40:43], v65 offset:28672\n"
  "s_waitcnt lgkmcnt(5)\n"
  "v_mfma_f32_16x16x32_bf16 v[52:55], a[48:51], v[20:23], v[52:55]\n"
  "ds_read_b128 v[44:47], v65 offset:30720\n"
  "v_mfma_f32_16x16x32_bf16 v[56:59], a[120:123], v[16:19], v[56:59]\n"
  "v_mfma_f32_16x16x32_bf16 v[60:63], a[120:123], v[20:23], v[60:63]\n"
  "s_waitcnt lgkmcnt(5)\n"
  "v_mfma_f32_16x16x32_bf16 v[48:51], a[52:55], v[24:27], v[48:51]\n"
  "ds_read_b128 v[16:19], v64 offset:32768\n"
  "s_waitcnt lgkmcnt(5)\n"
  "v_mfma_f32_16x16x32_bf16 v[52:55], a[52:55], v[28:31], v[52:55]\n"
  "ds_read_b128 v[20:23], v64 offset:34816\n"
  "v_mfma_f32_16x16x32_bf16 v[56:59], a[124:127], v[24:27], v[56:59]\n"
  "v_mfma_f32_16x16x32_bf16 v[60:63], a[124:127], v[28:31], v[60:63]\n"
  "s_waitcnt lgkmcnt(5)\n"
  "v_mfma_f32_16x16x32_bf16 v[48:51], a[56:59], v[32:35], v[48:51]\n"
  "ds_read_b128 v[24:27], v65 offset:32768\n"
  "s_waitcnt lgkmcnt(5)\n"
  "v_mfma_f32_16x16x32_bf16 v[52:55], a[56:59], v[36:39], v[52:55]\n"
  "ds_read_b128 v[28:31], v65 offset:34816\n"
  "v_mfma_f32_16x16x32_bf16 v[56:59], a[128:131], v[32:35], v[56:59]\n"
  "v_mfma_f32_16x16x32_bf16 v[60:63], a[128:131], v[36:39], v[60:63]\n"
  "s_waitcnt lgkmcnt(5)\n"
  "v_mfma_f32_16x16x32_bf16 v[48:51], a[60:63], v[40:43], v[48:51]\n"
  "s_waitcnt lgkmcnt(4)\n"
  "v_mfma_f32_16x16x32_bf16 v[52:55], a[60:63], v[44:47], v[52:55]\n"
  "v_mfma_f32_16x16x32_bf16 v[56:59], a[132:135], v[40:43], v[56:59]\n"
  "v_mfma_f32_16x16x32_bf16 v[60:63], a[132:135], v[44:47], v[60:63]\n"
  "s_waitcnt lgkmcnt(3)\n"
  "v_mfma_f32_16x16x32_bf16 v[48:51], a[64:67], v[16:19], v[48:51]\n"
  "s_waitcnt lgkmcnt(2)\n"
  "v_mfma_f32_16x16x32_bf16 v[52:55], a[64:67], v[20:23], v[52:55]\n"
  "v_mfma_f32_16x16x32_bf16 v[56:59], a[136:139], v[16:19], v[56:59]\n"
  "v_mfma_f32_16x16x32_bf16 v[60:63], a[136:139], v[20:23], v[60:63]\n"
  "s_waitcnt lgkmcnt(1)\n"
  "v_mfma_f32_16x16x32_bf16 v[48:51], a[68:71], v[24:27], v[48:51]\n"
  "s_waitcnt lgkmcnt(0)\n"
  "v_mfma_f32_16x16x32_bf16 v[52:55], a[68:71], v[28:31], v[52:55]\n"
  "v_mfma_f32_16x16x32_bf16 v[56:59], a[140:143], v[24:27], v[56:59]\n"
  "v_mfma_f32_16x16x32_bf16 v[60:63], a[140:143], v[28:31], v[60:63]\n"
  "v_cvt_pk_bf16_f32 v48, v48, v49\n"
  "v_cvt_pk_bf16_f32 v49, v50, v51\n"
  "v_cvt_pk_bf16_f32 v52, v52, v53\n"
  "v_cvt_pk_bf16_f32 v53, v54, v55\n"
  "s_nop 0\n"
  "v_cvt_pk_bf16_f32 v56, v56, v57\n"
  "v_cvt_pk_bf16_f32 v57, v58, v59\n"
  "s_nop 0\n"
  "v_cvt_pk_bf16_f32 v60, v60, v61\n"
  "v_cvt_pk_bf16_f32 v61, v62, v63\n"
  "ds_write_b64 v10, v[48:49] offset:0\n"
  "ds_write_b64 v10, v[52:53] offset:4864\n"
  "ds_write_b64 v11, v[56:57] offset:0\n"
  "ds_write_b64 v11, v[60:61] offset:4864\n"
  "s_waitcnt vmcnt(0)\n"
  "s_add_u32 s56, s56, 1\n"
  "s_waitcnt vmcnt(2) lgkmcnt(0)\n"
  "s_barrier\n"
  "v_add_u32 v64, 59136, v2\n"
  "v_xor_b32 v65, 64, v64\n"
  "s_add_u32 s58, s80, 22272\n"
  "s_cmp_lt_i32 s56, 62\n"
  "s_cselect_b32 s74, 36864, 0\n"
  "s_add_u32 s38, s38, s74\n"
  "s_addc_u32 s39, s39, 0\n"
  "ds_read_b128 v[16:19], v64 offset:0\n"
  "ds_read_b128 v[20:23], v64 offset:2048\n"
  "ds_read_b128 v[24:27], v65 offset:0\n"
  "ds_read_b128 v[28:31], v65 offset:2048\n"
  "ds_read_b128 v[32:35], v64 offset:4096\n"
  "ds_read_b128 v[36:39], v64 offset:6144\n"
  "s_waitcnt lgkmcnt(5)\n"
  "v_mfma_f32_16x16x32_bf16 v[48:51], a[0:3], v[16:19], 0\n"
  "ds_read_b128 v[40:43], v65 offset:4096\n"
  "ds_read_b128 v[76:79], v66\n"
  "s_nop 0\n"
  "s_waitcnt lgkmcnt(6)\n"
  "v_mfma_f32_16x16x32_bf16 v[52:55], a[0:3], v[20:23], 0\n"
  "ds_read_b128 v[44:47], v65 offset:6144\n"
  "ds_read_b128 v[80:83], v68\n"
  "s_nop 0\n"
  "v_mfma_f32_16x16x32_bf16 v[56:59], a[72:75], v[16:19], 0\n"
  "ds_read_b128 v[84:87], v69\n"
  "s_nop 0\n"
  "v_mfma_f32_16x16x32_bf16 v[60:63], a[72:75], v[20:23], 0\n"
  "ds_read_b64_tr_b16 v[88:89], v72 offset:0\n"
  "s_nop 0\n"
  "s_waitcnt lgkmcnt(9)\n"
  "v_mfma_f32_16x16x32_bf16 v[48:51], a[4:7], v[24:27], v[48:51]\n"
  "ds_read_b128 v[16:19], v64 offset:8192\n"
  "ds_read_b64_tr_b16 v[90:91], v73 offset:0\n"
  "s_nop 0\n"
  "s_waitcnt lgkmcnt(10)\n"
  "v_mfma_f32_16x16x32_bf16 v[52:55], a[4:7], v[28:31], v[52:55]\n"
  "ds_read_b128 v[20:23], v64 offset:10240\n"
  "ds_read_b64_tr_b16 v[92:93], v72 offset:32\n"
  "s_nop 0\n"
  "v_mfma_f32_16x16x32_bf16 v[56:59], a[76:79], v[24:27], v[56:59]\n"
  "s_add_u32 m0, s58, 0\n"
  "s_nop 0\n"
  "global_load_lds_dwordx4 v1, s[38:39] offset:0\n"
  "v_mfma_f32_16x16x32_bf16 v[60:63], a[76:79], v[28:31], v[60:63]\n"
  "ds_read_b64_tr_b16 v[94:95], v73 offset:32\n"
  "s_nop 0\n"
  "s_waitcnt lgkmcnt(12)\n"
  "v_mfma_f32_16x16x32_bf16 v[48:51], a[8:11], v[32:35], v[48:51]\n"
  "ds_read_b128 v[24:27], v65 offset:8192\n"
  "ds_read_b32 v126, v6 offset:0\n"
  "s_nop 0\n"
  "s_waitcnt lgkmcnt(13)\n"
  "v_mfma_f32_16x16x32_bf16 v[52:55], a[8:11], v[36:39], v[52:55]\n"
  "ds_read_b128 v[28:31], v65 offset:10240\n"
  "s_waitcnt lgkmcnt(10)\n"
  "v_mfma_f32_16x16x32_bf16 v[96:99], v[80:83], v[76:79], 0\n"
  "v_mfma_f32_16x16x32_bf16 v[56:59], a[80:83], v[32:35], v[56:59]\n"
  "s_add_u32 m0, s58, 3968\n"
  "s_nop 0\n"
  "global_load_lds_dwordx4 v1, s[38:39] offset:128\n"
  "v_mfma_f32_16x16x32_bf16 v[60:63], a[80:83], v[36:39], v[60:63]\n"
  "s_waitcnt lgkmcnt(9)\n"
  "v_mfma_f32_16x16x32_bf16 v[100:103], v[84:87], v[76:79], 0\n"
  "v_mfma_f32_16x16x32_bf16 v[48:51], a[12:15], v[40:43], v[48:51]\n"
  "ds_read_b128 v[32:35], v64 offset:12288\n"
  "s_waitcnt lgkmcnt(2)\n"
  "v_lshlrev_b32 v126, 16, v126\n"
  "v_mfma_f32_16x16x32_bf16 v[52:55], a[12:15], v[44:47], v[52:55]\n"
  "ds_read_b128 v[36:39], v64 offset:14336\n"
  "v_mul_f32 v126, 0xbfb8aa3b, v126\n"
  "v_exp_f32 v126, v126\n"
  "v_mfma_f32_16x16x32_bf16 v[56:59], a[84:87], v[40:43], v[56:59]\n"
  "s_add_u32 m0, s58, 7936\n"
  "s_nop 0\n"
  "global_load_lds_dwordx4 v1, s[38:39] offset:256\n"
  "v_mfma_f32_16x16x32_bf16 v[60:63], a[84:87], v[44:47], v[60:63]\n"
  "v_add_f32 v126, 1.0, v126\n"
  "v_rcp_f32 v126, v126\n"
  "v_mfma_f32_16x16x32_bf16 v[48:51], a[16:19], v[16:19], v[48:51]\n"
  "ds_read_b128 v[40:43], v65 offset:12288\n"
  "v_cvt_pk_bf16_f32 v126, v126, v126\n"
  "v_lshlrev_b32 v126, 16, v126\n"
  "v_mfma_f32_16x16x32_bf16 v[52:55], a[16:19], v[20:23], v[52:55]\n"
  "ds_read_b128 v[44:47], v65 offset:14336\n"
  "v_cndmask_b32 v104, v96, v97, s[70:71]\n"
  "v_cndmask_b32 v105, v98, v99, s[70:71]\n"
  "v_mfma_f32_16x16x32_bf16 v[56:59], a[88:91], v[16:19], v[56:59]\n"
  "s_add_u32 m0, s58, 11904\n"
  "s_nop 0\n"
  "global_load_lds_dwordx4 v1, s[38:39] offset:384\n"
  "v_mfma_f32_16x16x32_bf16 v[60:63], a[88:91], v[20:23], v[60:63]\n"
  "v_cndmask_b32 v106, v100, v101, s[70:71]\n"
  "v_cndmask_b32 v107, v102, v103, s[70:71]\n"
  "v_mfma_f32_16x16x32_bf16 v[48:51], a[20:23], v[24:27], v[48:51]\n"
  "ds_read_b128 v[16:19], v64 offset:16384\n"
  "v_cvt_pk_bf16_f32 v104, v104, v105\n"
  "v_and_b32 v105, 0xffff0000, v104\n"
  "s_waitcnt lgkmcnt(5)\n"
  "v_mfma_f32_16x16x32_bf16 v[52:55], a[20:23], v[28:31], v[52:55]\n"
  "ds_read_b128 v[20:23], v64 offset:18432\n"
  "v_lshlrev_b32 v104, 16, v104\n"
  "v_cvt_pk_bf16_f32 v106, v106, v107\n"
  "v_mfma_f32_16x16x32_bf16 v[56:59], a[92:95], v[24:27], v[56:59]\n"
  "s_add_u32 m0, s58, 15872\n"
  "s_nop 0\n"
  "global_load_lds_dwordx4 v1, s[38:39] offset:512\n"
  "v_mfma_f32_16x16x32_bf16 v[60:63], a[92:95], v[28:31], v[60:63]\n"
  "v_and_b32 v107, 0xffff0000, v106\n"
  "v_lshlrev_b32 v106, 16, v106\n"
  "s_waitcnt lgkmcnt(5)\n"
  "v_mfma_f32_16x16x32_bf16 v[48:51], a[24:27], v[32:35], v[48:51]\n"
  "ds_read_b128 v[24:27], v65 offset:16384\n"
  "v_mul_f32 v104, 0x3e715bef, v104\n"
  "v_mul_f32 v105, 0x3e715bef, v105\n"
  "s_waitcnt lgkmcnt(5)\n"
  "v_mfma_f32_16x16x32_bf16 v[52:55], a[24:27], v[36:39], v[52:55]\n"
  "ds_read_b128 v[28:31], v65 offset:18432\n"
  "v_mul_f32 v106, 0x3e715bef, v106\n"
  "v_mul_f32 v107, 0x3e715bef, v107\n"
  "v_mfma_f32_16x16x32_bf16 v[56:59], a[96:99], v[32:35], v[56:59]\n"
  "s_add_u32 m0, s58, 19840\n"
  "s_nop 0\n"
  "global_load_lds_dwordx4 v1, s[38:39] offset:640\n"
  "v_mfma_f32_16x16x32_bf16 v[60:63], a[96:99], v[36:39], v[60:63]\n"
  "v_cvt_pk_bf16_f32 v104, v104, v105\n"
  "v_and_b32 v105, 0xffff0000, v104\n"
  "s_waitcnt lgkmcnt(5)\n"
  "v_mfma_f32_16x16x32_bf16 v[48:51], a[28:31], v[40:43], v[48:51]\n"
  "ds_read_b128 v[32:35], v64 offset:20480\n"
  "v_lshlrev_b32 v104, 16, v104\n"
  "v_cvt_pk_bf16_f32 v106, v106, v107\n"
  "s_waitcnt lgkmcnt(5)\n"
  "v_mfma_f32_16x16x32_bf16 v[52:55], a[28:31], v[44:47], v[52:55]\n"
  "ds_read_b128 v[36:39], v64 offset:22528\n"
  "v_and_b32 v107, 0xffff0000, v106\n"
  "v_lshlrev_b32 v106, 16, v106\n"
  "v_mfma_f32_16x16x32_bf16 v[56:59], a[100:103], v[40:43], v[56:59]\n"
  "s_add_u32 m0, s58, 23808\n"
  "s_nop 0\n"
  "global_load_lds_dwordx4 v1, s[38:39] offset:768\n"
  "v_mfma_f32_16x16x32_bf16 v[60:63], a[100:103], v[44:47], v[60:63]\n"
  "v_cndmask_b32 v104, v128, v104, s[8:9]\n"
  "v_cndmask_b32 v105, v128, v105, s[10:11]\n"
  "s_waitcnt lgkmcnt(5)\n"
  "v_mfma_f32_16x16x32_bf16 v[48:51], a[32:35], v[16:19], v[48:51]\n"
  "ds_read_b128 v[40:43], v65 offset:20480\n"
  "v_cndmask_b32 v106, v128, v106, s[12:13]\n"
  "v_cndmask_b32 v107, v128, v107, s[14:15]\n"
  "s_waitcnt lgkmcnt(5)\n"
  "v_mfma_f32_16x16x32_bf16 v[52:55], a[32:35], v[20:23], v[52:55]\n"
  "ds_read_b128 v[44:47], v65 offset:22528\n"
  "v_max3_f32 v124, v104, v105, v106\n"
  "v_max_f32 v124, v124, v107\n"
  "v_mfma_f32_16x16x32_bf16 v[56:59], a[104:107], v[16:19], v[56:59]\n"
  "s_add_u32 m0, s58, 27776\n"
  "s_nop 0\n"
  "global_load_lds_dwordx4 v1, s[38:39] offset:896\n"
  "v_mfma_f32_16x16x32_bf16 v[60:63], a[104:107], v[20:23], v[60:63]\n"
  "v_mov_b32 v125, v124\n"
  "s_nop 1\n"
  "v_permlane16_swap_b32 v124, v125\n"
  "v_max_f32 v124, v124, v125\n"
  "s_waitcnt lgkmcnt(5)\n"
  "v_mfma_f32_16x16x32_bf16 v[48:51], a[36:39], v[24:27], v[48:51]\n"
  "ds_read_b128 v[16:19], v64 offset:24576\n"
  "v_mov_b32 v125, v124\n"
  "s_nop 1\n"
  "v_permlane32_swap_b32 v124, v125\n"
  "v_max_f32 v124, v124, v125\n"
  "s_waitcnt lgkmcnt(5)\n"
  "v_mfma_f32_16x16x32_bf16 v[52:55], a[36:39], v[28:31], v[52:55]\n"
  "ds_read_b128 v[20:23], v64 offset:26624\n"
  "v_sub_f32 v104, v104, v124\n"
  "v_sub_f32 v105, v105, v124\n"
  "v_mfma_f32_16x16x32_bf16 v[56:59], a[108:111], v[24:27], v[56:59]\n"
  "s_add_u32 m0, s58, 31744\n"
  "s_nop 0\n"
  "global_load_lds_dwordx4 v1, s[38:39] offset:1024\n"
  "v_mfma_f32_16x16x32_bf16 v[60:63], a[108:111], v[28:31], v[60:63]\n"
  "v_sub_f32 v106, v106, v124\n"
  "v_sub_f32 v107, v107, v124\n"
  "s_waitcnt lgkmcnt(5)\n"
  "v_mfma_f32_16x16x32_bf16 v[48:51], a[40:43], v[32:35], v[48:51]\n"
  "ds_read_b128 v[24:27], v65 offset:24576\n"
  "v_mul_f32 v104, 0x3fb8aa3b, v104\n"
  "v_mul_f32 v105, 0x3fb8aa3b, v105\n"
  "s_waitcnt lgkmcnt(5)\n"
  "v_mfma_f32_16x16x32_bf16 v[52:55], a[40:43], v[36:39], v[52:55]\n"
  "ds_read_b128 v[28:31], v65 offset:26624\n"
  "v_mul_f32 v106, 0x3fb8aa3b, v106\n"
  "v_mul_f32 v107, 0x3fb8aa3b, v107\n"
  "v_mfma_f32_16x16x32_bf16 v[56:59], a[112:115], v[32:35], v[56:59]\n"
  "v_exp_f32 v104, v104\n"
  "v_mfma_f32_16x16x32_bf16 v[60:63], a[112:115], v[36:39], v[60:63]\n"
  "v_exp_f32 v105, v105\n"
  "s_waitcnt lgkmcnt(5)\n"
  "v_mfma_f32_16x16x32_bf16 v[48:51], a[44:47], v[40:43], v[48:51]\n"
  "ds_read_b128 v[32:35], v64 offset:28672\n"
  "v_exp_f32 v106, v106\n"
  "s_waitcnt lgkmcnt(5)\n"
  "v_mfma_f32_16x16x32_bf16 v[52:55], a[44:47], v[44:47], v[52:55]\n"
  "ds_read_b128 v[36:39], v64 offset:30720\n"
  "v_exp_f32 v107, v107\n"
  "v_mfma_f32_16x16x32_bf16 v[56:59], a[116:119], v[40:43], v[56:59]\n"
  "s_mov_b64 s[8:9], s[16:17]\n"
  "s_mov_b64 s[10:11], s[18:19]\n"
  "v_mfma_f32_16x16x32_bf16 v[60:63], a[116:119], v[44:47], v[60:63]\n"
  "v_add_f32 v124, v104, v105\n"
  "v_add_f32 v124, v124, v106\n"
  "s_waitcnt lgkmcnt(5)\n"
  "v_mfma_f32_16x16x32_bf16 v[48:51], a[48:51], v[16:19], v[48:51]\n"
  "ds_read_b128 v[40:43], v65 offset:28672\n"
  "v_add_f32 v124, v124, v107\n"
  "v_mov_b32 v125, v124\n"
  "s_nop 1\n"
  "v_permlane16_swap_b32 v124, v125\n"
  "v_add_f32 v124, v124, v125\n"
  "s_waitcnt lgkmcnt(5)\n"
  "v_mfma_f32_16x16x32_bf16 v[52:55], a[48:51], v[20:23], v[52:55]\n"
  "ds_read_b128 v[44:47], v65 offset:30720\n"
  "v_mov_b32 v125, v124\n"
  "s_nop 1\n"
  "v_permlane32_swap_b32 v124, v125\n"
  "v_add_f32 v124, v124, v125\n"
  "v_mfma_f32_16x16x32_bf16 v[56:59], a[120:123], v[16:19], v[56:59]\n"
  "v_rcp_f32 v124, v124\n"
  "v_mfma_f32_16x16x32_bf16 v[60:63], a[120:123], v[20:23], v[60:63]\n"
  "v_mul_f32 v104, v124, v104\n"
  "v_mul_f32 v105, v124, v105\n"
  "s_waitcnt lgkmcnt(5)\n"
  "v_mfma_f32_16x16x32_bf16 v[48:51], a[52:55], v[24:27], v[48:51]\n"
  "ds_read_b128 v[16:19], v64 offset:32768\n"
  "v_mul_f32 v106, v124, v106\n"
  "v_mul_f32 v107, v124, v107\n"
  "s_waitcnt lgkmcnt(5)\n"
  "v_mfma_f32_16x16x32_bf16 v[52:55], a[52:55], v[28:31], v[52:55]\n"
  "ds_read_b128 v[20:23], v64 offset:34816\n"
  "v_cvt_pk_bf16_f32 v108, v104, v104\n"
  "v_cvt_pk_bf16_f32 v109, v105, v105\n"
  "v_mfma_f32_16x16x32_bf16 v[56:59], a[124:127], v[24:27], v[56:59]\n"
  "v_cvt_pk_bf16_f32 v110, v106, v106\n"
  "v_cvt_pk_bf16_f32 v111, v107, v107\n"
  "v_mfma_f32_16x16x32_bf16 v[60:63], a[124:127], v[28:31], v[60:63]\n"
  "v_and_b32 v108, v8, v108\n"
  "v_and_b32 v109, v8, v109\n"
  "s_waitcnt lgkmcnt(5)\n"
  "v_mfma_f32_16x16x32_bf16 v[48:51], a[56:59], v[32:35], v[48:51]\n"
  "ds_read_b128 v[24:27], v65 offset:32768\n"
  "v_and_b32 v110, v8, v110\n"
  "v_and_b32 v111, v8, v111\n"
  "s_waitcnt lgkmcnt(5)\n"
  "v_mfma_f32_16x16x32_bf16 v[52:55], a[56:59], v[36:39], v[52:55]\n"
  "ds_read_b128 v[28:31], v65 offset:34816\n"
  "v_mfma_f32_16x16x32_bf16 v[112:115], v[88:91], v[108:111], 0\n"
  "v_mfma_f32_16x16x32_bf16 v[56:59], a[128:131], v[32:35], v[56:59]\n"
  "s_waitcnt vmcnt(9)\n"
  "s_barrier\n"
  "v_mov_b32 v9, 0\n"
  "v_mov_b32 v15, s56\n"
  "s_cmp_eq_u32 s47, 0\n"
  "s_cselect_b64 exec, 1, 0\n"
  "global_store_dword v9, v15, s[64:65] sc1\n"
  "s_mov_b64 exec, -1\n"
  "v_mfma_f32_16x16x32_bf16 v[60:63], a[128:131], v[36:39], v[60:63]\n"
  "v_mfma_f32_16x16x32_bf16 v[116:119], v[92:95], v[108:111], 0\n"
  "s_waitcnt lgkmcnt(5)\n"
  "v_mfma_f32_16x16x32_bf16 v[48:51], a[60:63], v[40:43], v[48:51]\n"
  "s_waitcnt lgkmcnt(4)\n"
  "v_mfma_f32_16x16x32_bf16 v[52:55], a[60:63], v[44:47], v[52:55]\n"
  "v_mfma_f32_16x16x32_bf16 v[56:59], a[132:135], v[40:43], v[56:59]\n"
  "v_mfma_f32_16x16x32_bf16 v[60:63], a[132:135], v[44:47], v[60:63]\n"
  "s_waitcnt lgkmcnt(3)\n"
  "v_mfma_f32_16x16x32_bf16 v[48:51], a[64:67], v[16:19], v[48:51]\n"
  "s_waitcnt lgkmcnt(2)\n"
  "v_mfma_f32_16x16x32_bf16 v[52:55], a[64:67], v[20:23], v[52:55]\n"
  "v_mfma_f32_16x16x32_bf16 v[56:59], a[136:139], v[16:19], v[56:59]\n"
  "v_mfma_f32_16x16x32_bf16 v[60:63], a[136:139], v[20:23], v[60:63]\n"
  "s_waitcnt lgkmcnt(1)\n"
  "v_mfma_f32_16x16x32_bf16 v[48:51], a[68:71], v[24:27], v[48:51]\n"
  "s_waitcnt lgkmcnt(0)\n"
  "v_mfma_f32_16x16x32_bf16 v[52:55], a[68:71], v[28:31], v[52:55]\n"
  "v_mfma_f32_16x16x32_bf16 v[56:59], a[140:143], v[24:27], v[56:59]\n"
  "v_mfma_f32_16x16x32_bf16 v[60:63], a[140:143], v[28:31], v[60:63]\n"
  "v_cvt_pk_bf16_f32 v112, v112, v113\n"
  "v_cvt_pk_bf16_f32 v113, v114, v115\n"
  "v_cvt_pk_bf16_f32 v114, v116, v117\n"
  "v_cvt_pk_bf16_f32 v115, v118, v119\n"
  "ds_write_b64 v3, v[112:113]\n"
  "s_mov_b64 exec, 0xffff\n"
  "ds_write_b64 v3, v[114:115] offset:32\n"
  "s_mov_b64 exec, s[68:69]\n"
  "global_store_dword v127, v126, s[62:63] sc1\n"
  "s_mov_b64 exec, -1\n"
  "s_add_u32 s62, s62, 2048\n"
  "s_addc_u32 s63, s63, 0\n"
  "v_cvt_pk_bf16_f32 v48, v48, v49\n"
  "v_cvt_pk_bf16_f32 v49, v50, v51\n"
  "v_cvt_pk_bf16_f32 v52, v52, v53\n"
  "v_cvt_pk_bf16_f32 v53, v54, v55\n"
  "v_cvt_pk_bf16_f32 v56, v56, v57\n"
  "v_cvt_pk_bf16_f32 v57, v58, v59\n"
  "v_cvt_pk_bf16_f32 v60, v60, v61\n"
  "v_cvt_pk_bf16_f32 v61, v62, v63\n"
  "ds_write_b64 v10, v[48:49] offset:9728\n"
  "ds_write_b64 v10, v[52:53] offset:14592\n"
  "ds_write_b64 v11, v[56:57] offset:9728\n"
  "ds_write_b64 v11, v[60:61] offset:14592\n"
  "s_add_u32 s56, s56, 1\n"
  "ALOOP:\n"
  "s_waitcnt vmcnt(2) lgkmcnt(0)\n"
  "s_barrier\n"
  "v_add_u32 v64, 22272, v2\n"
  "v_xor_b32 v65, 64, v64\n"
  "s_add_u32 s58, s80, 59136\n"
  "s_cmp_lt_i32 s56, 62\n"
  "s_cselect_b32 s74, 36864, 0\n"
  "s_add_u32 s38, s38, s74\n"
  "s_addc_u32 s39, s39, 0\n"
  "ds_read_b128 v[120:123], v4\n"
  "ds_read_b128 v[16:19], v64 offset:0\n"
  "ds_read_b128 v[20:23], v64 offset:2048\n"
  "ds_read_b128 v[24:27], v65 offset:0\n"
  "ds_read_b128 v[28:31], v65 offset:2048\n"
  "ds_read_b128 v[32:35], v64 offset:4096\n"
  "ds_read_b128 v[36:39], v64 offset:6144\n"
  "s_waitcnt lgkmcnt(5)\n"
  "v_mfma_f32_16x16x32_bf16 v[48:51], a[0:3], v[16:19], 0\n"
  "ds_read_b128 v[40:43], v65 offset:4096\n"
  "ds_read_b128 v[76:79], v67\n"
  "s_nop 0\n"
  "s_waitcnt lgkmcnt(6)\n"
  "v_mfma_f32_16x16x32_bf16 v[52:55], a[0:3], v[20:23], 0\n"
  "ds_read_b128 v[44:47], v65 offset:6144\n"
  "ds_read_b128 v[80:83], v70\n"
  "s_nop 0\n"
  "v_mfma_f32_16x16x32_bf16 v[56:59], a[72:75], v[16:19], 0\n"
  "s_bfm_b64 exec, 40, 0\n"
  "global_store_dwordx4 v5, v[120:123], s[60:61] sc1\n"
  "s_mov_b64 exec, -1\n"
  "s_add_u32 s60, s60, 40960\n"
  "s_addc_u32 s61, s61, 0\n"
  "v_mfma_f32_16x16x32_bf16 v[60:63], a[72:75], v[20:23], 0\n"
  "ds_read_b128 v[84:87], v71\n"
  "s_nop 0\n"
  "s_waitcnt lgkmcnt(8)\n"
  "v_mfma_f32_16x16x32_bf16 v[48:51], a[4:7], v[24:27], v[48:51]\n"
  "ds_read_b128 v[16:19], v64 offset:8192\n"
  "ds_read_b64_tr_b16 v[88:89], v74 offset:0\n"
  "s_nop 0\n"
  "s_waitcnt lgkmcnt(9)\n"
  "v_mfma_f32_16x16x32_bf16 v[52:55], a[4:7], v[28:31], v[52:55]\n"
  "ds_read_b128 v[20:23], v64 offset:10240\n"
  "ds_read_b64_tr_b16 v[90:91], v75 offset:0\n"
  "s_nop 0\n"
  "v_mfma_f32_16x16x32_bf16 v[56:59], a[76:79], v[24:27], v[56:59]\n"
  "s_add_u32 m0, s58, 0\n"
  "s_nop 0\n"
  "global_load_lds_dwordx4 v1, s[38:39] offset:0\n"
  "v_mfma_f32_16x16x32_bf16 v[60:63], a[76:79], v[28:31], v[60:63]\n"
  "ds_read_b64_tr_b16 v[92:93], v74 offset:32\n"
  "s_nop 0\n"
  "s_waitcnt lgkmcnt(11)\n"
  "v_mfma_f32_16x16x32_bf16 v[48:51], a[8:11], v[32:35], v[48:51]\n"
  "ds_read_b128 v[24:27], v65 offset:8192\n"
  "ds_read_b64_tr_b16 v[94:95], v75 offset:32\n"
  "s_nop 0\n"
  "s_waitcnt lgkmcnt(12)\n"
  "v_mfma_f32_16x16x32_bf16 v[52:55], a[8:11], v[36:39], v[52:55]\n"
  "ds_read_b128 v[28:31], v65 offset:10240\n"
  "ds_read_b32 v126, v6 offset:9728\n"
  "s_nop 0\n"
  "v_mfma_f32_16x16x32_bf16 v[56:59], a[80:83], v[32:35], v[56:59]\n"
  "s_add_u32 m0, s58, 3968\n"
  "s_nop 0\n"
  "global_load_lds_dwordx4 v1, s[38:39] offset:128\n"
  "v_mfma_f32_16x16x32_bf16 v[60:63], a[80:83], v[36:39], v[60:63]\n"
  "s_waitcnt lgkmcnt(10)\n"
  "v_mfma_f32_16x16x32_bf16 v[96:99], v[80:83], v[76:79], 0\n"
  "v_mfma_f32_16x16x32_bf16 v[48:51], a[12:15], v[40:43], v[48:51]\n"
  "ds_read_b128 v[32:35], v64 offset:12288\n"
  "s_waitcnt lgkmcnt(10)\n"
  "v_mfma_f32_16x16x32_bf16 v[100:103], v[84:87], v[76:79], 0\n"
  "v_mfma_f32_16x16x32_bf16 v[52:55], a[12:15], v[44:47], v[52:55]\n"
  "ds_read_b128 v[36:39], v64 offset:14336\n"
  "s_waitcnt lgkmcnt(2)\n"
  "v_lshlrev_b32 v126, 16, v126\n"
  "v_mfma_f32_16x16x32_bf16 v[56:59], a[84:87], v[40:43], v[56:59]\n"
  "s_add_u32 m0, s58, 7936\n"
  "s_nop 0\n"
  "global_load_lds_dwordx4 v1, s[38:39] offset:256\n"
  "v_mfma_f32_16x16x32_bf16 v[60:63], a[84:87], v[44:47], v[60:63]\n"
  "v_mul_f32 v126, 0xbfb8aa3b, v126\n"
  "v_exp_f32 v126, v126\n"
  "v_mfma_f32_16x16x32_bf16 v[48:51], a[16:19], v[16:19], v[48:51]\n"
  "ds_read_b128 v[40:43], v65 offset:12288\n"
  "v_add_f32 v126, 1.0, v126\n"
  "v_rcp_f32 v126, v126\n"
  "v_mfma_f32_16x16x32_bf16 v[52:55], a[16:19], v[20:23], v[52:55]\n"
  "ds_read_b128 v[44:47], v65 offset:14336\n"
  "v_cvt_pk_bf16_f32 v126, v126, v126\n"
  "v_lshlrev_b32 v126, 16, v126\n"
  "v_mfma_f32_16x16x32_bf16 v[56:59], a[88:91], v[16:19], v[56:59]\n"
  "s_add_u32 m0, s58, 11904\n"
  "s_nop 0\n"
  "global_load_lds_dwordx4 v1, s[38:39] offset:384\n"
  "v_mfma_f32_16x16x32_bf16 v[60:63], a[88:91], v[20:23], v[60:63]\n"
  "v_cndmask_b32 v104, v96, v97, s[70:71]\n"
  "v_cndmask_b32 v105, v98, v99, s[70:71]\n"
  "v_mfma_f32_16x16x32_bf16 v[48:51], a[20:23], v[24:27], v[48:51]\n"
  "ds_read_b128 v[16:19], v64 offset:16384\n"
  "v_cndmask_b32 v106, v100, v101, s[70:71]\n"
  "v_cndmask_b32 v107, v102, v103, s[70:71]\n"
  "v_mfma_f32_16x16x32_bf16 v[52:55], a[20:23], v[28:31], v[52:55]\n"
  "ds_read_b128 v[20:23], v64 offset:18432\n"
  "v_cvt_pk_bf16_f32 v104, v104, v105\n"
  "v_and_b32 v105, 0xffff0000, v104\n"
  "v_mfma_f32_16x16x32_bf16 v[56:59], a[92:95], v[24:27], v[56:59]\n"
  "s_add_u32 m0, s58, 15872\n"
  "s_nop 0\n"
  "global_load_lds_dwordx4 v1, s[38:39] offset:512\n"
  "v_mfma_f32_16x16x32_bf16 v[60:63], a[92:95], v[28:31], v[60:63]\n"
  "v_lshlrev_b32 v104, 16, v104\n"
  "v_cvt_pk_bf16_f32 v106, v106, v107\n"
  "s_waitcnt lgkmcnt(5)\n"
  "v_mfma_f32_16x16x32_bf16 v[48:51], a[24:27], v[32:35], v[48:51]\n"
  "ds_read_b128 v[24:27], v65 offset:16384\n"
  "v_and_b32 v107, 0xffff0000, v106\n"
  "v_lshlrev_b32 v106, 16, v106\n"
  "s_waitcnt lgkmcnt(5)\n"
  "v_mfma_f32_16x16x32_bf16 v[52:55], a[24:27], v[36:39], v[52:55]\n"
  "ds_read_b128 v[28:31], v65 offset:18432\n"
  "v_mul_f32 v104, 0x3e715bef, v104\n"
  "v_mul_f32 v105, 0x3e715bef, v105\n"
  "v_mfma_f32_16x16x32_bf16 v[56:59], a[96:99], v[32:35], v[56:59]\n"
  "s_add_u32 m0, s58, 19840\n"
  "s_nop 0\n"
  "global_load_lds_dwordx4 v1, s[38:39] offset:640\n"
  "v_mfma_f32_16x16x32_bf16 v[60:63], a[96:99], v[36:39], v[60:63]\n"
  "v_mul_f32 v106, 0x3e715bef, v106\n"
  "v_mul_f32 v107, 0x3e715bef, v107\n"
  "s_waitcnt lgkmcnt(5)\n"
  "v_mfma_f32_16x16x32_bf16 v[48:51], a[28:31], v[40:43], v[48:51]\n"
  "ds_read_b128 v[32:35], v64 offset:20480\n"
  "v_cvt_pk_bf16_f32 v104, v104, v105\n"
  "v_and_b32 v105, 0xffff0000, v104\n"
  "s_waitcnt lgkmcnt(5)\n"
  "v_mfma_f32_16x16x32_bf16 v[52:55], a[28:31], v[44:47], v[52:55]\n"
  "ds_read_b128 v[36:39], v64 offset:22528\n"
  "v_lshlrev_b32 v104, 16, v104\n"
  "v_cvt_pk_bf16_f32 v106, v106, v107\n"
  "v_mfma_f32_16x16x32_bf16 v[56:59], a[100:103], v[40:43], v[56:59]\n"
  "s_add_u32 m0, s58, 23808\n"
  "s_nop 0\n"
  "global_load_lds_dwordx4 v1, s[38:39] offset:768\n"
  "v_mfma_f32_16x16x32_bf16 v[60:63], a[100:103], v[44:47], v[60:63]\n"
  "v_and_b32 v107, 0xffff0000, v106\n"
  "v_lshlrev_b32 v106, 16, v106\n"
  "s_waitcnt lgkmcnt(5)\n"
  "v_mfma_f32_16x16x32_bf16 v[48:51], a[32:35], v[16:19], v[48:51]\n"
  "ds_read_b128 v[40:43], v65 offset:20480\n"
  "v_cndmask_b32 v104, v128, v104, s[8:9]\n"
  "v_cndmask_b32 v105, v128, v105, s[10:11]\n"
  "s_waitcnt lgkmcnt(5)\n"
  "v_mfma_f32_16x16x32_bf16 v[52:55], a[32:35], v[20:23], v[52:55]\n"
  "ds_read_b128 v[44:47], v65 offset:22528\n"
  "v_cndmask_b32 v106, v128, v106, s[12:13]\n"
  "v_cndmask_b32 v107, v128, v107, s[14:15]\n"
  "v_mfma_f32_16x16x32_bf16 v[56:59], a[104:107], v[16:19], v[56:59]\n"
  "s_add_u32 m0, s58, 27776\n"
  "s_nop 0\n"
  "global_load_lds_dwordx4 v1, s[38:39] offset:896\n"
  "v_mfma_f32_16x16x32_bf16 v[60:63], a[104:107], v[20:23], v[60:63]\n"
  "v_max3_f32 v124, v104, v105, v106\n"
  "v_max_f32 v124, v124, v107\n"
  "s_waitcnt lgkmcnt(5)\n"
  "v_mfma_f32_16x16x32_bf16 v[48:51], a[36:39], v[24:27], v[48:51]\n"
  "ds_read_b128 v[16:19], v64 offset:24576\n"
  "v_mov_b32 v125, v124\n"
  "s_nop 1\n"
  "v_permlane16_swap_b32 v124, v125\n"
  "v_max_f32 v124, v124, v125\n"
  "s_waitcnt lgkmcnt(5)\n"
  "v_mfma_f32_16x16x32_bf16 v[52:55], a[36:39], v[28:31], v[52:55]\n"
  "ds_read_b128 v[20:23], v64 offset:26624\n"
  "v_mov_b32 v125, v124\n"
  "s_nop 1\n"
  "v_permlane32_swap_b32 v124, v125\n"
  "v_max_f32 v124, v124, v125\n"
  "v_mfma_f32_16x16x32_bf16 v[56:59], a[108:111], v[24:27], v[56:59]\n"
  "s_add_u32 m0, s58, 31744\n"
  "s_nop 0\n"
  "global_load_lds_dwordx4 v1, s[38:39] offset:1024\n"
  "v_mfma_f32_16x16x32_bf16 v[60:63], a[108:111], v[28:31], v[60:63]\n"
  "v_sub_f32 v104, v104, v124\n"
  "v_sub_f32 v105, v105, v124\n"
  "s_waitcnt lgkmcnt(5)\n"
  "v_mfma_f32_16x16x32_bf16 v[48:51], a[40:43], v[32:35], v[48:51]\n"
  "ds_read_b128 v[24:27], v65 offset:24576\n"
  "v_sub_f32 v106, v106, v124\n"
  "v_sub_f32 v107, v107, v124\n"
  "s_waitcnt lgkmcnt(5)\n"
  "v_mfma_f32_16x16x32_bf16 v[52:55], a[40:43], v[36:39], v[52:55]\n"
  "ds_read_b128 v[28:31], v65 offset:26624\n"
  "v_mul_f32 v104, 0x3fb8aa3b, v104\n"
  "v_mul_f32 v105, 0x3fb8aa3b, v105\n"
  "v_mfma_f32_16x16x32_bf16 v[56:59], a[112:115], v[32:35], v[56:59]\n"
  "v_mul_f32 v106, 0x3fb8aa3b, v106\n"
  "v_mul_f32 v107, 0x3fb8aa3b, v107\n"
  "v_mfma_f32_16x16x32_bf16 v[60:63], a[112:115], v[36:39], v[60:63]\n"
  "v_exp_f32 v104, v104\n"
  "s_waitcnt lgkmcnt(5)\n"
  "v_mfma_f32_16x16x32_bf16 v[48:51], a[44:47], v[40:43], v[48:51]\n"
  "ds_read_b128 v[32:35], v64 offset:28672\n"
  "v_exp_f32 v105, v105\n"
  "s_waitcnt lgkmcnt(5)\n"
  "v_mfma_f32_16x16x32_bf16 v[52:55], a[44:47], v[44:47], v[52:55]\n"
  "ds_read_b128 v[36:39], v64 offset:30720\n"
  "v_exp_f32 v106, v106\n"
  "v_mfma_f32_16x16x32_bf16 v[56:59], a[116:119], v[40:43], v[56:59]\n"
  "v_exp_f32 v107, v107\n"
  "v_mfma_f32_16x16x32_bf16 v[60:63], a[116:119], v[44:47], v[60:63]\n"
  "s_mov_b64 s[8:9], s[16:17]\n"
  "s_mov_b64 s[10:11], s[18:19]\n"
  "s_waitcnt lgkmcnt(5)\n"
  "v_mfma_f32_16x16x32_bf16 v[48:51], a[48:51], v[16:19], v[48:51]\n"
  "ds_read_b128 v[40:43], v65 offset:28672\n"
  "v_add_f32 v124, v104, v105\n"
  "v_add_f32 v124, v124, v106\n"
  "s_waitcnt lgkmcnt(5)\n"
  "v_mfma_f32_16x16x32_bf16 v[52:55], a[48:51], v[20:23], v[52:55]\n"
  "ds_read_b128 v[44:47], v65 offset:30720\n"
  "v_add_f32 v124, v124, v107\n"
  "v_mov_b32 v125, v124\n"
  "s_nop 1\n"
  "v_permlane16_swap_b32 v124, v125\n"
  "v_add_f32 v124, v124, v125\n"
  "v_mfma_f32_16x16x32_bf16 v[56:59], a[120:123], v[16:19], v[56:59]\n"
  "v_mov_b32 v125, v124\n"
  "s_nop 1\n"
  "v_permlane32_swap_b32 v124, v125\n"
  "v_add_f32 v124, v124, v125\n"
  "v_mfma_f32_16x16x32_bf16 v[60:63], a[120:123], v[20:23], v[60:63]\n"
  "v_rcp_f32 v124, v124\n"
  "s_waitcnt lgkmcnt(5)\n"
  "v_mfma_f32_16x16x32_bf16 v[48:51], a[52:55], v[24:27], v[48:51]\n"
  "ds_read_b128 v[16:19], v64 offset:32768\n"
  "v_mul_f32 v104, v124, v104\n"
  "v_mul_f32 v105, v124, v105\n"
  "s_waitcnt lgkmcnt(5)\n"
  "v_mfma_f32_16x16x32_bf16 v[52:55], a[52:55], v[28:31], v[52:55]\n"
  "ds_read_b128 v[20:23], v64 offset:34816\n"
  "v_mul_f32 v106, v124, v106\n"
  "v_mul_f32 v107, v124, v107\n"
  "v_mfma_f32_16x16x32_bf16 v[56:59], a[124:127], v[24:27], v[56:59]\n"
  "v_cvt_pk_bf16_f32 v108, v104, v104\n"
  "v_cvt_pk_bf16_f32 v109, v105, v105\n"
  "v_mfma_f32_16x16x32_bf16 v[60:63], a[124:127], v[28:31], v[60:63]\n"
  "v_cvt_pk_bf16_f32 v110, v106, v106\n"
  "v_cvt_pk_bf16_f32 v111, v107, v107\n"
  "s_waitcnt lgkmcnt(5)\n"
  "v_mfma_f32_16x16x32_bf16 v[48:51], a[56:59], v[32:35], v[48:51]\n"
  "ds_read_b128 v[24:27], v65 offset:32768\n"
  "v_and_b32 v108, v8, v108\n"
  "v_and_b32 v109, v8, v109\n"
  "s_waitcnt lgkmcnt(5)\n"
  "v_mfma_f32_16x16x32_bf16 v[52:55], a[56:59], v[36:39], v[52:55]\n"
  "ds_read_b128 v[28:31], v65 offset:34816\n"
  "v_and_b32 v110, v8, v110\n"
  "v_and_b32 v111, v8, v111\n"
  "v_mfma_f32_16x16x32_bf16 v[56:59], a[128:131], v[32:35], v[56:59]\n"
  "s_waitcnt vmcnt(9)\n"
  "s_barrier\n"
  "v_mov_b32 v9, 0\n"
  "v_mov_b32 v15, s56\n"
  "s_cmp_eq_u32 s47, 0\n"
  "s_cselect_b64 exec, 1, 0\n"
  "global_store_dword v9, v15, s[64:65] sc1\n"
  "s_mov_b64 exec, -1\n"
  "v_mfma_f32_16x16x32_bf16 v[60:63], a[128:131], v[36:39], v[60:63]\n"
  "v_mfma_f32_16x16x32_bf16 v[112:115], v[88:91], v[108:111], 0\n"
  "s_waitcnt lgkmcnt(5)\n"
  "v_mfma_f32_16x16x32_bf16 v[48:51], a[60:63], v[40:43], v[48:51]\n"
  "v_mfma_f32_16x16x32_bf16 v[116:119], v[92:95], v[108:111], 0\n"
  "s_waitcnt lgkmcnt(4)\n"
  "v_mfma_f32_16x16x32_bf16 v[52:55], a[60:63], v[44:47], v[52:55]\n"
  "v_mfma_f32_16x16x32_bf16 v[56:59], a[132:135], v[40:43], v[56:59]\n"
  "v_mfma_f32_16x16x32_bf16 v[60:63], a[132:135], v[44:47], v[60:63]\n"
  "s_waitcnt lgkmcnt(3)\n"
  "v_mfma_f32_16x16x32_bf16 v[48:51], a[64:67], v[16:19], v[48:51]\n"
  "s_waitcnt lgkmcnt(2)\n"
  "v_mfma_f32_16x16x32_bf16 v[52:55], a[64:67], v[20:23], v[52:55]\n"
  "v_mfma_f32_16x16x32_bf16 v[56:59], a[136:139], v[16:19], v[56:59]\n"
  "v_mfma_f32_16x16x32_bf16 v[60:63], a[136:139], v[20:23], v[60:63]\n"
  "s_waitcnt lgkmcnt(1)\n"
  "v_mfma_f32_16x16x32_bf16 v[48:51], a[68:71], v[24:27], v[48:51]\n"
  "s_waitcnt lgkmcnt(0)\n"
  "v_mfma_f32_16x16x32_bf16 v[52:55], a[68:71], v[28:31], v[52:55]\n"
  "v_mfma_f32_16x16x32_bf16 v[56:59], a[140:143], v[24:27], v[56:59]\n"
  "v_mfma_f32_16x16x32_bf16 v[60:63], a[140:143], v[28:31], v[60:63]\n"
  "v_cvt_pk_bf16_f32 v112, v112, v113\n"
  "v_cvt_pk_bf16_f32 v113, v114, v115\n"
  "v_cvt_pk_bf16_f32 v114, v116, v117\n"
  "v_cvt_pk_bf16_f32 v115, v118, v119\n"
  "ds_write_b64 v3, v[112:113]\n"
  "s_mov_b64 exec, 0xffff\n"
  "ds_write_b64 v3, v[114:115] offset:32\n"
  "s_mov_b64 exec, s[68:69]\n"
  "global_store_dword v127, v126, s[62:63] sc1\n"
  "s_mov_b64 exec, -1\n"
  "s_add_u32 s62, s62, 2048\n"
  "s_addc_u32 s63, s63, 0\n"
  "v_cvt_pk_bf16_f32 v48, v48, v49\n"
  "v_cvt_pk_bf16_f32 v49, v50, v51\n"
  "v_cvt_pk_bf16_f32 v52, v52, v53\n"
  "v_cvt_pk_bf16_f32 v53, v54, v55\n"
  "v_cvt_pk_bf16_f32 v56, v56, v57\n"
  "v_cvt_pk_bf16_f32 v57, v58, v59\n"
  "v_cvt_pk_bf16_f32 v60, v60, v61\n"
  "v_cvt_pk_bf16_f32 v61, v62, v63\n"
  "ds_write_b64 v10, v[48:49] offset:0\n"
  "ds_write_b64 v10, v[52:53] offset:4864\n"
  "ds_write_b64 v11, v[56:57] offset:0\n"
  "ds_write_b64 v11, v[60:61] offset:4864\n"
  "s_add_u32 s56, s56, 1\n"
  "s_waitcnt vmcnt(2) lgkmcnt(0)\n"
  "s_barrier\n"
  "v_add_u32 v64, 59136, v2\n"
  "v_xor_b32 v65, 64, v64\n"
  "s_add_u32 s58, s80, 22272\n"
  "s_cmp_lt_i32 s56, 62\n"
  "s_cselect_b32 s74, 36864, 0\n"
  "s_add_u32 s38, s38, s74\n"
  "s_addc_u32 s39, s39, 0\n"
  "ds_read_b128 v[120:123], v4\n"
  "ds_read_b128 v[16:19], v64 offset:0\n"
  "ds_read_b128 v[20:23], v64 offset:2048\n"
  "ds_read_b128 v[24:27], v65 offset:0\n"
  "ds_read_b128 v[28:31], v65 offset:2048\n"
  "ds_read_b128 v[32:35], v64 offset:4096\n"
  "ds_read_b128 v[36:39], v64 offset:6144\n"
  "s_waitcnt lgkmcnt(5)\n"
  "v_mfma_f32_16x16x32_bf16 v[48:51], a[0:3], v[16:19], 0\n"
  "ds_read_b128 v[40:43], v65 offset:4096\n"
  "ds_read_b128 v[76:79], v66\n"
  "s_nop 0\n"
  "s_waitcnt lgkmcnt(6)\n"
  "v_mfma_f32_16x16x32_bf16 v[52:55], a[0:3], v[20:23], 0\n"
  "ds_read_b128 v[44:47], v65 offset:6144\n"
  "ds_read_b128 v[80:83], v68\n"
  "s_nop 0\n"
  "v_mfma_f32_16x16x32_bf16 v[56:59], a[72:75], v[16:19], 0\n"
  "s_bfm_b64 exec, 40, 0\n"
  "global_store_dwordx4 v5, v[120:123], s[60:61] sc1\n"
  "s_mov_b64 exec, -1\n"
  "s_add_u32 s60, s60, 40960\n"
  "s_addc_u32 s61, s61, 0\n"
  "v_mfma_f32_16x16x32_bf16 v[60:63], a[72:75], v[20:23], 0\n"
  "ds_read_b128 v[84:87], v69\n"
  "s_nop 0\n"
  "s_waitcnt lgkmcnt(8)\n"
  "v_mfma_f32_16x16x32_bf16 v[48:51], a[4:7], v[24:27], v[48:51]\n"
  "ds_read_b128 v[16:19], v64 offset:8192\n"
  "ds_read_b64_tr_b16 v[88:89], v72 offset:0\n"
  "s_nop 0\n"
  "s_waitcnt lgkmcnt(9)\n"
  "v_mfma_f32_16x16x32_bf16 v[52:55], a[4:7], v[28:31], v[52:55]\n"
  "ds_read_b128 v[20:23], v64 offset:10240\n"
  "ds_read_b64_tr_b16 v[90:91], v73 offset:0\n"
  "s_nop 0\n"
  "v_mfma_f32_16x16x32_bf16 v[56:59], a[76:79], v[24:27], v[56:59]\n"
  "s_add_u32 m0, s58, 0\n"
  "s_nop 0\n"
  "global_load_lds_dwordx4 v1, s[38:39] offset:0\n"
  "v_mfma_f32_16x16x32_bf16 v[60:63], a[76:79], v[28:31], v[60:63]\n"
  "ds_read_b64_tr_b16 v[92:93], v72 offset:32\n"
  "s_nop 0\n"
  "s_waitcnt lgkmcnt(11)\n"
  "v_mfma_f32_16x16x32_bf16 v[48:51], a[8:11], v[32:35], v[48:51]\n"
  "ds_read_b128 v[24:27], v65 offset:8192\n"
  "ds_read_b64_tr_b16 v[94:95], v73 offset:32\n"
  "s_nop 0\n"
  "s_waitcnt lgkmcnt(12)\n"
  "v_mfma_f32_16x16x32_bf16 v[52:55], a[8:11], v[36:39], v[52:55]\n"
  "ds_read_b128 v[28:31], v65 offset:10240\n"
  "ds_read_b32 v126, v6 offset:0\n"
  "s_nop 0\n"
  "v_mfma_f32_16x16x32_bf16 v[56:59], a[80:83], v[32:35], v[56:59]\n"
  "s_add_u32 m0, s58, 3968\n"
  "s_nop 0\n"
  "global_load_lds_dwordx4 v1, s[38:39] offset:128\n"
  "v_mfma_f32_16x16x32_bf16 v[60:63], a[80:83], v[36:39], v[60:63]\n"
  "s_waitcnt lgkmcnt(10)\n"
  "v_mfma_f32_16x16x32_bf16 v[96:99], v[80:83], v[76:79], 0\n"
  "v_mfma_f32_16x16x32_bf16 v[48:51], a[12:15], v[40:43], v[48:51]\n"
  "ds_read_b128 v[32:35], v64 offset:12288\n"
  "s_waitcnt lgkmcnt(10)\n"
  "v_mfma_f32_16x16x32_bf16 v[100:103], v[84:87], v[76:79], 0\n"
  "v_mfma_f32_16x16x32_bf16 v[52:55], a[12:15], v[44:47], v[52:55]\n"
  "ds_read_b128 v[36:39], v64 offset:14336\n"
  "s_waitcnt lgkmcnt(2)\n"
  "v_lshlrev_b32 v126, 16, v126\n"
  "v_mfma_f32_16x16x32_bf16 v[56:59], a[84:87], v[40:43], v[56:59]\n"
  "s_add_u32 m0, s58, 7936\n"
  "s_nop 0\n"
  "global_load_lds_dwordx4 v1, s[38:39] offset:256\n"
  "v_mfma_f32_16x16x32_bf16 v[60:63], a[84:87], v[44:47], v[60:63]\n"
  "v_mul_f32 v126, 0xbfb8aa3b, v126\n"
  "v_exp_f32 v126, v126\n"
  "v_mfma_f32_16x16x32_bf16 v[48:51], a[16:19], v[16:19], v[48:51]\n"
  "ds_read_b128 v[40:43], v65 offset:12288\n"
  "v_add_f32 v126, 1.0, v126\n"
  "v_rcp_f32 v126, v126\n"
  "v_mfma_f32_16x16x32_bf16 v[52:55], a[16:19], v[20:23], v[52:55]\n"
  "ds_read_b128 v[44:47], v65 offset:14336\n"
  "v_cvt_pk_bf16_f32 v126, v126, v126\n"
  "v_lshlrev_b32 v126, 16, v126\n"
  "v_mfma_f32_16x16x32_bf16 v[56:59], a[88:91], v[16:19], v[56:59]\n"
  "s_add_u32 m0, s58, 11904\n"
  "s_nop 0\n"
  "global_load_lds_dwordx4 v1, s[38:39] offset:384\n"
  "v_mfma_f32_16x16x32_bf16 v[60:63], a[88:91], v[20:23], v[60:63]\n"
  "v_cndmask_b32 v104, v96, v97, s[70:71]\n"
  "v_cndmask_b32 v105, v98, v99, s[70:71]\n"
  "v_mfma_f32_16x16x32_bf16 v[48:51], a[20:23], v[24:27], v[48:51]\n"
  "ds_read_b128 v[16:19], v64 offset:16384\n"
  "v_cndmask_b32 v106, v100, v101, s[70:71]\n"
  "v_cndmask_b32 v107, v102, v103, s[70:71]\n"
  "v_mfma_f32_16x16x32_bf16 v[52:55], a[20:23], v[28:31], v[52:55]\n"
  "ds_read_b128 v[20:23], v64 offset:18432\n"
  "v_cvt_pk_bf16_f32 v104, v104, v105\n"
  "v_and_b32 v105, 0xffff0000, v104\n"
  "v_mfma_f32_16x16x32_bf16 v[56:59], a[92:95], v[24:27], v[56:59]\n"
  "s_add_u32 m0, s58, 15872\n"
  "s_nop 0\n"
  "global_load_lds_dwordx4 v1, s[38:39] offset:512\n"
  "v_mfma_f32_16x16x32_bf16 v[60:63], a[92:95], v[28:31], v[60:63]\n"
  "v_lshlrev_b32 v104, 16, v104\n"
  "v_cvt_pk_bf16_f32 v106, v106, v107\n"
  "s_waitcnt lgkmcnt(5)\n"
  "v_mfma_f32_16x16x32_bf16 v[48:51], a[24:27], v[32:35], v[48:51]\n"
  "ds_read_b128 v[24:27], v65 offset:16384\n"
  "v_and_b32 v107, 0xffff0000, v106\n"
  "v_lshlrev_b32 v106, 16, v106\n"
  "s_waitcnt lgkmcnt(5)\n"
  "v_mfma_f32_16x16x32_bf16 v[52:55], a[24:27], v[36:39], v[52:55]\n"
  "ds_read_b128 v[28:31], v65 offset:18432\n"
  "v_mul_f32 v104, 0x3e715bef, v104\n"
  "v_mul_f32 v105, 0x3e715bef, v105\n"
  "v_mfma_f32_16x16x32_bf16 v[56:59], a[96:99], v[32:35], v[56:59]\n"
  "s_add_u32 m0, s58, 19840\n"
  "s_nop 0\n"
  "global_load_lds_dwordx4 v1, s[38:39] offset:640\n"
  "v_mfma_f32_16x16x32_bf16 v[60:63], a[96:99], v[36:39], v[60:63]\n"
  "v_mul_f32 v106, 0x3e715bef, v106\n"
  "v_mul_f32 v107, 0x3e715bef, v107\n"
  "s_waitcnt lgkmcnt(5)\n"
  "v_mfma_f32_16x16x32_bf16 v[48:51], a[28:31], v[40:43], v[48:51]\n"
  "ds_read_b128 v[32:35], v64 offset:20480\n"
  "v_cvt_pk_bf16_f32 v104, v104, v105\n"
  "v_and_b32 v105, 0xffff0000, v104\n"
  "s_waitcnt lgkmcnt(5)\n"
  "v_mfma_f32_16x16x32_bf16 v[52:55], a[28:31], v[44:47], v[52:55]\n"
  "ds_read_b128 v[36:39], v64 offset:22528\n"
  "v_lshlrev_b32 v104, 16, v104\n"
  "v_cvt_pk_bf16_f32 v106, v106, v107\n"
  "v_mfma_f32_16x16x32_bf16 v[56:59], a[100:103], v[40:43], v[56:59]\n"
  "s_add_u32 m0, s58, 23808\n"
  "s_nop 0\n"
  "global_load_lds_dwordx4 v1, s[38:39] offset:768\n"
  "v_mfma_f32_16x16x32_bf16 v[60:63], a[100:103], v[44:47], v[60:63]\n"
  "v_and_b32 v107, 0xffff0000, v106\n"
  "v_lshlrev_b32 v106, 16, v106\n"
  "s_waitcnt lgkmcnt(5)\n"
  "v_mfma_f32_16x16x32_bf16 v[48:51], a[32:35], v[16:19], v[48:51]\n"
  "ds_read_b128 v[40:43], v65 offset:20480\n"
  "v_cndmask_b32 v104, v128, v104, s[8:9]\n"
  "v_cndmask_b32 v105, v128, v105, s[10:11]\n"
  "s_waitcnt lgkmcnt(5)\n"
  "v_mfma_f32_16x16x32_bf16 v[52:55], a[32:35], v[20:23], v[52:55]\n"
  "ds_read_b128 v[44:47], v65 offset:22528\n"
  "v_cndmask_b32 v106, v128, v106, s[12:13]\n"
  "v_cndmask_b32 v107, v128, v107, s[14:15]\n"
  "v_mfma_f32_16x16x32_bf16 v[56:59], a[104:107], v[16:19], v[56:59]\n"
  "s_add_u32 m0, s58, 27776\n"
  "s_nop 0\n"
  "global_load_lds_dwordx4 v1, s[38:39] offset:896\n"
  "v_mfma_f32_16x16x32_bf16 v[60:63], a[104:107], v[20:23], v[60:63]\n"
  "v_max3_f32 v124, v104, v105, v106\n"
  "v_max_f32 v124, v124, v107\n"
  "s_waitcnt lgkmcnt(5)\n"
  "v_mfma_f32_16x16x32_bf16 v[48:51], a[36:39], v[24:27], v[48:51]\n"
  "ds_read_b128 v[16:19], v64 offset:24576\n"
  "v_mov_b32 v125, v124\n"
  "s_nop 1\n"
  "v_permlane16_swap_b32 v124, v125\n"
  "v_max_f32 v124, v124, v125\n"
  "s_waitcnt lgkmcnt(5)\n"
  "v_mfma_f32_16x16x32_bf16 v[52:55], a[36:39], v[28:31], v[52:55]\n"
  "ds_read_b128 v[20:23], v64 offset:26624\n"
  "v_mov_b32 v125, v124\n"
  "s_nop 1\n"
  "v_permlane32_swap_b32 v124, v125\n"
  "v_max_f32 v124, v124, v125\n"
  "v_mfma_f32_16x16x32_bf16 v[56:59], a[108:111], v[24:27], v[56:59]\n"
  "s_add_u32 m0, s58, 31744\n"
  "s_nop 0\n"
  "global_load_lds_dwordx4 v1, s[38:39] offset:1024\n"
  "v_mfma_f32_16x16x32_bf16 v[60:63], a[108:111], v[28:31], v[60:63]\n"
  "v_sub_f32 v104, v104, v124\n"
  "v_sub_f32 v105, v105, v124\n"
  "s_waitcnt lgkmcnt(5)\n"
  "v_mfma_f32_16x16x32_bf16 v[48:51], a[40:43], v[32:35], v[48:51]\n"
  "ds_read_b128 v[24:27], v65 offset:24576\n"
  "v_sub_f32 v106, v106, v124\n"
  "v_sub_f32 v107, v107, v124\n"
  "s_waitcnt lgkmcnt(5)\n"
  "v_mfma_f32_16x16x32_bf16 v[52:55], a[40:43], v[36:39], v[52:55]\n"
  "ds_read_b128 v[28:31], v65 offset:26624\n"
  "v_mul_f32 v104, 0x3fb8aa3b, v104\n"
  "v_mul_f32 v105, 0x3fb8aa3b, v105\n"
  "v_mfma_f32_16x16x32_bf16 v[56:59], a[112:115], v[32:35], v[56:59]\n"
  "v_mul_f32 v106, 0x3fb8aa3b, v106\n"
  "v_mul_f32 v107, 0x3fb8aa3b, v107\n"
  "v_mfma_f32_16x16x32_bf16 v[60:63], a[112:115], v[36:39], v[60:63]\n"
  "v_exp_f32 v104, v104\n"
  "s_waitcnt lgkmcnt(5)\n"
  "v_mfma_f32_16x16x32_bf16 v[48:51], a[44:47], v[40:43], v[48:51]\n"
  "ds_read_b128 v[32:35], v64 offset:28672\n"
  "v_exp_f32 v105, v105\n"
  "s_waitcnt lgkmcnt(5)\n"
  "v_mfma_f32_16x16x32_bf16 v[52:55], a[44:47], v[44:47], v[52:55]\n"
  "ds_read_b128 v[36:39], v64 offset:30720\n"
  "v_exp_f32 v106, v106\n"
  "v_mfma_f32_16x16x32_bf16 v[56:59], a[116:119], v[40:43], v[56:59]\n"
  "v_exp_f32 v107, v107\n"
  "v_mfma_f32_16x16x32_bf16 v[60:63], a[116:119], v[44:47], v[60:63]\n"
  "s_mov_b64 s[8:9], s[16:17]\n"
  "s_mov_b64 s[10:11], s[18:19]\n"
  "s_waitcnt lgkmcnt(5)\n"
  "v_mfma_f32_16x16x32_bf16 v[48:51], a[48:51], v[16:19], v[48:51]\n"
  "ds_read_b128 v[40:43], v65 offset:28672\n"
  "v_add_f32 v124, v104, v105\n"
  "v_add_f32 v124, v124, v106\n"
  "s_waitcnt lgkmcnt(5)\n"
  "v_mfma_f32_16x16x32_bf16 v[52:55], a[48:51], v[20:23], v[52:55]\n"
  "ds_read_b128 v[44:47], v65 offset:30720\n"
  "v_add_f32 v124, v124, v107\n"
  "v_mov_b32 v125, v124\n"
  "s_nop 1\n"
  "v_permlane16_swap_b32 v124, v125\n"
  "v_add_f32 v124, v124, v125\n"
  "v_mfma_f32_16x16x32_bf16 v[56:59], a[120:123], v[16:19], v[56:59]\n"
  "v_mov_b32 v125, v124\n"
  "s_nop 1\n"
  "v_permlane32_swap_b32 v124, v125\n"
  "v_add_f32 v124, v124, v125\n"
  "v_mfma_f32_16x16x32_bf16 v[60:63], a[120:123], v[20:23], v[60:63]\n"
  "v_rcp_f32 v124, v124\n"
  "s_waitcnt lgkmcnt(5)\n"
  "v_mfma_f32_16x16x32_bf16 v[48:51], a[52:55], v[24:27], v[48:51]\n"
  "ds_read_b128 v[16:19], v64 offset:32768\n"
  "v_mul_f32 v104, v124, v104\n"
  "v_mul_f32 v105, v124, v105\n"
  "s_waitcnt lgkmcnt(5)\n"
  "v_mfma_f32_16x16x32_bf16 v[52:55], a[52:55], v[28:31], v[52:55]\n"
  "ds_read_b128 v[20:23], v64 offset:34816\n"
  "v_mul_f32 v106, v124, v106\n"
  "v_mul_f32 v107, v124, v107\n"
  "v_mfma_f32_16x16x32_bf16 v[56:59], a[124:127], v[24:27], v[56:59]\n"
  "v_cvt_pk_bf16_f32 v108, v104, v104\n"
  "v_cvt_pk_bf16_f32 v109, v105, v105\n"
  "v_mfma_f32_16x16x32_bf16 v[60:63], a[124:127], v[28:31], v[60:63]\n"
  "v_cvt_pk_bf16_f32 v110, v106, v106\n"
  "v_cvt_pk_bf16_f32 v111, v107, v107\n"
  "s_waitcnt lgkmcnt(5)\n"
  "v_mfma_f32_16x16x32_bf16 v[48:51], a[56:59], v[32:35], v[48:51]\n"
  "ds_read_b128 v[24:27], v65 offset:32768\n"
  "v_and_b32 v108, v8, v108\n"
  "v_and_b32 v109, v8, v109\n"
  "s_waitcnt lgkmcnt(5)\n"
  "v_mfma_f32_16x16x32_bf16 v[52:55], a[56:59], v[36:39], v[52:55]\n"
  "ds_read_b128 v[28:31], v65 offset:34816\n"
  "v_and_b32 v110, v8, v110\n"
  "v_and_b32 v111, v8, v111\n"
  "v_mfma_f32_16x16x32_bf16 v[56:59], a[128:131], v[32:35], v[56:59]\n"
  "s_waitcnt vmcnt(9)\n"
  "s_barrier\n"
  "v_mov_b32 v9, 0\n"
  "v_mov_b32 v15, s56\n"
  "s_cmp_eq_u32 s47, 0\n"
  "s_cselect_b64 exec, 1, 0\n"
  "global_store_dword v9, v15, s[64:65] sc1\n"
  "s_mov_b64 exec, -1\n"
  "v_mfma_f32_16x16x32_bf16 v[60:63], a[128:131], v[36:39], v[60:63]\n"
  "v_mfma_f32_16x16x32_bf16 v[112:115], v[88:91], v[108:111], 0\n"
  "s_waitcnt lgkmcnt(5)\n"
  "v_mfma_f32_16x16x32_bf16 v[48:51], a[60:63], v[40:43], v[48:51]\n"
  "v_mfma_f32_16x16x32_bf16 v[116:119], v[92:95], v[108:111], 0\n"
  "s_waitcnt lgkmcnt(4)\n"
  "v_mfma_f32_16x16x32_bf16 v[52:55], a[60:63], v[44:47], v[52:55]\n"
  "v_mfma_f32_16x16x32_bf16 v[56:59], a[132:135], v[40:43], v[56:59]\n"
  "v_mfma_f32_16x16x32_bf16 v[60:63], a[132:135], v[44:47], v[60:63]\n"
  "s_waitcnt lgkmcnt(3)\n"
  "v_mfma_f32_16x16x32_bf16 v[48:51], a[64:67], v[16:19], v[48:51]\n"
  "s_waitcnt lgkmcnt(2)\n"
  "v_mfma_f32_16x16x32_bf16 v[52:55], a[64:67], v[20:23], v[52:55]\n"
  "v_mfma_f32_16x16x32_bf16 v[56:59], a[136:139], v[16:19], v[56:59]\n"
  "v_mfma_f32_16x16x32_bf16 v[60:63], a[136:139], v[20:23], v[60:63]\n"
  "s_waitcnt lgkmcnt(1)\n"
  "v_mfma_f32_16x16x32_bf16 v[48:51], a[68:71], v[24:27], v[48:51]\n"
  "s_waitcnt lgkmcnt(0)\n"
  "v_mfma_f32_16x16x32_bf16 v[52:55], a[68:71], v[28:31], v[52:55]\n"
  "v_mfma_f32_16x16x32_bf16 v[56:59], a[140:143], v[24:27], v[56:59]\n"
  "v_mfma_f32_16x16x32_bf16 v[60:63], a[140:143], v[28:31], v[60:63]\n"
  "v_cvt_pk_bf16_f32 v112, v112, v113\n"
  "v_cvt_pk_bf16_f32 v113, v114, v115\n"
  "v_cvt_pk_bf16_f32 v114, v116, v117\n"
  "v_cvt_pk_bf16_f32 v115, v118, v119\n"
  "ds_write_b64 v3, v[112:113]\n"
  "s_mov_b64 exec, 0xffff\n"
  "ds_write_b64 v3, v[114:115] offset:32\n"
  "s_mov_b64 exec, s[68:69]\n"
  "global_store_dword v127, v126, s[62:63] sc1\n"
  "s_mov_b64 exec, -1\n"
  "s_add_u32 s62, s62, 2048\n"
  "s_addc_u32 s63, s63, 0\n"
  "v_cvt_pk_bf16_f32 v48, v48, v49\n"
  "v_cvt_pk_bf16_f32 v49, v50, v51\n"
  "v_cvt_pk_bf16_f32 v52, v52, v53\n"
  "v_cvt_pk_bf16_f32 v53, v54, v55\n"
  "v_cvt_pk_bf16_f32 v56, v56, v57\n"
  "v_cvt_pk_bf16_f32 v57, v58, v59\n"
  "v_cvt_pk_bf16_f32 v60, v60, v61\n"
  "v_cvt_pk_bf16_f32 v61, v62, v63\n"
  "ds_write_b64 v10, v[48:49] offset:9728\n"
  "ds_write_b64 v10, v[52:53] offset:14592\n"
  "ds_write_b64 v11, v[56:57] offset:9728\n"
  "ds_write_b64 v11, v[60:61] offset:14592\n"
  "s_add_u32 s56, s56, 1\n"
  "s_cmp_lt_u32 s56, 63\n"
  "s_cbranch_scc1 ALOOP\n"
  "s_waitcnt lgkmcnt(0)\n"
  "s_barrier\n"
  "ds_read_b128 v[120:123], v4\n"
  "s_bfm_b64 exec, 40, 0\n"
  "s_waitcnt lgkmcnt(0)\n"
  "global_store_dwordx4 v5, v[120:123], s[60:61] sc1\n"
  "s_mov_b64 exec, -1\n"
  "s_add_u32 s60, s60, 40960\n"
  "s_addc_u32 s61, s61, 0\n"
  "ds_read_b128 v[76:79], v67\n"
  "s_nop 0\n"
  "ds_read_b128 v[80:83], v70\n"
  "s_nop 0\n"
  "ds_read_b128 v[84:87], v71\n"
  "s_nop 0\n"
  "ds_read_b64_tr_b16 v[88:89], v74 offset:0\n"
  "s_nop 0\n"
  "ds_read_b64_tr_b16 v[90:91], v75 offset:0\n"
  "s_nop 0\n"
  "ds_read_b64_tr_b16 v[92:93], v74 offset:32\n"
  "s_nop 0\n"
  "ds_read_b64_tr_b16 v[94:95], v75 offset:32\n"
  "s_nop 0\n"
  "ds_read_b32 v126, v6 offset:9728\n"
  "s_nop 0\n"
  "s_waitcnt lgkmcnt(6)\n"
  "v_mfma_f32_16x16x32_bf16 v[96:99], v[80:83], v[76:79], 0\n"
  "s_waitcnt lgkmcnt(5)\n"
  "v_mfma_f32_16x16x32_bf16 v[100:103], v[84:87], v[76:79], 0\n"
  "s_waitcnt lgkmcnt(0)\n"
  "v_lshlrev_b32 v126, 16, v126\n"
  "v_mul_f32 v126, 0xbfb8aa3b, v126\n"
  "v_exp_f32 v126, v126\n"
  "s_nop 0\n"
  "v_add_f32 v126, 1.0, v126\n"
  "v_rcp_f32 v126, v126\n"
  "s_nop 0\n"
  "v_cvt_pk_bf16_f32 v126, v126, v126\n"
  "v_lshlrev_b32 v126, 16, v126\n"
  "v_cndmask_b32 v104, v96, v97, s[70:71]\n"
  "v_cndmask_b32 v105, v98, v99, s[70:71]\n"
  "v_cndmask_b32 v106, v100, v101, s[70:71]\n"
  "v_cndmask_b32 v107, v102, v103, s[70:71]\n"
  "v_cvt_pk_bf16_f32 v104, v104, v105\n"
  "v_and_b32 v105, 0xffff0000, v104\n"
  "v_lshlrev_b32 v104, 16, v104\n"
  "v_cvt_pk_bf16_f32 v106, v106, v107\n"
  "v_and_b32 v107, 0xffff0000, v106\n"
  "v_lshlrev_b32 v106, 16, v106\n"
  "v_mul_f32 v104, 0x3e715bef, v104\n"
  "v_mul_f32 v105, 0x3e715bef, v105\n"
  "v_mul_f32 v106, 0x3e715bef, v106\n"
  "v_mul_f32 v107, 0x3e715bef, v107\n"
  "v_cvt_pk_bf16_f32 v104, v104, v105\n"
  "v_and_b32 v105, 0xffff0000, v104\n"
  "v_lshlrev_b32 v104, 16, v104\n"
  "v_cvt_pk_bf16_f32 v106, v106, v107\n"
  "v_and_b32 v107, 0xffff0000, v106\n"
  "v_lshlrev_b32 v106, 16, v106\n"
  "v_cndmask_b32 v104, v128, v104, s[8:9]\n"
  "v_cndmask_b32 v105, v128, v105, s[10:11]\n"
  "v_cndmask_b32 v106, v128, v106, s[12:13]\n"
  "v_cndmask_b32 v107, v128, v107, s[14:15]\n"
  "v_max3_f32 v124, v104, v105, v106\n"
  "v_max_f32 v124, v124, v107\n"
  "v_mov_b32 v125, v124\n"
  "s_nop 1\n"
  "v_permlane16_swap_b32 v124, v125\n"
  "v_max_f32 v124, v124, v125\n"
  "v_mov_b32 v125, v124\n"
  "s_nop 1\n"
  "v_permlane32_swap_b32 v124, v125\n"
  "v_max_f32 v124, v124, v125\n"
  "v_sub_f32 v104, v104, v124\n"
  "v_sub_f32 v105, v105, v124\n"
  "v_sub_f32 v106, v106, v124\n"
  "v_sub_f32 v107, v107, v124\n"
  "v_mul_f32 v104, 0x3fb8aa3b, v104\n"
  "v_mul_f32 v105, 0x3fb8aa3b, v105\n"
  "v_mul_f32 v106, 0x3fb8aa3b, v106\n"
  "v_mul_f32 v107, 0x3fb8aa3b, v107\n"
  "v_exp_f32 v104, v104\n"
  "v_exp_f32 v105, v105\n"
  "v_exp_f32 v106, v106\n"
  "v_exp_f32 v107, v107\n"
  "s_mov_b64 s[8:9], s[16:17]\n"
  "s_mov_b64 s[10:11], s[18:19]\n"
  "v_add_f32 v124, v104, v105\n"
  "v_add_f32 v124, v124, v106\n"
  "v_add_f32 v124, v124, v107\n"
  "v_mov_b32 v125, v124\n"
  "s_nop 1\n"
  "v_permlane16_swap_b32 v124, v125\n"
  "v_add_f32 v124, v124, v125\n"
  "v_mov_b32 v125, v124\n"
  "s_nop 1\n"
  "v_permlane32_swap_b32 v124, v125\n"
  "v_add_f32 v124, v124, v125\n"
  "v_rcp_f32 v124, v124\n"
  "s_nop 0\n"
  "v_mul_f32 v104, v124, v104\n"
  "v_mul_f32 v105, v124, v105\n"
  "v_mul_f32 v106, v124, v106\n"
  "v_mul_f32 v107, v124, v107\n"
  "v_cvt_pk_bf16_f32 v108, v104, v104\n"
  "v_cvt_pk_bf16_f32 v109, v105, v105\n"
  "v_cvt_pk_bf16_f32 v110, v106, v106\n"
  "v_cvt_pk_bf16_f32 v111, v107, v107\n"
  "v_and_b32 v108, v8, v108\n"
  "v_and_b32 v109, v8, v109\n"
  "v_and_b32 v110, v8, v110\n"
  "v_and_b32 v111, v8, v111\n"
  "s_nop 1\n"
  "v_mfma_f32_16x16x32_bf16 v[112:115], v[88:91], v[108:111], 0\n"
  "v_mfma_f32_16x16x32_bf16 v[116:119], v[92:95], v[108:111], 0\n"
  "s_barrier\n"
  "s_nop 3\n"
  "v_cvt_pk_bf16_f32 v112, v112, v113\n"
  "v_cvt_pk_bf16_f32 v113, v114, v115\n"
  "s_nop 0\n"
  "v_cvt_pk_bf16_f32 v114, v116, v117\n"
  "v_cvt_pk_bf16_f32 v115, v118, v119\n"
  "ds_write_b64 v3, v[112:113]\n"
  "s_mov_b64 exec, 0xffff\n"
  "ds_write_b64 v3, v[114:115] offset:32\n"
  "s_mov_b64 exec, s[68:69]\n"
  "global_store_dword v127, v126, s[62:63] sc1\n"
  "s_mov_b64 exec, -1\n"
  "s_add_u32 s62, s62, 2048\n"
  "s_addc_u32 s63, s63, 0\n"
  "s_waitcnt lgkmcnt(0)\n"
  "s_barrier\n"
  "ds_read_b128 v[120:123], v4\n"
  "s_bfm_b64 exec, 40, 0\n"
  "s_waitcnt lgkmcnt(0)\n"
  "global_store_dwordx4 v5, v[120:123], s[60:61] sc1\n"
  "s_mov_b64 exec, -1\n"
  "s_add_u32 s60, s60, 40960\n"
  "s_addc_u32 s61, s61, 0\n"
  "s_waitcnt vmcnt(0)\n"
  "s_barrier\n"
  "v_mov_b32 v9, 0\n"
  "v_mov_b32 v15, 64\n"
  "s_cmp_eq_u32 s47, 0\n"
  "s_cselect_b64 exec, 1, 0\n"
  "global_store_dword v9, v15, s[64:65] sc1\n"
  "s_mov_b64 exec, -1\n"
  "s_branch AR\n"
  "AQ:\n"
  "s_endpgm\n"
  "AR:\n"
      : "+{v[0:15]}"(vin), "+{s[36:43]}"(sa), "+{s[44:59]}"(sb) :: "a0", "a1", "a2", "a3", "a4", "a5", "a6", "a7", "a8", "a9", "a10", "a11", "a12", "a13", "a14", "a15", "a16", "a17", "a18", "a19", "a20", "a21", "a22", "a23", "a24", "a25", "a26", "a27", "a28", "a29", "a30", "a31", "a32", "a33", "a34", "a35", "a36", "a37", "a38", "a39", "a40", "a41", "a42", "a43", "a44", "a45", "a46", "a47", "a48", "a49", "a50", "a51", "a52", "a53", "a54", "a55", "a56", "a57", "a58", "a59", "a60", "a61", "a62", "a63", "a64", "a65", "a66", "a67", "a68", "a69", "a70", "a71", "a72", "a73", "a74", "a75", "a76", "a77", "a78", "a79", "a80", "a81", "a82", "a83", "a84", "a85", "a86", "a87", "a88", "a89", "a90", "a91", "a92", "a93", "a94", "a95", "a96", "a97", "a98", "a99", "a100", "a101", "a102", "a103", "a104", "a105", "a106", "a107", "a108", "a109", "a110", "a111", "a112", "a113", "a114", "a115", "a116", "a117", "a118", "a119", "a120", "a121", "a122", "a123", "a124", "a125", "a126", "a127", "a128", "a129", "a130", "a131", "a132", "a133", "a134", "a135", "a136", "a137", "a138", "a139", "a140", "a141", "a142", "a143", "s4", "s5", "s8", "s9", "s10", "s11", "s12", "s13", "s14", "s15", "s16", "s17", "s18", "s19", "s60", "s61", "s62", "s63", "s64", "s65", "s68", "s69", "s70", "s71", "s74", "s75", "s76", "s80", "v16", "v17", "v18", "v19", "v20", "v21", "v22", "v23", "v24", "v25", "v26", "v27", "v28", "v29", "v30", "v31", "v32", "v33", "v34", "v35", "v36", "v37", "v38", "v39", "v40", "v41", "v42", "v43", "v44", "v45", "v46", "v47", "v48", "v49", "v50", "v51", "v52", "v53", "v54", "v55", "v56", "v57", "v58", "v59", "v60", "v61", "v62", "v63", "v64", "v65", "v66", "v67", "v68", "v69", "v70", "v71", "v72", "v73", "v74", "v75", "v76", "v77", "v78", "v79", "v80", "v81", "v82", "v83", "v84", "v85", "v86", "v87", "v88", "v89", "v90", "v91", "v92", "v93", "v94", "v95", "v96", "v97", "v98", "v99", "v100", "v101", "v102", "v103", "v104", "v105", "v106", "v107", "v108", "v109", "v110", "v111", "v112", "v113", "v114", "v115", "v116", "v117", "v118", "v119", "v120", "v121", "v122", "v123", "v124", "v125", "v126", "v127", "v128", "vcc", "scc", "memory");
  }
#pragma unroll
  for (int i = 0; i < 16; ++i) { vin[i] = 0; sb[i] = 0; sa[i & 7] = 0; }
  vin[0] = lane * 16;
  if (tk >= 24) { ARGS; PTR(sa, 0, p.ws);
    const bool last = tk < 28;
    const int h = fd * 8 + (tk & 3) * 2 + (wave & 1);
    if (wave >= 2) return;
    vin[1] = lane < 2 ? (C_KVP) + (2 * h + lane) * 128 : lane == 2 ? (C_HF) + h * 256 : (C_VF) + (2 * h + (lane & 1)) * 128;
    vin[2] = lane < 2 ? 2 : 1; vin[3] = qq * 256 + g * 64 + c4 * 16;
    vin[4] = wave * 2048 + lane * 16; vin[5] = wave * 2048 + c4 * 128 + (4 * g + qq) * 8;
    vin[6] = n - 4 * g;
    sb[0] = KVH + h * 65536 + 4096; sb[1] = h * 65536 + 4096 + 32768; sb[2] = WO_KM + h * 2048; sb[3] = sb[2] + (WO_VP - WO_KM);
    sb[4] = sb[1] + 16384 + (last ? 8192 : 0); sb[5] = (last ? C_QAL : C_QAP) + h * 128; sb[8] = last ? 63 : 0; sb[9] = last ? 1 : 63; sb[6] = __float_as_uint(QS); sb[7] = WO_ST + (role * 4 + wave) * 4096 + (last ? 60 * 64 : 0);
    UNIFORM(sa, 8); UNIFORM(sb, 16);
    asm volatile(
  "s_nop 4\n"
  "v_cmp_le_i32 s[8:9], 0, v6\n"
  "v_cmp_le_i32 s[10:11], 1, v6\n"
  "v_cmp_le_i32 s[12:13], 2, v6\n"
  "v_cmp_le_i32 s[14:15], 3, v6\n"
  "v_mov_b64 v[156:157], 0\n"
  "v_mov_b64 v[158:159], 0\n"
  "v_mov_b64 v[160:161], 0\n"
  "v_mov_b64 v[162:163], 0\n"
  "v_mov_b64 v[164:165], 0\n"
  "v_mov_b64 v[166:167], 0\n"
  "v_mov_b64 v[168:169], 0\n"
  "v_mov_b64 v[170:171], 0\n"
  "v_mov_b64 v[172:173], 0\n"
  "v_mov_b64 v[174:175], 0\n"
  "v_mov_b64 v[176:177], 0\n"
  "v_mov_b64 v[178:179], 0\n"
  "v_mov_b64 v[180:181], 0\n"
  "v_mov_b64 v[182:183], 0\n"
  "v_mov_b64 v[184:185], 0\n"
  "v_mov_b64 v[186:187], 0\n"
  "v_mov_b64 v[150:151], 0\n"
  "v_mov_b32 v12, 0xff7fffff\n"
  "s_mov_b32 s56, 0\n"
  "s_mov_b32 s57, 400000\n"
  "s_add_u32 s74, s36, 16778624\n"
  "s_addc_u32 s75, s37, 0\n"
  "PLCT:\n"
  "v_mov_b32 v7, 0\n"
  "global_load_dword v7, v7, s[74:75] sc1\n"
  "s_waitcnt vmcnt(0)\n"
  "v_readfirstlane_b32 s76, v7\n"
  "s_cmp_ge_u32 s76, 0x40000\n"
  "s_cbranch_scc1 PDCT\n"
  "s_sub_u32 s57, s57, 1\n"
  "s_cbranch_scc1 TQUIT\n"
  "s_sleep 4\n"
  "s_branch PLCT\n"
  "PDCT:\n"
  "s_and_b32 s74, s76, 2\n"
  "s_lshr_b32 s75, s74, 1\n"
  "s_lshl_b32 s4, 0x55555555, s75\n"
  "s_mov_b32 s5, s4\n"
  "s_lshl_b32 s74, s74, 12\n"
  "v_xor_b32 v1, s74, v1\n"
  "s_xor_b32 s49, s74, s49\n"
  "TINIT:\n"
  "s_add_u32 s64, s36, s46\n"
  "s_addc_u32 s65, s37, 0\n"
  "s_add_u32 s66, s36, s47\n"
  "s_addc_u32 s67, s37, 0\n"
  "s_add_u32 s70, s36, s49\n"
  "s_addc_u32 s71, s37, 0\n"
  "s_bitcmp1_b64 s[4:5], s56\n"
  "s_cselect_b32 s74, 8192, 0\n"
  "s_and_b32 s75, s56, 63\n"
  "s_lshl_b32 s75, s75, 21\n"
  "s_add_u32 s74, s74, s75\n"
  "s_add_u32 s74, s74, 20971520\n"
  "s_xor_b32 s75, s74, s44\n"
  "s_add_u32 s60, s36, s75\n"
  "s_addc_u32 s61, s37, 0\n"
  "s_xor_b32 s75, s74, s45\n"
  "s_add_u32 s62, s36, s75\n"
  "s_addc_u32 s63, s37, 0\n"
  "s_mov_b64 exec, 3\n"
  "TPS:\n"
  "global_load_dword v9, v1, s[36:37] sc1\n"
  "v_add_u32 v7, s56, v2\n"
  "v_add_u32 v7, -1, v7\n"
  "v_min_i32 v7, 64, v7\n"
  "s_waitcnt vmcnt(0)\n"
  "v_cmp_gt_i32 vcc, v7, v9\n"
  "s_cbranch_vccz TGS\n"
  "s_sub_u32 s57, s57, 1\n"
  "s_cbranch_scc1 TQUIT\n"
  "s_sleep 2\n"
  "s_branch TPS\n"
  "TGS:\n"
  "s_mov_b64 exec, -1\n"
  "global_load_dwordx4 v[16:19], v0, s[62:63] offset:-4096 sc1\n"
  "global_load_dwordx4 v[20:23], v0, s[62:63] offset:-3072 sc1\n"
  "global_load_dwordx4 v[24:27], v0, s[62:63] offset:-2048 sc1\n"
  "global_load_dwordx4 v[28:31], v0, s[62:63] offset:-1024 sc1\n"
  "global_load_dwordx4 v[32:35], v0, s[62:63] offset:0 sc1\n"
  "global_load_dwordx4 v[36:39], v0, s[62:63] offset:1024 sc1\n"
  "global_load_dwordx4 v[40:43], v0, s[62:63] offset:2048 sc1\n"
  "global_load_dwordx4 v[44:47], v0, s[62:63] offset:3072 sc1\n"
  "global_load_dwordx4 a[0:3], v0, s[60:61] offset:-4096 sc1\n"
  "global_load_dwordx4 a[4:7], v0, s[60:61] offset:-3072 sc1\n"
  "global_load_dwordx4 a[8:11], v0, s[60:61] offset:-2048 sc1\n"
  "global_load_dwordx4 a[12:15], v0, s[60:61] offset:-1024 sc1\n"
  "global_load_dwordx4 a[16:19], v0, s[60:61] offset:0 sc1\n"
  "global_load_dwordx4 a[20:23], v0, s[60:61] offset:1024 sc1\n"
  "global_load_dwordx4 a[24:27], v0, s[60:61] offset:2048 sc1\n"
  "global_load_dwordx4 a[28:31], v0, s[60:61] offset:3072 sc1\n"
  "s_add_u32 s60, s60, 16384\n"
  "s_addc_u32 s61, s61, 0\n"
  "global_load_dwordx4 a[32:35], v0, s[60:61] offset:-4096 sc1\n"
  "global_load_dwordx4 a[36:39], v0, s[60:61] offset:-3072 sc1\n"
  "global_load_dwordx4 a[40:43], v0, s[60:61] offset:-2048 sc1\n"
  "global_load_dwordx4 a[44:47], v0, s[60:61] offset:-1024 sc1\n"
  "global_load_dwordx4 a[48:51], v0, s[60:61] offset:0 sc1\n"
  "global_load_dwordx4 a[52:55], v0, s[60:61] offset:1024 sc1\n"
  "global_load_dwordx4 a[56:59], v0, s[60:61] offset:2048 sc1\n"
  "global_load_dwordx4 a[60:63], v0, s[60:61] offset:3072 sc1\n"
  "s_add_u32 s76, s56, 1\n"
  "s_min_u32 s76, s76, 63\n"
  "s_bitcmp1_b64 s[4:5], s76\n"
  "s_cselect_b32 s74, 8192, 0\n"
  "s_and_b32 s75, s76, 63\n"
  "s_lshl_b32 s75, s75, 21\n"
  "s_add_u32 s74, s74, s75\n"
  "s_add_u32 s74, s74, 20971520\n"
  "s_xor_b32 s75, s74, s44\n"
  "s_add_u32 s60, s36, s75\n"
  "s_addc_u32 s61, s37, 0\n"
  "s_xor_b32 s75, s74, s45\n"
  "s_add_u32 s62, s36, s75\n"
  "s_addc_u32 s63, s37, 0\n"
  "s_mov_b64 s[16:17], -1\n"
  "s_waitcnt vmcnt(0)\n"
  "TOP0:\n"
  "s_waitcnt vmcnt(8)\n"
  "ds_write_b128 v4, v[80:83] offset:0\n"
  "ds_write_b128 v4, v[84:87] offset:1024\n"
  "ds_read_b64_tr_b16 v[88:89], v5 offset:0\n"
  "ds_read_b64_tr_b16 v[90:91], v5 offset:512\n"
  "ds_read_b64_tr_b16 v[92:93], v5 offset:1024\n"
  "ds_read_b64_tr_b16 v[94:95], v5 offset:1536\n"
  "v_mfma_f32_16x16x32_bf16 v[120:123], a[0:3], v[16:19], 0\n"
  "v_mfma_f32_16x16x32_bf16 v[124:127], a[0:3], v[20:23], 0\n"
  "v_mfma_f32_16x16x32_bf16 v[128:131], a[4:7], v[20:23], 0\n"
  "v_mfma_f32_16x16x32_bf16 v[120:123], a[8:11], v[24:27], v[120:123]\n"
  "v_mfma_f32_16x16x32_bf16 v[124:127], a[8:11], v[28:31], v[124:127]\n"
  "v_mfma_f32_16x16x32_bf16 v[128:131], a[12:15], v[28:31], v[128:131]\n"
  "v_mfma_f32_16x16x32_bf16 v[120:123], a[16:19], v[32:35], v[120:123]\n"
  "v_mfma_f32_16x16x32_bf16 v[124:127], a[16:19], v[36:39], v[124:127]\n"
  "v_mfma_f32_16x16x32_bf16 v[128:131], a[20:23], v[36:39], v[128:131]\n"
  "v_mfma_f32_16x16x32_bf16 v[120:123], a[24:27], v[40:43], v[120:123]\n"
  "v_mfma_f32_16x16x32_bf16 v[124:127], a[24:27], v[44:47], v[124:127]\n"
  "v_mfma_f32_16x16x32_bf16 v[128:131], a[28:31], v[44:47], v[128:131]\n"
  "s_cmp_eq_u64 s[16:17], 0\n"
  "s_cbranch_scc1 TGO0\n"
  "s_waitcnt vmcnt(0)\n"
  "v_mov_b32 v7, 0\n"
  "v_mov_b32 v8, s56\n"
  "s_mov_b64 exec, 1\n"
  "global_store_dword v7, v8, s[70:71] sc1\n"
  "s_mov_b64 exec, -1\n"
  "TP0:\n"
  "global_load_dword v9, v1, s[36:37] sc1\n"
  "v_add_u32 v7, s56, v2\n"
  "v_add_u32 v7, 0, v7\n"
  "v_min_i32 v7, 64, v7\n"
  "s_waitcnt vmcnt(0)\n"
  "v_cmp_gt_i32 vcc, v7, v9\n"
  "s_cbranch_vccz TG0\n"
  "s_sub_u32 s57, s57, 1\n"
  "s_cbranch_scc1 TQUIT\n"
  "s_sleep 2\n"
  "s_branch TP0\n"
  "TG0:\n"
  "global_load_dwordx4 v[80:83], v0, s[64:65] offset:0 sc1\n"
  "global_load_dwordx4 v[84:87], v0, s[64:65] offset:1024 sc1\n"
  "global_load_dwordx4 v[96:99], v3, s[66:67] offset:0 sc1\n"
  "global_load_dwordx4 v[100:103], v3, s[66:67] offset:1024 sc1\n"
  "s_add_u32 s64, s64, 65536\n"
  "s_addc_u32 s65, s65, 0\n"
  "s_add_u32 s66, s66, 65536\n"
  "s_addc_u32 s67, s67, 0\n"
  "s_waitcnt vmcnt(3)\n"
  "ds_write_b128 v4, v[80:83] offset:0\n"
  "s_waitcnt vmcnt(2)\n"
  "ds_write_b128 v4, v[84:87] offset:1024\n"
  "s_waitcnt lgkmcnt(5)\n"
  "ds_read_b64_tr_b16 v[88:89], v5 offset:0\n"
  "s_waitcnt lgkmcnt(5)\n"
  "ds_read_b64_tr_b16 v[90:91], v5 offset:512\n"
  "s_waitcnt lgkmcnt(5)\n"
  "ds_read_b64_tr_b16 v[92:93], v5 offset:1024\n"
  "s_waitcnt lgkmcnt(5)\n"
  "ds_read_b64_tr_b16 v[94:95], v5 offset:1536\n"
  "s_waitcnt vmcnt(0)\n"
  "TGO0:\n"
  "s_waitcnt lgkmcnt(3)\n"
  "v_mfma_f32_16x16x16_bf16 v[132:135], v[88:89], v[16:17], 0\n"
  "global_load_dword v9, v1, s[36:37] sc1\n"
  "v_mfma_f32_16x16x16_bf16 v[140:143], v[88:89], v[18:19], 0\n"
  "global_load_dwordx4 v[48:51], v0, s[62:63] offset:-4096 sc1\n"
  "s_waitcnt lgkmcnt(2)\n"
  "v_mfma_f32_16x16x16_bf16 v[132:135], v[90:91], v[24:25], v[132:135]\n"
  "global_load_dwordx4 v[52:55], v0, s[62:63] offset:-3072 sc1\n"
  "v_mfma_f32_16x16x16_bf16 v[140:143], v[90:91], v[26:27], v[140:143]\n"
  "global_load_dwordx4 v[56:59], v0, s[62:63] offset:-2048 sc1\n"
  "s_waitcnt lgkmcnt(1)\n"
  "v_mfma_f32_16x16x16_bf16 v[132:135], v[92:93], v[32:33], v[132:135]\n"
  "global_load_dwordx4 v[60:63], v0, s[62:63] offset:-1024 sc1\n"
  "v_mfma_f32_16x16x16_bf16 v[140:143], v[92:93], v[34:35], v[140:143]\n"
  "global_load_dwordx4 v[64:67], v0, s[62:63] offset:0 sc1\n"
  "s_waitcnt lgkmcnt(0)\n"
  "v_mfma_f32_16x16x16_bf16 v[132:135], v[94:95], v[40:41], v[132:135]\n"
  "global_load_dwordx4 v[68:71], v0, s[62:63] offset:1024 sc1\n"
  "v_mfma_f32_16x16x16_bf16 v[140:143], v[94:95], v[42:43], v[140:143]\n"
  "global_load_dwordx4 v[72:75], v0, s[62:63] offset:2048 sc1\n"
  "v_mfma_f32_16x16x16_bf16 v[136:139], v[88:89], v[20:21], 0\n"
  "global_load_dwordx4 v[76:79], v0, s[62:63] offset:3072 sc1\n"
  "v_mfma_f32_16x16x16_bf16 v[144:147], v[88:89], v[22:23], 0\n"
  "global_load_dwordx4 a[0:3], v0, s[60:61] offset:-4096 sc1\n"
  "v_mfma_f32_16x16x16_bf16 v[136:139], v[90:91], v[28:29], v[136:139]\n"
  "global_load_dwordx4 a[4:7], v0, s[60:61] offset:-3072 sc1\n"
  "v_mfma_f32_16x16x16_bf16 v[144:147], v[90:91], v[30:31], v[144:147]\n"
  "global_load_dwordx4 a[8:11], v0, s[60:61] offset:-2048 sc1\n"
  "v_mfma_f32_16x16x16_bf16 v[136:139], v[92:93], v[36:37], v[136:139]\n"
  "v_cndmask_b32 v120, v12, v120, s[8:9]\n"
  "v_cndmask_b32 v121, v12, v121, s[10:11]\n"
  "v_mfma_f32_16x16x16_bf16 v[144:147], v[92:93], v[38:39], v[144:147]\n"
  "v_cndmask_b32 v122, v12, v122, s[12:13]\n"
  "v_cndmask_b32 v123, v12, v123, s[14:15]\n"
  "v_mfma_f32_16x16x16_bf16 v[136:139], v[94:95], v[44:45], v[136:139]\n"
  "v_cndmask_b32 v128, v12, v128, s[8:9]\n"
  "v_cndmask_b32 v129, v12, v129, s[10:11]\n"
  "v_mfma_f32_16x16x16_bf16 v[144:147], v[94:95], v[46:47], v[144:147]\n"
  "v_cndmask_b32 v130, v12, v130, s[12:13]\n"
  "v_cndmask_b32 v131, v12, v131, s[14:15]\n"
  "v_add_f32 v132, v132, v142\n"
  "v_add_f32 v133, v133, v143\n"
  "v_mul_f32 v132, s50, v132\n"
  "v_mul_f32 v133, s50, v133\n"
  "v_max_f32 v10, v132, v133\n"
  "v_max3_f32 v10, v10, v120, v121\n"
  "v_max3_f32 v10, v10, v122, v123\n"
  "v_mov_b32 v11, v10\n"
  "s_nop 1\n"
  "v_permlane16_swap_b32 v10, v11\n"
  "v_max_f32 v10, v10, v11\n"
  "v_mov_b32 v11, v10\n"
  "s_nop 1\n"
  "v_permlane32_swap_b32 v10, v11\n"
  "v_max_f32 v10, v10, v11\n"
  "v_sub_f32 v120, v120, v10\n"
  "v_sub_f32 v121, v121, v10\n"
  "v_sub_f32 v122, v122, v10\n"
  "v_sub_f32 v123, v123, v10\n"
  "v_sub_f32 v132, v132, v10\n"
  "v_sub_f32 v133, v133, v10\n"
  "v_exp_f32 v120, v120\n"
  "v_exp_f32 v121, v121\n"
  "v_exp_f32 v122, v122\n"
  "v_exp_f32 v123, v123\n"
  "v_exp_f32 v132, v132\n"
  "v_exp_f32 v133, v133\n"
  "v_add_f32 v10, v120, v121\n"
  "v_add_f32 v10, v10, v122\n"
  "v_add_f32 v10, v10, v123\n"
  "v_add_f32 v10, v10, v132\n"
  "v_add_f32 v10, v10, v133\n"
  "v_mov_b32 v11, v10\n"
  "s_nop 1\n"
  "v_permlane16_swap_b32 v10, v11\n"
  "v_add_f32 v10, v10, v11\n"
  "v_mov_b32 v11, v10\n"
  "s_nop 1\n"
  "v_permlane32_swap_b32 v10, v11\n"
  "v_add_f32 v10, v10, v11\n"
  "v_rcp_f32 v10, v10\n"
  "s_nop 0\n"
  "v_mul_f32 v120, v10, v120\n"
  "v_mul_f32 v121, v10, v121\n"
  "v_mul_f32 v122, v10, v122\n"
  "v_mul_f32 v123, v10, v123\n"
  "v_mul_f32 v132, v10, v132\n"
  "v_mul_f32 v133, v10, v133\n"
  "v_cvt_pk_bf16_f32 v148, v120, v121\n"
  "v_cvt_pk_bf16_f32 v149, v122, v123\n"
  "v_cvt_pk_bf16_f32 v156, v132, v133\n"
  "v_cvt_pk_bf16_f32 v161, v132, v133\n"
  "v_cvt_pk_bf16_f32 v166, v132, v133\n"
  "v_cvt_pk_bf16_f32 v171, v132, v133\n"
  "v_add_u32 v7, s56, v2\n"
  "v_add_u32 v7, 1, v7\n"
  "v_min_i32 v7, 64, v7\n"
  "s_waitcnt vmcnt(11)\n"
  "v_cmp_gt_i32 s[16:17], v7, v9\n"
  "v_mov_b32 v7, 0\n"
  "v_mov_b32 v8, s56\n"
  "s_mov_b64 exec, 1\n"
  "global_store_dword v7, v8, s[70:71] sc1\n"
  "s_mov_b64 exec, -1\n"
  "s_cmp_lg_u64 s[16:17], 0\n"
  "s_cbranch_scc1 TNM0\n"
  "global_load_dwordx4 v[80:83], v0, s[64:65] offset:0 sc1\n"
  "global_load_dwordx4 v[84:87], v0, s[64:65] offset:1024 sc1\n"
  "global_load_dwordx4 v[104:107], v3, s[66:67] offset:0 sc1\n"
  "global_load_dwordx4 v[108:111], v3, s[66:67] offset:1024 sc1\n"
  "s_add_u32 s64, s64, 65536\n"
  "s_addc_u32 s65, s65, 0\n"
  "s_add_u32 s66, s66, 65536\n"
  "s_addc_u32 s67, s67, 0\n"
  "TNM0:\n"
  "v_mfma_f32_16x16x32_bf16 v[188:191], a[32:35], v[148:151], 0\n"
  "global_load_dwordx4 a[12:15], v0, s[60:61] offset:-1024 sc1\n"
  "v_mfma_f32_16x16x32_bf16 v[188:191], v[96:99], v[156:159], v[188:191]\n"
  "global_load_dwordx4 a[16:19], v0, s[60:61] offset:0 sc1\n"
  "v_mfma_f32_16x16x32_bf16 v[192:195], a[36:39], v[148:151], 0\n"
  "global_load_dwordx4 a[20:23], v0, s[60:61] offset:1024 sc1\n"
  "v_mfma_f32_16x16x32_bf16 v[192:195], v[96:99], v[160:163], v[192:195]\n"
  "global_load_dwordx4 a[24:27], v0, s[60:61] offset:2048 sc1\n"
  "v_mfma_f32_16x16x32_bf16 v[196:199], a[40:43], v[148:151], 0\n"
  "global_load_dwordx4 a[28:31], v0, s[60:61] offset:3072 sc1\n"
  "v_mfma_f32_16x16x32_bf16 v[196:199], v[96:99], v[164:167], v[196:199]\n"
  "s_add_u32 s60, s60, 16384\n"
  "s_addc_u32 s61, s61, 0\n"
  "global_load_dwordx4 a[64:67], v0, s[60:61] offset:-4096 sc1\n"
  "v_mfma_f32_16x16x32_bf16 v[200:203], a[44:47], v[148:151], 0\n"
  "global_load_dwordx4 a[68:71], v0, s[60:61] offset:-3072 sc1\n"
  "v_mfma_f32_16x16x32_bf16 v[200:203], v[96:99], v[168:171], v[200:203]\n"
  "global_load_dwordx4 a[72:75], v0, s[60:61] offset:-2048 sc1\n"
  "v_mfma_f32_16x16x32_bf16 v[204:207], a[48:51], v[148:151], 0\n"
  "global_load_dwordx4 a[76:79], v0, s[60:61] offset:-1024 sc1\n"
  "v_mfma_f32_16x16x32_bf16 v[204:207], v[100:103], v[156:159], v[204:207]\n"
  "global_load_dwordx4 a[80:83], v0, s[60:61] offset:0 sc1\n"
  "v_mfma_f32_16x16x32_bf16 v[208:211], a[52:55], v[148:151], 0\n"
  "global_load_dwordx4 a[84:87], v0, s[60:61] offset:1024 sc1\n"
  "v_mfma_f32_16x16x32_bf16 v[208:211], v[100:103], v[160:163], v[208:211]\n"
  "global_load_dwordx4 a[88:91], v0, s[60:61] offset:2048 sc1\n"
  "v_mfma_f32_16x16x32_bf16 v[212:215], a[56:59], v[148:151], 0\n"
  "global_load_dwordx4 a[92:95], v0, s[60:61] offset:3072 sc1\n"
  "v_mfma_f32_16x16x32_bf16 v[212:215], v[100:103], v[164:167], v[212:215]\n"
  "v_mfma_f32_16x16x32_bf16 v[216:219], a[60:63], v[148:151], 0\n"
  "v_mfma_f32_16x16x32_bf16 v[216:219], v[100:103], v[168:171], v[216:219]\n"
  "s_add_u32 s76, s56, 2\n"
  "s_min_u32 s76, s76, 63\n"
  "s_bitcmp1_b64 s[4:5], s76\n"
  "s_cselect_b32 s74, 8192, 0\n"
  "s_and_b32 s75, s76, 63\n"
  "s_lshl_b32 s75, s75, 21\n"
  "s_add_u32 s74, s74, s75\n"
  "s_add_u32 s74, s74, 20971520\n"
  "s_xor_b32 s75, s74, s44\n"
  "s_add_u32 s60, s36, s75\n"
  "s_addc_u32 s61, s37, 0\n"
  "s_xor_b32 s75, s74, s45\n"
  "s_add_u32 s62, s36, s75\n"
  "s_addc_u32 s63, s37, 0\n"
  "s_bitcmp1_b64 s[4:5], s56\n"
  "s_cselect_b32 s74, 8192, 0\n"
  "s_and_b32 s75, s56, 63\n"
  "s_lshl_b32 s75, s75, 21\n"
  "s_add_u32 s74, s74, s75\n"
  "s_add_u32 s74, s74, 20971520\n"
  "s_xor_b32 s75, s74, s48\n"
  "s_add_u32 s68, s36, s75\n"
  "s_addc_u32 s69, s37, 0\n"
  "v_add_f32 v136, v136, v146\n"
  "v_add_f32 v137, v137, v147\n"
  "v_mul_f32 v136, s50, v136\n"
  "v_mul_f32 v137, s50, v137\n"
  "v_max_f32 v10, v136, v137\n"
  "v_max3_f32 v10, v10, v124, v125\n"
  "v_max3_f32 v10, v10, v126, v127\n"
  "v_max3_f32 v10, v10, v128, v129\n"
  "v_max3_f32 v10, v10, v130, v131\n"
  "v_mov_b32 v11, v10\n"
  "s_nop 1\n"
  "v_permlane16_swap_b32 v10, v11\n"
  "v_max_f32 v10, v10, v11\n"
  "v_mov_b32 v11, v10\n"
  "s_nop 1\n"
  "v_permlane32_swap_b32 v10, v11\n"
  "v_max_f32 v10, v10, v11\n"
  "v_sub_f32 v124, v124, v10\n"
  "v_sub_f32 v125, v125, v10\n"
  "v_sub_f32 v126, v126, v10\n"
  "v_sub_f32 v127, v127, v10\n"
  "v_sub_f32 v128, v128, v10\n"
  "v_sub_f32 v129, v129, v10\n"
  "v_sub_f32 v130, v130, v10\n"
  "v_sub_f32 v131, v131, v10\n"
  "v_sub_f32 v136, v136, v10\n"
  "v_sub_f32 v137, v137, v10\n"
  "v_exp_f32 v124, v124\n"
  "v_exp_f32 v125, v125\n"
  "v_exp_f32 v126, v126\n"
  "v_exp_f32 v127, v127\n"
  "v_exp_f32 v128, v128\n"
  "v_exp_f32 v129, v129\n"
  "v_exp_f32 v130, v130\n"
  "v_exp_f32 v131, v131\n"
  "v_exp_f32 v136, v136\n"
  "v_exp_f32 v137, v137\n"
  "v_add_f32 v10, v124, v125\n"
  "v_add_f32 v10, v10, v126\n"
  "v_add_f32 v10, v10, v127\n"
  "v_add_f32 v10, v10, v128\n"
  "v_add_f32 v10, v10, v129\n"
  "v_add_f32 v10, v10, v130\n"
  "v_add_f32 v10, v10, v131\n"
  "v_add_f32 v10, v10, v136\n"
  "v_add_f32 v10, v10, v137\n"
  "v_mov_b32 v11, v10\n"
  "s_nop 1\n"
  "v_permlane16_swap_b32 v10, v11\n"
  "v_add_f32 v10, v10, v11\n"
  "v_mov_b32 v11, v10\n"
  "s_nop 1\n"
  "v_permlane32_swap_b32 v10, v11\n"
  "v_add_f32 v10, v10, v11\n"
  "v_rcp_f32 v10, v10\n"
  "s_nop 0\n"
  "v_mul_f32 v124, v10, v124\n"
  "v_mul_f32 v125, v10, v125\n"
  "v_mul_f32 v126, v10, v126\n"
  "v_mul_f32 v127, v10, v127\n"
  "v_mul_f32 v128, v10, v128\n"
  "v_mul_f32 v129, v10, v129\n"
  "v_mul_f32 v130, v10, v130\n"
  "v_mul_f32 v131, v10, v131\n"
  "v_mul_f32 v136, v10, v136\n"
  "v_mul_f32 v137, v10, v137\n"
  "v_cvt_pk_bf16_f32 v152, v124, v125\n"
  "v_cvt_pk_bf16_f32 v153, v126, v127\n"
  "v_cvt_pk_bf16_f32 v154, v128, v129\n"
  "v_cvt_pk_bf16_f32 v155, v130, v131\n"
  "v_cvt_pk_bf16_f32 v172, v136, v137\n"
  "v_cvt_pk_bf16_f32 v177, v136, v137\n"
  "v_cvt_pk_bf16_f32 v182, v136, v137\n"
  "v_cvt_pk_bf16_f32 v187, v136, v137\n"
  "v_cvt_pk_bf16_f32 v220, v188, v189\n"
  "v_cvt_pk_bf16_f32 v221, v190, v191\n"
  "v_cvt_pk_bf16_f32 v222, v192, v193\n"
  "v_cvt_pk_bf16_f32 v223, v194, v195\n"
  "global_store_dwordx4 v0, v[220:223], s[68:69] offset:-4096 sc1\n"
  "v_mfma_f32_16x16x32_bf16 v[188:191], a[32:35], v[152:155], 0\n"
  "v_cvt_pk_bf16_f32 v224, v196, v197\n"
  "v_cvt_pk_bf16_f32 v225, v198, v199\n"
  "v_mfma_f32_16x16x32_bf16 v[188:191], v[96:99], v[172:175], v[188:191]\n"
  "v_cvt_pk_bf16_f32 v226, v200, v201\n"
  "v_cvt_pk_bf16_f32 v227, v202, v203\n"
  "v_mfma_f32_16x16x32_bf16 v[192:195], a[36:39], v[152:155], 0\n"
  "global_store_dwordx4 v0, v[224:227], s[68:69] offset:-3072 sc1\n"
  "v_mfma_f32_16x16x32_bf16 v[192:195], v[96:99], v[176:179], v[192:195]\n"
  "v_mfma_f32_16x16x32_bf16 v[196:199], a[40:43], v[152:155], 0\n"
  "v_cvt_pk_bf16_f32 v228, v204, v205\n"
  "v_cvt_pk_bf16_f32 v229, v206, v207\n"
  "v_mfma_f32_16x16x32_bf16 v[196:199], v[96:99], v[180:183], v[196:199]\n"
  "v_cvt_pk_bf16_f32 v230, v208, v209\n"
  "v_cvt_pk_bf16_f32 v231, v210, v211\n"
  "v_mfma_f32_16x16x32_bf16 v[200:203], a[44:47], v[152:155], 0\n"
  "global_store_dwordx4 v0, v[228:231], s[68:69] offset:-2048 sc1\n"
  "v_mfma_f32_16x16x32_bf16 v[200:203], v[96:99], v[184:187], v[200:203]\n"
  "v_mfma_f32_16x16x32_bf16 v[204:207], a[48:51], v[152:155], 0\n"
  "v_cvt_pk_bf16_f32 v232, v212, v213\n"
  "v_cvt_pk_bf16_f32 v233, v214, v215\n"
  "v_mfma_f32_16x16x32_bf16 v[204:207], v[100:103], v[172:175], v[204:207]\n"
  "v_cvt_pk_bf16_f32 v234, v216, v217\n"
  "v_cvt_pk_bf16_f32 v235, v218, v219\n"
  "v_mfma_f32_16x16x32_bf16 v[208:211], a[52:55], v[152:155], 0\n"
  "global_store_dwordx4 v0, v[232:235], s[68:69] offset:-1024 sc1\n"
  "v_mfma_f32_16x16x32_bf16 v[208:211], v[100:103], v[176:179], v[208:211]\n"
  "v_mfma_f32_16x16x32_bf16 v[212:215], a[56:59], v[152:155], 0\n"
  "v_cvt_pk_bf16_f32 v220, v188, v189\n"
  "v_cvt_pk_bf16_f32 v221, v190, v191\n"
  "v_mfma_f32_16x16x32_bf16 v[212:215], v[100:103], v[180:183], v[212:215]\n"
  "v_cvt_pk_bf16_f32 v222, v192, v193\n"
  "v_cvt_pk_bf16_f32 v223, v194, v195\n"
  "v_mfma_f32_16x16x32_bf16 v[216:219], a[60:63], v[152:155], 0\n"
  "global_store_dwordx4 v0, v[220:223], s[68:69] offset:0 sc1\n"
  "v_mfma_f32_16x16x32_bf16 v[216:219], v[100:103], v[184:187], v[216:219]\n"
  "v_cvt_pk_bf16_f32 v224, v196, v197\n"
  "v_cvt_pk_bf16_f32 v225, v198, v199\n"
  "v_cvt_pk_bf16_f32 v226, v200, v201\n"
  "v_cvt_pk_bf16_f32 v227, v202, v203\n"
  "global_store_dwordx4 v0, v[224:227], s[68:69] offset:1024 sc1\n"
  "v_cvt_pk_bf16_f32 v228, v204, v205\n"
  "v_cvt_pk_bf16_f32 v229, v206, v207\n"
  "v_cvt_pk_bf16_f32 v230, v208, v209\n"
  "v_cvt_pk_bf16_f32 v231, v210, v211\n"
  "global_store_dwordx4 v0, v[228:231], s[68:69] offset:2048 sc1\n"
  "v_cvt_pk_bf16_f32 v232, v212, v213\n"
  "v_cvt_pk_bf16_f32 v233, v214, v215\n"
  "v_cvt_pk_bf16_f32 v234, v216, v217\n"
  "v_cvt_pk_bf16_f32 v235, v218, v219\n"
  "global_store_dwordx4 v0, v[232:235], s[68:69] offset:3072 sc1\n"
  "s_add_u32 s56, s56, 1\n"
  "s_cmp_lt_u32 s56, s53\n"
  "s_cbranch_scc0 TEND\n"
  "TOP1:\n"
  "s_waitcnt vmcnt(8)\n"
  "ds_write_b128 v4, v[80:83] offset:0\n"
  "ds_write_b128 v4, v[84:87] offset:1024\n"
  "ds_read_b64_tr_b16 v[88:89], v5 offset:0\n"
  "ds_read_b64_tr_b16 v[90:91], v5 offset:512\n"
  "ds_read_b64_tr_b16 v[92:93], v5 offset:1024\n"
  "ds_read_b64_tr_b16 v[94:95], v5 offset:1536\n"
  "v_mfma_f32_16x16x32_bf16 v[120:123], a[0:3], v[48:51], 0\n"
  "v_mfma_f32_16x16x32_bf16 v[124:127], a[0:3], v[52:55], 0\n"
  "v_mfma_f32_16x16x32_bf16 v[128:131], a[4:7], v[52:55], 0\n"
  "v_mfma_f32_16x16x32_bf16 v[120:123], a[8:11], v[56:59], v[120:123]\n"
  "v_mfma_f32_16x16x32_bf16 v[124:127], a[8:11], v[60:63], v[124:127]\n"
  "v_mfma_f32_16x16x32_bf16 v[128:131], a[12:15], v[60:63], v[128:131]\n"
  "v_mfma_f32_16x16x32_bf16 v[120:123], a[16:19], v[64:67], v[120:123]\n"
  "v_mfma_f32_16x16x32_bf16 v[124:127], a[16:19], v[68:71], v[124:127]\n"
  "v_mfma_f32_16x16x32_bf16 v[128:131], a[20:23], v[68:71], v[128:131]\n"
  "v_mfma_f32_16x16x32_bf16 v[120:123], a[24:27], v[72:75], v[120:123]\n"
  "v_mfma_f32_16x16x32_bf16 v[124:127], a[24:27], v[76:79], v[124:127]\n"
  "v_mfma_f32_16x16x32_bf16 v[128:131], a[28:31], v[76:79], v[128:131]\n"
  "s_cmp_eq_u64 s[16:17], 0\n"
  "s_cbranch_scc1 TGO1\n"
  "s_waitcnt vmcnt(0)\n"
  "v_mov_b32 v7, 0\n"
  "v_mov_b32 v8, s56\n"
  "s_mov_b64 exec, 1\n"
  "global_store_dword v7, v8, s[70:71] sc1\n"
  "s_mov_b64 exec, -1\n"
  "TP1:\n"
  "global_load_dword v9, v1, s[36:37] sc1\n"
  "v_add_u32 v7, s56, v2\n"
  "v_add_u32 v7, 0, v7\n"
  "v_min_i32 v7, 64, v7\n"
  "s_waitcnt vmcnt(0)\n"
  "v_cmp_gt_i32 vcc, v7, v9\n"
  "s_cbranch_vccz TG1\n"
  "s_sub_u32 s57, s57, 1\n"
  "s_cbranch_scc1 TQUIT\n"
  "s_sleep 2\n"
  "s_branch TP1\n"
  "TG1:\n"
  "global_load_dwordx4 v[80:83], v0, s[64:65] offset:0 sc1\n"
  "global_load_dwordx4 v[84:87], v0, s[64:65] offset:1024 sc1\n"
  "global_load_dwordx4 v[104:107], v3, s[66:67] offset:0 sc1\n"
  "global_load_dwordx4 v[108:111], v3, s[66:67] offset:1024 sc1\n"
  "s_add_u32 s64, s64, 65536\n"
  "s_addc_u32 s65, s65, 0\n"
  "s_add_u32 s66, s66, 65536\n"
  "s_addc_u32 s67, s67, 0\n"
  "s_waitcnt vmcnt(3)\n"
  "ds_write_b128 v4, v[80:83] offset:0\n"
  "s_waitcnt vmcnt(2)\n"
  "ds_write_b128 v4, v[84:87] offset:1024\n"
  "s_waitcnt lgkmcnt(5)\n"
  "ds_read_b64_tr_b16 v[88:89], v5 offset:0\n"
  "s_waitcnt lgkmcnt(5)\n"
  "ds_read_b64_tr_b16 v[90:91], v5 offset:512\n"
  "s_waitcnt lgkmcnt(5)\n"
  "ds_read_b64_tr_b16 v[92:93], v5 offset:1024\n"
  "s_waitcnt lgkmcnt(5)\n"
  "ds_read_b64_tr_b16 v[94:95], v5 offset:1536\n"
  "s_waitcnt vmcnt(0)\n"
  "TGO1:\n"
  "s_waitcnt lgkmcnt(3)\n"
  "v_mfma_f32_16x16x16_bf16 v[132:135], v[88:89], v[48:49], 0\n"
  "global_load_dword v9, v1, s[36:37] sc1\n"
  "v_mfma_f32_16x16x16_bf16 v[140:143], v[88:89], v[50:51], 0\n"
  "global_load_dwordx4 v[16:19], v0, s[62:63] offset:-4096 sc1\n"
  "s_waitcnt lgkmcnt(2)\n"
  "v_mfma_f32_16x16x16_bf16 v[132:135], v[90:91], v[56:57], v[132:135]\n"
  "global_load_dwordx4 v[20:23], v0, s[62:63] offset:-3072 sc1\n"
  "v_mfma_f32_16x16x16_bf16 v[140:143], v[90:91], v[58:59], v[140:143]\n"
  "global_load_dwordx4 v[24:27], v0, s[62:63] offset:-2048 sc1\n"
  "s_waitcnt lgkmcnt(1)\n"
  "v_mfma_f32_16x16x16_bf16 v[132:135], v[92:93], v[64:65], v[132:135]\n"
  "global_load_dwordx4 v[28:31], v0, s[62:63] offset:-1024 sc1\n"
  "v_mfma_f32_16x16x16_bf16 v[140:143], v[92:93], v[66:67], v[140:143]\n"
  "global_load_dwordx4 v[32:35], v0, s[62:63] offset:0 sc1\n"
  "s_waitcnt lgkmcnt(0)\n"
  "v_mfma_f32_16x16x16_bf16 v[132:135], v[94:95], v[72:73], v[132:135]\n"
  "global_load_dwordx4 v[36:39], v0, s[62:63] offset:1024 sc1\n"
  "v_mfma_f32_16x16x16_bf16 v[140:143], v[94:95], v[74:75], v[140:143]\n"
  "global_load_dwordx4 v[40:43], v0, s[62:63] offset:2048 sc1\n"
  "v_mfma_f32_16x16x16_bf16 v[136:139], v[88:89], v[52:53], 0\n"
  "global_load_dwordx4 v[44:47], v0, s[62:63] offset:3072 sc1\n"
  "v_mfma_f32_16x16x16_bf16 v[144:147], v[88:89], v[54:55], 0\n"
  "global_load_dwordx4 a[0:3], v0, s[60:61] offset:-4096 sc1\n"
  "v_mfma_f32_16x16x16_bf16 v[136:139], v[90:91], v[60:61], v[136:139]\n"
  "global_load_dwordx4 a[4:7], v0, s[60:61] offset:-3072 sc1\n"
  "v_mfma_f32_16x16x16_bf16 v[144:147], v[90:91], v[62:63], v[144:147]\n"
  "global_load_dwordx4 a[8:11], v0, s[60:61] offset:-2048 sc1\n"
  "v_mfma_f32_16x16x16_bf16 v[136:139], v[92:93], v[68:69], v[136:139]\n"
  "v_cndmask_b32 v120, v12, v120, s[8:9]\n"
  "v_cndmask_b32 v121, v12, v121, s[10:11]\n"
  "v_mfma_f32_16x16x16_bf16 v[144:147], v[92:93], v[70:71], v[144:147]\n"
  "v_cndmask_b32 v122, v12, v122, s[12:13]\n"
  "v_cndmask_b32 v123, v12, v123, s[14:15]\n"
  "v_mfma_f32_16x16x16_bf16 v[136:139], v[94:95], v[76:77], v[136:139]\n"
  "v_cndmask_b32 v128, v12, v128, s[8:9]\n"
  "v_cndmask_b32 v129, v12, v129, s[10:11]\n"
  "v_mfma_f32_16x16x16_bf16 v[144:147], v[94:95], v[78:79], v[144:147]\n"
  "v_cndmask_b32 v130, v12, v130, s[12:13]\n"
  "v_cndmask_b32 v131, v12, v131, s[14:15]\n"
  "v_add_f32 v132, v132, v142\n"
  "v_add_f32 v133, v133, v143\n"
  "v_mul_f32 v132, s50, v132\n"
  "v_mul_f32 v133, s50, v133\n"
  "v_max_f32 v10, v132, v133\n"
  "v_max3_f32 v10, v10, v120, v121\n"
  "v_max3_f32 v10, v10, v122, v123\n"
  "v_mov_b32 v11, v10\n"
  "s_nop 1\n"
  "v_permlane16_swap_b32 v10, v11\n"
  "v_max_f32 v10, v10, v11\n"
  "v_mov_b32 v11, v10\n"
  "s_nop 1\n"
  "v_permlane32_swap_b32 v10, v11\n"
  "v_max_f32 v10, v10, v11\n"
  "v_sub_f32 v120, v120, v10\n"
  "v_sub_f32 v121, v121, v10\n"
  "v_sub_f32 v122, v122, v10\n"
  "v_sub_f32 v123, v123, v10\n"
  "v_sub_f32 v132, v132, v10\n"
  "v_sub_f32 v133, v133, v10\n"
  "v_exp_f32 v120, v120\n"
  "v_exp_f32 v121, v121\n"
  "v_exp_f32 v122, v122\n"
  "v_exp_f32 v123, v123\n"
  "v_exp_f32 v132, v132\n"
  "v_exp_f32 v133, v133\n"
  "v_add_f32 v10, v120, v121\n"
  "v_add_f32 v10, v10, v122\n"
  "v_add_f32 v10, v10, v123\n"
  "v_add_f32 v10, v10, v132\n"
  "v_add_f32 v10, v10, v133\n"
  "v_mov_b32 v11, v10\n"
  "s_nop 1\n"
  "v_permlane16_swap_b32 v10, v11\n"
  "v_add_f32 v10, v10, v11\n"
  "v_mov_b32 v11, v10\n"
  "s_nop 1\n"
  "v_permlane32_swap_b32 v10, v11\n"
  "v_add_f32 v10, v10, v11\n"
  "v_rcp_f32 v10, v10\n"
  "s_nop 0\n"
  "v_mul_f32 v120, v10, v120\n"
  "v_mul_f32 v121, v10, v121\n"
  "v_mul_f32 v122, v10, v122\n"
  "v_mul_f32 v123, v10, v123\n"
  "v_mul_f32 v132, v10, v132\n"
  "v_mul_f32 v133, v10, v133\n"
  "v_cvt_pk_bf16_f32 v148, v120, v121\n"
  "v_cvt_pk_bf16_f32 v149, v122, v123\n"
  "v_cvt_pk_bf16_f32 v156, v132, v133\n"
  "v_cvt_pk_bf16_f32 v161, v132, v133\n"
  "v_cvt_pk_bf16_f32 v166, v132, v133\n"
  "v_cvt_pk_bf16_f32 v171, v132, v133\n"
  "v_add_u32 v7, s56, v2\n"
  "v_add_u32 v7, 1, v7\n"
  "v_min_i32 v7, 64, v7\n"
  "s_waitcnt vmcnt(11)\n"
  "v_cmp_gt_i32 s[16:17], v7, v9\n"
  "v_mov_b32 v7, 0\n"
  "v_mov_b32 v8, s56\n"
  "s_mov_b64 exec, 1\n"
  "global_store_dword v7, v8, s[70:71] sc1\n"
  "s_mov_b64 exec, -1\n"
  "s_cmp_lg_u64 s[16:17], 0\n"
  "s_cbranch_scc1 TNM1\n"
  "global_load_dwordx4 v[80:83], v0, s[64:65] offset:0 sc1\n"
  "global_load_dwordx4 v[84:87], v0, s[64:65] offset:1024 sc1\n"
  "global_load_dwordx4 v[96:99], v3, s[66:67] offset:0 sc1\n"
  "global_load_dwordx4 v[100:103], v3, s[66:67] offset:1024 sc1\n"
  "s_add_u32 s64, s64, 65536\n"
  "s_addc_u32 s65, s65, 0\n"
  "s_add_u32 s66, s66, 65536\n"
  "s_addc_u32 s67, s67, 0\n"
  "TNM1:\n"
  "v_mfma_f32_16x16x32_bf16 v[188:191], a[64:67], v[148:151], 0\n"
  "global_load_dwordx4 a[12:15], v0, s[60:61] offset:-1024 sc1\n"
  "v_mfma_f32_16x16x32_bf16 v[188:191], v[104:107], v[156:159], v[188:191]\n"
  "global_load_dwordx4 a[16:19], v0, s[60:61] offset:0 sc1\n"
  "v_mfma_f32_16x16x32_bf16 v[192:195], a[68:71], v[148:151], 0\n"
  "global_load_dwordx4 a[20:23], v0, s[60:61] offset:1024 sc1\n"
  "v_mfma_f32_16x16x32_bf16 v[192:195], v[104:107], v[160:163], v[192:195]\n"
  "global_load_dwordx4 a[24:27], v0, s[60:61] offset:2048 sc1\n"
  "v_mfma_f32_16x16x32_bf16 v[196:199], a[72:75], v[148:151], 0\n"
  "global_load_dwordx4 a[28:31], v0, s[60:61] offset:3072 sc1\n"
  "v_mfma_f32_16x16x32_bf16 v[196:199], v[104:107], v[164:167], v[196:199]\n"
  "s_add_u32 s60, s60, 16384\n"
  "s_addc_u32 s61, s61, 0\n"
  "global_load_dwordx4 a[32:35], v0, s[60:61] offset:-4096 sc1\n"
  "v_mfma_f32_16x16x32_bf16 v[200:203], a[76:79], v[148:151], 0\n"
  "global_load_dwordx4 a[36:39], v0, s[60:61] offset:-3072 sc1\n"
  "v_mfma_f32_16x16x32_bf16 v[200:203], v[104:107], v[168:171], v[200:203]\n"
  "global_load_dwordx4 a[40:43], v0, s[60:61] offset:-2048 sc1\n"
  "v_mfma_f32_16x16x32_bf16 v[204:207], a[80:83], v[148:151], 0\n"
  "global_load_dwordx4 a[44:47], v0, s[60:61] offset:-1024 sc1\n"
  "v_mfma_f32_16x16x32_bf16 v[204:207], v[108:111], v[156:159], v[204:207]\n"
  "global_load_dwordx4 a[48:51], v0, s[60:61] offset:0 sc1\n"
  "v_mfma_f32_16x16x32_bf16 v[208:211], a[84:87], v[148:151], 0\n"
  "global_load_dwordx4 a[52:55], v0, s[60:61] offset:1024 sc1\n"
  "v_mfma_f32_16x16x32_bf16 v[208:211], v[108:111], v[160:163], v[208:211]\n"
  "global_load_dwordx4 a[56:59], v0, s[60:61] offset:2048 sc1\n"
  "v_mfma_f32_16x16x32_bf16 v[212:215], a[88:91], v[148:151], 0\n"
  "global_load_dwordx4 a[60:63], v0, s[60:61] offset:3072 sc1\n"
  "v_mfma_f32_16x16x32_bf16 v[212:215], v[108:111], v[164:167], v[212:215]\n"
  "v_mfma_f32_16x16x32_bf16 v[216:219], a[92:95], v[148:151], 0\n"
  "v_mfma_f32_16x16x32_bf16 v[216:219], v[108:111], v[168:171], v[216:219]\n"
  "s_add_u32 s76, s56, 2\n"
  "s_min_u32 s76, s76, 63\n"
  "s_bitcmp1_b64 s[4:5], s76\n"
  "s_cselect_b32 s74, 8192, 0\n"
  "s_and_b32 s75, s76, 63\n"
  "s_lshl_b32 s75, s75, 21\n"
  "s_add_u32 s74, s74, s75\n"
  "s_add_u32 s74, s74, 20971520\n"
  "s_xor_b32 s75, s74, s44\n"
  "s_add_u32 s60, s36, s75\n"
  "s_addc_u32 s61, s37, 0\n"
  "s_xor_b32 s75, s74, s45\n"
  "s_add_u32 s62, s36, s75\n"
  "s_addc_u32 s63, s37, 0\n"
  "s_bitcmp1_b64 s[4:5], s56\n"
  "s_cselect_b32 s74, 8192, 0\n"
  "s_and_b32 s75, s56, 63\n"
  "s_lshl_b32 s75, s75, 21\n"
  "s_add_u32 s74, s74, s75\n"
  "s_add_u32 s74, s74, 20971520\n"
  "s_xor_b32 s75, s74, s48\n"
  "s_add_u32 s68, s36, s75\n"
  "s_addc_u32 s69, s37, 0\n"
  "v_add_f32 v136, v136, v146\n"
  "v_add_f32 v137, v137, v147\n"
  "v_mul_f32 v136, s50, v136\n"
  "v_mul_f32 v137, s50, v137\n"
  "v_max_f32 v10, v136, v137\n"
  "v_max3_f32 v10, v10, v124, v125\n"
  "v_max3_f32 v10, v10, v126, v127\n"
  "v_max3_f32 v10, v10, v128, v129\n"
  "v_max3_f32 v10, v10, v130, v131\n"
  "v_mov_b32 v11, v10\n"
  "s_nop 1\n"
  "v_permlane16_swap_b32 v10, v11\n"
  "v_max_f32 v10, v10, v11\n"
  "v_mov_b32 v11, v10\n"
  "s_nop 1\n"
  "v_permlane32_swap_b32 v10, v11\n"
  "v_max_f32 v10, v10, v11\n"
  "v_sub_f32 v124, v124, v10\n"
  "v_sub_f32 v125, v125, v10\n"
  "v_sub_f32 v126, v126, v10\n"
  "v_sub_f32 v127, v127, v10\n"
  "v_sub_f32 v128, v128, v10\n"
  "v_sub_f32 v129, v129, v10\n"
  "v_sub_f32 v130, v130, v10\n"
  "v_sub_f32 v131, v131, v10\n"
  "v_sub_f32 v136, v136, v10\n"
  "v_sub_f32 v137, v137, v10\n"
  "v_exp_f32 v124, v124\n"
  "v_exp_f32 v125, v125\n"
  "v_exp_f32 v126, v126\n"
  "v_exp_f32 v127, v127\n"
  "v_exp_f32 v128, v128\n"
  "v_exp_f32 v129, v129\n"
  "v_exp_f32 v130, v130\n"
  "v_exp_f32 v131, v131\n"
  "v_exp_f32 v136, v136\n"
  "v_exp_f32 v137, v137\n"
  "v_add_f32 v10, v124, v125\n"
  "v_add_f32 v10, v10, v126\n"
  "v_add_f32 v10, v10, v127\n"
  "v_add_f32 v10, v10, v128\n"
  "v_add_f32 v10, v10, v129\n"
  "v_add_f32 v10, v10, v130\n"
  "v_add_f32 v10, v10, v131\n"
  "v_add_f32 v10, v10, v136\n"
  "v_add_f32 v10, v10, v137\n"
  "v_mov_b32 v11, v10\n"
  "s_nop 1\n"
  "v_permlane16_swap_b32 v10, v11\n"
  "v_add_f32 v10, v10, v11\n"
  "v_mov_b32 v11, v10\n"
  "s_nop 1\n"
  "v_permlane32_swap_b32 v10, v11\n"
  "v_add_f32 v10, v10, v11\n"
  "v_rcp_f32 v10, v10\n"
  "s_nop 0\n"
  "v_mul_f32 v124, v10, v124\n"
  "v_mul_f32 v125, v10, v125\n"
  "v_mul_f32 v126, v10, v126\n"
  "v_mul_f32 v127, v10, v127\n"
  "v_mul_f32 v128, v10, v128\n"
  "v_mul_f32 v129, v10, v129\n"
  "v_mul_f32 v130, v10, v130\n"
  "v_mul_f32 v131, v10, v131\n"
  "v_mul_f32 v136, v10, v136\n"
  "v_mul_f32 v137, v10, v137\n"
  "v_cvt_pk_bf16_f32 v152, v124, v125\n"
  "v_cvt_pk_bf16_f32 v153, v126, v127\n"
  "v_cvt_pk_bf16_f32 v154, v128, v129\n"
  "v_cvt_pk_bf16_f32 v155, v130, v131\n"
  "v_cvt_pk_bf16_f32 v172, v136, v137\n"
  "v_cvt_pk_bf16_f32 v177, v136, v137\n"
  "v_cvt_pk_bf16_f32 v182, v136, v137\n"
  "v_cvt_pk_bf16_f32 v187, v136, v137\n"
  "v_cvt_pk_bf16_f32 v220, v188, v189\n"
  "v_cvt_pk_bf16_f32 v221, v190, v191\n"
  "v_cvt_pk_bf16_f32 v222, v192, v193\n"
  "v_cvt_pk_bf16_f32 v223, v194, v195\n"
  "global_store_dwordx4 v0, v[220:223], s[68:69] offset:-4096 sc1\n"
  "v_mfma_f32_16x16x32_bf16 v[188:191], a[64:67], v[152:155], 0\n"
  "v_cvt_pk_bf16_f32 v224, v196, v197\n"
  "v_cvt_pk_bf16_f32 v225, v198, v199\n"
  "v_mfma_f32_16x16x32_bf16 v[188:191], v[104:107], v[172:175], v[188:191]\n"
  "v_cvt_pk_bf16_f32 v226, v200, v201\n"
  "v_cvt_pk_bf16_f32 v227, v202, v203\n"
  "v_mfma_f32_16x16x32_bf16 v[192:195], a[68:71], v[152:155], 0\n"
  "global_store_dwordx4 v0, v[224:227], s[68:69] offset:-3072 sc1\n"
  "v_mfma_f32_16x16x32_bf16 v[192:195], v[104:107], v[176:179], v[192:195]\n"
  "v_mfma_f32_16x16x32_bf16 v[196:199], a[72:75], v[152:155], 0\n"
  "v_cvt_pk_bf16_f32 v228, v204, v205\n"
  "v_cvt_pk_bf16_f32 v229, v206, v207\n"
  "v_mfma_f32_16x16x32_bf16 v[196:199], v[104:107], v[180:183], v[196:199]\n"
  "v_cvt_pk_bf16_f32 v230, v208, v209\n"
  "v_cvt_pk_bf16_f32 v231, v210, v211\n"
  "v_mfma_f32_16x16x32_bf16 v[200:203], a[76:79], v[152:155], 0\n"
  "global_store_dwordx4 v0, v[228:231], s[68:69] offset:-2048 sc1\n"
  "v_mfma_f32_16x16x32_bf16 v[200:203], v[104:107], v[184:187], v[200:203]\n"
  "v_mfma_f32_16x16x32_bf16 v[204:207], a[80:83], v[152:155], 0\n"
  "v_cvt_pk_bf16_f32 v232, v212, v213\n"
  "v_cvt_pk_bf16_f32 v233, v214, v215\n"
  "v_mfma_f32_16x16x32_bf16 v[204:207], v[108:111], v[172:175], v[204:207]\n"
  "v_cvt_pk_bf16_f32 v234, v216, v217\n"
  "v_cvt_pk_bf16_f32 v235, v218, v219\n"
  "v_mfma_f32_16x16x32_bf16 v[208:211], a[84:87], v[152:155], 0\n"
  "global_store_dwordx4 v0, v[232:235], s[68:69] offset:-1024 sc1\n"
  "v_mfma_f32_16x16x32_bf16 v[208:211], v[108:111], v[176:179], v[208:211]\n"
  "v_mfma_f32_16x16x32_bf16 v[212:215], a[88:91], v[152:155], 0\n"
  "v_cvt_pk_bf16_f32 v220, v188, v189\n"
  "v_cvt_pk_bf16_f32 v221, v190, v191\n"
  "v_mfma_f32_16x16x32_bf16 v[212:215], v[108:111], v[180:183], v[212:215]\n"
  "v_cvt_pk_bf16_f32 v222, v192, v193\n"
  "v_cvt_pk_bf16_f32 v223, v194, v195\n"
  "v_mfma_f32_16x16x32_bf16 v[216:219], a[92:95], v[152:155], 0\n"
  "global_store_dwordx4 v0, v[220:223], s[68:69] offset:0 sc1\n"
  "v_mfma_f32_16x16x32_bf16 v[216:219], v[108:111], v[184:187], v[216:219]\n"
  "v_cvt_pk_bf16_f32 v224, v196, v197\n"
  "v_cvt_pk_bf16_f32 v225, v198, v199\n"
  "v_cvt_pk_bf16_f32 v226, v200, v201\n"
  "v_cvt_pk_bf16_f32 v227, v202, v203\n"
  "global_store_dwordx4 v0, v[224:227], s[68:69] offset:1024 sc1\n"
  "v_cvt_pk_bf16_f32 v228, v204, v205\n"
  "v_cvt_pk_bf16_f32 v229, v206, v207\n"
  "v_cvt_pk_bf16_f32 v230, v208, v209\n"
  "v_cvt_pk_bf16_f32 v231, v210, v211\n"
  "global_store_dwordx4 v0, v[228:231], s[68:69] offset:2048 sc1\n"
  "v_cvt_pk_bf16_f32 v232, v212, v213\n"
  "v_cvt_pk_bf16_f32 v233, v214, v215\n"
  "v_cvt_pk_bf16_f32 v234, v216, v217\n"
  "v_cvt_pk_bf16_f32 v235, v218, v219\n"
  "global_store_dwordx4 v0, v[232:235], s[68:69] offset:3072 sc1\n"
  "s_add_u32 s56, s56, 1\n"
  "s_cmp_lt_u32 s56, s53\n"
  "s_cbranch_scc1 TOP0\n"
  "TEND:\n"
  "s_waitcnt vmcnt(0)\n"
  "v_mov_b32 v7, 0\n"
  "v_mov_b32 v8, s56\n"
  "s_mov_b64 exec, 1\n"
  "global_store_dword v7, v8, s[70:71] sc1\n"
  "s_mov_b64 exec, -1\n"
  "s_mov_b32 s56, s52\n"
  "s_add_u32 s53, s52, 1\n"
  "s_lshl_b32 s74, s52, 16\n"
  "s_add_u32 s46, s46, s74\n"
  "s_add_u32 s47, s47, s74\n"
  "s_xor_b32 s48, s48, 8192\n"
  "s_mov_b32 s52, 0\n"
  "s_cmp_lg_u32 s56, 0\n"
  "s_cbranch_scc1 TINIT\n"
  "TQUIT:\n"
  "s_endpgm\n"
      : "+{v[0:15]}"(vin), "+{s[36:43]}"(sa), "+{s[44:59]}"(sb) :: CLOB);
  } else { ARGS; PTR(sa, 0, p.ws);
    const int id = tk < 16 ? 32 + fd * 16 + tk : fd * 8 + tk - 16, cg = id % 12, sh = (cg & 1) * 32;
    v16u wa;
#pragma unroll
    for (int i = 0; i < 16; ++i) {
      const int gr = 4 * (i >> 1) + g, k = 8 * (gr % 5) + 4 * (i & 1) + qq;
      wa[i] = wave * 40960 + (36 * (gr / 5) + 18 * (k / 20) + min(k % 20, 17)) * 128 + sh + c4 * 8;
    }
    const int r4 = min(lane / 12, 3), cq = lane % 12;
    vin[1] = (lane & 8) ? (C_AP) - CTL + (4 * wave + c4) * 128 : C_QAP - CTL + (8 * wave + (lane & 7)) * 128;
    vin[2] = wave * 512 + (4 * wave + r4) * 4;
    vin[3] = 73728 + (cq >> 2) * 1024 + (16 * (cq & 3) + 4 * wave + r4) * 16;
    vin[4] = ((4 * wave + r4) * DM + 48 * cg + 4 * cq) * 2;
    vin[5] = wave * 40960 + (4 * g + qq) * 128 + sh + c4 * 8; vin[6] = (lane >> 3) * 1152 + (lane & 7) * 16; vin[7] = (lane & 8) ? 0 : C_QAL - C_QAP;
    PTR(sa, 2, p.Wo + (size_t)(wave * 8 + (id & 7)) * 128 * DM + 48 * cg - sh / 2); PTR(sa, 4, p.out); PTR(sa, 6, p.wo_a + wave * 144 * DM + 48 * cg - sh / 2);
    sb[0] = (wave * 8 + (id & 7)) * 65536 + 49152; sb[1] = WO_OA + wave * 5120 + 2048; sb[2] = WO_G; sb[3] = wave;
    sb[4] = WO_ST + 0x400000 + (role * 4 + wave) * 4096; sb[5] = C_OP + cg * 128; sb[6] = 7 - (id & 7);
    UNIFORM(sa, 8); UNIFORM(sb, 16);
    asm volatile(
  "s_nop 4\n"
  "s_mul_i32 s80, s47, 40960\n"
  "v_mov_b32 v127, v7\n"
  "s_add_u32 m0, s80, 0\n"
  "s_nop 0\n"
  "global_load_lds_dwordx4 v6, s[42:43]\n"
  "s_add_u32 s42, s42, 9216\n"
  "s_addc_u32 s43, s43, 0\n"
  "s_add_u32 m0, s80, 1024\n"
  "s_nop 0\n"
  "global_load_lds_dwordx4 v6, s[42:43]\n"
  "s_add_u32 s42, s42, 9216\n"
  "s_addc_u32 s43, s43, 0\n"
  "s_add_u32 m0, s80, 2048\n"
  "s_nop 0\n"
  "global_load_lds_dwordx4 v6, s[42:43]\n"
  "s_add_u32 s42, s42, 9216\n"
  "s_addc_u32 s43, s43, 0\n"
  "s_add_u32 m0, s80, 3072\n"
  "s_nop 0\n"
  "global_load_lds_dwordx4 v6, s[42:43]\n"
  "s_add_u32 s42, s42, 9216\n"
  "s_addc_u32 s43, s43, 0\n"
  "s_add_u32 m0, s80, 4096\n"
  "s_nop 0\n"
  "global_load_lds_dwordx4 v6, s[42:43]\n"
  "s_add_u32 s42, s42, 9216\n"
  "s_addc_u32 s43, s43, 0\n"
  "s_add_u32 m0, s80, 5120\n"
  "s_nop 0\n"
  "global_load_lds_dwordx4 v6, s[42:43]\n"
  "s_add_u32 s42, s42, 9216\n"
  "s_addc_u32 s43, s43, 0\n"
  "s_add_u32 m0, s80, 6144\n"
  "s_nop 0\n"
  "global_load_lds_dwordx4 v6, s[42:43]\n"
  "s_add_u32 s42, s42, 9216\n"
  "s_addc_u32 s43, s43, 0\n"
  "s_add_u32 m0, s80, 7168\n"
  "s_nop 0\n"
  "global_load_lds_dwordx4 v6, s[42:43]\n"
  "s_add_u32 s42, s42, 9216\n"
  "s_addc_u32 s43, s43, 0\n"
  "s_add_u32 m0, s80, 8192\n"
  "s_nop 0\n"
  "global_load_lds_dwordx4 v6, s[42:43]\n"
  "s_add_u32 s42, s42, 9216\n"
  "s_addc_u32 s43, s43, 0\n"
  "s_add_u32 m0, s80, 9216\n"
  "s_nop 0\n"
  "global_load_lds_dwordx4 v6, s[42:43]\n"
  "s_add_u32 s42, s42, 9216\n"
  "s_addc_u32 s43, s43, 0\n"
  "s_add_u32 m0, s80, 10240\n"
  "s_nop 0\n"
  "global_load_lds_dwordx4 v6, s[42:43]\n"
  "s_add_u32 s42, s42, 9216\n"
  "s_addc_u32 s43, s43, 0\n"
  "s_add_u32 m0, s80, 11264\n"
  "s_nop 0\n"
  "global_load_lds_dwordx4 v6, s[42:43]\n"
  "s_add_u32 s42, s42, 9216\n"
  "s_addc_u32 s43, s43, 0\n"
  "s_add_u32 m0, s80, 12288\n"
  "s_nop 0\n"
  "global_load_lds_dwordx4 v6, s[42:43]\n"
  "s_add_u32 s42, s42, 9216\n"
  "s_addc_u32 s43, s43, 0\n"
  "s_add_u32 m0, s80, 13312\n"
  "s_nop 0\n"
  "global_load_lds_dwordx4 v6, s[42:43]\n"
  "s_add_u32 s42, s42, 9216\n"
  "s_addc_u32 s43, s43, 0\n"
  "s_add_u32 m0, s80, 14336\n"
  "s_nop 0\n"
  "global_load_lds_dwordx4 v6, s[42:43]\n"
  "s_add_u32 s42, s42, 9216\n"
  "s_addc_u32 s43, s43, 0\n"
  "s_add_u32 m0, s80, 15360\n"
  "s_nop 0\n"
  "global_load_lds_dwordx4 v6, s[42:43]\n"
  "s_add_u32 s42, s42, 9216\n"
  "s_addc_u32 s43, s43, 0\n"
  "s_add_u32 m0, s80, 16384\n"
  "s_nop 0\n"
  "global_load_lds_dwordx4 v6, s[42:43]\n"
  "s_add_u32 s42, s42, 9216\n"
  "s_addc_u32 s43, s43, 0\n"
  "s_add_u32 m0, s80, 17408\n"
  "s_nop 0\n"
  "global_load_lds_dwordx4 v6, s[42:43]\n"
  "s_add_u32 s42, s42, 9216\n"
  "s_addc_u32 s43, s43, 0\n"
  "s_add_u32 m0, s80, 20480\n"
  "s_nop 0\n"
  "global_load_lds_dwordx4 v6, s[38:39]\n"
  "s_add_u32 s38, s38, 9216\n"
  "s_addc_u32 s39, s39, 0\n"
  "s_add_u32 m0, s80, 21504\n"
  "s_nop 0\n"
  "global_load_lds_dwordx4 v6, s[38:39]\n"
  "s_add_u32 s38, s38, 9216\n"
  "s_addc_u32 s39, s39, 0\n"
  "s_add_u32 m0, s80, 22528\n"
  "s_nop 0\n"
  "global_load_lds_dwordx4 v6, s[38:39]\n"
  "s_add_u32 s38, s38, 9216\n"
  "s_addc_u32 s39, s39, 0\n"
  "s_add_u32 m0, s80, 23552\n"
  "s_nop 0\n"
  "global_load_lds_dwordx4 v6, s[38:39]\n"
  "s_add_u32 s38, s38, 9216\n"
  "s_addc_u32 s39, s39, 0\n"
  "s_add_u32 m0, s80, 24576\n"
  "s_nop 0\n"
  "global_load_lds_dwordx4 v6, s[38:39]\n"
  "s_add_u32 s38, s38, 9216\n"
  "s_addc_u32 s39, s39, 0\n"
  "s_add_u32 m0, s80, 25600\n"
  "s_nop 0\n"
  "global_load_lds_dwordx4 v6, s[38:39]\n"
  "s_add_u32 s38, s38, 9216\n"
  "s_addc_u32 s39, s39, 0\n"
  "s_add_u32 m0, s80, 26624\n"
  "s_nop 0\n"
  "global_load_lds_dwordx4 v6, s[38:39]\n"
  "s_add_u32 s38, s38, 9216\n"
  "s_addc_u32 s39, s39, 0\n"
  "s_add_u32 m0, s80, 27648\n"
  "s_nop 0\n"
  "global_load_lds_dwordx4 v6, s[38:39]\n"
  "s_add_u32 s38, s38, 9216\n"
  "s_addc_u32 s39, s39, 0\n"
  "s_add_u32 m0, s80, 28672\n"
  "s_nop 0\n"
  "global_load_lds_dwordx4 v6, s[38:39]\n"
  "s_add_u32 s38, s38, 9216\n"
  "s_addc_u32 s39, s39, 0\n"
  "s_add_u32 m0, s80, 29696\n"
  "s_nop 0\n"
  "global_load_lds_dwordx4 v6, s[38:39]\n"
  "s_add_u32 s38, s38, 9216\n"
  "s_addc_u32 s39, s39, 0\n"
  "s_add_u32 m0, s80, 30720\n"
  "s_nop 0\n"
  "global_load_lds_dwordx4 v6, s[38:39]\n"
  "s_add_u32 s38, s38, 9216\n"
  "s_addc_u32 s39, s39, 0\n"
  "s_add_u32 m0, s80, 31744\n"
  "s_nop 0\n"
  "global_load_lds_dwordx4 v6, s[38:39]\n"
  "s_add_u32 s38, s38, 9216\n"
  "s_addc_u32 s39, s39, 0\n"
  "s_add_u32 m0, s80, 32768\n"
  "s_nop 0\n"
  "global_load_lds_dwordx4 v6, s[38:39]\n"
  "s_add_u32 s38, s38, 9216\n"
  "s_addc_u32 s39, s39, 0\n"
  "s_add_u32 m0, s80, 33792\n"
  "s_nop 0\n"
  "global_load_lds_dwordx4 v6, s[38:39]\n"
  "s_add_u32 s38, s38, 9216\n"
  "s_addc_u32 s39, s39, 0\n"
  "s_add_u32 m0, s80, 34816\n"
  "s_nop 0\n"
  "global_load_lds_dwordx4 v6, s[38:39]\n"
  "s_add_u32 s38, s38, 9216\n"
  "s_addc_u32 s39, s39, 0\n"
  "s_add_u32 m0, s80, 35840\n"
  "s_nop 0\n"
  "global_load_lds_dwordx4 v6, s[38:39]\n"
  "s_add_u32 s38, s38, 9216\n"
  "s_addc_u32 s39, s39, 0\n"
  "s_cmp_eq_u32 s50, 0\n"
  "s_cselect_b32 s74, 1179648, 0\n"
  "s_sub_u32 s38, s38, s74\n"
  "s_subb_u32 s39, s39, 0\n"
  "s_add_u32 m0, s80, 36864\n"
  "s_nop 0\n"
  "global_load_lds_dwordx4 v6, s[38:39]\n"
  "s_add_u32 s38, s38, 9216\n"
  "s_addc_u32 s39, s39, 0\n"
  "s_add_u32 m0, s80, 37888\n"
  "s_nop 0\n"
  "global_load_lds_dwordx4 v6, s[38:39]\n"
  "s_add_u32 s38, s38, 9216\n"
  "s_addc_u32 s39, s39, 0\n"
  "s_add_u32 m0, s80, 38912\n"
  "s_nop 0\n"
  "global_load_lds_dwordx4 v6, s[38:39]\n"
  "s_add_u32 s38, s38, 9216\n"
  "s_addc_u32 s39, s39, 0\n"
  "s_add_u32 m0, s80, 39936\n"
  "s_nop 0\n"
  "global_load_lds_dwordx4 v6, s[38:39]\n"
  "s_add_u32 s38, s38, 9216\n"
  "s_addc_u32 s39, s39, 0\n"
  "s_waitcnt vmcnt(20)\n"
  "ds_read_b64_tr_b16 v[52:53], v16 offset:0\n"
  "ds_read_b64_tr_b16 v[54:55], v17 offset:0\n"
  "ds_read_b64_tr_b16 v[56:57], v16 offset:32\n"
  "ds_read_b64_tr_b16 v[58:59], v17 offset:32\n"
  "ds_read_b64_tr_b16 v[60:61], v16 offset:64\n"
  "ds_read_b64_tr_b16 v[62:63], v17 offset:64\n"
  "ds_read_b64_tr_b16 v[64:65], v18 offset:0\n"
  "ds_read_b64_tr_b16 v[66:67], v19 offset:0\n"
  "ds_read_b64_tr_b16 v[68:69], v18 offset:32\n"
  "ds_read_b64_tr_b16 v[70:71], v19 offset:32\n"
  "ds_read_b64_tr_b16 v[72:73], v18 offset:64\n"
  "ds_read_b64_tr_b16 v[74:75], v19 offset:64\n"
  "ds_read_b64_tr_b16 v[76:77], v20 offset:0\n"
  "ds_read_b64_tr_b16 v[78:79], v21 offset:0\n"
  "ds_read_b64_tr_b16 v[80:81], v20 offset:32\n"
  "ds_read_b64_tr_b16 v[82:83], v21 offset:32\n"
  "ds_read_b64_tr_b16 v[84:85], v20 offset:64\n"
  "ds_read_b64_tr_b16 v[86:87], v21 offset:64\n"
  "ds_read_b64_tr_b16 v[88:89], v22 offset:0\n"
  "ds_read_b64_tr_b16 v[90:91], v23 offset:0\n"
  "ds_read_b64_tr_b16 v[92:93], v22 offset:32\n"
  "ds_read_b64_tr_b16 v[94:95], v23 offset:32\n"
  "ds_read_b64_tr_b16 v[96:97], v22 offset:64\n"
  "ds_read_b64_tr_b16 v[98:99], v23 offset:64\n"
  "ds_read_b64_tr_b16 v[100:101], v24 offset:0\n"
  "ds_read_b64_tr_b16 v[102:103], v25 offset:0\n"
  "ds_read_b64_tr_b16 v[104:105], v24 offset:32\n"
  "ds_read_b64_tr_b16 v[106:107], v25 offset:32\n"
  "ds_read_b64_tr_b16 v[108:109], v24 offset:64\n"
  "ds_read_b64_tr_b16 v[110:111], v25 offset:64\n"
  "s_waitcnt lgkmcnt(0)\n"
  "s_add_u32 m0, s80, 0\n"
  "s_nop 0\n"
  "global_load_lds_dwordx4 v6, s[38:39]\n"
  "s_add_u32 s38, s38, 9216\n"
  "s_addc_u32 s39, s39, 0\n"
  "s_add_u32 m0, s80, 1024\n"
  "s_nop 0\n"
  "global_load_lds_dwordx4 v6, s[38:39]\n"
  "s_add_u32 s38, s38, 9216\n"
  "s_addc_u32 s39, s39, 0\n"
  "s_add_u32 m0, s80, 2048\n"
  "s_nop 0\n"
  "global_load_lds_dwordx4 v6, s[38:39]\n"
  "s_add_u32 s38, s38, 9216\n"
  "s_addc_u32 s39, s39, 0\n"
  "s_add_u32 m0, s80, 3072\n"
  "s_nop 0\n"
  "global_load_lds_dwordx4 v6, s[38:39]\n"
  "s_add_u32 s38, s38, 9216\n"
  "s_addc_u32 s39, s39, 0\n"
  "s_add_u32 m0, s80, 4096\n"
  "s_nop 0\n"
  "global_load_lds_dwordx4 v6, s[38:39]\n"
  "s_add_u32 s38, s38, 9216\n"
  "s_addc_u32 s39, s39, 0\n"
  "s_add_u32 m0, s80, 5120\n"
  "s_nop 0\n"
  "global_load_lds_dwordx4 v6, s[38:39]\n"
  "s_add_u32 s38, s38, 9216\n"
  "s_addc_u32 s39, s39, 0\n"
  "s_add_u32 m0, s80, 6144\n"
  "s_nop 0\n"
  "global_load_lds_dwordx4 v6, s[38:39]\n"
  "s_add_u32 s38, s38, 9216\n"
  "s_addc_u32 s39, s39, 0\n"
  "s_add_u32 m0, s80, 7168\n"
  "s_nop 0\n"
  "global_load_lds_dwordx4 v6, s[38:39]\n"
  "s_add_u32 s38, s38, 9216\n"
  "s_addc_u32 s39, s39, 0\n"
  "s_add_u32 m0, s80, 8192\n"
  "s_nop 0\n"
  "global_load_lds_dwordx4 v6, s[38:39]\n"
  "s_add_u32 s38, s38, 9216\n"
  "s_addc_u32 s39, s39, 0\n"
  "s_add_u32 m0, s80, 9216\n"
  "s_nop 0\n"
  "global_load_lds_dwordx4 v6, s[38:39]\n"
  "s_add_u32 s38, s38, 9216\n"
  "s_addc_u32 s39, s39, 0\n"
  "s_add_u32 m0, s80, 10240\n"
  "s_nop 0\n"
  "global_load_lds_dwordx4 v6, s[38:39]\n"
  "s_add_u32 s38, s38, 9216\n"
  "s_addc_u32 s39, s39, 0\n"
  "s_add_u32 m0, s80, 11264\n"
  "s_nop 0\n"
  "global_load_lds_dwordx4 v6, s[38:39]\n"
  "s_add_u32 s38, s38, 9216\n"
  "s_addc_u32 s39, s39, 0\n"
  "s_cmp_eq_u32 s50, 1\n"
  "s_cselect_b32 s74, 1179648, 0\n"
  "s_sub_u32 s38, s38, s74\n"
  "s_subb_u32 s39, s39, 0\n"
  "s_add_u32 m0, s80, 12288\n"
  "s_nop 0\n"
  "global_load_lds_dwordx4 v6, s[38:39]\n"
  "s_add_u32 s38, s38, 9216\n"
  "s_addc_u32 s39, s39, 0\n"
  "s_add_u32 m0, s80, 13312\n"
  "s_nop 0\n"
  "global_load_lds_dwordx4 v6, s[38:39]\n"
  "s_add_u32 s38, s38, 9216\n"
  "s_addc_u32 s39, s39, 0\n"
  "s_add_u32 m0, s80, 14336\n"
  "s_nop 0\n"
  "global_load_lds_dwordx4 v6, s[38:39]\n"
  "s_add_u32 s38, s38, 9216\n"
  "s_addc_u32 s39, s39, 0\n"
  "s_add_u32 m0, s80, 15360\n"
  "s_nop 0\n"
  "global_load_lds_dwordx4 v6, s[38:39]\n"
  "s_add_u32 s38, s38, 9216\n"
  "s_addc_u32 s39, s39, 0\n"
  "s_waitcnt vmcnt(32) lgkmcnt(0)\n"
  "s_add_u32 m0, s80, 16384\n"
  "s_nop 0\n"
  "global_load_lds_dwordx4 v6, s[38:39]\n"
  "s_add_u32 s38, s38, 9216\n"
  "s_addc_u32 s39, s39, 0\n"
  "s_add_u32 m0, s80, 17408\n"
  "s_nop 0\n"
  "global_load_lds_dwordx4 v6, s[38:39]\n"
  "s_add_u32 s38, s38, 9216\n"
  "s_addc_u32 s39, s39, 0\n"
  "s_add_u32 m0, s80, 18432\n"
  "s_nop 0\n"
  "global_load_lds_dwordx4 v6, s[38:39]\n"
  "s_add_u32 s38, s38, 9216\n"
  "s_addc_u32 s39, s39, 0\n"
  "s_add_u32 m0, s80, 19456\n"
  "s_nop 0\n"
  "global_load_lds_dwordx4 v6, s[38:39]\n"
  "s_add_u32 s38, s38, 9216\n"
  "s_addc_u32 s39, s39, 0\n"
  "ds_read_b64_tr_b16 a[0:1], v5 offset:20480\n"
  "ds_read_b64_tr_b16 a[2:3], v5 offset:22528\n"
  "ds_read_b64_tr_b16 a[4:5], v5 offset:20512\n"
  "ds_read_b64_tr_b16 a[6:7], v5 offset:22560\n"
  "ds_read_b64_tr_b16 a[8:9], v5 offset:20544\n"
  "ds_read_b64_tr_b16 a[10:11], v5 offset:22592\n"
  "s_waitcnt vmcnt(32) lgkmcnt(0)\n"
  "s_add_u32 m0, s80, 20480\n"
  "s_nop 0\n"
  "global_load_lds_dwordx4 v6, s[38:39]\n"
  "s_add_u32 s38, s38, 9216\n"
  "s_addc_u32 s39, s39, 0\n"
  "s_add_u32 m0, s80, 21504\n"
  "s_nop 0\n"
  "global_load_lds_dwordx4 v6, s[38:39]\n"
  "s_add_u32 s38, s38, 9216\n"
  "s_addc_u32 s39, s39, 0\n"
  "s_add_u32 m0, s80, 22528\n"
  "s_nop 0\n"
  "global_load_lds_dwordx4 v6, s[38:39]\n"
  "s_add_u32 s38, s38, 9216\n"
  "s_addc_u32 s39, s39, 0\n"
  "s_add_u32 m0, s80, 23552\n"
  "s_nop 0\n"
  "global_load_lds_dwordx4 v6, s[38:39]\n"
  "s_add_u32 s38, s38, 9216\n"
  "s_addc_u32 s39, s39, 0\n"
  "ds_read_b64_tr_b16 a[12:13], v5 offset:24576\n"
  "ds_read_b64_tr_b16 a[14:15], v5 offset:26624\n"
  "ds_read_b64_tr_b16 a[16:17], v5 offset:24608\n"
  "ds_read_b64_tr_b16 a[18:19], v5 offset:26656\n"
  "ds_read_b64_tr_b16 a[20:21], v5 offset:24640\n"
  "ds_read_b64_tr_b16 a[22:23], v5 offset:26688\n"
  "s_waitcnt vmcnt(32) lgkmcnt(0)\n"
  "s_add_u32 m0, s80, 24576\n"
  "s_nop 0\n"
  "global_load_lds_dwordx4 v6, s[38:39]\n"
  "s_add_u32 s38, s38, 9216\n"
  "s_addc_u32 s39, s39, 0\n"
  "s_add_u32 m0, s80, 25600\n"
  "s_nop 0\n"
  "global_load_lds_dwordx4 v6, s[38:39]\n"
  "s_add_u32 s38, s38, 9216\n"
  "s_addc_u32 s39, s39, 0\n"
  "s_add_u32 m0, s80, 26624\n"
  "s_nop 0\n"
  "global_load_lds_dwordx4 v6, s[38:39]\n"
  "s_add_u32 s38, s38, 9216\n"
  "s_addc_u32 s39, s39, 0\n"
  "s_add_u32 m0, s80, 27648\n"
  "s_nop 0\n"
  "global_load_lds_dwordx4 v6, s[38:39]\n"
  "s_add_u32 s38, s38, 9216\n"
  "s_addc_u32 s39, s39, 0\n"
  "s_cmp_eq_u32 s50, 2\n"
  "s_cselect_b32 s74, 1179648, 0\n"
  "s_sub_u32 s38, s38, s74\n"
  "s_subb_u32 s39, s39, 0\n"
  "ds_read_b64_tr_b16 a[24:25], v5 offset:28672\n"
  "ds_read_b64_tr_b16 a[26:27], v5 offset:30720\n"
  "ds_read_b64_tr_b16 a[28:29], v5 offset:28704\n"
  "ds_read_b64_tr_b16 a[30:31], v5 offset:30752\n"
  "ds_read_b64_tr_b16 a[32:33], v5 offset:28736\n"
  "ds_read_b64_tr_b16 a[34:35], v5 offset:30784\n"
  "s_waitcnt vmcnt(32) lgkmcnt(0)\n"
  "s_add_u32 m0, s80, 28672\n"
  "s_nop 0\n"
  "global_load_lds_dwordx4 v6, s[38:39]\n"
  "s_add_u32 s38, s38, 9216\n"
  "s_addc_u32 s39, s39, 0\n"
  "s_add_u32 m0, s80, 29696\n"
  "s_nop 0\n"
  "global_load_lds_dwordx4 v6, s[38:39]\n"
  "s_add_u32 s38, s38, 9216\n"
  "s_addc_u32 s39, s39, 0\n"
  "s_add_u32 m0, s80, 30720\n"
  "s_nop 0\n"
  "global_load_lds_dwordx4 v6, s[38:39]\n"
  "s_add_u32 s38, s38, 9216\n"
  "s_addc_u32 s39, s39, 0\n"
  "s_add_u32 m0, s80, 31744\n"
  "s_nop 0\n"
  "global_load_lds_dwordx4 v6, s[38:39]\n"
  "s_add_u32 s38, s38, 9216\n"
  "s_addc_u32 s39, s39, 0\n"
  "ds_read_b64_tr_b16 a[36:37], v5 offset:32768\n"
  "ds_read_b64_tr_b16 a[38:39], v5 offset:34816\n"
  "ds_read_b64_tr_b16 a[40:41], v5 offset:32800\n"
  "ds_read_b64_tr_b16 a[42:43], v5 offset:34848\n"
  "ds_read_b64_tr_b16 a[44:45], v5 offset:32832\n"
  "ds_read_b64_tr_b16 a[46:47], v5 offset:34880\n"
  "s_waitcnt vmcnt(32) lgkmcnt(0)\n"
  "s_add_u32 m0, s80, 32768\n"
  "s_nop 0\n"
  "global_load_lds_dwordx4 v6, s[38:39]\n"
  "s_add_u32 s38, s38, 9216\n"
  "s_addc_u32 s39, s39, 0\n"
  "s_add_u32 m0, s80, 33792\n"
  "s_nop 0\n"
  "global_load_lds_dwordx4 v6, s[38:39]\n"
  "s_add_u32 s38, s38, 9216\n"
  "s_addc_u32 s39, s39, 0\n"
  "s_add_u32 m0, s80, 34816\n"
  "s_nop 0\n"
  "global_load_lds_dwordx4 v6, s[38:39]\n"
  "s_add_u32 s38, s38, 9216\n"
  "s_addc_u32 s39, s39, 0\n"
  "s_add_u32 m0, s80, 35840\n"
  "s_nop 0\n"
  "global_load_lds_dwordx4 v6, s[38:39]\n"
  "s_add_u32 s38, s38, 9216\n"
  "s_addc_u32 s39, s39, 0\n"
  "ds_read_b64_tr_b16 a[48:49], v5 offset:36864\n"
  "ds_read_b64_tr_b16 a[50:51], v5 offset:38912\n"
  "ds_read_b64_tr_b16 a[52:53], v5 offset:36896\n"
  "ds_read_b64_tr_b16 a[54:55], v5 offset:38944\n"
  "ds_read_b64_tr_b16 a[56:57], v5 offset:36928\n"
  "ds_read_b64_tr_b16 a[58:59], v5 offset:38976\n"
  "s_waitcnt vmcnt(32) lgkmcnt(0)\n"
  "s_add_u32 m0, s80, 36864\n"
  "s_nop 0\n"
  "global_load_lds_dwordx4 v6, s[38:39]\n"
  "s_add_u32 s38, s38, 9216\n"
  "s_addc_u32 s39, s39, 0\n"
  "s_add_u32 m0, s80, 37888\n"
  "s_nop 0\n"
  "global_load_lds_dwordx4 v6, s[38:39]\n"
  "s_add_u32 s38, s38, 9216\n"
  "s_addc_u32 s39, s39, 0\n"
  "s_add_u32 m0, s80, 38912\n"
  "s_nop 0\n"
  "global_load_lds_dwordx4 v6, s[38:39]\n"
  "s_add_u32 s38, s38, 9216\n"
  "s_addc_u32 s39, s39, 0\n"
  "s_add_u32 m0, s80, 39936\n"
  "s_nop 0\n"
  "global_load_lds_dwordx4 v6, s[38:39]\n"
  "s_add_u32 s38, s38, 9216\n"
  "s_addc_u32 s39, s39, 0\n"
  "ds_read_b64_tr_b16 a[60:61], v5 offset:0\n"
  "ds_read_b64_tr_b16 a[62:63], v5 offset:2048\n"
  "ds_read_b64_tr_b16 a[64:65], v5 offset:32\n"
  "ds_read_b64_tr_b16 a[66:67], v5 offset:2080\n"
  "ds_read_b64_tr_b16 a[68:69], v5 offset:64\n"
  "ds_read_b64_tr_b16 a[70:71], v5 offset:2112\n"
  "s_waitcnt vmcnt(32) lgkmcnt(0)\n"
  "s_add_u32 m0, s80, 0\n"
  "s_nop 0\n"
  "global_load_lds_dwordx4 v6, s[38:39]\n"
  "s_add_u32 s38, s38, 9216\n"
  "s_addc_u32 s39, s39, 0\n"
  "s_add_u32 m0, s80, 1024\n"
  "s_nop 0\n"
  "global_load_lds_dwordx4 v6, s[38:39]\n"
  "s_add_u32 s38, s38, 9216\n"
  "s_addc_u32 s39, s39, 0\n"
  "s_add_u32 m0, s80, 2048\n"
  "s_nop 0\n"
  "global_load_lds_dwordx4 v6, s[38:39]\n"
  "s_add_u32 s38, s38, 9216\n"
  "s_addc_u32 s39, s39, 0\n"
  "s_add_u32 m0, s80, 3072\n"
  "s_nop 0\n"
  "global_load_lds_dwordx4 v6, s[38:39]\n"
  "s_add_u32 s38, s38, 9216\n"
  "s_addc_u32 s39, s39, 0\n"
  "s_cmp_eq_u32 s50, 3\n"
  "s_cselect_b32 s74, 1179648, 0\n"
  "s_sub_u32 s38, s38, s74\n"
  "s_subb_u32 s39, s39, 0\n"
  "ds_read_b64_tr_b16 a[72:73], v5 offset:4096\n"
  "ds_read_b64_tr_b16 a[74:75], v5 offset:6144\n"
  "ds_read_b64_tr_b16 a[76:77], v5 offset:4128\n"
  "ds_read_b64_tr_b16 a[78:79], v5 offset:6176\n"
  "ds_read_b64_tr_b16 a[80:81], v5 offset:4160\n"
  "ds_read_b64_tr_b16 a[82:83], v5 offset:6208\n"
  "s_waitcnt vmcnt(32) lgkmcnt(0)\n"
  "s_add_u32 m0, s80, 4096\n"
  "s_nop 0\n"
  "global_load_lds_dwordx4 v6, s[38:39]\n"
  "s_add_u32 s38, s38, 9216\n"
  "s_addc_u32 s39, s39, 0\n"
  "s_add_u32 m0, s80, 5120\n"
  "s_nop 0\n"
  "global_load_lds_dwordx4 v6, s[38:39]\n"
  "s_add_u32 s38, s38, 9216\n"
  "s_addc_u32 s39, s39, 0\n"
  "s_add_u32 m0, s80, 6144\n"
  "s_nop 0\n"
  "global_load_lds_dwordx4 v6, s[38:39]\n"
  "s_add_u32 s38, s38, 9216\n"
  "s_addc_u32 s39, s39, 0\n"
  "s_add_u32 m0, s80, 7168\n"
  "s_nop 0\n"
  "global_load_lds_dwordx4 v6, s[38:39]\n"
  "s_add_u32 s38, s38, 9216\n"
  "s_addc_u32 s39, s39, 0\n"
  "ds_read_b64_tr_b16 a[84:85], v5 offset:8192\n"
  "ds_read_b64_tr_b16 a[86:87], v5 offset:10240\n"
  "ds_read_b64_tr_b16 a[88:89], v5 offset:8224\n"
  "ds_read_b64_tr_b16 a[90:91], v5 offset:10272\n"
  "ds_read_b64_tr_b16 a[92:93], v5 offset:8256\n"
  "ds_read_b64_tr_b16 a[94:95], v5 offset:10304\n"
  "s_waitcnt vmcnt(32) lgkmcnt(0)\n"
  "s_add_u32 m0, s80, 8192\n"
  "s_nop 0\n"
  "global_load_lds_dwordx4 v6, s[38:39]\n"
  "s_add_u32 s38, s38, 9216\n"
  "s_addc_u32 s39, s39, 0\n"
  "s_add_u32 m0, s80, 9216\n"
  "s_nop 0\n"
  "global_load_lds_dwordx4 v6, s[38:39]\n"
  "s_add_u32 s38, s38, 9216\n"
  "s_addc_u32 s39, s39, 0\n"
  "s_add_u32 m0, s80, 10240\n"
  "s_nop 0\n"
  "global_load_lds_dwordx4 v6, s[38:39]\n"
  "s_add_u32 s38, s38, 9216\n"
  "s_addc_u32 s39, s39, 0\n"
  "s_add_u32 m0, s80, 11264\n"
  "s_nop 0\n"
  "global_load_lds_dwordx4 v6, s[38:39]\n"
  "s_add_u32 s38, s38, 9216\n"
  "s_addc_u32 s39, s39, 0\n"
  "ds_read_b64_tr_b16 a[96:97], v5 offset:12288\n"
  "ds_read_b64_tr_b16 a[98:99], v5 offset:14336\n"
  "ds_read_b64_tr_b16 a[100:101], v5 offset:12320\n"
  "ds_read_b64_tr_b16 a[102:103], v5 offset:14368\n"
  "ds_read_b64_tr_b16 a[104:105], v5 offset:12352\n"
  "ds_read_b64_tr_b16 a[106:107], v5 offset:14400\n"
  "s_waitcnt vmcnt(32) lgkmcnt(0)\n"
  "s_add_u32 m0, s80, 12288\n"
  "s_nop 0\n"
  "global_load_lds_dwordx4 v6, s[38:39]\n"
  "s_add_u32 s38, s38, 9216\n"
  "s_addc_u32 s39, s39, 0\n"
  "s_add_u32 m0, s80, 13312\n"
  "s_nop 0\n"
  "global_load_lds_dwordx4 v6, s[38:39]\n"
  "s_add_u32 s38, s38, 9216\n"
  "s_addc_u32 s39, s39, 0\n"
  "s_add_u32 m0, s80, 14336\n"
  "s_nop 0\n"
  "global_load_lds_dwordx4 v6, s[38:39]\n"
  "s_add_u32 s38, s38, 9216\n"
  "s_addc_u32 s39, s39, 0\n"
  "s_add_u32 m0, s80, 15360\n"
  "s_nop 0\n"
  "global_load_lds_dwordx4 v6, s[38:39]\n"
  "s_add_u32 s38, s38, 9216\n"
  "s_addc_u32 s39, s39, 0\n"
  "ds_read_b64_tr_b16 a[108:109], v5 offset:16384\n"
  "ds_read_b64_tr_b16 a[110:111], v5 offset:18432\n"
  "ds_read_b64_tr_b16 a[112:113], v5 offset:16416\n"
  "ds_read_b64_tr_b16 a[114:115], v5 offset:18464\n"
  "ds_read_b64_tr_b16 a[116:117], v5 offset:16448\n"
  "ds_read_b64_tr_b16 a[118:119], v5 offset:18496\n"
  "s_waitcnt vmcnt(32) lgkmcnt(0)\n"
  "s_add_u32 m0, s80, 16384\n"
  "s_nop 0\n"
  "global_load_lds_dwordx4 v6, s[38:39]\n"
  "s_add_u32 s38, s38, 9216\n"
  "s_addc_u32 s39, s39, 0\n"
  "s_add_u32 m0, s80, 17408\n"
  "s_nop 0\n"
  "global_load_lds_dwordx4 v6, s[38:39]\n"
  "s_add_u32 s38, s38, 9216\n"
  "s_addc_u32 s39, s39, 0\n"
  "s_add_u32 m0, s80, 18432\n"
  "s_nop 0\n"
  "global_load_lds_dwordx4 v6, s[38:39]\n"
  "s_add_u32 s38, s38, 9216\n"
  "s_addc_u32 s39, s39, 0\n"
  "s_add_u32 m0, s80, 19456\n"
  "s_nop 0\n"
  "global_load_lds_dwordx4 v6, s[38:39]\n"
  "s_add_u32 s38, s38, 9216\n"
  "s_addc_u32 s39, s39, 0\n"
  "s_cmp_eq_u32 s50, 4\n"
  "s_cselect_b32 s74, 1179648, 0\n"
  "s_sub_u32 s38, s38, s74\n"
  "s_subb_u32 s39, s39, 0\n"
  "ds_read_b64_tr_b16 a[120:121], v5 offset:20480\n"
  "ds_read_b64_tr_b16 a[122:123], v5 offset:22528\n"
  "ds_read_b64_tr_b16 a[124:125], v5 offset:20512\n"
  "ds_read_b64_tr_b16 a[126:127], v5 offset:22560\n"
  "ds_read_b64_tr_b16 a[128:129], v5 offset:20544\n"
  "ds_read_b64_tr_b16 a[130:131], v5 offset:22592\n"
  "s_waitcnt vmcnt(32) lgkmcnt(0)\n"
  "s_add_u32 m0, s80, 20480\n"
  "s_nop 0\n"
  "global_load_lds_dwordx4 v6, s[38:39]\n"
  "s_add_u32 s38, s38, 9216\n"
  "s_addc_u32 s39, s39, 0\n"
  "s_add_u32 m0, s80, 21504\n"
  "s_nop 0\n"
  "global_load_lds_dwordx4 v6, s[38:39]\n"
  "s_add_u32 s38, s38, 9216\n"
  "s_addc_u32 s39, s39, 0\n"
  "s_add_u32 m0, s80, 22528\n"
  "s_nop 0\n"
  "global_load_lds_dwordx4 v6, s[38:39]\n"
  "s_add_u32 s38, s38, 9216\n"
  "s_addc_u32 s39, s39, 0\n"
  "s_add_u32 m0, s80, 23552\n"
  "s_nop 0\n"
  "global_load_lds_dwordx4 v6, s[38:39]\n"
  "s_add_u32 s38, s38, 9216\n"
  "s_addc_u32 s39, s39, 0\n"
  "ds_read_b64_tr_b16 a[132:133], v5 offset:24576\n"
  "ds_read_b64_tr_b16 a[134:135], v5 offset:26624\n"
  "ds_read_b64_tr_b16 a[136:137], v5 offset:24608\n"
  "ds_read_b64_tr_b16 a[138:139], v5 offset:26656\n"
  "ds_read_b64_tr_b16 a[140:141], v5 offset:24640\n"
  "ds_read_b64_tr_b16 a[142:143], v5 offset:26688\n"
  "s_waitcnt vmcnt(32) lgkmcnt(0)\n"
  "s_add_u32 m0, s80, 24576\n"
  "s_nop 0\n"
  "global_load_lds_dwordx4 v6, s[38:39]\n"
  "s_add_u32 s38, s38, 9216\n"
  "s_addc_u32 s39, s39, 0\n"
  "s_add_u32 m0, s80, 25600\n"
  "s_nop 0\n"
  "global_load_lds_dwordx4 v6, s[38:39]\n"
  "s_add_u32 s38, s38, 9216\n"
  "s_addc_u32 s39, s39, 0\n"
  "s_add_u32 m0, s80, 26624\n"
  "s_nop 0\n"
  "global_load_lds_dwordx4 v6, s[38:39]\n"
  "s_add_u32 s38, s38, 9216\n"
  "s_addc_u32 s39, s39, 0\n"
  "s_add_u32 m0, s80, 27648\n"
  "s_nop 0\n"
  "global_load_lds_dwordx4 v6, s[38:39]\n"
  "s_add_u32 s38, s38, 9216\n"
  "s_addc_u32 s39, s39, 0\n"
  "ds_read_b64_tr_b16 a[144:145], v5 offset:28672\n"
  "ds_read_b64_tr_b16 a[146:147], v5 offset:30720\n"
  "ds_read_b64_tr_b16 a[148:149], v5 offset:28704\n"
  "ds_read_b64_tr_b16 a[150:151], v5 offset:30752\n"
  "ds_read_b64_tr_b16 a[152:153], v5 offset:28736\n"
  "ds_read_b64_tr_b16 a[154:155], v5 offset:30784\n"
  "s_waitcnt vmcnt(32) lgkmcnt(0)\n"
  "s_add_u32 m0, s80, 28672\n"
  "s_nop 0\n"
  "global_load_lds_dwordx4 v6, s[38:39]\n"
  "s_add_u32 s38, s38, 9216\n"
  "s_addc_u32 s39, s39, 0\n"
  "s_add_u32 m0, s80, 29696\n"
  "s_nop 0\n"
  "global_load_lds_dwordx4 v6, s[38:39]\n"
  "s_add_u32 s38, s38, 9216\n"
  "s_addc_u32 s39, s39, 0\n"
  "s_add_u32 m0, s80, 30720\n"
  "s_nop 0\n"
  "global_load_lds_dwordx4 v6, s[38:39]\n"
  "s_add_u32 s38, s38, 9216\n"
  "s_addc_u32 s39, s39, 0\n"
  "s_add_u32 m0, s80, 31744\n"
  "s_nop 0\n"
  "global_load_lds_dwordx4 v6, s[38:39]\n"
  "s_add_u32 s38, s38, 9216\n"
  "s_addc_u32 s39, s39, 0\n"
  "ds_read_b64_tr_b16 a[156:157], v5 offset:32768\n"
  "ds_read_b64_tr_b16 a[158:159], v5 offset:34816\n"
  "ds_read_b64_tr_b16 a[160:161], v5 offset:32800\n"
  "ds_read_b64_tr_b16 a[162:163], v5 offset:34848\n"
  "ds_read_b64_tr_b16 a[164:165], v5 offset:32832\n"
  "ds_read_b64_tr_b16 a[166:167], v5 offset:34880\n"
  "s_waitcnt vmcnt(32) lgkmcnt(0)\n"
  "s_add_u32 m0, s80, 32768\n"
  "s_nop 0\n"
  "global_load_lds_dwordx4 v6, s[38:39]\n"
  "s_add_u32 s38, s38, 9216\n"
  "s_addc_u32 s39, s39, 0\n"
  "s_add_u32 m0, s80, 33792\n"
  "s_nop 0\n"
  "global_load_lds_dwordx4 v6, s[38:39]\n"
  "s_add_u32 s38, s38, 9216\n"
  "s_addc_u32 s39, s39, 0\n"
  "s_add_u32 m0, s80, 34816\n"
  "s_nop 0\n"
  "global_load_lds_dwordx4 v6, s[38:39]\n"
  "s_add_u32 s38, s38, 9216\n"
  "s_addc_u32 s39, s39, 0\n"
  "s_add_u32 m0, s80, 35840\n"
  "s_nop 0\n"
  "global_load_lds_dwordx4 v6, s[38:39]\n"
  "s_add_u32 s38, s38, 9216\n"
  "s_addc_u32 s39, s39, 0\n"
  "s_cmp_eq_u32 s50, 5\n"
  "s_cselect_b32 s74, 1179648, 0\n"
  "s_sub_u32 s38, s38, s74\n"
  "s_subb_u32 s39, s39, 0\n"
  "ds_read_b64_tr_b16 a[168:169], v5 offset:36864\n"
  "ds_read_b64_tr_b16 a[170:171], v5 offset:38912\n"
  "ds_read_b64_tr_b16 a[172:173], v5 offset:36896\n"
  "ds_read_b64_tr_b16 a[174:175], v5 offset:38944\n"
  "ds_read_b64_tr_b16 a[176:177], v5 offset:36928\n"
  "ds_read_b64_tr_b16 a[178:179], v5 offset:38976\n"
  "s_waitcnt vmcnt(32) lgkmcnt(0)\n"
  "s_add_u32 m0, s80, 36864\n"
  "s_nop 0\n"
  "global_load_lds_dwordx4 v6, s[38:39]\n"
  "s_add_u32 s38, s38, 9216\n"
  "s_addc_u32 s39, s39, 0\n"
  "s_add_u32 m0, s80, 37888\n"
  "s_nop 0\n"
  "global_load_lds_dwordx4 v6, s[38:39]\n"
  "s_add_u32 s38, s38, 9216\n"
  "s_addc_u32 s39, s39, 0\n"
  "s_add_u32 m0, s80, 38912\n"
  "s_nop 0\n"
  "global_load_lds_dwordx4 v6, s[38:39]\n"
  "s_add_u32 s38, s38, 9216\n"
  "s_addc_u32 s39, s39, 0\n"
  "s_add_u32 m0, s80, 39936\n"
  "s_nop 0\n"
  "global_load_lds_dwordx4 v6, s[38:39]\n"
  "s_add_u32 s38, s38, 9216\n"
  "s_addc_u32 s39, s39, 0\n"
  "ds_read_b64_tr_b16 a[180:181], v5 offset:0\n"
  "ds_read_b64_tr_b16 a[182:183], v5 offset:2048\n"
  "ds_read_b64_tr_b16 a[184:185], v5 offset:32\n"
  "ds_read_b64_tr_b16 a[186:187], v5 offset:2080\n"
  "ds_read_b64_tr_b16 a[188:189], v5 offset:64\n"
  "ds_read_b64_tr_b16 a[190:191], v5 offset:2112\n"
  "s_waitcnt vmcnt(32) lgkmcnt(0)\n"
  "s_add_u32 m0, s80, 0\n"
  "s_nop 0\n"
  "global_load_lds_dwordx4 v6, s[38:39]\n"
  "s_add_u32 s38, s38, 9216\n"
  "s_addc_u32 s39, s39, 0\n"
  "s_add_u32 m0, s80, 1024\n"
  "s_nop 0\n"
  "global_load_lds_dwordx4 v6, s[38:39]\n"
  "s_add_u32 s38, s38, 9216\n"
  "s_addc_u32 s39, s39, 0\n"
  "s_add_u32 m0, s80, 2048\n"
  "s_nop 0\n"
  "global_load_lds_dwordx4 v6, s[38:39]\n"
  "s_add_u32 s38, s38, 9216\n"
  "s_addc_u32 s39, s39, 0\n"
  "s_add_u32 m0, s80, 3072\n"
  "s_nop 0\n"
  "global_load_lds_dwordx4 v6, s[38:39]\n"
  "s_add_u32 s38, s38, 9216\n"
  "s_addc_u32 s39, s39, 0\n"
  "ds_read_b64_tr_b16 a[192:193], v5 offset:4096\n"
  "ds_read_b64_tr_b16 a[194:195], v5 offset:6144\n"
  "ds_read_b64_tr_b16 a[196:197], v5 offset:4128\n"
  "ds_read_b64_tr_b16 a[198:199], v5 offset:6176\n"
  "ds_read_b64_tr_b16 a[200:201], v5 offset:4160\n"
  "ds_read_b64_tr_b16 a[202:203], v5 offset:6208\n"
  "s_waitcnt vmcnt(32) lgkmcnt(0)\n"
  "s_add_u32 m0, s80, 4096\n"
  "s_nop 0\n"
  "global_load_lds_dwordx4 v6, s[38:39]\n"
  "s_add_u32 s38, s38, 9216\n"
  "s_addc_u32 s39, s39, 0\n"
  "s_add_u32 m0, s80, 5120\n"
  "s_nop 0\n"
  "global_load_lds_dwordx4 v6, s[38:39]\n"
  "s_add_u32 s38, s38, 9216\n"
  "s_addc_u32 s39, s39, 0\n"
  "s_add_u32 m0, s80, 6144\n"
  "s_nop 0\n"
  "global_load_lds_dwordx4 v6, s[38:39]\n"
  "s_add_u32 s38, s38, 9216\n"
  "s_addc_u32 s39, s39, 0\n"
  "s_add_u32 m0, s80, 7168\n"
  "s_nop 0\n"
  "global_load_lds_dwordx4 v6, s[38:39]\n"
  "s_add_u32 s38, s38, 9216\n"
  "s_addc_u32 s39, s39, 0\n"
  "ds_read_b64_tr_b16 a[204:205], v5 offset:8192\n"
  "ds_read_b64_tr_b16 a[206:207], v5 offset:10240\n"
  "ds_read_b64_tr_b16 a[208:209], v5 offset:8224\n"
  "ds_read_b64_tr_b16 a[210:211], v5 offset:10272\n"
  "ds_read_b64_tr_b16 a[212:213], v5 offset:8256\n"
  "ds_read_b64_tr_b16 a[214:215], v5 offset:10304\n"
  "s_waitcnt vmcnt(32) lgkmcnt(0)\n"
  "s_add_u32 m0, s80, 8192\n"
  "s_nop 0\n"
  "global_load_lds_dwordx4 v6, s[38:39]\n"
  "s_add_u32 s38, s38, 9216\n"
  "s_addc_u32 s39, s39, 0\n"
  "s_add_u32 m0, s80, 9216\n"
  "s_nop 0\n"
  "global_load_lds_dwordx4 v6, s[38:39]\n"
  "s_add_u32 s38, s38, 9216\n"
  "s_addc_u32 s39, s39, 0\n"
  "s_add_u32 m0, s80, 10240\n"
  "s_nop 0\n"
  "global_load_lds_dwordx4 v6, s[38:39]\n"
  "s_add_u32 s38, s38, 9216\n"
  "s_addc_u32 s39, s39, 0\n"
  "s_add_u32 m0, s80, 11264\n"
  "s_nop 0\n"
  "global_load_lds_dwordx4 v6, s[38:39]\n"
  "s_add_u32 s38, s38, 9216\n"
  "s_addc_u32 s39, s39, 0\n"
  "s_cmp_eq_u32 s50, 6\n"
  "s_cselect_b32 s74, 1179648, 0\n"
  "s_sub_u32 s38, s38, s74\n"
  "s_subb_u32 s39, s39, 0\n"
  "ds_read_b64_tr_b16 a[216:217], v5 offset:12288\n"
  "ds_read_b64_tr_b16 a[218:219], v5 offset:14336\n"
  "ds_read_b64_tr_b16 a[220:221], v5 offset:12320\n"
  "ds_read_b64_tr_b16 a[222:223], v5 offset:14368\n"
  "ds_read_b64_tr_b16 a[224:225], v5 offset:12352\n"
  "ds_read_b64_tr_b16 a[226:227], v5 offset:14400\n"
  "s_waitcnt vmcnt(32) lgkmcnt(0)\n"
  "s_add_u32 m0, s80, 12288\n"
  "s_nop 0\n"
  "global_load_lds_dwordx4 v6, s[38:39]\n"
  "s_add_u32 s38, s38, 9216\n"
  "s_addc_u32 s39, s39, 0\n"
  "s_add_u32 m0, s80, 13312\n"
  "s_nop 0\n"
  "global_load_lds_dwordx4 v6, s[38:39]\n"
  "s_add_u32 s38, s38, 9216\n"
  "s_addc_u32 s39, s39, 0\n"
  "s_add_u32 m0, s80, 14336\n"
  "s_nop 0\n"
  "global_load_lds_dwordx4 v6, s[38:39]\n"
  "s_add_u32 s38, s38, 9216\n"
  "s_addc_u32 s39, s39, 0\n"
  "s_add_u32 m0, s80, 15360\n"
  "s_nop 0\n"
  "global_load_lds_dwordx4 v6, s[38:39]\n"
  "s_add_u32 s38, s38, 9216\n"
  "s_addc_u32 s39, s39, 0\n"
  "ds_read_b64_tr_b16 a[228:229], v5 offset:16384\n"
  "ds_read_b64_tr_b16 a[230:231], v5 offset:18432\n"
  "ds_read_b64_tr_b16 a[232:233], v5 offset:16416\n"
  "ds_read_b64_tr_b16 a[234:235], v5 offset:18464\n"
  "ds_read_b64_tr_b16 a[236:237], v5 offset:16448\n"
  "ds_read_b64_tr_b16 a[238:239], v5 offset:18496\n"
  "s_waitcnt vmcnt(32) lgkmcnt(0)\n"
  "s_add_u32 m0, s80, 16384\n"
  "s_nop 0\n"
  "global_load_lds_dwordx4 v6, s[38:39]\n"
  "s_add_u32 s38, s38, 9216\n"
  "s_addc_u32 s39, s39, 0\n"
  "s_add_u32 m0, s80, 17408\n"
  "s_nop 0\n"
  "global_load_lds_dwordx4 v6, s[38:39]\n"
  "s_add_u32 s38, s38, 9216\n"
  "s_addc_u32 s39, s39, 0\n"
  "s_add_u32 m0, s80, 18432\n"
  "s_nop 0\n"
  "global_load_lds_dwordx4 v6, s[38:39]\n"
  "s_add_u32 s38, s38, 9216\n"
  "s_addc_u32 s39, s39, 0\n"
  "s_add_u32 m0, s80, 19456\n"
  "s_nop 0\n"
  "global_load_lds_dwordx4 v6, s[38:39]\n"
  "s_add_u32 s38, s38, 9216\n"
  "s_addc_u32 s39, s39, 0\n"
  "ds_read_b64_tr_b16 a[240:241], v5 offset:20480\n"
  "ds_read_b64_tr_b16 a[242:243], v5 offset:22528\n"
  "ds_read_b64_tr_b16 a[244:245], v5 offset:20512\n"
  "ds_read_b64_tr_b16 a[246:247], v5 offset:22560\n"
  "ds_read_b64_tr_b16 a[248:249], v5 offset:20544\n"
  "ds_read_b64_tr_b16 a[250:251], v5 offset:22592\n"
  "s_waitcnt vmcnt(32) lgkmcnt(0)\n"
  "s_add_u32 m0, s80, 20480\n"
  "s_nop 0\n"
  "global_load_lds_dwordx4 v6, s[38:39]\n"
  "s_add_u32 s38, s38, 9216\n"
  "s_addc_u32 s39, s39, 0\n"
  "s_add_u32 m0, s80, 21504\n"
  "s_nop 0\n"
  "global_load_lds_dwordx4 v6, s[38:39]\n"
  "s_add_u32 s38, s38, 9216\n"
  "s_addc_u32 s39, s39, 0\n"
  "s_add_u32 m0, s80, 22528\n"
  "s_nop 0\n"
  "global_load_lds_dwordx4 v6, s[38:39]\n"
  "s_add_u32 s38, s38, 9216\n"
  "s_addc_u32 s39, s39, 0\n"
  "s_add_u32 m0, s80, 23552\n"
  "s_nop 0\n"
  "global_load_lds_dwordx4 v6, s[38:39]\n"
  "s_add_u32 s38, s38, 9216\n"
  "s_addc_u32 s39, s39, 0\n"
  "ds_read_b64_tr_b16 a[252:253], v5 offset:24576\n"
  "ds_read_b64_tr_b16 a[254:255], v5 offset:26624\n"
  "ds_read_b64_tr_b16 v[140:141], v5 offset:24608\n"
  "ds_read_b64_tr_b16 v[142:143], v5 offset:26656\n"
  "ds_read_b64_tr_b16 v[144:145], v5 offset:24640\n"
  "ds_read_b64_tr_b16 v[146:147], v5 offset:26688\n"
  "s_waitcnt vmcnt(32) lgkmcnt(0)\n"
  "s_add_u32 m0, s80, 24576\n"
  "s_nop 0\n"
  "global_load_lds_dwordx4 v6, s[38:39]\n"
  "s_add_u32 s38, s38, 9216\n"
  "s_addc_u32 s39, s39, 0\n"
  "s_add_u32 m0, s80, 25600\n"
  "s_nop 0\n"
  "global_load_lds_dwordx4 v6, s[38:39]\n"
  "s_add_u32 s38, s38, 9216\n"
  "s_addc_u32 s39, s39, 0\n"
  "s_add_u32 m0, s80, 26624\n"
  "s_nop 0\n"
  "global_load_lds_dwordx4 v6, s[38:39]\n"
  "s_add_u32 s38, s38, 9216\n"
  "s_addc_u32 s39, s39, 0\n"
  "s_add_u32 m0, s80, 27648\n"
  "s_nop 0\n"
  "global_load_lds_dwordx4 v6, s[38:39]\n"
  "s_add_u32 s38, s38, 9216\n"
  "s_addc_u32 s39, s39, 0\n"
  "s_cmp_eq_u32 s50, 7\n"
  "s_cselect_b32 s74, 1179648, 0\n"
  "s_sub_u32 s38, s38, s74\n"
  "s_subb_u32 s39, s39, 0\n"
  "ds_read_b64_tr_b16 v[148:149], v5 offset:28672\n"
  "ds_read_b64_tr_b16 v[150:151], v5 offset:30720\n"
  "ds_read_b64_tr_b16 v[152:153], v5 offset:28704\n"
  "ds_read_b64_tr_b16 v[154:155], v5 offset:30752\n"
  "ds_read_b64_tr_b16 v[156:157], v5 offset:28736\n"
  "ds_read_b64_tr_b16 v[158:159], v5 offset:30784\n"
  "s_waitcnt vmcnt(32) lgkmcnt(0)\n"
  "ds_read_b64_tr_b16 v[160:161], v5 offset:32768\n"
  "ds_read_b64_tr_b16 v[162:163], v5 offset:34816\n"
  "ds_read_b64_tr_b16 v[164:165], v5 offset:32800\n"
  "ds_read_b64_tr_b16 v[166:167], v5 offset:34848\n"
  "ds_read_b64_tr_b16 v[168:169], v5 offset:32832\n"
  "ds_read_b64_tr_b16 v[170:171], v5 offset:34880\n"
  "s_waitcnt vmcnt(28) lgkmcnt(0)\n"
  "ds_read_b64_tr_b16 v[172:173], v5 offset:36864\n"
  "ds_read_b64_tr_b16 v[174:175], v5 offset:38912\n"
  "ds_read_b64_tr_b16 v[176:177], v5 offset:36896\n"
  "ds_read_b64_tr_b16 v[178:179], v5 offset:38944\n"
  "ds_read_b64_tr_b16 v[180:181], v5 offset:36928\n"
  "ds_read_b64_tr_b16 v[182:183], v5 offset:38976\n"
  "s_waitcnt vmcnt(24) lgkmcnt(0)\n"
  "ds_read_b64_tr_b16 v[184:185], v5 offset:0\n"
  "ds_read_b64_tr_b16 v[186:187], v5 offset:2048\n"
  "ds_read_b64_tr_b16 v[188:189], v5 offset:32\n"
  "ds_read_b64_tr_b16 v[190:191], v5 offset:2080\n"
  "ds_read_b64_tr_b16 v[192:193], v5 offset:64\n"
  "ds_read_b64_tr_b16 v[194:195], v5 offset:2112\n"
  "s_waitcnt vmcnt(20) lgkmcnt(0)\n"
  "ds_read_b64_tr_b16 v[196:197], v5 offset:4096\n"
  "ds_read_b64_tr_b16 v[198:199], v5 offset:6144\n"
  "ds_read_b64_tr_b16 v[200:201], v5 offset:4128\n"
  "ds_read_b64_tr_b16 v[202:203], v5 offset:6176\n"
  "ds_read_b64_tr_b16 v[204:205], v5 offset:4160\n"
  "ds_read_b64_tr_b16 v[206:207], v5 offset:6208\n"
  "s_waitcnt vmcnt(16) lgkmcnt(0)\n"
  "ds_read_b64_tr_b16 v[208:209], v5 offset:8192\n"
  "ds_read_b64_tr_b16 v[210:211], v5 offset:10240\n"
  "ds_read_b64_tr_b16 v[212:213], v5 offset:8224\n"
  "ds_read_b64_tr_b16 v[214:215], v5 offset:10272\n"
  "ds_read_b64_tr_b16 v[216:217], v5 offset:8256\n"
  "ds_read_b64_tr_b16 v[218:219], v5 offset:10304\n"
  "s_waitcnt vmcnt(12) lgkmcnt(0)\n"
  "ds_read_b64_tr_b16 v[220:221], v5 offset:12288\n"
  "ds_read_b64_tr_b16 v[222:223], v5 offset:14336\n"
  "ds_read_b64_tr_b16 v[224:225], v5 offset:12320\n"
  "ds_read_b64_tr_b16 v[226:227], v5 offset:14368\n"
  "ds_read_b64_tr_b16 v[228:229], v5 offset:12352\n"
  "ds_read_b64_tr_b16 v[230:231], v5 offset:14400\n"
  "s_waitcnt vmcnt(8) lgkmcnt(0)\n"
  "ds_read_b64_tr_b16 v[232:233], v5 offset:16384\n"
  "ds_read_b64_tr_b16 v[234:235], v5 offset:18432\n"
  "ds_read_b64_tr_b16 v[236:237], v5 offset:16416\n"
  "ds_read_b64_tr_b16 v[238:239], v5 offset:18464\n"
  "ds_read_b64_tr_b16 v[240:241], v5 offset:16448\n"
  "ds_read_b64_tr_b16 v[242:243], v5 offset:18496\n"
  "s_waitcnt vmcnt(4) lgkmcnt(0)\n"
  "ds_read_b64_tr_b16 v[244:245], v5 offset:20480\n"
  "ds_read_b64_tr_b16 v[246:247], v5 offset:22528\n"
  "ds_read_b64_tr_b16 v[248:249], v5 offset:20512\n"
  "ds_read_b64_tr_b16 v[250:251], v5 offset:22560\n"
  "ds_read_b64_tr_b16 v[252:253], v5 offset:20544\n"
  "ds_read_b64_tr_b16 v[254:255], v5 offset:22592\n"
  "s_waitcnt vmcnt(0) lgkmcnt(0)\n"
  "ds_read_b64_tr_b16 v[112:113], v5 offset:24576\n"
  "ds_read_b64_tr_b16 v[114:115], v5 offset:26624\n"
  "ds_read_b64_tr_b16 v[116:117], v5 offset:24608\n"
  "ds_read_b64_tr_b16 v[118:119], v5 offset:26656\n"
  "ds_read_b64_tr_b16 v[120:121], v5 offset:24640\n"
  "ds_read_b64_tr_b16 v[122:123], v5 offset:26688\n"
  "s_waitcnt lgkmcnt(0)\n"
  "s_barrier\n"
  "s_mul_i32 s74, s47, 18432\n"
  "v_add_u32 v5, s74, v0\n"
  "ds_write_b128 v5, v[52:55] offset:0\n"
  "ds_write_b128 v5, v[56:59] offset:1024\n"
  "ds_write_b128 v5, v[60:63] offset:2048\n"
  "ds_write_b128 v5, v[64:67] offset:3072\n"
  "ds_write_b128 v5, v[68:71] offset:4096\n"
  "ds_write_b128 v5, v[72:75] offset:5120\n"
  "ds_write_b128 v5, v[76:79] offset:6144\n"
  "ds_write_b128 v5, v[80:83] offset:7168\n"
  "ds_write_b128 v5, v[84:87] offset:8192\n"
  "ds_write_b128 v5, v[88:91] offset:9216\n"
  "ds_write_b128 v5, v[92:95] offset:10240\n"
  "ds_write_b128 v5, v[96:99] offset:11264\n"
  "ds_write_b128 v5, v[100:103] offset:12288\n"
  "ds_write_b128 v5, v[104:107] offset:13312\n"
  "ds_write_b128 v5, v[108:111] offset:14336\n"
  "ds_write_b128 v5, v[112:115] offset:15360\n"
  "ds_write_b128 v5, v[116:119] offset:16384\n"
  "ds_write_b128 v5, v[120:123] offset:17408\n"
  "s_mul_i32 s74, s47, 6144\n"
  "s_add_u32 s74, s74, 73728\n"
  "v_add_u32 v11, s74, v0\n"
  "s_mov_b32 s57, 400000\n"
  "s_mov_b32 s56, 24576\n"
  "s_add_u32 s66, s36, 16777216\n"
  "s_addc_u32 s67, s37, 0\n"
  "s_add_u32 s74, s36, 16778624\n"
  "s_addc_u32 s75, s37, 0\n"
  "PLCO:\n"
  "v_mov_b32 v6, 0\n"
  "global_load_dword v6, v6, s[74:75] sc1\n"
  "s_waitcnt vmcnt(0)\n"
  "v_readfirstlane_b32 s76, v6\n"
  "s_cmp_ge_u32 s76, 0x40000\n"
  "s_cbranch_scc1 PDCO\n"
  "s_sub_u32 s57, s57, 1\n"
  "s_cbranch_scc1 OQUIT\n"
  "s_sleep 4\n"
  "s_branch PLCO\n"
  "PDCO:\n"
  "s_and_b32 s74, s76, 2\n"
  "s_lshr_b32 s75, s74, 1\n"
  "s_lshl_b32 s4, 0x55555555, s75\n"
  "s_mov_b32 s5, s4\n"
  "s_lshl_b32 s74, s74, 12\n"
  "v_xor_b32 v1, s74, v1\n"
  "s_xor_b32 s49, s74, s49\n"
  "s_add_u32 s68, s36, s49\n"
  "s_addc_u32 s69, s37, 0\n"
  "v_mov_b32 v124, 1\n"
  "s_cmp_eq_u32 s47, 0\n"
  "s_cselect_b64 exec, 1, 0\n"
  "global_atomic_add v124, v0, v124, s[68:69] sc0 sc1\n"
  "s_mov_b64 exec, -1\n"
  "v_mov_b32 v6, 122880\n"
  "s_cmp_eq_u32 s47, 0\n"
  "s_cselect_b64 exec, 1, 0\n"
  "s_waitcnt vmcnt(0)\n"
  "ds_write_b32 v6, v124\n"
  "s_mov_b64 exec, -1\n"
  "s_waitcnt lgkmcnt(0)\n"
  "s_barrier\n"
  "ds_read_b32 v6, v6\n"
  "s_waitcnt lgkmcnt(0)\n"
  "v_readfirstlane_b32 s54, v6\n"
  "s_barrier\n"
  "ONEXT:\n"
  "s_cmp_ge_u32 s54, 128\n"
  "s_cbranch_scc1 OQUIT\n"
  "s_cmp_ge_u32 s54, 126\n"
  "s_cbranch_scc0 OPOLL\n"
  "v_add_u32 v1, v1, v127\n"
  "v_mov_b32 v127, 0\n"
  "OPOLL:\n"
  "global_load_dword v8, v1, s[66:67] sc1\n"
  "s_lshr_b32 s74, s54, 1\n"
  "s_waitcnt vmcnt(0)\n"
  "v_cmp_ge_u32 vcc, s74, v8\n"
  "s_cbranch_vccz OGO\n"
  "s_sub_u32 s57, s57, 1\n"
  "s_cbranch_scc1 OQUIT\n"
  "s_sleep 4\n"
  "s_branch OPOLL\n"
  "OGO:\n"
  "s_lshr_b32 s75, s54, 1\n"
  "s_and_b32 s76, s54, 1\n"
  "s_mul_i32 s74, s54, 20480\n"
  "s_add_u32 s74, s74, s45\n"
  "s_add_u32 s62, s36, s74\n"
  "s_addc_u32 s63, s37, 0\n"
  "s_lshl_b32 s77, s76, 12\n"
  "s_add_u32 s77, s77, s44\n"
  "s_bitcmp1_b64 s[4:5], s75\n"
  "s_cselect_b32 s78, 8192, 0\n"
  "s_and_b32 s79, s75, 63\n"
  "s_lshl_b32 s79, s79, 21\n"
  "s_add_u32 s78, s78, s79\n"
  "s_add_u32 s78, s78, 20971520\n"
  "s_xor_b32 s79, s78, s77\n"
  "s_add_u32 s60, s36, s79\n"
  "s_addc_u32 s61, s37, 0\n"
  "s_lshl_b32 s74, s75, 11\n"
  "s_lshl_b32 s77, s76, 6\n"
  "s_add_u32 s74, s74, s77\n"
  "s_add_u32 s74, s74, s46\n"
  "s_add_u32 s64, s36, s74\n"
  "s_addc_u32 s65, s37, 0\n"
  "global_load_dwordx4 v[52:55], v0, s[60:61] offset:0 sc1\n"
  "global_load_dwordx4 v[56:59], v0, s[60:61] offset:1024 sc1\n"
  "global_load_dwordx4 v[60:63], v0, s[60:61] offset:2048 sc1\n"
  "global_load_dwordx4 v[64:67], v0, s[60:61] offset:3072 sc1\n"
  "s_cmp_eq_u32 s50, 0\n"
  "s_cselect_b32 s74, 524288, 0\n"
  "s_sub_u32 s60, s60, s74\n"
  "s_subb_u32 s61, s61, 0\n"
  "s_add_u32 s60, s60, 65536\n"
  "s_addc_u32 s61, s61, 0\n"
  "global_load_dwordx4 v[68:71], v0, s[60:61] offset:0 sc1\n"
  "global_load_dwordx4 v[72:75], v0, s[60:61] offset:1024 sc1\n"
  "global_load_dwordx4 v[76:79], v0, s[60:61] offset:2048 sc1\n"
  "global_load_dwordx4 v[80:83], v0, s[60:61] offset:3072 sc1\n"
  "s_cmp_eq_u32 s50, 1\n"
  "s_cselect_b32 s74, 524288, 0\n"
  "s_sub_u32 s60, s60, s74\n"
  "s_subb_u32 s61, s61, 0\n"
  "s_add_u32 s60, s60, 65536\n"
  "s_addc_u32 s61, s61, 0\n"
  "global_load_dwordx4 v[84:87], v0, s[60:61] offset:0 sc1\n"
  "global_load_dwordx4 v[88:91], v0, s[60:61] offset:1024 sc1\n"
  "global_load_dwordx4 v[92:95], v0, s[60:61] offset:2048 sc1\n"
  "global_load_dwordx4 v[96:99], v0, s[60:61] offset:3072 sc1\n"
  "s_cmp_eq_u32 s50, 2\n"
  "s_cselect_b32 s74, 524288, 0\n"
  "s_sub_u32 s60, s60, s74\n"
  "s_subb_u32 s61, s61, 0\n"
  "s_add_u32 s60, s60, 65536\n"
  "s_addc_u32 s61, s61, 0\n"
  "global_load_dwordx4 v[100:103], v0, s[60:61] offset:0 sc1\n"
  "global_load_dwordx4 v[104:107], v0, s[60:61] offset:1024 sc1\n"
  "global_load_dwordx4 v[108:111], v0, s[60:61] offset:2048 sc1\n"
  "global_load_dwordx4 v[112:115], v0, s[60:61] offset:3072 sc1\n"
  "s_cmp_eq_u32 s50, 3\n"
  "s_cselect_b32 s74, 524288, 0\n"
  "s_sub_u32 s60, s60, s74\n"
  "s_subb_u32 s61, s61, 0\n"
  "s_add_u32 s60, s60, 65536\n"
  "s_addc_u32 s61, s61, 0\n"
  "global_load_dwordx4 v[116:119], v0, s[60:61] offset:0 sc1\n"
  "global_load_dwordx4 v[120:123], v0, s[60:61] offset:1024 sc1\n"
  "global_load_dwordx4 v[48:51], v0, s[60:61] offset:2048 sc1\n"
  "OSEG:\n"
  "v_mov_b32 v124, 1\n"
  "s_cmp_eq_u32 s47, 0\n"
  "s_cselect_b64 exec, 1, 0\n"
  "global_atomic_add v124, v0, v124, s[68:69] sc0 sc1\n"
  "s_mov_b64 exec, -1\n"
  "global_load_dword v8, v1, s[66:67] sc1\n"
  "s_waitcnt vmcnt(20)\n"
  "v_mfma_f32_16x16x32_bf16 v[12:15], a[0:3], v[52:55], 0\n"
  "v_mfma_f32_16x16x32_bf16 v[16:19], a[4:7], v[52:55], 0\n"
  "v_mfma_f32_16x16x32_bf16 v[20:23], a[8:11], v[52:55], 0\n"
  "global_load_dwordx4 v[52:55], v0, s[60:61] offset:3072 sc1\n"
  "s_cmp_eq_u32 s50, 4\n"
  "s_cselect_b32 s74, 524288, 0\n"
  "s_sub_u32 s60, s60, s74\n"
  "s_subb_u32 s61, s61, 0\n"
  "s_add_u32 s60, s60, 65536\n"
  "s_addc_u32 s61, s61, 0\n"
  "s_waitcnt vmcnt(20)\n"
  "v_mfma_f32_16x16x32_bf16 v[12:15], a[12:15], v[56:59], v[12:15]\n"
  "v_mfma_f32_16x16x32_bf16 v[16:19], a[16:19], v[56:59], v[16:19]\n"
  "v_mfma_f32_16x16x32_bf16 v[20:23], a[20:23], v[56:59], v[20:23]\n"
  "global_load_dwordx4 v[56:59], v0, s[60:61] offset:0 sc1\n"
  "s_waitcnt vmcnt(20)\n"
  "v_mfma_f32_16x16x32_bf16 v[12:15], a[24:27], v[60:63], v[12:15]\n"
  "v_mfma_f32_16x16x32_bf16 v[16:19], a[28:31], v[60:63], v[16:19]\n"
  "v_mfma_f32_16x16x32_bf16 v[20:23], a[32:35], v[60:63], v[20:23]\n"
  "global_load_dwordx4 v[60:63], v0, s[60:61] offset:1024 sc1\n"
  "s_waitcnt vmcnt(20)\n"
  "v_mfma_f32_16x16x32_bf16 v[12:15], a[36:39], v[64:67], v[12:15]\n"
  "v_mfma_f32_16x16x32_bf16 v[16:19], a[40:43], v[64:67], v[16:19]\n"
  "v_mfma_f32_16x16x32_bf16 v[20:23], a[44:47], v[64:67], v[20:23]\n"
  "global_load_dwordx4 v[64:67], v0, s[60:61] offset:2048 sc1\n"
  "s_waitcnt vmcnt(20)\n"
  "v_mfma_f32_16x16x32_bf16 v[12:15], a[48:51], v[68:71], v[12:15]\n"
  "v_mfma_f32_16x16x32_bf16 v[16:19], a[52:55], v[68:71], v[16:19]\n"
  "v_mfma_f32_16x16x32_bf16 v[20:23], a[56:59], v[68:71], v[20:23]\n"
  "global_load_dwordx4 v[68:71], v0, s[60:61] offset:3072 sc1\n"
  "s_cmp_eq_u32 s50, 5\n"
  "s_cselect_b32 s74, 524288, 0\n"
  "s_sub_u32 s60, s60, s74\n"
  "s_subb_u32 s61, s61, 0\n"
  "s_add_u32 s60, s60, 65536\n"
  "s_addc_u32 s61, s61, 0\n"
  "s_waitcnt vmcnt(20)\n"
  "v_mfma_f32_16x16x32_bf16 v[12:15], a[60:63], v[72:75], v[12:15]\n"
  "v_mfma_f32_16x16x32_bf16 v[16:19], a[64:67], v[72:75], v[16:19]\n"
  "v_mfma_f32_16x16x32_bf16 v[20:23], a[68:71], v[72:75], v[20:23]\n"
  "global_load_dwordx4 v[72:75], v0, s[60:61] offset:0 sc1\n"
  "s_waitcnt vmcnt(20)\n"
  "v_mfma_f32_16x16x32_bf16 v[12:15], a[72:75], v[76:79], v[12:15]\n"
  "v_mfma_f32_16x16x32_bf16 v[16:19], a[76:79], v[76:79], v[16:19]\n"
  "v_mfma_f32_16x16x32_bf16 v[20:23], a[80:83], v[76:79], v[20:23]\n"
  "global_load_dwordx4 v[76:79], v0, s[60:61] offset:1024 sc1\n"
  "s_waitcnt vmcnt(20)\n"
  "v_mfma_f32_16x16x32_bf16 v[12:15], a[84:87], v[80:83], v[12:15]\n"
  "v_mfma_f32_16x16x32_bf16 v[16:19], a[88:91], v[80:83], v[16:19]\n"
  "v_mfma_f32_16x16x32_bf16 v[20:23], a[92:95], v[80:83], v[20:23]\n"
  "global_load_dwordx4 v[80:83], v0, s[60:61] offset:2048 sc1\n"
  "s_waitcnt vmcnt(20)\n"
  "v_mfma_f32_16x16x32_bf16 v[12:15], a[96:99], v[84:87], v[12:15]\n"
  "v_mfma_f32_16x16x32_bf16 v[16:19], a[100:103], v[84:87], v[16:19]\n"
  "v_mfma_f32_16x16x32_bf16 v[20:23], a[104:107], v[84:87], v[20:23]\n"
  "global_load_dwordx4 v[84:87], v0, s[60:61] offset:3072 sc1\n"
  "s_cmp_eq_u32 s50, 6\n"
  "s_cselect_b32 s74, 524288, 0\n"
  "s_sub_u32 s60, s60, s74\n"
  "s_subb_u32 s61, s61, 0\n"
  "s_add_u32 s60, s60, 65536\n"
  "s_addc_u32 s61, s61, 0\n"
  "s_waitcnt vmcnt(20)\n"
  "v_mfma_f32_16x16x32_bf16 v[12:15], a[108:111], v[88:91], v[12:15]\n"
  "v_mfma_f32_16x16x32_bf16 v[16:19], a[112:115], v[88:91], v[16:19]\n"
  "v_mfma_f32_16x16x32_bf16 v[20:23], a[116:119], v[88:91], v[20:23]\n"
  "global_load_dwordx4 v[88:91], v0, s[60:61] offset:0 sc1\n"
  "s_waitcnt vmcnt(20)\n"
  "v_mfma_f32_16x16x32_bf16 v[12:15], a[120:123], v[92:95], v[12:15]\n"
  "v_mfma_f32_16x16x32_bf16 v[16:19], a[124:127], v[92:95], v[16:19]\n"
  "v_mfma_f32_16x16x32_bf16 v[20:23], a[128:131], v[92:95], v[20:23]\n"
  "global_load_dwordx4 v[92:95], v0, s[60:61] offset:1024 sc1\n"
  "s_waitcnt vmcnt(20)\n"
  "v_mfma_f32_16x16x32_bf16 v[12:15], a[132:135], v[96:99], v[12:15]\n"
  "v_mfma_f32_16x16x32_bf16 v[16:19], a[136:139], v[96:99], v[16:19]\n"
  "v_mfma_f32_16x16x32_bf16 v[20:23], a[140:143], v[96:99], v[20:23]\n"
  "global_load_dwordx4 v[96:99], v0, s[60:61] offset:2048 sc1\n"
  "s_waitcnt vmcnt(20)\n"
  "v_mfma_f32_16x16x32_bf16 v[12:15], a[144:147], v[100:103], v[12:15]\n"
  "v_mfma_f32_16x16x32_bf16 v[16:19], a[148:151], v[100:103], v[16:19]\n"
  "v_mfma_f32_16x16x32_bf16 v[20:23], a[152:155], v[100:103], v[20:23]\n"
  "global_load_dwordx4 v[100:103], v0, s[60:61] offset:3072 sc1\n"
  "s_cmp_eq_u32 s50, 7\n"
  "s_cselect_b32 s74, 524288, 0\n"
  "s_sub_u32 s60, s60, s74\n"
  "s_subb_u32 s61, s61, 0\n"
  "s_add_u32 s60, s60, 65536\n"
  "s_addc_u32 s61, s61, 0\n"
  "s_waitcnt vmcnt(20)\n"
  "v_mfma_f32_16x16x32_bf16 v[12:15], a[156:159], v[104:107], v[12:15]\n"
  "v_mfma_f32_16x16x32_bf16 v[16:19], a[160:163], v[104:107], v[16:19]\n"
  "v_mfma_f32_16x16x32_bf16 v[20:23], a[164:167], v[104:107], v[20:23]\n"
  "global_load_dwordx4 v[104:107], v0, s[62:63] offset:-2048 sc1\n"
  "s_waitcnt vmcnt(20)\n"
  "v_mfma_f32_16x16x32_bf16 v[12:15], a[168:171], v[108:111], v[12:15]\n"
  "v_mfma_f32_16x16x32_bf16 v[16:19], a[172:175], v[108:111], v[16:19]\n"
  "v_mfma_f32_16x16x32_bf16 v[20:23], a[176:179], v[108:111], v[20:23]\n"
  "global_load_dwordx4 v[108:111], v0, s[62:63] offset:-1024 sc1\n"
  "s_waitcnt vmcnt(20)\n"
  "v_mfma_f32_16x16x32_bf16 v[12:15], a[180:183], v[112:115], v[12:15]\n"
  "v_mfma_f32_16x16x32_bf16 v[16:19], a[184:187], v[112:115], v[16:19]\n"
  "v_mfma_f32_16x16x32_bf16 v[20:23], a[188:191], v[112:115], v[20:23]\n"
  "global_load_dwordx4 v[112:115], v0, s[62:63] offset:0 sc1\n"
  "s_waitcnt vmcnt(20)\n"
  "v_mfma_f32_16x16x32_bf16 v[12:15], a[192:195], v[116:119], v[12:15]\n"
  "v_mfma_f32_16x16x32_bf16 v[16:19], a[196:199], v[116:119], v[16:19]\n"
  "v_mfma_f32_16x16x32_bf16 v[20:23], a[200:203], v[116:119], v[20:23]\n"
  "global_load_dwordx4 v[116:119], v0, s[62:63] offset:1024 sc1\n"
  "s_waitcnt vmcnt(20)\n"
  "v_mfma_f32_16x16x32_bf16 v[12:15], a[204:207], v[120:123], v[12:15]\n"
  "v_mfma_f32_16x16x32_bf16 v[16:19], a[208:211], v[120:123], v[16:19]\n"
  "v_mfma_f32_16x16x32_bf16 v[20:23], a[212:215], v[120:123], v[20:23]\n"
  "global_load_dwordx4 v[120:123], v0, s[62:63] offset:2048 sc1\n"
  "s_waitcnt vmcnt(20)\n"
  "v_mfma_f32_16x16x32_bf16 v[12:15], a[216:219], v[48:51], v[12:15]\n"
  "v_mfma_f32_16x16x32_bf16 v[16:19], a[220:223], v[48:51], v[16:19]\n"
  "v_mfma_f32_16x16x32_bf16 v[20:23], a[224:227], v[48:51], v[20:23]\n"
  "global_load_dword v9, v2, s[64:65] sc1\n"
  "v_mov_b32 v6, 122880\n"
  "s_cmp_eq_u32 s47, 0\n"
  "s_cselect_b64 exec, 1, 0\n"
  "s_waitcnt vmcnt(20)\n"
  "ds_write_b32 v6, v124\n"
  "s_mov_b64 exec, -1\n"
  "s_waitcnt lgkmcnt(0)\n"
  "s_barrier\n"
  "ds_read_b32 v6, v6\n"
  "s_waitcnt lgkmcnt(0)\n"
  "v_readfirstlane_b32 s55, v6\n"
  "s_lshr_b32 s75, s55, 1\n"
  "s_and_b32 s76, s55, 1\n"
  "s_mul_i32 s74, s55, 20480\n"
  "s_add_u32 s74, s74, s45\n"
  "s_add_u32 s62, s36, s74\n"
  "s_addc_u32 s63, s37, 0\n"
  "s_lshl_b32 s77, s76, 12\n"
  "s_add_u32 s77, s77, s44\n"
  "s_bitcmp1_b64 s[4:5], s75\n"
  "s_cselect_b32 s78, 8192, 0\n"
  "s_and_b32 s79, s75, 63\n"
  "s_lshl_b32 s79, s79, 21\n"
  "s_add_u32 s78, s78, s79\n"
  "s_add_u32 s78, s78, 20971520\n"
  "s_xor_b32 s79, s78, s77\n"
  "s_add_u32 s60, s36, s79\n"
  "s_addc_u32 s61, s37, 0\n"
  "s_lshl_b32 s74, s75, 11\n"
  "s_lshl_b32 s77, s76, 6\n"
  "s_add_u32 s74, s74, s77\n"
  "s_add_u32 s74, s74, s46\n"
  "s_add_u32 s64, s36, s74\n"
  "s_addc_u32 s65, s37, 0\n"
  "s_cmp_lt_u32 s55, 126\n"
  "s_cselect_b32 s75, s75, 64\n"
  "s_waitcnt vmcnt(19)\n"
  "v_cmp_ge_u32 vcc, s75, v8\n"
  "s_cbranch_vccnz ODRAIN\n"
  "s_waitcnt vmcnt(18)\n"
  "v_mfma_f32_16x16x32_bf16 v[12:15], a[228:231], v[52:55], v[12:15]\n"
  "v_mfma_f32_16x16x32_bf16 v[16:19], a[232:235], v[52:55], v[16:19]\n"
  "v_mfma_f32_16x16x32_bf16 v[20:23], a[236:239], v[52:55], v[20:23]\n"
  "global_load_dwordx4 v[52:55], v0, s[60:61] offset:0 sc1\n"
  "s_waitcnt vmcnt(18)\n"
  "v_mfma_f32_16x16x32_bf16 v[12:15], a[240:243], v[56:59], v[12:15]\n"
  "v_mfma_f32_16x16x32_bf16 v[16:19], a[244:247], v[56:59], v[16:19]\n"
  "v_mfma_f32_16x16x32_bf16 v[20:23], a[248:251], v[56:59], v[20:23]\n"
  "global_load_dwordx4 v[56:59], v0, s[60:61] offset:1024 sc1\n"
  "s_waitcnt vmcnt(18)\n"
  "v_mfma_f32_16x16x32_bf16 v[12:15], a[252:255], v[60:63], v[12:15]\n"
  "v_mfma_f32_16x16x32_bf16 v[16:19], v[140:143], v[60:63], v[16:19]\n"
  "v_mfma_f32_16x16x32_bf16 v[20:23], v[144:147], v[60:63], v[20:23]\n"
  "global_load_dwordx4 v[60:63], v0, s[60:61] offset:2048 sc1\n"
  "s_waitcnt vmcnt(18)\n"
  "v_mfma_f32_16x16x32_bf16 v[12:15], v[148:151], v[64:67], v[12:15]\n"
  "v_mfma_f32_16x16x32_bf16 v[16:19], v[152:155], v[64:67], v[16:19]\n"
  "v_mfma_f32_16x16x32_bf16 v[20:23], v[156:159], v[64:67], v[20:23]\n"
  "global_load_dwordx4 v[64:67], v0, s[60:61] offset:3072 sc1\n"
  "s_cmp_eq_u32 s50, 0\n"
  "s_cselect_b32 s74, 524288, 0\n"
  "s_sub_u32 s60, s60, s74\n"
  "s_subb_u32 s61, s61, 0\n"
  "s_add_u32 s60, s60, 65536\n"
  "s_addc_u32 s61, s61, 0\n"
  "s_waitcnt vmcnt(18)\n"
  "v_mfma_f32_16x16x32_bf16 v[12:15], v[160:163], v[68:71], v[12:15]\n"
  "v_mfma_f32_16x16x32_bf16 v[16:19], v[164:167], v[68:71], v[16:19]\n"
  "v_mfma_f32_16x16x32_bf16 v[20:23], v[168:171], v[68:71], v[20:23]\n"
  "global_load_dwordx4 v[68:71], v0, s[60:61] offset:0 sc1\n"
  "s_waitcnt vmcnt(18)\n"
  "v_mfma_f32_16x16x32_bf16 v[12:15], v[172:175], v[72:75], v[12:15]\n"
  "v_mfma_f32_16x16x32_bf16 v[16:19], v[176:179], v[72:75], v[16:19]\n"
  "v_mfma_f32_16x16x32_bf16 v[20:23], v[180:183], v[72:75], v[20:23]\n"
  "global_load_dwordx4 v[72:75], v0, s[60:61] offset:1024 sc1\n"
  "s_waitcnt vmcnt(18)\n"
  "v_mfma_f32_16x16x32_bf16 v[12:15], v[184:187], v[76:79], v[12:15]\n"
  "v_mfma_f32_16x16x32_bf16 v[16:19], v[188:191], v[76:79], v[16:19]\n"
  "v_mfma_f32_16x16x32_bf16 v[20:23], v[192:195], v[76:79], v[20:23]\n"
  "global_load_dwordx4 v[76:79], v0, s[60:61] offset:2048 sc1\n"
  "s_waitcnt vmcnt(18)\n"
  "v_mfma_f32_16x16x32_bf16 v[12:15], v[196:199], v[80:83], v[12:15]\n"
  "v_mfma_f32_16x16x32_bf16 v[16:19], v[200:203], v[80:83], v[16:19]\n"
  "v_mfma_f32_16x16x32_bf16 v[20:23], v[204:207], v[80:83], v[20:23]\n"
  "global_load_dwordx4 v[80:83], v0, s[60:61] offset:3072 sc1\n"
  "s_cmp_eq_u32 s50, 1\n"
  "s_cselect_b32 s74, 524288, 0\n"
  "s_sub_u32 s60, s60, s74\n"
  "s_subb_u32 s61, s61, 0\n"
  "s_add_u32 s60, s60, 65536\n"
  "s_addc_u32 s61, s61, 0\n"
  "s_waitcnt vmcnt(18)\n"
  "v_mfma_f32_16x16x32_bf16 v[12:15], v[208:211], v[84:87], v[12:15]\n"
  "v_mfma_f32_16x16x32_bf16 v[16:19], v[212:215], v[84:87], v[16:19]\n"
  "v_mfma_f32_16x16x32_bf16 v[20:23], v[216:219], v[84:87], v[20:23]\n"
  "global_load_dwordx4 v[84:87], v0, s[60:61] offset:0 sc1\n"
  "s_waitcnt vmcnt(18)\n"
  "v_mfma_f32_16x16x32_bf16 v[12:15], v[220:223], v[88:91], v[12:15]\n"
  "v_mfma_f32_16x16x32_bf16 v[16:19], v[224:227], v[88:91], v[16:19]\n"
  "v_mfma_f32_16x16x32_bf16 v[20:23], v[228:231], v[88:91], v[20:23]\n"
  "global_load_dwordx4 v[88:91], v0, s[60:61] offset:1024 sc1\n"
  "s_waitcnt vmcnt(18)\n"
  "v_mfma_f32_16x16x32_bf16 v[12:15], v[232:235], v[92:95], v[12:15]\n"
  "v_mfma_f32_16x16x32_bf16 v[16:19], v[236:239], v[92:95], v[16:19]\n"
  "v_mfma_f32_16x16x32_bf16 v[20:23], v[240:243], v[92:95], v[20:23]\n"
  "global_load_dwordx4 v[92:95], v0, s[60:61] offset:2048 sc1\n"
  "ds_read_b128 v[36:39], v5 offset:15360\n"
  "ds_read_b128 v[40:43], v5 offset:16384\n"
  "ds_read_b128 v[44:47], v5 offset:17408\n"
  "s_waitcnt vmcnt(18)\n"
  "v_mfma_f32_16x16x32_bf16 v[12:15], v[244:247], v[96:99], v[12:15]\n"
  "v_mfma_f32_16x16x32_bf16 v[16:19], v[248:251], v[96:99], v[16:19]\n"
  "v_mfma_f32_16x16x32_bf16 v[20:23], v[252:255], v[96:99], v[20:23]\n"
  "global_load_dwordx4 v[96:99], v0, s[60:61] offset:3072 sc1\n"
  "s_cmp_eq_u32 s50, 2\n"
  "s_cselect_b32 s74, 524288, 0\n"
  "s_sub_u32 s60, s60, s74\n"
  "s_subb_u32 s61, s61, 0\n"
  "s_add_u32 s60, s60, 65536\n"
  "s_addc_u32 s61, s61, 0\n"
  "ds_read_b128 v[128:131], v5 offset:0\n"
  "ds_read_b128 v[132:135], v5 offset:1024\n"
  "ds_read_b128 v[136:139], v5 offset:2048\n"
  "s_waitcnt vmcnt(18) lgkmcnt(5)\n"
  "v_mfma_f32_16x16x32_bf16 v[12:15], v[36:39], v[100:103], v[12:15]\n"
  "s_waitcnt lgkmcnt(4)\n"
  "v_mfma_f32_16x16x32_bf16 v[16:19], v[40:43], v[100:103], v[16:19]\n"
  "s_waitcnt lgkmcnt(3)\n"
  "v_mfma_f32_16x16x32_bf16 v[20:23], v[44:47], v[100:103], v[20:23]\n"
  "global_load_dwordx4 v[100:103], v0, s[60:61] offset:0 sc1\n"
  "ds_read_b128 v[36:39], v5 offset:3072\n"
  "ds_read_b128 v[40:43], v5 offset:4096\n"
  "ds_read_b128 v[44:47], v5 offset:5120\n"
  "s_waitcnt vmcnt(18) lgkmcnt(5)\n"
  "v_mfma_f32_16x16x32_bf16 v[24:27], v[128:131], v[104:107], 0\n"
  "s_waitcnt lgkmcnt(4)\n"
  "v_mfma_f32_16x16x32_bf16 v[28:31], v[132:135], v[104:107], 0\n"
  "s_waitcnt lgkmcnt(3)\n"
  "v_mfma_f32_16x16x32_bf16 v[32:35], v[136:139], v[104:107], 0\n"
  "global_load_dwordx4 v[104:107], v0, s[60:61] offset:1024 sc1\n"
  "ds_read_b128 v[128:131], v5 offset:6144\n"
  "ds_read_b128 v[132:135], v5 offset:7168\n"
  "ds_read_b128 v[136:139], v5 offset:8192\n"
  "s_waitcnt vmcnt(18) lgkmcnt(5)\n"
  "v_mfma_f32_16x16x32_bf16 v[24:27], v[36:39], v[108:111], v[24:27]\n"
  "s_waitcnt lgkmcnt(4)\n"
  "v_mfma_f32_16x16x32_bf16 v[28:31], v[40:43], v[108:111], v[28:31]\n"
  "s_waitcnt lgkmcnt(3)\n"
  "v_mfma_f32_16x16x32_bf16 v[32:35], v[44:47], v[108:111], v[32:35]\n"
  "global_load_dwordx4 v[108:111], v0, s[60:61] offset:2048 sc1\n"
  "ds_read_b128 v[36:39], v5 offset:9216\n"
  "ds_read_b128 v[40:43], v5 offset:10240\n"
  "ds_read_b128 v[44:47], v5 offset:11264\n"
  "s_waitcnt vmcnt(18) lgkmcnt(5)\n"
  "v_mfma_f32_16x16x32_bf16 v[24:27], v[128:131], v[112:115], v[24:27]\n"
  "s_waitcnt lgkmcnt(4)\n"
  "v_mfma_f32_16x16x32_bf16 v[28:31], v[132:135], v[112:115], v[28:31]\n"
  "s_waitcnt lgkmcnt(3)\n"
  "v_mfma_f32_16x16x32_bf16 v[32:35], v[136:139], v[112:115], v[32:35]\n"
  "global_load_dwordx4 v[112:115], v0, s[60:61] offset:3072 sc1\n"
  "s_cmp_eq_u32 s50, 3\n"
  "s_cselect_b32 s74, 524288, 0\n"
  "s_sub_u32 s60, s60, s74\n"
  "s_subb_u32 s61, s61, 0\n"
  "s_add_u32 s60, s60, 65536\n"
  "s_addc_u32 s61, s61, 0\n"
  "ds_read_b128 v[128:131], v5 offset:12288\n"
  "ds_read_b128 v[132:135], v5 offset:13312\n"
  "ds_read_b128 v[136:139], v5 offset:14336\n"
  "s_waitcnt vmcnt(18) lgkmcnt(5)\n"
  "v_mfma_f32_16x16x32_bf16 v[24:27], v[36:39], v[116:119], v[24:27]\n"
  "s_waitcnt lgkmcnt(4)\n"
  "v_mfma_f32_16x16x32_bf16 v[28:31], v[40:43], v[116:119], v[28:31]\n"
  "s_waitcnt lgkmcnt(3)\n"
  "v_mfma_f32_16x16x32_bf16 v[32:35], v[44:47], v[116:119], v[32:35]\n"
  "global_load_dwordx4 v[116:119], v0, s[60:61] offset:0 sc1\n"
  "s_waitcnt vmcnt(18) lgkmcnt(2)\n"
  "v_mfma_f32_16x16x32_bf16 v[24:27], v[128:131], v[120:123], v[24:27]\n"
  "s_waitcnt lgkmcnt(1)\n"
  "v_mfma_f32_16x16x32_bf16 v[28:31], v[132:135], v[120:123], v[28:31]\n"
  "s_waitcnt lgkmcnt(0)\n"
  "v_mfma_f32_16x16x32_bf16 v[32:35], v[136:139], v[120:123], v[32:35]\n"
  "global_load_dwordx4 v[120:123], v0, s[60:61] offset:1024 sc1\n"
  "s_waitcnt vmcnt(18)\n"
  "v_mov_b32 v10, v9\n"
  "global_load_dwordx4 v[48:51], v0, s[60:61] offset:2048 sc1\n"
  "ds_write_b128 v11, v[12:15] offset:0\n"
  "ds_write_b128 v11, v[16:19] offset:1024\n"
  "ds_write_b128 v11, v[20:23] offset:2048\n"
  "ds_write_b128 v11, v[24:27] offset:3072\n"
  "ds_write_b128 v11, v[28:31] offset:4096\n"
  "ds_write_b128 v11, v[32:35] offset:5120\n"
  "v_sub_f32 v6, 1.0, v10\n"
  "v_cvt_pk_bf16_f32 v6, v6, v6\n"
  "v_lshlrev_b32 v6, 16, v6\n"
  "s_waitcnt lgkmcnt(0)\n"
  "s_barrier\n"
  "ds_read_b128 v[28:31], v3 offset:0\n"
  "ds_read_b128 v[32:35], v3 offset:6144\n"
  "ds_read_b128 v[36:39], v3 offset:12288\n"
  "ds_read_b128 v[40:43], v3 offset:18432\n"
  "ds_read_b128 v[12:15], v3 offset:3072\n"
  "ds_read_b128 v[16:19], v3 offset:9216\n"
  "ds_read_b128 v[20:23], v3 offset:15360\n"
  "ds_read_b128 v[24:27], v3 offset:21504\n"
  "v_add_u32 v11, s56, v11\n"
  "v_add_u32 v3, s56, v3\n"
  "s_sub_u32 s56, 0, s56\n"
  "s_mul_i32 s74, s54, 18432\n"
  "s_add_u32 s70, s40, s74\n"
  "s_addc_u32 s71, s41, 0\n"
  "s_waitcnt lgkmcnt(6)\n"
  "v_add_f32 v28, v28, v32\n"
  "s_waitcnt lgkmcnt(4)\n"
  "v_add_f32 v36, v36, v40\n"
  "v_add_f32 v28, v28, v36\n"
  "v_add_f32 v29, v29, v33\n"
  "v_add_f32 v37, v37, v41\n"
  "v_add_f32 v29, v29, v37\n"
  "v_add_f32 v30, v30, v34\n"
  "v_add_f32 v38, v38, v42\n"
  "v_add_f32 v30, v30, v38\n"
  "v_add_f32 v31, v31, v35\n"
  "v_add_f32 v39, v39, v43\n"
  "v_add_f32 v31, v31, v39\n"
  "v_cvt_pk_bf16_f32 v28, v28, v29\n"
  "v_and_b32 v29, 0xffff0000, v28\n"
  "v_lshlrev_b32 v28, 16, v28\n"
  "v_cvt_pk_bf16_f32 v30, v30, v31\n"
  "v_and_b32 v31, 0xffff0000, v30\n"
  "v_lshlrev_b32 v30, 16, v30\n"
  "v_mul_f32 v28, v6, v28\n"
  "v_mul_f32 v29, v6, v29\n"
  "v_mul_f32 v30, v6, v30\n"
  "v_mul_f32 v31, v6, v31\n"
  "v_cvt_pk_bf16_f32 v28, v28, v29\n"
  "v_and_b32 v29, 0xffff0000, v28\n"
  "v_lshlrev_b32 v28, 16, v28\n"
  "v_cvt_pk_bf16_f32 v30, v30, v31\n"
  "v_and_b32 v31, 0xffff0000, v30\n"
  "v_lshlrev_b32 v30, 16, v30\n"
  "s_waitcnt lgkmcnt(2)\n"
  "v_add_f32 v12, v12, v16\n"
  "s_waitcnt lgkmcnt(0)\n"
  "v_add_f32 v20, v20, v24\n"
  "v_add_f32 v12, v12, v20\n"
  "v_add_f32 v13, v13, v17\n"
  "v_add_f32 v21, v21, v25\n"
  "v_add_f32 v13, v13, v21\n"
  "v_add_f32 v14, v14, v18\n"
  "v_add_f32 v22, v22, v26\n"
  "v_add_f32 v14, v14, v22\n"
  "v_add_f32 v15, v15, v19\n"
  "v_add_f32 v23, v23, v27\n"
  "v_add_f32 v15, v15, v23\n"
  "v_cvt_pk_bf16_f32 v12, v12, v13\n"
  "v_and_b32 v13, 0xffff0000, v12\n"
  "v_lshlrev_b32 v12, 16, v12\n"
  "v_cvt_pk_bf16_f32 v14, v14, v15\n"
  "v_and_b32 v15, 0xffff0000, v14\n"
  "v_lshlrev_b32 v14, 16, v14\n"
  "v_mul_f32 v12, v10, v12\n"
  "v_mul_f32 v13, v10, v13\n"
  "v_mul_f32 v14, v10, v14\n"
  "v_mul_f32 v15, v10, v15\n"
  "v_cvt_pk_bf16_f32 v12, v12, v13\n"
  "v_and_b32 v13, 0xffff0000, v12\n"
  "v_lshlrev_b32 v12, 16, v12\n"
  "v_cvt_pk_bf16_f32 v14, v14, v15\n"
  "v_and_b32 v15, 0xffff0000, v14\n"
  "v_lshlrev_b32 v14, 16, v14\n"
  "v_add_f32 v28, v28, v12\n"
  "v_add_f32 v29, v29, v13\n"
  "v_add_f32 v30, v30, v14\n"
  "v_add_f32 v31, v31, v15\n"
  "v_cvt_pk_bf16_f32 v28, v28, v29\n"
  "v_cvt_pk_bf16_f32 v29, v30, v31\n"
  "s_bfm_b64 exec, 48, 0\n"
  "global_store_dwordx2 v4, v[28:29], s[70:71]\n"
  "s_mov_b64 exec, -1\n"
  "s_mov_b32 s54, s55\n"
  "s_branch OSEG\n"
  "ODRAIN:\n"
  "s_waitcnt vmcnt(18)\n"
  "v_mfma_f32_16x16x32_bf16 v[12:15], a[228:231], v[52:55], v[12:15]\n"
  "v_mfma_f32_16x16x32_bf16 v[16:19], a[232:235], v[52:55], v[16:19]\n"
  "v_mfma_f32_16x16x32_bf16 v[20:23], a[236:239], v[52:55], v[20:23]\n"
  "s_waitcnt vmcnt(17)\n"
  "v_mfma_f32_16x16x32_bf16 v[12:15], a[240:243], v[56:59], v[12:15]\n"
  "v_mfma_f32_16x16x32_bf16 v[16:19], a[244:247], v[56:59], v[16:19]\n"
  "v_mfma_f32_16x16x32_bf16 v[20:23], a[248:251], v[56:59], v[20:23]\n"
  "s_waitcnt vmcnt(16)\n"
  "v_mfma_f32_16x16x32_bf16 v[12:15], a[252:255], v[60:63], v[12:15]\n"
  "v_mfma_f32_16x16x32_bf16 v[16:19], v[140:143], v[60:63], v[16:19]\n"
  "v_mfma_f32_16x16x32_bf16 v[20:23], v[144:147], v[60:63], v[20:23]\n"
  "s_waitcnt vmcnt(15)\n"
  "v_mfma_f32_16x16x32_bf16 v[12:15], v[148:151], v[64:67], v[12:15]\n"
  "v_mfma_f32_16x16x32_bf16 v[16:19], v[152:155], v[64:67], v[16:19]\n"
  "v_mfma_f32_16x16x32_bf16 v[20:23], v[156:159], v[64:67], v[20:23]\n"
  "s_waitcnt vmcnt(14)\n"
  "v_mfma_f32_16x16x32_bf16 v[12:15], v[160:163], v[68:71], v[12:15]\n"
  "v_mfma_f32_16x16x32_bf16 v[16:19], v[164:167], v[68:71], v[16:19]\n"
  "v_mfma_f32_16x16x32_bf16 v[20:23], v[168:171], v[68:71], v[20:23]\n"
  "s_waitcnt vmcnt(13)\n"
  "v_mfma_f32_16x16x32_bf16 v[12:15], v[172:175], v[72:75], v[12:15]\n"
  "v_mfma_f32_16x16x32_bf16 v[16:19], v[176:179], v[72:75], v[16:19]\n"
  "v_mfma_f32_16x16x32_bf16 v[20:23], v[180:183], v[72:75], v[20:23]\n"
  "s_waitcnt vmcnt(12)\n"
  "v_mfma_f32_16x16x32_bf16 v[12:15], v[184:187], v[76:79], v[12:15]\n"
  "v_mfma_f32_16x16x32_bf16 v[16:19], v[188:191], v[76:79], v[16:19]\n"
  "v_mfma_f32_16x16x32_bf16 v[20:23], v[192:195], v[76:79], v[20:23]\n"
  "s_waitcnt vmcnt(11)\n"
  "v_mfma_f32_16x16x32_bf16 v[12:15], v[196:199], v[80:83], v[12:15]\n"
  "v_mfma_f32_16x16x32_bf16 v[16:19], v[200:203], v[80:83], v[16:19]\n"
  "v_mfma_f32_16x16x32_bf16 v[20:23], v[204:207], v[80:83], v[20:23]\n"
  "s_waitcnt vmcnt(10)\n"
  "v_mfma_f32_16x16x32_bf16 v[12:15], v[208:211], v[84:87], v[12:15]\n"
  "v_mfma_f32_16x16x32_bf16 v[16:19], v[212:215], v[84:87], v[16:19]\n"
  "v_mfma_f32_16x16x32_bf16 v[20:23], v[216:219], v[84:87], v[20:23]\n"
  "s_waitcnt vmcnt(9)\n"
  "v_mfma_f32_16x16x32_bf16 v[12:15], v[220:223], v[88:91], v[12:15]\n"
  "v_mfma_f32_16x16x32_bf16 v[16:19], v[224:227], v[88:91], v[16:19]\n"
  "v_mfma_f32_16x16x32_bf16 v[20:23], v[228:231], v[88:91], v[20:23]\n"
  "s_waitcnt vmcnt(8)\n"
  "v_mfma_f32_16x16x32_bf16 v[12:15], v[232:235], v[92:95], v[12:15]\n"
  "v_mfma_f32_16x16x32_bf16 v[16:19], v[236:239], v[92:95], v[16:19]\n"
  "v_mfma_f32_16x16x32_bf16 v[20:23], v[240:243], v[92:95], v[20:23]\n"
  "ds_read_b128 v[36:39], v5 offset:15360\n"
  "ds_read_b128 v[40:43], v5 offset:16384\n"
  "ds_read_b128 v[44:47], v5 offset:17408\n"
  "s_waitcnt vmcnt(7)\n"
  "v_mfma_f32_16x16x32_bf16 v[12:15], v[244:247], v[96:99], v[12:15]\n"
  "v_mfma_f32_16x16x32_bf16 v[16:19], v[248:251], v[96:99], v[16:19]\n"
  "v_mfma_f32_16x16x32_bf16 v[20:23], v[252:255], v[96:99], v[20:23]\n"
  "ds_read_b128 v[128:131], v5 offset:0\n"
  "ds_read_b128 v[132:135], v5 offset:1024\n"
  "ds_read_b128 v[136:139], v5 offset:2048\n"
  "s_waitcnt vmcnt(6) lgkmcnt(5)\n"
  "v_mfma_f32_16x16x32_bf16 v[12:15], v[36:39], v[100:103], v[12:15]\n"
  "s_waitcnt lgkmcnt(4)\n"
  "v_mfma_f32_16x16x32_bf16 v[16:19], v[40:43], v[100:103], v[16:19]\n"
  "s_waitcnt lgkmcnt(3)\n"
  "v_mfma_f32_16x16x32_bf16 v[20:23], v[44:47], v[100:103], v[20:23]\n"
  "ds_read_b128 v[36:39], v5 offset:3072\n"
  "ds_read_b128 v[40:43], v5 offset:4096\n"
  "ds_read_b128 v[44:47], v5 offset:5120\n"
  "s_waitcnt vmcnt(5) lgkmcnt(5)\n"
  "v_mfma_f32_16x16x32_bf16 v[24:27], v[128:131], v[104:107], 0\n"
  "s_waitcnt lgkmcnt(4)\n"
  "v_mfma_f32_16x16x32_bf16 v[28:31], v[132:135], v[104:107], 0\n"
  "s_waitcnt lgkmcnt(3)\n"
  "v_mfma_f32_16x16x32_bf16 v[32:35], v[136:139], v[104:107], 0\n"
  "ds_read_b128 v[128:131], v5 offset:6144\n"
  "ds_read_b128 v[132:135], v5 offset:7168\n"
  "ds_read_b128 v[136:139], v5 offset:8192\n"
  "s_waitcnt vmcnt(4) lgkmcnt(5)\n"
  "v_mfma_f32_16x16x32_bf16 v[24:27], v[36:39], v[108:111], v[24:27]\n"
  "s_waitcnt lgkmcnt(4)\n"
  "v_mfma_f32_16x16x32_bf16 v[28:31], v[40:43], v[108:111], v[28:31]\n"
  "s_waitcnt lgkmcnt(3)\n"
  "v_mfma_f32_16x16x32_bf16 v[32:35], v[44:47], v[108:111], v[32:35]\n"
  "ds_read_b128 v[36:39], v5 offset:9216\n"
  "ds_read_b128 v[40:43], v5 offset:10240\n"
  "ds_read_b128 v[44:47], v5 offset:11264\n"
  "s_waitcnt vmcnt(3) lgkmcnt(5)\n"
  "v_mfma_f32_16x16x32_bf16 v[24:27], v[128:131], v[112:115], v[24:27]\n"
  "s_waitcnt lgkmcnt(4)\n"
  "v_mfma_f32_16x16x32_bf16 v[28:31], v[132:135], v[112:115], v[28:31]\n"
  "s_waitcnt lgkmcnt(3)\n"
  "v_mfma_f32_16x16x32_bf16 v[32:35], v[136:139], v[112:115], v[32:35]\n"
  "ds_read_b128 v[128:131], v5 offset:12288\n"
  "ds_read_b128 v[132:135], v5 offset:13312\n"
  "ds_read_b128 v[136:139], v5 offset:14336\n"
  "s_waitcnt vmcnt(2) lgkmcnt(5)\n"
  "v_mfma_f32_16x16x32_bf16 v[24:27], v[36:39], v[116:119], v[24:27]\n"
  "s_waitcnt lgkmcnt(4)\n"
  "v_mfma_f32_16x16x32_bf16 v[28:31], v[40:43], v[116:119], v[28:31]\n"
  "s_waitcnt lgkmcnt(3)\n"
  "v_mfma_f32_16x16x32_bf16 v[32:35], v[44:47], v[116:119], v[32:35]\n"
  "s_waitcnt vmcnt(1) lgkmcnt(2)\n"
  "v_mfma_f32_16x16x32_bf16 v[24:27], v[128:131], v[120:123], v[24:27]\n"
  "s_waitcnt lgkmcnt(1)\n"
  "v_mfma_f32_16x16x32_bf16 v[28:31], v[132:135], v[120:123], v[28:31]\n"
  "s_waitcnt lgkmcnt(0)\n"
  "v_mfma_f32_16x16x32_bf16 v[32:35], v[136:139], v[120:123], v[32:35]\n"
  "s_waitcnt vmcnt(0)\n"
  "v_mov_b32 v10, v9\n"
  "ds_write_b128 v11, v[12:15] offset:0\n"
  "ds_write_b128 v11, v[16:19] offset:1024\n"
  "ds_write_b128 v11, v[20:23] offset:2048\n"
  "ds_write_b128 v11, v[24:27] offset:3072\n"
  "ds_write_b128 v11, v[28:31] offset:4096\n"
  "s_nop 0\n"
  "ds_write_b128 v11, v[32:35] offset:5120\n"
  "v_sub_f32 v6, 1.0, v10\n"
  "v_cvt_pk_bf16_f32 v6, v6, v6\n"
  "v_lshlrev_b32 v6, 16, v6\n"
  "s_waitcnt lgkmcnt(0)\n"
  "s_barrier\n"
  "ds_read_b128 v[28:31], v3 offset:0\n"
  "ds_read_b128 v[32:35], v3 offset:6144\n"
  "ds_read_b128 v[36:39], v3 offset:12288\n"
  "ds_read_b128 v[40:43], v3 offset:18432\n"
  "ds_read_b128 v[12:15], v3 offset:3072\n"
  "ds_read_b128 v[16:19], v3 offset:9216\n"
  "ds_read_b128 v[20:23], v3 offset:15360\n"
  "ds_read_b128 v[24:27], v3 offset:21504\n"
  "v_add_u32 v11, s56, v11\n"
  "v_add_u32 v3, s56, v3\n"
  "s_sub_u32 s56, 0, s56\n"
  "s_mul_i32 s74, s54, 18432\n"
  "s_add_u32 s70, s40, s74\n"
  "s_addc_u32 s71, s41, 0\n"
  "s_waitcnt lgkmcnt(6)\n"
  "v_add_f32 v28, v28, v32\n"
  "s_waitcnt lgkmcnt(4)\n"
  "v_add_f32 v36, v36, v40\n"
  "v_add_f32 v28, v28, v36\n"
  "v_add_f32 v29, v29, v33\n"
  "v_add_f32 v37, v37, v41\n"
  "v_add_f32 v29, v29, v37\n"
  "v_add_f32 v30, v30, v34\n"
  "v_add_f32 v38, v38, v42\n"
  "v_add_f32 v30, v30, v38\n"
  "v_add_f32 v31, v31, v35\n"
  "v_add_f32 v39, v39, v43\n"
  "v_add_f32 v31, v31, v39\n"
  "v_cvt_pk_bf16_f32 v28, v28, v29\n"
  "v_and_b32 v29, 0xffff0000, v28\n"
  "v_lshlrev_b32 v28, 16, v28\n"
  "v_cvt_pk_bf16_f32 v30, v30, v31\n"
  "v_and_b32 v31, 0xffff0000, v30\n"
  "v_lshlrev_b32 v30, 16, v30\n"
  "v_mul_f32 v28, v6, v28\n"
  "v_mul_f32 v29, v6, v29\n"
  "v_mul_f32 v30, v6, v30\n"
  "v_mul_f32 v31, v6, v31\n"
  "v_cvt_pk_bf16_f32 v28, v28, v29\n"
  "v_and_b32 v29, 0xffff0000, v28\n"
  "v_lshlrev_b32 v28, 16, v28\n"
  "v_cvt_pk_bf16_f32 v30, v30, v31\n"
  "v_and_b32 v31, 0xffff0000, v30\n"
  "v_lshlrev_b32 v30, 16, v30\n"
  "s_waitcnt lgkmcnt(2)\n"
  "v_add_f32 v12, v12, v16\n"
  "s_waitcnt lgkmcnt(0)\n"
  "v_add_f32 v20, v20, v24\n"
  "v_add_f32 v12, v12, v20\n"
  "v_add_f32 v13, v13, v17\n"
  "v_add_f32 v21, v21, v25\n"
  "v_add_f32 v13, v13, v21\n"
  "v_add_f32 v14, v14, v18\n"
  "v_add_f32 v22, v22, v26\n"
  "v_add_f32 v14, v14, v22\n"
  "v_add_f32 v15, v15, v19\n"
  "v_add_f32 v23, v23, v27\n"
  "v_add_f32 v15, v15, v23\n"
  "v_cvt_pk_bf16_f32 v12, v12, v13\n"
  "v_and_b32 v13, 0xffff0000, v12\n"
  "v_lshlrev_b32 v12, 16, v12\n"
  "v_cvt_pk_bf16_f32 v14, v14, v15\n"
  "v_and_b32 v15, 0xffff0000, v14\n"
  "v_lshlrev_b32 v14, 16, v14\n"
  "v_mul_f32 v12, v10, v12\n"
  "v_mul_f32 v13, v10, v13\n"
  "v_mul_f32 v14, v10, v14\n"
  "v_mul_f32 v15, v10, v15\n"
  "v_cvt_pk_bf16_f32 v12, v12, v13\n"
  "v_and_b32 v13, 0xffff0000, v12\n"
  "v_lshlrev_b32 v12, 16, v12\n"
  "v_cvt_pk_bf16_f32 v14, v14, v15\n"
  "v_and_b32 v15, 0xffff0000, v14\n"
  "v_lshlrev_b32 v14, 16, v14\n"
  "v_add_f32 v28, v28, v12\n"
  "v_add_f32 v29, v29, v13\n"
  "v_add_f32 v30, v30, v14\n"
  "v_add_f32 v31, v31, v15\n"
  "v_cvt_pk_bf16_f32 v28, v28, v29\n"
  "v_cvt_pk_bf16_f32 v29, v30, v31\n"
  "s_bfm_b64 exec, 48, 0\n"
  "global_store_dwordx2 v4, v[28:29], s[70:71]\n"
  "s_mov_b64 exec, -1\n"
  "s_mov_b32 s54, s55\n"
  "s_branch ONEXT\n"
  "OQUIT:\n"
  "s_endpgm\n"
      : "+{v[0:15]}"(vin), "+{v[16:31]}"(wa), "+{s[36:43]}"(sa), "+{s[44:59]}"(sb) :: CLOB);
  }
}

extern "C" void kernel_launch(void* const* d_in, const int* in_sizes, int n_in, void* d_out, int out_size, void* d_ws, size_t ws_size,
                              hipStream_t stream) {
  Params p;
  p.x = (const u16*)d_in[0]; p.wqkv = (const u16*)d_in[1]; p.wo_a = (const u16*)d_in[2];
  p.mem0 = (const u16*)d_in[4]; p.Wq = (const u16*)d_in[5]; p.Wk = (const u16*)d_in[6]; p.Wv = (const u16*)d_in[7];
  p.Wo = (const u16*)d_in[8]; p.gate = (const u16*)d_in[10];
  p.out = (u16*)d_out; p.ws = (unsigned char*)d_ws + ((0x200000ull - ((unsigned long long)d_ws & 0x1FFFFFull)) & 0x1FFFFFull);
  (void)hipFuncSetAttribute((const void*)gm_kernel, hipFuncAttributeMaxDynamicSharedMemorySize, LDS_BYTES);
  (void)hipMemsetAsync(p.ws + CTL, 0, CTL_BYTES, stream);
  hipLaunchKernelGGL(gm_kernel, dim3(256), dim3(256), LDS_BYTES, stream, p);
}
```

```cpp
#include <hip/hip_runtime.h>
#include <stdint.h>
#define LAS __attribute__((address_space(3)))
#define GAS __attribute__((address_space(1)))
typedef unsigned v2u __attribute__((ext_vector_type(2)));
typedef unsigned v4u __attribute__((ext_vector_type(4)));
typedef unsigned v8u __attribute__((ext_vector_type(8)));
typedef unsigned v16u __attribute__((ext_vector_type(16)));
typedef unsigned v32u __attribute__((ext_vector_type(32)));
typedef float f4v __attribute__((ext_vector_type(4)));
typedef short s4v __attribute__((ext_vector_type(4)));
typedef __bf16 b8v __attribute__((ext_vector_type(8)));
typedef __bf16 b2v __attribute__((ext_vector_type(2)));
typedef unsigned short u16;
typedef GAS unsigned gu32;
typedef LAS unsigned char* lptr;
#define RLX_AGENT __ATOMIC_RELAXED, __HIP_MEMORY_SCOPE_AGENT

constexpr int DM = 576, NSEG = 64, LDS_BYTES = 160 * 1024, SPIN = 400000;
constexpr unsigned CH = 32768;
constexpr unsigned CTL = 16u << 20, CTL_BYTES = 11 * 16384;
constexpr unsigned C_ACK = CTL, C_TAB = CTL + CTL_BYTES;
constexpr unsigned B_Q = C_TAB + 8192, B_R = B_Q + 1024, B_S = B_R + 2048, B_N = B_S + 512;
constexpr unsigned C_REP = CTL + 16384, C_KVP = CTL + 163840, C_HF = CTL + 24576, C_VF = CTL + 40960, C_QAP = CTL + 57344, C_AP = C_QAP + 4096, C_QAL = CTL + 73728, C_OP = CTL + 90112;
constexpr unsigned WO_B = 20u << 20;
constexpr unsigned KVH = 0;
constexpr unsigned WO_KM = 150u << 20, WO_VP = 154u << 20;
constexpr unsigned WO_OA = 158u << 20;
constexpr unsigned WO_G = 161u << 20;
constexpr unsigned WO_ST = 162u << 20, WO_DB = 172u << 20;
constexpr float QS = 0.08838834764831845f * 1.4426950408889634f;

struct Params { const u16* x; const u16* wqkv; const u16* wo_a; const u16* mem0; const u16* Wq; const u16* Wk; const u16* Wv; const u16* Wo; const u16* gate;
                u16* out; unsigned char* ws; };

__device__ __forceinline__ unsigned xcc_id() { return (unsigned)__builtin_amdgcn_s_getreg(20 | (3 << 11)) & 0xfu; }
__device__ __forceinline__ unsigned rfl(unsigned v) { return __builtin_amdgcn_readfirstlane(v); }
__device__ __forceinline__ f4v mfma32(v4u a, v4u b, f4v c) { return __builtin_amdgcn_mfma_f32_16x16x32_bf16(__builtin_bit_cast(b8v, a), __builtin_bit_cast(b8v, b), c, 0, 0, 0); }
__device__ __forceinline__ unsigned pack2(float lo, float hi) { b2v p; p.x = (__bf16)lo; p.y = (__bf16)hi; return __builtin_bit_cast(unsigned, p); }
__device__ __forceinline__ v4u pack8(f4v a, f4v b) { return (v4u){pack2(a.x, a.y), pack2(a.z, a.w), pack2(b.x, b.y), pack2(b.z, b.w)}; }
__device__ __forceinline__ float rbf(float x) { return (float)(__bf16)x; }
__device__ __forceinline__ float bflo(unsigned u) { return __uint_as_float(u << 16); }
__device__ __forceinline__ float bfhi(unsigned u) { return __uint_as_float(u & 0xffff0000u); }
__device__ __forceinline__ v2u lds_tr(lptr p) { return __builtin_bit_cast(v2u, __builtin_amdgcn_ds_read_tr16_b64_v4i16((LAS s4v*)p)); }
__device__ __forceinline__ v4u lds16(lptr p) { return *(LAS v4u*)p; }
#define WAIT_VM(n) asm volatile("s_waitcnt vmcnt(" #n ")" ::: "memory")
__device__ __forceinline__ void wg_barrier() { asm volatile("s_waitcnt lgkmcnt(0)\n\ts_barrier" ::: "memory"); }
#define DMA_BODY(FL) unsigned keep; asm volatile("s_mov_b32 %0, m0\n\ts_mov_b32 m0, %3\n\ts_nop 0\n\tglobal_load_lds_dwordx4 %1, %2" FL "\n\ts_mov_b32 m0, %0" \
                                              : "=&s"(keep) : "v"(off), "s"(base), "s"(dst) : "memory")
__device__ __forceinline__ void dma(const void* base, unsigned off, unsigned dst) { DMA_BODY(""); }
__device__ __forceinline__ void dma_dev(const void* base, unsigned off, unsigned dst) { DMA_BODY(" sc1"); }
__device__ __forceinline__ void st16(unsigned char* ws, unsigned off, v4u v) { asm volatile("global_store_dwordx4 %0, %1, %2 sc1\n\ts_nop 1" :: "v"(off), "v"(v), "s"(ws) : "memory"); }
__device__ __forceinline__ unsigned ctl_ld(unsigned char* ws, unsigned off) { return __hip_atomic_load((gu32*)(ws + off), RLX_AGENT); }
__device__ __forceinline__ void ctl_st(unsigned char* ws, unsigned off, unsigned v) { __hip_atomic_store((gu32*)(ws + off), v, RLX_AGENT); }
__device__ __forceinline__ unsigned ctl_add(unsigned char* ws, unsigned off, unsigned v) { return __hip_atomic_fetch_add((gu32*)(ws + off), v, RLX_AGENT); }
__device__ __forceinline__ unsigned long long ld8(unsigned char* ws, unsigned off) { return __hip_atomic_load((GAS unsigned long long*)(ws + off), RLX_AGENT); }
__device__ __forceinline__ void st8(unsigned char* ws, unsigned off, unsigned a, unsigned b) { __hip_atomic_store((GAS unsigned long long*)(ws + off), a | ((unsigned long long)b << 32), RLX_AGENT); }
__device__ __forceinline__ bool wait_ge(unsigned char* ws, unsigned off, unsigned want) {
  for (int i = 0; i < SPIN; ++i) {
    if (__all(ctl_ld(ws, off) >= want)) return true;
    __builtin_amdgcn_s_sleep(4);
  }
  return false;
}
__device__ __forceinline__ unsigned chunk(unsigned cls, unsigned g, unsigned c, unsigned off) {
  const unsigned fix = (((c >> 4) & 1) << 2) | ((c >> 5) & 1);
  return g * 0x200000u + c * CH + ((((cls >> (4 * g)) & 7) ^ fix) << 12) + off;
}
__device__ __forceinline__ float xor16(float v) { auto s = __builtin_amdgcn_permlane16_swap(__float_as_uint(v), __float_as_uint(v), false, false); return __uint_as_float((threadIdx.x & 16) ? s[0] : s[1]); }
__device__ __forceinline__ float xor32(float v) { auto s = __builtin_amdgcn_permlane32_swap(__float_as_uint(v), __float_as_uint(v), false, false); return __uint_as_float((threadIdx.x & 32) ? s[0] : s[1]); }
template <int C> __device__ __forceinline__ float dpp(float v) { return __int_as_float(__builtin_amdgcn_update_dpp(0, __float_as_int(v), C, 0xf, 0xf, true)); }
#define STAMP(s, i) do { } while (0)
__device__ __forceinline__ unsigned x_src(int wave, int lane) { return (8 * wave + (lane >> 3)) * 1152 + ((lane & 7) ^ ((4 * wave + (lane >> 4)) & 7)) * 16; }
__device__ __forceinline__ unsigned x_frag(int lane) { return (lane & 15) * 128 + ((lane >> 4) ^ ((lane >> 1) & 7)) * 16; }

#define UNIFORM(v, n) _Pragma("unroll") for (int i_ = 0; i_ < n; ++i_) v[i_] = rfl(v[i_])
#define PTR(v, i, ptr) do { const unsigned long long a_ = (unsigned long long)(ptr); v[i] = (unsigned)a_; v[(i) + 1] = (unsigned)(a_ >> 32); } while (0)
#define CLOB "v255", "a255", "s101", "vcc", "memory"

#define ARGS const __attribute__((address_space(4))) Params* pp_ = (const __attribute__((address_space(4))) Params*)__builtin_amdgcn_kernarg_segment_ptr(); \
             asm volatile("" : "+s"(pp_)); const __attribute__((address_space(4))) Params& p = *pp_

__global__ void __launch_bounds__(256, 1) gm_kernel(Params) {
  ARGS;
  extern __shared__ __attribute__((aligned(16))) unsigned char smem[];
  const lptr lds = (lptr)smem;
  const int tid = threadIdx.x, lane = tid & 63, wave = rfl(tid >> 6);
  int role = (blockIdx.x & 7) * 32 + (blockIdx.x >> 3);
  unsigned ro = B_R + role * 8;
  asm volatile("" : "+s"(role), "+s"(ro));
  const unsigned probe = chunk(0, (role >> 5) & 3, 15, (role & 31) * 128);
  if (role < 128 && (role & 16)) {
    if (wave == 1 && lane < 8) (void)ctl_add(p.ws, probe + (lane << 12), 1u);
    if (wave > 0 && tid - 64 < CTL_BYTES / 1024) st16(p.ws, CTL + (((role >> 5) * 16 + (role & 15)) * (CTL_BYTES / 1024) + tid - 64) * 16, (v4u){0, 0, 0, 0});
    WAIT_VM(0);
  }
  if (tid == 0) { const unsigned t = ctl_ld(p.ws, ro) + 1; ctl_st(p.ws, B_Q + role * 4, t); *(LAS unsigned*)lds = t; }
  wg_barrier();
  unsigned token;
  asm volatile("s_nop 0\n\tv_readfirstlane_b32 %0, %1\n\ts_nop 4" : "=s"(token) : "v"(*(LAS unsigned*)lds));
  const v2u ask = {token, ro};
  wg_barrier();
  STAMP(63, 15);
  const int n = lane & 15, g = lane >> 4, qq = n >> 2, c4 = n & 3, gam = role >> 5, tk = role & 31;
  v16u vin; v8u sa; v16u sb;
#pragma unroll
  for (int i = 0; i < 16; ++i) { vin[i] = 0; sb[i] = 0; }
#pragma unroll
  for (int i = 0; i < 8; ++i) sa[i] = 0;
  vin[0] = lane * 16;
  if (gam < 4) {
    const unsigned m1pat = ((lane >> 5) * 4 + qq) * 128 + (g & 1) * 64 + c4 * 16, idx = (lane & 2) ? 0xEEEEu : 0x4444u;
    const int trrow = 16 * (g & 1) + 2 * (g >> 1) + 4 * (qq & 1) + (qq >> 1);
    sb[4] = wave; sb[10] = WO_ST + (role * 4 + wave) * 4096;
    if (tk < 16) { ARGS; PTR(sa, 0, p.ws);
      const int h = gam * 8 + (tk >> 1), half = tk & 1, flip = gam & 1;
      vin[1] = m1pat + wave * 1024; vin[2] = qq * 256 + g * 64 + c4 * 16;
      vin[3] = 9216 + (32 * wave + n) * 32 + 8 * (g ^ qq);
      vin[4] = 9216 + 4096 + (16 * wave + n) * 32 + 8 * (g ^ qq);
      vin[5] = 9216 + (4 * g + qq) * 32 + 8 * (c4 ^ g);
      vin[6] = idx; vin[7] = (lane & 2) ? 0x0c0c0c0cu : 0x07060302u; vin[8] = (lane & 2) ? 0x07060302u : 0x0c0c0c0cu;
#pragma unroll
      for (int i = 0; i < 3; ++i) {
        const int cg = i < 2 ? 8 * half + 2 * wave + (i ^ flip) : 16 + half, gr = (cg & 1) + 2 * (g >> 1), hs = ((0x3120 >> (4 * (gr ^ gam))) & 3) * 8 + (h & 7);
        vin[9 + i] = chunk(0, gr, 28 + 4 * (cg >> 1) + (hs >> 4), (hs & 15) * 256 + (i == 2 ? (wave & 1) * 128 : 0) + (g & 1) * 64 + n * 4);
      }
      { const int row = 4 * (wave & 1) + 16 * (wave >> 1) + g, sw = 2 * ((row & 1) | ((row >> 1) & 2) | ((row >> 2) & 4)); vin[12] = row * 8192 + (n ^ sw) * 16; }
      { const int sw = 2 * ((trrow & 1) | ((trrow >> 1) & 2) | ((trrow >> 2) & 4)); vin[13] = trrow * 256 + ((4 * wave + (c4 >> 1)) ^ sw) * 16 + (c4 & 1) * 8; }
      vin[14] = (16 * wave + (lane >> 3)) * 1152 + ((lane & 7) ^ (2 * wave)) * 16;
      { const int row = 32 * (g & 1) + 4 * (g >> 1) + (qq & 1) + 16 * (qq >> 1); vin[15] = row * 128 + ((c4 >> 1) ^ (2 * ((row >> 4) & 3))) * 16 + (c4 & 1) * 8; }
      PTR(sa, 2, p.Wq + h * 128); PTR(sa, 4, p.Wk + h * 128); PTR(sa, 6, p.Wo + (size_t)h * 128 * DM + half * 256);
      sb[0] = chunk(0, gam, 0, 0); sb[1] = (C_KVP) + h * 256; sb[2] = chunk(0, gam, 16 + ((h & 7) >> 1), (h & 1) * 2048);
      sb[3] = KVH + h * 65536 + (wave >> 1) * 16384 + (wave & 1) * 4096; sb[6] = (C_HF) + h * 256; sb[7] = C_REP; sb[8] = 1024 - 512 * half; sb[9] = WO_KM + h * 2048 + wave * 512;
      sb[11] = __float_as_uint(QS); sb[12] = flip * 64; sb[13] = gam * 128; sb[14] = 2500; sb[15] = 720;
      UNIFORM(sa, 8); UNIFORM(sb, 16);
      asm volatile(
  "s_nop 4\n"
  "s_lshl_b32 s80, s48, 10\n"
  "s_lshl_b32 s78, s48, 11\n"
  "s_and_b32 s79, s80, 1024\n"
  "s_and_b32 s74, s78, 4096\n"
  "s_add_u32 s79, s79, s74\n"
  "s_add_u32 m0, s79, 0\n"
  "s_add_u32 s74, s40, 0\n"
  "s_addc_u32 s75, s41, 0\n"
  "global_load_lds_dwordx4 v12, s[74:75]\n"
  "s_add_u32 m0, s79, 2048\n"
  "s_add_u32 s74, s40, 65536\n"
  "s_addc_u32 s75, s41, 0\n"
  "global_load_lds_dwordx4 v12, s[74:75]\n"
  "s_add_u32 m0, s79, 8192\n"
  "s_add_u32 s74, s40, 262144\n"
  "s_addc_u32 s75, s41, 0\n"
  "global_load_lds_dwordx4 v12, s[74:75]\n"
  "s_add_u32 m0, s79, 10240\n"
  "s_add_u32 s74, s40, 327680\n"
  "s_addc_u32 s75, s41, 0\n"
  "global_load_lds_dwordx4 v12, s[74:75]\n"
  "s_add_u32 m0, s79, 16384\n"
  "s_add_u32 s74, s40, 524288\n"
  "s_addc_u32 s75, s41, 0\n"
  "global_load_lds_dwordx4 v12, s[74:75]\n"
  "s_add_u32 m0, s79, 18432\n"
  "s_add_u32 s74, s40, 589824\n"
  "s_addc_u32 s75, s41, 0\n"
  "global_load_lds_dwordx4 v12, s[74:75]\n"
  "s_add_u32 m0, s79, 24576\n"
  "s_add_u32 s74, s40, 786432\n"
  "s_addc_u32 s75, s41, 0\n"
  "global_load_lds_dwordx4 v12, s[74:75]\n"
  "s_add_u32 m0, s79, 26624\n"
  "s_add_u32 s74, s40, 851968\n"
  "s_addc_u32 s75, s41, 0\n"
  "global_load_lds_dwordx4 v12, s[74:75]\n"
  "s_add_u32 m0, s79, 32768\n"
  "s_add_u32 s74, s40, 1048576\n"
  "s_addc_u32 s75, s41, 0\n"
  "global_load_lds_dwordx4 v12, s[74:75]\n"
  "s_add_u32 m0, s79, 34816\n"
  "s_add_u32 s74, s40, 1114112\n"
  "s_addc_u32 s75, s41, 0\n"
  "global_load_lds_dwordx4 v12, s[74:75]\n"
  "s_add_u32 m0, s79, 40960\n"
  "s_add_u32 s74, s40, 1310720\n"
  "s_addc_u32 s75, s41, 0\n"
  "global_load_lds_dwordx4 v12, s[74:75]\n"
  "s_add_u32 m0, s79, 43008\n"
  "s_add_u32 s74, s40, 1376256\n"
  "s_addc_u32 s75, s41, 0\n"
  "global_load_lds_dwordx4 v12, s[74:75]\n"
  "s_add_u32 m0, s79, 49152\n"
  "s_add_u32 s74, s40, 1572864\n"
  "s_addc_u32 s75, s41, 0\n"
  "global_load_lds_dwordx4 v12, s[74:75]\n"
  "s_add_u32 m0, s79, 51200\n"
  "s_add_u32 s74, s40, 1638400\n"
  "s_addc_u32 s75, s41, 0\n"
  "global_load_lds_dwordx4 v12, s[74:75]\n"
  "s_add_u32 m0, s79, 57344\n"
  "s_add_u32 s74, s40, 1835008\n"
  "s_addc_u32 s75, s41, 0\n"
  "global_load_lds_dwordx4 v12, s[74:75]\n"
  "s_add_u32 m0, s79, 59392\n"
  "s_add_u32 s74, s40, 1900544\n"
  "s_addc_u32 s75, s41, 0\n"
  "global_load_lds_dwordx4 v12, s[74:75]\n"
  "s_add_u32 m0, s79, 65536\n"
  "s_add_u32 s74, s40, 2097152\n"
  "s_addc_u32 s75, s41, 0\n"
  "global_load_lds_dwordx4 v12, s[74:75]\n"
  "s_add_u32 m0, s79, 67584\n"
  "s_add_u32 s74, s40, 2162688\n"
  "s_addc_u32 s75, s41, 0\n"
  "global_load_lds_dwordx4 v12, s[74:75]\n"
  "s_add_u32 m0, s79, 73728\n"
  "s_add_u32 s74, s40, 2359296\n"
  "s_addc_u32 s75, s41, 0\n"
  "global_load_lds_dwordx4 v12, s[74:75]\n"
  "s_add_u32 m0, s79, 75776\n"
  "s_add_u32 s74, s40, 2424832\n"
  "s_addc_u32 s75, s41, 0\n"
  "global_load_lds_dwordx4 v12, s[74:75]\n"
  "s_add_u32 m0, s79, 81920\n"
  "s_add_u32 s74, s40, 2621440\n"
  "s_addc_u32 s75, s41, 0\n"
  "global_load_lds_dwordx4 v12, s[74:75]\n"
  "s_add_u32 m0, s79, 83968\n"
  "s_add_u32 s74, s40, 2686976\n"
  "s_addc_u32 s75, s41, 0\n"
  "global_load_lds_dwordx4 v12, s[74:75]\n"
  "s_add_u32 m0, s79, 90112\n"
  "s_add_u32 s74, s40, 2883584\n"
  "s_addc_u32 s75, s41, 0\n"
  "global_load_lds_dwordx4 v12, s[74:75]\n"
  "s_add_u32 m0, s79, 92160\n"
  "s_add_u32 s74, s40, 2949120\n"
  "s_addc_u32 s75, s41, 0\n"
  "global_load_lds_dwordx4 v12, s[74:75]\n"
  "s_add_u32 m0, s79, 98304\n"
  "s_add_u32 s74, s40, 3145728\n"
  "s_addc_u32 s75, s41, 0\n"
  "global_load_lds_dwordx4 v12, s[74:75]\n"
  "s_add_u32 m0, s79, 100352\n"
  "s_add_u32 s74, s40, 3211264\n"
  "s_addc_u32 s75, s41, 0\n"
  "global_load_lds_dwordx4 v12, s[74:75]\n"
  "s_add_u32 m0, s79, 106496\n"
  "s_add_u32 s74, s40, 3407872\n"
  "s_addc_u32 s75, s41, 0\n"
  "global_load_lds_dwordx4 v12, s[74:75]\n"
  "s_add_u32 m0, s79, 108544\n"
  "s_add_u32 s74, s40, 3473408\n"
  "s_addc_u32 s75, s41, 0\n"
  "global_load_lds_dwordx4 v12, s[74:75]\n"
  "s_add_u32 m0, s79, 114688\n"
  "s_add_u32 s74, s40, 3670016\n"
  "s_addc_u32 s75, s41, 0\n"
  "global_load_lds_dwordx4 v12, s[74:75]\n"
  "s_add_u32 m0, s79, 116736\n"
  "s_add_u32 s74, s40, 3735552\n"
  "s_addc_u32 s75, s41, 0\n"
  "global_load_lds_dwordx4 v12, s[74:75]\n"
  "s_add_u32 m0, s79, 122880\n"
  "s_add_u32 s74, s40, 3932160\n"
  "s_addc_u32 s75, s41, 0\n"
  "global_load_lds_dwordx4 v12, s[74:75]\n"
  "s_add_u32 m0, s79, 124928\n"
  "s_add_u32 s74, s40, 3997696\n"
  "s_addc_u32 s75, s41, 0\n"
  "global_load_lds_dwordx4 v12, s[74:75]\n"
  "s_waitcnt vmcnt(28) lgkmcnt(0)\n"
  "s_barrier\n"
  "s_add_u32 m0, s79, 131072\n"
  "s_add_u32 s74, s40, 4194304\n"
  "s_addc_u32 s75, s41, 0\n"
  "global_load_lds_dwordx4 v12, s[74:75]\n"
  "s_add_u32 m0, s79, 133120\n"
  "s_add_u32 s74, s40, 4259840\n"
  "s_addc_u32 s75, s41, 0\n"
  "global_load_lds_dwordx4 v12, s[74:75]\n"
  "s_add_u32 m0, s79, 139264\n"
  "s_add_u32 s74, s40, 4456448\n"
  "s_addc_u32 s75, s41, 0\n"
  "global_load_lds_dwordx4 v12, s[74:75]\n"
  "s_add_u32 m0, s79, 141312\n"
  "s_add_u32 s74, s40, 4521984\n"
  "s_addc_u32 s75, s41, 0\n"
  "global_load_lds_dwordx4 v12, s[74:75]\n"
  "v_add_u32 v20, 0, v13\n"
  "v_xor_b32 v21, 32, v20\n"
  "ds_read_b64_tr_b16 a[0:1], v20 offset:0\n"
  "ds_read_b64_tr_b16 a[2:3], v20 offset:2048\n"
  "ds_read_b64_tr_b16 a[4:5], v20 offset:8192\n"
  "ds_read_b64_tr_b16 a[6:7], v20 offset:10240\n"
  "ds_read_b64_tr_b16 a[8:9], v21 offset:0\n"
  "ds_read_b64_tr_b16 a[10:11], v21 offset:2048\n"
  "ds_read_b64_tr_b16 a[12:13], v21 offset:8192\n"
  "ds_read_b64_tr_b16 a[14:15], v21 offset:10240\n"
  "s_waitcnt vmcnt(28) lgkmcnt(0)\n"
  "s_barrier\n"
  "s_add_u32 m0, s78, 0\n"
  "s_add_u32 s74, s42, 0\n"
  "s_addc_u32 s75, s43, 0\n"
  "global_load_lds_dwordx4 v14, s[74:75]\n"
  "s_add_u32 m0, s78, 1024\n"
  "s_add_u32 s74, s42, 9216\n"
  "s_addc_u32 s75, s43, 0\n"
  "global_load_lds_dwordx4 v14, s[74:75]\n"
  "s_add_u32 m0, s78, 8192\n"
  "s_add_u32 s74, s42, 73728\n"
  "s_addc_u32 s75, s43, 0\n"
  "global_load_lds_dwordx4 v14, s[74:75]\n"
  "s_add_u32 m0, s78, 9216\n"
  "s_add_u32 s74, s42, 82944\n"
  "s_addc_u32 s75, s43, 0\n"
  "global_load_lds_dwordx4 v14, s[74:75]\n"
  "v_add_u32 v20, 16384, v13\n"
  "v_xor_b32 v21, 32, v20\n"
  "ds_read_b64_tr_b16 a[16:17], v20 offset:0\n"
  "ds_read_b64_tr_b16 a[18:19], v20 offset:2048\n"
  "ds_read_b64_tr_b16 a[20:21], v20 offset:8192\n"
  "ds_read_b64_tr_b16 a[22:23], v20 offset:10240\n"
  "ds_read_b64_tr_b16 a[24:25], v21 offset:0\n"
  "ds_read_b64_tr_b16 a[26:27], v21 offset:2048\n"
  "ds_read_b64_tr_b16 a[28:29], v21 offset:8192\n"
  "ds_read_b64_tr_b16 a[30:31], v21 offset:10240\n"
  "s_waitcnt vmcnt(28) lgkmcnt(0)\n"
  "s_barrier\n"
  "s_add_u32 m0, s78, 16384\n"
  "s_add_u32 s74, s42, 128\n"
  "s_addc_u32 s75, s43, 0\n"
  "global_load_lds_dwordx4 v14, s[74:75]\n"
  "s_add_u32 m0, s78, 17408\n"
  "s_add_u32 s74, s42, 9344\n"
  "s_addc_u32 s75, s43, 0\n"
  "global_load_lds_dwordx4 v14, s[74:75]\n"
  "s_add_u32 m0, s78, 24576\n"
  "s_add_u32 s74, s42, 73856\n"
  "s_addc_u32 s75, s43, 0\n"
  "global_load_lds_dwordx4 v14, s[74:75]\n"
  "s_add_u32 m0, s78, 25600\n"
  "s_add_u32 s74, s42, 83072\n"
  "s_addc_u32 s75, s43, 0\n"
  "global_load_lds_dwordx4 v14, s[74:75]\n"
  "v_add_u32 v20, 32768, v13\n"
  "v_xor_b32 v21, 32, v20\n"
  "ds_read_b64_tr_b16 a[32:33], v20 offset:0\n"
  "ds_read_b64_tr_b16 a[34:35], v20 offset:2048\n"
  "ds_read_b64_tr_b16 a[36:37], v20 offset:8192\n"
  "ds_read_b64_tr_b16 a[38:39], v20 offset:10240\n"
  "ds_read_b64_tr_b16 a[40:41], v21 offset:0\n"
  "ds_read_b64_tr_b16 a[42:43], v21 offset:2048\n"
  "ds_read_b64_tr_b16 a[44:45], v21 offset:8192\n"
  "ds_read_b64_tr_b16 a[46:47], v21 offset:10240\n"
  "s_waitcnt vmcnt(28) lgkmcnt(0)\n"
  "s_barrier\n"
  "s_add_u32 m0, s78, 32768\n"
  "s_add_u32 s74, s42, 256\n"
  "s_addc_u32 s75, s43, 0\n"
  "global_load_lds_dwordx4 v14, s[74:75]\n"
  "s_add_u32 m0, s78, 33792\n"
  "s_add_u32 s74, s42, 9472\n"
  "s_addc_u32 s75, s43, 0\n"
  "global_load_lds_dwordx4 v14, s[74:75]\n"
  "s_add_u32 m0, s78, 40960\n"
  "s_add_u32 s74, s42, 73984\n"
  "s_addc_u32 s75, s43, 0\n"
  "global_load_lds_dwordx4 v14, s[74:75]\n"
  "s_add_u32 m0, s78, 41984\n"
  "s_add_u32 s74, s42, 83200\n"
  "s_addc_u32 s75, s43, 0\n"
  "global_load_lds_dwordx4 v14, s[74:75]\n"
  "v_add_u32 v20, 49152, v13\n"
  "v_xor_b32 v21, 32, v20\n"
  "ds_read_b64_tr_b16 a[48:49], v20 offset:0\n"
  "ds_read_b64_tr_b16 a[50:51], v20 offset:2048\n"
  "ds_read_b64_tr_b16 a[52:53], v20 offset:8192\n"
  "ds_read_b64_tr_b16 a[54:55], v20 offset:10240\n"
  "ds_read_b64_tr_b16 a[56:57], v21 offset:0\n"
  "ds_read_b64_tr_b16 a[58:59], v21 offset:2048\n"
  "ds_read_b64_tr_b16 a[60:61], v21 offset:8192\n"
  "ds_read_b64_tr_b16 a[62:63], v21 offset:10240\n"
  "s_waitcnt vmcnt(28) lgkmcnt(0)\n"
  "s_barrier\n"
  "s_add_u32 m0, s78, 49152\n"
  "s_add_u32 s74, s42, 384\n"
  "s_addc_u32 s75, s43, 0\n"
  "global_load_lds_dwordx4 v14, s[74:75]\n"
  "s_add_u32 m0, s78, 50176\n"
  "s_add_u32 s74, s42, 9600\n"
  "s_addc_u32 s75, s43, 0\n"
  "global_load_lds_dwordx4 v14, s[74:75]\n"
  "s_add_u32 m0, s78, 57344\n"
  "s_add_u32 s74, s42, 74112\n"
  "s_addc_u32 s75, s43, 0\n"
  "global_load_lds_dwordx4 v14, s[74:75]\n"
  "s_add_u32 m0, s78, 58368\n"
  "s_add_u32 s74, s42, 83328\n"
  "s_addc_u32 s75, s43, 0\n"
  "global_load_lds_dwordx4 v14, s[74:75]\n"
  "v_add_u32 v20, 65536, v13\n"
  "v_xor_b32 v21, 32, v20\n"
  "ds_read_b64_tr_b16 a[64:65], v20 offset:0\n"
  "ds_read_b64_tr_b16 a[66:67], v20 offset:2048\n"
  "ds_read_b64_tr_b16 a[68:69], v20 offset:8192\n"
  "ds_read_b64_tr_b16 a[70:71], v20 offset:10240\n"
  "ds_read_b64_tr_b16 a[72:73], v21 offset:0\n"
  "ds_read_b64_tr_b16 a[74:75], v21 offset:2048\n"
  "ds_read_b64_tr_b16 a[76:77], v21 offset:8192\n"
  "ds_read_b64_tr_b16 a[78:79], v21 offset:10240\n"
  "s_waitcnt vmcnt(28) lgkmcnt(0)\n"
  "s_barrier\n"
  "s_add_u32 m0, s78, 65536\n"
  "s_add_u32 s74, s42, 0\n"
  "s_addc_u32 s75, s43, 0\n"
  "s_add_u32 s74, s74, s52\n"
  "s_addc_u32 s75, s75, 0\n"
  "global_load_lds_dwordx4 v14, s[74:75]\n"
  "s_add_u32 m0, s78, 66560\n"
  "s_add_u32 s74, s42, 9216\n"
  "s_addc_u32 s75, s43, 0\n"
  "s_add_u32 s74, s74, s52\n"
  "s_addc_u32 s75, s75, 0\n"
  "global_load_lds_dwordx4 v14, s[74:75]\n"
  "s_add_u32 m0, s78, 73728\n"
  "s_add_u32 s74, s42, 73728\n"
  "s_addc_u32 s75, s43, 0\n"
  "s_add_u32 s74, s74, s52\n"
  "s_addc_u32 s75, s75, 0\n"
  "global_load_lds_dwordx4 v14, s[74:75]\n"
  "s_add_u32 m0, s78, 74752\n"
  "s_add_u32 s74, s42, 82944\n"
  "s_addc_u32 s75, s43, 0\n"
  "s_add_u32 s74, s74, s52\n"
  "s_addc_u32 s75, s75, 0\n"
  "global_load_lds_dwordx4 v14, s[74:75]\n"
  "v_add_u32 v20, 81920, v13\n"
  "v_xor_b32 v21, 32, v20\n"
  "ds_read_b64_tr_b16 a[80:81], v20 offset:0\n"
  "ds_read_b64_tr_b16 a[82:83], v20 offset:2048\n"
  "ds_read_b64_tr_b16 a[84:85], v20 offset:8192\n"
  "ds_read_b64_tr_b16 a[86:87], v20 offset:10240\n"
  "ds_read_b64_tr_b16 a[88:89], v21 offset:0\n"
  "ds_read_b64_tr_b16 a[90:91], v21 offset:2048\n"
  "ds_read_b64_tr_b16 a[92:93], v21 offset:8192\n"
  "ds_read_b64_tr_b16 a[94:95], v21 offset:10240\n"
  "s_waitcnt vmcnt(28) lgkmcnt(0)\n"
  "s_barrier\n"
  "s_add_u32 m0, s79, 81920\n"
  "s_add_u32 s74, s38, 0\n"
  "s_addc_u32 s75, s39, 0\n"
  "global_load_lds_dwordx4 v12, s[74:75]\n"
  "s_add_u32 m0, s79, 83968\n"
  "s_add_u32 s74, s38, 65536\n"
  "s_addc_u32 s75, s39, 0\n"
  "global_load_lds_dwordx4 v12, s[74:75]\n"
  "s_add_u32 m0, s79, 90112\n"
  "s_add_u32 s74, s38, 262144\n"
  "s_addc_u32 s75, s39, 0\n"
  "global_load_lds_dwordx4 v12, s[74:75]\n"
  "s_add_u32 m0, s79, 92160\n"
  "s_add_u32 s74, s38, 327680\n"
  "s_addc_u32 s75, s39, 0\n"
  "global_load_lds_dwordx4 v12, s[74:75]\n"
  "v_add_u32 v20, 98304, v13\n"
  "v_xor_b32 v21, 32, v20\n"
  "ds_read_b64_tr_b16 a[96:97], v20 offset:0\n"
  "ds_read_b64_tr_b16 a[98:99], v20 offset:2048\n"
  "ds_read_b64_tr_b16 a[100:101], v20 offset:8192\n"
  "ds_read_b64_tr_b16 a[102:103], v20 offset:10240\n"
  "ds_read_b64_tr_b16 a[104:105], v21 offset:0\n"
  "ds_read_b64_tr_b16 a[106:107], v21 offset:2048\n"
  "ds_read_b64_tr_b16 a[108:109], v21 offset:8192\n"
  "ds_read_b64_tr_b16 a[110:111], v21 offset:10240\n"
  "s_waitcnt vmcnt(28) lgkmcnt(0)\n"
  "s_barrier\n"
  "s_add_u32 m0, s79, 98304\n"
  "s_add_u32 s74, s38, 524288\n"
  "s_addc_u32 s75, s39, 0\n"
  "global_load_lds_dwordx4 v12, s[74:75]\n"
  "s_add_u32 m0, s79, 100352\n"
  "s_add_u32 s74, s38, 589824\n"
  "s_addc_u32 s75, s39, 0\n"
  "global_load_lds_dwordx4 v12, s[74:75]\n"
  "s_add_u32 m0, s79, 106496\n"
  "s_add_u32 s74, s38, 786432\n"
  "s_addc_u32 s75, s39, 0\n"
  "global_load_lds_dwordx4 v12, s[74:75]\n"
  "s_add_u32 m0, s79, 108544\n"
  "s_add_u32 s74, s38, 851968\n"
  "s_addc_u32 s75, s39, 0\n"
  "global_load_lds_dwordx4 v12, s[74:75]\n"
  "v_add_u32 v20, 114688, v13\n"
  "v_xor_b32 v21, 32, v20\n"
  "ds_read_b64_tr_b16 a[112:113], v20 offset:0\n"
  "ds_read_b64_tr_b16 a[114:115], v20 offset:2048\n"
  "ds_read_b64_tr_b16 a[116:117], v20 offset:8192\n"
  "ds_read_b64_tr_b16 a[118:119], v20 offset:10240\n"
  "ds_read_b64_tr_b16 a[120:121], v21 offset:0\n"
  "ds_read_b64_tr_b16 a[122:123], v21 offset:2048\n"
  "ds_read_b64_tr_b16 a[124:125], v21 offset:8192\n"
  "ds_read_b64_tr_b16 a[126:127], v21 offset:10240\n"
  "s_waitcnt vmcnt(28) lgkmcnt(0)\n"
  "s_barrier\n"
  "s_add_u32 m0, s79, 114688\n"
  "s_add_u32 s74, s38, 1048576\n"
  "s_addc_u32 s75, s39, 0\n"
  "global_load_lds_dwordx4 v12, s[74:75]\n"
  "s_add_u32 m0, s79, 116736\n"
  "s_add_u32 s74, s38, 1114112\n"
  "s_addc_u32 s75, s39, 0\n"
  "global_load_lds_dwordx4 v12, s[74:75]\n"
  "s_add_u32 m0, s79, 122880\n"
  "s_add_u32 s74, s38, 1310720\n"
  "s_addc_u32 s75, s39, 0\n"
  "global_load_lds_dwordx4 v12, s[74:75]\n"
  "s_add_u32 m0, s79, 124928\n"
  "s_add_u32 s74, s38, 1376256\n"
  "s_addc_u32 s75, s39, 0\n"
  "global_load_lds_dwordx4 v12, s[74:75]\n"
  "v_add_u32 v20, 131072, v13\n"
  "v_xor_b32 v21, 32, v20\n"
  "ds_read_b64_tr_b16 a[128:129], v20 offset:0\n"
  "ds_read_b64_tr_b16 a[130:131], v20 offset:2048\n"
  "ds_read_b64_tr_b16 a[132:133], v20 offset:8192\n"
  "ds_read_b64_tr_b16 a[134:135], v20 offset:10240\n"
  "ds_read_b64_tr_b16 a[136:137], v21 offset:0\n"
  "ds_read_b64_tr_b16 a[138:139], v21 offset:2048\n"
  "ds_read_b64_tr_b16 a[140:141], v21 offset:8192\n"
  "ds_read_b64_tr_b16 a[142:143], v21 offset:10240\n"
  "s_waitcnt vmcnt(28) lgkmcnt(0)\n"
  "s_barrier\n"
  "s_add_u32 m0, s79, 131072\n"
  "s_add_u32 s74, s38, 1572864\n"
  "s_addc_u32 s75, s39, 0\n"
  "global_load_lds_dwordx4 v12, s[74:75]\n"
  "s_add_u32 m0, s79, 133120\n"
  "s_add_u32 s74, s38, 1638400\n"
  "s_addc_u32 s75, s39, 0\n"
  "global_load_lds_dwordx4 v12, s[74:75]\n"
  "s_add_u32 m0, s79, 139264\n"
  "s_add_u32 s74, s38, 1835008\n"
  "s_addc_u32 s75, s39, 0\n"
  "global_load_lds_dwordx4 v12, s[74:75]\n"
  "s_add_u32 m0, s79, 141312\n"
  "s_add_u32 s74, s38, 1900544\n"
  "s_addc_u32 s75, s39, 0\n"
  "global_load_lds_dwordx4 v12, s[74:75]\n"
  "s_cmp_lg_u32 s48, 0\n"
  "s_cbranch_scc1 HLS9\n"
  "s_xor_b32 s74, s56, 0\n"
  "v_xor_b32 v20, s74, v15\n"
  "v_add_u32 v20, 0, v20\n"
  "ds_read_b64_tr_b16 a[144:145], v20 offset:0\n"
  "ds_read_b64_tr_b16 a[146:147], v20 offset:256\n"
  "ds_read_b64_tr_b16 a[148:149], v20 offset:1024\n"
  "ds_read_b64_tr_b16 a[150:151], v20 offset:1280\n"
  "ds_read_b64_tr_b16 a[152:153], v20 offset:8192\n"
  "ds_read_b64_tr_b16 a[154:155], v20 offset:8448\n"
  "ds_read_b64_tr_b16 a[156:157], v20 offset:9216\n"
  "ds_read_b64_tr_b16 a[158:159], v20 offset:9472\n"
  "s_xor_b32 s74, s56, 32\n"
  "v_xor_b32 v20, s74, v15\n"
  "v_add_u32 v20, 0, v20\n"
  "ds_read_b64_tr_b16 a[160:161], v20 offset:0\n"
  "ds_read_b64_tr_b16 a[162:163], v20 offset:256\n"
  "ds_read_b64_tr_b16 a[164:165], v20 offset:1024\n"
  "ds_read_b64_tr_b16 a[166:167], v20 offset:1280\n"
  "ds_read_b64_tr_b16 a[168:169], v20 offset:8192\n"
  "ds_read_b64_tr_b16 a[170:171], v20 offset:8448\n"
  "ds_read_b64_tr_b16 a[172:173], v20 offset:9216\n"
  "ds_read_b64_tr_b16 a[174:175], v20 offset:9472\n"
  "s_xor_b32 s74, s56, 64\n"
  "v_xor_b32 v20, s74, v15\n"
  "v_add_u32 v20, 0, v20\n"
  "ds_read_b64_tr_b16 a[176:177], v20 offset:0\n"
  "ds_read_b64_tr_b16 a[178:179], v20 offset:256\n"
  "ds_read_b64_tr_b16 a[180:181], v20 offset:1024\n"
  "ds_read_b64_tr_b16 a[182:183], v20 offset:1280\n"
  "ds_read_b64_tr_b16 a[184:185], v20 offset:8192\n"
  "ds_read_b64_tr_b16 a[186:187], v20 offset:8448\n"
  "ds_read_b64_tr_b16 a[188:189], v20 offset:9216\n"
  "ds_read_b64_tr_b16 a[190:191], v20 offset:9472\n"
  "s_xor_b32 s74, s56, 96\n"
  "v_xor_b32 v20, s74, v15\n"
  "v_add_u32 v20, 0, v20\n"
  "ds_read_b64_tr_b16 a[192:193], v20 offset:0\n"
  "ds_read_b64_tr_b16 a[194:195], v20 offset:256\n"
  "ds_read_b64_tr_b16 a[196:197], v20 offset:1024\n"
  "ds_read_b64_tr_b16 a[198:199], v20 offset:1280\n"
  "ds_read_b64_tr_b16 a[200:201], v20 offset:8192\n"
  "ds_read_b64_tr_b16 a[202:203], v20 offset:8448\n"
  "ds_read_b64_tr_b16 a[204:205], v20 offset:9216\n"
  "ds_read_b64_tr_b16 a[206:207], v20 offset:9472\n"
  "HLS9:\n"
  "s_waitcnt vmcnt(28) lgkmcnt(0)\n"
  "s_barrier\n"
  "s_add_u32 m0, s79, 0\n"
  "s_add_u32 s74, s38, 2097152\n"
  "s_addc_u32 s75, s39, 0\n"
  "global_load_lds_dwordx4 v12, s[74:75]\n"
  "s_add_u32 m0, s79, 2048\n"
  "s_add_u32 s74, s38, 2162688\n"
  "s_addc_u32 s75, s39, 0\n"
  "global_load_lds_dwordx4 v12, s[74:75]\n"
  "s_add_u32 m0, s79, 8192\n"
  "s_add_u32 s74, s38, 2359296\n"
  "s_addc_u32 s75, s39, 0\n"
  "global_load_lds_dwordx4 v12, s[74:75]\n"
  "s_add_u32 m0, s79, 10240\n"
  "s_add_u32 s74, s38, 2424832\n"
  "s_addc_u32 s75, s39, 0\n"
  "global_load_lds_dwordx4 v12, s[74:75]\n"
  "s_cmp_lg_u32 s48, 1\n"
  "s_cbranch_scc1 HLS10\n"
  "s_xor_b32 s74, s56, 0\n"
  "v_xor_b32 v20, s74, v15\n"
  "v_add_u32 v20, 16384, v20\n"
  "ds_read_b64_tr_b16 a[144:145], v20 offset:0\n"
  "ds_read_b64_tr_b16 a[146:147], v20 offset:256\n"
  "ds_read_b64_tr_b16 a[148:149], v20 offset:1024\n"
  "ds_read_b64_tr_b16 a[150:151], v20 offset:1280\n"
  "ds_read_b64_tr_b16 a[152:153], v20 offset:8192\n"
  "ds_read_b64_tr_b16 a[154:155], v20 offset:8448\n"
  "ds_read_b64_tr_b16 a[156:157], v20 offset:9216\n"
  "ds_read_b64_tr_b16 a[158:159], v20 offset:9472\n"
  "s_xor_b32 s74, s56, 32\n"
  "v_xor_b32 v20, s74, v15\n"
  "v_add_u32 v20, 16384, v20\n"
  "ds_read_b64_tr_b16 a[160:161], v20 offset:0\n"
  "ds_read_b64_tr_b16 a[162:163], v20 offset:256\n"
  "ds_read_b64_tr_b16 a[164:165], v20 offset:1024\n"
  "ds_read_b64_tr_b16 a[166:167], v20 offset:1280\n"
  "ds_read_b64_tr_b16 a[168:169], v20 offset:8192\n"
  "ds_read_b64_tr_b16 a[170:171], v20 offset:8448\n"
  "ds_read_b64_tr_b16 a[172:173], v20 offset:9216\n"
  "ds_read_b64_tr_b16 a[174:175], v20 offset:9472\n"
  "s_xor_b32 s74, s56, 64\n"
  "v_xor_b32 v20, s74, v15\n"
  "v_add_u32 v20, 16384, v20\n"
  "ds_read_b64_tr_b16 a[176:177], v20 offset:0\n"
  "ds_read_b64_tr_b16 a[178:179], v20 offset:256\n"
  "ds_read_b64_tr_b16 a[180:181], v20 offset:1024\n"
  "ds_read_b64_tr_b16 a[182:183], v20 offset:1280\n"
  "ds_read_b64_tr_b16 a[184:185], v20 offset:8192\n"
  "ds_read_b64_tr_b16 a[186:187], v20 offset:8448\n"
  "ds_read_b64_tr_b16 a[188:189], v20 offset:9216\n"
  "ds_read_b64_tr_b16 a[190:191], v20 offset:9472\n"
  "s_xor_b32 s74, s56, 96\n"
  "v_xor_b32 v20, s74, v15\n"
  "v_add_u32 v20, 16384, v20\n"
  "ds_read_b64_tr_b16 a[192:193], v20 offset:0\n"
  "ds_read_b64_tr_b16 a[194:195], v20 offset:256\n"
  "ds_read_b64_tr_b16 a[196:197], v20 offset:1024\n"
  "ds_read_b64_tr_b16 a[198:199], v20 offset:1280\n"
  "ds_read_b64_tr_b16 a[200:201], v20 offset:8192\n"
  "ds_read_b64_tr_b16 a[202:203], v20 offset:8448\n"
  "ds_read_b64_tr_b16 a[204:205], v20 offset:9216\n"
  "ds_read_b64_tr_b16 a[206:207], v20 offset:9472\n"
  "HLS10:\n"
  "s_waitcnt vmcnt(28) lgkmcnt(0)\n"
  "s_barrier\n"
  "s_add_u32 m0, s79, 16384\n"
  "s_add_u32 s74, s38, 2621440\n"
  "s_addc_u32 s75, s39, 0\n"
  "global_load_lds_dwordx4 v12, s[74:75]\n"
  "s_add_u32 m0, s79, 18432\n"
  "s_add_u32 s74, s38, 2686976\n"
  "s_addc_u32 s75, s39, 0\n"
  "global_load_lds_dwordx4 v12, s[74:75]\n"
  "s_add_u32 m0, s79, 24576\n"
  "s_add_u32 s74, s38, 2883584\n"
  "s_addc_u32 s75, s39, 0\n"
  "global_load_lds_dwordx4 v12, s[74:75]\n"
  "s_add_u32 m0, s79, 26624\n"
  "s_add_u32 s74, s38, 2949120\n"
  "s_addc_u32 s75, s39, 0\n"
  "global_load_lds_dwordx4 v12, s[74:75]\n"
  "s_cmp_lg_u32 s48, 2\n"
  "s_cbranch_scc1 HLS11\n"
  "s_xor_b32 s74, s56, 0\n"
  "v_xor_b32 v20, s74, v15\n"
  "v_add_u32 v20, 32768, v20\n"
  "ds_read_b64_tr_b16 a[144:145], v20 offset:0\n"
  "ds_read_b64_tr_b16 a[146:147], v20 offset:256\n"
  "ds_read_b64_tr_b16 a[148:149], v20 offset:1024\n"
  "ds_read_b64_tr_b16 a[150:151], v20 offset:1280\n"
  "ds_read_b64_tr_b16 a[152:153], v20 offset:8192\n"
  "ds_read_b64_tr_b16 a[154:155], v20 offset:8448\n"
  "ds_read_b64_tr_b16 a[156:157], v20 offset:9216\n"
  "ds_read_b64_tr_b16 a[158:159], v20 offset:9472\n"
  "s_xor_b32 s74, s56, 32\n"
  "v_xor_b32 v20, s74, v15\n"
  "v_add_u32 v20, 32768, v20\n"
  "ds_read_b64_tr_b16 a[160:161], v20 offset:0\n"
  "ds_read_b64_tr_b16 a[162:163], v20 offset:256\n"
  "ds_read_b64_tr_b16 a[164:165], v20 offset:1024\n"
  "ds_read_b64_tr_b16 a[166:167], v20 offset:1280\n"
  "ds_read_b64_tr_b16 a[168:169], v20 offset:8192\n"
  "ds_read_b64_tr_b16 a[170:171], v20 offset:8448\n"
  "ds_read_b64_tr_b16 a[172:173], v20 offset:9216\n"
  "ds_read_b64_tr_b16 a[174:175], v20 offset:9472\n"
  "s_xor_b32 s74, s56, 64\n"
  "v_xor_b32 v20, s74, v15\n"
  "v_add_u32 v20, 32768, v20\n"
  "ds_read_b64_tr_b16 a[176:177], v20 offset:0\n"
  "ds_read_b64_tr_b16 a[178:179], v20 offset:256\n"
  "ds_read_b64_tr_b16 a[180:181], v20 offset:1024\n"
  "ds_read_b64_tr_b16 a[182:183], v20 offset:1280\n"
  "ds_read_b64_tr_b16 a[184:185], v20 offset:8192\n"
  "ds_read_b64_tr_b16 a[186:187], v20 offset:8448\n"
  "ds_read_b64_tr_b16 a[188:189], v20 offset:9216\n"
  "ds_read_b64_tr_b16 a[190:191], v20 offset:9472\n"
  "s_xor_b32 s74, s56, 96\n"
  "v_xor_b32 v20, s74, v15\n"
  "v_add_u32 v20, 32768, v20\n"
  "ds_read_b64_tr_b16 a[192:193], v20 offset:0\n"
  "ds_read_b64_tr_b16 a[194:195], v20 offset:256\n"
  "ds_read_b64_tr_b16 a[196:197], v20 offset:1024\n"
  "ds_read_b64_tr_b16 a[198:199], v20 offset:1280\n"
  "ds_read_b64_tr_b16 a[200:201], v20 offset:8192\n"
  "ds_read_b64_tr_b16 a[202:203], v20 offset:8448\n"
  "ds_read_b64_tr_b16 a[204:205], v20 offset:9216\n"
  "ds_read_b64_tr_b16 a[206:207], v20 offset:9472\n"
  "HLS11:\n"
  "s_waitcnt vmcnt(28) lgkmcnt(0)\n"
  "s_barrier\n"
  "s_add_u32 m0, s79, 32768\n"
  "s_add_u32 s74, s38, 3145728\n"
  "s_addc_u32 s75, s39, 0\n"
  "global_load_lds_dwordx4 v12, s[74:75]\n"
  "s_add_u32 m0, s79, 34816\n"
  "s_add_u32 s74, s38, 3211264\n"
  "s_addc_u32 s75, s39, 0\n"
  "global_load_lds_dwordx4 v12, s[74:75]\n"
  "s_add_u32 m0, s79, 40960\n"
  "s_add_u32 s74, s38, 3407872\n"
  "s_addc_u32 s75, s39, 0\n"
  "global_load_lds_dwordx4 v12, s[74:75]\n"
  "s_add_u32 m0, s79, 43008\n"
  "s_add_u32 s74, s38, 3473408\n"
  "s_addc_u32 s75, s39, 0\n"
  "global_load_lds_dwordx4 v12, s[74:75]\n"
  "s_cmp_lg_u32 s48, 3\n"
  "s_cbranch_scc1 HLS12\n"
  "s_xor_b32 s74, s56, 0\n"
  "v_xor_b32 v20, s74, v15\n"
  "v_add_u32 v20, 49152, v20\n"
  "ds_read_b64_tr_b16 a[144:145], v20 offset:0\n"
  "ds_read_b64_tr_b16 a[146:147], v20 offset:256\n"
  "ds_read_b64_tr_b16 a[148:149], v20 offset:1024\n"
  "ds_read_b64_tr_b16 a[150:151], v20 offset:1280\n"
  "ds_read_b64_tr_b16 a[152:153], v20 offset:8192\n"
  "ds_read_b64_tr_b16 a[154:155], v20 offset:8448\n"
  "ds_read_b64_tr_b16 a[156:157], v20 offset:9216\n"
  "ds_read_b64_tr_b16 a[158:159], v20 offset:9472\n"
  "s_xor_b32 s74, s56, 32\n"
  "v_xor_b32 v20, s74, v15\n"
  "v_add_u32 v20, 49152, v20\n"
  "ds_read_b64_tr_b16 a[160:161], v20 offset:0\n"
  "ds_read_b64_tr_b16 a[162:163], v20 offset:256\n"
  "ds_read_b64_tr_b16 a[164:165], v20 offset:1024\n"
  "ds_read_b64_tr_b16 a[166:167], v20 offset:1280\n"
  "ds_read_b64_tr_b16 a[168:169], v20 offset:8192\n"
  "ds_read_b64_tr_b16 a[170:171], v20 offset:8448\n"
  "ds_read_b64_tr_b16 a[172:173], v20 offset:9216\n"
  "ds_read_b64_tr_b16 a[174:175], v20 offset:9472\n"
  "s_xor_b32 s74, s56, 64\n"
  "v_xor_b32 v20, s74, v15\n"
  "v_add_u32 v20, 49152, v20\n"
  "ds_read_b64_tr_b16 a[176:177], v20 offset:0\n"
  "ds_read_b64_tr_b16 a[178:179], v20 offset:256\n"
  "ds_read_b64_tr_b16 a[180:181], v20 offset:1024\n"
  "ds_read_b64_tr_b16 a[182:183], v20 offset:1280\n"
  "ds_read_b64_tr_b16 a[184:185], v20 offset:8192\n"
  "ds_read_b64_tr_b16 a[186:187], v20 offset:8448\n"
  "ds_read_b64_tr_b16 a[188:189], v20 offset:9216\n"
  "ds_read_b64_tr_b16 a[190:191], v20 offset:9472\n"
  "s_xor_b32 s74, s56, 96\n"
  "v_xor_b32 v20, s74, v15\n"
  "v_add_u32 v20, 49152, v20\n"
  "ds_read_b64_tr_b16 a[192:193], v20 offset:0\n"
  "ds_read_b64_tr_b16 a[194:195], v20 offset:256\n"
  "ds_read_b64_tr_b16 a[196:197], v20 offset:1024\n"
  "ds_read_b64_tr_b16 a[198:199], v20 offset:1280\n"
  "ds_read_b64_tr_b16 a[200:201], v20 offset:8192\n"
  "ds_read_b64_tr_b16 a[202:203], v20 offset:8448\n"
  "ds_read_b64_tr_b16 a[204:205], v20 offset:9216\n"
  "ds_read_b64_tr_b16 a[206:207], v20 offset:9472\n"
  "HLS12:\n"
  "s_waitcnt vmcnt(28) lgkmcnt(0)\n"
  "s_barrier\n"
  "s_add_u32 m0, s79, 49152\n"
  "s_add_u32 s74, s38, 3670016\n"
  "s_addc_u32 s75, s39, 0\n"
  "global_load_lds_dwordx4 v12, s[74:75]\n"
  "s_add_u32 m0, s79, 51200\n"
  "s_add_u32 s74, s38, 3735552\n"
  "s_addc_u32 s75, s39, 0\n"
  "global_load_lds_dwordx4 v12, s[74:75]\n"
  "s_add_u32 m0, s79, 57344\n"
  "s_add_u32 s74, s38, 3932160\n"
  "s_addc_u32 s75, s39, 0\n"
  "global_load_lds_dwordx4 v12, s[74:75]\n"
  "s_add_u32 m0, s79, 59392\n"
  "s_add_u32 s74, s38, 3997696\n"
  "s_addc_u32 s75, s39, 0\n"
  "global_load_lds_dwordx4 v12, s[74:75]\n"
  "s_lshr_b32 s74, s52, 3\n"
  "s_and_b32 s74, s74, 64\n"
  "s_and_b32 s75, s48, 1\n"
  "s_lshl_b32 s75, s75, 5\n"
  "s_add_u32 s74, s74, s75\n"
  "v_xor_b32 v20, s74, v15\n"
  "v_add_u32 v20, 65536, v20\n"
  "ds_read_b64_tr_b16 a[208:209], v20 offset:0\n"
  "ds_read_b64_tr_b16 a[210:211], v20 offset:256\n"
  "ds_read_b64_tr_b16 a[212:213], v20 offset:1024\n"
  "ds_read_b64_tr_b16 a[214:215], v20 offset:1280\n"
  "ds_read_b64_tr_b16 a[216:217], v20 offset:8192\n"
  "ds_read_b64_tr_b16 a[218:219], v20 offset:8448\n"
  "ds_read_b64_tr_b16 a[220:221], v20 offset:9216\n"
  "ds_read_b64_tr_b16 a[222:223], v20 offset:9472\n"
  "s_waitcnt vmcnt(28) lgkmcnt(0)\n"
  "s_barrier\n"
  "s_add_u32 m0, s79, 65536\n"
  "s_add_u32 s74, s38, 4194304\n"
  "s_addc_u32 s75, s39, 0\n"
  "global_load_lds_dwordx4 v12, s[74:75]\n"
  "s_add_u32 m0, s79, 67584\n"
  "s_add_u32 s74, s38, 4259840\n"
  "s_addc_u32 s75, s39, 0\n"
  "global_load_lds_dwordx4 v12, s[74:75]\n"
  "s_add_u32 m0, s79, 73728\n"
  "s_add_u32 s74, s38, 4456448\n"
  "s_addc_u32 s75, s39, 0\n"
  "global_load_lds_dwordx4 v12, s[74:75]\n"
  "s_add_u32 m0, s79, 75776\n"
  "s_add_u32 s74, s38, 4521984\n"
  "s_addc_u32 s75, s39, 0\n"
  "global_load_lds_dwordx4 v12, s[74:75]\n"
  "v_add_u32 v20, 81920, v13\n"
  "v_xor_b32 v21, 32, v20\n"
  "ds_read_b64_tr_b16 v[112:113], v20 offset:0\n"
  "ds_read_b64_tr_b16 v[114:115], v20 offset:2048\n"
  "ds_read_b64_tr_b16 v[116:117], v20 offset:8192\n"
  "ds_read_b64_tr_b16 v[118:119], v20 offset:10240\n"
  "ds_read_b64_tr_b16 v[120:121], v21 offset:0\n"
  "ds_read_b64_tr_b16 v[122:123], v21 offset:2048\n"
  "ds_read_b64_tr_b16 v[124:125], v21 offset:8192\n"
  "ds_read_b64_tr_b16 v[126:127], v21 offset:10240\n"
  "s_waitcnt vmcnt(28) lgkmcnt(0)\n"
  "s_barrier\n"
  "v_add_u32 v20, 98304, v13\n"
  "v_xor_b32 v21, 32, v20\n"
  "ds_read_b64_tr_b16 v[128:129], v20 offset:0\n"
  "ds_read_b64_tr_b16 v[130:131], v20 offset:2048\n"
  "ds_read_b64_tr_b16 v[132:133], v20 offset:8192\n"
  "ds_read_b64_tr_b16 v[134:135], v20 offset:10240\n"
  "ds_read_b64_tr_b16 v[136:137], v21 offset:0\n"
  "ds_read_b64_tr_b16 v[138:139], v21 offset:2048\n"
  "ds_read_b64_tr_b16 v[140:141], v21 offset:8192\n"
  "ds_read_b64_tr_b16 v[142:143], v21 offset:10240\n"
  "s_waitcnt vmcnt(24) lgkmcnt(0)\n"
  "s_barrier\n"
  "v_add_u32 v20, 114688, v13\n"
  "v_xor_b32 v21, 32, v20\n"
  "ds_read_b64_tr_b16 v[144:145], v20 offset:0\n"
  "ds_read_b64_tr_b16 v[146:147], v20 offset:2048\n"
  "ds_read_b64_tr_b16 v[148:149], v20 offset:8192\n"
  "ds_read_b64_tr_b16 v[150:151], v20 offset:10240\n"
  "ds_read_b64_tr_b16 v[152:153], v21 offset:0\n"
  "ds_read_b64_tr_b16 v[154:155], v21 offset:2048\n"
  "ds_read_b64_tr_b16 v[156:157], v21 offset:8192\n"
  "ds_read_b64_tr_b16 v[158:159], v21 offset:10240\n"
  "s_waitcnt vmcnt(20) lgkmcnt(0)\n"
  "s_barrier\n"
  "v_add_u32 v20, 131072, v13\n"
  "v_xor_b32 v21, 32, v20\n"
  "ds_read_b64_tr_b16 v[160:161], v20 offset:0\n"
  "ds_read_b64_tr_b16 v[162:163], v20 offset:2048\n"
  "ds_read_b64_tr_b16 v[164:165], v20 offset:8192\n"
  "ds_read_b64_tr_b16 v[166:167], v20 offset:10240\n"
  "ds_read_b64_tr_b16 v[168:169], v21 offset:0\n"
  "ds_read_b64_tr_b16 v[170:171], v21 offset:2048\n"
  "ds_read_b64_tr_b16 v[172:173], v21 offset:8192\n"
  "ds_read_b64_tr_b16 v[174:175], v21 offset:10240\n"
  "s_waitcnt vmcnt(16) lgkmcnt(0)\n"
  "s_barrier\n"
  "v_add_u32 v20, 0, v13\n"
  "v_xor_b32 v21, 32, v20\n"
  "ds_read_b64_tr_b16 v[176:177], v20 offset:0\n"
  "ds_read_b64_tr_b16 v[178:179], v20 offset:2048\n"
  "ds_read_b64_tr_b16 v[180:181], v20 offset:8192\n"
  "ds_read_b64_tr_b16 v[182:183], v20 offset:10240\n"
  "ds_read_b64_tr_b16 v[184:185], v21 offset:0\n"
  "ds_read_b64_tr_b16 v[186:187], v21 offset:2048\n"
  "ds_read_b64_tr_b16 v[188:189], v21 offset:8192\n"
  "ds_read_b64_tr_b16 v[190:191], v21 offset:10240\n"
  "s_waitcnt vmcnt(12) lgkmcnt(0)\n"
  "s_barrier\n"
  "v_add_u32 v20, 16384, v13\n"
  "v_xor_b32 v21, 32, v20\n"
  "ds_read_b64_tr_b16 v[192:193], v20 offset:0\n"
  "ds_read_b64_tr_b16 v[194:195], v20 offset:2048\n"
  "ds_read_b64_tr_b16 v[196:197], v20 offset:8192\n"
  "ds_read_b64_tr_b16 v[198:199], v20 offset:10240\n"
  "ds_read_b64_tr_b16 v[200:201], v21 offset:0\n"
  "ds_read_b64_tr_b16 v[202:203], v21 offset:2048\n"
  "ds_read_b64_tr_b16 v[204:205], v21 offset:8192\n"
  "ds_read_b64_tr_b16 v[206:207], v21 offset:10240\n"
  "s_waitcnt vmcnt(8) lgkmcnt(0)\n"
  "s_barrier\n"
  "v_add_u32 v20, 32768, v13\n"
  "v_xor_b32 v21, 32, v20\n"
  "ds_read_b64_tr_b16 v[208:209], v20 offset:0\n"
  "ds_read_b64_tr_b16 v[210:211], v20 offset:2048\n"
  "ds_read_b64_tr_b16 v[212:213], v20 offset:8192\n"
  "ds_read_b64_tr_b16 v[214:215], v20 offset:10240\n"
  "ds_read_b64_tr_b16 v[216:217], v21 offset:0\n"
  "ds_read_b64_tr_b16 v[218:219], v21 offset:2048\n"
  "ds_read_b64_tr_b16 v[220:221], v21 offset:8192\n"
  "ds_read_b64_tr_b16 v[222:223], v21 offset:10240\n"
  "s_waitcnt vmcnt(4) lgkmcnt(0)\n"
  "s_barrier\n"
  "v_add_u32 v20, 49152, v13\n"
  "v_xor_b32 v21, 32, v20\n"
  "ds_read_b64_tr_b16 v[224:225], v20 offset:0\n"
  "ds_read_b64_tr_b16 v[226:227], v20 offset:2048\n"
  "ds_read_b64_tr_b16 v[228:229], v20 offset:8192\n"
  "ds_read_b64_tr_b16 v[230:231], v20 offset:10240\n"
  "ds_read_b64_tr_b16 v[232:233], v21 offset:0\n"
  "ds_read_b64_tr_b16 v[234:235], v21 offset:2048\n"
  "ds_read_b64_tr_b16 v[236:237], v21 offset:8192\n"
  "ds_read_b64_tr_b16 v[238:239], v21 offset:10240\n"
  "s_waitcnt vmcnt(0) lgkmcnt(0)\n"
  "s_barrier\n"
  "v_add_u32 v20, 65536, v13\n"
  "v_xor_b32 v21, 32, v20\n"
  "ds_read_b64_tr_b16 v[240:241], v20 offset:0\n"
  "ds_read_b64_tr_b16 v[242:243], v20 offset:2048\n"
  "ds_read_b64_tr_b16 v[244:245], v20 offset:8192\n"
  "ds_read_b64_tr_b16 v[246:247], v20 offset:10240\n"
  "ds_read_b64_tr_b16 v[248:249], v21 offset:0\n"
  "ds_read_b64_tr_b16 v[250:251], v21 offset:2048\n"
  "ds_read_b64_tr_b16 v[252:253], v21 offset:8192\n"
  "ds_read_b64_tr_b16 v[254:255], v21 offset:10240\n"
  "s_waitcnt lgkmcnt(0)\n"
  "s_barrier\n"
  "v_mov_b32 v16, 0x3f803f80\n"
  "v_mov_b32 v17, 0x3f803f80\n"
  "v_mov_b32 v18, 0x3f803f80\n"
  "v_mov_b32 v19, 0x3f803f80\n"
  "v_mov_b32 v77, s55\n"
  "s_cmp_lt_u32 s48, 2\n"
  "s_cselect_b64 s[38:39], -1, 0\n"
  "s_mov_b32 s2, s45\n"
  "s_mov_b32 s100, s50\n"
  "s_mov_b32 s101, s51\n"
  "s_lshr_b32 s3, s52, 8\n"
  "s_and_b32 s3, s3, 4\n"
  "s_add_u32 s3, s3, s48\n"
  "s_mov_b32 s7, s57\n"
  "s_mov_b32 s14, s58\n"
  "s_mov_b32 s15, s59\n"
  "s_mov_b32 s55, s58\n"
  "s_mov_b32 s51, s59\n"
  "s_mov_b32 s56, 0\n"
  "s_mov_b32 s57, 0\n"
  "s_mov_b32 s58, 100000\n"
  "s_mov_b32 s59, 0\n"
  "s_mov_b32 s60, 562036736\n"
  "s_mov_b32 s61, 1006632960\n"
  "s_mov_b32 s81, -1\n"
  "s_add_u32 s74, s36, s5\n"
  "s_addc_u32 s75, s37, 0\n"
  "BLH:\n"
  "v_mov_b32 v78, 0\n"
  "global_load_dwordx2 v[78:79], v78, s[74:75] sc1\n"
  "s_waitcnt vmcnt(0)\n"
  "v_readfirstlane_b32 s77, v78\n"
  "v_readfirstlane_b32 s76, v79\n"
  "s_cmp_eq_u32 s77, s4\n"
  "s_cbranch_scc1 BDH\n"
  "s_sub_u32 s58, s58, 1\n"
  "s_cbranch_scc1 QUITH\n"
  "s_sleep 4\n"
  "s_branch BLH\n"
  "BDH:\n"
  "s_sub_u32 s77, s5, 16966656\n"
  "s_lshr_b32 s77, s77, 8\n"
  "s_lshl_b32 s77, s77, 7\n"
  "s_add_u32 s77, s77, 16777216\n"
  "s_add_u32 s74, s36, s77\n"
  "s_addc_u32 s75, s37, 0\n"
  "v_mov_b32 v78, 0\n"
  "v_mov_b32 v79, 1\n"
  "s_cmp_eq_u32 s48, 0\n"
  "s_cselect_b64 exec, 1, 0\n"
  "global_atomic_add v78, v79, s[74:75] sc1\n"
  "s_mov_b64 exec, -1\n"
  "s_and_b32 s74, s76, 2\n"
  "s_lshr_b32 s75, s74, 1\n"
  "s_lshl_b32 s4, 0x55555555, s75\n"
  "s_mov_b32 s5, s4\n"
  "s_lshl_b32 s74, s74, 12\n"
  "s_xor_b32 s2, s74, s2\n"
  "s_xor_b32 s100, s74, s100\n"
  "s_xor_b32 s101, s74, s101\n"
  "v_lshrrev_b32 v79, 21, v9\n"
  "v_lshlrev_b32 v79, 2, v79\n"
  "v_lshrrev_b32 v79, v79, s76\n"
  "v_and_b32 v79, 7, v79\n"
  "v_lshlrev_b32 v79, 12, v79\n"
  "v_xor_b32 v9, v9, v79\n"
  "v_lshrrev_b32 v79, 21, v10\n"
  "v_lshlrev_b32 v79, 2, v79\n"
  "v_lshrrev_b32 v79, v79, s76\n"
  "v_and_b32 v79, 7, v79\n"
  "v_lshlrev_b32 v79, 12, v79\n"
  "v_xor_b32 v10, v10, v79\n"
  "v_lshrrev_b32 v79, 21, v11\n"
  "v_lshlrev_b32 v79, 2, v79\n"
  "v_lshrrev_b32 v79, v79, s76\n"
  "v_and_b32 v79, 7, v79\n"
  "v_lshlrev_b32 v79, 12, v79\n"
  "v_xor_b32 v11, v11, v79\n"
  "s_lshr_b32 s74, s44, 19\n"
  "s_lshr_b32 s74, s76, s74\n"
  "s_and_b32 s74, s74, 7\n"
  "s_lshl_b32 s74, s74, 12\n"
  "s_xor_b32 s44, s44, s74\n"
  "s_xor_b32 s46, s46, s74\n"
  "s_add_u32 s45, s44, 128000\n"
  "s_add_u32 s76, s101, s7\n"
  "s_add_u32 s76, s76, 137216\n"
  "s_add_u32 s74, s36, s76\n"
  "s_addc_u32 s75, s37, 0\n"
  "v_mov_b32 v79, 1\n"
  "s_cmp_eq_u32 s48, 0\n"
  "s_cselect_b64 exec, 1, 0\n"
  "global_atomic_add v0, v79, s[74:75] sc1\n"
  "s_mov_b64 exec, -1\n"
  "s_add_u32 s18, s36, s2\n"
  "s_addc_u32 s19, s37, 0\n"
  "s_sub_u32 s18, s18, 768\n"
  "s_subb_u32 s19, s19, 0\n"
  "s_cmp_eq_u32 s48, 0\n"
  "s_cselect_b32 s98, -1, 0\n"
  "v_mov_b64 v[56:57], 0\n"
  "v_mov_b64 v[58:59], 0\n"
  "v_mov_b64 v[60:61], 0\n"
  "v_mov_b64 v[62:63], 0\n"
  "v_mov_b64 v[64:65], 0\n"
  "v_mov_b64 v[66:67], 0\n"
  "v_mov_b64 v[68:69], 0\n"
  "v_mov_b64 v[70:71], 0\n"
  "v_mov_b64 v[72:73], 0\n"
  "v_mov_b64 v[74:75], 0\n"
  "v_add_u32 v76, 34816, v0\n"
  "ds_write_b32 v0, v56 offset:33792\n"
  "ds_write_b32 v0, v56 offset:51200\n"
  "ds_write_b32 v76, v56 offset:33792\n"
  "s_mov_b32 s32, 0\n"
  "s_mov_b32 s35, 0\n"
  "v_mov_b32 v76, v0\n"
  "s_cmp_eq_u32 s52, 1024\n"
  "s_cselect_b64 s[0:1], -1, 0\n"
  "s_waitcnt lgkmcnt(0)\n"
  "s_barrier\n"
  "s_add_u32 s74, s36, s2\n"
  "s_addc_u32 s75, s37, 0\n"
  "PLHK:\n"
  "v_and_b32 v79, 128, v0\n"
  "global_load_dword v79, v79, s[74:75] sc1\n"
  "s_waitcnt vmcnt(0)\n"
  "v_readfirstlane_b32 s76, v79\n"
  "v_readlane_b32 s77, v79, 8\n"
  "s_min_u32 s76, s76, s77\n"
  "s_cmp_ge_u32 s76, 1\n"
  "s_cbranch_scc1 PDHK\n"
  "s_sub_u32 s58, s58, 1\n"
  "s_cbranch_scc1 QUITH\n"
  "s_sleep 4\n"
  "s_branch PLHK\n"
  "PDHK:\n"
  "s_bitcmp1_b64 s[4:5], 0\n"
  "s_cselect_b32 s74, 8192, 0\n"
  "s_and_b32 s75, 0, 63\n"
  "s_lshl_b32 s75, s75, 21\n"
  "s_add_u32 s74, s74, s75\n"
  "s_add_u32 s74, s74, 20971520\n"
  "s_xor_b32 s75, s74, s47\n"
  "s_add_u32 s40, s36, s75\n"
  "s_addc_u32 s41, s37, 0\n"
  "s_lshl_b32 s42, s80, 2\n"
  "s_add_u32 s42, s42, 17408\n"
  "s_mov_b32 m0, s42\n"
  "s_mov_b32 s52, 1\n"
  "global_load_lds_dwordx4 v0, s[40:41] offset:0 sc1\n"
  "global_load_lds_dwordx4 v0, s[40:41] offset:1024 sc1\n"
  "global_load_lds_dwordx4 v0, s[40:41] offset:2048 sc1\n"
  "global_load_lds_dwordx4 v0, s[40:41] offset:3072 sc1\n"
  "s_add_u32 s77, s101, s7\n"
  "s_add_u32 s77, s77, 138240\n"
  "s_add_u32 s74, s36, s77\n"
  "s_addc_u32 s75, s37, 0\n"
  "PLGH:\n"
  "v_mov_b32 v79, 0\n"
  "global_load_dword v79, v79, s[74:75] sc1\n"
  "s_waitcnt vmcnt(0)\n"
  "v_readfirstlane_b32 s76, v79\n"
  "s_cmp_lg_u32 s76, 0\n"
  "s_cbranch_scc1 PDGH\n"
  "s_sub_u32 s58, s58, 1\n"
  "s_cbranch_scc1 QUITH\n"
  "s_sleep 1\n"
  "s_branch PLGH\n"
  "PDGH:\n"
  "s_mov_b32 s49, s76\n"
  "s_mov_b32 s43, s49\n"
  "s_sub_u32 s6, s49, 0x100000\n"
  "s_mov_b32 s16, 0\n"
  "s_mov_b32 s17, 1\n"
  "s_mov_b32 m0, 33792\n"
  "s_mov_b32 s33, -1\n"
  "s_mov_b32 exec_lo, 0\n"
  "s_and_b32 exec_hi, s98, 0x01010000\n"
  "global_load_lds_dword v0, s[18:19] sc1\n"
  "s_mov_b64 exec, -1\n"
  "HTRY:\n"
  "s_sub_u32 s74, s49, s43\n"
  "v_mov_b32 v78, s74\n"
  "v_mov_b32 v15, s56\n"
  "v_lshrrev_b32 v79, 1, v0\n"
  "s_mul_i32 s78, s57, 163840\n"
  "s_lshl_b32 s79, s32, 16\n"
  "s_add_u32 s79, s79, s78\n"
  "s_add_u32 s79, s79, s44\n"
  "s_add_u32 s62, s36, s79\n"
  "s_addc_u32 s63, s37, 0\n"
  "s_add_u32 s64, s62, 32768\n"
  "s_addc_u32 s65, s63, 0\n"
  "s_lshl_b32 s79, s32, 10\n"
  "s_add_u32 s79, s79, s78\n"
  "s_add_u32 s79, s79, s45\n"
  "s_add_u32 s66, s36, s79\n"
  "s_addc_u32 s67, s37, 0\n"
  "s_add_u32 m0, s80, 0\n"
  "s_add_u32 s79, s80, 4096\n"
  "s_branch WGH\n"
  "WH:\n"
  "s_memrealtime s[74:75]\n"
  "s_waitcnt lgkmcnt(0)\n"
  "s_mul_i32 s74, s74, 24\n"
  "s_sub_i32 s74, s49, s74\n"
  "s_cmp_lt_i32 s74, 750\n"
  "s_cbranch_scc1 WFH\n"
  "s_cmp_gt_i32 s74, 0x40000\n"
  "s_cbranch_scc1 WEH\n"
  "s_sleep 2\n"
  "s_branch WH\n"
  "WFH:\n"
  "s_cmp_lt_i32 s74, 100\n"
  "s_cbranch_scc1 WEH\n"
  "WGH:\n"
  "s_memtime s[76:77]\n"
  "s_memrealtime s[20:21]\n"
  "s_nop 2\n"
  "s_memrealtime s[22:23]\n"
  "s_nop 2\n"
  "s_memrealtime s[24:25]\n"
  "s_nop 2\n"
  "s_memrealtime s[26:27]\n"
  "s_nop 2\n"
  "s_memrealtime s[28:29]\n"
  "s_nop 2\n"
  "s_memrealtime s[30:31]\n"
  "s_waitcnt lgkmcnt(0)\n"
  "s_add_u32 s20, s20, s22\n"
  "s_add_u32 s24, s24, s26\n"
  "s_add_u32 s28, s28, s30\n"
  "s_add_u32 s20, s20, s24\n"
  "s_add_u32 s20, s20, s28\n"
  "s_lshl_b32 s74, s20, 2\n"
  "s_sub_u32 s34, s74, s76\n"
  "s_sub_i32 s74, s49, s74\n"
  "s_sub_i32 s74, s74, 238\n"
  "s_max_i32 s74, s74, 0\n"
  "s_cmp_ge_i32 s74, 512\n"
  "s_cbranch_scc1 WH\n"
  "s_lshr_b32 s74, s74, 3\n"
  "s_lshl_b32 s74, s74, 2\n"
  "s_sub_i32 s74, 268, s74\n"
  "s_getpc_b64 s[76:77]\n"
  "s_add_u32 s76, s76, s74\n"
  "s_addc_u32 s77, s77, 0\n"
  "s_setpc_b64 s[76:77]\n"
  "s_nop 1\n"
  "s_nop 1\n"
  "s_nop 1\n"
  "s_nop 1\n"
  "s_nop 1\n"
  "s_nop 1\n"
  "s_nop 1\n"
  "s_nop 1\n"
  "s_nop 1\n"
  "s_nop 1\n"
  "s_nop 1\n"
  "s_nop 1\n"
  "s_nop 1\n"
  "s_nop 1\n"
  "s_nop 1\n"
  "s_nop 1\n"
  "s_nop 1\n"
  "s_nop 1\n"
  "s_nop 1\n"
  "s_nop 1\n"
  "s_nop 1\n"
  "s_nop 1\n"
  "s_nop 1\n"
  "s_nop 1\n"
  "s_nop 1\n"
  "s_nop 1\n"
  "s_nop 1\n"
  "s_nop 1\n"
  "s_nop 1\n"
  "s_nop 1\n"
  "s_nop 1\n"
  "s_nop 1\n"
  "s_nop 1\n"
  "s_nop 1\n"
  "s_nop 1\n"
  "s_nop 1\n"
  "s_nop 1\n"
  "s_nop 1\n"
  "s_nop 1\n"
  "s_nop 1\n"
  "s_nop 1\n"
  "s_nop 1\n"
  "s_nop 1\n"
  "s_nop 1\n"
  "s_nop 1\n"
  "s_nop 1\n"
  "s_nop 1\n"
  "s_nop 1\n"
  "s_nop 1\n"
  "s_nop 1\n"
  "s_nop 1\n"
  "s_nop 1\n"
  "s_nop 1\n"
  "s_nop 1\n"
  "s_nop 1\n"
  "s_nop 1\n"
  "s_nop 1\n"
  "s_nop 1\n"
  "s_nop 1\n"
  "s_nop 1\n"
  "s_nop 1\n"
  "s_nop 1\n"
  "s_nop 1\n"
  "s_nop 1\n"
  "WEH:\n"
  "global_load_lds_dwordx4 v1, s[62:63] sc1\n"
  "s_mov_b32 m0, s79\n"
  "s_cmp_lg_u32 s48, 3\n"
  "global_load_lds_dwordx4 v1, s[64:65] sc1\n"
  "s_cbranch_scc1 HN8\n"
  "s_mov_b32 m0, 8192\n"
  "s_nop 0\n"
  "global_load_lds_dwordx4 v1, s[66:67] sc1\n"
  "s_waitcnt vmcnt(14)\n"
  "s_branch HM8\n"
  "HN8:\n"
  "s_waitcnt vmcnt(13)\n"
  "HM8:\n"
  "s_cmp_eq_u32 s17, 0\n"
  "s_cbranch_scc1 HW8\n"
  "s_waitcnt vmcnt(9)\n"
  "s_cmp_eq_u32 s17, 1\n"
  "s_cbranch_scc1 HW8\n"
  "s_waitcnt vmcnt(3)\n"
  "HW8:\n"
  "s_barrier\n"
  "ds_read_b128 v[80:83], v76 offset:17408\n"
  "s_add_u32 s64, s36, s100\n"
  "s_addc_u32 s65, s37, 0\n"
  "s_cmp_eq_u32 s3, 4\n"
  "ds_read_b128 v[84:87], v76 offset:18432\n"
  "s_cselect_b64 s[22:23], 1, 0\n"
  "s_mov_b64 exec, s[22:23]\n"
  "global_store_dword v0, v15, s[64:65] sc1\n"
  "s_mov_b64 exec, -1\n"
  "ds_read_b128 v[88:91], v76 offset:19456\n"
  "s_add_u32 s26, s57, 1\n"
  "s_cmp_ge_u32 s26, 3\n"
  "s_cselect_b32 s75, 3, 0\n"
  "ds_read_b128 v[92:95], v76 offset:20480\n"
  "s_sub_u32 s26, s26, s75\n"
  "s_mul_i32 s26, s26, 17408\n"
  "s_add_u32 m0, s26, 33792\n"
  "ds_read_b128 v[96:99], v76 offset:21504\n"
  "s_add_u32 s75, s56, 1\n"
  "s_and_b32 s74, s75, 0x78\n"
  "s_add_u32 s74, s74, s75\n"
  "ds_read_b128 v[100:103], v76 offset:22528\n"
  "s_lshl_b32 s74, s74, 10\n"
  "s_add_u32 s74, s74, s101\n"
  "s_add_u32 s62, s36, s74\n"
  "ds_read_b128 v[104:107], v76 offset:23552\n"
  "s_addc_u32 s63, s37, 0\n"
  "s_mov_b32 exec_lo, s98\n"
  "s_and_b32 exec_hi, s98, 0xffff\n"
  "global_load_lds_dword v0, s[62:63] sc1\n"
  "s_mov_b64 exec, -1\n"
  "ds_read_b128 v[108:111], v76 offset:24576\n"
  "s_add_u32 s76, s57, s52\n"
  "s_cmp_ge_u32 s76, 3\n"
  "s_cselect_b32 s75, 3, 0\n"
  "ds_read_b128 a[224:227], v76 offset:25600\n"
  "s_sub_u32 s76, s76, s75\n"
  "s_mul_i32 s76, s76, 17408\n"
  "s_add_u32 m0, s42, s76\n"
  "ds_read_b128 a[228:231], v76 offset:26624\n"
  "s_and_b32 s77, s56, 1\n"
  "s_mov_b64 exec, 0x8000\n"
  "ds_max_u32 v76, v78 offset:33792\n"
  "s_mov_b64 exec, -1\n"
  "ds_read_b128 a[232:235], v76 offset:27648\n"
  "s_lshl_b32 s75, s77, 16\n"
  "s_add_u32 s70, s36, s75\n"
  "s_addc_u32 s71, s37, 0\n"
  "ds_read_b128 a[236:239], v76 offset:28672\n"
  "s_add_u32 s75, s56, 4\n"
  "s_and_b32 s74, s75, 0x78\n"
  "s_add_u32 s74, s74, s75\n"
  "ds_read_b128 a[240:243], v76 offset:29696\n"
  "s_lshl_b32 s74, s74, 10\n"
  "s_add_u32 s74, s74, s101\n"
  "ds_read_b128 a[244:247], v76 offset:30720\n"
  "ds_read_b128 a[248:251], v76 offset:31744\n"
  "ds_read_b128 a[252:255], v76 offset:32768\n"
  "s_cmp_lg_u32 s48, 3\n"
  "s_waitcnt vmcnt(4) lgkmcnt(0)\n"
  "s_cbranch_scc0 HD8\n"
  "s_waitcnt vmcnt(3)\n"
  "HD8:\n"
  "s_barrier\n"
  "; BODY\n"
  "ds_read_b128 v[20:23], v0 offset:0\n"
  "ds_read_b128 v[24:27], v0 offset:1024\n"
  "ds_read_b128 v[28:31], v0 offset:2048\n"
  "ds_read_b128 v[32:35], v0 offset:3072\n"
  "s_waitcnt lgkmcnt(3)\n"
  "v_smfmac_f32_16x16x64_bf16 v[56:59], v[20:23], v[112:119], v6\n"
  "v_smfmac_f32_16x16x64_bf16 v[60:63], v[20:23], v[120:127], v6\n"
  "s_waitcnt vmcnt(2)\n"
  "s_barrier\n"
  "ds_read_b128 v[36:39], v0 offset:4096\n"
  "s_waitcnt lgkmcnt(3)\n"
  "v_smfmac_f32_16x16x64_bf16 v[56:59], v[24:27], v[128:135], v6\n"
  "ds_read_b128 v[40:43], v0 offset:5120\n"
  "v_smfmac_f32_16x16x64_bf16 v[60:63], v[24:27], v[136:143], v6\n"
  "ds_read_b128 v[44:47], v0 offset:6144\n"
  "s_waitcnt lgkmcnt(4)\n"
  "v_smfmac_f32_16x16x64_bf16 v[56:59], v[28:31], v[144:151], v6\n"
  "ds_read_b128 v[48:51], v0 offset:7168\n"
  "v_smfmac_f32_16x16x64_bf16 v[60:63], v[28:31], v[152:159], v6\n"
  "ds_read_b128 v[52:55], v0 offset:8192\n"
  "s_waitcnt lgkmcnt(5)\n"
  "v_smfmac_f32_16x16x64_bf16 v[56:59], v[32:35], v[160:167], v6\n"
  "s_add_u32 s74, s74, s7\n"
  "s_add_u32 s66, s36, s74\n"
  "v_smfmac_f32_16x16x64_bf16 v[60:63], v[32:35], v[168:175], v6\n"
  "s_addc_u32 s67, s37, 0\n"
  "s_mul_i32 s74, s57, 131072\n"
  "s_waitcnt lgkmcnt(4)\n"
  "v_smfmac_f32_16x16x64_bf16 v[56:59], v[36:39], v[176:183], v6\n"
  "s_add_u32 s74, s74, s46\n"
  "s_add_u32 s68, s36, s74\n"
  "s_waitcnt lgkmcnt(3)\n"
  "v_smfmac_f32_16x16x64_bf16 v[56:59], v[40:43], v[192:199], v6\n"
  "s_addc_u32 s69, s37, 0\n"
  "s_add_u32 s72, s36, s53\n"
  "s_waitcnt lgkmcnt(2)\n"
  "v_smfmac_f32_16x16x64_bf16 v[56:59], v[44:47], v[208:215], v6\n"
  "s_addc_u32 s73, s37, 0\n"
  "s_add_u32 s27, s56, s52\n"
  "s_waitcnt lgkmcnt(1)\n"
  "v_smfmac_f32_16x16x64_bf16 v[56:59], v[48:51], v[224:231], v6\n"
  "s_min_u32 s27, s27, 63\n"
  "s_bitcmp1_b64 s[4:5], s27\n"
  "s_waitcnt lgkmcnt(0)\n"
  "v_smfmac_f32_16x16x64_bf16 v[56:59], v[52:55], v[240:247], v6\n"
  "s_cselect_b32 s74, 8192, 0\n"
  "s_and_b32 s75, s27, 63\n"
  "v_smfmac_f32_16x16x64_bf16 v[60:63], v[36:39], v[184:191], v6\n"
  "s_lshl_b32 s75, s75, 21\n"
  "s_add_u32 s74, s74, s75\n"
  "s_add_u32 s74, s74, 20971520\n"
  "v_smfmac_f32_16x16x64_bf16 v[60:63], v[40:43], v[200:207], v6\n"
  "s_xor_b32 s75, s74, s47\n"
  "s_add_u32 s40, s36, s75\n"
  "s_addc_u32 s41, s37, 0\n"
  "v_smfmac_f32_16x16x64_bf16 v[60:63], v[44:47], v[216:223], v6\n"
  "s_add_u32 s76, s16, s34\n"
  "s_sub_u32 s76, s76, s6\n"
  "v_add_f32 v56, v56, v58\n"
  "v_smfmac_f32_16x16x64_bf16 v[60:63], v[48:51], v[232:239], v6\n"
  "s_min_u32 s33, s33, s76\n"
  "s_sub_u32 s6, s6, 0x100000\n"
  "v_add_f32 v57, v57, v59\n"
  "v_smfmac_f32_16x16x64_bf16 v[60:63], v[52:55], v[248:255], v6\n"
  "s_cmp_lt_u32 s33, 6000\n"
  "s_cselect_b32 s24, 1, 0\n"
  "v_cvt_pk_bf16_f32 v14, v56, v57\n"
  "v_smfmac_f32_16x16x64_bf16 v[64:67], v[20:23], a[0:7], v6\n"
  "ds_write2_b32 v3, v14, v14 offset0:0 offset1:1\n"
  "v_smfmac_f32_16x16x64_bf16 v[68:71], v[20:23], a[8:15], v6\n"
  "v_cmp_u_f32 vcc, v56, v57\n"
  "v_smfmac_f32_16x16x64_bf16 v[64:67], v[24:27], a[16:23], v6\n"
  "v_add_f32 v60, v60, v62\n"
  "v_smfmac_f32_16x16x64_bf16 v[68:71], v[24:27], a[24:31], v6\n"
  "v_add_f32 v61, v61, v63\n"
  "v_smfmac_f32_16x16x64_bf16 v[64:67], v[28:31], a[32:39], v6\n"
  "v_cvt_pk_bf16_f32 v15, v60, v61\n"
  "v_smfmac_f32_16x16x64_bf16 v[68:71], v[28:31], a[40:47], v6\n"
  "ds_write2_b32 v3, v15, v15 offset0:128 offset1:129\n"
  "v_smfmac_f32_16x16x64_bf16 v[64:67], v[32:35], a[48:55], v6\n"
  "ds_write_b64 v4, v[14:15]\n"
  "v_smfmac_f32_16x16x64_bf16 v[68:71], v[32:35], a[56:63], v6\n"
  "s_cbranch_vccnz HBADM\n"
  "HOKM:\n"
  "v_smfmac_f32_16x16x64_bf16 v[64:67], v[36:39], a[64:71], v6\n"
  "s_add_u32 s74, s56, s3\n"
  "s_and_b32 s74, s74, 7\n"
  "s_cselect_b32 s24, 0, s24\n"
  "v_smfmac_f32_16x16x64_bf16 v[68:71], v[36:39], a[72:79], v6\n"
  "s_mov_b32 s25, 0\n"
  "v_smfmac_f32_16x16x64_bf16 v[64:67], v[40:43], a[80:87], v6\n"
  "s_waitcnt lgkmcnt(0)\n"
  "s_barrier\n"
  "v_smfmac_f32_16x16x64_bf16 v[68:71], v[40:43], a[88:95], v6\n"
  "ds_read_b64_tr_b16 v[20:21], v5 offset:0\n"
  "ds_read_b64_tr_b16 v[22:23], v5 offset:512\n"
  "v_smfmac_f32_16x16x64_bf16 v[64:67], v[44:47], a[96:103], v6\n"
  "global_load_dwordx4 v[36:39], v2, s[68:69] offset:0 sc1\n"
  "ds_read_b64_tr_b16 v[24:25], v5 offset:1024\n"
  "v_smfmac_f32_16x16x64_bf16 v[64:67], v[48:51], a[112:119], v6\n"
  "ds_read_b64_tr_b16 v[26:27], v5 offset:1536\n"
  "ds_read_b64_tr_b16 v[28:29], v5 offset:2048\n"
  "v_smfmac_f32_16x16x64_bf16 v[64:67], v[52:55], a[128:135], v6\n"
  "ds_read_b64_tr_b16 v[30:31], v5 offset:2560\n"
  "ds_read_b64_tr_b16 v[32:33], v5 offset:3072\n"
  "v_smfmac_f32_16x16x64_bf16 v[68:71], v[44:47], a[104:111], v6\n"
  "ds_read_b64_tr_b16 v[34:35], v5 offset:3584\n"
  "v_smfmac_f32_16x16x64_bf16 v[68:71], v[48:51], a[120:127], v6\n"
  "global_load_dwordx4 v[40:43], v2, s[68:69] offset:1024 sc1\n"
  "ds_read_b64_tr_b16 v[44:45], v5 offset:4096\n"
  "ds_read_b64_tr_b16 v[46:47], v5 offset:4608\n"
  "v_smfmac_f32_16x16x64_bf16 v[68:71], v[52:55], a[136:143], v6\n"
  "v_add_f32 v64, v64, v66\n"
  "s_waitcnt lgkmcnt(8)\n"
  "v_mfma_f32_16x16x32_bf16 v[56:59], v[80:83], v[20:23], 0\n"
  "v_add_f32 v65, v65, v67\n"
  "v_cvt_pk_bf16_f32 v14, v64, v65\n"
  "s_waitcnt lgkmcnt(6)\n"
  "v_mfma_f32_16x16x32_bf16 v[56:59], v[88:91], v[24:27], v[56:59]\n"
  "s_nop 0\n"
  "v_add_f32 v68, v68, v70\n"
  "v_add_f32 v69, v69, v71\n"
  "s_waitcnt lgkmcnt(4)\n"
  "v_mfma_f32_16x16x32_bf16 v[56:59], v[96:99], v[28:31], v[56:59]\n"
  "v_cvt_pk_bf16_f32 v15, v68, v69\n"
  "ds_write_b64 v4, v[14:15] offset:2048\n"
  "s_waitcnt lgkmcnt(3)\n"
  "v_mfma_f32_16x16x32_bf16 v[56:59], v[104:107], v[32:35], v[56:59]\n"
  "ds_read_b64_tr_b16 v[48:49], v5 offset:5120\n"
  "ds_read_b64_tr_b16 v[50:51], v5 offset:5632\n"
  "v_mfma_f32_16x16x32_bf16 v[60:63], v[84:87], v[20:23], 0\n"
  "s_mov_b64 exec, s[0:1]\n"
  "global_store_dwordx2 v79, v[14:15], s[72:73] sc1\n"
  "s_mov_b64 exec, -1\n"
  "v_mfma_f32_16x16x32_bf16 v[60:63], v[92:95], v[24:27], v[60:63]\n"
  "s_cmp_eq_u32 s56, 63\n"
  "s_cbranch_scc1 ENDH\n"
  "v_mfma_f32_16x16x32_bf16 v[60:63], v[100:103], v[28:31], v[60:63]\n"
  "v_exp_f32 v56, v56\n"
  "v_mfma_f32_16x16x32_bf16 v[60:63], v[108:111], v[32:35], v[60:63]\n"
  "s_waitcnt lgkmcnt(2)\n"
  "s_barrier\n"
  "v_mfma_f32_32x32x16_bf16 v[80:95], v[72:75], v[72:75], 0\n"
  "ds_read_b64_tr_b16 v[96:97], v5 offset:6144\n"
  "ds_read_b64_tr_b16 v[98:99], v5 offset:6656\n"
  "ds_read_b64_tr_b16 v[100:101], v5 offset:7168\n"
  "ds_read_b64_tr_b16 v[102:103], v5 offset:7680\n"
  "global_load_lds_dwordx4 v0, s[40:41] offset:0 sc1\n"
  "v_exp_f32 v57, v57\n"
  "v_exp_f32 v58, v58\n"
  "v_exp_f32 v59, v59\n"
  "v_exp_f32 v60, v60\n"
  "s_waitcnt lgkmcnt(2)\n"
  "v_mfma_f32_16x16x32_bf16 v[52:55], v[96:99], v[44:47], 0\n"
  "v_exp_f32 v61, v61\n"
  "s_waitcnt lgkmcnt(0)\n"
  "v_mfma_f32_16x16x32_bf16 v[52:55], v[100:103], v[48:51], v[52:55]\n"
  "global_load_lds_dwordx4 v0, s[40:41] offset:1024 sc1\n"
  "v_exp_f32 v62, v62\n"
  "v_exp_f32 v63, v63\n"
  "v_perm_b32 v44, v57, v56, v7\n"
  "v_perm_b32 v45, v59, v58, v7\n"
  "v_perm_b32 v46, v61, v60, v7\n"
  "v_perm_b32 v47, v63, v62, v7\n"
  "v_perm_b32 v48, v57, v56, v8\n"
  "v_perm_b32 v49, v59, v58, v8\n"
  "v_mfma_f32_16x16x32_bf16 v[68:71], v[16:19], v[44:47], 0\n"
  "v_perm_b32 v50, v61, v60, v8\n"
  "v_perm_b32 v51, v63, v62, v8\n"
  "v_mfma_f32_16x16x32_bf16 v[20:23], a[224:227], v[44:47], 0\n"
  "v_mul_f32_dpp v64, v52, v77 quad_perm:[0,1,0,1] row_mask:0xf bank_mask:0xf\n"
  "v_mul_f32_dpp v65, v53, v77 quad_perm:[0,1,0,1] row_mask:0xf bank_mask:0xf\n"
  "v_mfma_f32_16x16x32_bf16 v[68:71], v[16:19], v[48:51], v[68:71]\n"
  "v_fmac_f32_dpp v64, v54, v77 quad_perm:[2,3,2,3] row_mask:0xf bank_mask:0xf\n"
  "v_fmac_f32_dpp v65, v55, v77 quad_perm:[2,3,2,3] row_mask:0xf bank_mask:0xf\n"
  "v_mfma_f32_16x16x32_bf16 v[20:23], a[228:231], v[48:51], v[20:23]\n"
  "global_load_lds_dwordx4 v0, s[40:41] offset:2048 sc1\n"
  "v_exp_f32 v64, v64\n"
  "v_mfma_f32_16x16x32_bf16 v[24:27], a[232:235], v[44:47], 0\n"
  "v_exp_f32 v65, v65\n"
  "v_mfma_f32_16x16x32_bf16 v[24:27], a[236:239], v[48:51], v[24:27]\n"
  "v_perm_b32 v12, v65, v64, v7\n"
  "v_perm_b32 v13, v65, v64, v8\n"
  "v_mfma_f32_16x16x32_bf16 v[28:31], a[240:243], v[44:47], 0\n"
  "global_load_lds_dwordx4 v0, s[40:41] offset:3072 sc1\n"
  "HPV:\n"
  "v_mfma_f32_16x16x16_bf16 v[68:71], v[16:17], v[12:13], v[68:71]\n"
  "s_waitcnt vmcnt(6)\n"
  "v_mfma_f32_16x16x16_bf16 v[20:23], v[36:37], v[12:13], v[20:23]\n"
  "v_mfma_f32_16x16x16_bf16 v[24:27], v[38:39], v[12:13], v[24:27]\n"
  "v_mfma_f32_16x16x32_bf16 v[28:31], a[244:247], v[48:51], v[28:31]\n"
  "v_rcp_f32 v78, v68\n"
  "v_mfma_f32_16x16x32_bf16 v[32:35], a[248:251], v[44:47], 0\n"
  "v_mul_f32 v20, v78, v20\n"
  "v_mul_f32 v21, v78, v21\n"
  "v_mfma_f32_16x16x32_bf16 v[32:35], a[252:255], v[48:51], v[32:35]\n"
  "v_mul_f32 v22, v78, v22\n"
  "v_mul_f32 v23, v78, v23\n"
  "s_waitcnt vmcnt(5)\n"
  "v_mfma_f32_16x16x16_bf16 v[28:31], v[40:41], v[12:13], v[28:31]\n"
  "v_cvt_pk_bf16_f32 v104, v20, v21\n"
  "v_cvt_pk_bf16_f32 v105, v22, v23\n"
  "v_mul_f32 v24, v78, v24\n"
  "v_mul_f32 v25, v78, v25\n"
  "v_mfma_f32_16x16x16_bf16 v[32:35], v[42:43], v[12:13], v[32:35]\n"
  "v_mul_f32 v26, v78, v26\n"
  "v_mul_f32 v27, v78, v27\n"
  "v_cvt_pk_bf16_f32 v106, v24, v25\n"
  "v_cvt_pk_bf16_f32 v107, v26, v27\n"
  "v_subrev_u32 v79, s60, v68\n"
  "s_nop 0\n"
  "v_smfmac_f32_16x16x64_bf16 v[72:75], v[104:107], a[208:215], v6\n"
  "v_mul_f32 v28, v78, v28\n"
  "v_mul_f32 v29, v78, v29\n"
  "v_mul_f32 v30, v78, v30\n"
  "v_smfmac_f32_16x16x64_bf16 v[80:83], v[104:107], a[144:151], v6\n"
  "v_mul_f32 v31, v78, v31\n"
  "v_cvt_pk_bf16_f32 v108, v28, v29\n"
  "v_cvt_pk_bf16_f32 v109, v30, v31\n"
  "v_smfmac_f32_16x16x64_bf16 v[84:87], v[104:107], a[160:167], v6\n"
  "v_mul_f32 v32, v78, v32\n"
  "v_mul_f32 v33, v78, v33\n"
  "v_mul_f32 v34, v78, v34\n"
  "v_smfmac_f32_16x16x64_bf16 v[88:91], v[104:107], a[176:183], v6\n"
  "v_mul_f32 v35, v78, v35\n"
  "v_cvt_pk_bf16_f32 v110, v32, v33\n"
  "v_cvt_pk_bf16_f32 v111, v34, v35\n"
  "v_smfmac_f32_16x16x64_bf16 v[92:95], v[104:107], a[192:199], v6\n"
  "v_cmp_le_u32 vcc, s61, v79\n"
  "v_smfmac_f32_16x16x64_bf16 v[72:75], v[108:111], a[216:223], v6\n"
  "v_smfmac_f32_16x16x64_bf16 v[80:83], v[108:111], a[152:159], v6\n"
  "v_smfmac_f32_16x16x64_bf16 v[84:87], v[108:111], a[168:175], v6\n"
  "s_nop 0\n"
  "v_add_f32 v72, v72, v74\n"
  "s_cbranch_vccnz HBADZ\n"
  "HOKZ:\n"
  "v_smfmac_f32_16x16x64_bf16 v[88:91], v[108:111], a[184:191], v6\n"
  "v_add_f32 v73, v73, v75\n"
  "v_cvt_pk_bf16_f32 v74, v72, v73\n"
  "v_smfmac_f32_16x16x64_bf16 v[92:95], v[108:111], a[200:207], v6\n"
  "s_mov_b64 exec, s[38:39]\n"
  "global_store_dword v11, v74, s[70:71] offset:0 sc1\n"
  "s_mov_b64 exec, -1\n"
  "v_add_f32 v80, v80, v82\n"
  "v_add_f32 v81, v81, v83\n"
  "v_cvt_pk_bf16_f32 v82, v80, v81\n"
  "global_store_dword v9, v82, s[70:71] offset:0 sc1\n"
  "v_add_f32 v84, v84, v86\n"
  "v_add_f32 v85, v85, v87\n"
  "v_cvt_pk_bf16_f32 v86, v84, v85\n"
  "global_store_dword v9, v86, s[70:71] offset:128 sc1\n"
  "v_add_f32 v88, v88, v90\n"
  "v_add_f32 v89, v89, v91\n"
  "v_cvt_pk_bf16_f32 v90, v88, v89\n"
  "global_store_dword v10, v90, s[70:71] offset:0 sc1\n"
  "v_add_f32 v92, v92, v94\n"
  "v_add_f32 v93, v93, v95\n"
  "v_cvt_pk_bf16_f32 v94, v92, v93\n"
  "global_store_dword v10, v94, s[70:71] offset:128 sc1\n"
  "s_memtime s[16:17]\n"
  "; BODYEND\n"
  "v_add_f32 v79, v80, v81\n"
  "v_add_f32 v78, v84, v85\n"
  "v_add_f32 v79, v79, v78\n"
  "v_add_f32 v78, v88, v89\n"
  "v_add_f32 v79, v79, v78\n"
  "v_add_f32 v78, v92, v93\n"
  "v_add_f32 v79, v79, v78\n"
  "v_add_f32 v78, v72, v73\n"
  "v_add_f32 v79, v79, v78\n"
  "v_cmp_u_f32 vcc, v79, v79\n"
  "ds_read_b32 v15, v76 offset:33792\n"
  "s_memrealtime s[76:77]\n"
  "v_mov_b64 v[56:57], 0\n"
  "v_mov_b64 v[58:59], 0\n"
  "v_mov_b64 v[60:61], 0\n"
  "v_mov_b64 v[62:63], 0\n"
  "v_mov_b64 v[64:65], 0\n"
  "v_mov_b64 v[66:67], 0\n"
  "v_mov_b64 v[68:69], 0\n"
  "v_mov_b64 v[70:71], 0\n"
  "s_cbranch_vccnz HBADV\n"
  "HOKV:\n"
  "v_mov_b64 v[72:73], 0\n"
  "v_mov_b64 v[74:75], 0\n"
  "v_mov_b32 v14, s33\n"
  "v_add_u32 v76, s26, v0\n"
  "s_waitcnt lgkmcnt(0)\n"
  "v_readlane_b32 s8, v15, 0\n"
  "v_readlane_b32 s9, v15, 2\n"
  "v_readlane_b32 s10, v15, 4\n"
  "v_readlane_b32 s11, v15, 6\n"
  "v_readlane_b32 s12, v15, 8\n"
  "v_readlane_b32 s13, v15, 10\n"
  "v_readlane_b32 s74, v15, 12\n"
  "v_readlane_b32 s75, v15, 14\n"
  "v_readlane_b32 s49, v15, 15\n"
  "s_mov_b64 exec, s[24:25]\n"
  "global_atomic_umax v0, v14, s[66:67] sc1\n"
  "s_mov_b64 exec, -1\n"
  "s_add_u32 s56, s56, 1\n"
  "s_min_u32 s74, s74, s75\n"
  "s_sub_u32 s17, 2, s52\n"
  "s_cmp_le_u32 s74, s27\n"
  "s_cbranch_scc1 HKW\n"
  "s_add_u32 s75, s27, 3\n"
  "s_cmp_ge_u32 s74, s75\n"
  "s_cselect_b32 s75, s17, 0\n"
  "s_cmp_lg_u32 s75, 0\n"
  "s_cbranch_scc1 HFAR\n"
  "HKOK:\n"
  "s_add_u32 m0, s26, 33792\n"
  "s_mov_b32 exec_lo, 0\n"
  "s_and_b32 exec_hi, s98, 0x01010000\n"
  "global_load_lds_dword v0, s[18:19] sc1\n"
  "s_mov_b64 exec, -1\n"
  "s_sub_u32 s78, s56, 1\n"
  "s_max_u32 s8, s8, s9\n"
  "s_max_u32 s10, s10, s11\n"
  "s_max_u32 s8, s8, s10\n"
  "s_max_u32 s12, s12, s13\n"
  "s_cmp_eq_u32 s8, 0\n"
  "s_cselect_b32 s8, s14, s8\n"
  "s_sub_i32 s75, s8, s55\n"
  "s_min_i32 s75, s75, 24\n"
  "s_max_i32 s75, s75, -6\n"
  "s_add_u32 s55, s55, s75\n"
  "s_min_u32 s75, s8, s14\n"
  "s_max_u32 s55, s55, s75\n"
  "s_max_u32 s75, s8, s14\n"
  "s_add_u32 s75, s75, 40\n"
  "s_min_u32 s55, s55, s75\n"
  "s_mov_b32 s14, s8\n"
  "s_cmp_eq_u32 s12, 0\n"
  "s_cselect_b32 s12, s15, s12\n"
  "s_sub_i32 s75, s12, s51\n"
  "s_min_i32 s75, s75, 24\n"
  "s_max_i32 s75, s75, -6\n"
  "s_add_u32 s51, s51, s75\n"
  "s_min_u32 s75, s12, s15\n"
  "s_max_u32 s51, s51, s75\n"
  "s_max_u32 s75, s12, s15\n"
  "s_add_u32 s75, s75, 40\n"
  "s_min_u32 s51, s51, s75\n"
  "s_mov_b32 s15, s12\n"
  "s_min_u32 s75, s78, 31\n"
  "s_lshr_b32 s75, 700, s75\n"
  "s_add_u32 s74, s55, s75\n"
  "s_add_u32 s74, s74, 130\n"
  "s_add_u32 s50, s74, s51\n"
  "s_add_u32 s50, s50, s75\n"
  "s_add_u32 s50, s50, 230\n"
  "s_mov_b32 s33, -1\n"
  "s_add_u32 s53, s53, 65536\n"
  "s_add_u32 s57, s57, 1\n"
  "s_cmp_eq_u32 s57, 3\n"
  "s_cselect_b32 s57, 0, s57\n"
  "s_mul_i32 s99, s76, 24\n"
  "s_add_u32 s49, s49, s43\n"
  "s_sub_u32 s6, s49, s35\n"
  "s_mov_b32 s35, 0\n"
  "s_add_u32 s49, s49, s50\n"
  "s_mov_b32 s32, 0\n"
  "s_sub_i32 s74, s49, s99\n"
  "s_cmp_ge_i32 s74, 500\n"
  "s_cbranch_scc1 HTRY\n"
  "s_mov_b32 s32, 1\n"
  "s_add_u32 s99, s99, 680\n"
  "HSK:\n"
  "s_add_u32 s49, s49, 2200\n"
  "s_sub_i32 s74, s49, s99\n"
  "s_cmp_lt_i32 s74, 0\n"
  "s_cbranch_scc1 HSK\n"
  "s_branch HTRY\n"
  "HBADM:\n"
  "s_waitcnt lgkmcnt(0)\n"
  "s_nop 3\n"
  "v_mov_b32 v1, v1\n"
  "s_nop 2\n"
  "v_mov_b32 v1, v1\n"
  "s_nop 2\n"
  "s_mov_b32 s79, 0xff820000\n"
  "v_max3_u32 v78, v20, v21, v22\n"
  "v_max3_u32 v78, v78, v23, v24\n"
  "v_max3_u32 v78, v78, v25, v26\n"
  "v_max3_u32 v78, v78, v27, v28\n"
  "v_max3_u32 v78, v78, v29, v30\n"
  "v_max3_u32 v78, v78, v31, v32\n"
  "v_max3_u32 v78, v78, v33, v34\n"
  "v_max3_u32 v78, v78, v35, v36\n"
  "v_max3_u32 v78, v78, v37, v38\n"
  "v_max3_u32 v78, v78, v39, v40\n"
  "v_max3_u32 v78, v78, v41, v42\n"
  "v_max3_u32 v78, v78, v43, v44\n"
  "v_max3_u32 v78, v78, v45, v46\n"
  "v_max3_u32 v78, v78, v47, v48\n"
  "v_max3_u32 v78, v78, v49, v50\n"
  "v_max3_u32 v78, v78, v51, v52\n"
  "v_max3_u32 v78, v78, v53, v54\n"
  "v_max_u32 v78, v78, v55\n"
  "v_cmp_le_u32 vcc, s79, v78\n"
  "s_cbranch_vccnz HBM2\n"
  "s_mov_b32 s79, 0x7fff7fff\n"
  "v_and_b32 v78, s79, v20\n"
  "v_and_b32 v12, s79, v21\n"
  "v_pk_max_u16 v78, v78, v12\n"
  "v_and_b32 v12, s79, v22\n"
  "v_pk_max_u16 v78, v78, v12\n"
  "v_and_b32 v12, s79, v23\n"
  "v_pk_max_u16 v78, v78, v12\n"
  "v_and_b32 v12, s79, v24\n"
  "v_pk_max_u16 v78, v78, v12\n"
  "v_and_b32 v12, s79, v25\n"
  "v_pk_max_u16 v78, v78, v12\n"
  "v_and_b32 v12, s79, v26\n"
  "v_pk_max_u16 v78, v78, v12\n"
  "v_and_b32 v12, s79, v27\n"
  "v_pk_max_u16 v78, v78, v12\n"
  "v_and_b32 v12, s79, v28\n"
  "v_pk_max_u16 v78, v78, v12\n"
  "v_and_b32 v12, s79, v29\n"
  "v_pk_max_u16 v78, v78, v12\n"
  "v_and_b32 v12, s79, v30\n"
  "v_pk_max_u16 v78, v78, v12\n"
  "v_and_b32 v12, s79, v31\n"
  "v_pk_max_u16 v78, v78, v12\n"
  "v_and_b32 v12, s79, v32\n"
  "v_pk_max_u16 v78, v78, v12\n"
  "v_and_b32 v12, s79, v33\n"
  "v_pk_max_u16 v78, v78, v12\n"
  "v_and_b32 v12, s79, v34\n"
  "v_pk_max_u16 v78, v78, v12\n"
  "v_and_b32 v12, s79, v35\n"
  "v_pk_max_u16 v78, v78, v12\n"
  "v_and_b32 v12, s79, v36\n"
  "v_pk_max_u16 v78, v78, v12\n"
  "v_and_b32 v12, s79, v37\n"
  "v_pk_max_u16 v78, v78, v12\n"
  "v_and_b32 v12, s79, v38\n"
  "v_pk_max_u16 v78, v78, v12\n"
  "v_and_b32 v12, s79, v39\n"
  "v_pk_max_u16 v78, v78, v12\n"
  "v_and_b32 v12, s79, v40\n"
  "v_pk_max_u16 v78, v78, v12\n"
  "v_and_b32 v12, s79, v41\n"
  "v_pk_max_u16 v78, v78, v12\n"
  "v_and_b32 v12, s79, v42\n"
  "v_pk_max_u16 v78, v78, v12\n"
  "v_and_b32 v12, s79, v43\n"
  "v_pk_max_u16 v78, v78, v12\n"
  "v_and_b32 v12, s79, v44\n"
  "v_pk_max_u16 v78, v78, v12\n"
  "v_and_b32 v12, s79, v45\n"
  "v_pk_max_u16 v78, v78, v12\n"
  "v_and_b32 v12, s79, v46\n"
  "v_pk_max_u16 v78, v78, v12\n"
  "v_and_b32 v12, s79, v47\n"
  "v_pk_max_u16 v78, v78, v12\n"
  "v_and_b32 v12, s79, v48\n"
  "v_pk_max_u16 v78, v78, v12\n"
  "v_and_b32 v12, s79, v49\n"
  "v_pk_max_u16 v78, v78, v12\n"
  "v_and_b32 v12, s79, v50\n"
  "v_pk_max_u16 v78, v78, v12\n"
  "v_and_b32 v12, s79, v51\n"
  "v_pk_max_u16 v78, v78, v12\n"
  "v_and_b32 v12, s79, v52\n"
  "v_pk_max_u16 v78, v78, v12\n"
  "v_and_b32 v12, s79, v53\n"
  "v_pk_max_u16 v78, v78, v12\n"
  "v_and_b32 v12, s79, v54\n"
  "v_pk_max_u16 v78, v78, v12\n"
  "v_and_b32 v12, s79, v55\n"
  "v_pk_max_u16 v78, v78, v12\n"
  "v_lshrrev_b32 v12, 16, v78\n"
  "v_and_b32 v78, 0xffff, v78\n"
  "v_max_u32 v78, v78, v12\n"
  "v_cmp_lt_u32 vcc, 0x7f81, v78\n"
  "s_cbranch_vccz HOKM\n"
  "HBM2:\n"
  "s_memrealtime s[76:77]\n"
  "v_mov_b64 v[56:57], 0\n"
  "v_mov_b64 v[58:59], 0\n"
  "v_mov_b64 v[60:61], 0\n"
  "v_mov_b64 v[62:63], 0\n"
  "v_mov_b64 v[64:65], 0\n"
  "v_mov_b64 v[66:67], 0\n"
  "v_mov_b64 v[68:69], 0\n"
  "v_mov_b64 v[70:71], 0\n"
  "s_barrier\n"
  "s_sub_u32 s58, s58, 1\n"
  "s_cbranch_scc1 QUITH\n"
  "s_add_u32 s59, s59, 1\n"
  "s_mov_b32 s32, 1\n"
  "s_waitcnt lgkmcnt(0)\n"
  "s_mul_i32 s99, s76, 24\n"
  "s_add_u32 s99, s99, 480\n"
  "s_branch HSK\n"
  "HKW:\n"
  "s_waitcnt vmcnt(0) lgkmcnt(0)\n"
  "s_nop 3\n"
  "v_mov_b32 v1, v1\n"
  "s_nop 2\n"
  "v_mov_b32 v1, v1\n"
  "s_nop 2\n"
  "s_add_u32 s74, s36, s2\n"
  "s_addc_u32 s75, s37, 0\n"
  "PLHK2:\n"
  "v_and_b32 v12, 128, v0\n"
  "global_load_dword v12, v12, s[74:75] sc1\n"
  "s_waitcnt vmcnt(0)\n"
  "v_readfirstlane_b32 s76, v12\n"
  "v_readlane_b32 s77, v12, 8\n"
  "s_min_u32 s76, s76, s77\n"
  "s_cmp_gt_u32 s76, s27\n"
  "s_cbranch_scc1 PDHK2\n"
  "s_sub_u32 s58, s58, 1\n"
  "s_cbranch_scc1 QUITH\n"
  "s_sleep 2\n"
  "s_branch PLHK2\n"
  "PDHK2:\n"
  "s_add_u32 s59, s59, 0x100\n"
  "s_lshl_b32 s17, s17, 1\n"
  "global_load_lds_dwordx4 v0, s[40:41] offset:0 sc1\n"
  "global_load_lds_dwordx4 v0, s[40:41] offset:1024 sc1\n"
  "global_load_lds_dwordx4 v0, s[40:41] offset:2048 sc1\n"
  "global_load_lds_dwordx4 v0, s[40:41] offset:3072 sc1\n"
  "s_memrealtime s[76:77]\n"
  "s_waitcnt lgkmcnt(0)\n"
  "s_branch HKOK\n"
  "HFAR:\n"
  "s_add_u32 s78, s57, 2\n"
  "s_cmp_ge_u32 s78, 3\n"
  "s_cselect_b32 s75, 3, 0\n"
  "s_sub_u32 s78, s78, s75\n"
  "s_mul_i32 s78, s78, 17408\n"
  "s_add_u32 m0, s42, s78\n"
  "s_add_u32 s27, s27, 1\n"
  "s_bitcmp1_b64 s[4:5], s27\n"
  "s_cselect_b32 s74, 8192, 0\n"
  "s_and_b32 s75, s27, 63\n"
  "s_lshl_b32 s75, s75, 21\n"
  "s_add_u32 s74, s74, s75\n"
  "s_add_u32 s74, s74, 20971520\n"
  "s_xor_b32 s75, s74, s47\n"
  "s_add_u32 s40, s36, s75\n"
  "s_addc_u32 s41, s37, 0\n"
  "s_mov_b32 s52, 2\n"
  "global_load_lds_dwordx4 v0, s[40:41] offset:0 sc1\n"
  "global_load_lds_dwordx4 v0, s[40:41] offset:1024 sc1\n"
  "global_load_lds_dwordx4 v0, s[40:41] offset:2048 sc1\n"
  "global_load_lds_dwordx4 v0, s[40:41] offset:3072 sc1\n"
  "s_branch HKOK\n"
  "HBADV:\n"
  "s_waitcnt vmcnt(0) lgkmcnt(0)\n"
  "s_nop 3\n"
  "v_mov_b32 v1, v1\n"
  "s_nop 2\n"
  "v_mov_b32 v1, v1\n"
  "s_nop 2\n"
  "s_mov_b32 s74, 0x7fff7fff\n"
  "v_and_b32 v79, s74, v36\n"
  "v_and_b32 v78, s74, v37\n"
  "v_pk_max_u16 v79, v79, v78\n"
  "v_and_b32 v78, s74, v38\n"
  "v_pk_max_u16 v79, v79, v78\n"
  "v_and_b32 v78, s74, v39\n"
  "v_pk_max_u16 v79, v79, v78\n"
  "v_and_b32 v78, s74, v40\n"
  "v_pk_max_u16 v79, v79, v78\n"
  "v_and_b32 v78, s74, v41\n"
  "v_pk_max_u16 v79, v79, v78\n"
  "v_and_b32 v78, s74, v42\n"
  "v_pk_max_u16 v79, v79, v78\n"
  "v_and_b32 v78, s74, v43\n"
  "v_pk_max_u16 v79, v79, v78\n"
  "v_lshrrev_b32 v78, 16, v79\n"
  "v_and_b32 v79, 0xffff, v79\n"
  "v_max_u32 v79, v79, v78\n"
  "v_cmp_lt_u32 vcc, 0x7f81, v79\n"
  "s_cbranch_vccnz HVL\n"
  "s_mov_b32 s74, 0x7fff7fff\n"
  "s_mov_b32 s75, 0x7f817f81\n"
  "v_and_b32 v79, s74, v82\n"
  "v_pk_min_u16 v79, v79, s75\n"
  "v_bfi_b32 v82, s74, v79, v82\n"
  "v_and_b32 v79, s74, v86\n"
  "v_pk_min_u16 v79, v79, s75\n"
  "v_bfi_b32 v86, s74, v79, v86\n"
  "v_and_b32 v79, s74, v90\n"
  "v_pk_min_u16 v79, v79, s75\n"
  "v_bfi_b32 v90, s74, v79, v90\n"
  "v_and_b32 v79, s74, v94\n"
  "v_pk_min_u16 v79, v79, s75\n"
  "v_bfi_b32 v94, s74, v79, v94\n"
  "v_and_b32 v79, s74, v74\n"
  "v_pk_min_u16 v79, v79, s75\n"
  "v_bfi_b32 v74, s74, v79, v74\n"
  "s_mov_b64 exec, s[38:39]\n"
  "global_store_dword v11, v74, s[70:71] offset:0 sc1\n"
  "s_mov_b64 exec, -1\n"
  "global_store_dword v9, v82, s[70:71] offset:0 sc1\n"
  "global_store_dword v9, v86, s[70:71] offset:128 sc1\n"
  "global_store_dword v10, v90, s[70:71] offset:0 sc1\n"
  "global_store_dword v10, v94, s[70:71] offset:128 sc1\n"
  "s_branch HOKV\n"
  "HVL:\n"
  "s_sub_u32 s58, s58, 1\n"
  "s_cbranch_scc1 QUITH\n"
  "s_add_u32 s59, s59, 0x10000\n"
  "s_mov_b32 s35, 0x100000\n"
  "global_load_dwordx4 v[36:39], v2, s[68:69] offset:0 sc1\n"
  "global_load_dwordx4 v[40:43], v2, s[68:69] offset:1024 sc1\n"
  "s_mov_b32 s74, 0x7fff7fff\n"
  "s_waitcnt vmcnt(1)\n"
  "v_and_b32 v79, s74, v36\n"
  "v_and_b32 v78, s74, v37\n"
  "v_pk_max_u16 v79, v79, v78\n"
  "v_and_b32 v78, s74, v38\n"
  "v_pk_max_u16 v79, v79, v78\n"
  "v_and_b32 v78, s74, v39\n"
  "v_pk_max_u16 v79, v79, v78\n"
  "s_waitcnt vmcnt(0)\n"
  "v_and_b32 v78, s74, v40\n"
  "v_pk_max_u16 v79, v79, v78\n"
  "v_and_b32 v78, s74, v41\n"
  "v_pk_max_u16 v79, v79, v78\n"
  "v_and_b32 v78, s74, v42\n"
  "v_pk_max_u16 v79, v79, v78\n"
  "v_and_b32 v78, s74, v43\n"
  "v_pk_max_u16 v79, v79, v78\n"
  "v_lshrrev_b32 v78, 16, v79\n"
  "v_and_b32 v79, 0xffff, v79\n"
  "v_max_u32 v79, v79, v78\n"
  "v_cmp_lt_u32 vcc, 0x7f81, v79\n"
  "s_cbranch_vccz HREDO\n"
  "s_sleep 4\n"
  "s_branch HVL\n"
  "HREDO:\n"
  "v_mov_b64 v[80:81], 0\n"
  "v_mov_b64 v[82:83], 0\n"
  "v_mov_b64 v[84:85], 0\n"
  "v_mov_b64 v[86:87], 0\n"
  "v_mov_b64 v[88:89], 0\n"
  "v_mov_b64 v[90:91], 0\n"
  "v_mov_b64 v[92:93], 0\n"
  "v_mov_b64 v[94:95], 0\n"
  "v_mov_b64 v[72:73], 0\n"
  "v_mov_b64 v[74:75], 0\n"
  "v_mfma_f32_16x16x32_bf16 v[68:71], v[16:19], v[44:47], 0\n"
  "v_mfma_f32_16x16x32_bf16 v[20:23], a[224:227], v[44:47], 0\n"
  "v_mfma_f32_16x16x32_bf16 v[68:71], v[16:19], v[48:51], v[68:71]\n"
  "v_mfma_f32_16x16x32_bf16 v[20:23], a[228:231], v[48:51], v[20:23]\n"
  "v_mfma_f32_16x16x32_bf16 v[24:27], a[232:235], v[44:47], 0\n"
  "v_mfma_f32_16x16x32_bf16 v[24:27], a[236:239], v[48:51], v[24:27]\n"
  "v_mfma_f32_16x16x32_bf16 v[28:31], a[240:243], v[44:47], 0\n"
  "s_nop 7\n"
  "s_branch HPV\n"
  "HBADZ:\n"
  "s_waitcnt vmcnt(0) lgkmcnt(0)\n"
  "s_nop 3\n"
  "v_mov_b32 v1, v1\n"
  "s_nop 2\n"
  "v_mov_b32 v1, v1\n"
  "s_nop 2\n"
  "s_cmp_eq_u32 s81, s56\n"
  "s_cbranch_scc1 HOKZ\n"
  "s_mov_b32 s81, s56\n"
  "ds_read_b128 v[80:83], v76 offset:17408\n"
  "ds_read_b128 v[84:87], v76 offset:18432\n"
  "ds_read_b128 v[88:91], v76 offset:19456\n"
  "ds_read_b128 v[92:95], v76 offset:20480\n"
  "ds_read_b128 v[96:99], v76 offset:21504\n"
  "ds_read_b128 v[100:103], v76 offset:22528\n"
  "ds_read_b128 v[104:107], v76 offset:23552\n"
  "ds_read_b128 v[108:111], v76 offset:24576\n"
  "ds_read_b64_tr_b16 v[20:21], v5 offset:0\n"
  "ds_read_b64_tr_b16 v[22:23], v5 offset:512\n"
  "ds_read_b64_tr_b16 v[24:25], v5 offset:1024\n"
  "ds_read_b64_tr_b16 v[26:27], v5 offset:1536\n"
  "ds_read_b64_tr_b16 v[28:29], v5 offset:2048\n"
  "ds_read_b64_tr_b16 v[30:31], v5 offset:2560\n"
  "ds_read_b64_tr_b16 v[32:33], v5 offset:3072\n"
  "ds_read_b64_tr_b16 v[34:35], v5 offset:3584\n"
  "ds_read_b64_tr_b16 v[44:45], v5 offset:4096\n"
  "ds_read_b64_tr_b16 v[46:47], v5 offset:4608\n"
  "ds_read_b64_tr_b16 v[48:49], v5 offset:5120\n"
  "ds_read_b64_tr_b16 v[50:51], v5 offset:5632\n"
  "s_waitcnt lgkmcnt(10)\n"
  "v_mfma_f32_16x16x32_bf16 v[56:59], v[80:83], v[20:23], 0\n"
  "s_waitcnt lgkmcnt(8)\n"
  "v_mfma_f32_16x16x32_bf16 v[56:59], v[88:91], v[24:27], v[56:59]\n"
  "s_waitcnt lgkmcnt(6)\n"
  "v_mfma_f32_16x16x32_bf16 v[56:59], v[96:99], v[28:31], v[56:59]\n"
  "s_waitcnt lgkmcnt(4)\n"
  "v_mfma_f32_16x16x32_bf16 v[56:59], v[104:107], v[32:35], v[56:59]\n"
  "v_mfma_f32_16x16x32_bf16 v[60:63], v[84:87], v[20:23], 0\n"
  "v_mfma_f32_16x16x32_bf16 v[60:63], v[92:95], v[24:27], v[60:63]\n"
  "v_mfma_f32_16x16x32_bf16 v[60:63], v[100:103], v[28:31], v[60:63]\n"
  "v_mfma_f32_16x16x32_bf16 v[60:63], v[108:111], v[32:35], v[60:63]\n"
  "ds_read_b64_tr_b16 v[96:97], v5 offset:6144\n"
  "ds_read_b64_tr_b16 v[98:99], v5 offset:6656\n"
  "ds_read_b64_tr_b16 v[100:101], v5 offset:7168\n"
  "ds_read_b64_tr_b16 v[102:103], v5 offset:7680\n"
  "s_waitcnt lgkmcnt(2)\n"
  "v_mfma_f32_16x16x32_bf16 v[52:55], v[96:99], v[44:47], 0\n"
  "s_waitcnt lgkmcnt(0)\n"
  "v_mfma_f32_16x16x32_bf16 v[52:55], v[100:103], v[48:51], v[52:55]\n"
  "s_nop 3\n"
  "v_mov_b32 v1, v1\n"
  "s_nop 2\n"
  "v_mul_f32_dpp v64, v52, v77 quad_perm:[0,1,0,1] row_mask:0xf bank_mask:0xf\n"
  "v_mul_f32_dpp v65, v53, v77 quad_perm:[0,1,0,1] row_mask:0xf bank_mask:0xf\n"
  "s_nop 0\n"
  "v_fmac_f32_dpp v64, v54, v77 quad_perm:[2,3,2,3] row_mask:0xf bank_mask:0xf\n"
  "v_fmac_f32_dpp v65, v55, v77 quad_perm:[2,3,2,3] row_mask:0xf bank_mask:0xf\n"
  "v_max3_f32 v79, v56, v57, v58\n"
  "v_max3_f32 v79, v79, v59, v60\n"
  "v_max3_f32 v79, v79, v61, v62\n"
  "v_max3_f32 v79, v79, v63, v64\n"
  "v_max_f32 v79, v79, v65\n"
  "v_mov_b32 v78, v79\n"
  "s_nop 1\n"
  "v_permlane16_swap_b32 v79, v78\n"
  "v_max_f32 v79, v79, v78\n"
  "v_mov_b32 v78, v79\n"
  "s_nop 1\n"
  "v_permlane32_swap_b32 v79, v78\n"
  "v_max_f32 v79, v79, v78\n"
  "v_sub_f32 v56, v56, v79\n"
  "v_sub_f32 v57, v57, v79\n"
  "v_sub_f32 v58, v58, v79\n"
  "v_sub_f32 v59, v59, v79\n"
  "v_sub_f32 v60, v60, v79\n"
  "v_sub_f32 v61, v61, v79\n"
  "v_sub_f32 v62, v62, v79\n"
  "v_sub_f32 v63, v63, v79\n"
  "v_sub_f32 v64, v64, v79\n"
  "v_sub_f32 v65, v65, v79\n"
  "v_exp_f32 v56, v56\n"
  "v_exp_f32 v57, v57\n"
  "v_exp_f32 v58, v58\n"
  "v_exp_f32 v59, v59\n"
  "v_exp_f32 v60, v60\n"
  "v_exp_f32 v61, v61\n"
  "v_exp_f32 v62, v62\n"
  "v_exp_f32 v63, v63\n"
  "v_perm_b32 v44, v57, v56, v7\n"
  "v_perm_b32 v45, v59, v58, v7\n"
  "v_perm_b32 v46, v61, v60, v7\n"
  "v_perm_b32 v47, v63, v62, v7\n"
  "v_perm_b32 v48, v57, v56, v8\n"
  "v_perm_b32 v49, v59, v58, v8\n"
  "v_perm_b32 v50, v61, v60, v8\n"
  "v_perm_b32 v51, v63, v62, v8\n"
  "v_exp_f32 v64, v64\n"
  "v_exp_f32 v65, v65\n"
  "s_nop 0\n"
  "v_perm_b32 v12, v65, v64, v7\n"
  "v_perm_b32 v13, v65, v64, v8\n"
  "s_nop 1\n"
  "s_branch HREDO\n"
  "ENDH:\n"
  "s_waitcnt vmcnt(0) lgkmcnt(0)\n"
  "s_barrier\n"
  "v_mov_b32 v78, 64\n"
  "s_mov_b64 exec, s[22:23]\n"
  "global_store_dword v0, v78, s[64:65] sc1\n"
  "QUITH:\n"
  "s_endpgm\n"
        : "+{v[0:15]}"(vin), "+{s[36:43]}"(sa), "+{s[44:59]}"(sb) : "{s[4:5]}"(ask) : CLOB);
    } else { ARGS; PTR(sa, 0, p.ws);
      const int ci = tk - 16, cv = gam * 16 + ci, h = cv >> 1, vt = (cv & 1) * 4 + wave;
      v4u iv = {~0u, ~0u, ~0u, ~0u};
      const v4u snt = iv;
      const int line = (2 * ci + (gam & 1)) * 4 + (gam >> 1) * 2 + (g & 1), kb = line >> 3, cp = ((lane >> 3) & 1) + 2 * (lane >> 5);
      unsigned vote = 0, tag = 0;
      if (wave == 1) {
        const unsigned map = (0x7160u >> (4 * gam)) & 7, hf = ci & 1, bit = (ci >> 1) & 1, first = ((ci >> 2) & 1) ^ 1;
        unsigned m[2] = {~0u, ~0u};
#pragma unroll 1
        for (int r = 0; r < 4; ++r) {
          const unsigned sel = (first ^ r ^ (r >> 1)) & 1, off = probe + (((2 * hf + (sel << (2 * bit)) + ((ci >> 3) << (2 - 2 * bit))) ^ map) << 12);
          unsigned v = 0; unsigned long long t0, t1;
          asm volatile("s_mov_b64 exec, 1\n\ts_memtime %1\n\ts_waitcnt lgkmcnt(0)\n\tglobal_atomic_add %0, %3, %0, %4 sc0 sc1\n\ts_waitcnt vmcnt(0)\n\t"
                       "s_memtime %2\n\ts_waitcnt lgkmcnt(0)\n\ts_mov_b64 exec, -1" : "+v"(v), "=&s"(t0), "=&s"(t1) : "v"(off), "s"(p.ws) : "memory");
          const unsigned t = (unsigned)(t1 - t0);
          if (sel) m[1] = min(m[1], t); else m[0] = min(m[0], t);
        }
        vote = min(min(m[0], m[1]), 4095u) | ((m[1] < m[0]) << 12);
        tag = rfl(ctl_ld(p.ws, B_N));
        if (lane == 0) st8(p.ws, B_S + cv * 8, tag, vote);
        STAMP(63, 13);
      }
      wg_barrier();
      if (wave == 0 && ci < 9) {
        const int j = lane & 7, m = 2 * (line & 3) + (j & 1), col = 32 * (line >> 2) + (j >> 1);
#pragma unroll
        for (int e = 0; e < 4; ++e) {
          const unsigned lo = p.mem0[m * DM + col + 8 * e], hi = p.mem0[m * DM + col + 8 * e + 4];
          iv[e] = ((lo & 0x7fff) > 0x7f81 ? 0x7f81 : lo) | (((hi & 0x7fff) > 0x7f81 ? 0x7f81 : hi) << 16);
        }
      }
      {
        const int row = 8 * wave + (lane >> 3);
#pragma unroll 6
        for (int j = 0; j < 18; ++j) dma(p.Wv + cv * 64 + (size_t)j * 32 * 4096, row * 8192 + ((lane & 7) ^ (2 * (((row >> 2) & 1) | ((row >> 3) & 2)))) * 16, wave * 1024 + j * 4096);
      }
      unsigned cls = 0;
      for (int i = 0;; ++i) {
        const unsigned long long r = ld8(p.ws, ro);
        if (rfl((unsigned)r) == token) { cls = rfl((unsigned)(r >> 32)); break; }
        if (i == SPIN) return;
        if (wave == 1) { const unsigned t = rfl(ctl_ld(p.ws, B_N)); if (t != tag && lane == 0) st8(p.ws, B_S + cv * 8, t, vote); tag = t; }
        __builtin_amdgcn_s_sleep(4);
      }
      if (tid == 0) (void)ctl_add(p.ws, C_ACK + gam * 128, 1u);
      const unsigned cx = (cls & 2) << 12;
      const unsigned ms = chunk(cls, cp, kb < 8 ? (kb >> 2) : 4, (kb < 8 ? (kb & 3) : 0) * 1024 + (line & 7) * 128 + (lane & 7) * 16);
      const unsigned ms2 = ms + (kb < 8 ? 2 * CH : 1024), inb = chunk(cls, gam, 28 + 4 * ci, 0);
      const unsigned m2 = chunk(cls, gam, 16 + ((h & 7) >> 1), (h & 1) * 2048 + vt * 256);
      if (ci < 9) {
        if (wave < 3) { st16(p.ws, ms + wave * 5 * CH, iv); st16(p.ws, ms2 + wave * 5 * CH, iv); }
#pragma unroll
        for (int e = 0; e < 4; ++e) st16(p.ws, inb + e * CH + wave * 1024 + lane * 16, snt);
      }
      if (lane < 48) st16(p.ws, m2 + g * 4 * CH + n * 16, snt);
      STAMP(63, 14);
      vin[1] = m1pat; vin[2] = lane * 4; vin[3] = ms; vin[4] = idx;
      vin[6] = trrow * 128 + ((2 * wave + (c4 >> 1)) ^ (2 * (((trrow >> 2) & 1) | ((trrow >> 3) & 2)))) * 16 + (c4 & 1) * 8;
      vin[7] = line * 128 + (lane & 7) * 16;
#pragma unroll
      for (int i = 0; i < 4; ++i) vin[8 + i] = ((lane & 7) >> 1) == i ? ((lane & 1) ? 0x3f800000u : 0x3f80u) : 0u;
      vin[12] = c4 * 18432 + (16 * g + 4 * wave + qq) * 16; vin[13] = ms2;
      sa[2] = (C_VF ^ cx) + cv * 128; sa[3] = C_REP ^ cx; sb[5] = gam * 128; sb[6] = cv == 15 && wave == 0 ? C_KVP ^ cx : 0; sb[7] = 2200; sa[5] = 512 + (gam & 1) * 128; sa[6] = 2500; sa[7] = 720;
      sb[0] = chunk(cls, gam, 0, 0) + (wave - 1) * 1024;
      sb[1] = wave == 3 ? chunk(cls, gam, 4, 0) : chunk(cls, gam, 1, (wave + 1) * 1024);
      sb[2] = m2; sb[3] = WO_VP + h * 2048 + vt * 256;
      sb[9] = inb; sb[12] = ci < 9 ? 1 + wave : 0; sb[13] = wave == 1 ? 3072 : CH - 1024; sb[14] = WO_DB;
      UNIFORM(sa, 8); UNIFORM(sb, 16);
      asm volatile(
  "s_nop 4\n"
  "s_lshl_b32 s80, s48, 10\n"
  "s_waitcnt vmcnt(0)\n"
  "s_barrier\n"
  "v_add_u32 v60, 0x8000, v6\n"
  "ds_read_b64_tr_b16 v[96:97], v6 offset:0\n"
  "ds_read_b64_tr_b16 v[98:99], v6 offset:1024\n"
  "ds_read_b64_tr_b16 v[100:101], v6 offset:4096\n"
  "ds_read_b64_tr_b16 v[102:103], v6 offset:5120\n"
  "ds_read_b64_tr_b16 v[104:105], v6 offset:8192\n"
  "ds_read_b64_tr_b16 v[106:107], v6 offset:9216\n"
  "ds_read_b64_tr_b16 v[108:109], v6 offset:12288\n"
  "ds_read_b64_tr_b16 v[110:111], v6 offset:13312\n"
  "ds_read_b64_tr_b16 v[112:113], v6 offset:16384\n"
  "ds_read_b64_tr_b16 v[114:115], v6 offset:17408\n"
  "ds_read_b64_tr_b16 v[116:117], v6 offset:20480\n"
  "ds_read_b64_tr_b16 v[118:119], v6 offset:21504\n"
  "ds_read_b64_tr_b16 v[120:121], v6 offset:24576\n"
  "ds_read_b64_tr_b16 v[122:123], v6 offset:25600\n"
  "ds_read_b64_tr_b16 v[124:125], v6 offset:28672\n"
  "ds_read_b64_tr_b16 v[126:127], v6 offset:29696\n"
  "ds_read_b64_tr_b16 v[128:129], v60 offset:0\n"
  "ds_read_b64_tr_b16 v[130:131], v60 offset:1024\n"
  "ds_read_b64_tr_b16 v[132:133], v60 offset:4096\n"
  "ds_read_b64_tr_b16 v[134:135], v60 offset:5120\n"
  "ds_read_b64_tr_b16 v[136:137], v60 offset:8192\n"
  "ds_read_b64_tr_b16 v[138:139], v60 offset:9216\n"
  "ds_read_b64_tr_b16 v[140:141], v60 offset:12288\n"
  "ds_read_b64_tr_b16 v[142:143], v60 offset:13312\n"
  "ds_read_b64_tr_b16 v[144:145], v60 offset:16384\n"
  "ds_read_b64_tr_b16 v[146:147], v60 offset:17408\n"
  "ds_read_b64_tr_b16 v[148:149], v60 offset:20480\n"
  "ds_read_b64_tr_b16 v[150:151], v60 offset:21504\n"
  "ds_read_b64_tr_b16 v[152:153], v60 offset:24576\n"
  "ds_read_b64_tr_b16 v[154:155], v60 offset:25600\n"
  "ds_read_b64_tr_b16 v[156:157], v60 offset:28672\n"
  "ds_read_b64_tr_b16 v[158:159], v60 offset:29696\n"
  "ds_read_b64_tr_b16 v[160:161], v60 offset:32768\n"
  "ds_read_b64_tr_b16 v[162:163], v60 offset:33792\n"
  "ds_read_b64_tr_b16 v[164:165], v60 offset:36864\n"
  "ds_read_b64_tr_b16 v[166:167], v60 offset:37888\n"
  "v_mov_b32 v64, -1\n"
  "v_mov_b32 v65, -1\n"
  "v_mov_b32 v66, -1\n"
  "v_mov_b32 v67, -1\n"
  "s_mov_b32 s7, s41\n"
  "s_mov_b32 s14, s42\n"
  "s_mov_b32 s15, s43\n"
  "s_mov_b32 s6, 0\n"
  "s_mov_b32 s16, s42\n"
  "s_mov_b32 s17, s43\n"
  "s_mov_b32 s40, 0\n"
  "s_mov_b32 s41, 0\n"
  "s_mov_b32 s42, 100000\n"
  "s_mov_b32 s43, 0\n"
  "s_waitcnt lgkmcnt(0)\n"
  "s_barrier\n"
  "s_mul_i32 s74, s48, 18432\n"
  "v_add_u32 v60, s74, v0\n"
  "ds_write_b128 v60, v[96:99] offset:0\n"
  "ds_write_b128 v60, v[100:103] offset:1024\n"
  "ds_write_b128 v60, v[104:107] offset:2048\n"
  "ds_write_b128 v60, v[108:111] offset:3072\n"
  "ds_write_b128 v60, v[112:115] offset:4096\n"
  "ds_write_b128 v60, v[116:119] offset:5120\n"
  "ds_write_b128 v60, v[120:123] offset:6144\n"
  "ds_write_b128 v60, v[124:127] offset:7168\n"
  "ds_write_b128 v60, v[128:131] offset:8192\n"
  "ds_write_b128 v60, v[132:135] offset:9216\n"
  "ds_write_b128 v60, v[136:139] offset:10240\n"
  "ds_write_b128 v60, v[140:143] offset:11264\n"
  "ds_write_b128 v60, v[144:147] offset:12288\n"
  "ds_write_b128 v60, v[148:151] offset:13312\n"
  "ds_write_b128 v60, v[152:155] offset:14336\n"
  "ds_write_b128 v60, v[156:159] offset:15360\n"
  "ds_write_b128 v60, v[160:163] offset:16384\n"
  "ds_write_b128 v60, v[164:167] offset:17408\n"
  "s_waitcnt lgkmcnt(0)\n"
  "s_barrier\n"
  "ds_read_b128 v[96:99], v12 offset:0\n"
  "ds_read_b128 v[100:103], v12 offset:1024\n"
  "ds_read_b128 v[104:107], v12 offset:2048\n"
  "ds_read_b128 v[108:111], v12 offset:3072\n"
  "ds_read_b128 v[112:115], v12 offset:4096\n"
  "ds_read_b128 v[116:119], v12 offset:5120\n"
  "ds_read_b128 v[120:123], v12 offset:6144\n"
  "ds_read_b128 v[124:127], v12 offset:7168\n"
  "ds_read_b128 v[128:131], v12 offset:8192\n"
  "ds_read_b128 v[132:135], v12 offset:9216\n"
  "ds_read_b128 v[136:139], v12 offset:10240\n"
  "ds_read_b128 v[140:143], v12 offset:11264\n"
  "ds_read_b128 v[144:147], v12 offset:12288\n"
  "ds_read_b128 v[148:151], v12 offset:13312\n"
  "ds_read_b128 v[152:155], v12 offset:14336\n"
  "ds_read_b128 v[156:159], v12 offset:15360\n"
  "ds_read_b128 v[160:163], v12 offset:16384\n"
  "ds_read_b128 v[164:167], v12 offset:17408\n"
  "s_waitcnt lgkmcnt(0)\n"
  "s_barrier\n"
  "s_add_u32 s76, s39, s49\n"
  "s_add_u32 s76, s76, 137216\n"
  "s_add_u32 s74, s36, s76\n"
  "s_addc_u32 s75, s37, 0\n"
  "v_mov_b32 v60, 1\n"
  "s_cmp_eq_u32 s48, 0\n"
  "s_cselect_b64 exec, 1, 0\n"
  "global_atomic_add v0, v60, s[74:75] sc1\n"
  "s_mov_b64 exec, -1\n"
  "s_cmp_eq_u32 s50, 0\n"
  "s_cbranch_scc1 RCD\n"
  "s_add_u32 s62, s36, s50\n"
  "s_addc_u32 s63, s37, 0\n"
  "s_add_u32 s74, s39, 137216\n"
  "s_add_u32 s64, s36, s74\n"
  "s_addc_u32 s65, s37, 0\n"
  "v_lshlrev_b32 v56, 3, v0\n"
  "v_and_b32 v57, 0x180, v56\n"
  "RCP:\n"
  "global_load_dword v60, v56, s[62:63] sc1\n"
  "global_load_dword v61, v57, s[64:65] sc1\n"
  "s_waitcnt vmcnt(0)\n"
  "v_lshrrev_b32 v61, 5, v61\n"
  "v_min_u32 v60, v60, v61\n"
  "v_cmp_eq_u32 vcc, 0, v60\n"
  "s_cbranch_vccz RCG\n"
  "s_sub_u32 s42, s42, 1\n"
  "s_cbranch_scc1 QUITV\n"
  "s_branch RCP\n"
  "RCG:\n"
  "s_memrealtime s[74:75]\n"
  "s_waitcnt lgkmcnt(0)\n"
  "s_mul_i32 s76, s74, 24\n"
  "s_add_u32 s76, s76, 1500\n"
  "s_or_b32 s76, s76, 1\n"
  "v_mov_b32 v60, s76\n"
  "s_mov_b64 exec, 15\n"
  "global_store_dword v57, v60, s[64:65] offset:1024 sc1\n"
  "s_mov_b64 exec, -1\n"
  "RCD:\n"
  "s_add_u32 s77, s39, s49\n"
  "s_add_u32 s77, s77, 138240\n"
  "s_add_u32 s74, s36, s77\n"
  "s_addc_u32 s75, s37, 0\n"
  "PLGV:\n"
  "v_mov_b32 v60, 0\n"
  "global_load_dword v60, v60, s[74:75] sc1\n"
  "s_waitcnt vmcnt(0)\n"
  "v_readfirstlane_b32 s76, v60\n"
  "s_cmp_lg_u32 s76, 0\n"
  "s_cbranch_scc1 PDGV\n"
  "s_sub_u32 s42, s42, 1\n"
  "s_cbranch_scc1 QUITV\n"
  "s_sleep 1\n"
  "s_branch PLGV\n"
  "PDGV:\n"
  "s_mov_b32 s49, s76\n"
  "v_mov_b32 v60, 0\n"
  "v_mov_b32 v61, s49\n"
  "ds_write_b32 v60, v61 offset:9232\n"
  "s_sub_u32 s80, s80, 1024\n"
  "RLOOP:\n"
  "s_mov_b32 s52, 0\n"
  "s_mov_b32 s98, s49\n"
  "RTRY:\n"
  "v_mov_b64 v[56:57], 0\n"
  "v_mov_b64 v[58:59], 0\n"
  "s_mul_i32 s74, s41, 131072\n"
  "s_add_u32 s74, s74, s46\n"
  "s_add_u32 s68, s36, s74\n"
  "s_addc_u32 s69, s37, 0\n"
  "v_mov_b32 v61, 0\n"
  "s_cmp_eq_u32 s48, 0\n"
  "s_cbranch_scc1 NDV\n"
  "s_mul_i32 s78, s41, 163840\n"
  "s_lshl_b32 s79, s52, 16\n"
  "s_add_u32 s79, s79, s78\n"
  "s_add_u32 s79, s79, s44\n"
  "s_add_u32 s62, s36, s79\n"
  "s_addc_u32 s63, s37, 0\n"
  "s_add_u32 s64, s62, s57\n"
  "s_addc_u32 s65, s63, 0\n"
  "s_add_u32 s79, s78, s45\n"
  "s_cmp_eq_u32 s48, 3\n"
  "s_cselect_b32 s74, 10, 16\n"
  "s_lshl_b32 s74, s52, s74\n"
  "s_add_u32 s79, s79, s74\n"
  "s_add_u32 s66, s36, s79\n"
  "s_addc_u32 s67, s37, 0\n"
  "s_add_u32 m0, s80, 0\n"
  "s_add_u32 s79, s80, 3072\n"
  "s_add_u32 s78, s80, 6144\n"
  "s_add_u32 s81, s49, 0\n"
  "WV:\n"
  "s_memrealtime s[74:75]\n"
  "s_waitcnt lgkmcnt(0)\n"
  "s_mul_i32 s74, s74, 24\n"
  "s_sub_i32 s74, s81, s74\n"
  "s_cmp_lt_i32 s74, 750\n"
  "s_cbranch_scc1 WFV\n"
  "s_cmp_gt_i32 s74, 0x40000\n"
  "s_cbranch_scc1 WEV\n"
  "s_sleep 2\n"
  "s_branch WV\n"
  "WFV:\n"
  "s_cmp_lt_i32 s74, 100\n"
  "s_cbranch_scc1 WEV\n"
  "WGV:\n"
  "s_memtime s[76:77]\n"
  "s_memrealtime s[20:21]\n"
  "s_nop 2\n"
  "s_memrealtime s[22:23]\n"
  "s_nop 2\n"
  "s_memrealtime s[24:25]\n"
  "s_nop 2\n"
  "s_memrealtime s[26:27]\n"
  "s_nop 2\n"
  "s_memrealtime s[28:29]\n"
  "s_nop 2\n"
  "s_memrealtime s[30:31]\n"
  "s_waitcnt lgkmcnt(0)\n"
  "s_add_u32 s20, s20, s22\n"
  "s_add_u32 s24, s24, s26\n"
  "s_add_u32 s28, s28, s30\n"
  "s_add_u32 s20, s20, s24\n"
  "s_add_u32 s20, s20, s28\n"
  "s_lshl_b32 s74, s20, 2\n"
  "s_nop 0\n"
  "s_sub_i32 s74, s81, s74\n"
  "s_sub_i32 s74, s74, 238\n"
  "s_max_i32 s74, s74, 0\n"
  "s_cmp_ge_i32 s74, 512\n"
  "s_cbranch_scc1 WV\n"
  "s_lshr_b32 s74, s74, 3\n"
  "s_lshl_b32 s74, s74, 2\n"
  "s_sub_i32 s74, 268, s74\n"
  "s_getpc_b64 s[76:77]\n"
  "s_add_u32 s76, s76, s74\n"
  "s_addc_u32 s77, s77, 0\n"
  "s_setpc_b64 s[76:77]\n"
  "s_nop 1\n"
  "s_nop 1\n"
  "s_nop 1\n"
  "s_nop 1\n"
  "s_nop 1\n"
  "s_nop 1\n"
  "s_nop 1\n"
  "s_nop 1\n"
  "s_nop 1\n"
  "s_nop 1\n"
  "s_nop 1\n"
  "s_nop 1\n"
  "s_nop 1\n"
  "s_nop 1\n"
  "s_nop 1\n"
  "s_nop 1\n"
  "s_nop 1\n"
  "s_nop 1\n"
  "s_nop 1\n"
  "s_nop 1\n"
  "s_nop 1\n"
  "s_nop 1\n"
  "s_nop 1\n"
  "s_nop 1\n"
  "s_nop 1\n"
  "s_nop 1\n"
  "s_nop 1\n"
  "s_nop 1\n"
  "s_nop 1\n"
  "s_nop 1\n"
  "s_nop 1\n"
  "s_nop 1\n"
  "s_nop 1\n"
  "s_nop 1\n"
  "s_nop 1\n"
  "s_nop 1\n"
  "s_nop 1\n"
  "s_nop 1\n"
  "s_nop 1\n"
  "s_nop 1\n"
  "s_nop 1\n"
  "s_nop 1\n"
  "s_nop 1\n"
  "s_nop 1\n"
  "s_nop 1\n"
  "s_nop 1\n"
  "s_nop 1\n"
  "s_nop 1\n"
  "s_nop 1\n"
  "s_nop 1\n"
  "s_nop 1\n"
  "s_nop 1\n"
  "s_nop 1\n"
  "s_nop 1\n"
  "s_nop 1\n"
  "s_nop 1\n"
  "s_nop 1\n"
  "s_nop 1\n"
  "s_nop 1\n"
  "s_nop 1\n"
  "s_nop 1\n"
  "s_nop 1\n"
  "s_nop 1\n"
  "s_nop 1\n"
  "WEV:\n"
  "global_load_lds_dwordx4 v1, s[62:63] sc1\n"
  "s_mov_b32 m0, s79\n"
  "s_nop 0\n"
  "global_load_lds_dwordx4 v1, s[64:65] sc1\n"
  "s_mov_b32 m0, s78\n"
  "s_nop 0\n"
  "global_load_lds_dwordx4 v1, s[66:67] sc1\n"
  "s_waitcnt vmcnt(2)\n"
  "NDV:\n"
  "s_waitcnt lgkmcnt(0)\n"
  "s_barrier\n"
  "ds_read_b32 v60, v61 offset:9232\n"
  "ds_read_b128 v[20:23], v0 offset:0\n"
  "ds_read_b128 v[24:27], v0 offset:1024\n"
  "ds_read_b128 v[28:31], v0 offset:2048\n"
  "s_waitcnt lgkmcnt(3)\n"
  "v_readfirstlane_b32 s74, v60\n"
  "s_sub_i32 s75, s74, s49\n"
  "s_cmp_gt_i32 s75, 0\n"
  "s_cbranch_scc1 RLATE\n"
  "s_cmp_eq_u32 s48, 0\n"
  "s_cbranch_scc1 RNW\n"
  "s_waitcnt vmcnt(0)\n"
  "RNW:\n"
  "s_barrier\n"
  "ds_read_b128 v[32:35], v0 offset:3072\n"
  "ds_read_b128 v[36:39], v0 offset:4096\n"
  "s_waitcnt lgkmcnt(4)\n"
  "v_smfmac_f32_16x16x64_bf16 v[56:59], v[20:23], v[96:103], v4\n"
  "ds_read_b128 v[40:43], v0 offset:5120\n"
  "s_waitcnt lgkmcnt(4)\n"
  "v_smfmac_f32_16x16x64_bf16 v[56:59], v[24:27], v[104:111], v4\n"
  "ds_read_b128 v[44:47], v0 offset:6144\n"
  "s_waitcnt lgkmcnt(4)\n"
  "v_smfmac_f32_16x16x64_bf16 v[56:59], v[28:31], v[112:119], v4\n"
  "ds_read_b128 v[48:51], v0 offset:7168\n"
  "s_waitcnt lgkmcnt(4)\n"
  "v_smfmac_f32_16x16x64_bf16 v[56:59], v[32:35], v[120:127], v4\n"
  "ds_read_b128 v[52:55], v0 offset:8192\n"
  "s_waitcnt lgkmcnt(4)\n"
  "v_smfmac_f32_16x16x64_bf16 v[56:59], v[36:39], v[128:135], v4\n"
  "s_waitcnt lgkmcnt(3)\n"
  "v_smfmac_f32_16x16x64_bf16 v[56:59], v[40:43], v[136:143], v4\n"
  "s_waitcnt lgkmcnt(2)\n"
  "v_smfmac_f32_16x16x64_bf16 v[56:59], v[44:47], v[144:151], v4\n"
  "s_waitcnt lgkmcnt(1)\n"
  "v_smfmac_f32_16x16x64_bf16 v[56:59], v[48:51], v[152:159], v4\n"
  "s_waitcnt lgkmcnt(0)\n"
  "v_smfmac_f32_16x16x64_bf16 v[56:59], v[52:55], v[160:167], v4\n"
  "s_nop 3\n"
  "v_mov_b32 v7, v7\n"
  "s_nop 2\n"
  "v_add_f32 v56, v56, v58\n"
  "v_add_f32 v57, v57, v59\n"
  "v_cvt_pk_bf16_f32 v58, v56, v57\n"
  "global_store_dword v2, v58, s[68:69] sc1\n"
  "v_cmp_u_f32 vcc, v56, v57\n"
  "s_cbranch_vccnz RBADM\n"
  "ROKM:\n"
  "v_readlane_b32 s8, v14, 0\n"
  "v_readlane_b32 s9, v14, 8\n"
  "v_readlane_b32 s10, v14, 16\n"
  "v_readlane_b32 s11, v14, 24\n"
  "v_readlane_b32 s12, v14, 32\n"
  "v_readlane_b32 s13, v14, 40\n"
  "s_max_u32 s8, s8, s9\n"
  "s_max_u32 s10, s10, s11\n"
  "s_max_u32 s8, s8, s10\n"
  "s_max_u32 s12, s12, s13\n"
  "s_cmp_eq_u32 s8, 0\n"
  "s_cselect_b32 s8, s14, s8\n"
  "s_sub_i32 s74, s8, s16\n"
  "s_min_i32 s74, s74, 24\n"
  "s_max_i32 s74, s74, -6\n"
  "s_add_u32 s16, s16, s74\n"
  "s_min_u32 s74, s8, s14\n"
  "s_max_u32 s16, s16, s74\n"
  "s_max_u32 s74, s8, s14\n"
  "s_add_u32 s74, s74, 40\n"
  "s_min_u32 s16, s16, s74\n"
  "s_mov_b32 s14, s8\n"
  "s_cmp_eq_u32 s12, 0\n"
  "s_cselect_b32 s12, s15, s12\n"
  "s_sub_i32 s74, s12, s17\n"
  "s_min_i32 s74, s74, 24\n"
  "s_max_i32 s74, s74, -6\n"
  "s_add_u32 s17, s17, s74\n"
  "s_min_u32 s74, s12, s15\n"
  "s_max_u32 s17, s17, s74\n"
  "s_max_u32 s74, s12, s15\n"
  "s_add_u32 s74, s74, 40\n"
  "s_min_u32 s17, s17, s74\n"
  "s_mov_b32 s15, s12\n"
  "s_min_u32 s74, s40, 31\n"
  "s_lshr_b32 s74, 700, s74\n"
  "s_add_u32 s55, s16, s74\n"
  "s_add_u32 s55, s55, 130\n"
  "s_add_u32 s50, s55, s17\n"
  "s_add_u32 s50, s50, s74\n"
  "s_add_u32 s50, s50, 230\n"
  "s_add_u32 s74, s36, s47\n"
  "s_addc_u32 s75, s37, 0\n"
  "global_store_dword v2, v58, s[74:75] sc1\n"
  "s_add_u32 s47, s47, 65536\n"
  "s_cmp_eq_u32 s40, 63\n"
  "s_cbranch_scc1 ENDV\n"
  "s_add_u32 s75, s40, 3\n"
  "s_and_b32 s74, s75, 0x78\n"
  "s_add_u32 s74, s74, s75\n"
  "s_lshl_b32 s74, s74, 10\n"
  "s_add_u32 s74, s74, s39\n"
  "s_add_u32 s74, s74, s7\n"
  "s_add_u32 s18, s36, s74\n"
  "s_addc_u32 s19, s37, 0\n"
  "v_mov_b32 v61, s6\n"
  "s_cmp_lg_u32 s6, 0\n"
  "s_cselect_b64 exec, 1, 0\n"
  "global_atomic_umax v0, v61, s[18:19] sc1\n"
  "s_mov_b64 exec, -1\n"
  "s_add_u32 s75, s40, 1\n"
  "s_and_b32 s74, s75, 0x78\n"
  "s_add_u32 s74, s74, s75\n"
  "s_lshl_b32 s74, s74, 10\n"
  "s_add_u32 s74, s74, s39\n"
  "s_add_u32 s18, s36, s74\n"
  "s_addc_u32 s19, s37, 0\n"
  "s_mov_b32 exec_lo, 0x01010101\n"
  "s_mov_b32 exec_hi, 0x0101\n"
  "global_load_dword v14, v0, s[18:19] sc1\n"
  "s_mov_b64 exec, -1\n"
  "s_add_u32 s78, s41, 2\n"
  "s_cmp_ge_u32 s78, 3\n"
  "s_cselect_b32 s75, 3, 0\n"
  "s_sub_u32 s78, s78, s75\n"
  "s_mul_i32 s74, s78, 131072\n"
  "s_add_u32 s76, s74, s46\n"
  "s_add_u32 s74, s36, s76\n"
  "s_addc_u32 s75, s37, 0\n"
  "global_store_dword v2, v64, s[74:75] sc1\n"
  "s_add_u32 s40, s40, 1\n"
  "s_add_u32 s81, s49, s55\n"
  "s_add_u32 s49, s49, s50\n"
  "s_add_u32 s41, s41, 1\n"
  "s_cmp_eq_u32 s41, 3\n"
  "s_cselect_b32 s41, 0, s41\n"
  "s_cmp_eq_u32 s56, 1\n"
  "s_cbranch_scc1 RUNIT\n"
  "s_cmp_eq_u32 s56, 0\n"
  "s_cbranch_scc1 RNOA\n"
  "s_and_b32 s74, s40, 1\n"
  "s_lshl_b32 s74, s74, 1\n"
  "s_add_u32 s74, s74, s48\n"
  "s_sub_u32 s74, s74, 1\n"
  "s_lshl_b32 s74, s74, 15\n"
  "s_add_u32 s74, s74, s53\n"
  "s_add_u32 s70, s36, s74\n"
  "s_addc_u32 s71, s37, 0\n"
  "s_mul_i32 s78, s78, 163840\n"
  "s_add_u32 s72, s36, s78\n"
  "s_addc_u32 s73, s37, 0\n"
  "s_cmp_eq_u32 s48, 3\n"
  "s_cbranch_scc1 RAM\n"
  "global_store_dwordx4 v0, v[64:67], s[70:71] offset:0 sc1\n"
  "global_store_dwordx4 v0, v[64:67], s[70:71] offset:1024 sc1\n"
  "global_store_dwordx4 v0, v[64:67], s[70:71] offset:2048 sc1\n"
  "global_store_dwordx4 v0, v[64:67], s[70:71] offset:3072 sc1\n"
  "s_branch RNOA\n"
  "RAM:\n"
  "global_store_dwordx4 v3, v[64:67], s[72:73] sc1\n"
  "global_store_dwordx4 v13, v[64:67], s[72:73] sc1\n"
  "RNOA:\n"
  "s_waitcnt vmcnt(0)\n"
  "s_barrier\n"
  "s_add_u32 s74, s36, s38\n"
  "s_addc_u32 s75, s37, 0\n"
  "v_mov_b32 v61, s40\n"
  "s_cmp_eq_u32 s48, 1\n"
  "s_cselect_b64 exec, 1, 0\n"
  "global_store_dword v0, v61, s[74:75] sc1\n"
  "s_mov_b64 exec, -1\n"
  "s_branch RTAIL\n"
  "RUNIT:\n"
  "s_mul_i32 s78, s41, 163840\n"
  "s_add_u32 s72, s36, s78\n"
  "s_addc_u32 s73, s37, 0\n"
  "s_and_b32 s74, s40, 1\n"
  "s_xor_b32 s74, s74, 1\n"
  "s_lshl_b32 s74, s74, 16\n"
  "s_add_u32 s74, s74, s53\n"
  "s_add_u32 s70, s36, s74\n"
  "s_addc_u32 s71, s37, 0\n"
  "s_add_u32 s66, s70, 32768\n"
  "s_addc_u32 s67, s71, 0\n"
  "s_branch WGR3\n"
  "WR3:\n"
  "s_memrealtime s[74:75]\n"
  "s_waitcnt lgkmcnt(0)\n"
  "s_mul_i32 s74, s74, 24\n"
  "s_sub_i32 s74, s81, s74\n"
  "s_cmp_lt_i32 s74, 750\n"
  "s_cbranch_scc1 WFR3\n"
  "s_cmp_gt_i32 s74, 0x40000\n"
  "s_cbranch_scc1 WER3\n"
  "s_sleep 2\n"
  "s_branch WR3\n"
  "WFR3:\n"
  "s_cmp_lt_i32 s74, 100\n"
  "s_cbranch_scc1 WER3\n"
  "WGR3:\n"
  "s_memtime s[76:77]\n"
  "s_memrealtime s[20:21]\n"
  "s_nop 2\n"
  "s_memrealtime s[22:23]\n"
  "s_nop 2\n"
  "s_memrealtime s[24:25]\n"
  "s_nop 2\n"
  "s_memrealtime s[26:27]\n"
  "s_nop 2\n"
  "s_memrealtime s[28:29]\n"
  "s_nop 2\n"
  "s_memrealtime s[30:31]\n"
  "s_waitcnt lgkmcnt(0)\n"
  "s_add_u32 s20, s20, s22\n"
  "s_add_u32 s24, s24, s26\n"
  "s_add_u32 s28, s28, s30\n"
  "s_add_u32 s20, s20, s24\n"
  "s_add_u32 s20, s20, s28\n"
  "s_lshl_b32 s74, s20, 2\n"
  "s_sub_u32 s64, s74, s76\n"
  "s_sub_i32 s74, s81, s74\n"
  "s_sub_i32 s74, s74, 238\n"
  "s_max_i32 s74, s74, 0\n"
  "s_cmp_ge_i32 s74, 512\n"
  "s_cbranch_scc1 WR3\n"
  "s_lshr_b32 s74, s74, 3\n"
  "s_lshl_b32 s74, s74, 2\n"
  "s_sub_i32 s74, 268, s74\n"
  "s_getpc_b64 s[76:77]\n"
  "s_add_u32 s76, s76, s74\n"
  "s_addc_u32 s77, s77, 0\n"
  "s_setpc_b64 s[76:77]\n"
  "s_nop 1\n"
  "s_nop 1\n"
  "s_nop 1\n"
  "s_nop 1\n"
  "s_nop 1\n"
  "s_nop 1\n"
  "s_nop 1\n"
  "s_nop 1\n"
  "s_nop 1\n"
  "s_nop 1\n"
  "s_nop 1\n"
  "s_nop 1\n"
  "s_nop 1\n"
  "s_nop 1\n"
  "s_nop 1\n"
  "s_nop 1\n"
  "s_nop 1\n"
  "s_nop 1\n"
  "s_nop 1\n"
  "s_nop 1\n"
  "s_nop 1\n"
  "s_nop 1\n"
  "s_nop 1\n"
  "s_nop 1\n"
  "s_nop 1\n"
  "s_nop 1\n"
  "s_nop 1\n"
  "s_nop 1\n"
  "s_nop 1\n"
  "s_nop 1\n"
  "s_nop 1\n"
  "s_nop 1\n"
  "s_nop 1\n"
  "s_nop 1\n"
  "s_nop 1\n"
  "s_nop 1\n"
  "s_nop 1\n"
  "s_nop 1\n"
  "s_nop 1\n"
  "s_nop 1\n"
  "s_nop 1\n"
  "s_nop 1\n"
  "s_nop 1\n"
  "s_nop 1\n"
  "s_nop 1\n"
  "s_nop 1\n"
  "s_nop 1\n"
  "s_nop 1\n"
  "s_nop 1\n"
  "s_nop 1\n"
  "s_nop 1\n"
  "s_nop 1\n"
  "s_nop 1\n"
  "s_nop 1\n"
  "s_nop 1\n"
  "s_nop 1\n"
  "s_nop 1\n"
  "s_nop 1\n"
  "s_nop 1\n"
  "s_nop 1\n"
  "s_nop 1\n"
  "s_nop 1\n"
  "s_nop 1\n"
  "s_nop 1\n"
  "WER3:\n"
  "s_memtime s[76:77]\n"
  "global_load_dwordx4 v[168:171], v0, s[70:71] offset:0 sc1\n"
  "global_load_dwordx4 v[172:175], v0, s[70:71] offset:1024 sc1\n"
  "global_load_dwordx4 v[176:179], v0, s[70:71] offset:2048 sc1\n"
  "global_load_dwordx4 v[180:183], v0, s[70:71] offset:3072 sc1\n"
  "global_load_dwordx4 v[184:187], v0, s[66:67] offset:0 sc1\n"
  "global_load_dwordx4 v[188:191], v0, s[66:67] offset:1024 sc1\n"
  "global_load_dwordx4 v[192:195], v0, s[66:67] offset:2048 sc1\n"
  "global_load_dwordx4 v[196:199], v0, s[66:67] offset:3072 sc1\n"
  "s_waitcnt lgkmcnt(0)\n"
  "s_add_u32 s64, s64, s76\n"
  "s_sub_i32 s64, s49, s64\n"
  "s_sub_i32 s64, s64, 230\n"
  "s_waitcnt vmcnt(8)\n"
  "s_barrier\n"
  "s_waitcnt vmcnt(7)\n"
  "v_mfma_f32_16x16x32_bf16 v[84:87], v[168:171], v[8:11], 0\n"
  "s_waitcnt vmcnt(6)\n"
  "v_mfma_f32_16x16x32_bf16 v[84:87], v[172:175], v[8:11], v[84:87]\n"
  "s_waitcnt vmcnt(5)\n"
  "v_mfma_f32_16x16x32_bf16 v[84:87], v[176:179], v[8:11], v[84:87]\n"
  "s_waitcnt vmcnt(4)\n"
  "v_mfma_f32_16x16x32_bf16 v[84:87], v[180:183], v[8:11], v[84:87]\n"
  "s_waitcnt vmcnt(3)\n"
  "v_mfma_f32_16x16x32_bf16 v[84:87], v[184:187], v[8:11], v[84:87]\n"
  "s_memtime s[74:75]\n"
  "s_waitcnt vmcnt(2)\n"
  "v_mfma_f32_16x16x32_bf16 v[84:87], v[188:191], v[8:11], v[84:87]\n"
  "s_waitcnt vmcnt(1)\n"
  "v_mfma_f32_16x16x32_bf16 v[84:87], v[192:195], v[8:11], v[84:87]\n"
  "s_waitcnt vmcnt(0)\n"
  "v_mfma_f32_16x16x32_bf16 v[84:87], v[196:199], v[8:11], v[84:87]\n"
  "s_waitcnt lgkmcnt(0)\n"
  "s_sub_i32 s74, s74, s76\n"
  "s_cmp_gt_i32 s74, s64\n"
  "s_cbranch_scc1 RSLOW\n"
  "s_nop 3\n"
  "v_cvt_pk_bf16_f32 v88, v84, v85\n"
  "v_cvt_pk_bf16_f32 v89, v86, v87\n"
  "v_cvt_pk_bf16_f32 v90, v84, v85\n"
  "v_cvt_pk_bf16_f32 v91, v86, v87\n"
  "s_nop 0\n"
  "v_permlane32_swap_b32 v88, v90\n"
  "v_permlane32_swap_b32 v89, v91\n"
  "global_store_dwordx4 v3, v[88:91], s[72:73] sc1\n"
  "s_memtime s[74:75]\n"
  "global_store_dwordx4 v13, v[88:91], s[72:73] sc1\n"
  "v_add_f32 v60, v84, v85\n"
  "v_add_f32 v61, v86, v87\n"
  "v_cmp_u_f32 vcc, v60, v61\n"
  "s_cbranch_vccnz RAGAIN\n"
  "s_waitcnt lgkmcnt(0)\n"
  "s_sub_u32 s6, s74, s76\n"
  "RDONE:\n"
  "RTAIL:\n"
  "s_branch RLOOP\n"
  "RBADM:\n"
  "s_waitcnt lgkmcnt(0)\n"
  "s_nop 3\n"
  "v_mov_b32 v7, v7\n"
  "s_nop 2\n"
  "v_mov_b32 v7, v7\n"
  "s_nop 2\n"
  "s_mov_b32 s74, 0xff820000\n"
  "v_max3_u32 v60, v20, v21, v22\n"
  "v_max3_u32 v60, v60, v23, v24\n"
  "v_max3_u32 v60, v60, v25, v26\n"
  "v_max3_u32 v60, v60, v27, v28\n"
  "v_max3_u32 v60, v60, v29, v30\n"
  "v_max3_u32 v60, v60, v31, v32\n"
  "v_max3_u32 v60, v60, v33, v34\n"
  "v_max3_u32 v60, v60, v35, v36\n"
  "v_max3_u32 v60, v60, v37, v38\n"
  "v_max3_u32 v60, v60, v39, v40\n"
  "v_max3_u32 v60, v60, v41, v42\n"
  "v_max3_u32 v60, v60, v43, v44\n"
  "v_max3_u32 v60, v60, v45, v46\n"
  "v_max3_u32 v60, v60, v47, v48\n"
  "v_max3_u32 v60, v60, v49, v50\n"
  "v_max3_u32 v60, v60, v51, v52\n"
  "v_max3_u32 v60, v60, v53, v54\n"
  "v_max_u32 v60, v60, v55\n"
  "v_cmp_le_u32 vcc, s74, v60\n"
  "s_cbranch_vccnz REV\n"
  "s_mov_b32 s74, 0x7fff7fff\n"
  "v_and_b32 v60, s74, v20\n"
  "v_and_b32 v61, s74, v21\n"
  "v_pk_max_u16 v60, v60, v61\n"
  "v_and_b32 v61, s74, v22\n"
  "v_pk_max_u16 v60, v60, v61\n"
  "v_and_b32 v61, s74, v23\n"
  "v_pk_max_u16 v60, v60, v61\n"
  "v_and_b32 v61, s74, v24\n"
  "v_pk_max_u16 v60, v60, v61\n"
  "v_and_b32 v61, s74, v25\n"
  "v_pk_max_u16 v60, v60, v61\n"
  "v_and_b32 v61, s74, v26\n"
  "v_pk_max_u16 v60, v60, v61\n"
  "v_and_b32 v61, s74, v27\n"
  "v_pk_max_u16 v60, v60, v61\n"
  "v_and_b32 v61, s74, v28\n"
  "v_pk_max_u16 v60, v60, v61\n"
  "v_and_b32 v61, s74, v29\n"
  "v_pk_max_u16 v60, v60, v61\n"
  "v_and_b32 v61, s74, v30\n"
  "v_pk_max_u16 v60, v60, v61\n"
  "v_and_b32 v61, s74, v31\n"
  "v_pk_max_u16 v60, v60, v61\n"
  "v_and_b32 v61, s74, v32\n"
  "v_pk_max_u16 v60, v60, v61\n"
  "v_and_b32 v61, s74, v33\n"
  "v_pk_max_u16 v60, v60, v61\n"
  "v_and_b32 v61, s74, v34\n"
  "v_pk_max_u16 v60, v60, v61\n"
  "v_and_b32 v61, s74, v35\n"
  "v_pk_max_u16 v60, v60, v61\n"
  "v_and_b32 v61, s74, v36\n"
  "v_pk_max_u16 v60, v60, v61\n"
  "v_and_b32 v61, s74, v37\n"
  "v_pk_max_u16 v60, v60, v61\n"
  "v_and_b32 v61, s74, v38\n"
  "v_pk_max_u16 v60, v60, v61\n"
  "v_and_b32 v61, s74, v39\n"
  "v_pk_max_u16 v60, v60, v61\n"
  "v_and_b32 v61, s74, v40\n"
  "v_pk_max_u16 v60, v60, v61\n"
  "v_and_b32 v61, s74, v41\n"
  "v_pk_max_u16 v60, v60, v61\n"
  "v_and_b32 v61, s74, v42\n"
  "v_pk_max_u16 v60, v60, v61\n"
  "v_and_b32 v61, s74, v43\n"
  "v_pk_max_u16 v60, v60, v61\n"
  "v_and_b32 v61, s74, v44\n"
  "v_pk_max_u16 v60, v60, v61\n"
  "v_and_b32 v61, s74, v45\n"
  "v_pk_max_u16 v60, v60, v61\n"
  "v_and_b32 v61, s74, v46\n"
  "v_pk_max_u16 v60, v60, v61\n"
  "v_and_b32 v61, s74, v47\n"
  "v_pk_max_u16 v60, v60, v61\n"
  "v_and_b32 v61, s74, v48\n"
  "v_pk_max_u16 v60, v60, v61\n"
  "v_and_b32 v61, s74, v49\n"
  "v_pk_max_u16 v60, v60, v61\n"
  "v_and_b32 v61, s74, v50\n"
  "v_pk_max_u16 v60, v60, v61\n"
  "v_and_b32 v61, s74, v51\n"
  "v_pk_max_u16 v60, v60, v61\n"
  "v_and_b32 v61, s74, v52\n"
  "v_pk_max_u16 v60, v60, v61\n"
  "v_and_b32 v61, s74, v53\n"
  "v_pk_max_u16 v60, v60, v61\n"
  "v_and_b32 v61, s74, v54\n"
  "v_pk_max_u16 v60, v60, v61\n"
  "v_and_b32 v61, s74, v55\n"
  "v_pk_max_u16 v60, v60, v61\n"
  "v_lshrrev_b32 v61, 16, v60\n"
  "v_and_b32 v60, 0xffff, v60\n"
  "v_max_u32 v60, v60, v61\n"
  "v_cmp_lt_u32 vcc, 0x7f81, v60\n"
  "s_cbranch_vccnz REV\n"
  "s_mov_b32 s74, 0x7fff7fff\n"
  "s_mov_b32 s75, 0x7f817f81\n"
  "v_and_b32 v60, s74, v58\n"
  "v_pk_min_u16 v60, v60, s75\n"
  "v_bfi_b32 v58, s74, v60, v58\n"
  "s_waitcnt vmcnt(0)\n"
  "global_store_dword v2, v58, s[68:69] sc1\n"
  "s_branch ROKM\n"
  "REV:\n"
  "s_sub_u32 s42, s42, 1\n"
  "s_cbranch_scc1 QUITV\n"
  "s_add_u32 s43, s43, 1\n"
  "s_memrealtime s[74:75]\n"
  "s_waitcnt lgkmcnt(0)\n"
  "s_mul_i32 s78, s74, 24\n"
  "s_add_u32 s78, s78, 650\n"
  "RSV:\n"
  "s_add_u32 s49, s49, s51\n"
  "s_sub_i32 s74, s49, s78\n"
  "s_cmp_lt_i32 s74, 0\n"
  "s_cbranch_scc1 RSV\n"
  "AGV:\n"
  "v_mov_b32 v60, s49\n"
  "v_mov_b32 v61, s48\n"
  "v_lshlrev_b32 v61, 2, v61\n"
  "ds_write_b32 v61, v60 offset:9216\n"
  "s_waitcnt lgkmcnt(0)\n"
  "s_barrier\n"
  "v_mov_b32 v61, 0\n"
  "ds_read_b128 v[20:23], v61 offset:9216\n"
  "s_waitcnt lgkmcnt(0)\n"
  "v_readfirstlane_b32 s74, v20\n"
  "s_sub_i32 s75, s74, s49\n"
  "s_cmp_gt_i32 s75, 0\n"
  "s_cselect_b32 s49, s74, s49\n"
  "v_readfirstlane_b32 s74, v21\n"
  "s_sub_i32 s75, s74, s49\n"
  "s_cmp_gt_i32 s75, 0\n"
  "s_cselect_b32 s49, s74, s49\n"
  "v_readfirstlane_b32 s74, v22\n"
  "s_sub_i32 s75, s74, s49\n"
  "s_cmp_gt_i32 s75, 0\n"
  "s_cselect_b32 s49, s74, s49\n"
  "v_readfirstlane_b32 s74, v23\n"
  "s_sub_i32 s75, s74, s49\n"
  "s_cmp_gt_i32 s75, 0\n"
  "s_cselect_b32 s49, s74, s49\n"
  "s_cmp_lg_u32 s49, s98\n"
  "s_cselect_b32 s52, 1, 0\n"
  "s_branch RTRY\n"
  "RLATE:\n"
  "s_waitcnt lgkmcnt(0)\n"
  "s_mov_b32 s49, s74\n"
  "s_mov_b32 s52, 1\n"
  "s_add_u32 s43, s43, 1\n"
  "s_barrier\n"
  "s_branch RTRY\n"
  "RSLOW:\n"
  "s_waitcnt lgkmcnt(0)\n"
  "s_nop 3\n"
  "v_mov_b32 v7, v7\n"
  "s_nop 2\n"
  "v_mov_b32 v7, v7\n"
  "s_nop 2\n"
  "s_add_u32 s6, s74, 110\n"
  "s_cmp_gt_i32 s64, 0\n"
  "s_cselect_b32 s6, s6, 0\n"
  "v_add_f32 v60, v84, v85\n"
  "v_add_f32 v61, v86, v87\n"
  "v_cmp_u_f32 vcc, v60, v61\n"
  "s_cbranch_vccz RFULL\n"
  "RAGAIN:\n"
  "s_waitcnt lgkmcnt(0)\n"
  "s_nop 3\n"
  "v_mov_b32 v7, v7\n"
  "s_nop 2\n"
  "v_mov_b32 v7, v7\n"
  "s_nop 2\n"
  "s_mov_b32 s6, 0\n"
  "RAG2:\n"
  "s_sub_u32 s42, s42, 1\n"
  "s_cbranch_scc1 QUITV\n"
  "s_add_u32 s43, s43, 0x10000\n"
  "global_load_dwordx4 v[168:171], v0, s[70:71] offset:0 sc1\n"
  "global_load_dwordx4 v[172:175], v0, s[70:71] offset:1024 sc1\n"
  "global_load_dwordx4 v[176:179], v0, s[70:71] offset:2048 sc1\n"
  "global_load_dwordx4 v[180:183], v0, s[70:71] offset:3072 sc1\n"
  "global_load_dwordx4 v[184:187], v0, s[66:67] offset:0 sc1\n"
  "global_load_dwordx4 v[188:191], v0, s[66:67] offset:1024 sc1\n"
  "global_load_dwordx4 v[192:195], v0, s[66:67] offset:2048 sc1\n"
  "global_load_dwordx4 v[196:199], v0, s[66:67] offset:3072 sc1\n"
  "s_waitcnt vmcnt(7)\n"
  "v_mfma_f32_16x16x32_bf16 v[84:87], v[168:171], v[8:11], 0\n"
  "s_waitcnt vmcnt(6)\n"
  "v_mfma_f32_16x16x32_bf16 v[84:87], v[172:175], v[8:11], v[84:87]\n"
  "s_waitcnt vmcnt(5)\n"
  "v_mfma_f32_16x16x32_bf16 v[84:87], v[176:179], v[8:11], v[84:87]\n"
  "s_waitcnt vmcnt(4)\n"
  "v_mfma_f32_16x16x32_bf16 v[84:87], v[180:183], v[8:11], v[84:87]\n"
  "s_waitcnt vmcnt(3)\n"
  "v_mfma_f32_16x16x32_bf16 v[84:87], v[184:187], v[8:11], v[84:87]\n"
  "s_waitcnt vmcnt(2)\n"
  "v_mfma_f32_16x16x32_bf16 v[84:87], v[188:191], v[8:11], v[84:87]\n"
  "s_waitcnt vmcnt(1)\n"
  "v_mfma_f32_16x16x32_bf16 v[84:87], v[192:195], v[8:11], v[84:87]\n"
  "s_waitcnt vmcnt(0)\n"
  "v_mfma_f32_16x16x32_bf16 v[84:87], v[196:199], v[8:11], v[84:87]\n"
  "s_nop 3\n"
  "v_mov_b32 v7, v7\n"
  "s_nop 2\n"
  "v_add_f32 v60, v84, v85\n"
  "v_add_f32 v61, v86, v87\n"
  "v_cmp_u_f32 vcc, v60, v61\n"
  "s_cbranch_vccz RFULL\n"
  "s_mov_b32 s74, 0xff820000\n"
  "v_max3_u32 v60, v168, v169, v170\n"
  "v_max3_u32 v60, v60, v171, v172\n"
  "v_max3_u32 v60, v60, v173, v174\n"
  "v_max3_u32 v60, v60, v175, v176\n"
  "v_max3_u32 v60, v60, v177, v178\n"
  "v_max3_u32 v60, v60, v179, v180\n"
  "v_max3_u32 v60, v60, v181, v182\n"
  "v_max3_u32 v60, v60, v183, v184\n"
  "v_max3_u32 v60, v60, v185, v186\n"
  "v_max3_u32 v60, v60, v187, v188\n"
  "v_max3_u32 v60, v60, v189, v190\n"
  "v_max3_u32 v60, v60, v191, v192\n"
  "v_max3_u32 v60, v60, v193, v194\n"
  "v_max3_u32 v60, v60, v195, v196\n"
  "v_max3_u32 v60, v60, v197, v198\n"
  "v_max_u32 v60, v60, v199\n"
  "v_cmp_le_u32 vcc, s74, v60\n"
  "s_cbranch_vccnz RAG2\n"
  "s_mov_b32 s74, 0x7fff7fff\n"
  "v_and_b32 v60, s74, v168\n"
  "v_and_b32 v61, s74, v169\n"
  "v_pk_max_u16 v60, v60, v61\n"
  "v_and_b32 v61, s74, v170\n"
  "v_pk_max_u16 v60, v60, v61\n"
  "v_and_b32 v61, s74, v171\n"
  "v_pk_max_u16 v60, v60, v61\n"
  "v_and_b32 v61, s74, v172\n"
  "v_pk_max_u16 v60, v60, v61\n"
  "v_and_b32 v61, s74, v173\n"
  "v_pk_max_u16 v60, v60, v61\n"
  "v_and_b32 v61, s74, v174\n"
  "v_pk_max_u16 v60, v60, v61\n"
  "v_and_b32 v61, s74, v175\n"
  "v_pk_max_u16 v60, v60, v61\n"
  "v_and_b32 v61, s74, v176\n"
  "v_pk_max_u16 v60, v60, v61\n"
  "v_and_b32 v61, s74, v177\n"
  "v_pk_max_u16 v60, v60, v61\n"
  "v_and_b32 v61, s74, v178\n"
  "v_pk_max_u16 v60, v60, v61\n"
  "v_and_b32 v61, s74, v179\n"
  "v_pk_max_u16 v60, v60, v61\n"
  "v_and_b32 v61, s74, v180\n"
  "v_pk_max_u16 v60, v60, v61\n"
  "v_and_b32 v61, s74, v181\n"
  "v_pk_max_u16 v60, v60, v61\n"
  "v_and_b32 v61, s74, v182\n"
  "v_pk_max_u16 v60, v60, v61\n"
  "v_and_b32 v61, s74, v183\n"
  "v_pk_max_u16 v60, v60, v61\n"
  "v_and_b32 v61, s74, v184\n"
  "v_pk_max_u16 v60, v60, v61\n"
  "v_and_b32 v61, s74, v185\n"
  "v_pk_max_u16 v60, v60, v61\n"
  "v_and_b32 v61, s74, v186\n"
  "v_pk_max_u16 v60, v60, v61\n"
  "v_and_b32 v61, s74, v187\n"
  "v_pk_max_u16 v60, v60, v61\n"
  "v_and_b32 v61, s74, v188\n"
  "v_pk_max_u16 v60, v60, v61\n"
  "v_and_b32 v61, s74, v189\n"
  "v_pk_max_u16 v60, v60, v61\n"
  "v_and_b32 v61, s74, v190\n"
  "v_pk_max_u16 v60, v60, v61\n"
  "v_and_b32 v61, s74, v191\n"
  "v_pk_max_u16 v60, v60, v61\n"
  "v_and_b32 v61, s74, v192\n"
  "v_pk_max_u16 v60, v60, v61\n"
  "v_and_b32 v61, s74, v193\n"
  "v_pk_max_u16 v60, v60, v61\n"
  "v_and_b32 v61, s74, v194\n"
  "v_pk_max_u16 v60, v60, v61\n"
  "v_and_b32 v61, s74, v195\n"
  "v_pk_max_u16 v60, v60, v61\n"
  "v_and_b32 v61, s74, v196\n"
  "v_pk_max_u16 v60, v60, v61\n"
  "v_and_b32 v61, s74, v197\n"
  "v_pk_max_u16 v60, v60, v61\n"
  "v_and_b32 v61, s74, v198\n"
  "v_pk_max_u16 v60, v60, v61\n"
  "v_and_b32 v61, s74, v199\n"
  "v_pk_max_u16 v60, v60, v61\n"
  "v_lshrrev_b32 v61, 16, v60\n"
  "v_and_b32 v60, 0xffff, v60\n"
  "v_max_u32 v60, v60, v61\n"
  "v_cmp_lt_u32 vcc, 0x7f81, v60\n"
  "s_cbranch_vccnz RAG2\n"
  "RFULL:\n"
  "v_cvt_pk_bf16_f32 v88, v84, v85\n"
  "v_cvt_pk_bf16_f32 v89, v86, v87\n"
  "v_cvt_pk_bf16_f32 v90, v84, v85\n"
  "v_cvt_pk_bf16_f32 v91, v86, v87\n"
  "s_nop 0\n"
  "v_permlane32_swap_b32 v88, v90\n"
  "v_permlane32_swap_b32 v89, v91\n"
  "s_mov_b32 s74, 0x7fff7fff\n"
  "s_mov_b32 s75, 0x7f817f81\n"
  "v_and_b32 v60, s74, v88\n"
  "v_pk_min_u16 v60, v60, s75\n"
  "v_bfi_b32 v88, s74, v60, v88\n"
  "v_and_b32 v60, s74, v89\n"
  "v_pk_min_u16 v60, v60, s75\n"
  "v_bfi_b32 v89, s74, v60, v89\n"
  "v_and_b32 v60, s74, v90\n"
  "v_pk_min_u16 v60, v60, s75\n"
  "v_bfi_b32 v90, s74, v60, v90\n"
  "v_and_b32 v60, s74, v91\n"
  "v_pk_min_u16 v60, v60, s75\n"
  "v_bfi_b32 v91, s74, v60, v91\n"
  "s_add_u32 s43, s43, 0x100\n"
  "s_memrealtime s[74:75]\n"
  "s_waitcnt lgkmcnt(0)\n"
  "s_mul_i32 s78, s74, 24\n"
  "s_add_u32 s78, s78, 750\n"
  "s_mov_b32 s81, s49\n"
  "RSG:\n"
  "s_add_u32 s79, s81, s51\n"
  "s_sub_i32 s74, s79, s78\n"
  "s_cmp_ge_i32 s74, 0\n"
  "s_cbranch_scc1 RGOT\n"
  "s_mov_b32 s81, s79\n"
  "s_branch RSG\n"
  "RGOT:\n"
  "v_mov_b32 v60, 0\n"
  "v_mov_b32 v61, s79\n"
  "ds_write_b32 v60, v61 offset:9232\n"
  "s_add_u32 s81, s81, 200\n"
  "WR4:\n"
  "s_memrealtime s[74:75]\n"
  "s_waitcnt lgkmcnt(0)\n"
  "s_mul_i32 s74, s74, 24\n"
  "s_sub_i32 s74, s81, s74\n"
  "s_cmp_lt_i32 s74, 750\n"
  "s_cbranch_scc1 WFR4\n"
  "s_cmp_gt_i32 s74, 0x40000\n"
  "s_cbranch_scc1 WER4\n"
  "s_sleep 2\n"
  "s_branch WR4\n"
  "WFR4:\n"
  "s_cmp_lt_i32 s74, 100\n"
  "s_cbranch_scc1 WER4\n"
  "WGR4:\n"
  "s_memtime s[76:77]\n"
  "s_memrealtime s[20:21]\n"
  "s_nop 2\n"
  "s_memrealtime s[22:23]\n"
  "s_nop 2\n"
  "s_memrealtime s[24:25]\n"
  "s_nop 2\n"
  "s_memrealtime s[26:27]\n"
  "s_nop 2\n"
  "s_memrealtime s[28:29]\n"
  "s_nop 2\n"
  "s_memrealtime s[30:31]\n"
  "s_waitcnt lgkmcnt(0)\n"
  "s_add_u32 s20, s20, s22\n"
  "s_add_u32 s24, s24, s26\n"
  "s_add_u32 s28, s28, s30\n"
  "s_add_u32 s20, s20, s24\n"
  "s_add_u32 s20, s20, s28\n"
  "s_lshl_b32 s74, s20, 2\n"
  "s_nop 0\n"
  "s_sub_i32 s74, s81, s74\n"
  "s_sub_i32 s74, s74, 238\n"
  "s_max_i32 s74, s74, 0\n"
  "s_cmp_ge_i32 s74, 512\n"
  "s_cbranch_scc1 WR4\n"
  "s_lshr_b32 s74, s74, 3\n"
  "s_lshl_b32 s74, s74, 2\n"
  "s_sub_i32 s74, 268, s74\n"
  "s_getpc_b64 s[76:77]\n"
  "s_add_u32 s76, s76, s74\n"
  "s_addc_u32 s77, s77, 0\n"
  "s_setpc_b64 s[76:77]\n"
  "s_nop 1\n"
  "s_nop 1\n"
  "s_nop 1\n"
  "s_nop 1\n"
  "s_nop 1\n"
  "s_nop 1\n"
  "s_nop 1\n"
  "s_nop 1\n"
  "s_nop 1\n"
  "s_nop 1\n"
  "s_nop 1\n"
  "s_nop 1\n"
  "s_nop 1\n"
  "s_nop 1\n"
  "s_nop 1\n"
  "s_nop 1\n"
  "s_nop 1\n"
  "s_nop 1\n"
  "s_nop 1\n"
  "s_nop 1\n"
  "s_nop 1\n"
  "s_nop 1\n"
  "s_nop 1\n"
  "s_nop 1\n"
  "s_nop 1\n"
  "s_nop 1\n"
  "s_nop 1\n"
  "s_nop 1\n"
  "s_nop 1\n"
  "s_nop 1\n"
  "s_nop 1\n"
  "s_nop 1\n"
  "s_nop 1\n"
  "s_nop 1\n"
  "s_nop 1\n"
  "s_nop 1\n"
  "s_nop 1\n"
  "s_nop 1\n"
  "s_nop 1\n"
  "s_nop 1\n"
  "s_nop 1\n"
  "s_nop 1\n"
  "s_nop 1\n"
  "s_nop 1\n"
  "s_nop 1\n"
  "s_nop 1\n"
  "s_nop 1\n"
  "s_nop 1\n"
  "s_nop 1\n"
  "s_nop 1\n"
  "s_nop 1\n"
  "s_nop 1\n"
  "s_nop 1\n"
  "s_nop 1\n"
  "s_nop 1\n"
  "s_nop 1\n"
  "s_nop 1\n"
  "s_nop 1\n"
  "s_nop 1\n"
  "s_nop 1\n"
  "s_nop 1\n"
  "s_nop 1\n"
  "s_nop 1\n"
  "s_nop 1\n"
  "WER4:\n"
  "global_store_dwordx4 v3, v[88:91], s[72:73] sc1\n"
  "global_store_dwordx4 v13, v[88:91], s[72:73] sc1\n"
  "s_branch RDONE\n"
  "ENDV:\n"
  "s_waitcnt vmcnt(0) lgkmcnt(0)\n"
  "s_barrier\n"
  "s_add_u32 s74, s36, s38\n"
  "s_addc_u32 s75, s37, 0\n"
  "v_mov_b32 v60, 0\n"
  "v_mov_b32 v61, 64\n"
  "s_cmp_eq_u32 s48, 1\n"
  "s_cselect_b64 exec, 1, 0\n"
  "global_store_dword v60, v61, s[74:75] sc1\n"
  "QUITV:\n"
  "s_endpgm\n"
        : "+{v[0:15]}"(vin), "+{s[36:43]}"(sa), "+{s[44:59]}"(sb) : "{s[4:5]}"(ask) : CLOB);
    }
    return;
  }
  const int fd = gam - 4;
  v2u a2 = ask;
  if (tk >= 16 && tk < 28) for (int i = 0; i < 6; ++i) __builtin_amdgcn_s_sleep(32);
  if (tk < 16) { ARGS; PTR(sa, 0, p.ws);
    const int h = fd * 8 + (tk >> 1), isq = tk & 1;
    { const int row = 4 * wave + g, sw = 2 * ((row & 3) | ((row >> 1) & 4)); vin[1] = row * 8192 + (n ^ sw) * 16; }
    vin[2] = (8 * g + qq) * 256 + ((4 * wave + (c4 >> 1)) ^ (2 * (qq | ((g & 1) << 2)))) * 16 + (c4 & 1) * 8;
    vin[3] = x_src(wave, lane); vin[9] = x_frag(lane);
    { const int row = 8 * wave + (lane >> 3); vin[4] = row * 8192 + ((lane & 7) ^ (2 * (((row >> 1) & 1) | ((row >> 2) & 2)))) * 16; }
    vin[5] = (8 * g + qq) * 128 + ((2 * wave + (c4 >> 1)) ^ (2 * ((qq >> 1) | ((g & 1) << 1)))) * 16 + (c4 & 1) * 8;
    PTR(sa, 2, (isq ? p.Wq : p.Wk) + h * 128); PTR(sa, 4, p.Wv + h * 128 + isq * 64); PTR(sa, 6, p.x);
    sb[0] = h * 65536 + (isq ? 32768 : KVH) + wave * 2048; sb[1] = KVH + h * 65536 + 16384 + (4 * isq + wave) * 1024;
    sb[2] = (C_KVP) + (2 * h + isq) * 128; sb[3] = wave; sb[4] = __float_as_uint(isq ? 1.0f : QS);
    sb[6] = WO_ST + (role * 4 + wave) * 4096;
    UNIFORM(sa, 8); UNIFORM(sb, 16);
    asm volatile(
  "s_nop 4\n"
  "s_lshl_b32 s80, s47, 10\n"
  "v_mov_b32 v8, s48\n"
  "s_add_u32 s58, s80, 122880\n"
  "s_add_u32 m0, s58, 0\n"
  "s_nop 0\n"
  "global_load_lds_dwordx4 v3, s[42:43] offset:0\n"
  "s_add_u32 m0, s58, 3968\n"
  "s_nop 0\n"
  "global_load_lds_dwordx4 v3, s[42:43] offset:128\n"
  "s_add_u32 m0, s58, 7936\n"
  "s_nop 0\n"
  "global_load_lds_dwordx4 v3, s[42:43] offset:256\n"
  "s_add_u32 m0, s58, 11904\n"
  "s_nop 0\n"
  "global_load_lds_dwordx4 v3, s[42:43] offset:384\n"
  "s_add_u32 m0, s58, 15872\n"
  "s_nop 0\n"
  "global_load_lds_dwordx4 v3, s[42:43] offset:512\n"
  "s_add_u32 m0, s58, 19840\n"
  "s_nop 0\n"
  "global_load_lds_dwordx4 v3, s[42:43] offset:640\n"
  "s_add_u32 m0, s58, 23808\n"
  "s_nop 0\n"
  "global_load_lds_dwordx4 v3, s[42:43] offset:768\n"
  "s_add_u32 m0, s58, 27776\n"
  "s_nop 0\n"
  "global_load_lds_dwordx4 v3, s[42:43] offset:896\n"
  "s_add_u32 m0, s58, 31744\n"
  "s_nop 0\n"
  "global_load_lds_dwordx4 v3, s[42:43] offset:1024\n"
  "s_add_u32 m0, s80, 0\n"
  "s_add_u32 s74, s38, 0\n"
  "s_addc_u32 s75, s39, 0\n"
  "global_load_lds_dwordx4 v1, s[74:75]\n"
  "s_add_u32 m0, s80, 4096\n"
  "s_add_u32 s74, s38, 131072\n"
  "s_addc_u32 s75, s39, 0\n"
  "global_load_lds_dwordx4 v1, s[74:75]\n"
  "s_add_u32 m0, s80, 8192\n"
  "s_add_u32 s74, s38, 262144\n"
  "s_addc_u32 s75, s39, 0\n"
  "global_load_lds_dwordx4 v1, s[74:75]\n"
  "s_add_u32 m0, s80, 12288\n"
  "s_add_u32 s74, s38, 393216\n"
  "s_addc_u32 s75, s39, 0\n"
  "global_load_lds_dwordx4 v1, s[74:75]\n"
  "s_add_u32 m0, s80, 16384\n"
  "s_add_u32 s74, s40, 0\n"
  "s_addc_u32 s75, s41, 0\n"
  "global_load_lds_dwordx4 v4, s[74:75]\n"
  "s_add_u32 m0, s80, 20480\n"
  "s_add_u32 s74, s40, 262144\n"
  "s_addc_u32 s75, s41, 0\n"
  "global_load_lds_dwordx4 v4, s[74:75]\n"
  "s_add_u32 m0, s80, 24576\n"
  "s_add_u32 s74, s38, 524288\n"
  "s_addc_u32 s75, s39, 0\n"
  "global_load_lds_dwordx4 v1, s[74:75]\n"
  "s_add_u32 m0, s80, 28672\n"
  "s_add_u32 s74, s38, 655360\n"
  "s_addc_u32 s75, s39, 0\n"
  "global_load_lds_dwordx4 v1, s[74:75]\n"
  "s_add_u32 m0, s80, 32768\n"
  "s_add_u32 s74, s38, 786432\n"
  "s_addc_u32 s75, s39, 0\n"
  "global_load_lds_dwordx4 v1, s[74:75]\n"
  "s_add_u32 m0, s80, 36864\n"
  "s_add_u32 s74, s38, 917504\n"
  "s_addc_u32 s75, s39, 0\n"
  "global_load_lds_dwordx4 v1, s[74:75]\n"
  "s_add_u32 m0, s80, 40960\n"
  "s_add_u32 s74, s40, 524288\n"
  "s_addc_u32 s75, s41, 0\n"
  "global_load_lds_dwordx4 v4, s[74:75]\n"
  "s_add_u32 m0, s80, 45056\n"
  "s_add_u32 s74, s40, 786432\n"
  "s_addc_u32 s75, s41, 0\n"
  "global_load_lds_dwordx4 v4, s[74:75]\n"
  "s_add_u32 m0, s80, 49152\n"
  "s_add_u32 s74, s38, 1048576\n"
  "s_addc_u32 s75, s39, 0\n"
  "global_load_lds_dwordx4 v1, s[74:75]\n"
  "s_add_u32 m0, s80, 53248\n"
  "s_add_u32 s74, s38, 1179648\n"
  "s_addc_u32 s75, s39, 0\n"
  "global_load_lds_dwordx4 v1, s[74:75]\n"
  "s_add_u32 m0, s80, 57344\n"
  "s_add_u32 s74, s38, 1310720\n"
  "s_addc_u32 s75, s39, 0\n"
  "global_load_lds_dwordx4 v1, s[74:75]\n"
  "s_add_u32 m0, s80, 61440\n"
  "s_add_u32 s74, s38, 1441792\n"
  "s_addc_u32 s75, s39, 0\n"
  "global_load_lds_dwordx4 v1, s[74:75]\n"
  "s_add_u32 m0, s80, 65536\n"
  "s_add_u32 s74, s40, 1048576\n"
  "s_addc_u32 s75, s41, 0\n"
  "global_load_lds_dwordx4 v4, s[74:75]\n"
  "s_add_u32 m0, s80, 69632\n"
  "s_add_u32 s74, s40, 1310720\n"
  "s_addc_u32 s75, s41, 0\n"
  "global_load_lds_dwordx4 v4, s[74:75]\n"
  "s_add_u32 m0, s80, 73728\n"
  "s_add_u32 s74, s38, 1572864\n"
  "s_addc_u32 s75, s39, 0\n"
  "global_load_lds_dwordx4 v1, s[74:75]\n"
  "s_add_u32 m0, s80, 77824\n"
  "s_add_u32 s74, s38, 1703936\n"
  "s_addc_u32 s75, s39, 0\n"
  "global_load_lds_dwordx4 v1, s[74:75]\n"
  "s_add_u32 m0, s80, 81920\n"
  "s_add_u32 s74, s38, 1835008\n"
  "s_addc_u32 s75, s39, 0\n"
  "global_load_lds_dwordx4 v1, s[74:75]\n"
  "s_add_u32 m0, s80, 86016\n"
  "s_add_u32 s74, s38, 1966080\n"
  "s_addc_u32 s75, s39, 0\n"
  "global_load_lds_dwordx4 v1, s[74:75]\n"
  "s_add_u32 m0, s80, 90112\n"
  "s_add_u32 s74, s40, 1572864\n"
  "s_addc_u32 s75, s41, 0\n"
  "global_load_lds_dwordx4 v4, s[74:75]\n"
  "s_add_u32 m0, s80, 94208\n"
  "s_add_u32 s74, s40, 1835008\n"
  "s_addc_u32 s75, s41, 0\n"
  "global_load_lds_dwordx4 v4, s[74:75]\n"
  "s_waitcnt vmcnt(18) lgkmcnt(0)\n"
  "s_barrier\n"
  "s_add_u32 m0, s80, 98304\n"
  "s_add_u32 s74, s38, 2097152\n"
  "s_addc_u32 s75, s39, 0\n"
  "global_load_lds_dwordx4 v1, s[74:75]\n"
  "s_add_u32 m0, s80, 102400\n"
  "s_add_u32 s74, s38, 2228224\n"
  "s_addc_u32 s75, s39, 0\n"
  "global_load_lds_dwordx4 v1, s[74:75]\n"
  "s_add_u32 m0, s80, 106496\n"
  "s_add_u32 s74, s38, 2359296\n"
  "s_addc_u32 s75, s39, 0\n"
  "global_load_lds_dwordx4 v1, s[74:75]\n"
  "s_add_u32 m0, s80, 110592\n"
  "s_add_u32 s74, s38, 2490368\n"
  "s_addc_u32 s75, s39, 0\n"
  "global_load_lds_dwordx4 v1, s[74:75]\n"
  "s_add_u32 m0, s80, 114688\n"
  "s_add_u32 s74, s40, 2097152\n"
  "s_addc_u32 s75, s41, 0\n"
  "global_load_lds_dwordx4 v4, s[74:75]\n"
  "s_add_u32 m0, s80, 118784\n"
  "s_add_u32 s74, s40, 2359296\n"
  "s_addc_u32 s75, s41, 0\n"
  "global_load_lds_dwordx4 v4, s[74:75]\n"
  "v_xor_b32 v6, 0, v2\n"
  "v_add_u32 v6, 0, v6\n"
  "ds_read_b64_tr_b16 a[0:1], v6 offset:0\n"
  "ds_read_b64_tr_b16 a[2:3], v6 offset:1024\n"
  "ds_read_b64_tr_b16 a[4:5], v6 offset:8192\n"
  "ds_read_b64_tr_b16 a[6:7], v6 offset:9216\n"
  "v_xor_b32 v6, 32, v2\n"
  "v_add_u32 v6, 0, v6\n"
  "ds_read_b64_tr_b16 a[72:73], v6 offset:0\n"
  "ds_read_b64_tr_b16 a[74:75], v6 offset:1024\n"
  "ds_read_b64_tr_b16 a[76:77], v6 offset:8192\n"
  "ds_read_b64_tr_b16 a[78:79], v6 offset:9216\n"
  "v_add_u32 v6, 16384, v5\n"
  "ds_read_b64_tr_b16 a[144:145], v6 offset:0\n"
  "ds_read_b64_tr_b16 a[146:147], v6 offset:512\n"
  "ds_read_b64_tr_b16 a[148:149], v6 offset:4096\n"
  "ds_read_b64_tr_b16 a[150:151], v6 offset:4608\n"
  "s_waitcnt vmcnt(18) lgkmcnt(0)\n"
  "s_barrier\n"
  "s_add_u32 m0, s80, 0\n"
  "s_add_u32 s74, s38, 2621440\n"
  "s_addc_u32 s75, s39, 0\n"
  "global_load_lds_dwordx4 v1, s[74:75]\n"
  "s_add_u32 m0, s80, 4096\n"
  "s_add_u32 s74, s38, 2752512\n"
  "s_addc_u32 s75, s39, 0\n"
  "global_load_lds_dwordx4 v1, s[74:75]\n"
  "s_add_u32 m0, s80, 8192\n"
  "s_add_u32 s74, s38, 2883584\n"
  "s_addc_u32 s75, s39, 0\n"
  "global_load_lds_dwordx4 v1, s[74:75]\n"
  "s_add_u32 m0, s80, 12288\n"
  "s_add_u32 s74, s38, 3014656\n"
  "s_addc_u32 s75, s39, 0\n"
  "global_load_lds_dwordx4 v1, s[74:75]\n"
  "s_add_u32 m0, s80, 16384\n"
  "s_add_u32 s74, s40, 2621440\n"
  "s_addc_u32 s75, s41, 0\n"
  "global_load_lds_dwordx4 v4, s[74:75]\n"
  "s_add_u32 m0, s80, 20480\n"
  "s_add_u32 s74, s40, 2883584\n"
  "s_addc_u32 s75, s41, 0\n"
  "global_load_lds_dwordx4 v4, s[74:75]\n"
  "v_xor_b32 v6, 0, v2\n"
  "v_add_u32 v6, 24576, v6\n"
  "ds_read_b64_tr_b16 a[8:9], v6 offset:0\n"
  "ds_read_b64_tr_b16 a[10:11], v6 offset:1024\n"
  "ds_read_b64_tr_b16 a[12:13], v6 offset:8192\n"
  "ds_read_b64_tr_b16 a[14:15], v6 offset:9216\n"
  "v_xor_b32 v6, 32, v2\n"
  "v_add_u32 v6, 24576, v6\n"
  "ds_read_b64_tr_b16 a[80:81], v6 offset:0\n"
  "ds_read_b64_tr_b16 a[82:83], v6 offset:1024\n"
  "ds_read_b64_tr_b16 a[84:85], v6 offset:8192\n"
  "ds_read_b64_tr_b16 a[86:87], v6 offset:9216\n"
  "v_add_u32 v6, 40960, v5\n"
  "ds_read_b64_tr_b16 a[152:153], v6 offset:0\n"
  "ds_read_b64_tr_b16 a[154:155], v6 offset:512\n"
  "ds_read_b64_tr_b16 a[156:157], v6 offset:4096\n"
  "ds_read_b64_tr_b16 a[158:159], v6 offset:4608\n"
  "s_waitcnt vmcnt(18) lgkmcnt(0)\n"
  "s_barrier\n"
  "s_add_u32 m0, s80, 24576\n"
  "s_add_u32 s74, s38, 3145728\n"
  "s_addc_u32 s75, s39, 0\n"
  "global_load_lds_dwordx4 v1, s[74:75]\n"
  "s_add_u32 m0, s80, 28672\n"
  "s_add_u32 s74, s38, 3276800\n"
  "s_addc_u32 s75, s39, 0\n"
  "global_load_lds_dwordx4 v1, s[74:75]\n"
  "s_add_u32 m0, s80, 32768\n"
  "s_add_u32 s74, s38, 3407872\n"
  "s_addc_u32 s75, s39, 0\n"
  "global_load_lds_dwordx4 v1, s[74:75]\n"
  "s_add_u32 m0, s80, 36864\n"
  "s_add_u32 s74, s38, 3538944\n"
  "s_addc_u32 s75, s39, 0\n"
  "global_load_lds_dwordx4 v1, s[74:75]\n"
  "s_add_u32 m0, s80, 40960\n"
  "s_add_u32 s74, s40, 3145728\n"
  "s_addc_u32 s75, s41, 0\n"
  "global_load_lds_dwordx4 v4, s[74:75]\n"
  "s_add_u32 m0, s80, 45056\n"
  "s_add_u32 s74, s40, 3407872\n"
  "s_addc_u32 s75, s41, 0\n"
  "global_load_lds_dwordx4 v4, s[74:75]\n"
  "v_xor_b32 v6, 0, v2\n"
  "v_add_u32 v6, 49152, v6\n"
  "ds_read_b64_tr_b16 a[16:17], v6 offset:0\n"
  "ds_read_b64_tr_b16 a[18:19], v6 offset:1024\n"
  "ds_read_b64_tr_b16 a[20:21], v6 offset:8192\n"
  "ds_read_b64_tr_b16 a[22:23], v6 offset:9216\n"
  "v_xor_b32 v6, 32, v2\n"
  "v_add_u32 v6, 49152, v6\n"
  "ds_read_b64_tr_b16 a[88:89], v6 offset:0\n"
  "ds_read_b64_tr_b16 a[90:91], v6 offset:1024\n"
  "ds_read_b64_tr_b16 a[92:93], v6 offset:8192\n"
  "ds_read_b64_tr_b16 a[94:95], v6 offset:9216\n"
  "v_add_u32 v6, 65536, v5\n"
  "ds_read_b64_tr_b16 a[160:161], v6 offset:0\n"
  "ds_read_b64_tr_b16 a[162:163], v6 offset:512\n"
  "ds_read_b64_tr_b16 a[164:165], v6 offset:4096\n"
  "ds_read_b64_tr_b16 a[166:167], v6 offset:4608\n"
  "s_waitcnt vmcnt(18) lgkmcnt(0)\n"
  "s_barrier\n"
  "s_add_u32 m0, s80, 49152\n"
  "s_add_u32 s74, s38, 3670016\n"
  "s_addc_u32 s75, s39, 0\n"
  "global_load_lds_dwordx4 v1, s[74:75]\n"
  "s_add_u32 m0, s80, 53248\n"
  "s_add_u32 s74, s38, 3801088\n"
  "s_addc_u32 s75, s39, 0\n"
  "global_load_lds_dwordx4 v1, s[74:75]\n"
  "s_add_u32 m0, s80, 57344\n"
  "s_add_u32 s74, s38, 3932160\n"
  "s_addc_u32 s75, s39, 0\n"
  "global_load_lds_dwordx4 v1, s[74:75]\n"
  "s_add_u32 m0, s80, 61440\n"
  "s_add_u32 s74, s38, 4063232\n"
  "s_addc_u32 s75, s39, 0\n"
  "global_load_lds_dwordx4 v1, s[74:75]\n"
  "s_add_u32 m0, s80, 65536\n"
  "s_add_u32 s74, s40, 3670016\n"
  "s_addc_u32 s75, s41, 0\n"
  "global_load_lds_dwordx4 v4, s[74:75]\n"
  "s_add_u32 m0, s80, 69632\n"
  "s_add_u32 s74, s40, 3932160\n"
  "s_addc_u32 s75, s41, 0\n"
  "global_load_lds_dwordx4 v4, s[74:75]\n"
  "v_xor_b32 v6, 0, v2\n"
  "v_add_u32 v6, 73728, v6\n"
  "ds_read_b64_tr_b16 a[24:25], v6 offset:0\n"
  "ds_read_b64_tr_b16 a[26:27], v6 offset:1024\n"
  "ds_read_b64_tr_b16 a[28:29], v6 offset:8192\n"
  "ds_read_b64_tr_b16 a[30:31], v6 offset:9216\n"
  "v_xor_b32 v6, 32, v2\n"
  "v_add_u32 v6, 73728, v6\n"
  "ds_read_b64_tr_b16 a[96:97], v6 offset:0\n"
  "ds_read_b64_tr_b16 a[98:99], v6 offset:1024\n"
  "ds_read_b64_tr_b16 a[100:101], v6 offset:8192\n"
  "ds_read_b64_tr_b16 a[102:103], v6 offset:9216\n"
  "v_add_u32 v6, 90112, v5\n"
  "ds_read_b64_tr_b16 a[168:169], v6 offset:0\n"
  "ds_read_b64_tr_b16 a[170:171], v6 offset:512\n"
  "ds_read_b64_tr_b16 a[172:173], v6 offset:4096\n"
  "ds_read_b64_tr_b16 a[174:175], v6 offset:4608\n"
  "s_waitcnt vmcnt(18) lgkmcnt(0)\n"
  "s_barrier\n"
  "s_add_u32 m0, s80, 73728\n"
  "s_add_u32 s74, s38, 4194304\n"
  "s_addc_u32 s75, s39, 0\n"
  "global_load_lds_dwordx4 v1, s[74:75]\n"
  "s_add_u32 m0, s80, 77824\n"
  "s_add_u32 s74, s38, 4325376\n"
  "s_addc_u32 s75, s39, 0\n"
  "global_load_lds_dwordx4 v1, s[74:75]\n"
  "s_add_u32 m0, s80, 81920\n"
  "s_add_u32 s74, s38, 4456448\n"
  "s_addc_u32 s75, s39, 0\n"
  "global_load_lds_dwordx4 v1, s[74:75]\n"
  "s_add_u32 m0, s80, 86016\n"
  "s_add_u32 s74, s38, 4587520\n"
  "s_addc_u32 s75, s39, 0\n"
  "global_load_lds_dwordx4 v1, s[74:75]\n"
  "s_add_u32 m0, s80, 90112\n"
  "s_add_u32 s74, s40, 4194304\n"
  "s_addc_u32 s75, s41, 0\n"
  "global_load_lds_dwordx4 v4, s[74:75]\n"
  "s_add_u32 m0, s80, 94208\n"
  "s_add_u32 s74, s40, 4456448\n"
  "s_addc_u32 s75, s41, 0\n"
  "global_load_lds_dwordx4 v4, s[74:75]\n"
  "v_xor_b32 v6, 0, v2\n"
  "v_add_u32 v6, 98304, v6\n"
  "ds_read_b64_tr_b16 a[32:33], v6 offset:0\n"
  "ds_read_b64_tr_b16 a[34:35], v6 offset:1024\n"
  "ds_read_b64_tr_b16 a[36:37], v6 offset:8192\n"
  "ds_read_b64_tr_b16 a[38:39], v6 offset:9216\n"
  "v_xor_b32 v6, 32, v2\n"
  "v_add_u32 v6, 98304, v6\n"
  "ds_read_b64_tr_b16 a[104:105], v6 offset:0\n"
  "ds_read_b64_tr_b16 a[106:107], v6 offset:1024\n"
  "ds_read_b64_tr_b16 a[108:109], v6 offset:8192\n"
  "ds_read_b64_tr_b16 a[110:111], v6 offset:9216\n"
  "v_add_u32 v6, 114688, v5\n"
  "ds_read_b64_tr_b16 a[176:177], v6 offset:0\n"
  "ds_read_b64_tr_b16 a[178:179], v6 offset:512\n"
  "ds_read_b64_tr_b16 a[180:181], v6 offset:4096\n"
  "ds_read_b64_tr_b16 a[182:183], v6 offset:4608\n"
  "s_waitcnt vmcnt(18) lgkmcnt(0)\n"
  "s_barrier\n"
  "v_xor_b32 v6, 0, v2\n"
  "v_add_u32 v6, 0, v6\n"
  "ds_read_b64_tr_b16 a[40:41], v6 offset:0\n"
  "ds_read_b64_tr_b16 a[42:43], v6 offset:1024\n"
  "ds_read_b64_tr_b16 a[44:45], v6 offset:8192\n"
  "ds_read_b64_tr_b16 a[46:47], v6 offset:9216\n"
  "v_xor_b32 v6, 32, v2\n"
  "v_add_u32 v6, 0, v6\n"
  "ds_read_b64_tr_b16 a[112:113], v6 offset:0\n"
  "ds_read_b64_tr_b16 a[114:115], v6 offset:1024\n"
  "ds_read_b64_tr_b16 a[116:117], v6 offset:8192\n"
  "ds_read_b64_tr_b16 a[118:119], v6 offset:9216\n"
  "v_add_u32 v6, 16384, v5\n"
  "ds_read_b64_tr_b16 a[184:185], v6 offset:0\n"
  "ds_read_b64_tr_b16 a[186:187], v6 offset:512\n"
  "ds_read_b64_tr_b16 a[188:189], v6 offset:4096\n"
  "ds_read_b64_tr_b16 a[190:191], v6 offset:4608\n"
  "s_waitcnt vmcnt(12) lgkmcnt(0)\n"
  "s_barrier\n"
  "v_xor_b32 v6, 0, v2\n"
  "v_add_u32 v6, 24576, v6\n"
  "ds_read_b64_tr_b16 a[48:49], v6 offset:0\n"
  "ds_read_b64_tr_b16 a[50:51], v6 offset:1024\n"
  "ds_read_b64_tr_b16 a[52:53], v6 offset:8192\n"
  "ds_read_b64_tr_b16 a[54:55], v6 offset:9216\n"
  "v_xor_b32 v6, 32, v2\n"
  "v_add_u32 v6, 24576, v6\n"
  "ds_read_b64_tr_b16 a[120:121], v6 offset:0\n"
  "ds_read_b64_tr_b16 a[122:123], v6 offset:1024\n"
  "ds_read_b64_tr_b16 a[124:125], v6 offset:8192\n"
  "ds_read_b64_tr_b16 a[126:127], v6 offset:9216\n"
  "v_add_u32 v6, 40960, v5\n"
  "ds_read_b64_tr_b16 a[192:193], v6 offset:0\n"
  "ds_read_b64_tr_b16 a[194:195], v6 offset:512\n"
  "ds_read_b64_tr_b16 a[196:197], v6 offset:4096\n"
  "ds_read_b64_tr_b16 a[198:199], v6 offset:4608\n"
  "s_waitcnt vmcnt(6) lgkmcnt(0)\n"
  "s_barrier\n"
  "v_xor_b32 v6, 0, v2\n"
  "v_add_u32 v6, 49152, v6\n"
  "ds_read_b64_tr_b16 a[56:57], v6 offset:0\n"
  "ds_read_b64_tr_b16 a[58:59], v6 offset:1024\n"
  "ds_read_b64_tr_b16 a[60:61], v6 offset:8192\n"
  "ds_read_b64_tr_b16 a[62:63], v6 offset:9216\n"
  "v_xor_b32 v6, 32, v2\n"
  "v_add_u32 v6, 49152, v6\n"
  "ds_read_b64_tr_b16 a[128:129], v6 offset:0\n"
  "ds_read_b64_tr_b16 a[130:131], v6 offset:1024\n"
  "ds_read_b64_tr_b16 a[132:133], v6 offset:8192\n"
  "ds_read_b64_tr_b16 a[134:135], v6 offset:9216\n"
  "v_add_u32 v6, 65536, v5\n"
  "ds_read_b64_tr_b16 a[200:201], v6 offset:0\n"
  "ds_read_b64_tr_b16 a[202:203], v6 offset:512\n"
  "ds_read_b64_tr_b16 a[204:205], v6 offset:4096\n"
  "ds_read_b64_tr_b16 a[206:207], v6 offset:4608\n"
  "s_waitcnt vmcnt(0) lgkmcnt(0)\n"
  "s_barrier\n"
  "v_xor_b32 v6, 0, v2\n"
  "v_add_u32 v6, 73728, v6\n"
  "ds_read_b64_tr_b16 a[64:65], v6 offset:0\n"
  "ds_read_b64_tr_b16 a[66:67], v6 offset:1024\n"
  "ds_read_b64_tr_b16 a[68:69], v6 offset:8192\n"
  "ds_read_b64_tr_b16 a[70:71], v6 offset:9216\n"
  "v_xor_b32 v6, 32, v2\n"
  "v_add_u32 v6, 73728, v6\n"
  "ds_read_b64_tr_b16 a[136:137], v6 offset:0\n"
  "ds_read_b64_tr_b16 a[138:139], v6 offset:1024\n"
  "ds_read_b64_tr_b16 a[140:141], v6 offset:8192\n"
  "ds_read_b64_tr_b16 a[142:143], v6 offset:9216\n"
  "v_add_u32 v6, 90112, v5\n"
  "ds_read_b64_tr_b16 a[208:209], v6 offset:0\n"
  "ds_read_b64_tr_b16 a[210:211], v6 offset:512\n"
  "ds_read_b64_tr_b16 a[212:213], v6 offset:4096\n"
  "ds_read_b64_tr_b16 a[214:215], v6 offset:4608\n"
  "s_mov_b32 s59, 400000\n"
  "s_add_u32 s74, s36, s5\n"
  "s_addc_u32 s75, s37, 0\n"
  "BLP:\n"
  "v_mov_b32 v6, 0\n"
  "global_load_dwordx2 v[6:7], v6, s[74:75] sc1\n"
  "s_waitcnt vmcnt(0)\n"
  "v_readfirstlane_b32 s77, v6\n"
  "v_readfirstlane_b32 s76, v7\n"
  "s_cmp_eq_u32 s77, s4\n"
  "s_cbranch_scc1 BDP\n"
  "s_sub_u32 s59, s59, 1\n"
  "s_cbranch_scc1 PJQ\n"
  "s_sleep 4\n"
  "s_branch BLP\n"
  "BDP:\n"
  "s_sub_u32 s77, s5, 16966656\n"
  "s_lshr_b32 s77, s77, 8\n"
  "s_lshl_b32 s77, s77, 7\n"
  "s_add_u32 s77, s77, 16777216\n"
  "s_add_u32 s74, s36, s77\n"
  "s_addc_u32 s75, s37, 0\n"
  "v_mov_b32 v6, 0\n"
  "v_mov_b32 v7, 1\n"
  "s_cmp_eq_u32 s47, 0\n"
  "s_cselect_b64 exec, 1, 0\n"
  "global_atomic_add v6, v7, s[74:75] sc1\n"
  "s_mov_b64 exec, -1\n"
  "s_and_b32 s74, s76, 2\n"
  "s_lshr_b32 s75, s74, 1\n"
  "s_lshl_b32 s4, 0x55555555, s75\n"
  "s_mov_b32 s5, s4\n"
  "s_lshl_b32 s74, s74, 12\n"
  "s_xor_b32 s46, s74, s46\n"
  "s_add_u32 s64, s36, s46\n"
  "s_addc_u32 s65, s37, 0\n"
  "s_mov_b32 s56, 0\n"
  "s_mov_b32 s57, 122880\n"
  "v_add_u32 v12, 122880, v9\n"
  "v_xor_b32 v13, 64, v12\n"
  "s_waitcnt lgkmcnt(0)\n"
  "s_barrier\n"
  "s_add_u32 s58, s80, 12288\n"
  "s_add_u32 s42, s42, 36864\n"
  "s_addc_u32 s43, s43, 0\n"
  "ds_read_b128 v[16:19], v12 offset:0\n"
  "ds_read_b128 v[20:23], v12 offset:2048\n"
  "ds_read_b128 v[24:27], v13 offset:0\n"
  "ds_read_b128 v[28:31], v13 offset:2048\n"
  "ds_read_b128 v[32:35], v12 offset:4096\n"
  "ds_read_b128 v[36:39], v12 offset:6144\n"
  "s_add_u32 m0, s58, 0\n"
  "s_nop 0\n"
  "global_load_lds_dwordx4 v3, s[42:43] offset:0\n"
  "s_add_u32 m0, s58, 3968\n"
  "s_nop 0\n"
  "global_load_lds_dwordx4 v3, s[42:43] offset:128\n"
  "s_add_u32 m0, s58, 7936\n"
  "s_nop 0\n"
  "global_load_lds_dwordx4 v3, s[42:43] offset:256\n"
  "s_add_u32 m0, s58, 11904\n"
  "s_nop 0\n"
  "global_load_lds_dwordx4 v3, s[42:43] offset:384\n"
  "s_add_u32 m0, s58, 15872\n"
  "s_nop 0\n"
  "global_load_lds_dwordx4 v3, s[42:43] offset:512\n"
  "s_add_u32 m0, s58, 19840\n"
  "s_nop 0\n"
  "global_load_lds_dwordx4 v3, s[42:43] offset:640\n"
  "s_add_u32 m0, s58, 23808\n"
  "s_nop 0\n"
  "global_load_lds_dwordx4 v3, s[42:43] offset:768\n"
  "s_add_u32 m0, s58, 27776\n"
  "s_nop 0\n"
  "global_load_lds_dwordx4 v3, s[42:43] offset:896\n"
  "s_add_u32 m0, s58, 31744\n"
  "s_nop 0\n"
  "global_load_lds_dwordx4 v3, s[42:43] offset:1024\n"
  "v_mov_b64 v[10:11], v[12:13]\n"
  "s_waitcnt lgkmcnt(5)\n"
  "v_mfma_f32_16x16x32_bf16 v[48:51], a[0:3], v[16:19], 0\n"
  "ds_read_b128 v[40:43], v11 offset:4096\n"
  "s_waitcnt lgkmcnt(5)\n"
  "v_mfma_f32_16x16x32_bf16 v[52:55], a[0:3], v[20:23], 0\n"
  "ds_read_b128 v[44:47], v11 offset:6144\n"
  "v_mfma_f32_16x16x32_bf16 v[56:59], a[72:75], v[16:19], 0\n"
  "s_add_u32 s57, s57, 36864\n"
  "s_cmp_eq_u32 s57, 159744\n"
  "v_mfma_f32_16x16x32_bf16 v[60:63], a[72:75], v[20:23], 0\n"
  "s_cselect_b32 s57, 12288, s57\n"
  "v_add_u32 v12, s57, v9\n"
  "v_mfma_f32_16x16x32_bf16 v[64:67], v[16:19], a[144:147], 0\n"
  "v_xor_b32 v13, 64, v12\n"
  "s_add_u32 s58, s57, 36864\n"
  "v_mfma_f32_16x16x32_bf16 v[68:71], v[20:23], a[144:147], 0\n"
  "s_cmp_eq_u32 s57, 122880\n"
  "s_cselect_b32 s58, 12288, s58\n"
  "s_waitcnt lgkmcnt(5)\n"
  "v_mfma_f32_16x16x32_bf16 v[48:51], a[4:7], v[24:27], v[48:51]\n"
  "ds_read_b128 v[16:19], v10 offset:8192\n"
  "s_waitcnt lgkmcnt(5)\n"
  "v_mfma_f32_16x16x32_bf16 v[52:55], a[4:7], v[28:31], v[52:55]\n"
  "ds_read_b128 v[20:23], v10 offset:10240\n"
  "v_mfma_f32_16x16x32_bf16 v[56:59], a[76:79], v[24:27], v[56:59]\n"
  "s_add_u32 s58, s58, s80\n"
  "s_cmp_lt_u32 s56, 62\n"
  "v_mfma_f32_16x16x32_bf16 v[60:63], a[76:79], v[28:31], v[60:63]\n"
  "s_cselect_b32 s74, 36864, 0\n"
  "s_add_u32 s42, s42, s74\n"
  "v_mfma_f32_16x16x32_bf16 v[64:67], v[24:27], a[148:151], v[64:67]\n"
  "s_addc_u32 s43, s43, 0\n"
  "v_mfma_f32_16x16x32_bf16 v[68:71], v[28:31], a[148:151], v[68:71]\n"
  "s_waitcnt lgkmcnt(5)\n"
  "v_mfma_f32_16x16x32_bf16 v[48:51], a[8:11], v[32:35], v[48:51]\n"
  "ds_read_b128 v[24:27], v11 offset:8192\n"
  "s_waitcnt lgkmcnt(5)\n"
  "v_mfma_f32_16x16x32_bf16 v[52:55], a[8:11], v[36:39], v[52:55]\n"
  "ds_read_b128 v[28:31], v11 offset:10240\n"
  "v_mfma_f32_16x16x32_bf16 v[56:59], a[80:83], v[32:35], v[56:59]\n"
  "v_mfma_f32_16x16x32_bf16 v[60:63], a[80:83], v[36:39], v[60:63]\n"
  "v_mfma_f32_16x16x32_bf16 v[64:67], v[32:35], a[152:155], v[64:67]\n"
  "v_mfma_f32_16x16x32_bf16 v[68:71], v[36:39], a[152:155], v[68:71]\n"
  "s_waitcnt lgkmcnt(5)\n"
  "v_mfma_f32_16x16x32_bf16 v[48:51], a[12:15], v[40:43], v[48:51]\n"
  "ds_read_b128 v[32:35], v10 offset:12288\n"
  "s_waitcnt lgkmcnt(5)\n"
  "v_mfma_f32_16x16x32_bf16 v[52:55], a[12:15], v[44:47], v[52:55]\n"
  "ds_read_b128 v[36:39], v10 offset:14336\n"
  "v_mfma_f32_16x16x32_bf16 v[56:59], a[84:87], v[40:43], v[56:59]\n"
  "v_mfma_f32_16x16x32_bf16 v[60:63], a[84:87], v[44:47], v[60:63]\n"
  "v_mfma_f32_16x16x32_bf16 v[64:67], v[40:43], a[156:159], v[64:67]\n"
  "v_mfma_f32_16x16x32_bf16 v[68:71], v[44:47], a[156:159], v[68:71]\n"
  "s_waitcnt lgkmcnt(5)\n"
  "v_mfma_f32_16x16x32_bf16 v[48:51], a[16:19], v[16:19], v[48:51]\n"
  "ds_read_b128 v[40:43], v11 offset:12288\n"
  "s_waitcnt lgkmcnt(5)\n"
  "v_mfma_f32_16x16x32_bf16 v[52:55], a[16:19], v[20:23], v[52:55]\n"
  "ds_read_b128 v[44:47], v11 offset:14336\n"
  "v_mfma_f32_16x16x32_bf16 v[56:59], a[88:91], v[16:19], v[56:59]\n"
  "v_mfma_f32_16x16x32_bf16 v[60:63], a[88:91], v[20:23], v[60:63]\n"
  "v_mfma_f32_16x16x32_bf16 v[64:67], v[16:19], a[160:163], v[64:67]\n"
  "v_mfma_f32_16x16x32_bf16 v[68:71], v[20:23], a[160:163], v[68:71]\n"
  "s_waitcnt lgkmcnt(5)\n"
  "v_mfma_f32_16x16x32_bf16 v[48:51], a[20:23], v[24:27], v[48:51]\n"
  "ds_read_b128 v[16:19], v10 offset:16384\n"
  "s_waitcnt lgkmcnt(5)\n"
  "v_mfma_f32_16x16x32_bf16 v[52:55], a[20:23], v[28:31], v[52:55]\n"
  "ds_read_b128 v[20:23], v10 offset:18432\n"
  "v_mfma_f32_16x16x32_bf16 v[56:59], a[92:95], v[24:27], v[56:59]\n"
  "v_mfma_f32_16x16x32_bf16 v[60:63], a[92:95], v[28:31], v[60:63]\n"
  "v_mfma_f32_16x16x32_bf16 v[64:67], v[24:27], a[164:167], v[64:67]\n"
  "v_mfma_f32_16x16x32_bf16 v[68:71], v[28:31], a[164:167], v[68:71]\n"
  "s_waitcnt lgkmcnt(5)\n"
  "v_mfma_f32_16x16x32_bf16 v[48:51], a[24:27], v[32:35], v[48:51]\n"
  "ds_read_b128 v[24:27], v11 offset:16384\n"
  "s_waitcnt lgkmcnt(5)\n"
  "v_mfma_f32_16x16x32_bf16 v[52:55], a[24:27], v[36:39], v[52:55]\n"
  "ds_read_b128 v[28:31], v11 offset:18432\n"
  "v_mfma_f32_16x16x32_bf16 v[56:59], a[96:99], v[32:35], v[56:59]\n"
  "v_mfma_f32_16x16x32_bf16 v[60:63], a[96:99], v[36:39], v[60:63]\n"
  "s_add_u32 m0, s58, 0\n"
  "s_nop 0\n"
  "global_load_lds_dwordx4 v3, s[42:43] offset:0\n"
  "v_mfma_f32_16x16x32_bf16 v[64:67], v[32:35], a[168:171], v[64:67]\n"
  "v_mfma_f32_16x16x32_bf16 v[68:71], v[36:39], a[168:171], v[68:71]\n"
  "s_waitcnt lgkmcnt(5)\n"
  "v_mfma_f32_16x16x32_bf16 v[48:51], a[28:31], v[40:43], v[48:51]\n"
  "ds_read_b128 v[32:35], v10 offset:20480\n"
  "s_waitcnt lgkmcnt(5)\n"
  "v_mfma_f32_16x16x32_bf16 v[52:55], a[28:31], v[44:47], v[52:55]\n"
  "ds_read_b128 v[36:39], v10 offset:22528\n"
  "v_mfma_f32_16x16x32_bf16 v[56:59], a[100:103], v[40:43], v[56:59]\n"
  "v_mfma_f32_16x16x32_bf16 v[60:63], a[100:103], v[44:47], v[60:63]\n"
  "s_add_u32 m0, s58, 3968\n"
  "s_nop 0\n"
  "global_load_lds_dwordx4 v3, s[42:43] offset:128\n"
  "v_mfma_f32_16x16x32_bf16 v[64:67], v[40:43], a[172:175], v[64:67]\n"
  "v_mfma_f32_16x16x32_bf16 v[68:71], v[44:47], a[172:175], v[68:71]\n"
  "s_waitcnt lgkmcnt(5)\n"
  "v_mfma_f32_16x16x32_bf16 v[48:51], a[32:35], v[16:19], v[48:51]\n"
  "ds_read_b128 v[40:43], v11 offset:20480\n"
  "s_waitcnt lgkmcnt(5)\n"
  "v_mfma_f32_16x16x32_bf16 v[52:55], a[32:35], v[20:23], v[52:55]\n"
  "ds_read_b128 v[44:47], v11 offset:22528\n"
  "v_mfma_f32_16x16x32_bf16 v[56:59], a[104:107], v[16:19], v[56:59]\n"
  "v_mfma_f32_16x16x32_bf16 v[60:63], a[104:107], v[20:23], v[60:63]\n"
  "s_add_u32 m0, s58, 7936\n"
  "s_nop 0\n"
  "global_load_lds_dwordx4 v3, s[42:43] offset:256\n"
  "v_mfma_f32_16x16x32_bf16 v[64:67], v[16:19], a[176:179], v[64:67]\n"
  "v_mfma_f32_16x16x32_bf16 v[68:71], v[20:23], a[176:179], v[68:71]\n"
  "s_waitcnt lgkmcnt(5)\n"
  "v_mfma_f32_16x16x32_bf16 v[48:51], a[36:39], v[24:27], v[48:51]\n"
  "ds_read_b128 v[16:19], v10 offset:24576\n"
  "s_waitcnt lgkmcnt(5)\n"
  "v_mfma_f32_16x16x32_bf16 v[52:55], a[36:39], v[28:31], v[52:55]\n"
  "ds_read_b128 v[20:23], v10 offset:26624\n"
  "v_mfma_f32_16x16x32_bf16 v[56:59], a[108:111], v[24:27], v[56:59]\n"
  "v_mfma_f32_16x16x32_bf16 v[60:63], a[108:111], v[28:31], v[60:63]\n"
  "s_add_u32 m0, s58, 11904\n"
  "s_nop 0\n"
  "global_load_lds_dwordx4 v3, s[42:43] offset:384\n"
  "v_mfma_f32_16x16x32_bf16 v[64:67], v[24:27], a[180:183], v[64:67]\n"
  "v_mfma_f32_16x16x32_bf16 v[68:71], v[28:31], a[180:183], v[68:71]\n"
  "s_waitcnt lgkmcnt(5)\n"
  "v_mfma_f32_16x16x32_bf16 v[48:51], a[40:43], v[32:35], v[48:51]\n"
  "ds_read_b128 v[24:27], v11 offset:24576\n"
  "s_waitcnt lgkmcnt(5)\n"
  "v_mfma_f32_16x16x32_bf16 v[52:55], a[40:43], v[36:39], v[52:55]\n"
  "ds_read_b128 v[28:31], v11 offset:26624\n"
  "v_mfma_f32_16x16x32_bf16 v[56:59], a[112:115], v[32:35], v[56:59]\n"
  "v_mfma_f32_16x16x32_bf16 v[60:63], a[112:115], v[36:39], v[60:63]\n"
  "s_add_u32 m0, s58, 15872\n"
  "s_nop 0\n"
  "global_load_lds_dwordx4 v3, s[42:43] offset:512\n"
  "v_mfma_f32_16x16x32_bf16 v[64:67], v[32:35], a[184:187], v[64:67]\n"
  "v_mfma_f32_16x16x32_bf16 v[68:71], v[36:39], a[184:187], v[68:71]\n"
  "s_waitcnt lgkmcnt(5)\n"
  "v_mfma_f32_16x16x32_bf16 v[48:51], a[44:47], v[40:43], v[48:51]\n"
  "ds_read_b128 v[32:35], v10 offset:28672\n"
  "s_waitcnt lgkmcnt(5)\n"
  "v_mfma_f32_16x16x32_bf16 v[52:55], a[44:47], v[44:47], v[52:55]\n"
  "ds_read_b128 v[36:39], v10 offset:30720\n"
  "v_mfma_f32_16x16x32_bf16 v[56:59], a[116:119], v[40:43], v[56:59]\n"
  "v_mfma_f32_16x16x32_bf16 v[60:63], a[116:119], v[44:47], v[60:63]\n"
  "s_add_u32 m0, s58, 19840\n"
  "s_nop 0\n"
  "global_load_lds_dwordx4 v3, s[42:43] offset:640\n"
  "v_mfma_f32_16x16x32_bf16 v[64:67], v[40:43], a[188:191], v[64:67]\n"
  "v_mfma_f32_16x16x32_bf16 v[68:71], v[44:47], a[188:191], v[68:71]\n"
  "s_waitcnt lgkmcnt(5)\n"
  "v_mfma_f32_16x16x32_bf16 v[48:51], a[48:51], v[16:19], v[48:51]\n"
  "ds_read_b128 v[40:43], v11 offset:28672\n"
  "s_waitcnt lgkmcnt(5)\n"
  "v_mfma_f32_16x16x32_bf16 v[52:55], a[48:51], v[20:23], v[52:55]\n"
  "ds_read_b128 v[44:47], v11 offset:30720\n"
  "v_mfma_f32_16x16x32_bf16 v[56:59], a[120:123], v[16:19], v[56:59]\n"
  "v_mfma_f32_16x16x32_bf16 v[60:63], a[120:123], v[20:23], v[60:63]\n"
  "s_add_u32 m0, s58, 23808\n"
  "s_nop 0\n"
  "global_load_lds_dwordx4 v3, s[42:43] offset:768\n"
  "v_mfma_f32_16x16x32_bf16 v[64:67], v[16:19], a[192:195], v[64:67]\n"
  "v_mfma_f32_16x16x32_bf16 v[68:71], v[20:23], a[192:195], v[68:71]\n"
  "s_waitcnt lgkmcnt(5)\n"
  "v_mfma_f32_16x16x32_bf16 v[48:51], a[52:55], v[24:27], v[48:51]\n"
  "ds_read_b128 v[16:19], v10 offset:32768\n"
  "s_waitcnt lgkmcnt(5)\n"
  "v_mfma_f32_16x16x32_bf16 v[52:55], a[52:55], v[28:31], v[52:55]\n"
  "ds_read_b128 v[20:23], v10 offset:34816\n"
  "v_mfma_f32_16x16x32_bf16 v[56:59], a[124:127], v[24:27], v[56:59]\n"
  "v_mfma_f32_16x16x32_bf16 v[60:63], a[124:127], v[28:31], v[60:63]\n"
  "s_add_u32 m0, s58, 27776\n"
  "s_nop 0\n"
  "global_load_lds_dwordx4 v3, s[42:43] offset:896\n"
  "v_mfma_f32_16x16x32_bf16 v[64:67], v[24:27], a[196:199], v[64:67]\n"
  "v_mfma_f32_16x16x32_bf16 v[68:71], v[28:31], a[196:199], v[68:71]\n"
  "s_waitcnt lgkmcnt(5)\n"
  "v_mfma_f32_16x16x32_bf16 v[48:51], a[56:59], v[32:35], v[48:51]\n"
  "ds_read_b128 v[24:27], v11 offset:32768\n"
  "s_waitcnt lgkmcnt(5)\n"
  "v_mfma_f32_16x16x32_bf16 v[52:55], a[56:59], v[36:39], v[52:55]\n"
  "ds_read_b128 v[28:31], v11 offset:34816\n"
  "v_mfma_f32_16x16x32_bf16 v[56:59], a[128:131], v[32:35], v[56:59]\n"
  "v_mfma_f32_16x16x32_bf16 v[60:63], a[128:131], v[36:39], v[60:63]\n"
  "s_add_u32 m0, s58, 31744\n"
  "s_nop 0\n"
  "global_load_lds_dwordx4 v3, s[42:43] offset:1024\n"
  "v_mfma_f32_16x16x32_bf16 v[64:67], v[32:35], a[200:203], v[64:67]\n"
  "v_mfma_f32_16x16x32_bf16 v[68:71], v[36:39], a[200:203], v[68:71]\n"
  "s_waitcnt vmcnt(9)\n"
  "s_barrier\n"
  "s_waitcnt lgkmcnt(5)\n"
  "v_mfma_f32_16x16x32_bf16 v[48:51], a[60:63], v[40:43], v[48:51]\n"
  "ds_read_b128 v[32:35], v12 offset:0\n"
  "s_waitcnt lgkmcnt(5)\n"
  "v_mfma_f32_16x16x32_bf16 v[52:55], a[60:63], v[44:47], v[52:55]\n"
  "ds_read_b128 v[36:39], v12 offset:2048\n"
  "v_mfma_f32_16x16x32_bf16 v[56:59], a[132:135], v[40:43], v[56:59]\n"
  "s_bitcmp1_b64 s[4:5], s56\n"
  "s_cselect_b32 s74, 8192, 0\n"
  "v_mfma_f32_16x16x32_bf16 v[60:63], a[132:135], v[44:47], v[60:63]\n"
  "s_and_b32 s75, s56, 63\n"
  "s_lshl_b32 s75, s75, 21\n"
  "v_mfma_f32_16x16x32_bf16 v[64:67], v[40:43], a[204:207], v[64:67]\n"
  "s_add_u32 s74, s74, s75\n"
  "s_add_u32 s74, s74, 20971520\n"
  "v_mfma_f32_16x16x32_bf16 v[68:71], v[44:47], a[204:207], v[68:71]\n"
  "s_xor_b32 s75, s74, s44\n"
  "s_add_u32 s60, s36, s75\n"
  "s_waitcnt lgkmcnt(5)\n"
  "v_mfma_f32_16x16x32_bf16 v[48:51], a[64:67], v[16:19], v[48:51]\n"
  "ds_read_b128 v[40:43], v13 offset:0\n"
  "s_waitcnt lgkmcnt(5)\n"
  "v_mfma_f32_16x16x32_bf16 v[52:55], a[64:67], v[20:23], v[52:55]\n"
  "ds_read_b128 v[44:47], v13 offset:2048\n"
  "v_mfma_f32_16x16x32_bf16 v[56:59], a[136:139], v[16:19], v[56:59]\n"
  "s_addc_u32 s61, s37, 0\n"
  "s_xor_b32 s75, s74, s45\n"
  "v_mfma_f32_16x16x32_bf16 v[60:63], a[136:139], v[20:23], v[60:63]\n"
  "s_add_u32 s62, s36, s75\n"
  "s_addc_u32 s63, s37, 0\n"
  "v_mfma_f32_16x16x32_bf16 v[64:67], v[16:19], a[208:211], v[64:67]\n"
  "v_mfma_f32_16x16x32_bf16 v[68:71], v[20:23], a[208:211], v[68:71]\n"
  "s_waitcnt lgkmcnt(5)\n"
  "v_mfma_f32_16x16x32_bf16 v[48:51], a[68:71], v[24:27], v[48:51]\n"
  "ds_read_b128 v[16:19], v12 offset:4096\n"
  "s_waitcnt lgkmcnt(5)\n"
  "v_mfma_f32_16x16x32_bf16 v[52:55], a[68:71], v[28:31], v[52:55]\n"
  "ds_read_b128 v[20:23], v12 offset:6144\n"
  "v_mfma_f32_16x16x32_bf16 v[56:59], a[140:143], v[24:27], v[56:59]\n"
  "v_mfma_f32_16x16x32_bf16 v[60:63], a[140:143], v[28:31], v[60:63]\n"
  "v_mfma_f32_16x16x32_bf16 v[64:67], v[24:27], a[212:215], v[64:67]\n"
  "v_mfma_f32_16x16x32_bf16 v[68:71], v[28:31], a[212:215], v[68:71]\n"
  "s_add_u32 s56, s56, 1\n"
  "v_mul_f32 v48, v8, v48\n"
  "v_mul_f32 v49, v8, v49\n"
  "v_mul_f32 v50, v8, v50\n"
  "v_mul_f32 v51, v8, v51\n"
  "v_cvt_pk_bf16_f32 v96, v48, v49\n"
  "v_cvt_pk_bf16_f32 v97, v50, v51\n"
  "v_mul_f32 v56, v8, v56\n"
  "v_mul_f32 v57, v8, v57\n"
  "v_mul_f32 v58, v8, v58\n"
  "v_mul_f32 v59, v8, v59\n"
  "v_cvt_pk_bf16_f32 v98, v56, v57\n"
  "v_cvt_pk_bf16_f32 v99, v58, v59\n"
  "global_store_dwordx4 v0, v[96:99], s[60:61] offset:0 sc1\n"
  "v_mul_f32 v52, v8, v52\n"
  "v_mul_f32 v53, v8, v53\n"
  "v_mul_f32 v54, v8, v54\n"
  "v_mul_f32 v55, v8, v55\n"
  "v_cvt_pk_bf16_f32 v100, v52, v53\n"
  "v_cvt_pk_bf16_f32 v101, v54, v55\n"
  "v_mul_f32 v60, v8, v60\n"
  "v_mul_f32 v61, v8, v61\n"
  "v_mul_f32 v62, v8, v62\n"
  "v_mul_f32 v63, v8, v63\n"
  "v_cvt_pk_bf16_f32 v102, v60, v61\n"
  "v_cvt_pk_bf16_f32 v103, v62, v63\n"
  "global_store_dwordx4 v0, v[100:103], s[60:61] offset:1024 sc1\n"
  "v_cvt_pk_bf16_f32 v104, v64, v65\n"
  "v_cvt_pk_bf16_f32 v105, v66, v67\n"
  "v_cvt_pk_bf16_f32 v106, v68, v69\n"
  "v_cvt_pk_bf16_f32 v107, v70, v71\n"
  "global_store_dwordx4 v0, v[104:107], s[62:63] offset:0 sc1\n"
  "s_waitcnt vmcnt(0)\n"
  "s_barrier\n"
  "v_mov_b32 v6, 0\n"
  "v_mov_b32 v7, s56\n"
  "s_cmp_eq_u32 s47, 3\n"
  "s_cselect_b64 exec, 1, 0\n"
  "global_store_dword v6, v7, s[64:65] sc1\n"
  "s_mov_b64 exec, -1\n"
  "v_mov_b64 v[10:11], v[12:13]\n"
  "s_waitcnt lgkmcnt(5)\n"
  "v_mfma_f32_16x16x32_bf16 v[72:75], a[0:3], v[32:35], 0\n"
  "ds_read_b128 v[24:27], v11 offset:4096\n"
  "s_waitcnt lgkmcnt(5)\n"
  "v_mfma_f32_16x16x32_bf16 v[76:79], a[0:3], v[36:39], 0\n"
  "ds_read_b128 v[28:31], v11 offset:6144\n"
  "v_mfma_f32_16x16x32_bf16 v[80:83], a[72:75], v[32:35], 0\n"
  "s_add_u32 s57, s57, 36864\n"
  "s_cmp_eq_u32 s57, 159744\n"
  "v_mfma_f32_16x16x32_bf16 v[84:87], a[72:75], v[36:39], 0\n"
  "s_cselect_b32 s57, 12288, s57\n"
  "v_add_u32 v12, s57, v9\n"
  "v_mfma_f32_16x16x32_bf16 v[88:91], v[32:35], a[144:147], 0\n"
  "v_xor_b32 v13, 64, v12\n"
  "s_add_u32 s58, s57, 36864\n"
  "v_mfma_f32_16x16x32_bf16 v[92:95], v[36:39], a[144:147], 0\n"
  "s_cmp_eq_u32 s57, 122880\n"
  "s_cselect_b32 s58, 12288, s58\n"
  "s_waitcnt lgkmcnt(5)\n"
  "v_mfma_f32_16x16x32_bf16 v[72:75], a[4:7], v[40:43], v[72:75]\n"
  "ds_read_b128 v[32:35], v10 offset:8192\n"
  "s_waitcnt lgkmcnt(5)\n"
  "v_mfma_f32_16x16x32_bf16 v[76:79], a[4:7], v[44:47], v[76:79]\n"
  "ds_read_b128 v[36:39], v10 offset:10240\n"
  "v_mfma_f32_16x16x32_bf16 v[80:83], a[76:79], v[40:43], v[80:83]\n"
  "s_add_u32 s58, s58, s80\n"
  "s_cmp_lt_u32 s56, 62\n"
  "v_mfma_f32_16x16x32_bf16 v[84:87], a[76:79], v[44:47], v[84:87]\n"
  "s_cselect_b32 s74, 36864, 0\n"
  "s_add_u32 s42, s42, s74\n"
  "v_mfma_f32_16x16x32_bf16 v[88:91], v[40:43], a[148:151], v[88:91]\n"
  "s_addc_u32 s43, s43, 0\n"
  "v_mfma_f32_16x16x32_bf16 v[92:95], v[44:47], a[148:151], v[92:95]\n"
  "s_waitcnt lgkmcnt(5)\n"
  "v_mfma_f32_16x16x32_bf16 v[72:75], a[8:11], v[16:19], v[72:75]\n"
  "ds_read_b128 v[40:43], v11 offset:8192\n"
  "s_waitcnt lgkmcnt(5)\n"
  "v_mfma_f32_16x16x32_bf16 v[76:79], a[8:11], v[20:23], v[76:79]\n"
  "ds_read_b128 v[44:47], v11 offset:10240\n"
  "v_mfma_f32_16x16x32_bf16 v[80:83], a[80:83], v[16:19], v[80:83]\n"
  "v_mfma_f32_16x16x32_bf16 v[84:87], a[80:83], v[20:23], v[84:87]\n"
  "v_mfma_f32_16x16x32_bf16 v[88:91], v[16:19], a[152:155], v[88:91]\n"
  "v_mfma_f32_16x16x32_bf16 v[92:95], v[20:23], a[152:155], v[92:95]\n"
  "s_waitcnt lgkmcnt(5)\n"
  "v_mfma_f32_16x16x32_bf16 v[72:75], a[12:15], v[24:27], v[72:75]\n"
  "ds_read_b128 v[16:19], v10 offset:12288\n"
  "s_waitcnt lgkmcnt(5)\n"
  "v_mfma_f32_16x16x32_bf16 v[76:79], a[12:15], v[28:31], v[76:79]\n"
  "ds_read_b128 v[20:23], v10 offset:14336\n"
  "v_mfma_f32_16x16x32_bf16 v[80:83], a[84:87], v[24:27], v[80:83]\n"
  "v_mfma_f32_16x16x32_bf16 v[84:87], a[84:87], v[28:31], v[84:87]\n"
  "v_mfma_f32_16x16x32_bf16 v[88:91], v[24:27], a[156:159], v[88:91]\n"
  "v_mfma_f32_16x16x32_bf16 v[92:95], v[28:31], a[156:159], v[92:95]\n"
  "s_waitcnt lgkmcnt(5)\n"
  "v_mfma_f32_16x16x32_bf16 v[72:75], a[16:19], v[32:35], v[72:75]\n"
  "ds_read_b128 v[24:27], v11 offset:12288\n"
  "s_waitcnt lgkmcnt(5)\n"
  "v_mfma_f32_16x16x32_bf16 v[76:79], a[16:19], v[36:39], v[76:79]\n"
  "ds_read_b128 v[28:31], v11 offset:14336\n"
  "v_mfma_f32_16x16x32_bf16 v[80:83], a[88:91], v[32:35], v[80:83]\n"
  "v_mfma_f32_16x16x32_bf16 v[84:87], a[88:91], v[36:39], v[84:87]\n"
  "v_mfma_f32_16x16x32_bf16 v[88:91], v[32:35], a[160:163], v[88:91]\n"
  "v_mfma_f32_16x16x32_bf16 v[92:95], v[36:39], a[160:163], v[92:95]\n"
  "s_waitcnt lgkmcnt(5)\n"
  "v_mfma_f32_16x16x32_bf16 v[72:75], a[20:23], v[40:43], v[72:75]\n"
  "ds_read_b128 v[32:35], v10 offset:16384\n"
  "s_waitcnt lgkmcnt(5)\n"
  "v_mfma_f32_16x16x32_bf16 v[76:79], a[20:23], v[44:47], v[76:79]\n"
  "ds_read_b128 v[36:39], v10 offset:18432\n"
  "v_mfma_f32_16x16x32_bf16 v[80:83], a[92:95], v[40:43], v[80:83]\n"
  "v_mfma_f32_16x16x32_bf16 v[84:87], a[92:95], v[44:47], v[84:87]\n"
  "v_mfma_f32_16x16x32_bf16 v[88:91], v[40:43], a[164:167], v[88:91]\n"
  "v_mfma_f32_16x16x32_bf16 v[92:95], v[44:47], a[164:167], v[92:95]\n"
  "s_waitcnt lgkmcnt(5)\n"
  "v_mfma_f32_16x16x32_bf16 v[72:75], a[24:27], v[16:19], v[72:75]\n"
  "ds_read_b128 v[40:43], v11 offset:16384\n"
  "s_waitcnt lgkmcnt(5)\n"
  "v_mfma_f32_16x16x32_bf16 v[76:79], a[24:27], v[20:23], v[76:79]\n"
  "ds_read_b128 v[44:47], v11 offset:18432\n"
  "v_mfma_f32_16x16x32_bf16 v[80:83], a[96:99], v[16:19], v[80:83]\n"
  "v_mfma_f32_16x16x32_bf16 v[84:87], a[96:99], v[20:23], v[84:87]\n"
  "s_add_u32 m0, s58, 0\n"
  "s_nop 0\n"
  "global_load_lds_dwordx4 v3, s[42:43] offset:0\n"
  "v_mfma_f32_16x16x32_bf16 v[88:91], v[16:19], a[168:171], v[88:91]\n"
  "v_mfma_f32_16x16x32_bf16 v[92:95], v[20:23], a[168:171], v[92:95]\n"
  "s_waitcnt lgkmcnt(5)\n"
  "v_mfma_f32_16x16x32_bf16 v[72:75], a[28:31], v[24:27], v[72:75]\n"
  "ds_read_b128 v[16:19], v10 offset:20480\n"
  "s_waitcnt lgkmcnt(5)\n"
  "v_mfma_f32_16x16x32_bf16 v[76:79], a[28:31], v[28:31], v[76:79]\n"
  "ds_read_b128 v[20:23], v10 offset:22528\n"
  "v_mfma_f32_16x16x32_bf16 v[80:83], a[100:103], v[24:27], v[80:83]\n"
  "v_mfma_f32_16x16x32_bf16 v[84:87], a[100:103], v[28:31], v[84:87]\n"
  "s_add_u32 m0, s58, 3968\n"
  "s_nop 0\n"
  "global_load_lds_dwordx4 v3, s[42:43] offset:128\n"
  "v_mfma_f32_16x16x32_bf16 v[88:91], v[24:27], a[172:175], v[88:91]\n"
  "v_mfma_f32_16x16x32_bf16 v[92:95], v[28:31], a[172:175], v[92:95]\n"
  "s_waitcnt lgkmcnt(5)\n"
  "v_mfma_f32_16x16x32_bf16 v[72:75], a[32:35], v[32:35], v[72:75]\n"
  "ds_read_b128 v[24:27], v11 offset:20480\n"
  "s_waitcnt lgkmcnt(5)\n"
  "v_mfma_f32_16x16x32_bf16 v[76:79], a[32:35], v[36:39], v[76:79]\n"
  "ds_read_b128 v[28:31], v11 offset:22528\n"
  "v_mfma_f32_16x16x32_bf16 v[80:83], a[104:107], v[32:35], v[80:83]\n"
  "v_mfma_f32_16x16x32_bf16 v[84:87], a[104:107], v[36:39], v[84:87]\n"
  "s_add_u32 m0, s58, 7936\n"
  "s_nop 0\n"
  "global_load_lds_dwordx4 v3, s[42:43] offset:256\n"
  "v_mfma_f32_16x16x32_bf16 v[88:91], v[32:35], a[176:179], v[88:91]\n"
  "v_mfma_f32_16x16x32_bf16 v[92:95], v[36:39], a[176:179], v[92:95]\n"
  "s_waitcnt lgkmcnt(5)\n"
  "v_mfma_f32_16x16x32_bf16 v[72:75], a[36:39], v[40:43], v[72:75]\n"
  "ds_read_b128 v[32:35], v10 offset:24576\n"
  "s_waitcnt lgkmcnt(5)\n"
  "v_mfma_f32_16x16x32_bf16 v[76:79], a[36:39], v[44:47], v[76:79]\n"
  "ds_read_b128 v[36:39], v10 offset:26624\n"
  "v_mfma_f32_16x16x32_bf16 v[80:83], a[108:111], v[40:43], v[80:83]\n"
  "v_mfma_f32_16x16x32_bf16 v[84:87], a[108:111], v[44:47], v[84:87]\n"
  "s_add_u32 m0, s58, 11904\n"
  "s_nop 0\n"
  "global_load_lds_dwordx4 v3, s[42:43] offset:384\n"
  "v_mfma_f32_16x16x32_bf16 v[88:91], v[40:43], a[180:183], v[88:91]\n"
  "v_mfma_f32_16x16x32_bf16 v[92:95], v[44:47], a[180:183], v[92:95]\n"
  "s_waitcnt lgkmcnt(5)\n"
  "v_mfma_f32_16x16x32_bf16 v[72:75], a[40:43], v[16:19], v[72:75]\n"
  "ds_read_b128 v[40:43], v11 offset:24576\n"
  "s_waitcnt lgkmcnt(5)\n"
  "v_mfma_f32_16x16x32_bf16 v[76:79], a[40:43], v[20:23], v[76:79]\n"
  "ds_read_b128 v[44:47], v11 offset:26624\n"
  "v_mfma_f32_16x16x32_bf16 v[80:83], a[112:115], v[16:19], v[80:83]\n"
  "v_mfma_f32_16x16x32_bf16 v[84:87], a[112:115], v[20:23], v[84:87]\n"
  "s_add_u32 m0, s58, 15872\n"
  "s_nop 0\n"
  "global_load_lds_dwordx4 v3, s[42:43] offset:512\n"
  "v_mfma_f32_16x16x32_bf16 v[88:91], v[16:19], a[184:187], v[88:91]\n"
  "v_mfma_f32_16x16x32_bf16 v[92:95], v[20:23], a[184:187], v[92:95]\n"
  "s_waitcnt lgkmcnt(5)\n"
  "v_mfma_f32_16x16x32_bf16 v[72:75], a[44:47], v[24:27], v[72:75]\n"
  "ds_read_b128 v[16:19], v10 offset:28672\n"
  "s_waitcnt lgkmcnt(5)\n"
  "v_mfma_f32_16x16x32_bf16 v[76:79], a[44:47], v[28:31], v[76:79]\n"
  "ds_read_b128 v[20:23], v10 offset:30720\n"
  "v_mfma_f32_16x16x32_bf16 v[80:83], a[116:119], v[24:27], v[80:83]\n"
  "v_mfma_f32_16x16x32_bf16 v[84:87], a[116:119], v[28:31], v[84:87]\n"
  "s_add_u32 m0, s58, 19840\n"
  "s_nop 0\n"
  "global_load_lds_dwordx4 v3, s[42:43] offset:640\n"
  "v_mfma_f32_16x16x32_bf16 v[88:91], v[24:27], a[188:191], v[88:91]\n"
  "v_mfma_f32_16x16x32_bf16 v[92:95], v[28:31], a[188:191], v[92:95]\n"
  "s_waitcnt lgkmcnt(5)\n"
  "v_mfma_f32_16x16x32_bf16 v[72:75], a[48:51], v[32:35], v[72:75]\n"
  "ds_read_b128 v[24:27], v11 offset:28672\n"
  "s_waitcnt lgkmcnt(5)\n"
  "v_mfma_f32_16x16x32_bf16 v[76:79], a[48:51], v[36:39], v[76:79]\n"
  "ds_read_b128 v[28:31], v11 offset:30720\n"
  "v_mfma_f32_16x16x32_bf16 v[80:83], a[120:123], v[32:35], v[80:83]\n"
  "v_mfma_f32_16x16x32_bf16 v[84:87], a[120:123], v[36:39], v[84:87]\n"
  "s_add_u32 m0, s58, 23808\n"
  "s_nop 0\n"
  "global_load_lds_dwordx4 v3, s[42:43] offset:768\n"
  "v_mfma_f32_16x16x32_bf16 v[88:91], v[32:35], a[192:195], v[88:91]\n"
  "v_mfma_f32_16x16x32_bf16 v[92:95], v[36:39], a[192:195], v[92:95]\n"
  "s_waitcnt lgkmcnt(5)\n"
  "v_mfma_f32_16x16x32_bf16 v[72:75], a[52:55], v[40:43], v[72:75]\n"
  "ds_read_b128 v[32:35], v10 offset:32768\n"
  "s_waitcnt lgkmcnt(5)\n"
  "v_mfma_f32_16x16x32_bf16 v[76:79], a[52:55], v[44:47], v[76:79]\n"
  "ds_read_b128 v[36:39], v10 offset:34816\n"
  "v_mfma_f32_16x16x32_bf16 v[80:83], a[124:127], v[40:43], v[80:83]\n"
  "v_mfma_f32_16x16x32_bf16 v[84:87], a[124:127], v[44:47], v[84:87]\n"
  "s_add_u32 m0, s58, 27776\n"
  "s_nop 0\n"
  "global_load_lds_dwordx4 v3, s[42:43] offset:896\n"
  "v_mfma_f32_16x16x32_bf16 v[88:91], v[40:43], a[196:199], v[88:91]\n"
  "v_mfma_f32_16x16x32_bf16 v[92:95], v[44:47], a[196:199], v[92:95]\n"
  "s_waitcnt lgkmcnt(5)\n"
  "v_mfma_f32_16x16x32_bf16 v[72:75], a[56:59], v[16:19], v[72:75]\n"
  "ds_read_b128 v[40:43], v11 offset:32768\n"
  "s_waitcnt lgkmcnt(5)\n"
  "v_mfma_f32_16x16x32_bf16 v[76:79], a[56:59], v[20:23], v[76:79]\n"
  "ds_read_b128 v[44:47], v11 offset:34816\n"
  "v_mfma_f32_16x16x32_bf16 v[80:83], a[128:131], v[16:19], v[80:83]\n"
  "v_mfma_f32_16x16x32_bf16 v[84:87], a[128:131], v[20:23], v[84:87]\n"
  "s_add_u32 m0, s58, 31744\n"
  "s_nop 0\n"
  "global_load_lds_dwordx4 v3, s[42:43] offset:1024\n"
  "v_mfma_f32_16x16x32_bf16 v[88:91], v[16:19], a[200:203], v[88:91]\n"
  "v_mfma_f32_16x16x32_bf16 v[92:95], v[20:23], a[200:203], v[92:95]\n"
  "s_waitcnt vmcnt(9)\n"
  "s_barrier\n"
  "s_waitcnt lgkmcnt(5)\n"
  "v_mfma_f32_16x16x32_bf16 v[72:75], a[60:63], v[24:27], v[72:75]\n"
  "ds_read_b128 v[16:19], v12 offset:0\n"
  "s_waitcnt lgkmcnt(5)\n"
  "v_mfma_f32_16x16x32_bf16 v[76:79], a[60:63], v[28:31], v[76:79]\n"
  "ds_read_b128 v[20:23], v12 offset:2048\n"
  "v_mfma_f32_16x16x32_bf16 v[80:83], a[132:135], v[24:27], v[80:83]\n"
  "s_bitcmp1_b64 s[4:5], s56\n"
  "s_cselect_b32 s74, 8192, 0\n"
  "v_mfma_f32_16x16x32_bf16 v[84:87], a[132:135], v[28:31], v[84:87]\n"
  "s_and_b32 s75, s56, 63\n"
  "s_lshl_b32 s75, s75, 21\n"
  "v_mfma_f32_16x16x32_bf16 v[88:91], v[24:27], a[204:207], v[88:91]\n"
  "s_add_u32 s74, s74, s75\n"
  "s_add_u32 s74, s74, 20971520\n"
  "v_mfma_f32_16x16x32_bf16 v[92:95], v[28:31], a[204:207], v[92:95]\n"
  "s_xor_b32 s75, s74, s44\n"
  "s_add_u32 s60, s36, s75\n"
  "s_waitcnt lgkmcnt(5)\n"
  "v_mfma_f32_16x16x32_bf16 v[72:75], a[64:67], v[32:35], v[72:75]\n"
  "ds_read_b128 v[24:27], v13 offset:0\n"
  "s_waitcnt lgkmcnt(5)\n"
  "v_mfma_f32_16x16x32_bf16 v[76:79], a[64:67], v[36:39], v[76:79]\n"
  "ds_read_b128 v[28:31], v13 offset:2048\n"
  "v_mfma_f32_16x16x32_bf16 v[80:83], a[136:139], v[32:35], v[80:83]\n"
  "s_addc_u32 s61, s37, 0\n"
  "s_xor_b32 s75, s74, s45\n"
  "v_mfma_f32_16x16x32_bf16 v[84:87], a[136:139], v[36:39], v[84:87]\n"
  "s_add_u32 s62, s36, s75\n"
  "s_addc_u32 s63, s37, 0\n"
  "v_mfma_f32_16x16x32_bf16 v[88:91], v[32:35], a[208:211], v[88:91]\n"
  "v_mfma_f32_16x16x32_bf16 v[92:95], v[36:39], a[208:211], v[92:95]\n"
  "s_waitcnt lgkmcnt(5)\n"
  "v_mfma_f32_16x16x32_bf16 v[72:75], a[68:71], v[40:43], v[72:75]\n"
  "ds_read_b128 v[32:35], v12 offset:4096\n"
  "s_waitcnt lgkmcnt(5)\n"
  "v_mfma_f32_16x16x32_bf16 v[76:79], a[68:71], v[44:47], v[76:79]\n"
  "ds_read_b128 v[36:39], v12 offset:6144\n"
  "v_mfma_f32_16x16x32_bf16 v[80:83], a[140:143], v[40:43], v[80:83]\n"
  "v_mfma_f32_16x16x32_bf16 v[84:87], a[140:143], v[44:47], v[84:87]\n"
  "v_mfma_f32_16x16x32_bf16 v[88:91], v[40:43], a[212:215], v[88:91]\n"
  "v_mfma_f32_16x16x32_bf16 v[92:95], v[44:47], a[212:215], v[92:95]\n"
  "s_add_u32 s56, s56, 1\n"
  "v_mov_b64 v[10:11], v[12:13]\n"
  "s_waitcnt lgkmcnt(5)\n"
  "v_mfma_f32_16x16x32_bf16 v[48:51], a[0:3], v[16:19], 0\n"
  "ds_read_b128 v[40:43], v11 offset:4096\n"
  "s_waitcnt lgkmcnt(5)\n"
  "v_mfma_f32_16x16x32_bf16 v[52:55], a[0:3], v[20:23], 0\n"
  "ds_read_b128 v[44:47], v11 offset:6144\n"
  "v_mfma_f32_16x16x32_bf16 v[56:59], a[72:75], v[16:19], 0\n"
  "s_add_u32 s57, s57, 36864\n"
  "s_cmp_eq_u32 s57, 159744\n"
  "v_mfma_f32_16x16x32_bf16 v[60:63], a[72:75], v[20:23], 0\n"
  "s_cselect_b32 s57, 12288, s57\n"
  "v_mul_f32 v72, v8, v72\n"
  "v_mfma_f32_16x16x32_bf16 v[64:67], v[16:19], a[144:147], 0\n"
  "v_mul_f32 v73, v8, v73\n"
  "v_mul_f32 v74, v8, v74\n"
  "v_mfma_f32_16x16x32_bf16 v[68:71], v[20:23], a[144:147], 0\n"
  "v_mul_f32 v75, v8, v75\n"
  "v_cvt_pk_bf16_f32 v96, v72, v73\n"
  "s_waitcnt lgkmcnt(5)\n"
  "v_mfma_f32_16x16x32_bf16 v[48:51], a[4:7], v[24:27], v[48:51]\n"
  "ds_read_b128 v[16:19], v10 offset:8192\n"
  "s_waitcnt lgkmcnt(5)\n"
  "v_mfma_f32_16x16x32_bf16 v[52:55], a[4:7], v[28:31], v[52:55]\n"
  "ds_read_b128 v[20:23], v10 offset:10240\n"
  "v_mfma_f32_16x16x32_bf16 v[56:59], a[76:79], v[24:27], v[56:59]\n"
  "v_cvt_pk_bf16_f32 v97, v74, v75\n"
  "v_mul_f32 v80, v8, v80\n"
  "v_mfma_f32_16x16x32_bf16 v[60:63], a[76:79], v[28:31], v[60:63]\n"
  "v_mul_f32 v81, v8, v81\n"
  "v_mul_f32 v82, v8, v82\n"
  "v_mfma_f32_16x16x32_bf16 v[64:67], v[24:27], a[148:151], v[64:67]\n"
  "v_mul_f32 v83, v8, v83\n"
  "v_cvt_pk_bf16_f32 v98, v80, v81\n"
  "v_mfma_f32_16x16x32_bf16 v[68:71], v[28:31], a[148:151], v[68:71]\n"
  "v_cvt_pk_bf16_f32 v99, v82, v83\n"
  "global_store_dwordx4 v0, v[96:99], s[60:61] offset:0 sc1\n"
  "s_waitcnt lgkmcnt(5)\n"
  "v_mfma_f32_16x16x32_bf16 v[48:51], a[8:11], v[32:35], v[48:51]\n"
  "ds_read_b128 v[24:27], v11 offset:8192\n"
  "s_waitcnt lgkmcnt(5)\n"
  "v_mfma_f32_16x16x32_bf16 v[52:55], a[8:11], v[36:39], v[52:55]\n"
  "ds_read_b128 v[28:31], v11 offset:10240\n"
  "v_mfma_f32_16x16x32_bf16 v[56:59], a[80:83], v[32:35], v[56:59]\n"
  "v_mul_f32 v76, v8, v76\n"
  "v_mul_f32 v77, v8, v77\n"
  "v_mfma_f32_16x16x32_bf16 v[60:63], a[80:83], v[36:39], v[60:63]\n"
  "v_mul_f32 v78, v8, v78\n"
  "v_mul_f32 v79, v8, v79\n"
  "v_mfma_f32_16x16x32_bf16 v[64:67], v[32:35], a[152:155], v[64:67]\n"
  "v_cvt_pk_bf16_f32 v100, v76, v77\n"
  "v_cvt_pk_bf16_f32 v101, v78, v79\n"
  "v_mfma_f32_16x16x32_bf16 v[68:71], v[36:39], a[152:155], v[68:71]\n"
  "v_mul_f32 v84, v8, v84\n"
  "v_mul_f32 v85, v8, v85\n"
  "s_waitcnt lgkmcnt(5)\n"
  "v_mfma_f32_16x16x32_bf16 v[48:51], a[12:15], v[40:43], v[48:51]\n"
  "ds_read_b128 v[32:35], v10 offset:12288\n"
  "s_waitcnt lgkmcnt(5)\n"
  "v_mfma_f32_16x16x32_bf16 v[52:55], a[12:15], v[44:47], v[52:55]\n"
  "ds_read_b128 v[36:39], v10 offset:14336\n"
  "v_mfma_f32_16x16x32_bf16 v[56:59], a[84:87], v[40:43], v[56:59]\n"
  "v_mul_f32 v86, v8, v86\n"
  "v_mul_f32 v87, v8, v87\n"
  "v_mfma_f32_16x16x32_bf16 v[60:63], a[84:87], v[44:47], v[60:63]\n"
  "v_cvt_pk_bf16_f32 v102, v84, v85\n"
  "v_cvt_pk_bf16_f32 v103, v86, v87\n"
  "v_mfma_f32_16x16x32_bf16 v[64:67], v[40:43], a[156:159], v[64:67]\n"
  "global_store_dwordx4 v0, v[100:103], s[60:61] offset:1024 sc1\n"
  "v_cvt_pk_bf16_f32 v104, v88, v89\n"
  "v_mfma_f32_16x16x32_bf16 v[68:71], v[44:47], a[156:159], v[68:71]\n"
  "v_cvt_pk_bf16_f32 v105, v90, v91\n"
  "v_cvt_pk_bf16_f32 v106, v92, v93\n"
  "s_waitcnt lgkmcnt(5)\n"
  "v_mfma_f32_16x16x32_bf16 v[48:51], a[16:19], v[16:19], v[48:51]\n"
  "ds_read_b128 v[40:43], v11 offset:12288\n"
  "s_waitcnt lgkmcnt(5)\n"
  "v_mfma_f32_16x16x32_bf16 v[52:55], a[16:19], v[20:23], v[52:55]\n"
  "ds_read_b128 v[44:47], v11 offset:14336\n"
  "v_mfma_f32_16x16x32_bf16 v[56:59], a[88:91], v[16:19], v[56:59]\n"
  "v_cvt_pk_bf16_f32 v107, v94, v95\n"
  "global_store_dwordx4 v0, v[104:107], s[62:63] offset:0 sc1\n"
  "v_mfma_f32_16x16x32_bf16 v[60:63], a[88:91], v[20:23], v[60:63]\n"
  "v_add_u32 v12, s57, v9\n"
  "v_xor_b32 v13, 64, v12\n"
  "v_mfma_f32_16x16x32_bf16 v[64:67], v[16:19], a[160:163], v[64:67]\n"
  "s_add_u32 s58, s57, 36864\n"
  "s_cmp_eq_u32 s57, 122880\n"
  "v_mfma_f32_16x16x32_bf16 v[68:71], v[20:23], a[160:163], v[68:71]\n"
  "s_cselect_b32 s58, 12288, s58\n"
  "s_add_u32 s58, s58, s80\n"
  "s_waitcnt lgkmcnt(5)\n"
  "v_mfma_f32_16x16x32_bf16 v[48:51], a[20:23], v[24:27], v[48:51]\n"
  "ds_read_b128 v[16:19], v10 offset:16384\n"
  "s_waitcnt lgkmcnt(5)\n"
  "v_mfma_f32_16x16x32_bf16 v[52:55], a[20:23], v[28:31], v[52:55]\n"
  "ds_read_b128 v[20:23], v10 offset:18432\n"
  "v_mfma_f32_16x16x32_bf16 v[56:59], a[92:95], v[24:27], v[56:59]\n"
  "s_cmp_lt_u32 s56, 62\n"
  "s_cselect_b32 s74, 36864, 0\n"
  "v_mfma_f32_16x16x32_bf16 v[60:63], a[92:95], v[28:31], v[60:63]\n"
  "s_add_u32 s42, s42, s74\n"
  "s_addc_u32 s43, s43, 0\n"
  "v_mfma_f32_16x16x32_bf16 v[64:67], v[24:27], a[164:167], v[64:67]\n"
  "v_mfma_f32_16x16x32_bf16 v[68:71], v[28:31], a[164:167], v[68:71]\n"
  "s_waitcnt lgkmcnt(5)\n"
  "v_mfma_f32_16x16x32_bf16 v[48:51], a[24:27], v[32:35], v[48:51]\n"
  "ds_read_b128 v[24:27], v11 offset:16384\n"
  "s_waitcnt lgkmcnt(5)\n"
  "v_mfma_f32_16x16x32_bf16 v[52:55], a[24:27], v[36:39], v[52:55]\n"
  "ds_read_b128 v[28:31], v11 offset:18432\n"
  "v_mfma_f32_16x16x32_bf16 v[56:59], a[96:99], v[32:35], v[56:59]\n"
  "v_mfma_f32_16x16x32_bf16 v[60:63], a[96:99], v[36:39], v[60:63]\n"
  "s_add_u32 m0, s58, 0\n"
  "s_nop 0\n"
  "global_load_lds_dwordx4 v3, s[42:43] offset:0\n"
  "v_mfma_f32_16x16x32_bf16 v[64:67], v[32:35], a[168:171], v[64:67]\n"
  "v_mfma_f32_16x16x32_bf16 v[68:71], v[36:39], a[168:171], v[68:71]\n"
  "s_waitcnt lgkmcnt(5)\n"
  "v_mfma_f32_16x16x32_bf16 v[48:51], a[28:31], v[40:43], v[48:51]\n"
  "ds_read_b128 v[32:35], v10 offset:20480\n"
  "s_waitcnt lgkmcnt(5)\n"
  "v_mfma_f32_16x16x32_bf16 v[52:55], a[28:31], v[44:47], v[52:55]\n"
  "ds_read_b128 v[36:39], v10 offset:22528\n"
  "v_mfma_f32_16x16x32_bf16 v[56:59], a[100:103], v[40:43], v[56:59]\n"
  "v_mfma_f32_16x16x32_bf16 v[60:63], a[100:103], v[44:47], v[60:63]\n"
  "s_add_u32 m0, s58, 3968\n"
  "s_nop 0\n"
  "global_load_lds_dwordx4 v3, s[42:43] offset:128\n"
  "v_mfma_f32_16x16x32_bf16 v[64:67], v[40:43], a[172:175], v[64:67]\n"
  "v_mfma_f32_16x16x32_bf16 v[68:71], v[44:47], a[172:175], v[68:71]\n"
  "s_waitcnt lgkmcnt(5)\n"
  "v_mfma_f32_16x16x32_bf16 v[48:51], a[32:35], v[16:19], v[48:51]\n"
  "ds_read_b128 v[40:43], v11 offset:20480\n"
  "s_waitcnt lgkmcnt(5)\n"
  "v_mfma_f32_16x16x32_bf16 v[52:55], a[32:35], v[20:23], v[52:55]\n"
  "ds_read_b128 v[44:47], v11 offset:22528\n"
  "v_mfma_f32_16x16x32_bf16 v[56:59], a[104:107], v[16:19], v[56:59]\n"
  "v_mfma_f32_16x16x32_bf16 v[60:63], a[104:107], v[20:23], v[60:63]\n"
  "s_add_u32 m0, s58, 7936\n"
  "s_nop 0\n"
  "global_load_lds_dwordx4 v3, s[42:43] offset:256\n"
  "v_mfma_f32_16x16x32_bf16 v[64:67], v[16:19], a[176:179], v[64:67]\n"
  "v_mfma_f32_16x16x32_bf16 v[68:71], v[20:23], a[176:179], v[68:71]\n"
  "s_waitcnt lgkmcnt(5)\n"
  "v_mfma_f32_16x16x32_bf16 v[48:51], a[36:39], v[24:27], v[48:51]\n"
  "ds_read_b128 v[16:19], v10 offset:24576\n"
  "s_waitcnt lgkmcnt(5)\n"
  "v_mfma_f32_16x16x32_bf16 v[52:55], a[36:39], v[28:31], v[52:55]\n"
  "ds_read_b128 v[20:23], v10 offset:26624\n"
  "v_mfma_f32_16x16x32_bf16 v[56:59], a[108:111], v[24:27], v[56:59]\n"
  "v_mfma_f32_16x16x32_bf16 v[60:63], a[108:111], v[28:31], v[60:63]\n"
  "s_add_u32 m0, s58, 11904\n"
  "s_nop 0\n"
  "global_load_lds_dwordx4 v3, s[42:43] offset:384\n"
  "v_mfma_f32_16x16x32_bf16 v[64:67], v[24:27], a[180:183], v[64:67]\n"
  "v_mfma_f32_16x16x32_bf16 v[68:71], v[28:31], a[180:183], v[68:71]\n"
  "s_waitcnt lgkmcnt(5)\n"
  "v_mfma_f32_16x16x32_bf16 v[48:51], a[40:43], v[32:35], v[48:51]\n"
  "ds_read_b128 v[24:27], v11 offset:24576\n"
  "s_waitcnt lgkmcnt(5)\n"
  "v_mfma_f32_16x16x32_bf16 v[52:55], a[40:43], v[36:39], v[52:55]\n"
  "ds_read_b128 v[28:31], v11 offset:26624\n"
  "v_mfma_f32_16x16x32_bf16 v[56:59], a[112:115], v[32:35], v[56:59]\n"
  "v_mfma_f32_16x16x32_bf16 v[60:63], a[112:115], v[36:39], v[60:63]\n"
  "s_add_u32 m0, s58, 15872\n"
  "s_nop 0\n"
  "global_load_lds_dwordx4 v3, s[42:43] offset:512\n"
  "v_mfma_f32_16x16x32_bf16 v[64:67], v[32:35], a[184:187], v[64:67]\n"
  "v_mfma_f32_16x16x32_bf16 v[68:71], v[36:39], a[184:187], v[68:71]\n"
  "s_waitcnt lgkmcnt(5)\n"
  "v_mfma_f32_16x16x32_bf16 v[48:51], a[44:47], v[40:43], v[48:51]\n"
  "ds_read_b128 v[32:35], v10 offset:28672\n"
  "s_waitcnt lgkmcnt(5)\n"
  "v_mfma_f32_16x16x32_bf16 v[52:55], a[44:47], v[44:47], v[52:55]\n"
  "ds_read_b128 v[36:39], v10 offset:30720\n"
  "v_mfma_f32_16x16x32_bf16 v[56:59], a[116:119], v[40:43], v[56:59]\n"
  "v_mfma_f32_16x16x32_bf16 v[60:63], a[116:119], v[44:47], v[60:63]\n"
  "s_add_u32 m0, s58, 19840\n"
  "s_nop 0\n"
  "global_load_lds_dwordx4 v3, s[42:43] offset:640\n"
  "v_mfma_f32_16x16x32_bf16 v[64:67], v[40:43], a[188:191], v[64:67]\n"
  "v_mfma_f32_16x16x32_bf16 v[68:71], v[44:47], a[188:191], v[68:71]\n"
  "s_waitcnt lgkmcnt(5)\n"
  "v_mfma_f32_16x16x32_bf16 v[48:51], a[48:51], v[16:19], v[48:51]\n"
  "ds_read_b128 v[40:43], v11 offset:28672\n"
  "s_waitcnt lgkmcnt(5)\n"
  "v_mfma_f32_16x16x32_bf16 v[52:55], a[48:51], v[20:23], v[52:55]\n"
  "ds_read_b128 v[44:47], v11 offset:30720\n"
  "v_mfma_f32_16x16x32_bf16 v[56:59], a[120:123], v[16:19], v[56:59]\n"
  "v_mfma_f32_16x16x32_bf16 v[60:63], a[120:123], v[20:23], v[60:63]\n"
  "s_add_u32 m0, s58, 23808\n"
  "s_nop 0\n"
  "global_load_lds_dwordx4 v3, s[42:43] offset:768\n"
  "v_mfma_f32_16x16x32_bf16 v[64:67], v[16:19], a[192:195], v[64:67]\n"
  "v_mfma_f32_16x16x32_bf16 v[68:71], v[20:23], a[192:195], v[68:71]\n"
  "s_waitcnt lgkmcnt(5)\n"
  "v_mfma_f32_16x16x32_bf16 v[48:51], a[52:55], v[24:27], v[48:51]\n"
  "ds_read_b128 v[16:19], v10 offset:32768\n"
  "s_waitcnt lgkmcnt(5)\n"
  "v_mfma_f32_16x16x32_bf16 v[52:55], a[52:55], v[28:31], v[52:55]\n"
  "ds_read_b128 v[20:23], v10 offset:34816\n"
  "v_mfma_f32_16x16x32_bf16 v[56:59], a[124:127], v[24:27], v[56:59]\n"
  "v_mfma_f32_16x16x32_bf16 v[60:63], a[124:127], v[28:31], v[60:63]\n"
  "s_add_u32 m0, s58, 27776\n"
  "s_nop 0\n"
  "global_load_lds_dwordx4 v3, s[42:43] offset:896\n"
  "v_mfma_f32_16x16x32_bf16 v[64:67], v[24:27], a[196:199], v[64:67]\n"
  "v_mfma_f32_16x16x32_bf16 v[68:71], v[28:31], a[196:199], v[68:71]\n"
  "s_waitcnt lgkmcnt(5)\n"
  "v_mfma_f32_16x16x32_bf16 v[48:51], a[56:59], v[32:35], v[48:51]\n"
  "ds_read_b128 v[24:27], v11 offset:32768\n"
  "s_waitcnt lgkmcnt(5)\n"
  "v_mfma_f32_16x16x32_bf16 v[52:55], a[56:59], v[36:39], v[52:55]\n"
  "ds_read_b128 v[28:31], v11 offset:34816\n"
  "v_mfma_f32_16x16x32_bf16 v[56:59], a[128:131], v[32:35], v[56:59]\n"
  "v_mfma_f32_16x16x32_bf16 v[60:63], a[128:131], v[36:39], v[60:63]\n"
  "s_add_u32 m0, s58, 31744\n"
  "s_nop 0\n"
  "global_load_lds_dwordx4 v3, s[42:43] offset:1024\n"
  "v_mfma_f32_16x16x32_bf16 v[64:67], v[32:35], a[200:203], v[64:67]\n"
  "v_mfma_f32_16x16x32_bf16 v[68:71], v[36:39], a[200:203], v[68:71]\n"
  "s_waitcnt vmcnt(9)\n"
  "s_barrier\n"
  "s_waitcnt lgkmcnt(5)\n"
  "v_mfma_f32_16x16x32_bf16 v[48:51], a[60:63], v[40:43], v[48:51]\n"
  "ds_read_b128 v[32:35], v12 offset:0\n"
  "s_waitcnt lgkmcnt(5)\n"
  "v_mfma_f32_16x16x32_bf16 v[52:55], a[60:63], v[44:47], v[52:55]\n"
  "ds_read_b128 v[36:39], v12 offset:2048\n"
  "v_mfma_f32_16x16x32_bf16 v[56:59], a[132:135], v[40:43], v[56:59]\n"
  "v_mov_b32 v6, 0\n"
  "v_mov_b32 v7, s56\n"
  "s_cmp_eq_u32 s47, 3\n"
  "s_cselect_b64 exec, 1, 0\n"
  "global_store_dword v6, v7, s[64:65] sc1\n"
  "s_mov_b64 exec, -1\n"
  "v_mfma_f32_16x16x32_bf16 v[60:63], a[132:135], v[44:47], v[60:63]\n"
  "s_bitcmp1_b64 s[4:5], s56\n"
  "s_cselect_b32 s74, 8192, 0\n"
  "v_mfma_f32_16x16x32_bf16 v[64:67], v[40:43], a[204:207], v[64:67]\n"
  "s_and_b32 s75, s56, 63\n"
  "s_lshl_b32 s75, s75, 21\n"
  "v_mfma_f32_16x16x32_bf16 v[68:71], v[44:47], a[204:207], v[68:71]\n"
  "s_add_u32 s74, s74, s75\n"
  "s_add_u32 s74, s74, 20971520\n"
  "s_waitcnt lgkmcnt(5)\n"
  "v_mfma_f32_16x16x32_bf16 v[48:51], a[64:67], v[16:19], v[48:51]\n"
  "ds_read_b128 v[40:43], v13 offset:0\n"
  "s_waitcnt lgkmcnt(5)\n"
  "v_mfma_f32_16x16x32_bf16 v[52:55], a[64:67], v[20:23], v[52:55]\n"
  "ds_read_b128 v[44:47], v13 offset:2048\n"
  "v_mfma_f32_16x16x32_bf16 v[56:59], a[136:139], v[16:19], v[56:59]\n"
  "s_xor_b32 s75, s74, s44\n"
  "s_add_u32 s60, s36, s75\n"
  "v_mfma_f32_16x16x32_bf16 v[60:63], a[136:139], v[20:23], v[60:63]\n"
  "s_addc_u32 s61, s37, 0\n"
  "s_xor_b32 s75, s74, s45\n"
  "v_mfma_f32_16x16x32_bf16 v[64:67], v[16:19], a[208:211], v[64:67]\n"
  "s_add_u32 s62, s36, s75\n"
  "s_addc_u32 s63, s37, 0\n"
  "v_mfma_f32_16x16x32_bf16 v[68:71], v[20:23], a[208:211], v[68:71]\n"
  "s_waitcnt lgkmcnt(5)\n"
  "v_mfma_f32_16x16x32_bf16 v[48:51], a[68:71], v[24:27], v[48:51]\n"
  "ds_read_b128 v[16:19], v12 offset:4096\n"
  "s_waitcnt lgkmcnt(5)\n"
  "v_mfma_f32_16x16x32_bf16 v[52:55], a[68:71], v[28:31], v[52:55]\n"
  "ds_read_b128 v[20:23], v12 offset:6144\n"
  "v_mfma_f32_16x16x32_bf16 v[56:59], a[140:143], v[24:27], v[56:59]\n"
  "v_mfma_f32_16x16x32_bf16 v[60:63], a[140:143], v[28:31], v[60:63]\n"
  "v_mfma_f32_16x16x32_bf16 v[64:67], v[24:27], a[212:215], v[64:67]\n"
  "v_mfma_f32_16x16x32_bf16 v[68:71], v[28:31], a[212:215], v[68:71]\n"
  "s_add_u32 s56, s56, 1\n"
  "PJL:\n"
  "v_mov_b64 v[10:11], v[12:13]\n"
  "s_waitcnt lgkmcnt(5)\n"
  "v_mfma_f32_16x16x32_bf16 v[72:75], a[0:3], v[32:35], 0\n"
  "ds_read_b128 v[24:27], v11 offset:4096\n"
  "s_waitcnt lgkmcnt(5)\n"
  "v_mfma_f32_16x16x32_bf16 v[76:79], a[0:3], v[36:39], 0\n"
  "ds_read_b128 v[28:31], v11 offset:6144\n"
  "v_mfma_f32_16x16x32_bf16 v[80:83], a[72:75], v[32:35], 0\n"
  "s_add_u32 s57, s57, 36864\n"
  "s_cmp_eq_u32 s57, 159744\n"
  "v_mfma_f32_16x16x32_bf16 v[84:87], a[72:75], v[36:39], 0\n"
  "s_cselect_b32 s57, 12288, s57\n"
  "v_mul_f32 v48, v8, v48\n"
  "v_mfma_f32_16x16x32_bf16 v[88:91], v[32:35], a[144:147], 0\n"
  "v_mul_f32 v49, v8, v49\n"
  "v_mul_f32 v50, v8, v50\n"
  "v_mfma_f32_16x16x32_bf16 v[92:95], v[36:39], a[144:147], 0\n"
  "v_mul_f32 v51, v8, v51\n"
  "v_cvt_pk_bf16_f32 v96, v48, v49\n"
  "s_waitcnt lgkmcnt(5)\n"
  "v_mfma_f32_16x16x32_bf16 v[72:75], a[4:7], v[40:43], v[72:75]\n"
  "ds_read_b128 v[32:35], v10 offset:8192\n"
  "s_waitcnt lgkmcnt(5)\n"
  "v_mfma_f32_16x16x32_bf16 v[76:79], a[4:7], v[44:47], v[76:79]\n"
  "ds_read_b128 v[36:39], v10 offset:10240\n"
  "v_mfma_f32_16x16x32_bf16 v[80:83], a[76:79], v[40:43], v[80:83]\n"
  "v_cvt_pk_bf16_f32 v97, v50, v51\n"
  "v_mul_f32 v56, v8, v56\n"
  "v_mfma_f32_16x16x32_bf16 v[84:87], a[76:79], v[44:47], v[84:87]\n"
  "v_mul_f32 v57, v8, v57\n"
  "v_mul_f32 v58, v8, v58\n"
  "v_mfma_f32_16x16x32_bf16 v[88:91], v[40:43], a[148:151], v[88:91]\n"
  "v_mul_f32 v59, v8, v59\n"
  "v_cvt_pk_bf16_f32 v98, v56, v57\n"
  "v_mfma_f32_16x16x32_bf16 v[92:95], v[44:47], a[148:151], v[92:95]\n"
  "v_cvt_pk_bf16_f32 v99, v58, v59\n"
  "global_store_dwordx4 v0, v[96:99], s[60:61] offset:0 sc1\n"
  "s_waitcnt lgkmcnt(5)\n"
  "v_mfma_f32_16x16x32_bf16 v[72:75], a[8:11], v[16:19], v[72:75]\n"
  "ds_read_b128 v[40:43], v11 offset:8192\n"
  "s_waitcnt lgkmcnt(5)\n"
  "v_mfma_f32_16x16x32_bf16 v[76:79], a[8:11], v[20:23], v[76:79]\n"
  "ds_read_b128 v[44:47], v11 offset:10240\n"
  "v_mfma_f32_16x16x32_bf16 v[80:83], a[80:83], v[16:19], v[80:83]\n"
  "v_mul_f32 v52, v8, v52\n"
  "v_mul_f32 v53, v8, v53\n"
  "v_mfma_f32_16x16x32_bf16 v[84:87], a[80:83], v[20:23], v[84:87]\n"
  "v_mul_f32 v54, v8, v54\n"
  "v_mul_f32 v55, v8, v55\n"
  "v_mfma_f32_16x16x32_bf16 v[88:91], v[16:19], a[152:155], v[88:91]\n"
  "v_cvt_pk_bf16_f32 v100, v52, v53\n"
  "v_cvt_pk_bf16_f32 v101, v54, v55\n"
  "v_mfma_f32_16x16x32_bf16 v[92:95], v[20:23], a[152:155], v[92:95]\n"
  "v_mul_f32 v60, v8, v60\n"
  "v_mul_f32 v61, v8, v61\n"
  "s_waitcnt lgkmcnt(5)\n"
  "v_mfma_f32_16x16x32_bf16 v[72:75], a[12:15], v[24:27], v[72:75]\n"
  "ds_read_b128 v[16:19], v10 offset:12288\n"
  "s_waitcnt lgkmcnt(5)\n"
  "v_mfma_f32_16x16x32_bf16 v[76:79], a[12:15], v[28:31], v[76:79]\n"
  "ds_read_b128 v[20:23], v10 offset:14336\n"
  "v_mfma_f32_16x16x32_bf16 v[80:83], a[84:87], v[24:27], v[80:83]\n"
  "v_mul_f32 v62, v8, v62\n"
  "v_mul_f32 v63, v8, v63\n"
  "v_mfma_f32_16x16x32_bf16 v[84:87], a[84:87], v[28:31], v[84:87]\n"
  "v_cvt_pk_bf16_f32 v102, v60, v61\n"
  "v_cvt_pk_bf16_f32 v103, v62, v63\n"
  "v_mfma_f32_16x16x32_bf16 v[88:91], v[24:27], a[156:159], v[88:91]\n"
  "global_store_dwordx4 v0, v[100:103], s[60:61] offset:1024 sc1\n"
  "v_cvt_pk_bf16_f32 v104, v64, v65\n"
  "v_mfma_f32_16x16x32_bf16 v[92:95], v[28:31], a[156:159], v[92:95]\n"
  "v_cvt_pk_bf16_f32 v105, v66, v67\n"
  "v_cvt_pk_bf16_f32 v106, v68, v69\n"
  "s_waitcnt lgkmcnt(5)\n"
  "v_mfma_f32_16x16x32_bf16 v[72:75], a[16:19], v[32:35], v[72:75]\n"
  "ds_read_b128 v[24:27], v11 offset:12288\n"
  "s_waitcnt lgkmcnt(5)\n"
  "v_mfma_f32_16x16x32_bf16 v[76:79], a[16:19], v[36:39], v[76:79]\n"
  "ds_read_b128 v[28:31], v11 offset:14336\n"
  "v_mfma_f32_16x16x32_bf16 v[80:83], a[88:91], v[32:35], v[80:83]\n"
  "v_cvt_pk_bf16_f32 v107, v70, v71\n"
  "global_store_dwordx4 v0, v[104:107], s[62:63] offset:0 sc1\n"
  "v_mfma_f32_16x16x32_bf16 v[84:87], a[88:91], v[36:39], v[84:87]\n"
  "v_add_u32 v12, s57, v9\n"
  "v_xor_b32 v13, 64, v12\n"
  "v_mfma_f32_16x16x32_bf16 v[88:91], v[32:35], a[160:163], v[88:91]\n"
  "s_add_u32 s58, s57, 36864\n"
  "s_cmp_eq_u32 s57, 122880\n"
  "v_mfma_f32_16x16x32_bf16 v[92:95], v[36:39], a[160:163], v[92:95]\n"
  "s_cselect_b32 s58, 12288, s58\n"
  "s_add_u32 s58, s58, s80\n"
  "s_waitcnt lgkmcnt(5)\n"
  "v_mfma_f32_16x16x32_bf16 v[72:75], a[20:23], v[40:43], v[72:75]\n"
  "ds_read_b128 v[32:35], v10 offset:16384\n"
  "s_waitcnt lgkmcnt(5)\n"
  "v_mfma_f32_16x16x32_bf16 v[76:79], a[20:23], v[44:47], v[76:79]\n"
  "ds_read_b128 v[36:39], v10 offset:18432\n"
  "v_mfma_f32_16x16x32_bf16 v[80:83], a[92:95], v[40:43], v[80:83]\n"
  "s_cmp_lt_u32 s56, 62\n"
  "s_cselect_b32 s74, 36864, 0\n"
  "v_mfma_f32_16x16x32_bf16 v[84:87], a[92:95], v[44:47], v[84:87]\n"
  "s_add_u32 s42, s42, s74\n"
  "s_addc_u32 s43, s43, 0\n"
  "v_mfma_f32_16x16x32_bf16 v[88:91], v[40:43], a[164:167], v[88:91]\n"
  "v_mfma_f32_16x16x32_bf16 v[92:95], v[44:47], a[164:167], v[92:95]\n"
  "s_waitcnt lgkmcnt(5)\n"
  "v_mfma_f32_16x16x32_bf16 v[72:75], a[24:27], v[16:19], v[72:75]\n"
  "ds_read_b128 v[40:43], v11 offset:16384\n"
  "s_waitcnt lgkmcnt(5)\n"
  "v_mfma_f32_16x16x32_bf16 v[76:79], a[24:27], v[20:23], v[76:79]\n"
  "ds_read_b128 v[44:47], v11 offset:18432\n"
  "v_mfma_f32_16x16x32_bf16 v[80:83], a[96:99], v[16:19], v[80:83]\n"
  "v_mfma_f32_16x16x32_bf16 v[84:87], a[96:99], v[20:23], v[84:87]\n"
  "s_add_u32 m0, s58, 0\n"
  "s_nop 0\n"
  "global_load_lds_dwordx4 v3, s[42:43] offset:0\n"
  "v_mfma_f32_16x16x32_bf16 v[88:91], v[16:19], a[168:171], v[88:91]\n"
  "v_mfma_f32_16x16x32_bf16 v[92:95], v[20:23], a[168:171], v[92:95]\n"
  "s_waitcnt lgkmcnt(5)\n"
  "v_mfma_f32_16x16x32_bf16 v[72:75], a[28:31], v[24:27], v[72:75]\n"
  "ds_read_b128 v[16:19], v10 offset:20480\n"
  "s_waitcnt lgkmcnt(5)\n"
  "v_mfma_f32_16x16x32_bf16 v[76:79], a[28:31], v[28:31], v[76:79]\n"
  "ds_read_b128 v[20:23], v10 offset:22528\n"
  "v_mfma_f32_16x16x32_bf16 v[80:83], a[100:103], v[24:27], v[80:83]\n"
  "v_mfma_f32_16x16x32_bf16 v[84:87], a[100:103], v[28:31], v[84:87]\n"
  "s_add_u32 m0, s58, 3968\n"
  "s_nop 0\n"
  "global_load_lds_dwordx4 v3, s[42:43] offset:128\n"
  "v_mfma_f32_16x16x32_bf16 v[88:91], v[24:27], a[172:175], v[88:91]\n"
  "v_mfma_f32_16x16x32_bf16 v[92:95], v[28:31], a[172:175], v[92:95]\n"
  "s_waitcnt lgkmcnt(5)\n"
  "v_mfma_f32_16x16x32_bf16 v[72:75], a[32:35], v[32:35], v[72:75]\n"
  "ds_read_b128 v[24:27], v11 offset:20480\n"
  "s_waitcnt lgkmcnt(5)\n"
  "v_mfma_f32_16x16x32_bf16 v[76:79], a[32:35], v[36:39], v[76:79]\n"
  "ds_read_b128 v[28:31], v11 offset:22528\n"
  "v_mfma_f32_16x16x32_bf16 v[80:83], a[104:107], v[32:35], v[80:83]\n"
  "v_mfma_f32_16x16x32_bf16 v[84:87], a[104:107], v[36:39], v[84:87]\n"
  "s_add_u32 m0, s58, 7936\n"
  "s_nop 0\n"
  "global_load_lds_dwordx4 v3, s[42:43] offset:256\n"
  "v_mfma_f32_16x16x32_bf16 v[88:91], v[32:35], a[176:179], v[88:91]\n"
  "v_mfma_f32_16x16x32_bf16 v[92:95], v[36:39], a[176:179], v[92:95]\n"
  "s_waitcnt lgkmcnt(5)\n"
  "v_mfma_f32_16x16x32_bf16 v[72:75], a[36:39], v[40:43], v[72:75]\n"
  "ds_read_b128 v[32:35], v10 offset:24576\n"
  "s_waitcnt lgkmcnt(5)\n"
  "v_mfma_f32_16x16x32_bf16 v[76:79], a[36:39], v[44:47], v[76:79]\n"
  "ds_read_b128 v[36:39], v10 offset:26624\n"
  "v_mfma_f32_16x16x32_bf16 v[80:83], a[108:111], v[40:43], v[80:83]\n"
  "v_mfma_f32_16x16x32_bf16 v[84:87], a[108:111], v[44:47], v[84:87]\n"
  "s_add_u32 m0, s58, 11904\n"
  "s_nop 0\n"
  "global_load_lds_dwordx4 v3, s[42:43] offset:384\n"
  "v_mfma_f32_16x16x32_bf16 v[88:91], v[40:43], a[180:183], v[88:91]\n"
  "v_mfma_f32_16x16x32_bf16 v[92:95], v[44:47], a[180:183], v[92:95]\n"
  "s_waitcnt lgkmcnt(5)\n"
  "v_mfma_f32_16x16x32_bf16 v[72:75], a[40:43], v[16:19], v[72:75]\n"
  "ds_read_b128 v[40:43], v11 offset:24576\n"
  "s_waitcnt lgkmcnt(5)\n"
  "v_mfma_f32_16x16x32_bf16 v[76:79], a[40:43], v[20:23], v[76:79]\n"
  "ds_read_b128 v[44:47], v11 offset:26624\n"
  "v_mfma_f32_16x16x32_bf16 v[80:83], a[112:115], v[16:19], v[80:83]\n"
  "v_mfma_f32_16x16x32_bf16 v[84:87], a[112:115], v[20:23], v[84:87]\n"
  "s_add_u32 m0, s58, 15872\n"
  "s_nop 0\n"
  "global_load_lds_dwordx4 v3, s[42:43] offset:512\n"
  "v_mfma_f32_16x16x32_bf16 v[88:91], v[16:19], a[184:187], v[88:91]\n"
  "v_mfma_f32_16x16x32_bf16 v[92:95], v[20:23], a[184:187], v[92:95]\n"
  "s_waitcnt lgkmcnt(5)\n"
  "v_mfma_f32_16x16x32_bf16 v[72:75], a[44:47], v[24:27], v[72:75]\n"
  "ds_read_b128 v[16:19], v10 offset:28672\n"
  "s_waitcnt lgkmcnt(5)\n"
  "v_mfma_f32_16x16x32_bf16 v[76:79], a[44:47], v[28:31], v[76:79]\n"
  "ds_read_b128 v[20:23], v10 offset:30720\n"
  "v_mfma_f32_16x16x32_bf16 v[80:83], a[116:119], v[24:27], v[80:83]\n"
  "v_mfma_f32_16x16x32_bf16 v[84:87], a[116:119], v[28:31], v[84:87]\n"
  "s_add_u32 m0, s58, 19840\n"
  "s_nop 0\n"
  "global_load_lds_dwordx4 v3, s[42:43] offset:640\n"
  "v_mfma_f32_16x16x32_bf16 v[88:91], v[24:27], a[188:191], v[88:91]\n"
  "v_mfma_f32_16x16x32_bf16 v[92:95], v[28:31], a[188:191], v[92:95]\n"
  "s_waitcnt lgkmcnt(5)\n"
  "v_mfma_f32_16x16x32_bf16 v[72:75], a[48:51], v[32:35], v[72:75]\n"
  "ds_read_b128 v[24:27], v11 offset:28672\n"
  "s_waitcnt lgkmcnt(5)\n"
  "v_mfma_f32_16x16x32_bf16 v[76:79], a[48:51], v[36:39], v[76:79]\n"
  "ds_read_b128 v[28:31], v11 offset:30720\n"
  "v_mfma_f32_16x16x32_bf16 v[80:83], a[120:123], v[32:35], v[80:83]\n"
  "v_mfma_f32_16x16x32_bf16 v[84:87], a[120:123], v[36:39], v[84:87]\n"
  "s_add_u32 m0, s58, 23808\n"
  "s_nop 0\n"
  "global_load_lds_dwordx4 v3, s[42:43] offset:768\n"
  "v_mfma_f32_16x16x32_bf16 v[88:91], v[32:35], a[192:195], v[88:91]\n"
  "v_mfma_f32_16x16x32_bf16 v[92:95], v[36:39], a[192:195], v[92:95]\n"
  "s_waitcnt lgkmcnt(5)\n"
  "v_mfma_f32_16x16x32_bf16 v[72:75], a[52:55], v[40:43], v[72:75]\n"
  "ds_read_b128 v[32:35], v10 offset:32768\n"
  "s_waitcnt lgkmcnt(5)\n"
  "v_mfma_f32_16x16x32_bf16 v[76:79], a[52:55], v[44:47], v[76:79]\n"
  "ds_read_b128 v[36:39], v10 offset:34816\n"
  "v_mfma_f32_16x16x32_bf16 v[80:83], a[124:127], v[40:43], v[80:83]\n"
  "v_mfma_f32_16x16x32_bf16 v[84:87], a[124:127], v[44:47], v[84:87]\n"
  "s_add_u32 m0, s58, 27776\n"
  "s_nop 0\n"
  "global_load_lds_dwordx4 v3, s[42:43] offset:896\n"
  "v_mfma_f32_16x16x32_bf16 v[88:91], v[40:43], a[196:199], v[88:91]\n"
  "v_mfma_f32_16x16x32_bf16 v[92:95], v[44:47], a[196:199], v[92:95]\n"
  "s_waitcnt lgkmcnt(5)\n"
  "v_mfma_f32_16x16x32_bf16 v[72:75], a[56:59], v[16:19], v[72:75]\n"
  "ds_read_b128 v[40:43], v11 offset:32768\n"
  "s_waitcnt lgkmcnt(5)\n"
  "v_mfma_f32_16x16x32_bf16 v[76:79], a[56:59], v[20:23], v[76:79]\n"
  "ds_read_b128 v[44:47], v11 offset:34816\n"
  "v_mfma_f32_16x16x32_bf16 v[80:83], a[128:131], v[16:19], v[80:83]\n"
  "v_mfma_f32_16x16x32_bf16 v[84:87], a[128:131], v[20:23], v[84:87]\n"
  "s_add_u32 m0, s58, 31744\n"
  "s_nop 0\n"
  "global_load_lds_dwordx4 v3, s[42:43] offset:1024\n"
  "v_mfma_f32_16x16x32_bf16 v[88:91], v[16:19], a[200:203], v[88:91]\n"
  "v_mfma_f32_16x16x32_bf16 v[92:95], v[20:23], a[200:203], v[92:95]\n"
  "s_waitcnt vmcnt(9)\n"
  "s_barrier\n"
  "s_waitcnt lgkmcnt(5)\n"
  "v_mfma_f32_16x16x32_bf16 v[72:75], a[60:63], v[24:27], v[72:75]\n"
  "ds_read_b128 v[16:19], v12 offset:0\n"
  "s_waitcnt lgkmcnt(5)\n"
  "v_mfma_f32_16x16x32_bf16 v[76:79], a[60:63], v[28:31], v[76:79]\n"
  "ds_read_b128 v[20:23], v12 offset:2048\n"
  "v_mfma_f32_16x16x32_bf16 v[80:83], a[132:135], v[24:27], v[80:83]\n"
  "v_mov_b32 v6, 0\n"
  "v_mov_b32 v7, s56\n"
  "s_cmp_eq_u32 s47, 3\n"
  "s_cselect_b64 exec, 1, 0\n"
  "global_store_dword v6, v7, s[64:65] sc1\n"
  "s_mov_b64 exec, -1\n"
  "v_mfma_f32_16x16x32_bf16 v[84:87], a[132:135], v[28:31], v[84:87]\n"
  "s_bitcmp1_b64 s[4:5], s56\n"
  "s_cselect_b32 s74, 8192, 0\n"
  "v_mfma_f32_16x16x32_bf16 v[88:91], v[24:27], a[204:207], v[88:91]\n"
  "s_and_b32 s75, s56, 63\n"
  "s_lshl_b32 s75, s75, 21\n"
  "v_mfma_f32_16x16x32_bf16 v[92:95], v[28:31], a[204:207], v[92:95]\n"
  "s_add_u32 s74, s74, s75\n"
  "s_add_u32 s74, s74, 20971520\n"
  "s_waitcnt lgkmcnt(5)\n"
  "v_mfma_f32_16x16x32_bf16 v[72:75], a[64:67], v[32:35], v[72:75]\n"
  "ds_read_b128 v[24:27], v13 offset:0\n"
  "s_waitcnt lgkmcnt(5)\n"
  "v_mfma_f32_16x16x32_bf16 v[76:79], a[64:67], v[36:39], v[76:79]\n"
  "ds_read_b128 v[28:31], v13 offset:2048\n"
  "v_mfma_f32_16x16x32_bf16 v[80:83], a[136:139], v[32:35], v[80:83]\n"
  "s_xor_b32 s75, s74, s44\n"
  "s_add_u32 s60, s36, s75\n"
  "v_mfma_f32_16x16x32_bf16 v[84:87], a[136:139], v[36:39], v[84:87]\n"
  "s_addc_u32 s61, s37, 0\n"
  "s_xor_b32 s75, s74, s45\n"
  "v_mfma_f32_16x16x32_bf16 v[88:91], v[32:35], a[208:211], v[88:91]\n"
  "s_add_u32 s62, s36, s75\n"
  "s_addc_u32 s63, s37, 0\n"
  "v_mfma_f32_16x16x32_bf16 v[92:95], v[36:39], a[208:211], v[92:95]\n"
  "s_waitcnt lgkmcnt(5)\n"
  "v_mfma_f32_16x16x32_bf16 v[72:75], a[68:71], v[40:43], v[72:75]\n"
  "ds_read_b128 v[32:35], v12 offset:4096\n"
  "s_waitcnt lgkmcnt(5)\n"
  "v_mfma_f32_16x16x32_bf16 v[76:79], a[68:71], v[44:47], v[76:79]\n"
  "ds_read_b128 v[36:39], v12 offset:6144\n"
  "v_mfma_f32_16x16x32_bf16 v[80:83], a[140:143], v[40:43], v[80:83]\n"
  "v_mfma_f32_16x16x32_bf16 v[84:87], a[140:143], v[44:47], v[84:87]\n"
  "v_mfma_f32_16x16x32_bf16 v[88:91], v[40:43], a[212:215], v[88:91]\n"
  "v_mfma_f32_16x16x32_bf16 v[92:95], v[44:47], a[212:215], v[92:95]\n"
  "s_add_u32 s56, s56, 1\n"
  "s_cmp_eq_u32 s56, 64\n"
  "s_cbranch_scc1 PJE\n"
  "v_mov_b64 v[10:11], v[12:13]\n"
  "s_waitcnt lgkmcnt(5)\n"
  "v_mfma_f32_16x16x32_bf16 v[48:51], a[0:3], v[16:19], 0\n"
  "ds_read_b128 v[40:43], v11 offset:4096\n"
  "s_waitcnt lgkmcnt(5)\n"
  "v_mfma_f32_16x16x32_bf16 v[52:55], a[0:3], v[20:23], 0\n"
  "ds_read_b128 v[44:47], v11 offset:6144\n"
  "v_mfma_f32_16x16x32_bf16 v[56:59], a[72:75], v[16:19], 0\n"
  "s_add_u32 s57, s57, 36864\n"
  "s_cmp_eq_u32 s57, 159744\n"
  "v_mfma_f32_16x16x32_bf16 v[60:63], a[72:75], v[20:23], 0\n"
  "s_cselect_b32 s57, 12288, s57\n"
  "v_mul_f32 v72, v8, v72\n"
  "v_mfma_f32_16x16x32_bf16 v[64:67], v[16:19], a[144:147], 0\n"
  "v_mul_f32 v73, v8, v73\n"
  "v_mul_f32 v74, v8, v74\n"
  "v_mfma_f32_16x16x32_bf16 v[68:71], v[20:23], a[144:147], 0\n"
  "v_mul_f32 v75, v8, v75\n"
  "v_cvt_pk_bf16_f32 v96, v72, v73\n"
  "s_waitcnt lgkmcnt(5)\n"
  "v_mfma_f32_16x16x32_bf16 v[48:51], a[4:7], v[24:27], v[48:51]\n"
  "ds_read_b128 v[16:19], v10 offset:8192\n"
  "s_waitcnt lgkmcnt(5)\n"
  "v_mfma_f32_16x16x32_bf16 v[52:55], a[4:7], v[28:31], v[52:55]\n"
  "ds_read_b128 v[20:23], v10 offset:10240\n"
  "v_mfma_f32_16x16x32_bf16 v[56:59], a[76:79], v[24:27], v[56:59]\n"
  "v_cvt_pk_bf16_f32 v97, v74, v75\n"
  "v_mul_f32 v80, v8, v80\n"
  "v_mfma_f32_16x16x32_bf16 v[60:63], a[76:79], v[28:31], v[60:63]\n"
  "v_mul_f32 v81, v8, v81\n"
  "v_mul_f32 v82, v8, v82\n"
  "v_mfma_f32_16x16x32_bf16 v[64:67], v[24:27], a[148:151], v[64:67]\n"
  "v_mul_f32 v83, v8, v83\n"
  "v_cvt_pk_bf16_f32 v98, v80, v81\n"
  "v_mfma_f32_16x16x32_bf16 v[68:71], v[28:31], a[148:151], v[68:71]\n"
  "v_cvt_pk_bf16_f32 v99, v82, v83\n"
  "global_store_dwordx4 v0, v[96:99], s[60:61] offset:0 sc1\n"
  "s_waitcnt lgkmcnt(5)\n"
  "v_mfma_f32_16x16x32_bf16 v[48:51], a[8:11], v[32:35], v[48:51]\n"
  "ds_read_b128 v[24:27], v11 offset:8192\n"
  "s_waitcnt lgkmcnt(5)\n"
  "v_mfma_f32_16x16x32_bf16 v[52:55], a[8:11], v[36:39], v[52:55]\n"
  "ds_read_b128 v[28:31], v11 offset:10240\n"
  "v_mfma_f32_16x16x32_bf16 v[56:59], a[80:83], v[32:35], v[56:59]\n"
  "v_mul_f32 v76, v8, v76\n"
  "v_mul_f32 v77, v8, v77\n"
  "v_mfma_f32_16x16x32_bf16 v[60:63], a[80:83], v[36:39], v[60:63]\n"
  "v_mul_f32 v78, v8, v78\n"
  "v_mul_f32 v79, v8, v79\n"
  "v_mfma_f32_16x16x32_bf16 v[64:67], v[32:35], a[152:155], v[64:67]\n"
  "v_cvt_pk_bf16_f32 v100, v76, v77\n"
  "v_cvt_pk_bf16_f32 v101, v78, v79\n"
  "v_mfma_f32_16x16x32_bf16 v[68:71], v[36:39], a[152:155], v[68:71]\n"
  "v_mul_f32 v84, v8, v84\n"
  "v_mul_f32 v85, v8, v85\n"
  "s_waitcnt lgkmcnt(5)\n"
  "v_mfma_f32_16x16x32_bf16 v[48:51], a[12:15], v[40:43], v[48:51]\n"
  "ds_read_b128 v[32:35], v10 offset:12288\n"
  "s_waitcnt lgkmcnt(5)\n"
  "v_mfma_f32_16x16x32_bf16 v[52:55], a[12:15], v[44:47], v[52:55]\n"
  "ds_read_b128 v[36:39], v10 offset:14336\n"
  "v_mfma_f32_16x16x32_bf16 v[56:59], a[84:87], v[40:43], v[56:59]\n"
  "v_mul_f32 v86, v8, v86\n"
  "v_mul_f32 v87, v8, v87\n"
  "v_mfma_f32_16x16x32_bf16 v[60:63], a[84:87], v[44:47], v[60:63]\n"
  "v_cvt_pk_bf16_f32 v102, v84, v85\n"
  "v_cvt_pk_bf16_f32 v103, v86, v87\n"
  "v_mfma_f32_16x16x32_bf16 v[64:67], v[40:43], a[156:159], v[64:67]\n"
  "global_store_dwordx4 v0, v[100:103], s[60:61] offset:1024 sc1\n"
  "v_cvt_pk_bf16_f32 v104, v88, v89\n"
  "v_mfma_f32_16x16x32_bf16 v[68:71], v[44:47], a[156:159], v[68:71]\n"
  "v_cvt_pk_bf16_f32 v105, v90, v91\n"
  "v_cvt_pk_bf16_f32 v106, v92, v93\n"
  "s_waitcnt lgkmcnt(5)\n"
  "v_mfma_f32_16x16x32_bf16 v[48:51], a[16:19], v[16:19], v[48:51]\n"
  "ds_read_b128 v[40:43], v11 offset:12288\n"
  "s_waitcnt lgkmcnt(5)\n"
  "v_mfma_f32_16x16x32_bf16 v[52:55], a[16:19], v[20:23], v[52:55]\n"
  "ds_read_b128 v[44:47], v11 offset:14336\n"
  "v_mfma_f32_16x16x32_bf16 v[56:59], a[88:91], v[16:19], v[56:59]\n"
  "v_cvt_pk_bf16_f32 v107, v94, v95\n"
  "global_store_dwordx4 v0, v[104:107], s[62:63] offset:0 sc1\n"
  "v_mfma_f32_16x16x32_bf16 v[60:63], a[88:91], v[20:23], v[60:63]\n"
  "v_add_u32 v12, s57, v9\n"
  "v_xor_b32 v13, 64, v12\n"
  "v_mfma_f32_16x16x32_bf16 v[64:67], v[16:19], a[160:163], v[64:67]\n"
  "s_add_u32 s58, s57, 36864\n"
  "s_cmp_eq_u32 s57, 122880\n"
  "v_mfma_f32_16x16x32_bf16 v[68:71], v[20:23], a[160:163], v[68:71]\n"
  "s_cselect_b32 s58, 12288, s58\n"
  "s_add_u32 s58, s58, s80\n"
  "s_waitcnt lgkmcnt(5)\n"
  "v_mfma_f32_16x16x32_bf16 v[48:51], a[20:23], v[24:27], v[48:51]\n"
  "ds_read_b128 v[16:19], v10 offset:16384\n"
  "s_waitcnt lgkmcnt(5)\n"
  "v_mfma_f32_16x16x32_bf16 v[52:55], a[20:23], v[28:31], v[52:55]\n"
  "ds_read_b128 v[20:23], v10 offset:18432\n"
  "v_mfma_f32_16x16x32_bf16 v[56:59], a[92:95], v[24:27], v[56:59]\n"
  "s_cmp_lt_u32 s56, 62\n"
  "s_cselect_b32 s74, 36864, 0\n"
  "v_mfma_f32_16x16x32_bf16 v[60:63], a[92:95], v[28:31], v[60:63]\n"
  "s_add_u32 s42, s42, s74\n"
  "s_addc_u32 s43, s43, 0\n"
  "v_mfma_f32_16x16x32_bf16 v[64:67], v[24:27], a[164:167], v[64:67]\n"
  "v_mfma_f32_16x16x32_bf16 v[68:71], v[28:31], a[164:167], v[68:71]\n"
  "s_waitcnt lgkmcnt(5)\n"
  "v_mfma_f32_16x16x32_bf16 v[48:51], a[24:27], v[32:35], v[48:51]\n"
  "ds_read_b128 v[24:27], v11 offset:16384\n"
  "s_waitcnt lgkmcnt(5)\n"
  "v_mfma_f32_16x16x32_bf16 v[52:55], a[24:27], v[36:39], v[52:55]\n"
  "ds_read_b128 v[28:31], v11 offset:18432\n"
  "v_mfma_f32_16x16x32_bf16 v[56:59], a[96:99], v[32:35], v[56:59]\n"
  "v_mfma_f32_16x16x32_bf16 v[60:63], a[96:99], v[36:39], v[60:63]\n"
  "s_add_u32 m0, s58, 0\n"
  "s_nop 0\n"
  "global_load_lds_dwordx4 v3, s[42:43] offset:0\n"
  "v_mfma_f32_16x16x32_bf16 v[64:67], v[32:35], a[168:171], v[64:67]\n"
  "v_mfma_f32_16x16x32_bf16 v[68:71], v[36:39], a[168:171], v[68:71]\n"
  "s_waitcnt lgkmcnt(5)\n"
  "v_mfma_f32_16x16x32_bf16 v[48:51], a[28:31], v[40:43], v[48:51]\n"
  "ds_read_b128 v[32:35], v10 offset:20480\n"
  "s_waitcnt lgkmcnt(5)\n"
  "v_mfma_f32_16x16x32_bf16 v[52:55], a[28:31], v[44:47], v[52:55]\n"
  "ds_read_b128 v[36:39], v10 offset:22528\n"
  "v_mfma_f32_16x16x32_bf16 v[56:59], a[100:103], v[40:43], v[56:59]\n"
  "v_mfma_f32_16x16x32_bf16 v[60:63], a[100:103], v[44:47], v[60:63]\n"
  "s_add_u32 m0, s58, 3968\n"
  "s_nop 0\n"
  "global_load_lds_dwordx4 v3, s[42:43] offset:128\n"
  "v_mfma_f32_16x16x32_bf16 v[64:67], v[40:43], a[172:175], v[64:67]\n"
  "v_mfma_f32_16x16x32_bf16 v[68:71], v[44:47], a[172:175], v[68:71]\n"
  "s_waitcnt lgkmcnt(5)\n"
  "v_mfma_f32_16x16x32_bf16 v[48:51], a[32:35], v[16:19], v[48:51]\n"
  "ds_read_b128 v[40:43], v11 offset:20480\n"
  "s_waitcnt lgkmcnt(5)\n"
  "v_mfma_f32_16x16x32_bf16 v[52:55], a[32:35], v[20:23], v[52:55]\n"
  "ds_read_b128 v[44:47], v11 offset:22528\n"
  "v_mfma_f32_16x16x32_bf16 v[56:59], a[104:107], v[16:19], v[56:59]\n"
  "v_mfma_f32_16x16x32_bf16 v[60:63], a[104:107], v[20:23], v[60:63]\n"
  "s_add_u32 m0, s58, 7936\n"
  "s_nop 0\n"
  "global_load_lds_dwordx4 v3, s[42:43] offset:256\n"
  "v_mfma_f32_16x16x32_bf16 v[64:67], v[16:19], a[176:179], v[64:67]\n"
  "v_mfma_f32_16x16x32_bf16 v[68:71], v[20:23], a[176:179], v[68:71]\n"
  "s_waitcnt lgkmcnt(5)\n"
  "v_mfma_f32_16x16x32_bf16 v[48:51], a[36:39], v[24:27], v[48:51]\n"
  "ds_read_b128 v[16:19], v10 offset:24576\n"
  "s_waitcnt lgkmcnt(5)\n"
  "v_mfma_f32_16x16x32_bf16 v[52:55], a[36:39], v[28:31], v[52:55]\n"
  "ds_read_b128 v[20:23], v10 offset:26624\n"
  "v_mfma_f32_16x16x32_bf16 v[56:59], a[108:111], v[24:27], v[56:59]\n"
  "v_mfma_f32_16x16x32_bf16 v[60:63], a[108:111], v[28:31], v[60:63]\n"
  "s_add_u32 m0, s58, 11904\n"
  "s_nop 0\n"
  "global_load_lds_dwordx4 v3, s[42:43] offset:384\n"
  "v_mfma_f32_16x16x32_bf16 v[64:67], v[24:27], a[180:183], v[64:67]\n"
  "v_mfma_f32_16x16x32_bf16 v[68:71], v[28:31], a[180:183], v[68:71]\n"
  "s_waitcnt lgkmcnt(5)\n"
  "v_mfma_f32_16x16x32_bf16 v[48:51], a[40:43], v[32:35], v[48:51]\n"
  "ds_read_b128 v[24:27], v11 offset:24576\n"
  "s_waitcnt lgkmcnt(5)\n"
  "v_mfma_f32_16x16x32_bf16 v[52:55], a[40:43], v[36:39], v[52:55]\n"
  "ds_read_b128 v[28:31], v11 offset:26624\n"
  "v_mfma_f32_16x16x32_bf16 v[56:59], a[112:115], v[32:35], v[56:59]\n"
  "v_mfma_f32_16x16x32_bf16 v[60:63], a[112:115], v[36:39], v[60:63]\n"
  "s_add_u32 m0, s58, 15872\n"
  "s_nop 0\n"
  "global_load_lds_dwordx4 v3, s[42:43] offset:512\n"
  "v_mfma_f32_16x16x32_bf16 v[64:67], v[32:35], a[184:187], v[64:67]\n"
  "v_mfma_f32_16x16x32_bf16 v[68:71], v[36:39], a[184:187], v[68:71]\n"
  "s_waitcnt lgkmcnt(5)\n"
  "v_mfma_f32_16x16x32_bf16 v[48:51], a[44:47], v[40:43], v[48:51]\n"
  "ds_read_b128 v[32:35], v10 offset:28672\n"
  "s_waitcnt lgkmcnt(5)\n"
  "v_mfma_f32_16x16x32_bf16 v[52:55], a[44:47], v[44:47], v[52:55]\n"
  "ds_read_b128 v[36:39], v10 offset:30720\n"
  "v_mfma_f32_16x16x32_bf16 v[56:59], a[116:119], v[40:43], v[56:59]\n"
  "v_mfma_f32_16x16x32_bf16 v[60:63], a[116:119], v[44:47], v[60:63]\n"
  "s_add_u32 m0, s58, 19840\n"
  "s_nop 0\n"
  "global_load_lds_dwordx4 v3, s[42:43] offset:640\n"
  "v_mfma_f32_16x16x32_bf16 v[64:67], v[40:43], a[188:191], v[64:67]\n"
  "v_mfma_f32_16x16x32_bf16 v[68:71], v[44:47], a[188:191], v[68:71]\n"
  "s_waitcnt lgkmcnt(5)\n"
  "v_mfma_f32_16x16x32_bf16 v[48:51], a[48:51], v[16:19], v[48:51]\n"
  "ds_read_b128 v[40:43], v11 offset:28672\n"
  "s_waitcnt lgkmcnt(5)\n"
  "v_mfma_f32_16x16x32_bf16 v[52:55], a[48:51], v[20:23], v[52:55]\n"
  "ds_read_b128 v[44:47], v11 offset:30720\n"
  "v_mfma_f32_16x16x32_bf16 v[56:59], a[120:123], v[16:19], v[56:59]\n"
  "v_mfma_f32_16x16x32_bf16 v[60:63], a[120:123], v[20:23], v[60:63]\n"
  "s_add_u32 m0, s58, 23808\n"
  "s_nop 0\n"
  "global_load_lds_dwordx4 v3, s[42:43] offset:768\n"
  "v_mfma_f32_16x16x32_bf16 v[64:67], v[16:19], a[192:195], v[64:67]\n"
  "v_mfma_f32_16x16x32_bf16 v[68:71], v[20:23], a[192:195], v[68:71]\n"
  "s_waitcnt lgkmcnt(5)\n"
  "v_mfma_f32_16x16x32_bf16 v[48:51], a[52:55], v[24:27], v[48:51]\n"
  "ds_read_b128 v[16:19], v10 offset:32768\n"
  "s_waitcnt lgkmcnt(5)\n"
  "v_mfma_f32_16x16x32_bf16 v[52:55], a[52:55], v[28:31], v[52:55]\n"
  "ds_read_b128 v[20:23], v10 offset:34816\n"
  "v_mfma_f32_16x16x32_bf16 v[56:59], a[124:127], v[24:27], v[56:59]\n"
  "v_mfma_f32_16x16x32_bf16 v[60:63], a[124:127], v[28:31], v[60:63]\n"
  "s_add_u32 m0, s58, 27776\n"
  "s_nop 0\n"
  "global_load_lds_dwordx4 v3, s[42:43] offset:896\n"
  "v_mfma_f32_16x16x32_bf16 v[64:67], v[24:27], a[196:199], v[64:67]\n"
  "v_mfma_f32_16x16x32_bf16 v[68:71], v[28:31], a[196:199], v[68:71]\n"
  "s_waitcnt lgkmcnt(5)\n"
  "v_mfma_f32_16x16x32_bf16 v[48:51], a[56:59], v[32:35], v[48:51]\n"
  "ds_read_b128 v[24:27], v11 offset:32768\n"
  "s_waitcnt lgkmcnt(5)\n"
  "v_mfma_f32_16x16x32_bf16 v[52:55], a[56:59], v[36:39], v[52:55]\n"
  "ds_read_b128 v[28:31], v11 offset:34816\n"
  "v_mfma_f32_16x16x32_bf16 v[56:59], a[128:131], v[32:35], v[56:59]\n"
  "v_mfma_f32_16x16x32_bf16 v[60:63], a[128:131], v[36:39], v[60:63]\n"
  "s_add_u32 m0, s58, 31744\n"
  "s_nop 0\n"
  "global_load_lds_dwordx4 v3, s[42:43] offset:1024\n"
  "v_mfma_f32_16x16x32_bf16 v[64:67], v[32:35], a[200:203], v[64:67]\n"
  "v_mfma_f32_16x16x32_bf16 v[68:71], v[36:39], a[200:203], v[68:71]\n"
  "s_waitcnt vmcnt(9)\n"
  "s_barrier\n"
  "s_waitcnt lgkmcnt(5)\n"
  "v_mfma_f32_16x16x32_bf16 v[48:51], a[60:63], v[40:43], v[48:51]\n"
  "ds_read_b128 v[32:35], v12 offset:0\n"
  "s_waitcnt lgkmcnt(5)\n"
  "v_mfma_f32_16x16x32_bf16 v[52:55], a[60:63], v[44:47], v[52:55]\n"
  "ds_read_b128 v[36:39], v12 offset:2048\n"
  "v_mfma_f32_16x16x32_bf16 v[56:59], a[132:135], v[40:43], v[56:59]\n"
  "v_mov_b32 v6, 0\n"
  "v_mov_b32 v7, s56\n"
  "s_cmp_eq_u32 s47, 3\n"
  "s_cselect_b64 exec, 1, 0\n"
  "global_store_dword v6, v7, s[64:65] sc1\n"
  "s_mov_b64 exec, -1\n"
  "v_mfma_f32_16x16x32_bf16 v[60:63], a[132:135], v[44:47], v[60:63]\n"
  "s_bitcmp1_b64 s[4:5], s56\n"
  "s_cselect_b32 s74, 8192, 0\n"
  "v_mfma_f32_16x16x32_bf16 v[64:67], v[40:43], a[204:207], v[64:67]\n"
  "s_and_b32 s75, s56, 63\n"
  "s_lshl_b32 s75, s75, 21\n"
  "v_mfma_f32_16x16x32_bf16 v[68:71], v[44:47], a[204:207], v[68:71]\n"
  "s_add_u32 s74, s74, s75\n"
  "s_add_u32 s74, s74, 20971520\n"
  "s_waitcnt lgkmcnt(5)\n"
  "v_mfma_f32_16x16x32_bf16 v[48:51], a[64:67], v[16:19], v[48:51]\n"
  "ds_read_b128 v[40:43], v13 offset:0\n"
  "s_waitcnt lgkmcnt(5)\n"
  "v_mfma_f32_16x16x32_bf16 v[52:55], a[64:67], v[20:23], v[52:55]\n"
  "ds_read_b128 v[44:47], v13 offset:2048\n"
  "v_mfma_f32_16x16x32_bf16 v[56:59], a[136:139], v[16:19], v[56:59]\n"
  "s_xor_b32 s75, s74, s44\n"
  "s_add_u32 s60, s36, s75\n"
  "v_mfma_f32_16x16x32_bf16 v[60:63], a[136:139], v[20:23], v[60:63]\n"
  "s_addc_u32 s61, s37, 0\n"
  "s_xor_b32 s75, s74, s45\n"
  "v_mfma_f32_16x16x32_bf16 v[64:67], v[16:19], a[208:211], v[64:67]\n"
  "s_add_u32 s62, s36, s75\n"
  "s_addc_u32 s63, s37, 0\n"
  "v_mfma_f32_16x16x32_bf16 v[68:71], v[20:23], a[208:211], v[68:71]\n"
  "s_waitcnt lgkmcnt(5)\n"
  "v_mfma_f32_16x16x32_bf16 v[48:51], a[68:71], v[24:27], v[48:51]\n"
  "ds_read_b128 v[16:19], v12 offset:4096\n"
  "s_waitcnt lgkmcnt(5)\n"
  "v_mfma_f32_16x16x32_bf16 v[52:55], a[68:71], v[28:31], v[52:55]\n"
  "ds_read_b128 v[20:23], v12 offset:6144\n"
  "v_mfma_f32_16x16x32_bf16 v[56:59], a[140:143], v[24:27], v[56:59]\n"
  "v_mfma_f32_16x16x32_bf16 v[60:63], a[140:143], v[28:31], v[60:63]\n"
  "v_mfma_f32_16x16x32_bf16 v[64:67], v[24:27], a[212:215], v[64:67]\n"
  "v_mfma_f32_16x16x32_bf16 v[68:71], v[28:31], a[212:215], v[68:71]\n"
  "s_add_u32 s56, s56, 1\n"
  "s_branch PJL\n"
  "PJE:\n"
  "s_waitcnt vmcnt(0) lgkmcnt(0)\n"
  "s_nop 3\n"
  "v_mov_b32 v7, v7\n"
  "s_nop 2\n"
  "v_mov_b32 v7, v7\n"
  "s_nop 2\n"
  "v_mul_f32 v72, v8, v72\n"
  "v_mul_f32 v73, v8, v73\n"
  "v_mul_f32 v74, v8, v74\n"
  "v_mul_f32 v75, v8, v75\n"
  "v_cvt_pk_bf16_f32 v96, v72, v73\n"
  "v_cvt_pk_bf16_f32 v97, v74, v75\n"
  "v_mul_f32 v80, v8, v80\n"
  "v_mul_f32 v81, v8, v81\n"
  "v_mul_f32 v82, v8, v82\n"
  "v_mul_f32 v83, v8, v83\n"
  "v_cvt_pk_bf16_f32 v98, v80, v81\n"
  "v_cvt_pk_bf16_f32 v99, v82, v83\n"
  "global_store_dwordx4 v0, v[96:99], s[60:61] offset:0 sc1\n"
  "v_mul_f32 v76, v8, v76\n"
  "v_mul_f32 v77, v8, v77\n"
  "v_mul_f32 v78, v8, v78\n"
  "v_mul_f32 v79, v8, v79\n"
  "v_cvt_pk_bf16_f32 v100, v76, v77\n"
  "v_cvt_pk_bf16_f32 v101, v78, v79\n"
  "v_mul_f32 v84, v8, v84\n"
  "v_mul_f32 v85, v8, v85\n"
  "v_mul_f32 v86, v8, v86\n"
  "v_mul_f32 v87, v8, v87\n"
  "v_cvt_pk_bf16_f32 v102, v84, v85\n"
  "v_cvt_pk_bf16_f32 v103, v86, v87\n"
  "global_store_dwordx4 v0, v[100:103], s[60:61] offset:1024 sc1\n"
  "v_cvt_pk_bf16_f32 v104, v88, v89\n"
  "v_cvt_pk_bf16_f32 v105, v90, v91\n"
  "v_cvt_pk_bf16_f32 v106, v92, v93\n"
  "v_cvt_pk_bf16_f32 v107, v94, v95\n"
  "global_store_dwordx4 v0, v[104:107], s[62:63] offset:0 sc1\n"
  "s_waitcnt vmcnt(0)\n"
  "s_barrier\n"
  "v_mov_b32 v6, 0\n"
  "v_mov_b32 v7, s56\n"
  "s_cmp_eq_u32 s47, 3\n"
  "s_cselect_b64 exec, 1, 0\n"
  "global_store_dword v6, v7, s[64:65] sc1\n"
  "s_mov_b64 exec, -1\n"
  "s_branch PJR\n"
  "PJQ:\n"
  "s_endpgm\n"
  "PJR:\n"
      : "+{v[0:15]}"(vin), "+{s[36:43]}"(sa), "+{s[44:59]}"(sb), "+{s[4:5]}"(a2) :: "a0", "a1", "a2", "a3", "a4", "a5", "a6", "a7", "a8", "a9", "a10", "a11", "a12", "a13", "a14", "a15", "a16", "a17", "a18", "a19", "a20", "a21", "a22", "a23", "a24", "a25", "a26", "a27", "a28", "a29", "a30", "a31", "a32", "a33", "a34", "a35", "a36", "a37", "a38", "a39", "a40", "a41", "a42", "a43", "a44", "a45", "a46", "a47", "a48", "a49", "a50", "a51", "a52", "a53", "a54", "a55", "a56", "a57", "a58", "a59", "a60", "a61", "a62", "a63", "a64", "a65", "a66", "a67", "a68", "a69", "a70", "a71", "a72", "a73", "a74", "a75", "a76", "a77", "a78", "a79", "a80", "a81", "a82", "a83", "a84", "a85", "a86", "a87", "a88", "a89", "a90", "a91", "a92", "a93", "a94", "a95", "a96", "a97", "a98", "a99", "a100", "a101", "a102", "a103", "a104", "a105", "a106", "a107", "a108", "a109", "a110", "a111", "a112", "a113", "a114", "a115", "a116", "a117", "a118", "a119", "a120", "a121", "a122", "a123", "a124", "a125", "a126", "a127", "a128", "a129", "a130", "a131", "a132", "a133", "a134", "a135", "a136", "a137", "a138", "a139", "a140", "a141", "a142", "a143", "a144", "a145", "a146", "a147", "a148", "a149", "a150", "a151", "a152", "a153", "a154", "a155", "a156", "a157", "a158", "a159", "a160", "a161", "a162", "a163", "a164", "a165", "a166", "a167", "a168", "a169", "a170", "a171", "a172", "a173", "a174", "a175", "a176", "a177", "a178", "a179", "a180", "a181", "a182", "a183", "a184", "a185", "a186", "a187", "a188", "a189", "a190", "a191", "a192", "a193", "a194", "a195", "a196", "a197", "a198", "a199", "a200", "a201", "a202", "a203", "a204", "a205", "a206", "a207", "a208", "a209", "a210", "a211", "a212", "a213", "a214", "a215", "s60", "s61", "s62", "s63", "s64", "s65", "s74", "s75", "s76", "s77", "s80", "v16", "v17", "v18", "v19", "v20", "v21", "v22", "v23", "v24", "v25", "v26", "v27", "v28", "v29", "v30", "v31", "v32", "v33", "v34", "v35", "v36", "v37", "v38", "v39", "v40", "v41", "v42", "v43", "v44", "v45", "v46", "v47", "v48", "v49", "v50", "v51", "v52", "v53", "v54", "v55", "v56", "v57", "v58", "v59", "v60", "v61", "v62", "v63", "v64", "v65", "v66", "v67", "v68", "v69", "v70", "v71", "v72", "v73", "v74", "v75", "v76", "v77", "v78", "v79", "v80", "v81", "v82", "v83", "v84", "v85", "v86", "v87", "v88", "v89", "v90", "v91", "v92", "v93", "v94", "v95", "v96", "v97", "v98", "v99", "v100", "v101", "v102", "v103", "v104", "v105", "v106", "v107", "vcc", "scc", "memory");
  } else if (tk >= 24 && tk < 28) { ARGS; PTR(sa, 0, p.ws);
    const int c = fd * 4 + tk - 24, grp = min(lane / 10, 5), pos = lane % 10;
    const bool real = lane < 60 && pos < 9;
    const u16* src = p.wqkv + (size_t)wave * 1728 + (grp >> 1) * DM + 36 * c + (grp & 1) * 18 + 2 * min(pos, 8);
#pragma unroll 1
    for (int b = 0; b < 3; ++b) {
      unsigned v[48];
#pragma unroll
      for (int i = 0; i < 48; ++i) v[i] = *(const gu32*)(src + (size_t)(b * 48 + i) * 4 * 1728);
#pragma unroll
      for (int i = 0; i < 48; ++i) if (lane != 60) *(LAS unsigned*)(lds + ((b * 48 + i) * 4 + wave) * 264 + lane * 4) = real ? v[i] : 0u;
    }
#pragma unroll
    for (int i = 0; i < 3; ++i) { const int r = min(tid + 256 * i, 575); *(LAS unsigned*)(lds + r * 264 + 240) = p.gate[r]; }
    wg_barrier();
    const int rt = wave & 1, hh = wave >> 1;
    constexpr int RS = 304, OI = 64 * RS;
    vin[0] = (8 * g + qq) * 264 + wave * 64 + c4 * 8; vin[1] = x_src(wave, lane); vin[2] = x_frag(lane);
    vin[3] = OI + (16 * rt + n) * 80 + hh * 40 + g * 8; vin[4] = OI + (8 * wave + (lane & 7)) * 80 + min(lane >> 3, 4) * 16;
    vin[5] = (wave >> 1) * 20480 + (lane >> 3) * 256 + ((wave & 1) * 8 + (lane & 7)) * 16; vin[6] = (lane & 31) * RS + 288;
    vin[7] = (n & ~1) - 4 * g; vin[8] = (n & 1) ? 0xffff0000u : 0xffffu;
#pragma unroll
    for (int t = 0; t < 2; ++t) { const int g4 = 8 * wave + 4 * t + g; vin[10 + t] = n * RS + 48 * (g4 / 5) + 8 * (g4 % 5); }
    vin[12] = g == 3 ? OI + 32 * 80 : (16 * rt + n) * RS + 48 * hh + 16 * g;
    vin[13] = ((16 * rt - 16 + n) & 63) * RS + 96 + 48 * hh + 16 * min(g, 2);
    vin[14] = ((16 * rt - 16 + 4 * g + qq) & 63) * RS + 192 + 48 * hh + 8 * c4;
    PTR(sa, 2, p.x);
    sb[0] = WO_OA + c * 1280; sb[1] = WO_G + c * 128; sb[2] = (C_AP) + c * 128; sb[3] = wave; sb[4] = WO_ST + (role * 4 + wave) * 4096;
    UNIFORM(sa, 8); UNIFORM(sb, 16);
    asm volatile(
  "s_nop 4\n"
  "s_lshl_b32 s80, s47, 10\n"
  "v_add_u32 v9, 0, v0\n"
  "ds_read_b64_tr_b16 a[0:1], v9 offset:0\n"
  "ds_read_b64_tr_b16 a[2:3], v9 offset:1056\n"
  "ds_read_b64_tr_b16 a[4:5], v9 offset:8448\n"
  "ds_read_b64_tr_b16 a[6:7], v9 offset:9504\n"
  "ds_read_b64_tr_b16 a[8:9], v9 offset:16896\n"
  "ds_read_b64_tr_b16 a[10:11], v9 offset:17952\n"
  "ds_read_b64_tr_b16 a[12:13], v9 offset:25344\n"
  "ds_read_b64_tr_b16 a[14:15], v9 offset:26400\n"
  "ds_read_b64_tr_b16 a[16:17], v9 offset:33792\n"
  "ds_read_b64_tr_b16 a[18:19], v9 offset:34848\n"
  "ds_read_b64_tr_b16 a[20:21], v9 offset:42240\n"
  "ds_read_b64_tr_b16 a[22:23], v9 offset:43296\n"
  "v_add_u32 v9, 32, v0\n"
  "ds_read_b64_tr_b16 a[72:73], v9 offset:0\n"
  "ds_read_b64_tr_b16 a[74:75], v9 offset:1056\n"
  "ds_read_b64_tr_b16 a[76:77], v9 offset:8448\n"
  "ds_read_b64_tr_b16 a[78:79], v9 offset:9504\n"
  "ds_read_b64_tr_b16 a[80:81], v9 offset:16896\n"
  "ds_read_b64_tr_b16 a[82:83], v9 offset:17952\n"
  "ds_read_b64_tr_b16 a[84:85], v9 offset:25344\n"
  "ds_read_b64_tr_b16 a[86:87], v9 offset:26400\n"
  "ds_read_b64_tr_b16 a[88:89], v9 offset:33792\n"
  "ds_read_b64_tr_b16 a[90:91], v9 offset:34848\n"
  "ds_read_b64_tr_b16 a[92:93], v9 offset:42240\n"
  "ds_read_b64_tr_b16 a[94:95], v9 offset:43296\n"
  "v_add_u32 v9, 50688, v0\n"
  "ds_read_b64_tr_b16 a[24:25], v9 offset:0\n"
  "ds_read_b64_tr_b16 a[26:27], v9 offset:1056\n"
  "ds_read_b64_tr_b16 a[28:29], v9 offset:8448\n"
  "ds_read_b64_tr_b16 a[30:31], v9 offset:9504\n"
  "ds_read_b64_tr_b16 a[32:33], v9 offset:16896\n"
  "ds_read_b64_tr_b16 a[34:35], v9 offset:17952\n"
  "ds_read_b64_tr_b16 a[36:37], v9 offset:25344\n"
  "ds_read_b64_tr_b16 a[38:39], v9 offset:26400\n"
  "ds_read_b64_tr_b16 a[40:41], v9 offset:33792\n"
  "ds_read_b64_tr_b16 a[42:43], v9 offset:34848\n"
  "ds_read_b64_tr_b16 a[44:45], v9 offset:42240\n"
  "ds_read_b64_tr_b16 a[46:47], v9 offset:43296\n"
  "v_add_u32 v9, 50720, v0\n"
  "ds_read_b64_tr_b16 a[96:97], v9 offset:0\n"
  "ds_read_b64_tr_b16 a[98:99], v9 offset:1056\n"
  "ds_read_b64_tr_b16 a[100:101], v9 offset:8448\n"
  "ds_read_b64_tr_b16 a[102:103], v9 offset:9504\n"
  "ds_read_b64_tr_b16 a[104:105], v9 offset:16896\n"
  "ds_read_b64_tr_b16 a[106:107], v9 offset:17952\n"
  "ds_read_b64_tr_b16 a[108:109], v9 offset:25344\n"
  "ds_read_b64_tr_b16 a[110:111], v9 offset:26400\n"
  "ds_read_b64_tr_b16 a[112:113], v9 offset:33792\n"
  "ds_read_b64_tr_b16 a[114:115], v9 offset:34848\n"
  "ds_read_b64_tr_b16 a[116:117], v9 offset:42240\n"
  "ds_read_b64_tr_b16 a[118:119], v9 offset:43296\n"
  "v_add_u32 v9, 101376, v0\n"
  "ds_read_b64_tr_b16 a[48:49], v9 offset:0\n"
  "ds_read_b64_tr_b16 a[50:51], v9 offset:1056\n"
  "ds_read_b64_tr_b16 a[52:53], v9 offset:8448\n"
  "ds_read_b64_tr_b16 a[54:55], v9 offset:9504\n"
  "ds_read_b64_tr_b16 a[56:57], v9 offset:16896\n"
  "ds_read_b64_tr_b16 a[58:59], v9 offset:17952\n"
  "ds_read_b64_tr_b16 a[60:61], v9 offset:25344\n"
  "ds_read_b64_tr_b16 a[62:63], v9 offset:26400\n"
  "ds_read_b64_tr_b16 a[64:65], v9 offset:33792\n"
  "ds_read_b64_tr_b16 a[66:67], v9 offset:34848\n"
  "ds_read_b64_tr_b16 a[68:69], v9 offset:42240\n"
  "ds_read_b64_tr_b16 a[70:71], v9 offset:43296\n"
  "v_add_u32 v9, 101408, v0\n"
  "ds_read_b64_tr_b16 a[120:121], v9 offset:0\n"
  "ds_read_b64_tr_b16 a[122:123], v9 offset:1056\n"
  "ds_read_b64_tr_b16 a[124:125], v9 offset:8448\n"
  "ds_read_b64_tr_b16 a[126:127], v9 offset:9504\n"
  "ds_read_b64_tr_b16 a[128:129], v9 offset:16896\n"
  "ds_read_b64_tr_b16 a[130:131], v9 offset:17952\n"
  "ds_read_b64_tr_b16 a[132:133], v9 offset:25344\n"
  "ds_read_b64_tr_b16 a[134:135], v9 offset:26400\n"
  "ds_read_b64_tr_b16 a[136:137], v9 offset:33792\n"
  "ds_read_b64_tr_b16 a[138:139], v9 offset:34848\n"
  "ds_read_b64_tr_b16 a[140:141], v9 offset:42240\n"
  "ds_read_b64_tr_b16 a[142:143], v9 offset:43296\n"
  "s_waitcnt lgkmcnt(0)\n"
  "s_barrier\n"
  "v_mbcnt_lo_u32_b32 v127, -1, 0\n"
  "v_mbcnt_hi_u32_b32 v127, -1, v127\n"
  "v_lshlrev_b32 v9, 4, v127\n"
  "v_add_u32 v9, s80, v9\n"
  "v_lshlrev_b32 v127, 2, v127\n"
  "v_mov_b64 v[120:121], 0\n"
  "v_mov_b64 v[122:123], 0\n"
  "v_mov_b32 v128, 0xce6e6b28\n"
  "ds_write_b128 v9, v[120:123] offset:0\n"
  "ds_write_b128 v9, v[120:123] offset:4096\n"
  "ds_write_b128 v9, v[120:123] offset:8192\n"
  "ds_write_b128 v9, v[120:123] offset:12288\n"
  "ds_write_b128 v9, v[120:123] offset:16384\n"
  "ds_write_b128 v9, v[120:123] offset:20480\n"
  "s_waitcnt lgkmcnt(0)\n"
  "s_barrier\n"
  "s_add_u32 s58, s80, 22272\n"
  "s_add_u32 m0, s58, 0\n"
  "s_nop 0\n"
  "global_load_lds_dwordx4 v1, s[38:39] offset:0\n"
  "s_add_u32 m0, s58, 3968\n"
  "s_nop 0\n"
  "global_load_lds_dwordx4 v1, s[38:39] offset:128\n"
  "s_add_u32 m0, s58, 7936\n"
  "s_nop 0\n"
  "global_load_lds_dwordx4 v1, s[38:39] offset:256\n"
  "s_add_u32 m0, s58, 11904\n"
  "s_nop 0\n"
  "global_load_lds_dwordx4 v1, s[38:39] offset:384\n"
  "s_add_u32 m0, s58, 15872\n"
  "s_nop 0\n"
  "global_load_lds_dwordx4 v1, s[38:39] offset:512\n"
  "s_add_u32 m0, s58, 19840\n"
  "s_nop 0\n"
  "global_load_lds_dwordx4 v1, s[38:39] offset:640\n"
  "s_add_u32 m0, s58, 23808\n"
  "s_nop 0\n"
  "global_load_lds_dwordx4 v1, s[38:39] offset:768\n"
  "s_add_u32 m0, s58, 27776\n"
  "s_nop 0\n"
  "global_load_lds_dwordx4 v1, s[38:39] offset:896\n"
  "s_add_u32 m0, s58, 31744\n"
  "s_nop 0\n"
  "global_load_lds_dwordx4 v1, s[38:39] offset:1024\n"
  "v_add_u32 v68, 0, v13\n"
  "v_subrev_u32 v9, 19456, v68\n"
  "v_min_u32 v68, v68, v9\n"
  "v_add_u32 v72, 0, v14\n"
  "v_subrev_u32 v9, 19456, v72\n"
  "v_min_u32 v72, v72, v9\n"
  "v_add_u32 v69, 4864, v13\n"
  "v_subrev_u32 v9, 19456, v69\n"
  "v_min_u32 v69, v69, v9\n"
  "v_add_u32 v73, 4864, v14\n"
  "v_subrev_u32 v9, 19456, v73\n"
  "v_min_u32 v73, v73, v9\n"
  "v_add_u32 v70, 9728, v13\n"
  "v_subrev_u32 v9, 19456, v70\n"
  "v_min_u32 v70, v70, v9\n"
  "v_add_u32 v74, 9728, v14\n"
  "v_subrev_u32 v9, 19456, v74\n"
  "v_min_u32 v74, v74, v9\n"
  "v_add_u32 v71, 14592, v13\n"
  "v_subrev_u32 v9, 19456, v71\n"
  "v_min_u32 v71, v71, v9\n"
  "v_add_u32 v75, 14592, v14\n"
  "v_subrev_u32 v9, 19456, v75\n"
  "v_min_u32 v75, v75, v9\n"
  "v_mov_b32 v66, v12\n"
  "v_add_u32 v67, 9728, v12\n"
  "v_min_u32 v67, 22016, v67\n"
  "v_add_u32 v9, 16, v7\n"
  "v_cmp_gt_u32 s[8:9], 15, v9\n"
  "v_add_u32 v9, 14, v7\n"
  "v_cmp_gt_u32 s[10:11], 15, v9\n"
  "v_add_u32 v9, 0, v7\n"
  "v_cmp_gt_u32 s[12:13], 15, v9\n"
  "v_add_u32 v9, -2, v7\n"
  "v_cmp_gt_u32 s[14:15], 15, v9\n"
  "s_mov_b64 s[16:17], s[8:9]\n"
  "s_mov_b64 s[18:19], s[10:11]\n"
  "s_bitcmp1_b32 s47, 0\n"
  "s_cselect_b64 s[8:9], s[8:9], 0\n"
  "s_cselect_b64 s[10:11], s[10:11], 0\n"
  "s_mov_b32 s56, 400000\n"
  "s_add_u32 s74, s36, s5\n"
  "s_addc_u32 s75, s37, 0\n"
  "BLA:\n"
  "v_mov_b32 v124, 0\n"
  "global_load_dwordx2 v[124:125], v124, s[74:75] sc1\n"
  "s_waitcnt vmcnt(0)\n"
  "v_readfirstlane_b32 s77, v124\n"
  "v_readfirstlane_b32 s76, v125\n"
  "s_cmp_eq_u32 s77, s4\n"
  "s_cbranch_scc1 BDA\n"
  "s_sub_u32 s56, s56, 1\n"
  "s_cbranch_scc1 AQ\n"
  "s_sleep 4\n"
  "s_branch BLA\n"
  "BDA:\n"
  "s_sub_u32 s77, s5, 16966656\n"
  "s_lshr_b32 s77, s77, 8\n"
  "s_lshl_b32 s77, s77, 7\n"
  "s_add_u32 s77, s77, 16777216\n"
  "s_add_u32 s74, s36, s77\n"
  "s_addc_u32 s75, s37, 0\n"
  "v_mov_b32 v124, 0\n"
  "v_mov_b32 v125, 1\n"
  "s_cmp_eq_u32 s47, 0\n"
  "s_cselect_b64 exec, 1, 0\n"
  "global_atomic_add v124, v125, s[74:75] sc1\n"
  "s_mov_b64 exec, -1\n"
  "s_and_b32 s74, s76, 2\n"
  "s_lshr_b32 s75, s74, 1\n"
  "s_lshl_b32 s4, 0x55555555, s75\n"
  "s_mov_b32 s5, s4\n"
  "s_lshl_b32 s74, s74, 12\n"
  "s_xor_b32 s46, s74, s46\n"
  "s_add_u32 s60, s36, s44\n"
  "s_addc_u32 s61, s37, 0\n"
  "s_add_u32 s62, s36, s45\n"
  "s_addc_u32 s63, s37, 0\n"
  "s_add_u32 s64, s36, s46\n"
  "s_addc_u32 s65, s37, 0\n"
  "s_cmp_eq_u32 s47, 3\n"
  "s_cselect_b32 s68, -1, 0\n"
  "s_mov_b32 s69, 0\n"
  "s_mov_b32 s70, 0xaaaaaaaa\n"
  "s_mov_b32 s71, 0xaaaaaaaa\n"
  "s_mov_b32 s56, -1\n"
  "s_waitcnt vmcnt(0)\n"
  "s_waitcnt vmcnt(2) lgkmcnt(0)\n"
  "s_barrier\n"
  "v_add_u32 v64, 22272, v2\n"
  "v_xor_b32 v65, 64, v64\n"
  "s_add_u32 s58, s80, 59136\n"
  "s_cmp_lt_i32 s56, 62\n"
  "s_cselect_b32 s74, 36864, 0\n"
  "s_add_u32 s38, s38, s74\n"
  "s_addc_u32 s39, s39, 0\n"
  "ds_read_b128 v[16:19], v64 offset:0\n"
  "ds_read_b128 v[20:23], v64 offset:2048\n"
  "ds_read_b128 v[24:27], v65 offset:0\n"
  "ds_read_b128 v[28:31], v65 offset:2048\n"
  "ds_read_b128 v[32:35], v64 offset:4096\n"
  "ds_read_b128 v[36:39], v64 offset:6144\n"
  "s_waitcnt lgkmcnt(5)\n"
  "v_mfma_f32_16x16x32_bf16 v[48:51], a[0:3], v[16:19], 0\n"
  "ds_read_b128 v[40:43], v65 offset:4096\n"
  "s_waitcnt lgkmcnt(5)\n"
  "v_mfma_f32_16x16x32_bf16 v[52:55], a[0:3], v[20:23], 0\n"
  "ds_read_b128 v[44:47], v65 offset:6144\n"
  "v_mfma_f32_16x16x32_bf16 v[56:59], a[72:75], v[16:19], 0\n"
  "v_mfma_f32_16x16x32_bf16 v[60:63], a[72:75], v[20:23], 0\n"
  "s_waitcnt lgkmcnt(5)\n"
  "v_mfma_f32_16x16x32_bf16 v[48:51], a[4:7], v[24:27], v[48:51]\n"
  "ds_read_b128 v[16:19], v64 offset:8192\n"
  "s_waitcnt lgkmcnt(5)\n"
  "v_mfma_f32_16x16x32_bf16 v[52:55], a[4:7], v[28:31], v[52:55]\n"
  "ds_read_b128 v[20:23], v64 offset:10240\n"
  "v_mfma_f32_16x16x32_bf16 v[56:59], a[76:79], v[24:27], v[56:59]\n"
  "s_add_u32 m0, s58, 0\n"
  "s_nop 0\n"
  "global_load_lds_dwordx4 v1, s[38:39] offset:0\n"
  "v_mfma_f32_16x16x32_bf16 v[60:63], a[76:79], v[28:31], v[60:63]\n"
  "s_waitcnt lgkmcnt(5)\n"
  "v_mfma_f32_16x16x32_bf16 v[48:51], a[8:11], v[32:35], v[48:51]\n"
  "ds_read_b128 v[24:27], v65 offset:8192\n"
  "s_waitcnt lgkmcnt(5)\n"
  "v_mfma_f32_16x16x32_bf16 v[52:55], a[8:11], v[36:39], v[52:55]\n"
  "ds_read_b128 v[28:31], v65 offset:10240\n"
  "v_mfma_f32_16x16x32_bf16 v[56:59], a[80:83], v[32:35], v[56:59]\n"
  "s_add_u32 m0, s58, 3968\n"
  "s_nop 0\n"
  "global_load_lds_dwordx4 v1, s[38:39] offset:128\n"
  "v_mfma_f32_16x16x32_bf16 v[60:63], a[80:83], v[36:39], v[60:63]\n"
  "s_waitcnt lgkmcnt(5)\n"
  "v_mfma_f32_16x16x32_bf16 v[48:51], a[12:15], v[40:43], v[48:51]\n"
  "ds_read_b128 v[32:35], v64 offset:12288\n"
  "s_waitcnt lgkmcnt(5)\n"
  "v_mfma_f32_16x16x32_bf16 v[52:55], a[12:15], v[44:47], v[52:55]\n"
  "ds_read_b128 v[36:39], v64 offset:14336\n"
  "v_mfma_f32_16x16x32_bf16 v[56:59], a[84:87], v[40:43], v[56:59]\n"
  "s_add_u32 m0, s58, 7936\n"
  "s_nop 0\n"
  "global_load_lds_dwordx4 v1, s[38:39] offset:256\n"
  "v_mfma_f32_16x16x32_bf16 v[60:63], a[84:87], v[44:47], v[60:63]\n"
  "s_waitcnt lgkmcnt(5)\n"
  "v_mfma_f32_16x16x32_bf16 v[48:51], a[16:19], v[16:19], v[48:51]\n"
  "ds_read_b128 v[40:43], v65 offset:12288\n"
  "s_waitcnt lgkmcnt(5)\n"
  "v_mfma_f32_16x16x32_bf16 v[52:55], a[16:19], v[20:23], v[52:55]\n"
  "ds_read_b128 v[44:47], v65 offset:14336\n"
  "v_mfma_f32_16x16x32_bf16 v[56:59], a[88:91], v[16:19], v[56:59]\n"
  "s_add_u32 m0, s58, 11904\n"
  "s_nop 0\n"
  "global_load_lds_dwordx4 v1, s[38:39] offset:384\n"
  "v_mfma_f32_16x16x32_bf16 v[60:63], a[88:91], v[20:23], v[60:63]\n"
  "s_waitcnt lgkmcnt(5)\n"
  "v_mfma_f32_16x16x32_bf16 v[48:51], a[20:23], v[24:27], v[48:51]\n"
  "ds_read_b128 v[16:19], v64 offset:16384\n"
  "s_waitcnt lgkmcnt(5)\n"
  "v_mfma_f32_16x16x32_bf16 v[52:55], a[20:23], v[28:31], v[52:55]\n"
  "ds_read_b128 v[20:23], v64 offset:18432\n"
  "v_mfma_f32_16x16x32_bf16 v[56:59], a[92:95], v[24:27], v[56:59]\n"
  "s_add_u32 m0, s58, 15872\n"
  "s_nop 0\n"
  "global_load_lds_dwordx4 v1, s[38:39] offset:512\n"
  "v_mfma_f32_16x16x32_bf16 v[60:63], a[92:95], v[28:31], v[60:63]\n"
  "s_waitcnt lgkmcnt(5)\n"
  "v_mfma_f32_16x16x32_bf16 v[48:51], a[24:27], v[32:35], v[48:51]\n"
  "ds_read_b128 v[24:27], v65 offset:16384\n"
  "s_waitcnt lgkmcnt(5)\n"
  "v_mfma_f32_16x16x32_bf16 v[52:55], a[24:27], v[36:39], v[52:55]\n"
  "ds_read_b128 v[28:31], v65 offset:18432\n"
  "v_mfma_f32_16x16x32_bf16 v[56:59], a[96:99], v[32:35], v[56:59]\n"
  "s_add_u32 m0, s58, 19840\n"
  "s_nop 0\n"
  "global_load_lds_dwordx4 v1, s[38:39] offset:640\n"
  "v_mfma_f32_16x16x32_bf16 v[60:63], a[96:99], v[36:39], v[60:63]\n"
  "s_waitcnt lgkmcnt(5)\n"
  "v_mfma_f32_16x16x32_bf16 v[48:51], a[28:31], v[40:43], v[48:51]\n"
  "ds_read_b128 v[32:35], v64 offset:20480\n"
  "s_waitcnt lgkmcnt(5)\n"
  "v_mfma_f32_16x16x32_bf16 v[52:55], a[28:31], v[44:47], v[52:55]\n"
  "ds_read_b128 v[36:39], v64 offset:22528\n"
  "v_mfma_f32_16x16x32_bf16 v[56:59], a[100:103], v[40:43], v[56:59]\n"
  "s_add_u32 m0, s58, 23808\n"
  "s_nop 0\n"
  "global_load_lds_dwordx4 v1, s[38:39] offset:768\n"
  "v_mfma_f32_16x16x32_bf16 v[60:63], a[100:103], v[44:47], v[60:63]\n"
  "s_waitcnt lgkmcnt(5)\n"
  "v_mfma_f32_16x16x32_bf16 v[48:51], a[32:35], v[16:19], v[48:51]\n"
  "ds_read_b128 v[40:43], v65 offset:20480\n"
  "s_waitcnt lgkmcnt(5)\n"
  "v_mfma_f32_16x16x32_bf16 v[52:55], a[32:35], v[20:23], v[52:55]\n"
  "ds_read_b128 v[44:47], v65 offset:22528\n"
  "v_mfma_f32_16x16x32_bf16 v[56:59], a[104:107], v[16:19], v[56:59]\n"
  "s_add_u32 m0, s58, 27776\n"
  "s_nop 0\n"
  "global_load_lds_dwordx4 v1, s[38:39] offset:896\n"
  "v_mfma_f32_16x16x32_bf16 v[60:63], a[104:107], v[20:23], v[60:63]\n"
  "s_waitcnt lgkmcnt(5)\n"
  "v_mfma_f32_16x16x32_bf16 v[48:51], a[36:39], v[24:27], v[48:51]\n"
  "ds_read_b128 v[16:19], v64 offset:24576\n"
  "s_waitcnt lgkmcnt(5)\n"
  "v_mfma_f32_16x16x32_bf16 v[52:55], a[36:39], v[28:31], v[52:55]\n"
  "ds_read_b128 v[20:23], v64 offset:26624\n"
  "v_mfma_f32_16x16x32_bf16 v[56:59], a[108:111], v[24:27], v[56:59]\n"
  "s_add_u32 m0, s58, 31744\n"
  "s_nop 0\n"
  "global_load_lds_dwordx4 v1, s[38:39] offset:1024\n"
  "v_mfma_f32_16x16x32_bf16 v[60:63], a[108:111], v[28:31], v[60:63]\n"
  "s_waitcnt lgkmcnt(5)\n"
  "v_mfma_f32_16x16x32_bf16 v[48:51], a[40:43], v[32:35], v[48:51]\n"
  "ds_read_b128 v[24:27], v65 offset:24576\n"
  "s_waitcnt lgkmcnt(5)\n"
  "v_mfma_f32_16x16x32_bf16 v[52:55], a[40:43], v[36:39], v[52:55]\n"
  "ds_read_b128 v[28:31], v65 offset:26624\n"
  "v_mfma_f32_16x16x32_bf16 v[56:59], a[112:115], v[32:35], v[56:59]\n"
  "v_mfma_f32_16x16x32_bf16 v[60:63], a[112:115], v[36:39], v[60:63]\n"
  "s_waitcnt lgkmcnt(5)\n"
  "v_mfma_f32_16x16x32_bf16 v[48:51], a[44:47], v[40:43], v[48:51]\n"
  "ds_read_b128 v[32:35], v64 offset:28672\n"
  "s_waitcnt lgkmcnt(5)\n"
  "v_mfma_f32_16x16x32_bf16 v[52:55], a[44:47], v[44:47], v[52:55]\n"
  "ds_read_b128 v[36:39], v64 offset:30720\n"
  "v_mfma_f32_16x16x32_bf16 v[56:59], a[116:119], v[40:43], v[56:59]\n"
  "v_mfma_f32_16x16x32_bf16 v[60:63], a[116:119], v[44:47], v[60:63]\n"
  "s_waitcnt lgkmcnt(5)\n"
  "v_mfma_f32_16x16x32_bf16 v[48:51], a[48:51], v[16:19], v[48:51]\n"
  "ds_read_b128 v[40:43], v65 offset:28672\n"
  "s_waitcnt lgkmcnt(5)\n"
  "v_mfma_f32_16x16x32_bf16 v[52:55], a[48:51], v[20:23], v[52:55]\n"
  "ds_read_b128 v[44:47], v65 offset:30720\n"
  "v_mfma_f32_16x16x32_bf16 v[56:59], a[120:123], v[16:19], v[56:59]\n"
  "v_mfma_f32_16x16x32_bf16 v[60:63], a[120:123], v[20:23], v[60:63]\n"
  "s_waitcnt lgkmcnt(5)\n"
  "v_mfma_f32_16x16x32_bf16 v[48:51], a[52:55], v[24:27], v[48:51]\n"
  "ds_read_b128 v[16:19], v64 offset:32768\n"
  "s_waitcnt lgkmcnt(5)\n"
  "v_mfma_f32_16x16x32_bf16 v[52:55], a[52:55], v[28:31], v[52:55]\n"
  "ds_read_b128 v[20:23], v64 offset:34816\n"
  "v_mfma_f32_16x16x32_bf16 v[56:59], a[124:127], v[24:27], v[56:59]\n"
  "v_mfma_f32_16x16x32_bf16 v[60:63], a[124:127], v[28:31], v[60:63]\n"
  "s_waitcnt lgkmcnt(5)\n"
  "v_mfma_f32_16x16x32_bf16 v[48:51], a[56:59], v[32:35], v[48:51]\n"
  "ds_read_b128 v[24:27], v65 offset:32768\n"
  "s_waitcnt lgkmcnt(5)\n"
  "v_mfma_f32_16x16x32_bf16 v[52:55], a[56:59], v[36:39], v[52:55]\n"
  "ds_read_b128 v[28:31], v65 offset:34816\n"
  "v_mfma_f32_16x16x32_bf16 v[56:59], a[128:131], v[32:35], v[56:59]\n"
  "v_mfma_f32_16x16x32_bf16 v[60:63], a[128:131], v[36:39], v[60:63]\n"
  "s_waitcnt lgkmcnt(5)\n"
  "v_mfma_f32_16x16x32_bf16 v[48:51], a[60:63], v[40:43], v[48:51]\n"
  "s_waitcnt lgkmcnt(4)\n"
  "v_mfma_f32_16x16x32_bf16 v[52:55], a[60:63], v[44:47], v[52:55]\n"
  "v_mfma_f32_16x16x32_bf16 v[56:59], a[132:135], v[40:43], v[56:59]\n"
  "v_mfma_f32_16x16x32_bf16 v[60:63], a[132:135], v[44:47], v[60:63]\n"
  "s_waitcnt lgkmcnt(3)\n"
  "v_mfma_f32_16x16x32_bf16 v[48:51], a[64:67], v[16:19], v[48:51]\n"
  "s_waitcnt lgkmcnt(2)\n"
  "v_mfma_f32_16x16x32_bf16 v[52:55], a[64:67], v[20:23], v[52:55]\n"
  "v_mfma_f32_16x16x32_bf16 v[56:59], a[136:139], v[16:19], v[56:59]\n"
  "v_mfma_f32_16x16x32_bf16 v[60:63], a[136:139], v[20:23], v[60:63]\n"
  "s_waitcnt lgkmcnt(1)\n"
  "v_mfma_f32_16x16x32_bf16 v[48:51], a[68:71], v[24:27], v[48:51]\n"
  "s_waitcnt lgkmcnt(0)\n"
  "v_mfma_f32_16x16x32_bf16 v[52:55], a[68:71], v[28:31], v[52:55]\n"
  "v_mfma_f32_16x16x32_bf16 v[56:59], a[140:143], v[24:27], v[56:59]\n"
  "v_mfma_f32_16x16x32_bf16 v[60:63], a[140:143], v[28:31], v[60:63]\n"
  "v_cvt_pk_bf16_f32 v48, v48, v49\n"
  "v_cvt_pk_bf16_f32 v49, v50, v51\n"
  "v_cvt_pk_bf16_f32 v52, v52, v53\n"
  "v_cvt_pk_bf16_f32 v53, v54, v55\n"
  "s_nop 0\n"
  "v_cvt_pk_bf16_f32 v56, v56, v57\n"
  "v_cvt_pk_bf16_f32 v57, v58, v59\n"
  "s_nop 0\n"
  "v_cvt_pk_bf16_f32 v60, v60, v61\n"
  "v_cvt_pk_bf16_f32 v61, v62, v63\n"
  "ds_write_b64 v10, v[48:49] offset:0\n"
  "ds_write_b64 v10, v[52:53] offset:4864\n"
  "ds_write_b64 v11, v[56:57] offset:0\n"
  "ds_write_b64 v11, v[60:61] offset:4864\n"
  "s_waitcnt vmcnt(0)\n"
  "s_add_u32 s56, s56, 1\n"
  "s_waitcnt vmcnt(2) lgkmcnt(0)\n"
  "s_barrier\n"
  "v_add_u32 v64, 59136, v2\n"
  "v_xor_b32 v65, 64, v64\n"
  "s_add_u32 s58, s80, 22272\n"
  "s_cmp_lt_i32 s56, 62\n"
  "s_cselect_b32 s74, 36864, 0\n"
  "s_add_u32 s38, s38, s74\n"
  "s_addc_u32 s39, s39, 0\n"
  "ds_read_b128 v[16:19], v64 offset:0\n"
  "ds_read_b128 v[20:23], v64 offset:2048\n"
  "ds_read_b128 v[24:27], v65 offset:0\n"
  "ds_read_b128 v[28:31], v65 offset:2048\n"
  "ds_read_b128 v[32:35], v64 offset:4096\n"
  "ds_read_b128 v[36:39], v64 offset:6144\n"
  "s_waitcnt lgkmcnt(5)\n"
  "v_mfma_f32_16x16x32_bf16 v[48:51], a[0:3], v[16:19], 0\n"
  "ds_read_b128 v[40:43], v65 offset:4096\n"
  "ds_read_b128 v[76:79], v66\n"
  "s_nop 0\n"
  "s_waitcnt lgkmcnt(6)\n"
  "v_mfma_f32_16x16x32_bf16 v[52:55], a[0:3], v[20:23], 0\n"
  "ds_read_b128 v[44:47], v65 offset:6144\n"
  "ds_read_b128 v[80:83], v68\n"
  "s_nop 0\n"
  "v_mfma_f32_16x16x32_bf16 v[56:59], a[72:75], v[16:19], 0\n"
  "ds_read_b128 v[84:87], v69\n"
  "s_nop 0\n"
  "v_mfma_f32_16x16x32_bf16 v[60:63], a[72:75], v[20:23], 0\n"
  "ds_read_b64_tr_b16 v[88:89], v72 offset:0\n"
  "s_nop 0\n"
  "s_waitcnt lgkmcnt(9)\n"
  "v_mfma_f32_16x16x32_bf16 v[48:51], a[4:7], v[24:27], v[48:51]\n"
  "ds_read_b128 v[16:19], v64 offset:8192\n"
  "ds_read_b64_tr_b16 v[90:91], v73 offset:0\n"
  "s_nop 0\n"
  "s_waitcnt lgkmcnt(10)\n"
  "v_mfma_f32_16x16x32_bf16 v[52:55], a[4:7], v[28:31], v[52:55]\n"
  "ds_read_b128 v[20:23], v64 offset:10240\n"
  "ds_read_b64_tr_b16 v[92:93], v72 offset:32\n"
  "s_nop 0\n"
  "v_mfma_f32_16x16x32_bf16 v[56:59], a[76:79], v[24:27], v[56:59]\n"
  "s_add_u32 m0, s58, 0\n"
  "s_nop 0\n"
  "global_load_lds_dwordx4 v1, s[38:39] offset:0\n"
  "v_mfma_f32_16x16x32_bf16 v[60:63], a[76:79], v[28:31], v[60:63]\n"
  "ds_read_b64_tr_b16 v[94:95], v73 offset:32\n"
  "s_nop 0\n"
  "s_waitcnt lgkmcnt(12)\n"
  "v_mfma_f32_16x16x32_bf16 v[48:51], a[8:11], v[32:35], v[48:51]\n"
  "ds_read_b128 v[24:27], v65 offset:8192\n"
  "ds_read_b32 v126, v6 offset:0\n"
  "s_nop 0\n"
  "s_waitcnt lgkmcnt(13)\n"
  "v_mfma_f32_16x16x32_bf16 v[52:55], a[8:11], v[36:39], v[52:55]\n"
  "ds_read_b128 v[28:31], v65 offset:10240\n"
  "s_waitcnt lgkmcnt(10)\n"
  "v_mfma_f32_16x16x32_bf16 v[96:99], v[80:83], v[76:79], 0\n"
  "v_mfma_f32_16x16x32_bf16 v[56:59], a[80:83], v[32:35], v[56:59]\n"
  "s_add_u32 m0, s58, 3968\n"
  "s_nop 0\n"
  "global_load_lds_dwordx4 v1, s[38:39] offset:128\n"
  "v_mfma_f32_16x16x32_bf16 v[60:63], a[80:83], v[36:39], v[60:63]\n"
  "s_waitcnt lgkmcnt(9)\n"
  "v_mfma_f32_16x16x32_bf16 v[100:103], v[84:87], v[76:79], 0\n"
  "v_mfma_f32_16x16x32_bf16 v[48:51], a[12:15], v[40:43], v[48:51]\n"
  "ds_read_b128 v[32:35], v64 offset:12288\n"
  "s_waitcnt lgkmcnt(2)\n"
  "v_lshlrev_b32 v126, 16, v126\n"
  "v_mfma_f32_16x16x32_bf16 v[52:55], a[12:15], v[44:47], v[52:55]\n"
  "ds_read_b128 v[36:39], v64 offset:14336\n"
  "v_mul_f32 v126, 0xbfb8aa3b, v126\n"
  "v_exp_f32 v126, v126\n"
  "v_mfma_f32_16x16x32_bf16 v[56:59], a[84:87], v[40:43], v[56:59]\n"
  "s_add_u32 m0, s58, 7936\n"
  "s_nop 0\n"
  "global_load_lds_dwordx4 v1, s[38:39] offset:256\n"
  "v_mfma_f32_16x16x32_bf16 v[60:63], a[84:87], v[44:47], v[60:63]\n"
  "v_add_f32 v126, 1.0, v126\n"
  "v_rcp_f32 v126, v126\n"
  "v_mfma_f32_16x16x32_bf16 v[48:51], a[16:19], v[16:19], v[48:51]\n"
  "ds_read_b128 v[40:43], v65 offset:12288\n"
  "v_cvt_pk_bf16_f32 v126, v126, v126\n"
  "v_lshlrev_b32 v126, 16, v126\n"
  "v_mfma_f32_16x16x32_bf16 v[52:55], a[16:19], v[20:23], v[52:55]\n"
  "ds_read_b128 v[44:47], v65 offset:14336\n"
  "v_cndmask_b32 v104, v96, v97, s[70:71]\n"
  "v_cndmask_b32 v105, v98, v99, s[70:71]\n"
  "v_mfma_f32_16x16x32_bf16 v[56:59], a[88:91], v[16:19], v[56:59]\n"
  "s_add_u32 m0, s58, 11904\n"
  "s_nop 0\n"
  "global_load_lds_dwordx4 v1, s[38:39] offset:384\n"
  "v_mfma_f32_16x16x32_bf16 v[60:63], a[88:91], v[20:23], v[60:63]\n"
  "v_cndmask_b32 v106, v100, v101, s[70:71]\n"
  "v_cndmask_b32 v107, v102, v103, s[70:71]\n"
  "v_mfma_f32_16x16x32_bf16 v[48:51], a[20:23], v[24:27], v[48:51]\n"
  "ds_read_b128 v[16:19], v64 offset:16384\n"
  "v_cvt_pk_bf16_f32 v104, v104, v105\n"
  "v_and_b32 v105, 0xffff0000, v104\n"
  "s_waitcnt lgkmcnt(5)\n"
  "v_mfma_f32_16x16x32_bf16 v[52:55], a[20:23], v[28:31], v[52:55]\n"
  "ds_read_b128 v[20:23], v64 offset:18432\n"
  "v_lshlrev_b32 v104, 16, v104\n"
  "v_cvt_pk_bf16_f32 v106, v106, v107\n"
  "v_mfma_f32_16x16x32_bf16 v[56:59], a[92:95], v[24:27], v[56:59]\n"
  "s_add_u32 m0, s58, 15872\n"
  "s_nop 0\n"
  "global_load_lds_dwordx4 v1, s[38:39] offset:512\n"
  "v_mfma_f32_16x16x32_bf16 v[60:63], a[92:95], v[28:31], v[60:63]\n"
  "v_and_b32 v107, 0xffff0000, v106\n"
  "v_lshlrev_b32 v106, 16, v106\n"
  "s_waitcnt lgkmcnt(5)\n"
  "v_mfma_f32_16x16x32_bf16 v[48:51], a[24:27], v[32:35], v[48:51]\n"
  "ds_read_b128 v[24:27], v65 offset:16384\n"
  "v_mul_f32 v104, 0x3e715bef, v104\n"
  "v_mul_f32 v105, 0x3e715bef, v105\n"
  "s_waitcnt lgkmcnt(5)\n"
  "v_mfma_f32_16x16x32_bf16 v[52:55], a[24:27], v[36:39], v[52:55]\n"
  "ds_read_b128 v[28:31], v65 offset:18432\n"
  "v_mul_f32 v106, 0x3e715bef, v106\n"
  "v_mul_f32 v107, 0x3e715bef, v107\n"
  "v_mfma_f32_16x16x32_bf16 v[56:59], a[96:99], v[32:35], v[56:59]\n"
  "s_add_u32 m0, s58, 19840\n"
  "s_nop 0\n"
  "global_load_lds_dwordx4 v1, s[38:39] offset:640\n"
  "v_mfma_f32_16x16x32_bf16 v[60:63], a[96:99], v[36:39], v[60:63]\n"
  "v_cvt_pk_bf16_f32 v104, v104, v105\n"
  "v_and_b32 v105, 0xffff0000, v104\n"
  "s_waitcnt lgkmcnt(5)\n"
  "v_mfma_f32_16x16x32_bf16 v[48:51], a[28:31], v[40:43], v[48:51]\n"
  "ds_read_b128 v[32:35], v64 offset:20480\n"
  "v_lshlrev_b32 v104, 16, v104\n"
  "v_cvt_pk_bf16_f32 v106, v106, v107\n"
  "s_waitcnt lgkmcnt(5)\n"
  "v_mfma_f32_16x16x32_bf16 v[52:55], a[28:31], v[44:47], v[52:55]\n"
  "ds_read_b128 v[36:39], v64 offset:22528\n"
  "v_and_b32 v107, 0xffff0000, v106\n"
  "v_lshlrev_b32 v106, 16, v106\n"
  "v_mfma_f32_16x16x32_bf16 v[56:59], a[100:103], v[40:43], v[56:59]\n"
  "s_add_u32 m0, s58, 23808\n"
  "s_nop 0\n"
  "global_load_lds_dwordx4 v1, s[38:39] offset:768\n"
  "v_mfma_f32_16x16x32_bf16 v[60:63], a[100:103], v[44:47], v[60:63]\n"
  "v_cndmask_b32 v104, v128, v104, s[8:9]\n"
  "v_cndmask_b32 v105, v128, v105, s[10:11]\n"
  "s_waitcnt lgkmcnt(5)\n"
  "v_mfma_f32_16x16x32_bf16 v[48:51], a[32:35], v[16:19], v[48:51]\n"
  "ds_read_b128 v[40:43], v65 offset:20480\n"
  "v_cndmask_b32 v106, v128, v106, s[12:13]\n"
  "v_cndmask_b32 v107, v128, v107, s[14:15]\n"
  "s_waitcnt lgkmcnt(5)\n"
  "v_mfma_f32_16x16x32_bf16 v[52:55], a[32:35], v[20:23], v[52:55]\n"
  "ds_read_b128 v[44:47], v65 offset:22528\n"
  "v_max3_f32 v124, v104, v105, v106\n"
  "v_max_f32 v124, v124, v107\n"
  "v_mfma_f32_16x16x32_bf16 v[56:59], a[104:107], v[16:19], v[56:59]\n"
  "s_add_u32 m0, s58, 27776\n"
  "s_nop 0\n"
  "global_load_lds_dwordx4 v1, s[38:39] offset:896\n"
  "v_mfma_f32_16x16x32_bf16 v[60:63], a[104:107], v[20:23], v[60:63]\n"
  "v_mov_b32 v125, v124\n"
  "s_nop 1\n"
  "v_permlane16_swap_b32 v124, v125\n"
  "v_max_f32 v124, v124, v125\n"
  "s_waitcnt lgkmcnt(5)\n"
  "v_mfma_f32_16x16x32_bf16 v[48:51], a[36:39], v[24:27], v[48:51]\n"
  "ds_read_b128 v[16:19], v64 offset:24576\n"
  "v_mov_b32 v125, v124\n"
  "s_nop 1\n"
  "v_permlane32_swap_b32 v124, v125\n"
  "v_max_f32 v124, v124, v125\n"
  "s_waitcnt lgkmcnt(5)\n"
  "v_mfma_f32_16x16x32_bf16 v[52:55], a[36:39], v[28:31], v[52:55]\n"
  "ds_read_b128 v[20:23], v64 offset:26624\n"
  "v_sub_f32 v104, v104, v124\n"
  "v_sub_f32 v105, v105, v124\n"
  "v_mfma_f32_16x16x32_bf16 v[56:59], a[108:111], v[24:27], v[56:59]\n"
  "s_add_u32 m0, s58, 31744\n"
  "s_nop 0\n"
  "global_load_lds_dwordx4 v1, s[38:39] offset:1024\n"
  "v_mfma_f32_16x16x32_bf16 v[60:63], a[108:111], v[28:31], v[60:63]\n"
  "v_sub_f32 v106, v106, v124\n"
  "v_sub_f32 v107, v107, v124\n"
  "s_waitcnt lgkmcnt(5)\n"
  "v_mfma_f32_16x16x32_bf16 v[48:51], a[40:43], v[32:35], v[48:51]\n"
  "ds_read_b128 v[24:27], v65 offset:24576\n"
  "v_mul_f32 v104, 0x3fb8aa3b, v104\n"
  "v_mul_f32 v105, 0x3fb8aa3b, v105\n"
  "s_waitcnt lgkmcnt(5)\n"
  "v_mfma_f32_16x16x32_bf16 v[52:55], a[40:43], v[36:39], v[52:55]\n"
  "ds_read_b128 v[28:31], v65 offset:26624\n"
  "v_mul_f32 v106, 0x3fb8aa3b, v106\n"
  "v_mul_f32 v107, 0x3fb8aa3b, v107\n"
  "v_mfma_f32_16x16x32_bf16 v[56:59], a[112:115], v[32:35], v[56:59]\n"
  "v_exp_f32 v104, v104\n"
  "v_mfma_f32_16x16x32_bf16 v[60:63], a[112:115], v[36:39], v[60:63]\n"
  "v_exp_f32 v105, v105\n"
  "s_waitcnt lgkmcnt(5)\n"
  "v_mfma_f32_16x16x32_bf16 v[48:51], a[44:47], v[40:43], v[48:51]\n"
  "ds_read_b128 v[32:35], v64 offset:28672\n"
  "v_exp_f32 v106, v106\n"
  "s_waitcnt lgkmcnt(5)\n"
  "v_mfma_f32_16x16x32_bf16 v[52:55], a[44:47], v[44:47], v[52:55]\n"
  "ds_read_b128 v[36:39], v64 offset:30720\n"
  "v_exp_f32 v107, v107\n"
  "v_mfma_f32_16x16x32_bf16 v[56:59], a[116:119], v[40:43], v[56:59]\n"
  "s_mov_b64 s[8:9], s[16:17]\n"
  "s_mov_b64 s[10:11], s[18:19]\n"
  "v_mfma_f32_16x16x32_bf16 v[60:63], a[116:119], v[44:47], v[60:63]\n"
  "v_add_f32 v124, v104, v105\n"
  "v_add_f32 v124, v124, v106\n"
  "s_waitcnt lgkmcnt(5)\n"
  "v_mfma_f32_16x16x32_bf16 v[48:51], a[48:51], v[16:19], v[48:51]\n"
  "ds_read_b128 v[40:43], v65 offset:28672\n"
  "v_add_f32 v124, v124, v107\n"
  "v_mov_b32 v125, v124\n"
  "s_nop 1\n"
  "v_permlane16_swap_b32 v124, v125\n"
  "v_add_f32 v124, v124, v125\n"
  "s_waitcnt lgkmcnt(5)\n"
  "v_mfma_f32_16x16x32_bf16 v[52:55], a[48:51], v[20:23], v[52:55]\n"
  "ds_read_b128 v[44:47], v65 offset:30720\n"
  "v_mov_b32 v125, v124\n"
  "s_nop 1\n"
  "v_permlane32_swap_b32 v124, v125\n"
  "v_add_f32 v124, v124, v125\n"
  "v_mfma_f32_16x16x32_bf16 v[56:59], a[120:123], v[16:19], v[56:59]\n"
  "v_rcp_f32 v124, v124\n"
  "v_mfma_f32_16x16x32_bf16 v[60:63], a[120:123], v[20:23], v[60:63]\n"
  "v_mul_f32 v104, v124, v104\n"
  "v_mul_f32 v105, v124, v105\n"
  "s_waitcnt lgkmcnt(5)\n"
  "v_mfma_f32_16x16x32_bf16 v[48:51], a[52:55], v[24:27], v[48:51]\n"
  "ds_read_b128 v[16:19], v64 offset:32768\n"
  "v_mul_f32 v106, v124, v106\n"
  "v_mul_f32 v107, v124, v107\n"
  "s_waitcnt lgkmcnt(5)\n"
  "v_mfma_f32_16x16x32_bf16 v[52:55], a[52:55], v[28:31], v[52:55]\n"
  "ds_read_b128 v[20:23], v64 offset:34816\n"
  "v_cvt_pk_bf16_f32 v108, v104, v104\n"
  "v_cvt_pk_bf16_f32 v109, v105, v105\n"
  "v_mfma_f32_16x16x32_bf16 v[56:59], a[124:127], v[24:27], v[56:59]\n"
  "v_cvt_pk_bf16_f32 v110, v106, v106\n"
  "v_cvt_pk_bf16_f32 v111, v107, v107\n"
  "v_mfma_f32_16x16x32_bf16 v[60:63], a[124:127], v[28:31], v[60:63]\n"
  "v_and_b32 v108, v8, v108\n"
  "v_and_b32 v109, v8, v109\n"
  "s_waitcnt lgkmcnt(5)\n"
  "v_mfma_f32_16x16x32_bf16 v[48:51], a[56:59], v[32:35], v[48:51]\n"
  "ds_read_b128 v[24:27], v65 offset:32768\n"
  "v_and_b32 v110, v8, v110\n"
  "v_and_b32 v111, v8, v111\n"
  "s_waitcnt lgkmcnt(5)\n"
  "v_mfma_f32_16x16x32_bf16 v[52:55], a[56:59], v[36:39], v[52:55]\n"
  "ds_read_b128 v[28:31], v65 offset:34816\n"
  "v_mfma_f32_16x16x32_bf16 v[112:115], v[88:91], v[108:111], 0\n"
  "v_mfma_f32_16x16x32_bf16 v[56:59], a[128:131], v[32:35], v[56:59]\n"
  "s_waitcnt vmcnt(9)\n"
  "s_barrier\n"
  "v_mov_b32 v9, 0\n"
  "v_mov_b32 v15, s56\n"
  "s_cmp_eq_u32 s47, 0\n"
  "s_cselect_b64 exec, 1, 0\n"
  "global_store_dword v9, v15, s[64:65] sc1\n"
  "s_mov_b64 exec, -1\n"
  "v_mfma_f32_16x16x32_bf16 v[60:63], a[128:131], v[36:39], v[60:63]\n"
  "v_mfma_f32_16x16x32_bf16 v[116:119], v[92:95], v[108:111], 0\n"
  "s_waitcnt lgkmcnt(5)\n"
  "v_mfma_f32_16x16x32_bf16 v[48:51], a[60:63], v[40:43], v[48:51]\n"
  "s_waitcnt lgkmcnt(4)\n"
  "v_mfma_f32_16x16x32_bf16 v[52:55], a[60:63], v[44:47], v[52:55]\n"
  "v_mfma_f32_16x16x32_bf16 v[56:59], a[132:135], v[40:43], v[56:59]\n"
  "v_mfma_f32_16x16x32_bf16 v[60:63], a[132:135], v[44:47], v[60:63]\n"
  "s_waitcnt lgkmcnt(3)\n"
  "v_mfma_f32_16x16x32_bf16 v[48:51], a[64:67], v[16:19], v[48:51]\n"
  "s_waitcnt lgkmcnt(2)\n"
  "v_mfma_f32_16x16x32_bf16 v[52:55], a[64:67], v[20:23], v[52:55]\n"
  "v_mfma_f32_16x16x32_bf16 v[56:59], a[136:139], v[16:19], v[56:59]\n"
  "v_mfma_f32_16x16x32_bf16 v[60:63], a[136:139], v[20:23], v[60:63]\n"
  "s_waitcnt lgkmcnt(1)\n"
  "v_mfma_f32_16x16x32_bf16 v[48:51], a[68:71], v[24:27], v[48:51]\n"
  "s_waitcnt lgkmcnt(0)\n"
  "v_mfma_f32_16x16x32_bf16 v[52:55], a[68:71], v[28:31], v[52:55]\n"
  "v_mfma_f32_16x16x32_bf16 v[56:59], a[140:143], v[24:27], v[56:59]\n"
  "v_mfma_f32_16x16x32_bf16 v[60:63], a[140:143], v[28:31], v[60:63]\n"
  "v_cvt_pk_bf16_f32 v112, v112, v113\n"
  "v_cvt_pk_bf16_f32 v113, v114, v115\n"
  "v_cvt_pk_bf16_f32 v114, v116, v117\n"
  "v_cvt_pk_bf16_f32 v115, v118, v119\n"
  "ds_write_b64 v3, v[112:113]\n"
  "s_mov_b64 exec, 0xffff\n"
  "ds_write_b64 v3, v[114:115] offset:32\n"
  "s_mov_b64 exec, s[68:69]\n"
  "global_store_dword v127, v126, s[62:63] sc1\n"
  "s_mov_b64 exec, -1\n"
  "s_add_u32 s62, s62, 2048\n"
  "s_addc_u32 s63, s63, 0\n"
  "v_cvt_pk_bf16_f32 v48, v48, v49\n"
  "v_cvt_pk_bf16_f32 v49, v50, v51\n"
  "v_cvt_pk_bf16_f32 v52, v52, v53\n"
  "v_cvt_pk_bf16_f32 v53, v54, v55\n"
  "v_cvt_pk_bf16_f32 v56, v56, v57\n"
  "v_cvt_pk_bf16_f32 v57, v58, v59\n"
  "v_cvt_pk_bf16_f32 v60, v60, v61\n"
  "v_cvt_pk_bf16_f32 v61, v62, v63\n"
  "ds_write_b64 v10, v[48:49] offset:9728\n"
  "ds_write_b64 v10, v[52:53] offset:14592\n"
  "ds_write_b64 v11, v[56:57] offset:9728\n"
  "ds_write_b64 v11, v[60:61] offset:14592\n"
  "s_add_u32 s56, s56, 1\n"
  "ALOOP:\n"
  "s_waitcnt vmcnt(2) lgkmcnt(0)\n"
  "s_barrier\n"
  "v_add_u32 v64, 22272, v2\n"
  "v_xor_b32 v65, 64, v64\n"
  "s_add_u32 s58, s80, 59136\n"
  "s_cmp_lt_i32 s56, 62\n"
  "s_cselect_b32 s74, 36864, 0\n"
  "s_add_u32 s38, s38, s74\n"
  "s_addc_u32 s39, s39, 0\n"
  "ds_read_b128 v[120:123], v4\n"
  "ds_read_b128 v[16:19], v64 offset:0\n"
  "ds_read_b128 v[20:23], v64 offset:2048\n"
  "ds_read_b128 v[24:27], v65 offset:0\n"
  "ds_read_b128 v[28:31], v65 offset:2048\n"
  "ds_read_b128 v[32:35], v64 offset:4096\n"
  "ds_read_b128 v[36:39], v64 offset:6144\n"
  "s_waitcnt lgkmcnt(5)\n"
  "v_mfma_f32_16x16x32_bf16 v[48:51], a[0:3], v[16:19], 0\n"
  "ds_read_b128 v[40:43], v65 offset:4096\n"
  "ds_read_b128 v[76:79], v67\n"
  "s_nop 0\n"
  "s_waitcnt lgkmcnt(6)\n"
  "v_mfma_f32_16x16x32_bf16 v[52:55], a[0:3], v[20:23], 0\n"
  "ds_read_b128 v[44:47], v65 offset:6144\n"
  "ds_read_b128 v[80:83], v70\n"
  "s_nop 0\n"
  "v_mfma_f32_16x16x32_bf16 v[56:59], a[72:75], v[16:19], 0\n"
  "s_bfm_b64 exec, 40, 0\n"
  "global_store_dwordx4 v5, v[120:123], s[60:61] sc1\n"
  "s_mov_b64 exec, -1\n"
  "s_add_u32 s60, s60, 40960\n"
  "s_addc_u32 s61, s61, 0\n"
  "v_mfma_f32_16x16x32_bf16 v[60:63], a[72:75], v[20:23], 0\n"
  "ds_read_b128 v[84:87], v71\n"
  "s_nop 0\n"
  "s_waitcnt lgkmcnt(8)\n"
  "v_mfma_f32_16x16x32_bf16 v[48:51], a[4:7], v[24:27], v[48:51]\n"
  "ds_read_b128 v[16:19], v64 offset:8192\n"
  "ds_read_b64_tr_b16 v[88:89], v74 offset:0\n"
  "s_nop 0\n"
  "s_waitcnt lgkmcnt(9)\n"
  "v_mfma_f32_16x16x32_bf16 v[52:55], a[4:7], v[28:31], v[52:55]\n"
  "ds_read_b128 v[20:23], v64 offset:10240\n"
  "ds_read_b64_tr_b16 v[90:91], v75 offset:0\n"
  "s_nop 0\n"
  "v_mfma_f32_16x16x32_bf16 v[56:59], a[76:79], v[24:27], v[56:59]\n"
  "s_add_u32 m0, s58, 0\n"
  "s_nop 0\n"
  "global_load_lds_dwordx4 v1, s[38:39] offset:0\n"
  "v_mfma_f32_16x16x32_bf16 v[60:63], a[76:79], v[28:31], v[60:63]\n"
  "ds_read_b64_tr_b16 v[92:93], v74 offset:32\n"
  "s_nop 0\n"
  "s_waitcnt lgkmcnt(11)\n"
  "v_mfma_f32_16x16x32_bf16 v[48:51], a[8:11], v[32:35], v[48:51]\n"
  "ds_read_b128 v[24:27], v65 offset:8192\n"
  "ds_read_b64_tr_b16 v[94:95], v75 offset:32\n"
  "s_nop 0\n"
  "s_waitcnt lgkmcnt(12)\n"
  "v_mfma_f32_16x16x32_bf16 v[52:55], a[8:11], v[36:39], v[52:55]\n"
  "ds_read_b128 v[28:31], v65 offset:10240\n"
  "ds_read_b32 v126, v6 offset:9728\n"
  "s_nop 0\n"
  "v_mfma_f32_16x16x32_bf16 v[56:59], a[80:83], v[32:35], v[56:59]\n"
  "s_add_u32 m0, s58, 3968\n"
  "s_nop 0\n"
  "global_load_lds_dwordx4 v1, s[38:39] offset:128\n"
  "v_mfma_f32_16x16x32_bf16 v[60:63], a[80:83], v[36:39], v[60:63]\n"
  "s_waitcnt lgkmcnt(10)\n"
  "v_mfma_f32_16x16x32_bf16 v[96:99], v[80:83], v[76:79], 0\n"
  "v_mfma_f32_16x16x32_bf16 v[48:51], a[12:15], v[40:43], v[48:51]\n"
  "ds_read_b128 v[32:35], v64 offset:12288\n"
  "s_waitcnt lgkmcnt(10)\n"
  "v_mfma_f32_16x16x32_bf16 v[100:103], v[84:87], v[76:79], 0\n"
  "v_mfma_f32_16x16x32_bf16 v[52:55], a[12:15], v[44:47], v[52:55]\n"
  "ds_read_b128 v[36:39], v64 offset:14336\n"
  "s_waitcnt lgkmcnt(2)\n"
  "v_lshlrev_b32 v126, 16, v126\n"
  "v_mfma_f32_16x16x32_bf16 v[56:59], a[84:87], v[40:43], v[56:59]\n"
  "s_add_u32 m0, s58, 7936\n"
  "s_nop 0\n"
  "global_load_lds_dwordx4 v1, s[38:39] offset:256\n"
  "v_mfma_f32_16x16x32_bf16 v[60:63], a[84:87], v[44:47], v[60:63]\n"
  "v_mul_f32 v126, 0xbfb8aa3b, v126\n"
  "v_exp_f32 v126, v126\n"
  "v_mfma_f32_16x16x32_bf16 v[48:51], a[16:19], v[16:19], v[48:51]\n"
  "ds_read_b128 v[40:43], v65 offset:12288\n"
  "v_add_f32 v126, 1.0, v126\n"
  "v_rcp_f32 v126, v126\n"
  "v_mfma_f32_16x16x32_bf16 v[52:55], a[16:19], v[20:23], v[52:55]\n"
  "ds_read_b128 v[44:47], v65 offset:14336\n"
  "v_cvt_pk_bf16_f32 v126, v126, v126\n"
  "v_lshlrev_b32 v126, 16, v126\n"
  "v_mfma_f32_16x16x32_bf16 v[56:59], a[88:91], v[16:19], v[56:59]\n"
  "s_add_u32 m0, s58, 11904\n"
  "s_nop 0\n"
  "global_load_lds_dwordx4 v1, s[38:39] offset:384\n"
  "v_mfma_f32_16x16x32_bf16 v[60:63], a[88:91], v[20:23], v[60:63]\n"
  "v_cndmask_b32 v104, v96, v97, s[70:71]\n"
  "v_cndmask_b32 v105, v98, v99, s[70:71]\n"
  "v_mfma_f32_16x16x32_bf16 v[48:51], a[20:23], v[24:27], v[48:51]\n"
  "ds_read_b128 v[16:19], v64 offset:16384\n"
  "v_cndmask_b32 v106, v100, v101, s[70:71]\n"
  "v_cndmask_b32 v107, v102, v103, s[70:71]\n"
  "v_mfma_f32_16x16x32_bf16 v[52:55], a[20:23], v[28:31], v[52:55]\n"
  "ds_read_b128 v[20:23], v64 offset:18432\n"
  "v_cvt_pk_bf16_f32 v104, v104, v105\n"
  "v_and_b32 v105, 0xffff0000, v104\n"
  "v_mfma_f32_16x16x32_bf16 v[56:59], a[92:95], v[24:27], v[56:59]\n"
  "s_add_u32 m0, s58, 15872\n"
  "s_nop 0\n"
  "global_load_lds_dwordx4 v1, s[38:39] offset:512\n"
  "v_mfma_f32_16x16x32_bf16 v[60:63], a[92:95], v[28:31], v[60:63]\n"
  "v_lshlrev_b32 v104, 16, v104\n"
  "v_cvt_pk_bf16_f32 v106, v106, v107\n"
  "s_waitcnt lgkmcnt(5)\n"
  "v_mfma_f32_16x16x32_bf16 v[48:51], a[24:27], v[32:35], v[48:51]\n"
  "ds_read_b128 v[24:27], v65 offset:16384\n"
  "v_and_b32 v107, 0xffff0000, v106\n"
  "v_lshlrev_b32 v106, 16, v106\n"
  "s_waitcnt lgkmcnt(5)\n"
  "v_mfma_f32_16x16x32_bf16 v[52:55], a[24:27], v[36:39], v[52:55]\n"
  "ds_read_b128 v[28:31], v65 offset:18432\n"
  "v_mul_f32 v104, 0x3e715bef, v104\n"
  "v_mul_f32 v105, 0x3e715bef, v105\n"
  "v_mfma_f32_16x16x32_bf16 v[56:59], a[96:99], v[32:35], v[56:59]\n"
  "s_add_u32 m0, s58, 19840\n"
  "s_nop 0\n"
  "global_load_lds_dwordx4 v1, s[38:39] offset:640\n"
  "v_mfma_f32_16x16x32_bf16 v[60:63], a[96:99], v[36:39], v[60:63]\n"
  "v_mul_f32 v106, 0x3e715bef, v106\n"
  "v_mul_f32 v107, 0x3e715bef, v107\n"
  "s_waitcnt lgkmcnt(5)\n"
  "v_mfma_f32_16x16x32_bf16 v[48:51], a[28:31], v[40:43], v[48:51]\n"
  "ds_read_b128 v[32:35], v64 offset:20480\n"
  "v_cvt_pk_bf16_f32 v104, v104, v105\n"
  "v_and_b32 v105, 0xffff0000, v104\n"
  "s_waitcnt lgkmcnt(5)\n"
  "v_mfma_f32_16x16x32_bf16 v[52:55], a[28:31], v[44:47], v[52:55]\n"
  "ds_read_b128 v[36:39], v64 offset:22528\n"
  "v_lshlrev_b32 v104, 16, v104\n"
  "v_cvt_pk_bf16_f32 v106, v106, v107\n"
  "v_mfma_f32_16x16x32_bf16 v[56:59], a[100:103], v[40:43], v[56:59]\n"
  "s_add_u32 m0, s58, 23808\n"
  "s_nop 0\n"
  "global_load_lds_dwordx4 v1, s[38:39] offset:768\n"
  "v_mfma_f32_16x16x32_bf16 v[60:63], a[100:103], v[44:47], v[60:63]\n"
  "v_and_b32 v107, 0xffff0000, v106\n"
  "v_lshlrev_b32 v106, 16, v106\n"
  "s_waitcnt lgkmcnt(5)\n"
  "v_mfma_f32_16x16x32_bf16 v[48:51], a[32:35], v[16:19], v[48:51]\n"
  "ds_read_b128 v[40:43], v65 offset:20480\n"
  "v_cndmask_b32 v104, v128, v104, s[8:9]\n"
  "v_cndmask_b32 v105, v128, v105, s[10:11]\n"
  "s_waitcnt lgkmcnt(5)\n"
  "v_mfma_f32_16x16x32_bf16 v[52:55], a[32:35], v[20:23], v[52:55]\n"
  "ds_read_b128 v[44:47], v65 offset:22528\n"
  "v_cndmask_b32 v106, v128, v106, s[12:13]\n"
  "v_cndmask_b32 v107, v128, v107, s[14:15]\n"
  "v_mfma_f32_16x16x32_bf16 v[56:59], a[104:107], v[16:19], v[56:59]\n"
  "s_add_u32 m0, s58, 27776\n"
  "s_nop 0\n"
  "global_load_lds_dwordx4 v1, s[38:39] offset:896\n"
  "v_mfma_f32_16x16x32_bf16 v[60:63], a[104:107], v[20:23], v[60:63]\n"
  "v_max3_f32 v124, v104, v105, v106\n"
  "v_max_f32 v124, v124, v107\n"
  "s_waitcnt lgkmcnt(5)\n"
  "v_mfma_f32_16x16x32_bf16 v[48:51], a[36:39], v[24:27], v[48:51]\n"
  "ds_read_b128 v[16:19], v64 offset:24576\n"
  "v_mov_b32 v125, v124\n"
  "s_nop 1\n"
  "v_permlane16_swap_b32 v124, v125\n"
  "v_max_f32 v124, v124, v125\n"
  "s_waitcnt lgkmcnt(5)\n"
  "v_mfma_f32_16x16x32_bf16 v[52:55], a[36:39], v[28:31], v[52:55]\n"
  "ds_read_b128 v[20:23], v64 offset:26624\n"
  "v_mov_b32 v125, v124\n"
  "s_nop 1\n"
  "v_permlane32_swap_b32 v124, v125\n"
  "v_max_f32 v124, v124, v125\n"
  "v_mfma_f32_16x16x32_bf16 v[56:59], a[108:111], v[24:27], v[56:59]\n"
  "s_add_u32 m0, s58, 31744\n"
  "s_nop 0\n"
  "global_load_lds_dwordx4 v1, s[38:39] offset:1024\n"
  "v_mfma_f32_16x16x32_bf16 v[60:63], a[108:111], v[28:31], v[60:63]\n"
  "v_sub_f32 v104, v104, v124\n"
  "v_sub_f32 v105, v105, v124\n"
  "s_waitcnt lgkmcnt(5)\n"
  "v_mfma_f32_16x16x32_bf16 v[48:51], a[40:43], v[32:35], v[48:51]\n"
  "ds_read_b128 v[24:27], v65 offset:24576\n"
  "v_sub_f32 v106, v106, v124\n"
  "v_sub_f32 v107, v107, v124\n"
  "s_waitcnt lgkmcnt(5)\n"
  "v_mfma_f32_16x16x32_bf16 v[52:55], a[40:43], v[36:39], v[52:55]\n"
  "ds_read_b128 v[28:31], v65 offset:26624\n"
  "v_mul_f32 v104, 0x3fb8aa3b, v104\n"
  "v_mul_f32 v105, 0x3fb8aa3b, v105\n"
  "v_mfma_f32_16x16x32_bf16 v[56:59], a[112:115], v[32:35], v[56:59]\n"
  "v_mul_f32 v106, 0x3fb8aa3b, v106\n"
  "v_mul_f32 v107, 0x3fb8aa3b, v107\n"
  "v_mfma_f32_16x16x32_bf16 v[60:63], a[112:115], v[36:39], v[60:63]\n"
  "v_exp_f32 v104, v104\n"
  "s_waitcnt lgkmcnt(5)\n"
  "v_mfma_f32_16x16x32_bf16 v[48:51], a[44:47], v[40:43], v[48:51]\n"
  "ds_read_b128 v[32:35], v64 offset:28672\n"
  "v_exp_f32 v105, v105\n"
  "s_waitcnt lgkmcnt(5)\n"
  "v_mfma_f32_16x16x32_bf16 v[52:55], a[44:47], v[44:47], v[52:55]\n"
  "ds_read_b128 v[36:39], v64 offset:30720\n"
  "v_exp_f32 v106, v106\n"
  "v_mfma_f32_16x16x32_bf16 v[56:59], a[116:119], v[40:43], v[56:59]\n"
  "v_exp_f32 v107, v107\n"
  "v_mfma_f32_16x16x32_bf16 v[60:63], a[116:119], v[44:47], v[60:63]\n"
  "s_mov_b64 s[8:9], s[16:17]\n"
  "s_mov_b64 s[10:11], s[18:19]\n"
  "s_waitcnt lgkmcnt(5)\n"
  "v_mfma_f32_16x16x32_bf16 v[48:51], a[48:51], v[16:19], v[48:51]\n"
  "ds_read_b128 v[40:43], v65 offset:28672\n"
  "v_add_f32 v124, v104, v105\n"
  "v_add_f32 v124, v124, v106\n"
  "s_waitcnt lgkmcnt(5)\n"
  "v_mfma_f32_16x16x32_bf16 v[52:55], a[48:51], v[20:23], v[52:55]\n"
  "ds_read_b128 v[44:47], v65 offset:30720\n"
  "v_add_f32 v124, v124, v107\n"
  "v_mov_b32 v125, v124\n"
  "s_nop 1\n"
  "v_permlane16_swap_b32 v124, v125\n"
  "v_add_f32 v124, v124, v125\n"
  "v_mfma_f32_16x16x32_bf16 v[56:59], a[120:123], v[16:19], v[56:59]\n"
  "v_mov_b32 v125, v124\n"
  "s_nop 1\n"
  "v_permlane32_swap_b32 v124, v125\n"
  "v_add_f32 v124, v124, v125\n"
  "v_mfma_f32_16x16x32_bf16 v[60:63], a[120:123], v[20:23], v[60:63]\n"
  "v_rcp_f32 v124, v124\n"
  "s_waitcnt lgkmcnt(5)\n"
  "v_mfma_f32_16x16x32_bf16 v[48:51], a[52:55], v[24:27], v[48:51]\n"
  "ds_read_b128 v[16:19], v64 offset:32768\n"
  "v_mul_f32 v104, v124, v104\n"
  "v_mul_f32 v105, v124, v105\n"
  "s_waitcnt lgkmcnt(5)\n"
  "v_mfma_f32_16x16x32_bf16 v[52:55], a[52:55], v[28:31], v[52:55]\n"
  "ds_read_b128 v[20:23], v64 offset:34816\n"
  "v_mul_f32 v106, v124, v106\n"
  "v_mul_f32 v107, v124, v107\n"
  "v_mfma_f32_16x16x32_bf16 v[56:59], a[124:127], v[24:27], v[56:59]\n"
  "v_cvt_pk_bf16_f32 v108, v104, v104\n"
  "v_cvt_pk_bf16_f32 v109, v105, v105\n"
  "v_mfma_f32_16x16x32_bf16 v[60:63], a[124:127], v[28:31], v[60:63]\n"
  "v_cvt_pk_bf16_f32 v110, v106, v106\n"
  "v_cvt_pk_bf16_f32 v111, v107, v107\n"
  "s_waitcnt lgkmcnt(5)\n"
  "v_mfma_f32_16x16x32_bf16 v[48:51], a[56:59], v[32:35], v[48:51]\n"
  "ds_read_b128 v[24:27], v65 offset:32768\n"
  "v_and_b32 v108, v8, v108\n"
  "v_and_b32 v109, v8, v109\n"
  "s_waitcnt lgkmcnt(5)\n"
  "v_mfma_f32_16x16x32_bf16 v[52:55], a[56:59], v[36:39], v[52:55]\n"
  "ds_read_b128 v[28:31], v65 offset:34816\n"
  "v_and_b32 v110, v8, v110\n"
  "v_and_b32 v111, v8, v111\n"
  "v_mfma_f32_16x16x32_bf16 v[56:59], a[128:131], v[32:35], v[56:59]\n"
  "s_waitcnt vmcnt(9)\n"
  "s_barrier\n"
  "v_mov_b32 v9, 0\n"
  "v_mov_b32 v15, s56\n"
  "s_cmp_eq_u32 s47, 0\n"
  "s_cselect_b64 exec, 1, 0\n"
  "global_store_dword v9, v15, s[64:65] sc1\n"
  "s_mov_b64 exec, -1\n"
  "v_mfma_f32_16x16x32_bf16 v[60:63], a[128:131], v[36:39], v[60:63]\n"
  "v_mfma_f32_16x16x32_bf16 v[112:115], v[88:91], v[108:111], 0\n"
  "s_waitcnt lgkmcnt(5)\n"
  "v_mfma_f32_16x16x32_bf16 v[48:51], a[60:63], v[40:43], v[48:51]\n"
  "v_mfma_f32_16x16x32_bf16 v[116:119], v[92:95], v[108:111], 0\n"
  "s_waitcnt lgkmcnt(4)\n"
  "v_mfma_f32_16x16x32_bf16 v[52:55], a[60:63], v[44:47], v[52:55]\n"
  "v_mfma_f32_16x16x32_bf16 v[56:59], a[132:135], v[40:43], v[56:59]\n"
  "v_mfma_f32_16x16x32_bf16 v[60:63], a[132:135], v[44:47], v[60:63]\n"
  "s_waitcnt lgkmcnt(3)\n"
  "v_mfma_f32_16x16x32_bf16 v[48:51], a[64:67], v[16:19], v[48:51]\n"
  "s_waitcnt lgkmcnt(2)\n"
  "v_mfma_f32_16x16x32_bf16 v[52:55], a[64:67], v[20:23], v[52:55]\n"
  "v_mfma_f32_16x16x32_bf16 v[56:59], a[136:139], v[16:19], v[56:59]\n"
  "v_mfma_f32_16x16x32_bf16 v[60:63], a[136:139], v[20:23], v[60:63]\n"
  "s_waitcnt lgkmcnt(1)\n"
  "v_mfma_f32_16x16x32_bf16 v[48:51], a[68:71], v[24:27], v[48:51]\n"
  "s_waitcnt lgkmcnt(0)\n"
  "v_mfma_f32_16x16x32_bf16 v[52:55], a[68:71], v[28:31], v[52:55]\n"
  "v_mfma_f32_16x16x32_bf16 v[56:59], a[140:143], v[24:27], v[56:59]\n"
  "v_mfma_f32_16x16x32_bf16 v[60:63], a[140:143], v[28:31], v[60:63]\n"
  "v_cvt_pk_bf16_f32 v112, v112, v113\n"
  "v_cvt_pk_bf16_f32 v113, v114, v115\n"
  "v_cvt_pk_bf16_f32 v114, v116, v117\n"
  "v_cvt_pk_bf16_f32 v115, v118, v119\n"
  "ds_write_b64 v3, v[112:113]\n"
  "s_mov_b64 exec, 0xffff\n"
  "ds_write_b64 v3, v[114:115] offset:32\n"
  "s_mov_b64 exec, s[68:69]\n"
  "global_store_dword v127, v126, s[62:63] sc1\n"
  "s_mov_b64 exec, -1\n"
  "s_add_u32 s62, s62, 2048\n"
  "s_addc_u32 s63, s63, 0\n"
  "v_cvt_pk_bf16_f32 v48, v48, v49\n"
  "v_cvt_pk_bf16_f32 v49, v50, v51\n"
  "v_cvt_pk_bf16_f32 v52, v52, v53\n"
  "v_cvt_pk_bf16_f32 v53, v54, v55\n"
  "v_cvt_pk_bf16_f32 v56, v56, v57\n"
  "v_cvt_pk_bf16_f32 v57, v58, v59\n"
  "v_cvt_pk_bf16_f32 v60, v60, v61\n"
  "v_cvt_pk_bf16_f32 v61, v62, v63\n"
  "ds_write_b64 v10, v[48:49] offset:0\n"
  "ds_write_b64 v10, v[52:53] offset:4864\n"
  "ds_write_b64 v11, v[56:57] offset:0\n"
  "ds_write_b64 v11, v[60:61] offset:4864\n"
  "s_add_u32 s56, s56, 1\n"
  "s_waitcnt vmcnt(2) lgkmcnt(0)\n"
  "s_barrier\n"
  "v_add_u32 v64, 59136, v2\n"
  "v_xor_b32 v65, 64, v64\n"
  "s_add_u32 s58, s80, 22272\n"
  "s_cmp_lt_i32 s56, 62\n"
  "s_cselect_b32 s74, 36864, 0\n"
  "s_add_u32 s38, s38, s74\n"
  "s_addc_u32 s39, s39, 0\n"
  "ds_read_b128 v[120:123], v4\n"
  "ds_read_b128 v[16:19], v64 offset:0\n"
  "ds_read_b128 v[20:23], v64 offset:2048\n"
  "ds_read_b128 v[24:27], v65 offset:0\n"
  "ds_read_b128 v[28:31], v65 offset:2048\n"
  "ds_read_b128 v[32:35], v64 offset:4096\n"
  "ds_read_b128 v[36:39], v64 offset:6144\n"
  "s_waitcnt lgkmcnt(5)\n"
  "v_mfma_f32_16x16x32_bf16 v[48:51], a[0:3], v[16:19], 0\n"
  "ds_read_b128 v[40:43], v65 offset:4096\n"
  "ds_read_b128 v[76:79], v66\n"
  "s_nop 0\n"
  "s_waitcnt lgkmcnt(6)\n"
  "v_mfma_f32_16x16x32_bf16 v[52:55], a[0:3], v[20:23], 0\n"
  "ds_read_b128 v[44:47], v65 offset:6144\n"
  "ds_read_b128 v[80:83], v68\n"
  "s_nop 0\n"
  "v_mfma_f32_16x16x32_bf16 v[56:59], a[72:75], v[16:19], 0\n"
  "s_bfm_b64 exec, 40, 0\n"
  "global_store_dwordx4 v5, v[120:123], s[60:61] sc1\n"
  "s_mov_b64 exec, -1\n"
  "s_add_u32 s60, s60, 40960\n"
  "s_addc_u32 s61, s61, 0\n"
  "v_mfma_f32_16x16x32_bf16 v[60:63], a[72:75], v[20:23], 0\n"
  "ds_read_b128 v[84:87], v69\n"
  "s_nop 0\n"
  "s_waitcnt lgkmcnt(8)\n"
  "v_mfma_f32_16x16x32_bf16 v[48:51], a[4:7], v[24:27], v[48:51]\n"
  "ds_read_b128 v[16:19], v64 offset:8192\n"
  "ds_read_b64_tr_b16 v[88:89], v72 offset:0\n"
  "s_nop 0\n"
  "s_waitcnt lgkmcnt(9)\n"
  "v_mfma_f32_16x16x32_bf16 v[52:55], a[4:7], v[28:31], v[52:55]\n"
  "ds_read_b128 v[20:23], v64 offset:10240\n"
  "ds_read_b64_tr_b16 v[90:91], v73 offset:0\n"
  "s_nop 0\n"
  "v_mfma_f32_16x16x32_bf16 v[56:59], a[76:79], v[24:27], v[56:59]\n"
  "s_add_u32 m0, s58, 0\n"
  "s_nop 0\n"
  "global_load_lds_dwordx4 v1, s[38:39] offset:0\n"
  "v_mfma_f32_16x16x32_bf16 v[60:63], a[76:79], v[28:31], v[60:63]\n"
  "ds_read_b64_tr_b16 v[92:93], v72 offset:32\n"
  "s_nop 0\n"
  "s_waitcnt lgkmcnt(11)\n"
  "v_mfma_f32_16x16x32_bf16 v[48:51], a[8:11], v[32:35], v[48:51]\n"
  "ds_read_b128 v[24:27], v65 offset:8192\n"
  "ds_read_b64_tr_b16 v[94:95], v73 offset:32\n"
  "s_nop 0\n"
  "s_waitcnt lgkmcnt(12)\n"
  "v_mfma_f32_16x16x32_bf16 v[52:55], a[8:11], v[36:39], v[52:55]\n"
  "ds_read_b128 v[28:31], v65 offset:10240\n"
  "ds_read_b32 v126, v6 offset:0\n"
  "s_nop 0\n"
  "v_mfma_f32_16x16x32_bf16 v[56:59], a[80:83], v[32:35], v[56:59]\n"
  "s_add_u32 m0, s58, 3968\n"
  "s_nop 0\n"
  "global_load_lds_dwordx4 v1, s[38:39] offset:128\n"
  "v_mfma_f32_16x16x32_bf16 v[60:63], a[80:83], v[36:39], v[60:63]\n"
  "s_waitcnt lgkmcnt(10)\n"
  "v_mfma_f32_16x16x32_bf16 v[96:99], v[80:83], v[76:79], 0\n"
  "v_mfma_f32_16x16x32_bf16 v[48:51], a[12:15], v[40:43], v[48:51]\n"
  "ds_read_b128 v[32:35], v64 offset:12288\n"
  "s_waitcnt lgkmcnt(10)\n"
  "v_mfma_f32_16x16x32_bf16 v[100:103], v[84:87], v[76:79], 0\n"
  "v_mfma_f32_16x16x32_bf16 v[52:55], a[12:15], v[44:47], v[52:55]\n"
  "ds_read_b128 v[36:39], v64 offset:14336\n"
  "s_waitcnt lgkmcnt(2)\n"
  "v_lshlrev_b32 v126, 16, v126\n"
  "v_mfma_f32_16x16x32_bf16 v[56:59], a[84:87], v[40:43], v[56:59]\n"
  "s_add_u32 m0, s58, 7936\n"
  "s_nop 0\n"
  "global_load_lds_dwordx4 v1, s[38:39] offset:256\n"
  "v_mfma_f32_16x16x32_bf16 v[60:63], a[84:87], v[44:47], v[60:63]\n"
  "v_mul_f32 v126, 0xbfb8aa3b, v126\n"
  "v_exp_f32 v126, v126\n"
  "v_mfma_f32_16x16x32_bf16 v[48:51], a[16:19], v[16:19], v[48:51]\n"
  "ds_read_b128 v[40:43], v65 offset:12288\n"
  "v_add_f32 v126, 1.0, v126\n"
  "v_rcp_f32 v126, v126\n"
  "v_mfma_f32_16x16x32_bf16 v[52:55], a[16:19], v[20:23], v[52:55]\n"
  "ds_read_b128 v[44:47], v65 offset:14336\n"
  "v_cvt_pk_bf16_f32 v126, v126, v126\n"
  "v_lshlrev_b32 v126, 16, v126\n"
  "v_mfma_f32_16x16x32_bf16 v[56:59], a[88:91], v[16:19], v[56:59]\n"
  "s_add_u32 m0, s58, 11904\n"
  "s_nop 0\n"
  "global_load_lds_dwordx4 v1, s[38:39] offset:384\n"
  "v_mfma_f32_16x16x32_bf16 v[60:63], a[88:91], v[20:23], v[60:63]\n"
  "v_cndmask_b32 v104, v96, v97, s[70:71]\n"
  "v_cndmask_b32 v105, v98, v99, s[70:71]\n"
  "v_mfma_f32_16x16x32_bf16 v[48:51], a[20:23], v[24:27], v[48:51]\n"
  "ds_read_b128 v[16:19], v64 offset:16384\n"
  "v_cndmask_b32 v106, v100, v101, s[70:71]\n"
  "v_cndmask_b32 v107, v102, v103, s[70:71]\n"
  "v_mfma_f32_16x16x32_bf16 v[52:55], a[20:23], v[28:31], v[52:55]\n"
  "ds_read_b128 v[20:23], v64 offset:18432\n"
  "v_cvt_pk_bf16_f32 v104, v104, v105\n"
  "v_and_b32 v105, 0xffff0000, v104\n"
  "v_mfma_f32_16x16x32_bf16 v[56:59], a[92:95], v[24:27], v[56:59]\n"
  "s_add_u32 m0, s58, 15872\n"
  "s_nop 0\n"
  "global_load_lds_dwordx4 v1, s[38:39] offset:512\n"
  "v_mfma_f32_16x16x32_bf16 v[60:63], a[92:95], v[28:31], v[60:63]\n"
  "v_lshlrev_b32 v104, 16, v104\n"
  "v_cvt_pk_bf16_f32 v106, v106, v107\n"
  "s_waitcnt lgkmcnt(5)\n"
  "v_mfma_f32_16x16x32_bf16 v[48:51], a[24:27], v[32:35], v[48:51]\n"
  "ds_read_b128 v[24:27], v65 offset:16384\n"
  "v_and_b32 v107, 0xffff0000, v106\n"
  "v_lshlrev_b32 v106, 16, v106\n"
  "s_waitcnt lgkmcnt(5)\n"
  "v_mfma_f32_16x16x32_bf16 v[52:55], a[24:27], v[36:39], v[52:55]\n"
  "ds_read_b128 v[28:31], v65 offset:18432\n"
  "v_mul_f32 v104, 0x3e715bef, v104\n"
  "v_mul_f32 v105, 0x3e715bef, v105\n"
  "v_mfma_f32_16x16x32_bf16 v[56:59], a[96:99], v[32:35], v[56:59]\n"
  "s_add_u32 m0, s58, 19840\n"
  "s_nop 0\n"
  "global_load_lds_dwordx4 v1, s[38:39] offset:640\n"
  "v_mfma_f32_16x16x32_bf16 v[60:63], a[96:99], v[36:39], v[60:63]\n"
  "v_mul_f32 v106, 0x3e715bef, v106\n"
  "v_mul_f32 v107, 0x3e715bef, v107\n"
  "s_waitcnt lgkmcnt(5)\n"
  "v_mfma_f32_16x16x32_bf16 v[48:51], a[28:31], v[40:43], v[48:51]\n"
  "ds_read_b128 v[32:35], v64 offset:20480\n"
  "v_cvt_pk_bf16_f32 v104, v104, v105\n"
  "v_and_b32 v105, 0xffff0000, v104\n"
  "s_waitcnt lgkmcnt(5)\n"
  "v_mfma_f32_16x16x32_bf16 v[52:55], a[28:31], v[44:47], v[52:55]\n"
  "ds_read_b128 v[36:39], v64 offset:22528\n"
  "v_lshlrev_b32 v104, 16, v104\n"
  "v_cvt_pk_bf16_f32 v106, v106, v107\n"
  "v_mfma_f32_16x16x32_bf16 v[56:59], a[100:103], v[40:43], v[56:59]\n"
  "s_add_u32 m0, s58, 23808\n"
  "s_nop 0\n"
  "global_load_lds_dwordx4 v1, s[38:39] offset:768\n"
  "v_mfma_f32_16x16x32_bf16 v[60:63], a[100:103], v[44:47], v[60:63]\n"
  "v_and_b32 v107, 0xffff0000, v106\n"
  "v_lshlrev_b32 v106, 16, v106\n"
  "s_waitcnt lgkmcnt(5)\n"
  "v_mfma_f32_16x16x32_bf16 v[48:51], a[32:35], v[16:19], v[48:51]\n"
  "ds_read_b128 v[40:43], v65 offset:20480\n"
  "v_cndmask_b32 v104, v128, v104, s[8:9]\n"
  "v_cndmask_b32 v105, v128, v105, s[10:11]\n"
  "s_waitcnt lgkmcnt(5)\n"
  "v_mfma_f32_16x16x32_bf16 v[52:55], a[32:35], v[20:23], v[52:55]\n"
  "ds_read_b128 v[44:47], v65 offset:22528\n"
  "v_cndmask_b32 v106, v128, v106, s[12:13]\n"
  "v_cndmask_b32 v107, v128, v107, s[14:15]\n"
  "v_mfma_f32_16x16x32_bf16 v[56:59], a[104:107], v[16:19], v[56:59]\n"
  "s_add_u32 m0, s58, 27776\n"
  "s_nop 0\n"
  "global_load_lds_dwordx4 v1, s[38:39] offset:896\n"
  "v_mfma_f32_16x16x32_bf16 v[60:63], a[104:107], v[20:23], v[60:63]\n"
  "v_max3_f32 v124, v104, v105, v106\n"
  "v_max_f32 v124, v124, v107\n"
  "s_waitcnt lgkmcnt(5)\n"
  "v_mfma_f32_16x16x32_bf16 v[48:51], a[36:39], v[24:27], v[48:51]\n"
  "ds_read_b128 v[16:19], v64 offset:24576\n"
  "v_mov_b32 v125, v124\n"
  "s_nop 1\n"
  "v_permlane16_swap_b32 v124, v125\n"
  "v_max_f32 v124, v124, v125\n"
  "s_waitcnt lgkmcnt(5)\n"
  "v_mfma_f32_16x16x32_bf16 v[52:55], a[36:39], v[28:31], v[52:55]\n"
  "ds_read_b128 v[20:23], v64 offset:26624\n"
  "v_mov_b32 v125, v124\n"
  "s_nop 1\n"
  "v_permlane32_swap_b32 v124, v125\n"
  "v_max_f32 v124, v124, v125\n"
  "v_mfma_f32_16x16x32_bf16 v[56:59], a[108:111], v[24:27], v[56:59]\n"
  "s_add_u32 m0, s58, 31744\n"
  "s_nop 0\n"
  "global_load_lds_dwordx4 v1, s[38:39] offset:1024\n"
  "v_mfma_f32_16x16x32_bf16 v[60:63], a[108:111], v[28:31], v[60:63]\n"
  "v_sub_f32 v104, v104, v124\n"
  "v_sub_f32 v105, v105, v124\n"
  "s_waitcnt lgkmcnt(5)\n"
  "v_mfma_f32_16x16x32_bf16 v[48:51], a[40:43], v[32:35], v[48:51]\n"
  "ds_read_b128 v[24:27], v65 offset:24576\n"
  "v_sub_f32 v106, v106, v124\n"
  "v_sub_f32 v107, v107, v124\n"
  "s_waitcnt lgkmcnt(5)\n"
  "v_mfma_f32_16x16x32_bf16 v[52:55], a[40:43], v[36:39], v[52:55]\n"
  "ds_read_b128 v[28:31], v65 offset:26624\n"
  "v_mul_f32 v104, 0x3fb8aa3b, v104\n"
  "v_mul_f32 v105, 0x3fb8aa3b, v105\n"
  "v_mfma_f32_16x16x32_bf16 v[56:59], a[112:115], v[32:35], v[56:59]\n"
  "v_mul_f32 v106, 0x3fb8aa3b, v106\n"
  "v_mul_f32 v107, 0x3fb8aa3b, v107\n"
  "v_mfma_f32_16x16x32_bf16 v[60:63], a[112:115], v[36:39], v[60:63]\n"
  "v_exp_f32 v104, v104\n"
  "s_waitcnt lgkmcnt(5)\n"
  "v_mfma_f32_16x16x32_bf16 v[48:51], a[44:47], v[40:43], v[48:51]\n"
  "ds_read_b128 v[32:35], v64 offset:28672\n"
  "v_exp_f32 v105, v105\n"
  "s_waitcnt lgkmcnt(5)\n"
  "v_mfma_f32_16x16x32_bf16 v[52:55], a[44:47], v[44:47], v[52:55]\n"
  "ds_read_b128 v[36:39], v64 offset:30720\n"
  "v_exp_f32 v106, v106\n"
  "v_mfma_f32_16x16x32_bf16 v[56:59], a[116:119], v[40:43], v[56:59]\n"
  "v_exp_f32 v107, v107\n"
  "v_mfma_f32_16x16x32_bf16 v[60:63], a[116:119], v[44:47], v[60:63]\n"
  "s_mov_b64 s[8:9], s[16:17]\n"
  "s_mov_b64 s[10:11], s[18:19]\n"
  "s_waitcnt lgkmcnt(5)\n"
  "v_mfma_f32_16x16x32_bf16 v[48:51], a[48:51], v[16:19], v[48:51]\n"
  "ds_read_b128 v[40:43], v65 offset:28672\n"
  "v_add_f32 v124, v104, v105\n"
  "v_add_f32 v124, v124, v106\n"
  "s_waitcnt lgkmcnt(5)\n"
  "v_mfma_f32_16x16x32_bf16 v[52:55], a[48:51], v[20:23], v[52:55]\n"
  "ds_read_b128 v[44:47], v65 offset:30720\n"
  "v_add_f32 v124, v124, v107\n"
  "v_mov_b32 v125, v124\n"
  "s_nop 1\n"
  "v_permlane16_swap_b32 v124, v125\n"
  "v_add_f32 v124, v124, v125\n"
  "v_mfma_f32_16x16x32_bf16 v[56:59], a[120:123], v[16:19], v[56:59]\n"
  "v_mov_b32 v125, v124\n"
  "s_nop 1\n"
  "v_permlane32_swap_b32 v124, v125\n"
  "v_add_f32 v124, v124, v125\n"
  "v_mfma_f32_16x16x32_bf16 v[60:63], a[120:123], v[20:23], v[60:63]\n"
  "v_rcp_f32 v124, v124\n"
  "s_waitcnt lgkmcnt(5)\n"
  "v_mfma_f32_16x16x32_bf16 v[48:51], a[52:55], v[24:27], v[48:51]\n"
  "ds_read_b128 v[16:19], v64 offset:32768\n"
  "v_mul_f32 v104, v124, v104\n"
  "v_mul_f32 v105, v124, v105\n"
  "s_waitcnt lgkmcnt(5)\n"
  "v_mfma_f32_16x16x32_bf16 v[52:55], a[52:55], v[28:31], v[52:55]\n"
  "ds_read_b128 v[20:23], v64 offset:34816\n"
  "v_mul_f32 v106, v124, v106\n"
  "v_mul_f32 v107, v124, v107\n"
  "v_mfma_f32_16x16x32_bf16 v[56:59], a[124:127], v[24:27], v[56:59]\n"
  "v_cvt_pk_bf16_f32 v108, v104, v104\n"
  "v_cvt_pk_bf16_f32 v109, v105, v105\n"
  "v_mfma_f32_16x16x32_bf16 v[60:63], a[124:127], v[28:31], v[60:63]\n"
  "v_cvt_pk_bf16_f32 v110, v106, v106\n"
  "v_cvt_pk_bf16_f32 v111, v107, v107\n"
  "s_waitcnt lgkmcnt(5)\n"
  "v_mfma_f32_16x16x32_bf16 v[48:51], a[56:59], v[32:35], v[48:51]\n"
  "ds_read_b128 v[24:27], v65 offset:32768\n"
  "v_and_b32 v108, v8, v108\n"
  "v_and_b32 v109, v8, v109\n"
  "s_waitcnt lgkmcnt(5)\n"
  "v_mfma_f32_16x16x32_bf16 v[52:55], a[56:59], v[36:39], v[52:55]\n"
  "ds_read_b128 v[28:31], v65 offset:34816\n"
  "v_and_b32 v110, v8, v110\n"
  "v_and_b32 v111, v8, v111\n"
  "v_mfma_f32_16x16x32_bf16 v[56:59], a[128:131], v[32:35], v[56:59]\n"
  "s_waitcnt vmcnt(9)\n"
  "s_barrier\n"
  "v_mov_b32 v9, 0\n"
  "v_mov_b32 v15, s56\n"
  "s_cmp_eq_u32 s47, 0\n"
  "s_cselect_b64 exec, 1, 0\n"
  "global_store_dword v9, v15, s[64:65] sc1\n"
  "s_mov_b64 exec, -1\n"
  "v_mfma_f32_16x16x32_bf16 v[60:63], a[128:131], v[36:39], v[60:63]\n"
  "v_mfma_f32_16x16x32_bf16 v[112:115], v[88:91], v[108:111], 0\n"
  "s_waitcnt lgkmcnt(5)\n"
  "v_mfma_f32_16x16x32_bf16 v[48:51], a[60:63], v[40:43], v[48:51]\n"
  "v_mfma_f32_16x16x32_bf16 v[116:119], v[92:95], v[108:111], 0\n"
  "s_waitcnt lgkmcnt(4)\n"
  "v_mfma_f32_16x16x32_bf16 v[52:55], a[60:63], v[44:47], v[52:55]\n"
  "v_mfma_f32_16x16x32_bf16 v[56:59], a[132:135], v[40:43], v[56:59]\n"
  "v_mfma_f32_16x16x32_bf16 v[60:63], a[132:135], v[44:47], v[60:63]\n"
  "s_waitcnt lgkmcnt(3)\n"
  "v_mfma_f32_16x16x32_bf16 v[48:51], a[64:67], v[16:19], v[48:51]\n"
  "s_waitcnt lgkmcnt(2)\n"
  "v_mfma_f32_16x16x32_bf16 v[52:55], a[64:67], v[20:23], v[52:55]\n"
  "v_mfma_f32_16x16x32_bf16 v[56:59], a[136:139], v[16:19], v[56:59]\n"
  "v_mfma_f32_16x16x32_bf16 v[60:63], a[136:139], v[20:23], v[60:63]\n"
  "s_waitcnt lgkmcnt(1)\n"
  "v_mfma_f32_16x16x32_bf16 v[48:51], a[68:71], v[24:27], v[48:51]\n"
  "s_waitcnt lgkmcnt(0)\n"
  "v_mfma_f32_16x16x32_bf16 v[52:55], a[68:71], v[28:31], v[52:55]\n"
  "v_mfma_f32_16x16x32_bf16 v[56:59], a[140:143], v[24:27], v[56:59]\n"
  "v_mfma_f32_16x16x32_bf16 v[60:63], a[140:143], v[28:31], v[60:63]\n"
  "v_cvt_pk_bf16_f32 v112, v112, v113\n"
  "v_cvt_pk_bf16_f32 v113, v114, v115\n"
  "v_cvt_pk_bf16_f32 v114, v116, v117\n"
  "v_cvt_pk_bf16_f32 v115, v118, v119\n"
  "ds_write_b64 v3, v[112:113]\n"
  "s_mov_b64 exec, 0xffff\n"
  "ds_write_b64 v3, v[114:115] offset:32\n"
  "s_mov_b64 exec, s[68:69]\n"
  "global_store_dword v127, v126, s[62:63] sc1\n"
  "s_mov_b64 exec, -1\n"
  "s_add_u32 s62, s62, 2048\n"
  "s_addc_u32 s63, s63, 0\n"
  "v_cvt_pk_bf16_f32 v48, v48, v49\n"
  "v_cvt_pk_bf16_f32 v49, v50, v51\n"
  "v_cvt_pk_bf16_f32 v52, v52, v53\n"
  "v_cvt_pk_bf16_f32 v53, v54, v55\n"
  "v_cvt_pk_bf16_f32 v56, v56, v57\n"
  "v_cvt_pk_bf16_f32 v57, v58, v59\n"
  "v_cvt_pk_bf16_f32 v60, v60, v61\n"
  "v_cvt_pk_bf16_f32 v61, v62, v63\n"
  "ds_write_b64 v10, v[48:49] offset:9728\n"
  "ds_write_b64 v10, v[52:53] offset:14592\n"
  "ds_write_b64 v11, v[56:57] offset:9728\n"
  "ds_write_b64 v11, v[60:61] offset:14592\n"
  "s_add_u32 s56, s56, 1\n"
  "s_cmp_lt_u32 s56, 63\n"
  "s_cbranch_scc1 ALOOP\n"
  "s_waitcnt lgkmcnt(0)\n"
  "s_barrier\n"
  "ds_read_b128 v[120:123], v4\n"
  "s_bfm_b64 exec, 40, 0\n"
  "s_waitcnt lgkmcnt(0)\n"
  "global_store_dwordx4 v5, v[120:123], s[60:61] sc1\n"
  "s_mov_b64 exec, -1\n"
  "s_add_u32 s60, s60, 40960\n"
  "s_addc_u32 s61, s61, 0\n"
  "ds_read_b128 v[76:79], v67\n"
  "s_nop 0\n"
  "ds_read_b128 v[80:83], v70\n"
  "s_nop 0\n"
  "ds_read_b128 v[84:87], v71\n"
  "s_nop 0\n"
  "ds_read_b64_tr_b16 v[88:89], v74 offset:0\n"
  "s_nop 0\n"
  "ds_read_b64_tr_b16 v[90:91], v75 offset:0\n"
  "s_nop 0\n"
  "ds_read_b64_tr_b16 v[92:93], v74 offset:32\n"
  "s_nop 0\n"
  "ds_read_b64_tr_b16 v[94:95], v75 offset:32\n"
  "s_nop 0\n"
  "ds_read_b32 v126, v6 offset:9728\n"
  "s_nop 0\n"
  "s_waitcnt lgkmcnt(6)\n"
  "v_mfma_f32_16x16x32_bf16 v[96:99], v[80:83], v[76:79], 0\n"
  "s_waitcnt lgkmcnt(5)\n"
  "v_mfma_f32_16x16x32_bf16 v[100:103], v[84:87], v[76:79], 0\n"
  "s_waitcnt lgkmcnt(0)\n"
  "v_lshlrev_b32 v126, 16, v126\n"
  "v_mul_f32 v126, 0xbfb8aa3b, v126\n"
  "v_exp_f32 v126, v126\n"
  "s_nop 0\n"
  "v_add_f32 v126, 1.0, v126\n"
  "v_rcp_f32 v126, v126\n"
  "s_nop 0\n"
  "v_cvt_pk_bf16_f32 v126, v126, v126\n"
  "v_lshlrev_b32 v126, 16, v126\n"
  "v_cndmask_b32 v104, v96, v97, s[70:71]\n"
  "v_cndmask_b32 v105, v98, v99, s[70:71]\n"
  "v_cndmask_b32 v106, v100, v101, s[70:71]\n"
  "v_cndmask_b32 v107, v102, v103, s[70:71]\n"
  "v_cvt_pk_bf16_f32 v104, v104, v105\n"
  "v_and_b32 v105, 0xffff0000, v104\n"
  "v_lshlrev_b32 v104, 16, v104\n"
  "v_cvt_pk_bf16_f32 v106, v106, v107\n"
  "v_and_b32 v107, 0xffff0000, v106\n"
  "v_lshlrev_b32 v106, 16, v106\n"
  "v_mul_f32 v104, 0x3e715bef, v104\n"
  "v_mul_f32 v105, 0x3e715bef, v105\n"
  "v_mul_f32 v106, 0x3e715bef, v106\n"
  "v_mul_f32 v107, 0x3e715bef, v107\n"
  "v_cvt_pk_bf16_f32 v104, v104, v105\n"
  "v_and_b32 v105, 0xffff0000, v104\n"
  "v_lshlrev_b32 v104, 16, v104\n"
  "v_cvt_pk_bf16_f32 v106, v106, v107\n"
  "v_and_b32 v107, 0xffff0000, v106\n"
  "v_lshlrev_b32 v106, 16, v106\n"
  "v_cndmask_b32 v104, v128, v104, s[8:9]\n"
  "v_cndmask_b32 v105, v128, v105, s[10:11]\n"
  "v_cndmask_b32 v106, v128, v106, s[12:13]\n"
  "v_cndmask_b32 v107, v128, v107, s[14:15]\n"
  "v_max3_f32 v124, v104, v105, v106\n"
  "v_max_f32 v124, v124, v107\n"
  "v_mov_b32 v125, v124\n"
  "s_nop 1\n"
  "v_permlane16_swap_b32 v124, v125\n"
  "v_max_f32 v124, v124, v125\n"
  "v_mov_b32 v125, v124\n"
  "s_nop 1\n"
  "v_permlane32_swap_b32 v124, v125\n"
  "v_max_f32 v124, v124, v125\n"
  "v_sub_f32 v104, v104, v124\n"
  "v_sub_f32 v105, v105, v124\n"
  "v_sub_f32 v106, v106, v124\n"
  "v_sub_f32 v107, v107, v124\n"
  "v_mul_f32 v104, 0x3fb8aa3b, v104\n"
  "v_mul_f32 v105, 0x3fb8aa3b, v105\n"
  "v_mul_f32 v106, 0x3fb8aa3b, v106\n"
  "v_mul_f32 v107, 0x3fb8aa3b, v107\n"
  "v_exp_f32 v104, v104\n"
  "v_exp_f32 v105, v105\n"
  "v_exp_f32 v106, v106\n"
  "v_exp_f32 v107, v107\n"
  "s_mov_b64 s[8:9], s[16:17]\n"
  "s_mov_b64 s[10:11], s[18:19]\n"
  "v_add_f32 v124, v104, v105\n"
  "v_add_f32 v124, v124, v106\n"
  "v_add_f32 v124, v124, v107\n"
  "v_mov_b32 v125, v124\n"
  "s_nop 1\n"
  "v_permlane16_swap_b32 v124, v125\n"
  "v_add_f32 v124, v124, v125\n"
  "v_mov_b32 v125, v124\n"
  "s_nop 1\n"
  "v_permlane32_swap_b32 v124, v125\n"
  "v_add_f32 v124, v124, v125\n"
  "v_rcp_f32 v124, v124\n"
  "s_nop 0\n"
  "v_mul_f32 v104, v124, v104\n"
  "v_mul_f32 v105, v124, v105\n"
  "v_mul_f32 v106, v124, v106\n"
  "v_mul_f32 v107, v124, v107\n"
  "v_cvt_pk_bf16_f32 v108, v104, v104\n"
  "v_cvt_pk_bf16_f32 v109, v105, v105\n"
  "v_cvt_pk_bf16_f32 v110, v106, v106\n"
  "v_cvt_pk_bf16_f32 v111, v107, v107\n"
  "v_and_b32 v108, v8, v108\n"
  "v_and_b32 v109, v8, v109\n"
  "v_and_b32 v110, v8, v110\n"
  "v_and_b32 v111, v8, v111\n"
  "s_nop 1\n"
  "v_mfma_f32_16x16x32_bf16 v[112:115], v[88:91], v[108:111], 0\n"
  "v_mfma_f32_16x16x32_bf16 v[116:119], v[92:95], v[108:111], 0\n"
  "s_barrier\n"
  "s_nop 3\n"
  "v_cvt_pk_bf16_f32 v112, v112, v113\n"
  "v_cvt_pk_bf16_f32 v113, v114, v115\n"
  "s_nop 0\n"
  "v_cvt_pk_bf16_f32 v114, v116, v117\n"
  "v_cvt_pk_bf16_f32 v115, v118, v119\n"
  "ds_write_b64 v3, v[112:113]\n"
  "s_mov_b64 exec, 0xffff\n"
  "ds_write_b64 v3, v[114:115] offset:32\n"
  "s_mov_b64 exec, s[68:69]\n"
  "global_store_dword v127, v126, s[62:63] sc1\n"
  "s_mov_b64 exec, -1\n"
  "s_add_u32 s62, s62, 2048\n"
  "s_addc_u32 s63, s63, 0\n"
  "s_waitcnt lgkmcnt(0)\n"
  "s_barrier\n"
  "ds_read_b128 v[120:123], v4\n"
  "s_bfm_b64 exec, 40, 0\n"
  "s_waitcnt lgkmcnt(0)\n"
  "global_store_dwordx4 v5, v[120:123], s[60:61] sc1\n"
  "s_mov_b64 exec, -1\n"
  "s_add_u32 s60, s60, 40960\n"
  "s_addc_u32 s61, s61, 0\n"
  "s_waitcnt vmcnt(0)\n"
  "s_barrier\n"
  "v_mov_b32 v9, 0\n"
  "v_mov_b32 v15, 64\n"
  "s_cmp_eq_u32 s47, 0\n"
  "s_cselect_b64 exec, 1, 0\n"
  "global_store_dword v9, v15, s[64:65] sc1\n"
  "s_mov_b64 exec, -1\n"
  "s_branch AR\n"
  "AQ:\n"
  "s_endpgm\n"
  "AR:\n"
      : "+{v[0:15]}"(vin), "+{s[36:43]}"(sa), "+{s[44:59]}"(sb), "+{s[4:5]}"(a2) :: "a0", "a1", "a2", "a3", "a4", "a5", "a6", "a7", "a8", "a9", "a10", "a11", "a12", "a13", "a14", "a15", "a16", "a17", "a18", "a19", "a20", "a21", "a22", "a23", "a24", "a25", "a26", "a27", "a28", "a29", "a30", "a31", "a32", "a33", "a34", "a35", "a36", "a37", "a38", "a39", "a40", "a41", "a42", "a43", "a44", "a45", "a46", "a47", "a48", "a49", "a50", "a51", "a52", "a53", "a54", "a55", "a56", "a57", "a58", "a59", "a60", "a61", "a62", "a63", "a64", "a65", "a66", "a67", "a68", "a69", "a70", "a71", "a72", "a73", "a74", "a75", "a76", "a77", "a78", "a79", "a80", "a81", "a82", "a83", "a84", "a85", "a86", "a87", "a88", "a89", "a90", "a91", "a92", "a93", "a94", "a95", "a96", "a97", "a98", "a99", "a100", "a101", "a102", "a103", "a104", "a105", "a106", "a107", "a108", "a109", "a110", "a111", "a112", "a113", "a114", "a115", "a116", "a117", "a118", "a119", "a120", "a121", "a122", "a123", "a124", "a125", "a126", "a127", "a128", "a129", "a130", "a131", "a132", "a133", "a134", "a135", "a136", "a137", "a138", "a139", "a140", "a141", "a142", "a143", "s8", "s9", "s10", "s11", "s12", "s13", "s14", "s15", "s16", "s17", "s18", "s19", "s60", "s61", "s62", "s63", "s64", "s65", "s68", "s69", "s70", "s71", "s74", "s75", "s76", "s77", "s80", "v16", "v17", "v18", "v19", "v20", "v21", "v22", "v23", "v24", "v25", "v26", "v27", "v28", "v29", "v30", "v31", "v32", "v33", "v34", "v35", "v36", "v37", "v38", "v39", "v40", "v41", "v42", "v43", "v44", "v45", "v46", "v47", "v48", "v49", "v50", "v51", "v52", "v53", "v54", "v55", "v56", "v57", "v58", "v59", "v60", "v61", "v62", "v63", "v64", "v65", "v66", "v67", "v68", "v69", "v70", "v71", "v72", "v73", "v74", "v75", "v76", "v77", "v78", "v79", "v80", "v81", "v82", "v83", "v84", "v85", "v86", "v87", "v88", "v89", "v90", "v91", "v92", "v93", "v94", "v95", "v96", "v97", "v98", "v99", "v100", "v101", "v102", "v103", "v104", "v105", "v106", "v107", "v108", "v109", "v110", "v111", "v112", "v113", "v114", "v115", "v116", "v117", "v118", "v119", "v120", "v121", "v122", "v123", "v124", "v125", "v126", "v127", "v128", "vcc", "scc", "memory");
  }
#pragma unroll
  for (int i = 0; i < 16; ++i) { vin[i] = 0; sb[i] = 0; sa[i & 7] = 0; }
  vin[0] = lane * 16;
  if (tk >= 24) { ARGS; PTR(sa, 0, p.ws);
    const bool last = tk < 28;
    const int h = fd * 8 + (tk & 3) * 2 + (wave & 1);
    if (role == 255 && wave == 2) {
      unsigned tag = (unsigned)ld8(p.ws, B_S + lane * 8), n_ = rfl(tag) + 1;
      while (__any(tag == n_)) ++n_;
      if (lane == 0) ctl_st(p.ws, B_N, n_);
      unsigned long long v = 0;
      for (int i = 0;; ++i) {
        v = ld8(p.ws, B_S + lane * 8);
        if (__all((unsigned)v == n_)) break;
        if (i == SPIN) return;
      }
      int d = (int)((v >> 32) & 4095) * ((lane & 1) ? 1 : -1);
#pragma unroll
      for (int o = 32; o; o >>= 1) d += __shfl_xor(d, o);
      const unsigned b1 = d < 0;
      const unsigned long long yes = __ballot((v >> 44) & 1) >> b1;
      const unsigned cls = (((__popcll(yes & 0x1111111111111111ull) > 8) | (b1 << 1) | ((__popcll(yes & 0x4444444444444444ull) > 8) << 2)) * 0x1111u) ^ 0x7160u;
      unsigned q[4] = {0, 0, 0, 0};
      for (int i = 0; i < SPIN; ++i) {
#pragma unroll
        for (int k = 0; k < 4; ++k) {
          const unsigned t = ctl_ld(p.ws, B_Q + (k * 64 + lane) * 4);
          if (i == 0 || t != q[k]) st8(p.ws, B_R + (k * 64 + lane) * 8, t, cls);
          q[k] = t;
        }
        __builtin_amdgcn_s_sleep(32);
        if (__all(ctl_ld(p.ws, C_ACK + (lane & 7) * 128) >= 32)) break;
      }
    }
    if (wave >= 2) return;
    vin[1] = lane < 2 ? (C_KVP) + (2 * h + lane) * 128 : lane == 2 ? (C_HF) + h * 256 : (C_VF) + (2 * h + (lane & 1)) * 128;
    vin[2] = lane < 2 ? 2 : 1; vin[3] = qq * 256 + g * 64 + c4 * 16;
    vin[4] = wave * 2048 + lane * 16; vin[5] = wave * 2048 + c4 * 128 + (4 * g + qq) * 8;
    vin[6] = n - 4 * g;
    sb[0] = KVH + h * 65536 + 4096; sb[1] = h * 65536 + 4096 + 32768; sb[2] = WO_KM + h * 2048; sb[3] = sb[2] + (WO_VP - WO_KM);
    sb[4] = sb[1] + 16384 + (last ? 8192 : 0); sb[5] = (last ? C_QAL : C_QAP) + h * 128; sb[10] = wave + last; sb[8] = last ? 63 : 0; sb[9] = last ? 1 : 63; sb[6] = __float_as_uint(QS); sb[7] = WO_ST + (role * 4 + wave) * 4096 + (last ? 60 * 64 : 0);
    UNIFORM(sa, 8); UNIFORM(sb, 16);
    asm volatile(
  "s_nop 4\n"
  "v_cmp_le_i32 s[8:9], 0, v6\n"
  "v_cmp_le_i32 s[10:11], 1, v6\n"
  "v_cmp_le_i32 s[12:13], 2, v6\n"
  "v_cmp_le_i32 s[14:15], 3, v6\n"
  "v_mov_b64 v[156:157], 0\n"
  "v_mov_b64 v[158:159], 0\n"
  "v_mov_b64 v[160:161], 0\n"
  "v_mov_b64 v[162:163], 0\n"
  "v_mov_b64 v[164:165], 0\n"
  "v_mov_b64 v[166:167], 0\n"
  "v_mov_b64 v[168:169], 0\n"
  "v_mov_b64 v[170:171], 0\n"
  "v_mov_b64 v[172:173], 0\n"
  "v_mov_b64 v[174:175], 0\n"
  "v_mov_b64 v[176:177], 0\n"
  "v_mov_b64 v[178:179], 0\n"
  "v_mov_b64 v[180:181], 0\n"
  "v_mov_b64 v[182:183], 0\n"
  "v_mov_b64 v[184:185], 0\n"
  "v_mov_b64 v[186:187], 0\n"
  "v_mov_b64 v[150:151], 0\n"
  "v_mov_b32 v12, 0xff7fffff\n"
  "s_mov_b32 s56, 0\n"
  "s_mov_b32 s57, 400000\n"
  "s_add_u32 s74, s36, s5\n"
  "s_addc_u32 s75, s37, 0\n"
  "BLT:\n"
  "v_mov_b32 v8, 0\n"
  "global_load_dwordx2 v[8:9], v8, s[74:75] sc1\n"
  "s_waitcnt vmcnt(0)\n"
  "v_readfirstlane_b32 s77, v8\n"
  "v_readfirstlane_b32 s76, v9\n"
  "s_cmp_eq_u32 s77, s4\n"
  "s_cbranch_scc1 BDT\n"
  "s_sub_u32 s57, s57, 1\n"
  "s_cbranch_scc1 TQUIT\n"
  "s_sleep 4\n"
  "s_branch BLT\n"
  "BDT:\n"
  "s_sub_u32 s77, s5, 16966656\n"
  "s_lshr_b32 s77, s77, 8\n"
  "s_lshl_b32 s77, s77, 7\n"
  "s_add_u32 s77, s77, 16777216\n"
  "s_add_u32 s74, s36, s77\n"
  "s_addc_u32 s75, s37, 0\n"
  "v_mov_b32 v8, 0\n"
  "v_mov_b32 v9, 1\n"
  "s_cmp_eq_u32 s54, 0\n"
  "s_cselect_b64 exec, 1, 0\n"
  "global_atomic_add v8, v9, s[74:75] sc1\n"
  "s_mov_b64 exec, -1\n"
  "s_and_b32 s74, s76, 2\n"
  "s_lshr_b32 s75, s74, 1\n"
  "s_lshl_b32 s4, 0x55555555, s75\n"
  "s_mov_b32 s5, s4\n"
  "s_lshl_b32 s74, s74, 12\n"
  "v_xor_b32 v1, s74, v1\n"
  "s_xor_b32 s49, s74, s49\n"
  "TINIT:\n"
  "s_add_u32 s64, s36, s46\n"
  "s_addc_u32 s65, s37, 0\n"
  "s_add_u32 s66, s36, s47\n"
  "s_addc_u32 s67, s37, 0\n"
  "s_add_u32 s70, s36, s49\n"
  "s_addc_u32 s71, s37, 0\n"
  "s_bitcmp1_b64 s[4:5], s56\n"
  "s_cselect_b32 s74, 8192, 0\n"
  "s_and_b32 s75, s56, 63\n"
  "s_lshl_b32 s75, s75, 21\n"
  "s_add_u32 s74, s74, s75\n"
  "s_add_u32 s74, s74, 20971520\n"
  "s_xor_b32 s75, s74, s44\n"
  "s_add_u32 s60, s36, s75\n"
  "s_addc_u32 s61, s37, 0\n"
  "s_xor_b32 s75, s74, s45\n"
  "s_add_u32 s62, s36, s75\n"
  "s_addc_u32 s63, s37, 0\n"
  "s_mov_b64 exec, 3\n"
  "TPS:\n"
  "global_load_dword v9, v1, s[36:37] sc1\n"
  "v_add_u32 v7, s56, v2\n"
  "v_add_u32 v7, -1, v7\n"
  "v_min_i32 v7, 64, v7\n"
  "s_waitcnt vmcnt(0)\n"
  "v_cmp_gt_i32 vcc, v7, v9\n"
  "s_cbranch_vccz TGS\n"
  "s_sub_u32 s57, s57, 1\n"
  "s_cbranch_scc1 TQUIT\n"
  "s_sleep 2\n"
  "s_branch TPS\n"
  "TGS:\n"
  "s_mov_b64 exec, -1\n"
  "global_load_dwordx4 v[16:19], v0, s[62:63] offset:-4096 sc1\n"
  "global_load_dwordx4 v[20:23], v0, s[62:63] offset:-3072 sc1\n"
  "global_load_dwordx4 v[24:27], v0, s[62:63] offset:-2048 sc1\n"
  "global_load_dwordx4 v[28:31], v0, s[62:63] offset:-1024 sc1\n"
  "global_load_dwordx4 v[32:35], v0, s[62:63] offset:0 sc1\n"
  "global_load_dwordx4 v[36:39], v0, s[62:63] offset:1024 sc1\n"
  "global_load_dwordx4 v[40:43], v0, s[62:63] offset:2048 sc1\n"
  "global_load_dwordx4 v[44:47], v0, s[62:63] offset:3072 sc1\n"
  "global_load_dwordx4 a[0:3], v0, s[60:61] offset:-4096 sc1\n"
  "global_load_dwordx4 a[4:7], v0, s[60:61] offset:-3072 sc1\n"
  "global_load_dwordx4 a[8:11], v0, s[60:61] offset:-2048 sc1\n"
  "global_load_dwordx4 a[12:15], v0, s[60:61] offset:-1024 sc1\n"
  "global_load_dwordx4 a[16:19], v0, s[60:61] offset:0 sc1\n"
  "global_load_dwordx4 a[20:23], v0, s[60:61] offset:1024 sc1\n"
  "global_load_dwordx4 a[24:27], v0, s[60:61] offset:2048 sc1\n"
  "global_load_dwordx4 a[28:31], v0, s[60:61] offset:3072 sc1\n"
  "s_add_u32 s60, s60, 16384\n"
  "s_addc_u32 s61, s61, 0\n"
  "global_load_dwordx4 a[32:35], v0, s[60:61] offset:-4096 sc1\n"
  "global_load_dwordx4 a[36:39], v0, s[60:61] offset:-3072 sc1\n"
  "global_load_dwordx4 a[40:43], v0, s[60:61] offset:-2048 sc1\n"
  "global_load_dwordx4 a[44:47], v0, s[60:61] offset:-1024 sc1\n"
  "global_load_dwordx4 a[48:51], v0, s[60:61] offset:0 sc1\n"
  "global_load_dwordx4 a[52:55], v0, s[60:61] offset:1024 sc1\n"
  "global_load_dwordx4 a[56:59], v0, s[60:61] offset:2048 sc1\n"
  "global_load_dwordx4 a[60:63], v0, s[60:61] offset:3072 sc1\n"
  "s_add_u32 s76, s56, 1\n"
  "s_min_u32 s76, s76, 63\n"
  "s_bitcmp1_b64 s[4:5], s76\n"
  "s_cselect_b32 s74, 8192, 0\n"
  "s_and_b32 s75, s76, 63\n"
  "s_lshl_b32 s75, s75, 21\n"
  "s_add_u32 s74, s74, s75\n"
  "s_add_u32 s74, s74, 20971520\n"
  "s_xor_b32 s75, s74, s44\n"
  "s_add_u32 s60, s36, s75\n"
  "s_addc_u32 s61, s37, 0\n"
  "s_xor_b32 s75, s74, s45\n"
  "s_add_u32 s62, s36, s75\n"
  "s_addc_u32 s63, s37, 0\n"
  "s_mov_b64 s[16:17], -1\n"
  "s_waitcnt vmcnt(0)\n"
  "TOP0:\n"
  "s_waitcnt vmcnt(8)\n"
  "ds_write_b128 v4, v[80:83] offset:0\n"
  "ds_write_b128 v4, v[84:87] offset:1024\n"
  "ds_read_b64_tr_b16 v[88:89], v5 offset:0\n"
  "ds_read_b64_tr_b16 v[90:91], v5 offset:512\n"
  "ds_read_b64_tr_b16 v[92:93], v5 offset:1024\n"
  "ds_read_b64_tr_b16 v[94:95], v5 offset:1536\n"
  "v_mfma_f32_16x16x32_bf16 v[120:123], a[0:3], v[16:19], 0\n"
  "v_mfma_f32_16x16x32_bf16 v[124:127], a[0:3], v[20:23], 0\n"
  "v_mfma_f32_16x16x32_bf16 v[128:131], a[4:7], v[20:23], 0\n"
  "v_mfma_f32_16x16x32_bf16 v[120:123], a[8:11], v[24:27], v[120:123]\n"
  "v_mfma_f32_16x16x32_bf16 v[124:127], a[8:11], v[28:31], v[124:127]\n"
  "v_mfma_f32_16x16x32_bf16 v[128:131], a[12:15], v[28:31], v[128:131]\n"
  "v_mfma_f32_16x16x32_bf16 v[120:123], a[16:19], v[32:35], v[120:123]\n"
  "v_mfma_f32_16x16x32_bf16 v[124:127], a[16:19], v[36:39], v[124:127]\n"
  "v_mfma_f32_16x16x32_bf16 v[128:131], a[20:23], v[36:39], v[128:131]\n"
  "v_mfma_f32_16x16x32_bf16 v[120:123], a[24:27], v[40:43], v[120:123]\n"
  "v_mfma_f32_16x16x32_bf16 v[124:127], a[24:27], v[44:47], v[124:127]\n"
  "v_mfma_f32_16x16x32_bf16 v[128:131], a[28:31], v[44:47], v[128:131]\n"
  "s_cmp_eq_u64 s[16:17], 0\n"
  "s_cbranch_scc1 TGO0\n"
  "s_waitcnt vmcnt(0)\n"
  "v_mov_b32 v7, 0\n"
  "v_mov_b32 v8, s56\n"
  "s_mov_b64 exec, 1\n"
  "global_store_dword v7, v8, s[70:71] sc1\n"
  "s_mov_b64 exec, -1\n"
  "TP0:\n"
  "global_load_dword v9, v1, s[36:37] sc1\n"
  "v_add_u32 v7, s56, v2\n"
  "v_add_u32 v7, 0, v7\n"
  "v_min_i32 v7, 64, v7\n"
  "s_waitcnt vmcnt(0)\n"
  "v_cmp_gt_i32 vcc, v7, v9\n"
  "s_cbranch_vccz TG0\n"
  "s_sub_u32 s57, s57, 1\n"
  "s_cbranch_scc1 TQUIT\n"
  "s_sleep 2\n"
  "s_branch TP0\n"
  "TG0:\n"
  "global_load_dwordx4 v[80:83], v0, s[64:65] offset:0 sc1\n"
  "global_load_dwordx4 v[84:87], v0, s[64:65] offset:1024 sc1\n"
  "global_load_dwordx4 v[96:99], v3, s[66:67] offset:0 sc1\n"
  "global_load_dwordx4 v[100:103], v3, s[66:67] offset:1024 sc1\n"
  "s_add_u32 s64, s64, 65536\n"
  "s_addc_u32 s65, s65, 0\n"
  "s_add_u32 s66, s66, 65536\n"
  "s_addc_u32 s67, s67, 0\n"
  "s_waitcnt vmcnt(3)\n"
  "ds_write_b128 v4, v[80:83] offset:0\n"
  "s_waitcnt vmcnt(2)\n"
  "ds_write_b128 v4, v[84:87] offset:1024\n"
  "s_waitcnt lgkmcnt(5)\n"
  "ds_read_b64_tr_b16 v[88:89], v5 offset:0\n"
  "s_waitcnt lgkmcnt(5)\n"
  "ds_read_b64_tr_b16 v[90:91], v5 offset:512\n"
  "s_waitcnt lgkmcnt(5)\n"
  "ds_read_b64_tr_b16 v[92:93], v5 offset:1024\n"
  "s_waitcnt lgkmcnt(5)\n"
  "ds_read_b64_tr_b16 v[94:95], v5 offset:1536\n"
  "s_waitcnt vmcnt(0)\n"
  "TGO0:\n"
  "s_waitcnt lgkmcnt(3)\n"
  "v_mfma_f32_16x16x16_bf16 v[132:135], v[88:89], v[16:17], 0\n"
  "global_load_dword v9, v1, s[36:37] sc1\n"
  "v_mfma_f32_16x16x16_bf16 v[140:143], v[88:89], v[18:19], 0\n"
  "global_load_dwordx4 v[48:51], v0, s[62:63] offset:-4096 sc1\n"
  "s_waitcnt lgkmcnt(2)\n"
  "v_mfma_f32_16x16x16_bf16 v[132:135], v[90:91], v[24:25], v[132:135]\n"
  "global_load_dwordx4 v[52:55], v0, s[62:63] offset:-3072 sc1\n"
  "v_mfma_f32_16x16x16_bf16 v[140:143], v[90:91], v[26:27], v[140:143]\n"
  "global_load_dwordx4 v[56:59], v0, s[62:63] offset:-2048 sc1\n"
  "s_waitcnt lgkmcnt(1)\n"
  "v_mfma_f32_16x16x16_bf16 v[132:135], v[92:93], v[32:33], v[132:135]\n"
  "global_load_dwordx4 v[60:63], v0, s[62:63] offset:-1024 sc1\n"
  "v_mfma_f32_16x16x16_bf16 v[140:143], v[92:93], v[34:35], v[140:143]\n"
  "global_load_dwordx4 v[64:67], v0, s[62:63] offset:0 sc1\n"
  "s_waitcnt lgkmcnt(0)\n"
  "v_mfma_f32_16x16x16_bf16 v[132:135], v[94:95], v[40:41], v[132:135]\n"
  "global_load_dwordx4 v[68:71], v0, s[62:63] offset:1024 sc1\n"
  "v_mfma_f32_16x16x16_bf16 v[140:143], v[94:95], v[42:43], v[140:143]\n"
  "global_load_dwordx4 v[72:75], v0, s[62:63] offset:2048 sc1\n"
  "v_mfma_f32_16x16x16_bf16 v[136:139], v[88:89], v[20:21], 0\n"
  "global_load_dwordx4 v[76:79], v0, s[62:63] offset:3072 sc1\n"
  "v_mfma_f32_16x16x16_bf16 v[144:147], v[88:89], v[22:23], 0\n"
  "global_load_dwordx4 a[0:3], v0, s[60:61] offset:-4096 sc1\n"
  "v_mfma_f32_16x16x16_bf16 v[136:139], v[90:91], v[28:29], v[136:139]\n"
  "global_load_dwordx4 a[4:7], v0, s[60:61] offset:-3072 sc1\n"
  "v_mfma_f32_16x16x16_bf16 v[144:147], v[90:91], v[30:31], v[144:147]\n"
  "global_load_dwordx4 a[8:11], v0, s[60:61] offset:-2048 sc1\n"
  "v_mfma_f32_16x16x16_bf16 v[136:139], v[92:93], v[36:37], v[136:139]\n"
  "v_cndmask_b32 v120, v12, v120, s[8:9]\n"
  "v_cndmask_b32 v121, v12, v121, s[10:11]\n"
  "v_mfma_f32_16x16x16_bf16 v[144:147], v[92:93], v[38:39], v[144:147]\n"
  "v_cndmask_b32 v122, v12, v122, s[12:13]\n"
  "v_cndmask_b32 v123, v12, v123, s[14:15]\n"
  "v_mfma_f32_16x16x16_bf16 v[136:139], v[94:95], v[44:45], v[136:139]\n"
  "v_cndmask_b32 v128, v12, v128, s[8:9]\n"
  "v_cndmask_b32 v129, v12, v129, s[10:11]\n"
  "v_mfma_f32_16x16x16_bf16 v[144:147], v[94:95], v[46:47], v[144:147]\n"
  "v_cndmask_b32 v130, v12, v130, s[12:13]\n"
  "v_cndmask_b32 v131, v12, v131, s[14:15]\n"
  "v_add_f32 v132, v132, v142\n"
  "v_add_f32 v133, v133, v143\n"
  "v_mul_f32 v132, s50, v132\n"
  "v_mul_f32 v133, s50, v133\n"
  "v_max_f32 v10, v132, v133\n"
  "v_max3_f32 v10, v10, v120, v121\n"
  "v_max3_f32 v10, v10, v122, v123\n"
  "v_mov_b32 v11, v10\n"
  "s_nop 1\n"
  "v_permlane16_swap_b32 v10, v11\n"
  "v_max_f32 v10, v10, v11\n"
  "v_mov_b32 v11, v10\n"
  "s_nop 1\n"
  "v_permlane32_swap_b32 v10, v11\n"
  "v_max_f32 v10, v10, v11\n"
  "v_sub_f32 v120, v120, v10\n"
  "v_sub_f32 v121, v121, v10\n"
  "v_sub_f32 v122, v122, v10\n"
  "v_sub_f32 v123, v123, v10\n"
  "v_sub_f32 v132, v132, v10\n"
  "v_sub_f32 v133, v133, v10\n"
  "v_exp_f32 v120, v120\n"
  "v_exp_f32 v121, v121\n"
  "v_exp_f32 v122, v122\n"
  "v_exp_f32 v123, v123\n"
  "v_exp_f32 v132, v132\n"
  "v_exp_f32 v133, v133\n"
  "v_add_f32 v10, v120, v121\n"
  "v_add_f32 v10, v10, v122\n"
  "v_add_f32 v10, v10, v123\n"
  "v_add_f32 v10, v10, v132\n"
  "v_add_f32 v10, v10, v133\n"
  "v_mov_b32 v11, v10\n"
  "s_nop 1\n"
  "v_permlane16_swap_b32 v10, v11\n"
  "v_add_f32 v10, v10, v11\n"
  "v_mov_b32 v11, v10\n"
  "s_nop 1\n"
  "v_permlane32_swap_b32 v10, v11\n"
  "v_add_f32 v10, v10, v11\n"
  "v_rcp_f32 v10, v10\n"
  "s_nop 0\n"
  "v_mul_f32 v120, v10, v120\n"
  "v_mul_f32 v121, v10, v121\n"
  "v_mul_f32 v122, v10, v122\n"
  "v_mul_f32 v123, v10, v123\n"
  "v_mul_f32 v132, v10, v132\n"
  "v_mul_f32 v133, v10, v133\n"
  "v_cvt_pk_bf16_f32 v148, v120, v121\n"
  "v_cvt_pk_bf16_f32 v149, v122, v123\n"
  "v_cvt_pk_bf16_f32 v156, v132, v133\n"
  "v_cvt_pk_bf16_f32 v161, v132, v133\n"
  "v_cvt_pk_bf16_f32 v166, v132, v133\n"
  "v_cvt_pk_bf16_f32 v171, v132, v133\n"
  "v_add_u32 v7, s56, v2\n"
  "v_add_u32 v7, 1, v7\n"
  "v_min_i32 v7, 64, v7\n"
  "s_waitcnt vmcnt(11)\n"
  "v_cmp_gt_i32 s[16:17], v7, v9\n"
  "v_mov_b32 v7, 0\n"
  "v_mov_b32 v8, s56\n"
  "s_mov_b64 exec, 1\n"
  "global_store_dword v7, v8, s[70:71] sc1\n"
  "s_mov_b64 exec, -1\n"
  "s_cmp_lg_u64 s[16:17], 0\n"
  "s_cbranch_scc1 TNM0\n"
  "global_load_dwordx4 v[80:83], v0, s[64:65] offset:0 sc1\n"
  "global_load_dwordx4 v[84:87], v0, s[64:65] offset:1024 sc1\n"
  "global_load_dwordx4 v[104:107], v3, s[66:67] offset:0 sc1\n"
  "global_load_dwordx4 v[108:111], v3, s[66:67] offset:1024 sc1\n"
  "s_add_u32 s64, s64, 65536\n"
  "s_addc_u32 s65, s65, 0\n"
  "s_add_u32 s66, s66, 65536\n"
  "s_addc_u32 s67, s67, 0\n"
  "TNM0:\n"
  "v_mfma_f32_16x16x32_bf16 v[188:191], a[32:35], v[148:151], 0\n"
  "global_load_dwordx4 a[12:15], v0, s[60:61] offset:-1024 sc1\n"
  "v_mfma_f32_16x16x32_bf16 v[188:191], v[96:99], v[156:159], v[188:191]\n"
  "global_load_dwordx4 a[16:19], v0, s[60:61] offset:0 sc1\n"
  "v_mfma_f32_16x16x32_bf16 v[192:195], a[36:39], v[148:151], 0\n"
  "global_load_dwordx4 a[20:23], v0, s[60:61] offset:1024 sc1\n"
  "v_mfma_f32_16x16x32_bf16 v[192:195], v[96:99], v[160:163], v[192:195]\n"
  "global_load_dwordx4 a[24:27], v0, s[60:61] offset:2048 sc1\n"
  "v_mfma_f32_16x16x32_bf16 v[196:199], a[40:43], v[148:151], 0\n"
  "global_load_dwordx4 a[28:31], v0, s[60:61] offset:3072 sc1\n"
  "v_mfma_f32_16x16x32_bf16 v[196:199], v[96:99], v[164:167], v[196:199]\n"
  "s_add_u32 s60, s60, 16384\n"
  "s_addc_u32 s61, s61, 0\n"
  "global_load_dwordx4 a[64:67], v0, s[60:61] offset:-4096 sc1\n"
  "v_mfma_f32_16x16x32_bf16 v[200:203], a[44:47], v[148:151], 0\n"
  "global_load_dwordx4 a[68:71], v0, s[60:61] offset:-3072 sc1\n"
  "v_mfma_f32_16x16x32_bf16 v[200:203], v[96:99], v[168:171], v[200:203]\n"
  "global_load_dwordx4 a[72:75], v0, s[60:61] offset:-2048 sc1\n"
  "v_mfma_f32_16x16x32_bf16 v[204:207], a[48:51], v[148:151], 0\n"
  "global_load_dwordx4 a[76:79], v0, s[60:61] offset:-1024 sc1\n"
  "v_mfma_f32_16x16x32_bf16 v[204:207], v[100:103], v[156:159], v[204:207]\n"
  "global_load_dwordx4 a[80:83], v0, s[60:61] offset:0 sc1\n"
  "v_mfma_f32_16x16x32_bf16 v[208:211], a[52:55], v[148:151], 0\n"
  "global_load_dwordx4 a[84:87], v0, s[60:61] offset:1024 sc1\n"
  "v_mfma_f32_16x16x32_bf16 v[208:211], v[100:103], v[160:163], v[208:211]\n"
  "global_load_dwordx4 a[88:91], v0, s[60:61] offset:2048 sc1\n"
  "v_mfma_f32_16x16x32_bf16 v[212:215], a[56:59], v[148:151], 0\n"
  "global_load_dwordx4 a[92:95], v0, s[60:61] offset:3072 sc1\n"
  "v_mfma_f32_16x16x32_bf16 v[212:215], v[100:103], v[164:167], v[212:215]\n"
  "v_mfma_f32_16x16x32_bf16 v[216:219], a[60:63], v[148:151], 0\n"
  "v_mfma_f32_16x16x32_bf16 v[216:219], v[100:103], v[168:171], v[216:219]\n"
  "s_add_u32 s76, s56, 2\n"
  "s_min_u32 s76, s76, 63\n"
  "s_bitcmp1_b64 s[4:5], s76\n"
  "s_cselect_b32 s74, 8192, 0\n"
  "s_and_b32 s75, s76, 63\n"
  "s_lshl_b32 s75, s75, 21\n"
  "s_add_u32 s74, s74, s75\n"
  "s_add_u32 s74, s74, 20971520\n"
  "s_xor_b32 s75, s74, s44\n"
  "s_add_u32 s60, s36, s75\n"
  "s_addc_u32 s61, s37, 0\n"
  "s_xor_b32 s75, s74, s45\n"
  "s_add_u32 s62, s36, s75\n"
  "s_addc_u32 s63, s37, 0\n"
  "s_bitcmp1_b64 s[4:5], s56\n"
  "s_cselect_b32 s74, 8192, 0\n"
  "s_and_b32 s75, s56, 63\n"
  "s_lshl_b32 s75, s75, 21\n"
  "s_add_u32 s74, s74, s75\n"
  "s_add_u32 s74, s74, 20971520\n"
  "s_xor_b32 s75, s74, s48\n"
  "s_add_u32 s68, s36, s75\n"
  "s_addc_u32 s69, s37, 0\n"
  "v_add_f32 v136, v136, v146\n"
  "v_add_f32 v137, v137, v147\n"
  "v_mul_f32 v136, s50, v136\n"
  "v_mul_f32 v137, s50, v137\n"
  "v_max_f32 v10, v136, v137\n"
  "v_max3_f32 v10, v10, v124, v125\n"
  "v_max3_f32 v10, v10, v126, v127\n"
  "v_max3_f32 v10, v10, v128, v129\n"
  "v_max3_f32 v10, v10, v130, v131\n"
  "v_mov_b32 v11, v10\n"
  "s_nop 1\n"
  "v_permlane16_swap_b32 v10, v11\n"
  "v_max_f32 v10, v10, v11\n"
  "v_mov_b32 v11, v10\n"
  "s_nop 1\n"
  "v_permlane32_swap_b32 v10, v11\n"
  "v_max_f32 v10, v10, v11\n"
  "v_sub_f32 v124, v124, v10\n"
  "v_sub_f32 v125, v125, v10\n"
  "v_sub_f32 v126, v126, v10\n"
  "v_sub_f32 v127, v127, v10\n"
  "v_sub_f32 v128, v128, v10\n"
  "v_sub_f32 v129, v129, v10\n"
  "v_sub_f32 v130, v130, v10\n"
  "v_sub_f32 v131, v131, v10\n"
  "v_sub_f32 v136, v136, v10\n"
  "v_sub_f32 v137, v137, v10\n"
  "v_exp_f32 v124, v124\n"
  "v_exp_f32 v125, v125\n"
  "v_exp_f32 v126, v126\n"
  "v_exp_f32 v127, v127\n"
  "v_exp_f32 v128, v128\n"
  "v_exp_f32 v129, v129\n"
  "v_exp_f32 v130, v130\n"
  "v_exp_f32 v131, v131\n"
  "v_exp_f32 v136, v136\n"
  "v_exp_f32 v137, v137\n"
  "v_add_f32 v10, v124, v125\n"
  "v_add_f32 v10, v10, v126\n"
  "v_add_f32 v10, v10, v127\n"
  "v_add_f32 v10, v10, v128\n"
  "v_add_f32 v10, v10, v129\n"
  "v_add_f32 v10, v10, v130\n"
  "v_add_f32 v10, v10, v131\n"
  "v_add_f32 v10, v10, v136\n"
  "v_add_f32 v10, v10, v137\n"
  "v_mov_b32 v11, v10\n"
  "s_nop 1\n"
  "v_permlane16_swap_b32 v10, v11\n"
  "v_add_f32 v10, v10, v11\n"
  "v_mov_b32 v11, v10\n"
  "s_nop 1\n"
  "v_permlane32_swap_b32 v10, v11\n"
  "v_add_f32 v10, v10, v11\n"
  "v_rcp_f32 v10, v10\n"
  "s_nop 0\n"
  "v_mul_f32 v124, v10, v124\n"
  "v_mul_f32 v125, v10, v125\n"
  "v_mul_f32 v126, v10, v126\n"
  "v_mul_f32 v127, v10, v127\n"
  "v_mul_f32 v128, v10, v128\n"
  "v_mul_f32 v129, v10, v129\n"
  "v_mul_f32 v130, v10, v130\n"
  "v_mul_f32 v131, v10, v131\n"
  "v_mul_f32 v136, v10, v136\n"
  "v_mul_f32 v137, v10, v137\n"
  "v_cvt_pk_bf16_f32 v152, v124, v125\n"
  "v_cvt_pk_bf16_f32 v153, v126, v127\n"
  "v_cvt_pk_bf16_f32 v154, v128, v129\n"
  "v_cvt_pk_bf16_f32 v155, v130, v131\n"
  "v_cvt_pk_bf16_f32 v172, v136, v137\n"
  "v_cvt_pk_bf16_f32 v177, v136, v137\n"
  "v_cvt_pk_bf16_f32 v182, v136, v137\n"
  "v_cvt_pk_bf16_f32 v187, v136, v137\n"
  "v_cvt_pk_bf16_f32 v220, v188, v189\n"
  "v_cvt_pk_bf16_f32 v221, v190, v191\n"
  "v_cvt_pk_bf16_f32 v222, v192, v193\n"
  "v_cvt_pk_bf16_f32 v223, v194, v195\n"
  "global_store_dwordx4 v0, v[220:223], s[68:69] offset:-4096 sc1\n"
  "v_mfma_f32_16x16x32_bf16 v[188:191], a[32:35], v[152:155], 0\n"
  "v_cvt_pk_bf16_f32 v224, v196, v197\n"
  "v_cvt_pk_bf16_f32 v225, v198, v199\n"
  "v_mfma_f32_16x16x32_bf16 v[188:191], v[96:99], v[172:175], v[188:191]\n"
  "v_cvt_pk_bf16_f32 v226, v200, v201\n"
  "v_cvt_pk_bf16_f32 v227, v202, v203\n"
  "v_mfma_f32_16x16x32_bf16 v[192:195], a[36:39], v[152:155], 0\n"
  "global_store_dwordx4 v0, v[224:227], s[68:69] offset:-3072 sc1\n"
  "v_mfma_f32_16x16x32_bf16 v[192:195], v[96:99], v[176:179], v[192:195]\n"
  "v_mfma_f32_16x16x32_bf16 v[196:199], a[40:43], v[152:155], 0\n"
  "v_cvt_pk_bf16_f32 v228, v204, v205\n"
  "v_cvt_pk_bf16_f32 v229, v206, v207\n"
  "v_mfma_f32_16x16x32_bf16 v[196:199], v[96:99], v[180:183], v[196:199]\n"
  "v_cvt_pk_bf16_f32 v230, v208, v209\n"
  "v_cvt_pk_bf16_f32 v231, v210, v211\n"
  "v_mfma_f32_16x16x32_bf16 v[200:203], a[44:47], v[152:155], 0\n"
  "global_store_dwordx4 v0, v[228:231], s[68:69] offset:-2048 sc1\n"
  "v_mfma_f32_16x16x32_bf16 v[200:203], v[96:99], v[184:187], v[200:203]\n"
  "v_mfma_f32_16x16x32_bf16 v[204:207], a[48:51], v[152:155], 0\n"
  "v_cvt_pk_bf16_f32 v232, v212, v213\n"
  "v_cvt_pk_bf16_f32 v233, v214, v215\n"
  "v_mfma_f32_16x16x32_bf16 v[204:207], v[100:103], v[172:175], v[204:207]\n"
  "v_cvt_pk_bf16_f32 v234, v216, v217\n"
  "v_cvt_pk_bf16_f32 v235, v218, v219\n"
  "v_mfma_f32_16x16x32_bf16 v[208:211], a[52:55], v[152:155], 0\n"
  "global_store_dwordx4 v0, v[232:235], s[68:69] offset:-1024 sc1\n"
  "v_mfma_f32_16x16x32_bf16 v[208:211], v[100:103], v[176:179], v[208:211]\n"
  "v_mfma_f32_16x16x32_bf16 v[212:215], a[56:59], v[152:155], 0\n"
  "v_cvt_pk_bf16_f32 v220, v188, v189\n"
  "v_cvt_pk_bf16_f32 v221, v190, v191\n"
  "v_mfma_f32_16x16x32_bf16 v[212:215], v[100:103], v[180:183], v[212:215]\n"
  "v_cvt_pk_bf16_f32 v222, v192, v193\n"
  "v_cvt_pk_bf16_f32 v223, v194, v195\n"
  "v_mfma_f32_16x16x32_bf16 v[216:219], a[60:63], v[152:155], 0\n"
  "global_store_dwordx4 v0, v[220:223], s[68:69] offset:0 sc1\n"
  "v_mfma_f32_16x16x32_bf16 v[216:219], v[100:103], v[184:187], v[216:219]\n"
  "v_cvt_pk_bf16_f32 v224, v196, v197\n"
  "v_cvt_pk_bf16_f32 v225, v198, v199\n"
  "v_cvt_pk_bf16_f32 v226, v200, v201\n"
  "v_cvt_pk_bf16_f32 v227, v202, v203\n"
  "global_store_dwordx4 v0, v[224:227], s[68:69] offset:1024 sc1\n"
  "v_cvt_pk_bf16_f32 v228, v204, v205\n"
  "v_cvt_pk_bf16_f32 v229, v206, v207\n"
  "v_cvt_pk_bf16_f32 v230, v208, v209\n"
  "v_cvt_pk_bf16_f32 v231, v210, v211\n"
  "global_store_dwordx4 v0, v[228:231], s[68:69] offset:2048 sc1\n"
  "v_cvt_pk_bf16_f32 v232, v212, v213\n"
  "v_cvt_pk_bf16_f32 v233, v214, v215\n"
  "v_cvt_pk_bf16_f32 v234, v216, v217\n"
  "v_cvt_pk_bf16_f32 v235, v218, v219\n"
  "global_store_dwordx4 v0, v[232:235], s[68:69] offset:3072 sc1\n"
  "s_add_u32 s56, s56, 1\n"
  "s_cmp_lt_u32 s56, s53\n"
  "s_cbranch_scc0 TEND\n"
  "TOP1:\n"
  "s_waitcnt vmcnt(8)\n"
  "ds_write_b128 v4, v[80:83] offset:0\n"
  "ds_write_b128 v4, v[84:87] offset:1024\n"
  "ds_read_b64_tr_b16 v[88:89], v5 offset:0\n"
  "ds_read_b64_tr_b16 v[90:91], v5 offset:512\n"
  "ds_read_b64_tr_b16 v[92:93], v5 offset:1024\n"
  "ds_read_b64_tr_b16 v[94:95], v5 offset:1536\n"
  "v_mfma_f32_16x16x32_bf16 v[120:123], a[0:3], v[48:51], 0\n"
  "v_mfma_f32_16x16x32_bf16 v[124:127], a[0:3], v[52:55], 0\n"
  "v_mfma_f32_16x16x32_bf16 v[128:131], a[4:7], v[52:55], 0\n"
  "v_mfma_f32_16x16x32_bf16 v[120:123], a[8:11], v[56:59], v[120:123]\n"
  "v_mfma_f32_16x16x32_bf16 v[124:127], a[8:11], v[60:63], v[124:127]\n"
  "v_mfma_f32_16x16x32_bf16 v[128:131], a[12:15], v[60:63], v[128:131]\n"
  "v_mfma_f32_16x16x32_bf16 v[120:123], a[16:19], v[64:67], v[120:123]\n"
  "v_mfma_f32_16x16x32_bf16 v[124:127], a[16:19], v[68:71], v[124:127]\n"
  "v_mfma_f32_16x16x32_bf16 v[128:131], a[20:23], v[68:71], v[128:131]\n"
  "v_mfma_f32_16x16x32_bf16 v[120:123], a[24:27], v[72:75], v[120:123]\n"
  "v_mfma_f32_16x16x32_bf16 v[124:127], a[24:27], v[76:79], v[124:127]\n"
  "v_mfma_f32_16x16x32_bf16 v[128:131], a[28:31], v[76:79], v[128:131]\n"
  "s_cmp_eq_u64 s[16:17], 0\n"
  "s_cbranch_scc1 TGO1\n"
  "s_waitcnt vmcnt(0)\n"
  "v_mov_b32 v7, 0\n"
  "v_mov_b32 v8, s56\n"
  "s_mov_b64 exec, 1\n"
  "global_store_dword v7, v8, s[70:71] sc1\n"
  "s_mov_b64 exec, -1\n"
  "TP1:\n"
  "global_load_dword v9, v1, s[36:37] sc1\n"
  "v_add_u32 v7, s56, v2\n"
  "v_add_u32 v7, 0, v7\n"
  "v_min_i32 v7, 64, v7\n"
  "s_waitcnt vmcnt(0)\n"
  "v_cmp_gt_i32 vcc, v7, v9\n"
  "s_cbranch_vccz TG1\n"
  "s_sub_u32 s57, s57, 1\n"
  "s_cbranch_scc1 TQUIT\n"
  "s_sleep 2\n"
  "s_branch TP1\n"
  "TG1:\n"
  "global_load_dwordx4 v[80:83], v0, s[64:65] offset:0 sc1\n"
  "global_load_dwordx4 v[84:87], v0, s[64:65] offset:1024 sc1\n"
  "global_load_dwordx4 v[104:107], v3, s[66:67] offset:0 sc1\n"
  "global_load_dwordx4 v[108:111], v3, s[66:67] offset:1024 sc1\n"
  "s_add_u32 s64, s64, 65536\n"
  "s_addc_u32 s65, s65, 0\n"
  "s_add_u32 s66, s66, 65536\n"
  "s_addc_u32 s67, s67, 0\n"
  "s_waitcnt vmcnt(3)\n"
  "ds_write_b128 v4, v[80:83] offset:0\n"
  "s_waitcnt vmcnt(2)\n"
  "ds_write_b128 v4, v[84:87] offset:1024\n"
  "s_waitcnt lgkmcnt(5)\n"
  "ds_read_b64_tr_b16 v[88:89], v5 offset:0\n"
  "s_waitcnt lgkmcnt(5)\n"
  "ds_read_b64_tr_b16 v[90:91], v5 offset:512\n"
  "s_waitcnt lgkmcnt(5)\n"
  "ds_read_b64_tr_b16 v[92:93], v5 offset:1024\n"
  "s_waitcnt lgkmcnt(5)\n"
  "ds_read_b64_tr_b16 v[94:95], v5 offset:1536\n"
  "s_waitcnt vmcnt(0)\n"
  "TGO1:\n"
  "s_waitcnt lgkmcnt(3)\n"
  "v_mfma_f32_16x16x16_bf16 v[132:135], v[88:89], v[48:49], 0\n"
  "global_load_dword v9, v1, s[36:37] sc1\n"
  "v_mfma_f32_16x16x16_bf16 v[140:143], v[88:89], v[50:51], 0\n"
  "global_load_dwordx4 v[16:19], v0, s[62:63] offset:-4096 sc1\n"
  "s_waitcnt lgkmcnt(2)\n"
  "v_mfma_f32_16x16x16_bf16 v[132:135], v[90:91], v[56:57], v[132:135]\n"
  "global_load_dwordx4 v[20:23], v0, s[62:63] offset:-3072 sc1\n"
  "v_mfma_f32_16x16x16_bf16 v[140:143], v[90:91], v[58:59], v[140:143]\n"
  "global_load_dwordx4 v[24:27], v0, s[62:63] offset:-2048 sc1\n"
  "s_waitcnt lgkmcnt(1)\n"
  "v_mfma_f32_16x16x16_bf16 v[132:135], v[92:93], v[64:65], v[132:135]\n"
  "global_load_dwordx4 v[28:31], v0, s[62:63] offset:-1024 sc1\n"
  "v_mfma_f32_16x16x16_bf16 v[140:143], v[92:93], v[66:67], v[140:143]\n"
  "global_load_dwordx4 v[32:35], v0, s[62:63] offset:0 sc1\n"
  "s_waitcnt lgkmcnt(0)\n"
  "v_mfma_f32_16x16x16_bf16 v[132:135], v[94:95], v[72:73], v[132:135]\n"
  "global_load_dwordx4 v[36:39], v0, s[62:63] offset:1024 sc1\n"
  "v_mfma_f32_16x16x16_bf16 v[140:143], v[94:95], v[74:75], v[140:143]\n"
  "global_load_dwordx4 v[40:43], v0, s[62:63] offset:2048 sc1\n"
  "v_mfma_f32_16x16x16_bf16 v[136:139], v[88:89], v[52:53], 0\n"
  "global_load_dwordx4 v[44:47], v0, s[62:63] offset:3072 sc1\n"
  "v_mfma_f32_16x16x16_bf16 v[144:147], v[88:89], v[54:55], 0\n"
  "global_load_dwordx4 a[0:3], v0, s[60:61] offset:-4096 sc1\n"
  "v_mfma_f32_16x16x16_bf16 v[136:139], v[90:91], v[60:61], v[136:139]\n"
  "global_load_dwordx4 a[4:7], v0, s[60:61] offset:-3072 sc1\n"
  "v_mfma_f32_16x16x16_bf16 v[144:147], v[90:91], v[62:63], v[144:147]\n"
  "global_load_dwordx4 a[8:11], v0, s[60:61] offset:-2048 sc1\n"
  "v_mfma_f32_16x16x16_bf16 v[136:139], v[92:93], v[68:69], v[136:139]\n"
  "v_cndmask_b32 v120, v12, v120, s[8:9]\n"
  "v_cndmask_b32 v121, v12, v121, s[10:11]\n"
  "v_mfma_f32_16x16x16_bf16 v[144:147], v[92:93], v[70:71], v[144:147]\n"
  "v_cndmask_b32 v122, v12, v122, s[12:13]\n"
  "v_cndmask_b32 v123, v12, v123, s[14:15]\n"
  "v_mfma_f32_16x16x16_bf16 v[136:139], v[94:95], v[76:77], v[136:139]\n"
  "v_cndmask_b32 v128, v12, v128, s[8:9]\n"
  "v_cndmask_b32 v129, v12, v129, s[10:11]\n"
  "v_mfma_f32_16x16x16_bf16 v[144:147], v[94:95], v[78:79], v[144:147]\n"
  "v_cndmask_b32 v130, v12, v130, s[12:13]\n"
  "v_cndmask_b32 v131, v12, v131, s[14:15]\n"
  "v_add_f32 v132, v132, v142\n"
  "v_add_f32 v133, v133, v143\n"
  "v_mul_f32 v132, s50, v132\n"
  "v_mul_f32 v133, s50, v133\n"
  "v_max_f32 v10, v132, v133\n"
  "v_max3_f32 v10, v10, v120, v121\n"
  "v_max3_f32 v10, v10, v122, v123\n"
  "v_mov_b32 v11, v10\n"
  "s_nop 1\n"
  "v_permlane16_swap_b32 v10, v11\n"
  "v_max_f32 v10, v10, v11\n"
  "v_mov_b32 v11, v10\n"
  "s_nop 1\n"
  "v_permlane32_swap_b32 v10, v11\n"
  "v_max_f32 v10, v10, v11\n"
  "v_sub_f32 v120, v120, v10\n"
  "v_sub_f32 v121, v121, v10\n"
  "v_sub_f32 v122, v122, v10\n"
  "v_sub_f32 v123, v123, v10\n"
  "v_sub_f32 v132, v132, v10\n"
  "v_sub_f32 v133, v133, v10\n"
  "v_exp_f32 v120, v120\n"
  "v_exp_f32 v121, v121\n"
  "v_exp_f32 v122, v122\n"
  "v_exp_f32 v123, v123\n"
  "v_exp_f32 v132, v132\n"
  "v_exp_f32 v133, v133\n"
  "v_add_f32 v10, v120, v121\n"
  "v_add_f32 v10, v10, v122\n"
  "v_add_f32 v10, v10, v123\n"
  "v_add_f32 v10, v10, v132\n"
  "v_add_f32 v10, v10, v133\n"
  "v_mov_b32 v11, v10\n"
  "s_nop 1\n"
  "v_permlane16_swap_b32 v10, v11\n"
  "v_add_f32 v10, v10, v11\n"
  "v_mov_b32 v11, v10\n"
  "s_nop 1\n"
  "v_permlane32_swap_b32 v10, v11\n"
  "v_add_f32 v10, v10, v11\n"
  "v_rcp_f32 v10, v10\n"
  "s_nop 0\n"
  "v_mul_f32 v120, v10, v120\n"
  "v_mul_f32 v121, v10, v121\n"
  "v_mul_f32 v122, v10, v122\n"
  "v_mul_f32 v123, v10, v123\n"
  "v_mul_f32 v132, v10, v132\n"
  "v_mul_f32 v133, v10, v133\n"
  "v_cvt_pk_bf16_f32 v148, v120, v121\n"
  "v_cvt_pk_bf16_f32 v149, v122, v123\n"
  "v_cvt_pk_bf16_f32 v156, v132, v133\n"
  "v_cvt_pk_bf16_f32 v161, v132, v133\n"
  "v_cvt_pk_bf16_f32 v166, v132, v133\n"
  "v_cvt_pk_bf16_f32 v171, v132, v133\n"
  "v_add_u32 v7, s56, v2\n"
  "v_add_u32 v7, 1, v7\n"
  "v_min_i32 v7, 64, v7\n"
  "s_waitcnt vmcnt(11)\n"
  "v_cmp_gt_i32 s[16:17], v7, v9\n"
  "v_mov_b32 v7, 0\n"
  "v_mov_b32 v8, s56\n"
  "s_mov_b64 exec, 1\n"
  "global_store_dword v7, v8, s[70:71] sc1\n"
  "s_mov_b64 exec, -1\n"
  "s_cmp_lg_u64 s[16:17], 0\n"
  "s_cbranch_scc1 TNM1\n"
  "global_load_dwordx4 v[80:83], v0, s[64:65] offset:0 sc1\n"
  "global_load_dwordx4 v[84:87], v0, s[64:65] offset:1024 sc1\n"
  "global_load_dwordx4 v[96:99], v3, s[66:67] offset:0 sc1\n"
  "global_load_dwordx4 v[100:103], v3, s[66:67] offset:1024 sc1\n"
  "s_add_u32 s64, s64, 65536\n"
  "s_addc_u32 s65, s65, 0\n"
  "s_add_u32 s66, s66, 65536\n"
  "s_addc_u32 s67, s67, 0\n"
  "TNM1:\n"
  "v_mfma_f32_16x16x32_bf16 v[188:191], a[64:67], v[148:151], 0\n"
  "global_load_dwordx4 a[12:15], v0, s[60:61] offset:-1024 sc1\n"
  "v_mfma_f32_16x16x32_bf16 v[188:191], v[104:107], v[156:159], v[188:191]\n"
  "global_load_dwordx4 a[16:19], v0, s[60:61] offset:0 sc1\n"
  "v_mfma_f32_16x16x32_bf16 v[192:195], a[68:71], v[148:151], 0\n"
  "global_load_dwordx4 a[20:23], v0, s[60:61] offset:1024 sc1\n"
  "v_mfma_f32_16x16x32_bf16 v[192:195], v[104:107], v[160:163], v[192:195]\n"
  "global_load_dwordx4 a[24:27], v0, s[60:61] offset:2048 sc1\n"
  "v_mfma_f32_16x16x32_bf16 v[196:199], a[72:75], v[148:151], 0\n"
  "global_load_dwordx4 a[28:31], v0, s[60:61] offset:3072 sc1\n"
  "v_mfma_f32_16x16x32_bf16 v[196:199], v[104:107], v[164:167], v[196:199]\n"
  "s_add_u32 s60, s60, 16384\n"
  "s_addc_u32 s61, s61, 0\n"
  "global_load_dwordx4 a[32:35], v0, s[60:61] offset:-4096 sc1\n"
  "v_mfma_f32_16x16x32_bf16 v[200:203], a[76:79], v[148:151], 0\n"
  "global_load_dwordx4 a[36:39], v0, s[60:61] offset:-3072 sc1\n"
  "v_mfma_f32_16x16x32_bf16 v[200:203], v[104:107], v[168:171], v[200:203]\n"
  "global_load_dwordx4 a[40:43], v0, s[60:61] offset:-2048 sc1\n"
  "v_mfma_f32_16x16x32_bf16 v[204:207], a[80:83], v[148:151], 0\n"
  "global_load_dwordx4 a[44:47], v0, s[60:61] offset:-1024 sc1\n"
  "v_mfma_f32_16x16x32_bf16 v[204:207], v[108:111], v[156:159], v[204:207]\n"
  "global_load_dwordx4 a[48:51], v0, s[60:61] offset:0 sc1\n"
  "v_mfma_f32_16x16x32_bf16 v[208:211], a[84:87], v[148:151], 0\n"
  "global_load_dwordx4 a[52:55], v0, s[60:61] offset:1024 sc1\n"
  "v_mfma_f32_16x16x32_bf16 v[208:211], v[108:111], v[160:163], v[208:211]\n"
  "global_load_dwordx4 a[56:59], v0, s[60:61] offset:2048 sc1\n"
  "v_mfma_f32_16x16x32_bf16 v[212:215], a[88:91], v[148:151], 0\n"
  "global_load_dwordx4 a[60:63], v0, s[60:61] offset:3072 sc1\n"
  "v_mfma_f32_16x16x32_bf16 v[212:215], v[108:111], v[164:167], v[212:215]\n"
  "v_mfma_f32_16x16x32_bf16 v[216:219], a[92:95], v[148:151], 0\n"
  "v_mfma_f32_16x16x32_bf16 v[216:219], v[108:111], v[168:171], v[216:219]\n"
  "s_add_u32 s76, s56, 2\n"
  "s_min_u32 s76, s76, 63\n"
  "s_bitcmp1_b64 s[4:5], s76\n"
  "s_cselect_b32 s74, 8192, 0\n"
  "s_and_b32 s75, s76, 63\n"
  "s_lshl_b32 s75, s75, 21\n"
  "s_add_u32 s74, s74, s75\n"
  "s_add_u32 s74, s74, 20971520\n"
  "s_xor_b32 s75, s74, s44\n"
  "s_add_u32 s60, s36, s75\n"
  "s_addc_u32 s61, s37, 0\n"
  "s_xor_b32 s75, s74, s45\n"
  "s_add_u32 s62, s36, s75\n"
  "s_addc_u32 s63, s37, 0\n"
  "s_bitcmp1_b64 s[4:5], s56\n"
  "s_cselect_b32 s74, 8192, 0\n"
  "s_and_b32 s75, s56, 63\n"
  "s_lshl_b32 s75, s75, 21\n"
  "s_add_u32 s74, s74, s75\n"
  "s_add_u32 s74, s74, 20971520\n"
  "s_xor_b32 s75, s74, s48\n"
  "s_add_u32 s68, s36, s75\n"
  "s_addc_u32 s69, s37, 0\n"
  "v_add_f32 v136, v136, v146\n"
  "v_add_f32 v137, v137, v147\n"
  "v_mul_f32 v136, s50, v136\n"
  "v_mul_f32 v137, s50, v137\n"
  "v_max_f32 v10, v136, v137\n"
  "v_max3_f32 v10, v10, v124, v125\n"
  "v_max3_f32 v10, v10, v126, v127\n"
  "v_max3_f32 v10, v10, v128, v129\n"
  "v_max3_f32 v10, v10, v130, v131\n"
  "v_mov_b32 v11, v10\n"
  "s_nop 1\n"
  "v_permlane16_swap_b32 v10, v11\n"
  "v_max_f32 v10, v10, v11\n"
  "v_mov_b32 v11, v10\n"
  "s_nop 1\n"
  "v_permlane32_swap_b32 v10, v11\n"
  "v_max_f32 v10, v10, v11\n"
  "v_sub_f32 v124, v124, v10\n"
  "v_sub_f32 v125, v125, v10\n"
  "v_sub_f32 v126, v126, v10\n"
  "v_sub_f32 v127, v127, v10\n"
  "v_sub_f32 v128, v128, v10\n"
  "v_sub_f32 v129, v129, v10\n"
  "v_sub_f32 v130, v130, v10\n"
  "v_sub_f32 v131, v131, v10\n"
  "v_sub_f32 v136, v136, v10\n"
  "v_sub_f32 v137, v137, v10\n"
  "v_exp_f32 v124, v124\n"
  "v_exp_f32 v125, v125\n"
  "v_exp_f32 v126, v126\n"
  "v_exp_f32 v127, v127\n"
  "v_exp_f32 v128, v128\n"
  "v_exp_f32 v129, v129\n"
  "v_exp_f32 v130, v130\n"
  "v_exp_f32 v131, v131\n"
  "v_exp_f32 v136, v136\n"
  "v_exp_f32 v137, v137\n"
  "v_add_f32 v10, v124, v125\n"
  "v_add_f32 v10, v10, v126\n"
  "v_add_f32 v10, v10, v127\n"
  "v_add_f32 v10, v10, v128\n"
  "v_add_f32 v10, v10, v129\n"
  "v_add_f32 v10, v10, v130\n"
  "v_add_f32 v10, v10, v131\n"
  "v_add_f32 v10, v10, v136\n"
  "v_add_f32 v10, v10, v137\n"
  "v_mov_b32 v11, v10\n"
  "s_nop 1\n"
  "v_permlane16_swap_b32 v10, v11\n"
  "v_add_f32 v10, v10, v11\n"
  "v_mov_b32 v11, v10\n"
  "s_nop 1\n"
  "v_permlane32_swap_b32 v10, v11\n"
  "v_add_f32 v10, v10, v11\n"
  "v_rcp_f32 v10, v10\n"
  "s_nop 0\n"
  "v_mul_f32 v124, v10, v124\n"
  "v_mul_f32 v125, v10, v125\n"
  "v_mul_f32 v126, v10, v126\n"
  "v_mul_f32 v127, v10, v127\n"
  "v_mul_f32 v128, v10, v128\n"
  "v_mul_f32 v129, v10, v129\n"
  "v_mul_f32 v130, v10, v130\n"
  "v_mul_f32 v131, v10, v131\n"
  "v_mul_f32 v136, v10, v136\n"
  "v_mul_f32 v137, v10, v137\n"
  "v_cvt_pk_bf16_f32 v152, v124, v125\n"
  "v_cvt_pk_bf16_f32 v153, v126, v127\n"
  "v_cvt_pk_bf16_f32 v154, v128, v129\n"
  "v_cvt_pk_bf16_f32 v155, v130, v131\n"
  "v_cvt_pk_bf16_f32 v172, v136, v137\n"
  "v_cvt_pk_bf16_f32 v177, v136, v137\n"
  "v_cvt_pk_bf16_f32 v182, v136, v137\n"
  "v_cvt_pk_bf16_f32 v187, v136, v137\n"
  "v_cvt_pk_bf16_f32 v220, v188, v189\n"
  "v_cvt_pk_bf16_f32 v221, v190, v191\n"
  "v_cvt_pk_bf16_f32 v222, v192, v193\n"
  "v_cvt_pk_bf16_f32 v223, v194, v195\n"
  "global_store_dwordx4 v0, v[220:223], s[68:69] offset:-4096 sc1\n"
  "v_mfma_f32_16x16x32_bf16 v[188:191], a[64:67], v[152:155], 0\n"
  "v_cvt_pk_bf16_f32 v224, v196, v197\n"
  "v_cvt_pk_bf16_f32 v225, v198, v199\n"
  "v_mfma_f32_16x16x32_bf16 v[188:191], v[104:107], v[172:175], v[188:191]\n"
  "v_cvt_pk_bf16_f32 v226, v200, v201\n"
  "v_cvt_pk_bf16_f32 v227, v202, v203\n"
  "v_mfma_f32_16x16x32_bf16 v[192:195], a[68:71], v[152:155], 0\n"
  "global_store_dwordx4 v0, v[224:227], s[68:69] offset:-3072 sc1\n"
  "v_mfma_f32_16x16x32_bf16 v[192:195], v[104:107], v[176:179], v[192:195]\n"
  "v_mfma_f32_16x16x32_bf16 v[196:199], a[72:75], v[152:155], 0\n"
  "v_cvt_pk_bf16_f32 v228, v204, v205\n"
  "v_cvt_pk_bf16_f32 v229, v206, v207\n"
  "v_mfma_f32_16x16x32_bf16 v[196:199], v[104:107], v[180:183], v[196:199]\n"
  "v_cvt_pk_bf16_f32 v230, v208, v209\n"
  "v_cvt_pk_bf16_f32 v231, v210, v211\n"
  "v_mfma_f32_16x16x32_bf16 v[200:203], a[76:79], v[152:155], 0\n"
  "global_store_dwordx4 v0, v[228:231], s[68:69] offset:-2048 sc1\n"
  "v_mfma_f32_16x16x32_bf16 v[200:203], v[104:107], v[184:187], v[200:203]\n"
  "v_mfma_f32_16x16x32_bf16 v[204:207], a[80:83], v[152:155], 0\n"
  "v_cvt_pk_bf16_f32 v232, v212, v213\n"
  "v_cvt_pk_bf16_f32 v233, v214, v215\n"
  "v_mfma_f32_16x16x32_bf16 v[204:207], v[108:111], v[172:175], v[204:207]\n"
  "v_cvt_pk_bf16_f32 v234, v216, v217\n"
  "v_cvt_pk_bf16_f32 v235, v218, v219\n"
  "v_mfma_f32_16x16x32_bf16 v[208:211], a[84:87], v[152:155], 0\n"
  "global_store_dwordx4 v0, v[232:235], s[68:69] offset:-1024 sc1\n"
  "v_mfma_f32_16x16x32_bf16 v[208:211], v[108:111], v[176:179], v[208:211]\n"
  "v_mfma_f32_16x16x32_bf16 v[212:215], a[88:91], v[152:155], 0\n"
  "v_cvt_pk_bf16_f32 v220, v188, v189\n"
  "v_cvt_pk_bf16_f32 v221, v190, v191\n"
  "v_mfma_f32_16x16x32_bf16 v[212:215], v[108:111], v[180:183], v[212:215]\n"
  "v_cvt_pk_bf16_f32 v222, v192, v193\n"
  "v_cvt_pk_bf16_f32 v223, v194, v195\n"
  "v_mfma_f32_16x16x32_bf16 v[216:219], a[92:95], v[152:155], 0\n"
  "global_store_dwordx4 v0, v[220:223], s[68:69] offset:0 sc1\n"
  "v_mfma_f32_16x16x32_bf16 v[216:219], v[108:111], v[184:187], v[216:219]\n"
  "v_cvt_pk_bf16_f32 v224, v196, v197\n"
  "v_cvt_pk_bf16_f32 v225, v198, v199\n"
  "v_cvt_pk_bf16_f32 v226, v200, v201\n"
  "v_cvt_pk_bf16_f32 v227, v202, v203\n"
  "global_store_dwordx4 v0, v[224:227], s[68:69] offset:1024 sc1\n"
  "v_cvt_pk_bf16_f32 v228, v204, v205\n"
  "v_cvt_pk_bf16_f32 v229, v206, v207\n"
  "v_cvt_pk_bf16_f32 v230, v208, v209\n"
  "v_cvt_pk_bf16_f32 v231, v210, v211\n"
  "global_store_dwordx4 v0, v[228:231], s[68:69] offset:2048 sc1\n"
  "v_cvt_pk_bf16_f32 v232, v212, v213\n"
  "v_cvt_pk_bf16_f32 v233, v214, v215\n"
  "v_cvt_pk_bf16_f32 v234, v216, v217\n"
  "v_cvt_pk_bf16_f32 v235, v218, v219\n"
  "global_store_dwordx4 v0, v[232:235], s[68:69] offset:3072 sc1\n"
  "s_add_u32 s56, s56, 1\n"
  "s_cmp_lt_u32 s56, s53\n"
  "s_cbranch_scc1 TOP0\n"
  "TEND:\n"
  "s_waitcnt vmcnt(0)\n"
  "v_mov_b32 v7, 0\n"
  "v_mov_b32 v8, s56\n"
  "s_mov_b64 exec, 1\n"
  "global_store_dword v7, v8, s[70:71] sc1\n"
  "s_mov_b64 exec, -1\n"
  "s_mov_b32 s56, s52\n"
  "s_add_u32 s53, s52, 1\n"
  "s_lshl_b32 s74, s52, 16\n"
  "s_add_u32 s46, s46, s74\n"
  "s_add_u32 s47, s47, s74\n"
  "s_xor_b32 s48, s48, 8192\n"
  "s_mov_b32 s52, 0\n"
  "s_cmp_lg_u32 s56, 0\n"
  "s_cbranch_scc1 TINIT\n"
  "TQUIT:\n"
  "s_endpgm\n"
      : "+{v[0:15]}"(vin), "+{s[36:43]}"(sa), "+{s[44:59]}"(sb) : "{s[4:5]}"(ask) : CLOB);
  } else { ARGS; PTR(sa, 0, p.ws);
    const int id = tk < 16 ? 32 + fd * 16 + tk : fd * 8 + tk - 16, cg = id % 12, sh = (cg & 1) * 32;
    v16u wa;
#pragma unroll
    for (int i = 0; i < 16; ++i) {
      const int gr = 4 * (i >> 1) + g, k = 8 * (gr % 5) + 4 * (i & 1) + qq;
      wa[i] = wave * 40960 + (36 * (gr / 5) + 18 * (k / 20) + min(k % 20, 17)) * 128 + sh + c4 * 8;
    }
    const int r4 = min(lane / 12, 3), cq = lane % 12;
    vin[1] = (lane & 8) ? (C_AP) - CTL + (4 * wave + c4) * 128 : C_QAP - CTL + (8 * wave + (lane & 7)) * 128;
    vin[2] = wave * 512 + (4 * wave + r4) * 4;
    vin[3] = 73728 + (cq >> 2) * 1024 + (16 * (cq & 3) + 4 * wave + r4) * 16;
    vin[4] = ((4 * wave + r4) * DM + 48 * cg + 4 * cq) * 2;
    vin[5] = wave * 40960 + (4 * g + qq) * 128 + sh + c4 * 8; vin[6] = (lane >> 3) * 1152 + (lane & 7) * 16; vin[7] = (lane & 8) ? 0 : C_QAL - C_QAP;
    PTR(sa, 2, p.Wo + (size_t)(wave * 8 + (cg & 7)) * 128 * DM + 48 * cg - sh / 2); PTR(sa, 4, p.out); PTR(sa, 6, p.wo_a + wave * 144 * DM + 48 * cg - sh / 2);
    sb[0] = (wave * 8 + (cg & 7)) * 65536 + 49152; sb[1] = WO_OA + wave * 5120 + 2048; sb[2] = WO_G; sb[3] = wave;
    sb[4] = WO_ST + 0x400000 + (role * 4 + wave) * 4096; sb[5] = C_OP + cg * 128; sb[6] = 7 - (cg & 7); sb[7] = wave + (tk < 16);
    UNIFORM(sa, 8); UNIFORM(sb, 16);
    asm volatile(
  "s_nop 4\n"
  "s_mul_i32 s80, s47, 40960\n"
  "v_mov_b32 v127, v7\n"
  "s_add_u32 m0, s80, 0\n"
  "s_nop 0\n"
  "global_load_lds_dwordx4 v6, s[42:43]\n"
  "s_add_u32 s42, s42, 9216\n"
  "s_addc_u32 s43, s43, 0\n"
  "s_add_u32 m0, s80, 1024\n"
  "s_nop 0\n"
  "global_load_lds_dwordx4 v6, s[42:43]\n"
  "s_add_u32 s42, s42, 9216\n"
  "s_addc_u32 s43, s43, 0\n"
  "s_add_u32 m0, s80, 2048\n"
  "s_nop 0\n"
  "global_load_lds_dwordx4 v6, s[42:43]\n"
  "s_add_u32 s42, s42, 9216\n"
  "s_addc_u32 s43, s43, 0\n"
  "s_add_u32 m0, s80, 3072\n"
  "s_nop 0\n"
  "global_load_lds_dwordx4 v6, s[42:43]\n"
  "s_add_u32 s42, s42, 9216\n"
  "s_addc_u32 s43, s43, 0\n"
  "s_add_u32 m0, s80, 4096\n"
  "s_nop 0\n"
  "global_load_lds_dwordx4 v6, s[42:43]\n"
  "s_add_u32 s42, s42, 9216\n"
  "s_addc_u32 s43, s43, 0\n"
  "s_add_u32 m0, s80, 5120\n"
  "s_nop 0\n"
  "global_load_lds_dwordx4 v6, s[42:43]\n"
  "s_add_u32 s42, s42, 9216\n"
  "s_addc_u32 s43, s43, 0\n"
  "s_add_u32 m0, s80, 6144\n"
  "s_nop 0\n"
  "global_load_lds_dwordx4 v6, s[42:43]\n"
  "s_add_u32 s42, s42, 9216\n"
  "s_addc_u32 s43, s43, 0\n"
  "s_add_u32 m0, s80, 7168\n"
  "s_nop 0\n"
  "global_load_lds_dwordx4 v6, s[42:43]\n"
  "s_add_u32 s42, s42, 9216\n"
  "s_addc_u32 s43, s43, 0\n"
  "s_add_u32 m0, s80, 8192\n"
  "s_nop 0\n"
  "global_load_lds_dwordx4 v6, s[42:43]\n"
  "s_add_u32 s42, s42, 9216\n"
  "s_addc_u32 s43, s43, 0\n"
  "s_add_u32 m0, s80, 9216\n"
  "s_nop 0\n"
  "global_load_lds_dwordx4 v6, s[42:43]\n"
  "s_add_u32 s42, s42, 9216\n"
  "s_addc_u32 s43, s43, 0\n"
  "s_add_u32 m0, s80, 10240\n"
  "s_nop 0\n"
  "global_load_lds_dwordx4 v6, s[42:43]\n"
  "s_add_u32 s42, s42, 9216\n"
  "s_addc_u32 s43, s43, 0\n"
  "s_add_u32 m0, s80, 11264\n"
  "s_nop 0\n"
  "global_load_lds_dwordx4 v6, s[42:43]\n"
  "s_add_u32 s42, s42, 9216\n"
  "s_addc_u32 s43, s43, 0\n"
  "s_add_u32 m0, s80, 12288\n"
  "s_nop 0\n"
  "global_load_lds_dwordx4 v6, s[42:43]\n"
  "s_add_u32 s42, s42, 9216\n"
  "s_addc_u32 s43, s43, 0\n"
  "s_add_u32 m0, s80, 13312\n"
  "s_nop 0\n"
  "global_load_lds_dwordx4 v6, s[42:43]\n"
  "s_add_u32 s42, s42, 9216\n"
  "s_addc_u32 s43, s43, 0\n"
  "s_add_u32 m0, s80, 14336\n"
  "s_nop 0\n"
  "global_load_lds_dwordx4 v6, s[42:43]\n"
  "s_add_u32 s42, s42, 9216\n"
  "s_addc_u32 s43, s43, 0\n"
  "s_add_u32 m0, s80, 15360\n"
  "s_nop 0\n"
  "global_load_lds_dwordx4 v6, s[42:43]\n"
  "s_add_u32 s42, s42, 9216\n"
  "s_addc_u32 s43, s43, 0\n"
  "s_add_u32 m0, s80, 16384\n"
  "s_nop 0\n"
  "global_load_lds_dwordx4 v6, s[42:43]\n"
  "s_add_u32 s42, s42, 9216\n"
  "s_addc_u32 s43, s43, 0\n"
  "s_add_u32 m0, s80, 17408\n"
  "s_nop 0\n"
  "global_load_lds_dwordx4 v6, s[42:43]\n"
  "s_add_u32 s42, s42, 9216\n"
  "s_addc_u32 s43, s43, 0\n"
  "s_add_u32 m0, s80, 20480\n"
  "s_nop 0\n"
  "global_load_lds_dwordx4 v6, s[38:39]\n"
  "s_add_u32 s38, s38, 9216\n"
  "s_addc_u32 s39, s39, 0\n"
  "s_add_u32 m0, s80, 21504\n"
  "s_nop 0\n"
  "global_load_lds_dwordx4 v6, s[38:39]\n"
  "s_add_u32 s38, s38, 9216\n"
  "s_addc_u32 s39, s39, 0\n"
  "s_add_u32 m0, s80, 22528\n"
  "s_nop 0\n"
  "global_load_lds_dwordx4 v6, s[38:39]\n"
  "s_add_u32 s38, s38, 9216\n"
  "s_addc_u32 s39, s39, 0\n"
  "s_add_u32 m0, s80, 23552\n"
  "s_nop 0\n"
  "global_load_lds_dwordx4 v6, s[38:39]\n"
  "s_add_u32 s38, s38, 9216\n"
  "s_addc_u32 s39, s39, 0\n"
  "s_add_u32 m0, s80, 24576\n"
  "s_nop 0\n"
  "global_load_lds_dwordx4 v6, s[38:39]\n"
  "s_add_u32 s38, s38, 9216\n"
  "s_addc_u32 s39, s39, 0\n"
  "s_add_u32 m0, s80, 25600\n"
  "s_nop 0\n"
  "global_load_lds_dwordx4 v6, s[38:39]\n"
  "s_add_u32 s38, s38, 9216\n"
  "s_addc_u32 s39, s39, 0\n"
  "s_add_u32 m0, s80, 26624\n"
  "s_nop 0\n"
  "global_load_lds_dwordx4 v6, s[38:39]\n"
  "s_add_u32 s38, s38, 9216\n"
  "s_addc_u32 s39, s39, 0\n"
  "s_add_u32 m0, s80, 27648\n"
  "s_nop 0\n"
  "global_load_lds_dwordx4 v6, s[38:39]\n"
  "s_add_u32 s38, s38, 9216\n"
  "s_addc_u32 s39, s39, 0\n"
  "s_add_u32 m0, s80, 28672\n"
  "s_nop 0\n"
  "global_load_lds_dwordx4 v6, s[38:39]\n"
  "s_add_u32 s38, s38, 9216\n"
  "s_addc_u32 s39, s39, 0\n"
  "s_add_u32 m0, s80, 29696\n"
  "s_nop 0\n"
  "global_load_lds_dwordx4 v6, s[38:39]\n"
  "s_add_u32 s38, s38, 9216\n"
  "s_addc_u32 s39, s39, 0\n"
  "s_add_u32 m0, s80, 30720\n"
  "s_nop 0\n"
  "global_load_lds_dwordx4 v6, s[38:39]\n"
  "s_add_u32 s38, s38, 9216\n"
  "s_addc_u32 s39, s39, 0\n"
  "s_add_u32 m0, s80, 31744\n"
  "s_nop 0\n"
  "global_load_lds_dwordx4 v6, s[38:39]\n"
  "s_add_u32 s38, s38, 9216\n"
  "s_addc_u32 s39, s39, 0\n"
  "s_add_u32 m0, s80, 32768\n"
  "s_nop 0\n"
  "global_load_lds_dwordx4 v6, s[38:39]\n"
  "s_add_u32 s38, s38, 9216\n"
  "s_addc_u32 s39, s39, 0\n"
  "s_add_u32 m0, s80, 33792\n"
  "s_nop 0\n"
  "global_load_lds_dwordx4 v6, s[38:39]\n"
  "s_add_u32 s38, s38, 9216\n"
  "s_addc_u32 s39, s39, 0\n"
  "s_add_u32 m0, s80, 34816\n"
  "s_nop 0\n"
  "global_load_lds_dwordx4 v6, s[38:39]\n"
  "s_add_u32 s38, s38, 9216\n"
  "s_addc_u32 s39, s39, 0\n"
  "s_add_u32 m0, s80, 35840\n"
  "s_nop 0\n"
  "global_load_lds_dwordx4 v6, s[38:39]\n"
  "s_add_u32 s38, s38, 9216\n"
  "s_addc_u32 s39, s39, 0\n"
  "s_cmp_eq_u32 s50, 0\n"
  "s_cselect_b32 s74, 1179648, 0\n"
  "s_sub_u32 s38, s38, s74\n"
  "s_subb_u32 s39, s39, 0\n"
  "s_add_u32 m0, s80, 36864\n"
  "s_nop 0\n"
  "global_load_lds_dwordx4 v6, s[38:39]\n"
  "s_add_u32 s38, s38, 9216\n"
  "s_addc_u32 s39, s39, 0\n"
  "s_add_u32 m0, s80, 37888\n"
  "s_nop 0\n"
  "global_load_lds_dwordx4 v6, s[38:39]\n"
  "s_add_u32 s38, s38, 9216\n"
  "s_addc_u32 s39, s39, 0\n"
  "s_add_u32 m0, s80, 38912\n"
  "s_nop 0\n"
  "global_load_lds_dwordx4 v6, s[38:39]\n"
  "s_add_u32 s38, s38, 9216\n"
  "s_addc_u32 s39, s39, 0\n"
  "s_add_u32 m0, s80, 39936\n"
  "s_nop 0\n"
  "global_load_lds_dwordx4 v6, s[38:39]\n"
  "s_add_u32 s38, s38, 9216\n"
  "s_addc_u32 s39, s39, 0\n"
  "s_waitcnt vmcnt(20)\n"
  "ds_read_b64_tr_b16 v[52:53], v16 offset:0\n"
  "ds_read_b64_tr_b16 v[54:55], v17 offset:0\n"
  "ds_read_b64_tr_b16 v[56:57], v16 offset:32\n"
  "ds_read_b64_tr_b16 v[58:59], v17 offset:32\n"
  "ds_read_b64_tr_b16 v[60:61], v16 offset:64\n"
  "ds_read_b64_tr_b16 v[62:63], v17 offset:64\n"
  "ds_read_b64_tr_b16 v[64:65], v18 offset:0\n"
  "ds_read_b64_tr_b16 v[66:67], v19 offset:0\n"
  "ds_read_b64_tr_b16 v[68:69], v18 offset:32\n"
  "ds_read_b64_tr_b16 v[70:71], v19 offset:32\n"
  "ds_read_b64_tr_b16 v[72:73], v18 offset:64\n"
  "ds_read_b64_tr_b16 v[74:75], v19 offset:64\n"
  "ds_read_b64_tr_b16 v[76:77], v20 offset:0\n"
  "ds_read_b64_tr_b16 v[78:79], v21 offset:0\n"
  "ds_read_b64_tr_b16 v[80:81], v20 offset:32\n"
  "ds_read_b64_tr_b16 v[82:83], v21 offset:32\n"
  "ds_read_b64_tr_b16 v[84:85], v20 offset:64\n"
  "ds_read_b64_tr_b16 v[86:87], v21 offset:64\n"
  "ds_read_b64_tr_b16 v[88:89], v22 offset:0\n"
  "ds_read_b64_tr_b16 v[90:91], v23 offset:0\n"
  "ds_read_b64_tr_b16 v[92:93], v22 offset:32\n"
  "ds_read_b64_tr_b16 v[94:95], v23 offset:32\n"
  "ds_read_b64_tr_b16 v[96:97], v22 offset:64\n"
  "ds_read_b64_tr_b16 v[98:99], v23 offset:64\n"
  "ds_read_b64_tr_b16 v[100:101], v24 offset:0\n"
  "ds_read_b64_tr_b16 v[102:103], v25 offset:0\n"
  "ds_read_b64_tr_b16 v[104:105], v24 offset:32\n"
  "ds_read_b64_tr_b16 v[106:107], v25 offset:32\n"
  "ds_read_b64_tr_b16 v[108:109], v24 offset:64\n"
  "ds_read_b64_tr_b16 v[110:111], v25 offset:64\n"
  "s_waitcnt lgkmcnt(0)\n"
  "s_add_u32 m0, s80, 0\n"
  "s_nop 0\n"
  "global_load_lds_dwordx4 v6, s[38:39]\n"
  "s_add_u32 s38, s38, 9216\n"
  "s_addc_u32 s39, s39, 0\n"
  "s_add_u32 m0, s80, 1024\n"
  "s_nop 0\n"
  "global_load_lds_dwordx4 v6, s[38:39]\n"
  "s_add_u32 s38, s38, 9216\n"
  "s_addc_u32 s39, s39, 0\n"
  "s_add_u32 m0, s80, 2048\n"
  "s_nop 0\n"
  "global_load_lds_dwordx4 v6, s[38:39]\n"
  "s_add_u32 s38, s38, 9216\n"
  "s_addc_u32 s39, s39, 0\n"
  "s_add_u32 m0, s80, 3072\n"
  "s_nop 0\n"
  "global_load_lds_dwordx4 v6, s[38:39]\n"
  "s_add_u32 s38, s38, 9216\n"
  "s_addc_u32 s39, s39, 0\n"
  "s_add_u32 m0, s80, 4096\n"
  "s_nop 0\n"
  "global_load_lds_dwordx4 v6, s[38:39]\n"
  "s_add_u32 s38, s38, 9216\n"
  "s_addc_u32 s39, s39, 0\n"
  "s_add_u32 m0, s80, 5120\n"
  "s_nop 0\n"
  "global_load_lds_dwordx4 v6, s[38:39]\n"
  "s_add_u32 s38, s38, 9216\n"
  "s_addc_u32 s39, s39, 0\n"
  "s_add_u32 m0, s80, 6144\n"
  "s_nop 0\n"
  "global_load_lds_dwordx4 v6, s[38:39]\n"
  "s_add_u32 s38, s38, 9216\n"
  "s_addc_u32 s39, s39, 0\n"
  "s_add_u32 m0, s80, 7168\n"
  "s_nop 0\n"
  "global_load_lds_dwordx4 v6, s[38:39]\n"
  "s_add_u32 s38, s38, 9216\n"
  "s_addc_u32 s39, s39, 0\n"
  "s_add_u32 m0, s80, 8192\n"
  "s_nop 0\n"
  "global_load_lds_dwordx4 v6, s[38:39]\n"
  "s_add_u32 s38, s38, 9216\n"
  "s_addc_u32 s39, s39, 0\n"
  "s_add_u32 m0, s80, 9216\n"
  "s_nop 0\n"
  "global_load_lds_dwordx4 v6, s[38:39]\n"
  "s_add_u32 s38, s38, 9216\n"
  "s_addc_u32 s39, s39, 0\n"
  "s_add_u32 m0, s80, 10240\n"
  "s_nop 0\n"
  "global_load_lds_dwordx4 v6, s[38:39]\n"
  "s_add_u32 s38, s38, 9216\n"
  "s_addc_u32 s39, s39, 0\n"
  "s_add_u32 m0, s80, 11264\n"
  "s_nop 0\n"
  "global_load_lds_dwordx4 v6, s[38:39]\n"
  "s_add_u32 s38, s38, 9216\n"
  "s_addc_u32 s39, s39, 0\n"
  "s_cmp_eq_u32 s50, 1\n"
  "s_cselect_b32 s74, 1179648, 0\n"
  "s_sub_u32 s38, s38, s74\n"
  "s_subb_u32 s39, s39, 0\n"
  "s_add_u32 m0, s80, 12288\n"
  "s_nop 0\n"
  "global_load_lds_dwordx4 v6, s[38:39]\n"
  "s_add_u32 s38, s38, 9216\n"
  "s_addc_u32 s39, s39, 0\n"
  "s_add_u32 m0, s80, 13312\n"
  "s_nop 0\n"
  "global_load_lds_dwordx4 v6, s[38:39]\n"
  "s_add_u32 s38, s38, 9216\n"
  "s_addc_u32 s39, s39, 0\n"
  "s_add_u32 m0, s80, 14336\n"
  "s_nop 0\n"
  "global_load_lds_dwordx4 v6, s[38:39]\n"
  "s_add_u32 s38, s38, 9216\n"
  "s_addc_u32 s39, s39, 0\n"
  "s_add_u32 m0, s80, 15360\n"
  "s_nop 0\n"
  "global_load_lds_dwordx4 v6, s[38:39]\n"
  "s_add_u32 s38, s38, 9216\n"
  "s_addc_u32 s39, s39, 0\n"
  "s_waitcnt vmcnt(32) lgkmcnt(0)\n"
  "s_add_u32 m0, s80, 16384\n"
  "s_nop 0\n"
  "global_load_lds_dwordx4 v6, s[38:39]\n"
  "s_add_u32 s38, s38, 9216\n"
  "s_addc_u32 s39, s39, 0\n"
  "s_add_u32 m0, s80, 17408\n"
  "s_nop 0\n"
  "global_load_lds_dwordx4 v6, s[38:39]\n"
  "s_add_u32 s38, s38, 9216\n"
  "s_addc_u32 s39, s39, 0\n"
  "s_add_u32 m0, s80, 18432\n"
  "s_nop 0\n"
  "global_load_lds_dwordx4 v6, s[38:39]\n"
  "s_add_u32 s38, s38, 9216\n"
  "s_addc_u32 s39, s39, 0\n"
  "s_add_u32 m0, s80, 19456\n"
  "s_nop 0\n"
  "global_load_lds_dwordx4 v6, s[38:39]\n"
  "s_add_u32 s38, s38, 9216\n"
  "s_addc_u32 s39, s39, 0\n"
  "ds_read_b64_tr_b16 a[0:1], v5 offset:20480\n"
  "ds_read_b64_tr_b16 a[2:3], v5 offset:22528\n"
  "ds_read_b64_tr_b16 a[4:5], v5 offset:20512\n"
  "ds_read_b64_tr_b16 a[6:7], v5 offset:22560\n"
  "ds_read_b64_tr_b16 a[8:9], v5 offset:20544\n"
  "ds_read_b64_tr_b16 a[10:11], v5 offset:22592\n"
  "s_waitcnt vmcnt(32) lgkmcnt(0)\n"
  "s_add_u32 m0, s80, 20480\n"
  "s_nop 0\n"
  "global_load_lds_dwordx4 v6, s[38:39]\n"
  "s_add_u32 s38, s38, 9216\n"
  "s_addc_u32 s39, s39, 0\n"
  "s_add_u32 m0, s80, 21504\n"
  "s_nop 0\n"
  "global_load_lds_dwordx4 v6, s[38:39]\n"
  "s_add_u32 s38, s38, 9216\n"
  "s_addc_u32 s39, s39, 0\n"
  "s_add_u32 m0, s80, 22528\n"
  "s_nop 0\n"
  "global_load_lds_dwordx4 v6, s[38:39]\n"
  "s_add_u32 s38, s38, 9216\n"
  "s_addc_u32 s39, s39, 0\n"
  "s_add_u32 m0, s80, 23552\n"
  "s_nop 0\n"
  "global_load_lds_dwordx4 v6, s[38:39]\n"
  "s_add_u32 s38, s38, 9216\n"
  "s_addc_u32 s39, s39, 0\n"
  "ds_read_b64_tr_b16 a[12:13], v5 offset:24576\n"
  "ds_read_b64_tr_b16 a[14:15], v5 offset:26624\n"
  "ds_read_b64_tr_b16 a[16:17], v5 offset:24608\n"
  "ds_read_b64_tr_b16 a[18:19], v5 offset:26656\n"
  "ds_read_b64_tr_b16 a[20:21], v5 offset:24640\n"
  "ds_read_b64_tr_b16 a[22:23], v5 offset:26688\n"
  "s_waitcnt vmcnt(32) lgkmcnt(0)\n"
  "s_add_u32 m0, s80, 24576\n"
  "s_nop 0\n"
  "global_load_lds_dwordx4 v6, s[38:39]\n"
  "s_add_u32 s38, s38, 9216\n"
  "s_addc_u32 s39, s39, 0\n"
  "s_add_u32 m0, s80, 25600\n"
  "s_nop 0\n"
  "global_load_lds_dwordx4 v6, s[38:39]\n"
  "s_add_u32 s38, s38, 9216\n"
  "s_addc_u32 s39, s39, 0\n"
  "s_add_u32 m0, s80, 26624\n"
  "s_nop 0\n"
  "global_load_lds_dwordx4 v6, s[38:39]\n"
  "s_add_u32 s38, s38, 9216\n"
  "s_addc_u32 s39, s39, 0\n"
  "s_add_u32 m0, s80, 27648\n"
  "s_nop 0\n"
  "global_load_lds_dwordx4 v6, s[38:39]\n"
  "s_add_u32 s38, s38, 9216\n"
  "s_addc_u32 s39, s39, 0\n"
  "s_cmp_eq_u32 s50, 2\n"
  "s_cselect_b32 s74, 1179648, 0\n"
  "s_sub_u32 s38, s38, s74\n"
  "s_subb_u32 s39, s39, 0\n"
  "ds_read_b64_tr_b16 a[24:25], v5 offset:28672\n"
  "ds_read_b64_tr_b16 a[26:27], v5 offset:30720\n"
  "ds_read_b64_tr_b16 a[28:29], v5 offset:28704\n"
  "ds_read_b64_tr_b16 a[30:31], v5 offset:30752\n"
  "ds_read_b64_tr_b16 a[32:33], v5 offset:28736\n"
  "ds_read_b64_tr_b16 a[34:35], v5 offset:30784\n"
  "s_waitcnt vmcnt(32) lgkmcnt(0)\n"
  "s_add_u32 m0, s80, 28672\n"
  "s_nop 0\n"
  "global_load_lds_dwordx4 v6, s[38:39]\n"
  "s_add_u32 s38, s38, 9216\n"
  "s_addc_u32 s39, s39, 0\n"
  "s_add_u32 m0, s80, 29696\n"
  "s_nop 0\n"
  "global_load_lds_dwordx4 v6, s[38:39]\n"
  "s_add_u32 s38, s38, 9216\n"
  "s_addc_u32 s39, s39, 0\n"
  "s_add_u32 m0, s80, 30720\n"
  "s_nop 0\n"
  "global_load_lds_dwordx4 v6, s[38:39]\n"
  "s_add_u32 s38, s38, 9216\n"
  "s_addc_u32 s39, s39, 0\n"
  "s_add_u32 m0, s80, 31744\n"
  "s_nop 0\n"
  "global_load_lds_dwordx4 v6, s[38:39]\n"
  "s_add_u32 s38, s38, 9216\n"
  "s_addc_u32 s39, s39, 0\n"
  "ds_read_b64_tr_b16 a[36:37], v5 offset:32768\n"
  "ds_read_b64_tr_b16 a[38:39], v5 offset:34816\n"
  "ds_read_b64_tr_b16 a[40:41], v5 offset:32800\n"
  "ds_read_b64_tr_b16 a[42:43], v5 offset:34848\n"
  "ds_read_b64_tr_b16 a[44:45], v5 offset:32832\n"
  "ds_read_b64_tr_b16 a[46:47], v5 offset:34880\n"
  "s_waitcnt vmcnt(32) lgkmcnt(0)\n"
  "s_add_u32 m0, s80, 32768\n"
  "s_nop 0\n"
  "global_load_lds_dwordx4 v6, s[38:39]\n"
  "s_add_u32 s38, s38, 9216\n"
  "s_addc_u32 s39, s39, 0\n"
  "s_add_u32 m0, s80, 33792\n"
  "s_nop 0\n"
  "global_load_lds_dwordx4 v6, s[38:39]\n"
  "s_add_u32 s38, s38, 9216\n"
  "s_addc_u32 s39, s39, 0\n"
  "s_add_u32 m0, s80, 34816\n"
  "s_nop 0\n"
  "global_load_lds_dwordx4 v6, s[38:39]\n"
  "s_add_u32 s38, s38, 9216\n"
  "s_addc_u32 s39, s39, 0\n"
  "s_add_u32 m0, s80, 35840\n"
  "s_nop 0\n"
  "global_load_lds_dwordx4 v6, s[38:39]\n"
  "s_add_u32 s38, s38, 9216\n"
  "s_addc_u32 s39, s39, 0\n"
  "ds_read_b64_tr_b16 a[48:49], v5 offset:36864\n"
  "ds_read_b64_tr_b16 a[50:51], v5 offset:38912\n"
  "ds_read_b64_tr_b16 a[52:53], v5 offset:36896\n"
  "ds_read_b64_tr_b16 a[54:55], v5 offset:38944\n"
  "ds_read_b64_tr_b16 a[56:57], v5 offset:36928\n"
  "ds_read_b64_tr_b16 a[58:59], v5 offset:38976\n"
  "s_waitcnt vmcnt(32) lgkmcnt(0)\n"
  "s_add_u32 m0, s80, 36864\n"
  "s_nop 0\n"
  "global_load_lds_dwordx4 v6, s[38:39]\n"
  "s_add_u32 s38, s38, 9216\n"
  "s_addc_u32 s39, s39, 0\n"
  "s_add_u32 m0, s80, 37888\n"
  "s_nop 0\n"
  "global_load_lds_dwordx4 v6, s[38:39]\n"
  "s_add_u32 s38, s38, 9216\n"
  "s_addc_u32 s39, s39, 0\n"
  "s_add_u32 m0, s80, 38912\n"
  "s_nop 0\n"
  "global_load_lds_dwordx4 v6, s[38:39]\n"
  "s_add_u32 s38, s38, 9216\n"
  "s_addc_u32 s39, s39, 0\n"
  "s_add_u32 m0, s80, 39936\n"
  "s_nop 0\n"
  "global_load_lds_dwordx4 v6, s[38:39]\n"
  "s_add_u32 s38, s38, 9216\n"
  "s_addc_u32 s39, s39, 0\n"
  "ds_read_b64_tr_b16 a[60:61], v5 offset:0\n"
  "ds_read_b64_tr_b16 a[62:63], v5 offset:2048\n"
  "ds_read_b64_tr_b16 a[64:65], v5 offset:32\n"
  "ds_read_b64_tr_b16 a[66:67], v5 offset:2080\n"
  "ds_read_b64_tr_b16 a[68:69], v5 offset:64\n"
  "ds_read_b64_tr_b16 a[70:71], v5 offset:2112\n"
  "s_waitcnt vmcnt(32) lgkmcnt(0)\n"
  "s_add_u32 m0, s80, 0\n"
  "s_nop 0\n"
  "global_load_lds_dwordx4 v6, s[38:39]\n"
  "s_add_u32 s38, s38, 9216\n"
  "s_addc_u32 s39, s39, 0\n"
  "s_add_u32 m0, s80, 1024\n"
  "s_nop 0\n"
  "global_load_lds_dwordx4 v6, s[38:39]\n"
  "s_add_u32 s38, s38, 9216\n"
  "s_addc_u32 s39, s39, 0\n"
  "s_add_u32 m0, s80, 2048\n"
  "s_nop 0\n"
  "global_load_lds_dwordx4 v6, s[38:39]\n"
  "s_add_u32 s38, s38, 9216\n"
  "s_addc_u32 s39, s39, 0\n"
  "s_add_u32 m0, s80, 3072\n"
  "s_nop 0\n"
  "global_load_lds_dwordx4 v6, s[38:39]\n"
  "s_add_u32 s38, s38, 9216\n"
  "s_addc_u32 s39, s39, 0\n"
  "s_cmp_eq_u32 s50, 3\n"
  "s_cselect_b32 s74, 1179648, 0\n"
  "s_sub_u32 s38, s38, s74\n"
  "s_subb_u32 s39, s39, 0\n"
  "ds_read_b64_tr_b16 a[72:73], v5 offset:4096\n"
  "ds_read_b64_tr_b16 a[74:75], v5 offset:6144\n"
  "ds_read_b64_tr_b16 a[76:77], v5 offset:4128\n"
  "ds_read_b64_tr_b16 a[78:79], v5 offset:6176\n"
  "ds_read_b64_tr_b16 a[80:81], v5 offset:4160\n"
  "ds_read_b64_tr_b16 a[82:83], v5 offset:6208\n"
  "s_waitcnt vmcnt(32) lgkmcnt(0)\n"
  "s_add_u32 m0, s80, 4096\n"
  "s_nop 0\n"
  "global_load_lds_dwordx4 v6, s[38:39]\n"
  "s_add_u32 s38, s38, 9216\n"
  "s_addc_u32 s39, s39, 0\n"
  "s_add_u32 m0, s80, 5120\n"
  "s_nop 0\n"
  "global_load_lds_dwordx4 v6, s[38:39]\n"
  "s_add_u32 s38, s38, 9216\n"
  "s_addc_u32 s39, s39, 0\n"
  "s_add_u32 m0, s80, 6144\n"
  "s_nop 0\n"
  "global_load_lds_dwordx4 v6, s[38:39]\n"
  "s_add_u32 s38, s38, 9216\n"
  "s_addc_u32 s39, s39, 0\n"
  "s_add_u32 m0, s80, 7168\n"
  "s_nop 0\n"
  "global_load_lds_dwordx4 v6, s[38:39]\n"
  "s_add_u32 s38, s38, 9216\n"
  "s_addc_u32 s39, s39, 0\n"
  "ds_read_b64_tr_b16 a[84:85], v5 offset:8192\n"
  "ds_read_b64_tr_b16 a[86:87], v5 offset:10240\n"
  "ds_read_b64_tr_b16 a[88:89], v5 offset:8224\n"
  "ds_read_b64_tr_b16 a[90:91], v5 offset:10272\n"
  "ds_read_b64_tr_b16 a[92:93], v5 offset:8256\n"
  "ds_read_b64_tr_b16 a[94:95], v5 offset:10304\n"
  "s_waitcnt vmcnt(32) lgkmcnt(0)\n"
  "s_add_u32 m0, s80, 8192\n"
  "s_nop 0\n"
  "global_load_lds_dwordx4 v6, s[38:39]\n"
  "s_add_u32 s38, s38, 9216\n"
  "s_addc_u32 s39, s39, 0\n"
  "s_add_u32 m0, s80, 9216\n"
  "s_nop 0\n"
  "global_load_lds_dwordx4 v6, s[38:39]\n"
  "s_add_u32 s38, s38, 9216\n"
  "s_addc_u32 s39, s39, 0\n"
  "s_add_u32 m0, s80, 10240\n"
  "s_nop 0\n"
  "global_load_lds_dwordx4 v6, s[38:39]\n"
  "s_add_u32 s38, s38, 9216\n"
  "s_addc_u32 s39, s39, 0\n"
  "s_add_u32 m0, s80, 11264\n"
  "s_nop 0\n"
  "global_load_lds_dwordx4 v6, s[38:39]\n"
  "s_add_u32 s38, s38, 9216\n"
  "s_addc_u32 s39, s39, 0\n"
  "ds_read_b64_tr_b16 a[96:97], v5 offset:12288\n"
  "ds_read_b64_tr_b16 a[98:99], v5 offset:14336\n"
  "ds_read_b64_tr_b16 a[100:101], v5 offset:12320\n"
  "ds_read_b64_tr_b16 a[102:103], v5 offset:14368\n"
  "ds_read_b64_tr_b16 a[104:105], v5 offset:12352\n"
  "ds_read_b64_tr_b16 a[106:107], v5 offset:14400\n"
  "s_waitcnt vmcnt(32) lgkmcnt(0)\n"
  "s_add_u32 m0, s80, 12288\n"
  "s_nop 0\n"
  "global_load_lds_dwordx4 v6, s[38:39]\n"
  "s_add_u32 s38, s38, 9216\n"
  "s_addc_u32 s39, s39, 0\n"
  "s_add_u32 m0, s80, 13312\n"
  "s_nop 0\n"
  "global_load_lds_dwordx4 v6, s[38:39]\n"
  "s_add_u32 s38, s38, 9216\n"
  "s_addc_u32 s39, s39, 0\n"
  "s_add_u32 m0, s80, 14336\n"
  "s_nop 0\n"
  "global_load_lds_dwordx4 v6, s[38:39]\n"
  "s_add_u32 s38, s38, 9216\n"
  "s_addc_u32 s39, s39, 0\n"
  "s_add_u32 m0, s80, 15360\n"
  "s_nop 0\n"
  "global_load_lds_dwordx4 v6, s[38:39]\n"
  "s_add_u32 s38, s38, 9216\n"
  "s_addc_u32 s39, s39, 0\n"
  "ds_read_b64_tr_b16 a[108:109], v5 offset:16384\n"
  "ds_read_b64_tr_b16 a[110:111], v5 offset:18432\n"
  "ds_read_b64_tr_b16 a[112:113], v5 offset:16416\n"
  "ds_read_b64_tr_b16 a[114:115], v5 offset:18464\n"
  "ds_read_b64_tr_b16 a[116:117], v5 offset:16448\n"
  "ds_read_b64_tr_b16 a[118:119], v5 offset:18496\n"
  "s_waitcnt vmcnt(32) lgkmcnt(0)\n"
  "s_add_u32 m0, s80, 16384\n"
  "s_nop 0\n"
  "global_load_lds_dwordx4 v6, s[38:39]\n"
  "s_add_u32 s38, s38, 9216\n"
  "s_addc_u32 s39, s39, 0\n"
  "s_add_u32 m0, s80, 17408\n"
  "s_nop 0\n"
  "global_load_lds_dwordx4 v6, s[38:39]\n"
  "s_add_u32 s38, s38, 9216\n"
  "s_addc_u32 s39, s39, 0\n"
  "s_add_u32 m0, s80, 18432\n"
  "s_nop 0\n"
  "global_load_lds_dwordx4 v6, s[38:39]\n"
  "s_add_u32 s38, s38, 9216\n"
  "s_addc_u32 s39, s39, 0\n"
  "s_add_u32 m0, s80, 19456\n"
  "s_nop 0\n"
  "global_load_lds_dwordx4 v6, s[38:39]\n"
  "s_add_u32 s38, s38, 9216\n"
  "s_addc_u32 s39, s39, 0\n"
  "s_cmp_eq_u32 s50, 4\n"
  "s_cselect_b32 s74, 1179648, 0\n"
  "s_sub_u32 s38, s38, s74\n"
  "s_subb_u32 s39, s39, 0\n"
  "ds_read_b64_tr_b16 a[120:121], v5 offset:20480\n"
  "ds_read_b64_tr_b16 a[122:123], v5 offset:22528\n"
  "ds_read_b64_tr_b16 a[124:125], v5 offset:20512\n"
  "ds_read_b64_tr_b16 a[126:127], v5 offset:22560\n"
  "ds_read_b64_tr_b16 a[128:129], v5 offset:20544\n"
  "ds_read_b64_tr_b16 a[130:131], v5 offset:22592\n"
  "s_waitcnt vmcnt(32) lgkmcnt(0)\n"
  "s_add_u32 m0, s80, 20480\n"
  "s_nop 0\n"
  "global_load_lds_dwordx4 v6, s[38:39]\n"
  "s_add_u32 s38, s38, 9216\n"
  "s_addc_u32 s39, s39, 0\n"
  "s_add_u32 m0, s80, 21504\n"
  "s_nop 0\n"
  "global_load_lds_dwordx4 v6, s[38:39]\n"
  "s_add_u32 s38, s38, 9216\n"
  "s_addc_u32 s39, s39, 0\n"
  "s_add_u32 m0, s80, 22528\n"
  "s_nop 0\n"
  "global_load_lds_dwordx4 v6, s[38:39]\n"
  "s_add_u32 s38, s38, 9216\n"
  "s_addc_u32 s39, s39, 0\n"
  "s_add_u32 m0, s80, 23552\n"
  "s_nop 0\n"
  "global_load_lds_dwordx4 v6, s[38:39]\n"
  "s_add_u32 s38, s38, 9216\n"
  "s_addc_u32 s39, s39, 0\n"
  "ds_read_b64_tr_b16 a[132:133], v5 offset:24576\n"
  "ds_read_b64_tr_b16 a[134:135], v5 offset:26624\n"
  "ds_read_b64_tr_b16 a[136:137], v5 offset:24608\n"
  "ds_read_b64_tr_b16 a[138:139], v5 offset:26656\n"
  "ds_read_b64_tr_b16 a[140:141], v5 offset:24640\n"
  "ds_read_b64_tr_b16 a[142:143], v5 offset:26688\n"
  "s_waitcnt vmcnt(32) lgkmcnt(0)\n"
  "s_add_u32 m0, s80, 24576\n"
  "s_nop 0\n"
  "global_load_lds_dwordx4 v6, s[38:39]\n"
  "s_add_u32 s38, s38, 9216\n"
  "s_addc_u32 s39, s39, 0\n"
  "s_add_u32 m0, s80, 25600\n"
  "s_nop 0\n"
  "global_load_lds_dwordx4 v6, s[38:39]\n"
  "s_add_u32 s38, s38, 9216\n"
  "s_addc_u32 s39, s39, 0\n"
  "s_add_u32 m0, s80, 26624\n"
  "s_nop 0\n"
  "global_load_lds_dwordx4 v6, s[38:39]\n"
  "s_add_u32 s38, s38, 9216\n"
  "s_addc_u32 s39, s39, 0\n"
  "s_add_u32 m0, s80, 27648\n"
  "s_nop 0\n"
  "global_load_lds_dwordx4 v6, s[38:39]\n"
  "s_add_u32 s38, s38, 9216\n"
  "s_addc_u32 s39, s39, 0\n"
  "ds_read_b64_tr_b16 a[144:145], v5 offset:28672\n"
  "ds_read_b64_tr_b16 a[146:147], v5 offset:30720\n"
  "ds_read_b64_tr_b16 a[148:149], v5 offset:28704\n"
  "ds_read_b64_tr_b16 a[150:151], v5 offset:30752\n"
  "ds_read_b64_tr_b16 a[152:153], v5 offset:28736\n"
  "ds_read_b64_tr_b16 a[154:155], v5 offset:30784\n"
  "s_waitcnt vmcnt(32) lgkmcnt(0)\n"
  "s_add_u32 m0, s80, 28672\n"
  "s_nop 0\n"
  "global_load_lds_dwordx4 v6, s[38:39]\n"
  "s_add_u32 s38, s38, 9216\n"
  "s_addc_u32 s39, s39, 0\n"
  "s_add_u32 m0, s80, 29696\n"
  "s_nop 0\n"
  "global_load_lds_dwordx4 v6, s[38:39]\n"
  "s_add_u32 s38, s38, 9216\n"
  "s_addc_u32 s39, s39, 0\n"
  "s_add_u32 m0, s80, 30720\n"
  "s_nop 0\n"
  "global_load_lds_dwordx4 v6, s[38:39]\n"
  "s_add_u32 s38, s38, 9216\n"
  "s_addc_u32 s39, s39, 0\n"
  "s_add_u32 m0, s80, 31744\n"
  "s_nop 0\n"
  "global_load_lds_dwordx4 v6, s[38:39]\n"
  "s_add_u32 s38, s38, 9216\n"
  "s_addc_u32 s39, s39, 0\n"
  "ds_read_b64_tr_b16 a[156:157], v5 offset:32768\n"
  "ds_read_b64_tr_b16 a[158:159], v5 offset:34816\n"
  "ds_read_b64_tr_b16 a[160:161], v5 offset:32800\n"
  "ds_read_b64_tr_b16 a[162:163], v5 offset:34848\n"
  "ds_read_b64_tr_b16 a[164:165], v5 offset:32832\n"
  "ds_read_b64_tr_b16 a[166:167], v5 offset:34880\n"
  "s_waitcnt vmcnt(32) lgkmcnt(0)\n"
  "s_add_u32 m0, s80, 32768\n"
  "s_nop 0\n"
  "global_load_lds_dwordx4 v6, s[38:39]\n"
  "s_add_u32 s38, s38, 9216\n"
  "s_addc_u32 s39, s39, 0\n"
  "s_add_u32 m0, s80, 33792\n"
  "s_nop 0\n"
  "global_load_lds_dwordx4 v6, s[38:39]\n"
  "s_add_u32 s38, s38, 9216\n"
  "s_addc_u32 s39, s39, 0\n"
  "s_add_u32 m0, s80, 34816\n"
  "s_nop 0\n"
  "global_load_lds_dwordx4 v6, s[38:39]\n"
  "s_add_u32 s38, s38, 9216\n"
  "s_addc_u32 s39, s39, 0\n"
  "s_add_u32 m0, s80, 35840\n"
  "s_nop 0\n"
  "global_load_lds_dwordx4 v6, s[38:39]\n"
  "s_add_u32 s38, s38, 9216\n"
  "s_addc_u32 s39, s39, 0\n"
  "s_cmp_eq_u32 s50, 5\n"
  "s_cselect_b32 s74, 1179648, 0\n"
  "s_sub_u32 s38, s38, s74\n"
  "s_subb_u32 s39, s39, 0\n"
  "ds_read_b64_tr_b16 a[168:169], v5 offset:36864\n"
  "ds_read_b64_tr_b16 a[170:171], v5 offset:38912\n"
  "ds_read_b64_tr_b16 a[172:173], v5 offset:36896\n"
  "ds_read_b64_tr_b16 a[174:175], v5 offset:38944\n"
  "ds_read_b64_tr_b16 a[176:177], v5 offset:36928\n"
  "ds_read_b64_tr_b16 a[178:179], v5 offset:38976\n"
  "s_waitcnt vmcnt(32) lgkmcnt(0)\n"
  "s_add_u32 m0, s80, 36864\n"
  "s_nop 0\n"
  "global_load_lds_dwordx4 v6, s[38:39]\n"
  "s_add_u32 s38, s38, 9216\n"
  "s_addc_u32 s39, s39, 0\n"
  "s_add_u32 m0, s80, 37888\n"
  "s_nop 0\n"
  "global_load_lds_dwordx4 v6, s[38:39]\n"
  "s_add_u32 s38, s38, 9216\n"
  "s_addc_u32 s39, s39, 0\n"
  "s_add_u32 m0, s80, 38912\n"
  "s_nop 0\n"
  "global_load_lds_dwordx4 v6, s[38:39]\n"
  "s_add_u32 s38, s38, 9216\n"
  "s_addc_u32 s39, s39, 0\n"
  "s_add_u32 m0, s80, 39936\n"
  "s_nop 0\n"
  "global_load_lds_dwordx4 v6, s[38:39]\n"
  "s_add_u32 s38, s38, 9216\n"
  "s_addc_u32 s39, s39, 0\n"
  "ds_read_b64_tr_b16 a[180:181], v5 offset:0\n"
  "ds_read_b64_tr_b16 a[182:183], v5 offset:2048\n"
  "ds_read_b64_tr_b16 a[184:185], v5 offset:32\n"
  "ds_read_b64_tr_b16 a[186:187], v5 offset:2080\n"
  "ds_read_b64_tr_b16 a[188:189], v5 offset:64\n"
  "ds_read_b64_tr_b16 a[190:191], v5 offset:2112\n"
  "s_waitcnt vmcnt(32) lgkmcnt(0)\n"
  "s_add_u32 m0, s80, 0\n"
  "s_nop 0\n"
  "global_load_lds_dwordx4 v6, s[38:39]\n"
  "s_add_u32 s38, s38, 9216\n"
  "s_addc_u32 s39, s39, 0\n"
  "s_add_u32 m0, s80, 1024\n"
  "s_nop 0\n"
  "global_load_lds_dwordx4 v6, s[38:39]\n"
  "s_add_u32 s38, s38, 9216\n"
  "s_addc_u32 s39, s39, 0\n"
  "s_add_u32 m0, s80, 2048\n"
  "s_nop 0\n"
  "global_load_lds_dwordx4 v6, s[38:39]\n"
  "s_add_u32 s38, s38, 9216\n"
  "s_addc_u32 s39, s39, 0\n"
  "s_add_u32 m0, s80, 3072\n"
  "s_nop 0\n"
  "global_load_lds_dwordx4 v6, s[38:39]\n"
  "s_add_u32 s38, s38, 9216\n"
  "s_addc_u32 s39, s39, 0\n"
  "ds_read_b64_tr_b16 a[192:193], v5 offset:4096\n"
  "ds_read_b64_tr_b16 a[194:195], v5 offset:6144\n"
  "ds_read_b64_tr_b16 a[196:197], v5 offset:4128\n"
  "ds_read_b64_tr_b16 a[198:199], v5 offset:6176\n"
  "ds_read_b64_tr_b16 a[200:201], v5 offset:4160\n"
  "ds_read_b64_tr_b16 a[202:203], v5 offset:6208\n"
  "s_waitcnt vmcnt(32) lgkmcnt(0)\n"
  "s_add_u32 m0, s80, 4096\n"
  "s_nop 0\n"
  "global_load_lds_dwordx4 v6, s[38:39]\n"
  "s_add_u32 s38, s38, 9216\n"
  "s_addc_u32 s39, s39, 0\n"
  "s_add_u32 m0, s80, 5120\n"
  "s_nop 0\n"
  "global_load_lds_dwordx4 v6, s[38:39]\n"
  "s_add_u32 s38, s38, 9216\n"
  "s_addc_u32 s39, s39, 0\n"
  "s_add_u32 m0, s80, 6144\n"
  "s_nop 0\n"
  "global_load_lds_dwordx4 v6, s[38:39]\n"
  "s_add_u32 s38, s38, 9216\n"
  "s_addc_u32 s39, s39, 0\n"
  "s_add_u32 m0, s80, 7168\n"
  "s_nop 0\n"
  "global_load_lds_dwordx4 v6, s[38:39]\n"
  "s_add_u32 s38, s38, 9216\n"
  "s_addc_u32 s39, s39, 0\n"
  "ds_read_b64_tr_b16 a[204:205], v5 offset:8192\n"
  "ds_read_b64_tr_b16 a[206:207], v5 offset:10240\n"
  "ds_read_b64_tr_b16 a[208:209], v5 offset:8224\n"
  "ds_read_b64_tr_b16 a[210:211], v5 offset:10272\n"
  "ds_read_b64_tr_b16 a[212:213], v5 offset:8256\n"
  "ds_read_b64_tr_b16 a[214:215], v5 offset:10304\n"
  "s_waitcnt vmcnt(32) lgkmcnt(0)\n"
  "s_add_u32 m0, s80, 8192\n"
  "s_nop 0\n"
  "global_load_lds_dwordx4 v6, s[38:39]\n"
  "s_add_u32 s38, s38, 9216\n"
  "s_addc_u32 s39, s39, 0\n"
  "s_add_u32 m0, s80, 9216\n"
  "s_nop 0\n"
  "global_load_lds_dwordx4 v6, s[38:39]\n"
  "s_add_u32 s38, s38, 9216\n"
  "s_addc_u32 s39, s39, 0\n"
  "s_add_u32 m0, s80, 10240\n"
  "s_nop 0\n"
  "global_load_lds_dwordx4 v6, s[38:39]\n"
  "s_add_u32 s38, s38, 9216\n"
  "s_addc_u32 s39, s39, 0\n"
  "s_add_u32 m0, s80, 11264\n"
  "s_nop 0\n"
  "global_load_lds_dwordx4 v6, s[38:39]\n"
  "s_add_u32 s38, s38, 9216\n"
  "s_addc_u32 s39, s39, 0\n"
  "s_cmp_eq_u32 s50, 6\n"
  "s_cselect_b32 s74, 1179648, 0\n"
  "s_sub_u32 s38, s38, s74\n"
  "s_subb_u32 s39, s39, 0\n"
  "ds_read_b64_tr_b16 a[216:217], v5 offset:12288\n"
  "ds_read_b64_tr_b16 a[218:219], v5 offset:14336\n"
  "ds_read_b64_tr_b16 a[220:221], v5 offset:12320\n"
  "ds_read_b64_tr_b16 a[222:223], v5 offset:14368\n"
  "ds_read_b64_tr_b16 a[224:225], v5 offset:12352\n"
  "ds_read_b64_tr_b16 a[226:227], v5 offset:14400\n"
  "s_waitcnt vmcnt(32) lgkmcnt(0)\n"
  "s_add_u32 m0, s80, 12288\n"
  "s_nop 0\n"
  "global_load_lds_dwordx4 v6, s[38:39]\n"
  "s_add_u32 s38, s38, 9216\n"
  "s_addc_u32 s39, s39, 0\n"
  "s_add_u32 m0, s80, 13312\n"
  "s_nop 0\n"
  "global_load_lds_dwordx4 v6, s[38:39]\n"
  "s_add_u32 s38, s38, 9216\n"
  "s_addc_u32 s39, s39, 0\n"
  "s_add_u32 m0, s80, 14336\n"
  "s_nop 0\n"
  "global_load_lds_dwordx4 v6, s[38:39]\n"
  "s_add_u32 s38, s38, 9216\n"
  "s_addc_u32 s39, s39, 0\n"
  "s_add_u32 m0, s80, 15360\n"
  "s_nop 0\n"
  "global_load_lds_dwordx4 v6, s[38:39]\n"
  "s_add_u32 s38, s38, 9216\n"
  "s_addc_u32 s39, s39, 0\n"
  "ds_read_b64_tr_b16 a[228:229], v5 offset:16384\n"
  "ds_read_b64_tr_b16 a[230:231], v5 offset:18432\n"
  "ds_read_b64_tr_b16 a[232:233], v5 offset:16416\n"
  "ds_read_b64_tr_b16 a[234:235], v5 offset:18464\n"
  "ds_read_b64_tr_b16 a[236:237], v5 offset:16448\n"
  "ds_read_b64_tr_b16 a[238:239], v5 offset:18496\n"
  "s_waitcnt vmcnt(32) lgkmcnt(0)\n"
  "s_add_u32 m0, s80, 16384\n"
  "s_nop 0\n"
  "global_load_lds_dwordx4 v6, s[38:39]\n"
  "s_add_u32 s38, s38, 9216\n"
  "s_addc_u32 s39, s39, 0\n"
  "s_add_u32 m0, s80, 17408\n"
  "s_nop 0\n"
  "global_load_lds_dwordx4 v6, s[38:39]\n"
  "s_add_u32 s38, s38, 9216\n"
  "s_addc_u32 s39, s39, 0\n"
  "s_add_u32 m0, s80, 18432\n"
  "s_nop 0\n"
  "global_load_lds_dwordx4 v6, s[38:39]\n"
  "s_add_u32 s38, s38, 9216\n"
  "s_addc_u32 s39, s39, 0\n"
  "s_add_u32 m0, s80, 19456\n"
  "s_nop 0\n"
  "global_load_lds_dwordx4 v6, s[38:39]\n"
  "s_add_u32 s38, s38, 9216\n"
  "s_addc_u32 s39, s39, 0\n"
  "ds_read_b64_tr_b16 a[240:241], v5 offset:20480\n"
  "ds_read_b64_tr_b16 a[242:243], v5 offset:22528\n"
  "ds_read_b64_tr_b16 a[244:245], v5 offset:20512\n"
  "ds_read_b64_tr_b16 a[246:247], v5 offset:22560\n"
  "ds_read_b64_tr_b16 a[248:249], v5 offset:20544\n"
  "ds_read_b64_tr_b16 a[250:251], v5 offset:22592\n"
  "s_waitcnt vmcnt(32) lgkmcnt(0)\n"
  "s_add_u32 m0, s80, 20480\n"
  "s_nop 0\n"
  "global_load_lds_dwordx4 v6, s[38:39]\n"
  "s_add_u32 s38, s38, 9216\n"
  "s_addc_u32 s39, s39, 0\n"
  "s_add_u32 m0, s80, 21504\n"
  "s_nop 0\n"
  "global_load_lds_dwordx4 v6, s[38:39]\n"
  "s_add_u32 s38, s38, 9216\n"
  "s_addc_u32 s39, s39, 0\n"
  "s_add_u32 m0, s80, 22528\n"
  "s_nop 0\n"
  "global_load_lds_dwordx4 v6, s[38:39]\n"
  "s_add_u32 s38, s38, 9216\n"
  "s_addc_u32 s39, s39, 0\n"
  "s_add_u32 m0, s80, 23552\n"
  "s_nop 0\n"
  "global_load_lds_dwordx4 v6, s[38:39]\n"
  "s_add_u32 s38, s38, 9216\n"
  "s_addc_u32 s39, s39, 0\n"
  "ds_read_b64_tr_b16 a[252:253], v5 offset:24576\n"
  "ds_read_b64_tr_b16 a[254:255], v5 offset:26624\n"
  "ds_read_b64_tr_b16 v[140:141], v5 offset:24608\n"
  "ds_read_b64_tr_b16 v[142:143], v5 offset:26656\n"
  "ds_read_b64_tr_b16 v[144:145], v5 offset:24640\n"
  "ds_read_b64_tr_b16 v[146:147], v5 offset:26688\n"
  "s_waitcnt vmcnt(32) lgkmcnt(0)\n"
  "s_add_u32 m0, s80, 24576\n"
  "s_nop 0\n"
  "global_load_lds_dwordx4 v6, s[38:39]\n"
  "s_add_u32 s38, s38, 9216\n"
  "s_addc_u32 s39, s39, 0\n"
  "s_add_u32 m0, s80, 25600\n"
  "s_nop 0\n"
  "global_load_lds_dwordx4 v6, s[38:39]\n"
  "s_add_u32 s38, s38, 9216\n"
  "s_addc_u32 s39, s39, 0\n"
  "s_add_u32 m0, s80, 26624\n"
  "s_nop 0\n"
  "global_load_lds_dwordx4 v6, s[38:39]\n"
  "s_add_u32 s38, s38, 9216\n"
  "s_addc_u32 s39, s39, 0\n"
  "s_add_u32 m0, s80, 27648\n"
  "s_nop 0\n"
  "global_load_lds_dwordx4 v6, s[38:39]\n"
  "s_add_u32 s38, s38, 9216\n"
  "s_addc_u32 s39, s39, 0\n"
  "s_cmp_eq_u32 s50, 7\n"
  "s_cselect_b32 s74, 1179648, 0\n"
  "s_sub_u32 s38, s38, s74\n"
  "s_subb_u32 s39, s39, 0\n"
  "ds_read_b64_tr_b16 v[148:149], v5 offset:28672\n"
  "ds_read_b64_tr_b16 v[150:151], v5 offset:30720\n"
  "ds_read_b64_tr_b16 v[152:153], v5 offset:28704\n"
  "ds_read_b64_tr_b16 v[154:155], v5 offset:30752\n"
  "ds_read_b64_tr_b16 v[156:157], v5 offset:28736\n"
  "ds_read_b64_tr_b16 v[158:159], v5 offset:30784\n"
  "s_waitcnt vmcnt(32) lgkmcnt(0)\n"
  "ds_read_b64_tr_b16 v[160:161], v5 offset:32768\n"
  "ds_read_b64_tr_b16 v[162:163], v5 offset:34816\n"
  "ds_read_b64_tr_b16 v[164:165], v5 offset:32800\n"
  "ds_read_b64_tr_b16 v[166:167], v5 offset:34848\n"
  "ds_read_b64_tr_b16 v[168:169], v5 offset:32832\n"
  "ds_read_b64_tr_b16 v[170:171], v5 offset:34880\n"
  "s_waitcnt vmcnt(28) lgkmcnt(0)\n"
  "ds_read_b64_tr_b16 v[172:173], v5 offset:36864\n"
  "ds_read_b64_tr_b16 v[174:175], v5 offset:38912\n"
  "ds_read_b64_tr_b16 v[176:177], v5 offset:36896\n"
  "ds_read_b64_tr_b16 v[178:179], v5 offset:38944\n"
  "ds_read_b64_tr_b16 v[180:181], v5 offset:36928\n"
  "ds_read_b64_tr_b16 v[182:183], v5 offset:38976\n"
  "s_waitcnt vmcnt(24) lgkmcnt(0)\n"
  "ds_read_b64_tr_b16 v[184:185], v5 offset:0\n"
  "ds_read_b64_tr_b16 v[186:187], v5 offset:2048\n"
  "ds_read_b64_tr_b16 v[188:189], v5 offset:32\n"
  "ds_read_b64_tr_b16 v[190:191], v5 offset:2080\n"
  "ds_read_b64_tr_b16 v[192:193], v5 offset:64\n"
  "ds_read_b64_tr_b16 v[194:195], v5 offset:2112\n"
  "s_waitcnt vmcnt(20) lgkmcnt(0)\n"
  "ds_read_b64_tr_b16 v[196:197], v5 offset:4096\n"
  "ds_read_b64_tr_b16 v[198:199], v5 offset:6144\n"
  "ds_read_b64_tr_b16 v[200:201], v5 offset:4128\n"
  "ds_read_b64_tr_b16 v[202:203], v5 offset:6176\n"
  "ds_read_b64_tr_b16 v[204:205], v5 offset:4160\n"
  "ds_read_b64_tr_b16 v[206:207], v5 offset:6208\n"
  "s_waitcnt vmcnt(16) lgkmcnt(0)\n"
  "ds_read_b64_tr_b16 v[208:209], v5 offset:8192\n"
  "ds_read_b64_tr_b16 v[210:211], v5 offset:10240\n"
  "ds_read_b64_tr_b16 v[212:213], v5 offset:8224\n"
  "ds_read_b64_tr_b16 v[214:215], v5 offset:10272\n"
  "ds_read_b64_tr_b16 v[216:217], v5 offset:8256\n"
  "ds_read_b64_tr_b16 v[218:219], v5 offset:10304\n"
  "s_waitcnt vmcnt(12) lgkmcnt(0)\n"
  "ds_read_b64_tr_b16 v[220:221], v5 offset:12288\n"
  "ds_read_b64_tr_b16 v[222:223], v5 offset:14336\n"
  "ds_read_b64_tr_b16 v[224:225], v5 offset:12320\n"
  "ds_read_b64_tr_b16 v[226:227], v5 offset:14368\n"
  "ds_read_b64_tr_b16 v[228:229], v5 offset:12352\n"
  "ds_read_b64_tr_b16 v[230:231], v5 offset:14400\n"
  "s_waitcnt vmcnt(8) lgkmcnt(0)\n"
  "ds_read_b64_tr_b16 v[232:233], v5 offset:16384\n"
  "ds_read_b64_tr_b16 v[234:235], v5 offset:18432\n"
  "ds_read_b64_tr_b16 v[236:237], v5 offset:16416\n"
  "ds_read_b64_tr_b16 v[238:239], v5 offset:18464\n"
  "ds_read_b64_tr_b16 v[240:241], v5 offset:16448\n"
  "ds_read_b64_tr_b16 v[242:243], v5 offset:18496\n"
  "s_waitcnt vmcnt(4) lgkmcnt(0)\n"
  "ds_read_b64_tr_b16 v[244:245], v5 offset:20480\n"
  "ds_read_b64_tr_b16 v[246:247], v5 offset:22528\n"
  "ds_read_b64_tr_b16 v[248:249], v5 offset:20512\n"
  "ds_read_b64_tr_b16 v[250:251], v5 offset:22560\n"
  "ds_read_b64_tr_b16 v[252:253], v5 offset:20544\n"
  "ds_read_b64_tr_b16 v[254:255], v5 offset:22592\n"
  "s_waitcnt vmcnt(0) lgkmcnt(0)\n"
  "ds_read_b64_tr_b16 v[112:113], v5 offset:24576\n"
  "ds_read_b64_tr_b16 v[114:115], v5 offset:26624\n"
  "ds_read_b64_tr_b16 v[116:117], v5 offset:24608\n"
  "ds_read_b64_tr_b16 v[118:119], v5 offset:26656\n"
  "ds_read_b64_tr_b16 v[120:121], v5 offset:24640\n"
  "ds_read_b64_tr_b16 v[122:123], v5 offset:26688\n"
  "s_waitcnt lgkmcnt(0)\n"
  "s_barrier\n"
  "s_mul_i32 s74, s47, 18432\n"
  "v_add_u32 v5, s74, v0\n"
  "ds_write_b128 v5, v[52:55] offset:0\n"
  "ds_write_b128 v5, v[56:59] offset:1024\n"
  "ds_write_b128 v5, v[60:63] offset:2048\n"
  "ds_write_b128 v5, v[64:67] offset:3072\n"
  "ds_write_b128 v5, v[68:71] offset:4096\n"
  "ds_write_b128 v5, v[72:75] offset:5120\n"
  "ds_write_b128 v5, v[76:79] offset:6144\n"
  "ds_write_b128 v5, v[80:83] offset:7168\n"
  "ds_write_b128 v5, v[84:87] offset:8192\n"
  "ds_write_b128 v5, v[88:91] offset:9216\n"
  "ds_write_b128 v5, v[92:95] offset:10240\n"
  "ds_write_b128 v5, v[96:99] offset:11264\n"
  "ds_write_b128 v5, v[100:103] offset:12288\n"
  "ds_write_b128 v5, v[104:107] offset:13312\n"
  "ds_write_b128 v5, v[108:111] offset:14336\n"
  "ds_write_b128 v5, v[112:115] offset:15360\n"
  "ds_write_b128 v5, v[116:119] offset:16384\n"
  "ds_write_b128 v5, v[120:123] offset:17408\n"
  "s_mul_i32 s74, s47, 6144\n"
  "s_add_u32 s74, s74, 73728\n"
  "v_add_u32 v11, s74, v0\n"
  "s_mov_b32 s57, 400000\n"
  "s_mov_b32 s56, 24576\n"
  "s_add_u32 s66, s36, 16777216\n"
  "s_addc_u32 s67, s37, 0\n"
  "s_add_u32 s74, s36, s5\n"
  "s_addc_u32 s75, s37, 0\n"
  "BLO:\n"
  "v_mov_b32 v6, 0\n"
  "global_load_dwordx2 v[6:7], v6, s[74:75] sc1\n"
  "s_waitcnt vmcnt(0)\n"
  "v_readfirstlane_b32 s77, v6\n"
  "v_readfirstlane_b32 s76, v7\n"
  "s_cmp_eq_u32 s77, s4\n"
  "s_cbranch_scc1 BDO\n"
  "s_sub_u32 s57, s57, 1\n"
  "s_cbranch_scc1 OQUIT\n"
  "s_sleep 4\n"
  "s_branch BLO\n"
  "BDO:\n"
  "s_sub_u32 s77, s5, 16966656\n"
  "s_lshr_b32 s77, s77, 8\n"
  "s_lshl_b32 s77, s77, 7\n"
  "s_add_u32 s77, s77, 16777216\n"
  "s_add_u32 s74, s36, s77\n"
  "s_addc_u32 s75, s37, 0\n"
  "v_mov_b32 v6, 0\n"
  "v_mov_b32 v7, 1\n"
  "s_cmp_eq_u32 s51, 0\n"
  "s_cselect_b64 exec, 1, 0\n"
  "global_atomic_add v6, v7, s[74:75] sc1\n"
  "s_mov_b64 exec, -1\n"
  "s_and_b32 s74, s76, 2\n"
  "s_lshr_b32 s75, s74, 1\n"
  "s_lshl_b32 s4, 0x55555555, s75\n"
  "s_mov_b32 s5, s4\n"
  "s_lshl_b32 s74, s74, 12\n"
  "v_xor_b32 v1, s74, v1\n"
  "s_xor_b32 s49, s74, s49\n"
  "s_add_u32 s68, s36, s49\n"
  "s_addc_u32 s69, s37, 0\n"
  "v_mov_b32 v124, 1\n"
  "s_cmp_eq_u32 s47, 0\n"
  "s_cselect_b64 exec, 1, 0\n"
  "global_atomic_add v124, v0, v124, s[68:69] sc0 sc1\n"
  "s_mov_b64 exec, -1\n"
  "v_mov_b32 v6, 122880\n"
  "s_cmp_eq_u32 s47, 0\n"
  "s_cselect_b64 exec, 1, 0\n"
  "s_waitcnt vmcnt(0)\n"
  "ds_write_b32 v6, v124\n"
  "s_mov_b64 exec, -1\n"
  "s_waitcnt lgkmcnt(0)\n"
  "s_barrier\n"
  "ds_read_b32 v6, v6\n"
  "s_waitcnt lgkmcnt(0)\n"
  "v_readfirstlane_b32 s54, v6\n"
  "s_barrier\n"
  "ONEXT:\n"
  "s_cmp_ge_u32 s54, 128\n"
  "s_cbranch_scc1 OQUIT\n"
  "s_cmp_ge_u32 s54, 126\n"
  "s_cbranch_scc0 OPOLL\n"
  "v_add_u32 v1, v1, v127\n"
  "v_mov_b32 v127, 0\n"
  "OPOLL:\n"
  "global_load_dword v8, v1, s[66:67] sc1\n"
  "s_lshr_b32 s74, s54, 1\n"
  "s_waitcnt vmcnt(0)\n"
  "v_cmp_ge_u32 vcc, s74, v8\n"
  "s_cbranch_vccz OGO\n"
  "s_sub_u32 s57, s57, 1\n"
  "s_cbranch_scc1 OQUIT\n"
  "s_sleep 4\n"
  "s_branch OPOLL\n"
  "OGO:\n"
  "s_lshr_b32 s75, s54, 1\n"
  "s_and_b32 s76, s54, 1\n"
  "s_mul_i32 s74, s54, 20480\n"
  "s_add_u32 s74, s74, s45\n"
  "s_add_u32 s62, s36, s74\n"
  "s_addc_u32 s63, s37, 0\n"
  "s_lshl_b32 s77, s76, 12\n"
  "s_add_u32 s77, s77, s44\n"
  "s_bitcmp1_b64 s[4:5], s75\n"
  "s_cselect_b32 s78, 8192, 0\n"
  "s_and_b32 s79, s75, 63\n"
  "s_lshl_b32 s79, s79, 21\n"
  "s_add_u32 s78, s78, s79\n"
  "s_add_u32 s78, s78, 20971520\n"
  "s_xor_b32 s79, s78, s77\n"
  "s_add_u32 s60, s36, s79\n"
  "s_addc_u32 s61, s37, 0\n"
  "s_lshl_b32 s74, s75, 11\n"
  "s_lshl_b32 s77, s76, 6\n"
  "s_add_u32 s74, s74, s77\n"
  "s_add_u32 s74, s74, s46\n"
  "s_add_u32 s64, s36, s74\n"
  "s_addc_u32 s65, s37, 0\n"
  "global_load_dwordx4 v[52:55], v0, s[60:61] offset:0 sc1\n"
  "global_load_dwordx4 v[56:59], v0, s[60:61] offset:1024 sc1\n"
  "global_load_dwordx4 v[60:63], v0, s[60:61] offset:2048 sc1\n"
  "global_load_dwordx4 v[64:67], v0, s[60:61] offset:3072 sc1\n"
  "s_cmp_eq_u32 s50, 0\n"
  "s_cselect_b32 s74, 524288, 0\n"
  "s_sub_u32 s60, s60, s74\n"
  "s_subb_u32 s61, s61, 0\n"
  "s_add_u32 s60, s60, 65536\n"
  "s_addc_u32 s61, s61, 0\n"
  "global_load_dwordx4 v[68:71], v0, s[60:61] offset:0 sc1\n"
  "global_load_dwordx4 v[72:75], v0, s[60:61] offset:1024 sc1\n"
  "global_load_dwordx4 v[76:79], v0, s[60:61] offset:2048 sc1\n"
  "global_load_dwordx4 v[80:83], v0, s[60:61] offset:3072 sc1\n"
  "s_cmp_eq_u32 s50, 1\n"
  "s_cselect_b32 s74, 524288, 0\n"
  "s_sub_u32 s60, s60, s74\n"
  "s_subb_u32 s61, s61, 0\n"
  "s_add_u32 s60, s60, 65536\n"
  "s_addc_u32 s61, s61, 0\n"
  "global_load_dwordx4 v[84:87], v0, s[60:61] offset:0 sc1\n"
  "global_load_dwordx4 v[88:91], v0, s[60:61] offset:1024 sc1\n"
  "global_load_dwordx4 v[92:95], v0, s[60:61] offset:2048 sc1\n"
  "global_load_dwordx4 v[96:99], v0, s[60:61] offset:3072 sc1\n"
  "s_cmp_eq_u32 s50, 2\n"
  "s_cselect_b32 s74, 524288, 0\n"
  "s_sub_u32 s60, s60, s74\n"
  "s_subb_u32 s61, s61, 0\n"
  "s_add_u32 s60, s60, 65536\n"
  "s_addc_u32 s61, s61, 0\n"
  "global_load_dwordx4 v[100:103], v0, s[60:61] offset:0 sc1\n"
  "global_load_dwordx4 v[104:107], v0, s[60:61] offset:1024 sc1\n"
  "global_load_dwordx4 v[108:111], v0, s[60:61] offset:2048 sc1\n"
  "global_load_dwordx4 v[112:115], v0, s[60:61] offset:3072 sc1\n"
  "s_cmp_eq_u32 s50, 3\n"
  "s_cselect_b32 s74, 524288, 0\n"
  "s_sub_u32 s60, s60, s74\n"
  "s_subb_u32 s61, s61, 0\n"
  "s_add_u32 s60, s60, 65536\n"
  "s_addc_u32 s61, s61, 0\n"
  "global_load_dwordx4 v[116:119], v0, s[60:61] offset:0 sc1\n"
  "global_load_dwordx4 v[120:123], v0, s[60:61] offset:1024 sc1\n"
  "global_load_dwordx4 v[48:51], v0, s[60:61] offset:2048 sc1\n"
  "OSEG:\n"
  "v_mov_b32 v124, 1\n"
  "s_cmp_eq_u32 s47, 0\n"
  "s_cselect_b64 exec, 1, 0\n"
  "global_atomic_add v124, v0, v124, s[68:69] sc0 sc1\n"
  "s_mov_b64 exec, -1\n"
  "global_load_dword v8, v1, s[66:67] sc1\n"
  "s_waitcnt vmcnt(20)\n"
  "v_mfma_f32_16x16x32_bf16 v[12:15], a[0:3], v[52:55], 0\n"
  "v_mfma_f32_16x16x32_bf16 v[16:19], a[4:7], v[52:55], 0\n"
  "v_mfma_f32_16x16x32_bf16 v[20:23], a[8:11], v[52:55], 0\n"
  "global_load_dwordx4 v[52:55], v0, s[60:61] offset:3072 sc1\n"
  "s_cmp_eq_u32 s50, 4\n"
  "s_cselect_b32 s74, 524288, 0\n"
  "s_sub_u32 s60, s60, s74\n"
  "s_subb_u32 s61, s61, 0\n"
  "s_add_u32 s60, s60, 65536\n"
  "s_addc_u32 s61, s61, 0\n"
  "s_waitcnt vmcnt(20)\n"
  "v_mfma_f32_16x16x32_bf16 v[12:15], a[12:15], v[56:59], v[12:15]\n"
  "v_mfma_f32_16x16x32_bf16 v[16:19], a[16:19], v[56:59], v[16:19]\n"
  "v_mfma_f32_16x16x32_bf16 v[20:23], a[20:23], v[56:59], v[20:23]\n"
  "global_load_dwordx4 v[56:59], v0, s[60:61] offset:0 sc1\n"
  "s_waitcnt vmcnt(20)\n"
  "v_mfma_f32_16x16x32_bf16 v[12:15], a[24:27], v[60:63], v[12:15]\n"
  "v_mfma_f32_16x16x32_bf16 v[16:19], a[28:31], v[60:63], v[16:19]\n"
  "v_mfma_f32_16x16x32_bf16 v[20:23], a[32:35], v[60:63], v[20:23]\n"
  "global_load_dwordx4 v[60:63], v0, s[60:61] offset:1024 sc1\n"
  "s_waitcnt vmcnt(20)\n"
  "v_mfma_f32_16x16x32_bf16 v[12:15], a[36:39], v[64:67], v[12:15]\n"
  "v_mfma_f32_16x16x32_bf16 v[16:19], a[40:43], v[64:67], v[16:19]\n"
  "v_mfma_f32_16x16x32_bf16 v[20:23], a[44:47], v[64:67], v[20:23]\n"
  "global_load_dwordx4 v[64:67], v0, s[60:61] offset:2048 sc1\n"
  "s_waitcnt vmcnt(20)\n"
  "v_mfma_f32_16x16x32_bf16 v[12:15], a[48:51], v[68:71], v[12:15]\n"
  "v_mfma_f32_16x16x32_bf16 v[16:19], a[52:55], v[68:71], v[16:19]\n"
  "v_mfma_f32_16x16x32_bf16 v[20:23], a[56:59], v[68:71], v[20:23]\n"
  "global_load_dwordx4 v[68:71], v0, s[60:61] offset:3072 sc1\n"
  "s_cmp_eq_u32 s50, 5\n"
  "s_cselect_b32 s74, 524288, 0\n"
  "s_sub_u32 s60, s60, s74\n"
  "s_subb_u32 s61, s61, 0\n"
  "s_add_u32 s60, s60, 65536\n"
  "s_addc_u32 s61, s61, 0\n"
  "s_waitcnt vmcnt(20)\n"
  "v_mfma_f32_16x16x32_bf16 v[12:15], a[60:63], v[72:75], v[12:15]\n"
  "v_mfma_f32_16x16x32_bf16 v[16:19], a[64:67], v[72:75], v[16:19]\n"
  "v_mfma_f32_16x16x32_bf16 v[20:23], a[68:71], v[72:75], v[20:23]\n"
  "global_load_dwordx4 v[72:75], v0, s[60:61] offset:0 sc1\n"
  "s_waitcnt vmcnt(20)\n"
  "v_mfma_f32_16x16x32_bf16 v[12:15], a[72:75], v[76:79], v[12:15]\n"
  "v_mfma_f32_16x16x32_bf16 v[16:19], a[76:79], v[76:79], v[16:19]\n"
  "v_mfma_f32_16x16x32_bf16 v[20:23], a[80:83], v[76:79], v[20:23]\n"
  "global_load_dwordx4 v[76:79], v0, s[60:61] offset:1024 sc1\n"
  "s_waitcnt vmcnt(20)\n"
  "v_mfma_f32_16x16x32_bf16 v[12:15], a[84:87], v[80:83], v[12:15]\n"
  "v_mfma_f32_16x16x32_bf16 v[16:19], a[88:91], v[80:83], v[16:19]\n"
  "v_mfma_f32_16x16x32_bf16 v[20:23], a[92:95], v[80:83], v[20:23]\n"
  "global_load_dwordx4 v[80:83], v0, s[60:61] offset:2048 sc1\n"
  "s_waitcnt vmcnt(20)\n"
  "v_mfma_f32_16x16x32_bf16 v[12:15], a[96:99], v[84:87], v[12:15]\n"
  "v_mfma_f32_16x16x32_bf16 v[16:19], a[100:103], v[84:87], v[16:19]\n"
  "v_mfma_f32_16x16x32_bf16 v[20:23], a[104:107], v[84:87], v[20:23]\n"
  "global_load_dwordx4 v[84:87], v0, s[60:61] offset:3072 sc1\n"
  "s_cmp_eq_u32 s50, 6\n"
  "s_cselect_b32 s74, 524288, 0\n"
  "s_sub_u32 s60, s60, s74\n"
  "s_subb_u32 s61, s61, 0\n"
  "s_add_u32 s60, s60, 65536\n"
  "s_addc_u32 s61, s61, 0\n"
  "s_waitcnt vmcnt(20)\n"
  "v_mfma_f32_16x16x32_bf16 v[12:15], a[108:111], v[88:91], v[12:15]\n"
  "v_mfma_f32_16x16x32_bf16 v[16:19], a[112:115], v[88:91], v[16:19]\n"
  "v_mfma_f32_16x16x32_bf16 v[20:23], a[116:119], v[88:91], v[20:23]\n"
  "global_load_dwordx4 v[88:91], v0, s[60:61] offset:0 sc1\n"
  "s_waitcnt vmcnt(20)\n"
  "v_mfma_f32_16x16x32_bf16 v[12:15], a[120:123], v[92:95], v[12:15]\n"
  "v_mfma_f32_16x16x32_bf16 v[16:19], a[124:127], v[92:95], v[16:19]\n"
  "v_mfma_f32_16x16x32_bf16 v[20:23], a[128:131], v[92:95], v[20:23]\n"
  "global_load_dwordx4 v[92:95], v0, s[60:61] offset:1024 sc1\n"
  "s_waitcnt vmcnt(20)\n"
  "v_mfma_f32_16x16x32_bf16 v[12:15], a[132:135], v[96:99], v[12:15]\n"
  "v_mfma_f32_16x16x32_bf16 v[16:19], a[136:139], v[96:99], v[16:19]\n"
  "v_mfma_f32_16x16x32_bf16 v[20:23], a[140:143], v[96:99], v[20:23]\n"
  "global_load_dwordx4 v[96:99], v0, s[60:61] offset:2048 sc1\n"
  "s_waitcnt vmcnt(20)\n"
  "v_mfma_f32_16x16x32_bf16 v[12:15], a[144:147], v[100:103], v[12:15]\n"
  "v_mfma_f32_16x16x32_bf16 v[16:19], a[148:151], v[100:103], v[16:19]\n"
  "v_mfma_f32_16x16x32_bf16 v[20:23], a[152:155], v[100:103], v[20:23]\n"
  "global_load_dwordx4 v[100:103], v0, s[60:61] offset:3072 sc1\n"
  "s_cmp_eq_u32 s50, 7\n"
  "s_cselect_b32 s74, 524288, 0\n"
  "s_sub_u32 s60, s60, s74\n"
  "s_subb_u32 s61, s61, 0\n"
  "s_add_u32 s60, s60, 65536\n"
  "s_addc_u32 s61, s61, 0\n"
  "s_waitcnt vmcnt(20)\n"
  "v_mfma_f32_16x16x32_bf16 v[12:15], a[156:159], v[104:107], v[12:15]\n"
  "v_mfma_f32_16x16x32_bf16 v[16:19], a[160:163], v[104:107], v[16:19]\n"
  "v_mfma_f32_16x16x32_bf16 v[20:23], a[164:167], v[104:107], v[20:23]\n"
  "global_load_dwordx4 v[104:107], v0, s[62:63] offset:-2048 sc1\n"
  "s_waitcnt vmcnt(20)\n"
  "v_mfma_f32_16x16x32_bf16 v[12:15], a[168:171], v[108:111], v[12:15]\n"
  "v_mfma_f32_16x16x32_bf16 v[16:19], a[172:175], v[108:111], v[16:19]\n"
  "v_mfma_f32_16x16x32_bf16 v[20:23], a[176:179], v[108:111], v[20:23]\n"
  "global_load_dwordx4 v[108:111], v0, s[62:63] offset:-1024 sc1\n"
  "s_waitcnt vmcnt(20)\n"
  "v_mfma_f32_16x16x32_bf16 v[12:15], a[180:183], v[112:115], v[12:15]\n"
  "v_mfma_f32_16x16x32_bf16 v[16:19], a[184:187], v[112:115], v[16:19]\n"
  "v_mfma_f32_16x16x32_bf16 v[20:23], a[188:191], v[112:115], v[20:23]\n"
  "global_load_dwordx4 v[112:115], v0, s[62:63] offset:0 sc1\n"
  "s_waitcnt vmcnt(20)\n"
  "v_mfma_f32_16x16x32_bf16 v[12:15], a[192:195], v[116:119], v[12:15]\n"
  "v_mfma_f32_16x16x32_bf16 v[16:19], a[196:199], v[116:119], v[16:19]\n"
  "v_mfma_f32_16x16x32_bf16 v[20:23], a[200:203], v[116:119], v[20:23]\n"
  "global_load_dwordx4 v[116:119], v0, s[62:63] offset:1024 sc1\n"
  "s_waitcnt vmcnt(20)\n"
  "v_mfma_f32_16x16x32_bf16 v[12:15], a[204:207], v[120:123], v[12:15]\n"
  "v_mfma_f32_16x16x32_bf16 v[16:19], a[208:211], v[120:123], v[16:19]\n"
  "v_mfma_f32_16x16x32_bf16 v[20:23], a[212:215], v[120:123], v[20:23]\n"
  "global_load_dwordx4 v[120:123], v0, s[62:63] offset:2048 sc1\n"
  "s_waitcnt vmcnt(20)\n"
  "v_mfma_f32_16x16x32_bf16 v[12:15], a[216:219], v[48:51], v[12:15]\n"
  "v_mfma_f32_16x16x32_bf16 v[16:19], a[220:223], v[48:51], v[16:19]\n"
  "v_mfma_f32_16x16x32_bf16 v[20:23], a[224:227], v[48:51], v[20:23]\n"
  "global_load_dword v9, v2, s[64:65] sc1\n"
  "v_mov_b32 v6, 122880\n"
  "s_cmp_eq_u32 s47, 0\n"
  "s_cselect_b64 exec, 1, 0\n"
  "s_waitcnt vmcnt(20)\n"
  "ds_write_b32 v6, v124\n"
  "s_mov_b64 exec, -1\n"
  "s_waitcnt lgkmcnt(0)\n"
  "s_barrier\n"
  "ds_read_b32 v6, v6\n"
  "s_waitcnt lgkmcnt(0)\n"
  "v_readfirstlane_b32 s55, v6\n"
  "s_lshr_b32 s75, s55, 1\n"
  "s_and_b32 s76, s55, 1\n"
  "s_mul_i32 s74, s55, 20480\n"
  "s_add_u32 s74, s74, s45\n"
  "s_add_u32 s62, s36, s74\n"
  "s_addc_u32 s63, s37, 0\n"
  "s_lshl_b32 s77, s76, 12\n"
  "s_add_u32 s77, s77, s44\n"
  "s_bitcmp1_b64 s[4:5], s75\n"
  "s_cselect_b32 s78, 8192, 0\n"
  "s_and_b32 s79, s75, 63\n"
  "s_lshl_b32 s79, s79, 21\n"
  "s_add_u32 s78, s78, s79\n"
  "s_add_u32 s78, s78, 20971520\n"
  "s_xor_b32 s79, s78, s77\n"
  "s_add_u32 s60, s36, s79\n"
  "s_addc_u32 s61, s37, 0\n"
  "s_lshl_b32 s74, s75, 11\n"
  "s_lshl_b32 s77, s76, 6\n"
  "s_add_u32 s74, s74, s77\n"
  "s_add_u32 s74, s74, s46\n"
  "s_add_u32 s64, s36, s74\n"
  "s_addc_u32 s65, s37, 0\n"
  "s_cmp_lt_u32 s55, 126\n"
  "s_cselect_b32 s75, s75, 64\n"
  "s_waitcnt vmcnt(19)\n"
  "v_cmp_ge_u32 vcc, s75, v8\n"
  "s_cbranch_vccnz ODRAIN\n"
  "s_waitcnt vmcnt(18)\n"
  "v_mfma_f32_16x16x32_bf16 v[12:15], a[228:231], v[52:55], v[12:15]\n"
  "v_mfma_f32_16x16x32_bf16 v[16:19], a[232:235], v[52:55], v[16:19]\n"
  "v_mfma_f32_16x16x32_bf16 v[20:23], a[236:239], v[52:55], v[20:23]\n"
  "global_load_dwordx4 v[52:55], v0, s[60:61] offset:0 sc1\n"
  "s_waitcnt vmcnt(18)\n"
  "v_mfma_f32_16x16x32_bf16 v[12:15], a[240:243], v[56:59], v[12:15]\n"
  "v_mfma_f32_16x16x32_bf16 v[16:19], a[244:247], v[56:59], v[16:19]\n"
  "v_mfma_f32_16x16x32_bf16 v[20:23], a[248:251], v[56:59], v[20:23]\n"
  "global_load_dwordx4 v[56:59], v0, s[60:61] offset:1024 sc1\n"
  "s_waitcnt vmcnt(18)\n"
  "v_mfma_f32_16x16x32_bf16 v[12:15], a[252:255], v[60:63], v[12:15]\n"
  "v_mfma_f32_16x16x32_bf16 v[16:19], v[140:143], v[60:63], v[16:19]\n"
  "v_mfma_f32_16x16x32_bf16 v[20:23], v[144:147], v[60:63], v[20:23]\n"
  "global_load_dwordx4 v[60:63], v0, s[60:61] offset:2048 sc1\n"
  "s_waitcnt vmcnt(18)\n"
  "v_mfma_f32_16x16x32_bf16 v[12:15], v[148:151], v[64:67], v[12:15]\n"
  "v_mfma_f32_16x16x32_bf16 v[16:19], v[152:155], v[64:67], v[16:19]\n"
  "v_mfma_f32_16x16x32_bf16 v[20:23], v[156:159], v[64:67], v[20:23]\n"
  "global_load_dwordx4 v[64:67], v0, s[60:61] offset:3072 sc1\n"
  "s_cmp_eq_u32 s50, 0\n"
  "s_cselect_b32 s74, 524288, 0\n"
  "s_sub_u32 s60, s60, s74\n"
  "s_subb_u32 s61, s61, 0\n"
  "s_add_u32 s60, s60, 65536\n"
  "s_addc_u32 s61, s61, 0\n"
  "s_waitcnt vmcnt(18)\n"
  "v_mfma_f32_16x16x32_bf16 v[12:15], v[160:163], v[68:71], v[12:15]\n"
  "v_mfma_f32_16x16x32_bf16 v[16:19], v[164:167], v[68:71], v[16:19]\n"
  "v_mfma_f32_16x16x32_bf16 v[20:23], v[168:171], v[68:71], v[20:23]\n"
  "global_load_dwordx4 v[68:71], v0, s[60:61] offset:0 sc1\n"
  "s_waitcnt vmcnt(18)\n"
  "v_mfma_f32_16x16x32_bf16 v[12:15], v[172:175], v[72:75], v[12:15]\n"
  "v_mfma_f32_16x16x32_bf16 v[16:19], v[176:179], v[72:75], v[16:19]\n"
  "v_mfma_f32_16x16x32_bf16 v[20:23], v[180:183], v[72:75], v[20:23]\n"
  "global_load_dwordx4 v[72:75], v0, s[60:61] offset:1024 sc1\n"
  "s_waitcnt vmcnt(18)\n"
  "v_mfma_f32_16x16x32_bf16 v[12:15], v[184:187], v[76:79], v[12:15]\n"
  "v_mfma_f32_16x16x32_bf16 v[16:19], v[188:191], v[76:79], v[16:19]\n"
  "v_mfma_f32_16x16x32_bf16 v[20:23], v[192:195], v[76:79], v[20:23]\n"
  "global_load_dwordx4 v[76:79], v0, s[60:61] offset:2048 sc1\n"
  "s_waitcnt vmcnt(18)\n"
  "v_mfma_f32_16x16x32_bf16 v[12:15], v[196:199], v[80:83], v[12:15]\n"
  "v_mfma_f32_16x16x32_bf16 v[16:19], v[200:203], v[80:83], v[16:19]\n"
  "v_mfma_f32_16x16x32_bf16 v[20:23], v[204:207], v[80:83], v[20:23]\n"
  "global_load_dwordx4 v[80:83], v0, s[60:61] offset:3072 sc1\n"
  "s_cmp_eq_u32 s50, 1\n"
  "s_cselect_b32 s74, 524288, 0\n"
  "s_sub_u32 s60, s60, s74\n"
  "s_subb_u32 s61, s61, 0\n"
  "s_add_u32 s60, s60, 65536\n"
  "s_addc_u32 s61, s61, 0\n"
  "s_waitcnt vmcnt(18)\n"
  "v_mfma_f32_16x16x32_bf16 v[12:15], v[208:211], v[84:87], v[12:15]\n"
  "v_mfma_f32_16x16x32_bf16 v[16:19], v[212:215], v[84:87], v[16:19]\n"
  "v_mfma_f32_16x16x32_bf16 v[20:23], v[216:219], v[84:87], v[20:23]\n"
  "global_load_dwordx4 v[84:87], v0, s[60:61] offset:0 sc1\n"
  "s_waitcnt vmcnt(18)\n"
  "v_mfma_f32_16x16x32_bf16 v[12:15], v[220:223], v[88:91], v[12:15]\n"
  "v_mfma_f32_16x16x32_bf16 v[16:19], v[224:227], v[88:91], v[16:19]\n"
  "v_mfma_f32_16x16x32_bf16 v[20:23], v[228:231], v[88:91], v[20:23]\n"
  "global_load_dwordx4 v[88:91], v0, s[60:61] offset:1024 sc1\n"
  "s_waitcnt vmcnt(18)\n"
  "v_mfma_f32_16x16x32_bf16 v[12:15], v[232:235], v[92:95], v[12:15]\n"
  "v_mfma_f32_16x16x32_bf16 v[16:19], v[236:239], v[92:95], v[16:19]\n"
  "v_mfma_f32_16x16x32_bf16 v[20:23], v[240:243], v[92:95], v[20:23]\n"
  "global_load_dwordx4 v[92:95], v0, s[60:61] offset:2048 sc1\n"
  "ds_read_b128 v[36:39], v5 offset:15360\n"
  "ds_read_b128 v[40:43], v5 offset:16384\n"
  "ds_read_b128 v[44:47], v5 offset:17408\n"
  "s_waitcnt vmcnt(18)\n"
  "v_mfma_f32_16x16x32_bf16 v[12:15], v[244:247], v[96:99], v[12:15]\n"
  "v_mfma_f32_16x16x32_bf16 v[16:19], v[248:251], v[96:99], v[16:19]\n"
  "v_mfma_f32_16x16x32_bf16 v[20:23], v[252:255], v[96:99], v[20:23]\n"
  "global_load_dwordx4 v[96:99], v0, s[60:61] offset:3072 sc1\n"
  "s_cmp_eq_u32 s50, 2\n"
  "s_cselect_b32 s74, 524288, 0\n"
  "s_sub_u32 s60, s60, s74\n"
  "s_subb_u32 s61, s61, 0\n"
  "s_add_u32 s60, s60, 65536\n"
  "s_addc_u32 s61, s61, 0\n"
  "ds_read_b128 v[128:131], v5 offset:0\n"
  "ds_read_b128 v[132:135], v5 offset:1024\n"
  "ds_read_b128 v[136:139], v5 offset:2048\n"
  "s_waitcnt vmcnt(18) lgkmcnt(5)\n"
  "v_mfma_f32_16x16x32_bf16 v[12:15], v[36:39], v[100:103], v[12:15]\n"
  "s_waitcnt lgkmcnt(4)\n"
  "v_mfma_f32_16x16x32_bf16 v[16:19], v[40:43], v[100:103], v[16:19]\n"
  "s_waitcnt lgkmcnt(3)\n"
  "v_mfma_f32_16x16x32_bf16 v[20:23], v[44:47], v[100:103], v[20:23]\n"
  "global_load_dwordx4 v[100:103], v0, s[60:61] offset:0 sc1\n"
  "ds_read_b128 v[36:39], v5 offset:3072\n"
  "ds_read_b128 v[40:43], v5 offset:4096\n"
  "ds_read_b128 v[44:47], v5 offset:5120\n"
  "s_waitcnt vmcnt(18) lgkmcnt(5)\n"
  "v_mfma_f32_16x16x32_bf16 v[24:27], v[128:131], v[104:107], 0\n"
  "s_waitcnt lgkmcnt(4)\n"
  "v_mfma_f32_16x16x32_bf16 v[28:31], v[132:135], v[104:107], 0\n"
  "s_waitcnt lgkmcnt(3)\n"
  "v_mfma_f32_16x16x32_bf16 v[32:35], v[136:139], v[104:107], 0\n"
  "global_load_dwordx4 v[104:107], v0, s[60:61] offset:1024 sc1\n"
  "ds_read_b128 v[128:131], v5 offset:6144\n"
  "ds_read_b128 v[132:135], v5 offset:7168\n"
  "ds_read_b128 v[136:139], v5 offset:8192\n"
  "s_waitcnt vmcnt(18) lgkmcnt(5)\n"
  "v_mfma_f32_16x16x32_bf16 v[24:27], v[36:39], v[108:111], v[24:27]\n"
  "s_waitcnt lgkmcnt(4)\n"
  "v_mfma_f32_16x16x32_bf16 v[28:31], v[40:43], v[108:111], v[28:31]\n"
  "s_waitcnt lgkmcnt(3)\n"
  "v_mfma_f32_16x16x32_bf16 v[32:35], v[44:47], v[108:111], v[32:35]\n"
  "global_load_dwordx4 v[108:111], v0, s[60:61] offset:2048 sc1\n"
  "ds_read_b128 v[36:39], v5 offset:9216\n"
  "ds_read_b128 v[40:43], v5 offset:10240\n"
  "ds_read_b128 v[44:47], v5 offset:11264\n"
  "s_waitcnt vmcnt(18) lgkmcnt(5)\n"
  "v_mfma_f32_16x16x32_bf16 v[24:27], v[128:131], v[112:115], v[24:27]\n"
  "s_waitcnt lgkmcnt(4)\n"
  "v_mfma_f32_16x16x32_bf16 v[28:31], v[132:135], v[112:115], v[28:31]\n"
  "s_waitcnt lgkmcnt(3)\n"
  "v_mfma_f32_16x16x32_bf16 v[32:35], v[136:139], v[112:115], v[32:35]\n"
  "global_load_dwordx4 v[112:115], v0, s[60:61] offset:3072 sc1\n"
  "s_cmp_eq_u32 s50, 3\n"
  "s_cselect_b32 s74, 524288, 0\n"
  "s_sub_u32 s60, s60, s74\n"
  "s_subb_u32 s61, s61, 0\n"
  "s_add_u32 s60, s60, 65536\n"
  "s_addc_u32 s61, s61, 0\n"
  "ds_read_b128 v[128:131], v5 offset:12288\n"
  "ds_read_b128 v[132:135], v5 offset:13312\n"
  "ds_read_b128 v[136:139], v5 offset:14336\n"
  "s_waitcnt vmcnt(18) lgkmcnt(5)\n"
  "v_mfma_f32_16x16x32_bf16 v[24:27], v[36:39], v[116:119], v[24:27]\n"
  "s_waitcnt lgkmcnt(4)\n"
  "v_mfma_f32_16x16x32_bf16 v[28:31], v[40:43], v[116:119], v[28:31]\n"
  "s_waitcnt lgkmcnt(3)\n"
  "v_mfma_f32_16x16x32_bf16 v[32:35], v[44:47], v[116:119], v[32:35]\n"
  "global_load_dwordx4 v[116:119], v0, s[60:61] offset:0 sc1\n"
  "s_waitcnt vmcnt(18) lgkmcnt(2)\n"
  "v_mfma_f32_16x16x32_bf16 v[24:27], v[128:131], v[120:123], v[24:27]\n"
  "s_waitcnt lgkmcnt(1)\n"
  "v_mfma_f32_16x16x32_bf16 v[28:31], v[132:135], v[120:123], v[28:31]\n"
  "s_waitcnt lgkmcnt(0)\n"
  "v_mfma_f32_16x16x32_bf16 v[32:35], v[136:139], v[120:123], v[32:35]\n"
  "global_load_dwordx4 v[120:123], v0, s[60:61] offset:1024 sc1\n"
  "s_waitcnt vmcnt(18)\n"
  "v_mov_b32 v10, v9\n"
  "global_load_dwordx4 v[48:51], v0, s[60:61] offset:2048 sc1\n"
  "ds_write_b128 v11, v[12:15] offset:0\n"
  "ds_write_b128 v11, v[16:19] offset:1024\n"
  "ds_write_b128 v11, v[20:23] offset:2048\n"
  "ds_write_b128 v11, v[24:27] offset:3072\n"
  "ds_write_b128 v11, v[28:31] offset:4096\n"
  "ds_write_b128 v11, v[32:35] offset:5120\n"
  "v_sub_f32 v6, 1.0, v10\n"
  "v_cvt_pk_bf16_f32 v6, v6, v6\n"
  "v_lshlrev_b32 v6, 16, v6\n"
  "s_waitcnt lgkmcnt(0)\n"
  "s_barrier\n"
  "ds_read_b128 v[28:31], v3 offset:0\n"
  "ds_read_b128 v[32:35], v3 offset:6144\n"
  "ds_read_b128 v[36:39], v3 offset:12288\n"
  "ds_read_b128 v[40:43], v3 offset:18432\n"
  "ds_read_b128 v[12:15], v3 offset:3072\n"
  "ds_read_b128 v[16:19], v3 offset:9216\n"
  "ds_read_b128 v[20:23], v3 offset:15360\n"
  "ds_read_b128 v[24:27], v3 offset:21504\n"
  "v_add_u32 v11, s56, v11\n"
  "v_add_u32 v3, s56, v3\n"
  "s_sub_u32 s56, 0, s56\n"
  "s_mul_i32 s74, s54, 18432\n"
  "s_add_u32 s70, s40, s74\n"
  "s_addc_u32 s71, s41, 0\n"
  "s_waitcnt lgkmcnt(6)\n"
  "v_add_f32 v28, v28, v32\n"
  "s_waitcnt lgkmcnt(4)\n"
  "v_add_f32 v36, v36, v40\n"
  "v_add_f32 v28, v28, v36\n"
  "v_add_f32 v29, v29, v33\n"
  "v_add_f32 v37, v37, v41\n"
  "v_add_f32 v29, v29, v37\n"
  "v_add_f32 v30, v30, v34\n"
  "v_add_f32 v38, v38, v42\n"
  "v_add_f32 v30, v30, v38\n"
  "v_add_f32 v31, v31, v35\n"
  "v_add_f32 v39, v39, v43\n"
  "v_add_f32 v31, v31, v39\n"
  "v_cvt_pk_bf16_f32 v28, v28, v29\n"
  "v_and_b32 v29, 0xffff0000, v28\n"
  "v_lshlrev_b32 v28, 16, v28\n"
  "v_cvt_pk_bf16_f32 v30, v30, v31\n"
  "v_and_b32 v31, 0xffff0000, v30\n"
  "v_lshlrev_b32 v30, 16, v30\n"
  "v_mul_f32 v28, v6, v28\n"
  "v_mul_f32 v29, v6, v29\n"
  "v_mul_f32 v30, v6, v30\n"
  "v_mul_f32 v31, v6, v31\n"
  "v_cvt_pk_bf16_f32 v28, v28, v29\n"
  "v_and_b32 v29, 0xffff0000, v28\n"
  "v_lshlrev_b32 v28, 16, v28\n"
  "v_cvt_pk_bf16_f32 v30, v30, v31\n"
  "v_and_b32 v31, 0xffff0000, v30\n"
  "v_lshlrev_b32 v30, 16, v30\n"
  "s_waitcnt lgkmcnt(2)\n"
  "v_add_f32 v12, v12, v16\n"
  "s_waitcnt lgkmcnt(0)\n"
  "v_add_f32 v20, v20, v24\n"
  "v_add_f32 v12, v12, v20\n"
  "v_add_f32 v13, v13, v17\n"
  "v_add_f32 v21, v21, v25\n"
  "v_add_f32 v13, v13, v21\n"
  "v_add_f32 v14, v14, v18\n"
  "v_add_f32 v22, v22, v26\n"
  "v_add_f32 v14, v14, v22\n"
  "v_add_f32 v15, v15, v19\n"
  "v_add_f32 v23, v23, v27\n"
  "v_add_f32 v15, v15, v23\n"
  "v_cvt_pk_bf16_f32 v12, v12, v13\n"
  "v_and_b32 v13, 0xffff0000, v12\n"
  "v_lshlrev_b32 v12, 16, v12\n"
  "v_cvt_pk_bf16_f32 v14, v14, v15\n"
  "v_and_b32 v15, 0xffff0000, v14\n"
  "v_lshlrev_b32 v14, 16, v14\n"
  "v_mul_f32 v12, v10, v12\n"
  "v_mul_f32 v13, v10, v13\n"
  "v_mul_f32 v14, v10, v14\n"
  "v_mul_f32 v15, v10, v15\n"
  "v_cvt_pk_bf16_f32 v12, v12, v13\n"
  "v_and_b32 v13, 0xffff0000, v12\n"
  "v_lshlrev_b32 v12, 16, v12\n"
  "v_cvt_pk_bf16_f32 v14, v14, v15\n"
  "v_and_b32 v15, 0xffff0000, v14\n"
  "v_lshlrev_b32 v14, 16, v14\n"
  "v_add_f32 v28, v28, v12\n"
  "v_add_f32 v29, v29, v13\n"
  "v_add_f32 v30, v30, v14\n"
  "v_add_f32 v31, v31, v15\n"
  "v_cvt_pk_bf16_f32 v28, v28, v29\n"
  "v_cvt_pk_bf16_f32 v29, v30, v31\n"
  "s_bfm_b64 exec, 48, 0\n"
  "global_store_dwordx2 v4, v[28:29], s[70:71]\n"
  "s_mov_b64 exec, -1\n"
  "s_mov_b32 s54, s55\n"
  "s_branch OSEG\n"
  "ODRAIN:\n"
  "s_waitcnt vmcnt(18)\n"
  "v_mfma_f32_16x16x32_bf16 v[12:15], a[228:231], v[52:55], v[12:15]\n"
  "v_mfma_f32_16x16x32_bf16 v[16:19], a[232:235], v[52:55], v[16:19]\n"
  "v_mfma_f32_16x16x32_bf16 v[20:23], a[236:239], v[52:55], v[20:23]\n"
  "s_waitcnt vmcnt(17)\n"
  "v_mfma_f32_16x16x32_bf16 v[12:15], a[240:243], v[56:59], v[12:15]\n"
  "v_mfma_f32_16x16x32_bf16 v[16:19], a[244:247], v[56:59], v[16:19]\n"
  "v_mfma_f32_16x16x32_bf16 v[20:23], a[248:251], v[56:59], v[20:23]\n"
  "s_waitcnt vmcnt(16)\n"
  "v_mfma_f32_16x16x32_bf16 v[12:15], a[252:255], v[60:63], v[12:15]\n"
  "v_mfma_f32_16x16x32_bf16 v[16:19], v[140:143], v[60:63], v[16:19]\n"
  "v_mfma_f32_16x16x32_bf16 v[20:23], v[144:147], v[60:63], v[20:23]\n"
  "s_waitcnt vmcnt(15)\n"
  "v_mfma_f32_16x16x32_bf16 v[12:15], v[148:151], v[64:67], v[12:15]\n"
  "v_mfma_f32_16x16x32_bf16 v[16:19], v[152:155], v[64:67], v[16:19]\n"
  "v_mfma_f32_16x16x32_bf16 v[20:23], v[156:159], v[64:67], v[20:23]\n"
  "s_waitcnt vmcnt(14)\n"
  "v_mfma_f32_16x16x32_bf16 v[12:15], v[160:163], v[68:71], v[12:15]\n"
  "v_mfma_f32_16x16x32_bf16 v[16:19], v[164:167], v[68:71], v[16:19]\n"
  "v_mfma_f32_16x16x32_bf16 v[20:23], v[168:171], v[68:71], v[20:23]\n"
  "s_waitcnt vmcnt(13)\n"
  "v_mfma_f32_16x16x32_bf16 v[12:15], v[172:175], v[72:75], v[12:15]\n"
  "v_mfma_f32_16x16x32_bf16 v[16:19], v[176:179], v[72:75], v[16:19]\n"
  "v_mfma_f32_16x16x32_bf16 v[20:23], v[180:183], v[72:75], v[20:23]\n"
  "s_waitcnt vmcnt(12)\n"
  "v_mfma_f32_16x16x32_bf16 v[12:15], v[184:187], v[76:79], v[12:15]\n"
  "v_mfma_f32_16x16x32_bf16 v[16:19], v[188:191], v[76:79], v[16:19]\n"
  "v_mfma_f32_16x16x32_bf16 v[20:23], v[192:195], v[76:79], v[20:23]\n"
  "s_waitcnt vmcnt(11)\n"
  "v_mfma_f32_16x16x32_bf16 v[12:15], v[196:199], v[80:83], v[12:15]\n"
  "v_mfma_f32_16x16x32_bf16 v[16:19], v[200:203], v[80:83], v[16:19]\n"
  "v_mfma_f32_16x16x32_bf16 v[20:23], v[204:207], v[80:83], v[20:23]\n"
  "s_waitcnt vmcnt(10)\n"
  "v_mfma_f32_16x16x32_bf16 v[12:15], v[208:211], v[84:87], v[12:15]\n"
  "v_mfma_f32_16x16x32_bf16 v[16:19], v[212:215], v[84:87], v[16:19]\n"
  "v_mfma_f32_16x16x32_bf16 v[20:23], v[216:219], v[84:87], v[20:23]\n"
  "s_waitcnt vmcnt(9)\n"
  "v_mfma_f32_16x16x32_bf16 v[12:15], v[220:223], v[88:91], v[12:15]\n"
  "v_mfma_f32_16x16x32_bf16 v[16:19], v[224:227], v[88:91], v[16:19]\n"
  "v_mfma_f32_16x16x32_bf16 v[20:23], v[228:231], v[88:91], v[20:23]\n"
  "s_waitcnt vmcnt(8)\n"
  "v_mfma_f32_16x16x32_bf16 v[12:15], v[232:235], v[92:95], v[12:15]\n"
  "v_mfma_f32_16x16x32_bf16 v[16:19], v[236:239], v[92:95], v[16:19]\n"
  "v_mfma_f32_16x16x32_bf16 v[20:23], v[240:243], v[92:95], v[20:23]\n"
  "ds_read_b128 v[36:39], v5 offset:15360\n"
  "ds_read_b128 v[40:43], v5 offset:16384\n"
  "ds_read_b128 v[44:47], v5 offset:17408\n"
  "s_waitcnt vmcnt(7)\n"
  "v_mfma_f32_16x16x32_bf16 v[12:15], v[244:247], v[96:99], v[12:15]\n"
  "v_mfma_f32_16x16x32_bf16 v[16:19], v[248:251], v[96:99], v[16:19]\n"
  "v_mfma_f32_16x16x32_bf16 v[20:23], v[252:255], v[96:99], v[20:23]\n"
  "ds_read_b128 v[128:131], v5 offset:0\n"
  "ds_read_b128 v[132:135], v5 offset:1024\n"
  "ds_read_b128 v[136:139], v5 offset:2048\n"
  "s_waitcnt vmcnt(6) lgkmcnt(5)\n"
  "v_mfma_f32_16x16x32_bf16 v[12:15], v[36:39], v[100:103], v[12:15]\n"
  "s_waitcnt lgkmcnt(4)\n"
  "v_mfma_f32_16x16x32_bf16 v[16:19], v[40:43], v[100:103], v[16:19]\n"
  "s_waitcnt lgkmcnt(3)\n"
  "v_mfma_f32_16x16x32_bf16 v[20:23], v[44:47], v[100:103], v[20:23]\n"
  "ds_read_b128 v[36:39], v5 offset:3072\n"
  "ds_read_b128 v[40:43], v5 offset:4096\n"
  "ds_read_b128 v[44:47], v5 offset:5120\n"
  "s_waitcnt vmcnt(5) lgkmcnt(5)\n"
  "v_mfma_f32_16x16x32_bf16 v[24:27], v[128:131], v[104:107], 0\n"
  "s_waitcnt lgkmcnt(4)\n"
  "v_mfma_f32_16x16x32_bf16 v[28:31], v[132:135], v[104:107], 0\n"
  "s_waitcnt lgkmcnt(3)\n"
  "v_mfma_f32_16x16x32_bf16 v[32:35], v[136:139], v[104:107], 0\n"
  "ds_read_b128 v[128:131], v5 offset:6144\n"
  "ds_read_b128 v[132:135], v5 offset:7168\n"
  "ds_read_b128 v[136:139], v5 offset:8192\n"
  "s_waitcnt vmcnt(4) lgkmcnt(5)\n"
  "v_mfma_f32_16x16x32_bf16 v[24:27], v[36:39], v[108:111], v[24:27]\n"
  "s_waitcnt lgkmcnt(4)\n"
  "v_mfma_f32_16x16x32_bf16 v[28:31], v[40:43], v[108:111], v[28:31]\n"
  "s_waitcnt lgkmcnt(3)\n"
  "v_mfma_f32_16x16x32_bf16 v[32:35], v[44:47], v[108:111], v[32:35]\n"
  "ds_read_b128 v[36:39], v5 offset:9216\n"
  "ds_read_b128 v[40:43], v5 offset:10240\n"
  "ds_read_b128 v[44:47], v5 offset:11264\n"
  "s_waitcnt vmcnt(3) lgkmcnt(5)\n"
  "v_mfma_f32_16x16x32_bf16 v[24:27], v[128:131], v[112:115], v[24:27]\n"
  "s_waitcnt lgkmcnt(4)\n"
  "v_mfma_f32_16x16x32_bf16 v[28:31], v[132:135], v[112:115], v[28:31]\n"
  "s_waitcnt lgkmcnt(3)\n"
  "v_mfma_f32_16x16x32_bf16 v[32:35], v[136:139], v[112:115], v[32:35]\n"
  "ds_read_b128 v[128:131], v5 offset:12288\n"
  "ds_read_b128 v[132:135], v5 offset:13312\n"
  "ds_read_b128 v[136:139], v5 offset:14336\n"
  "s_waitcnt vmcnt(2) lgkmcnt(5)\n"
  "v_mfma_f32_16x16x32_bf16 v[24:27], v[36:39], v[116:119], v[24:27]\n"
  "s_waitcnt lgkmcnt(4)\n"
  "v_mfma_f32_16x16x32_bf16 v[28:31], v[40:43], v[116:119], v[28:31]\n"
  "s_waitcnt lgkmcnt(3)\n"
  "v_mfma_f32_16x16x32_bf16 v[32:35], v[44:47], v[116:119], v[32:35]\n"
  "s_waitcnt vmcnt(1) lgkmcnt(2)\n"
  "v_mfma_f32_16x16x32_bf16 v[24:27], v[128:131], v[120:123], v[24:27]\n"
  "s_waitcnt lgkmcnt(1)\n"
  "v_mfma_f32_16x16x32_bf16 v[28:31], v[132:135], v[120:123], v[28:31]\n"
  "s_waitcnt lgkmcnt(0)\n"
  "v_mfma_f32_16x16x32_bf16 v[32:35], v[136:139], v[120:123], v[32:35]\n"
  "s_waitcnt vmcnt(0)\n"
  "v_mov_b32 v10, v9\n"
  "ds_write_b128 v11, v[12:15] offset:0\n"
  "ds_write_b128 v11, v[16:19] offset:1024\n"
  "ds_write_b128 v11, v[20:23] offset:2048\n"
  "ds_write_b128 v11, v[24:27] offset:3072\n"
  "ds_write_b128 v11, v[28:31] offset:4096\n"
  "s_nop 0\n"
  "ds_write_b128 v11, v[32:35] offset:5120\n"
  "v_sub_f32 v6, 1.0, v10\n"
  "v_cvt_pk_bf16_f32 v6, v6, v6\n"
  "v_lshlrev_b32 v6, 16, v6\n"
  "s_waitcnt lgkmcnt(0)\n"
  "s_barrier\n"
  "ds_read_b128 v[28:31], v3 offset:0\n"
  "ds_read_b128 v[32:35], v3 offset:6144\n"
  "ds_read_b128 v[36:39], v3 offset:12288\n"
  "ds_read_b128 v[40:43], v3 offset:18432\n"
  "ds_read_b128 v[12:15], v3 offset:3072\n"
  "ds_read_b128 v[16:19], v3 offset:9216\n"
  "ds_read_b128 v[20:23], v3 offset:15360\n"
  "ds_read_b128 v[24:27], v3 offset:21504\n"
  "v_add_u32 v11, s56, v11\n"
  "v_add_u32 v3, s56, v3\n"
  "s_sub_u32 s56, 0, s56\n"
  "s_mul_i32 s74, s54, 18432\n"
  "s_add_u32 s70, s40, s74\n"
  "s_addc_u32 s71, s41, 0\n"
  "s_waitcnt lgkmcnt(6)\n"
  "v_add_f32 v28, v28, v32\n"
  "s_waitcnt lgkmcnt(4)\n"
  "v_add_f32 v36, v36, v40\n"
  "v_add_f32 v28, v28, v36\n"
  "v_add_f32 v29, v29, v33\n"
  "v_add_f32 v37, v37, v41\n"
  "v_add_f32 v29, v29, v37\n"
  "v_add_f32 v30, v30, v34\n"
  "v_add_f32 v38, v38, v42\n"
  "v_add_f32 v30, v30, v38\n"
  "v_add_f32 v31, v31, v35\n"
  "v_add_f32 v39, v39, v43\n"
  "v_add_f32 v31, v31, v39\n"
  "v_cvt_pk_bf16_f32 v28, v28, v29\n"
  "v_and_b32 v29, 0xffff0000, v28\n"
  "v_lshlrev_b32 v28, 16, v28\n"
  "v_cvt_pk_bf16_f32 v30, v30, v31\n"
  "v_and_b32 v31, 0xffff0000, v30\n"
  "v_lshlrev_b32 v30, 16, v30\n"
  "v_mul_f32 v28, v6, v28\n"
  "v_mul_f32 v29, v6, v29\n"
  "v_mul_f32 v30, v6, v30\n"
  "v_mul_f32 v31, v6, v31\n"
  "v_cvt_pk_bf16_f32 v28, v28, v29\n"
  "v_and_b32 v29, 0xffff0000, v28\n"
  "v_lshlrev_b32 v28, 16, v28\n"
  "v_cvt_pk_bf16_f32 v30, v30, v31\n"
  "v_and_b32 v31, 0xffff0000, v30\n"
  "v_lshlrev_b32 v30, 16, v30\n"
  "s_waitcnt lgkmcnt(2)\n"
  "v_add_f32 v12, v12, v16\n"
  "s_waitcnt lgkmcnt(0)\n"
  "v_add_f32 v20, v20, v24\n"
  "v_add_f32 v12, v12, v20\n"
  "v_add_f32 v13, v13, v17\n"
  "v_add_f32 v21, v21, v25\n"
  "v_add_f32 v13, v13, v21\n"
  "v_add_f32 v14, v14, v18\n"
  "v_add_f32 v22, v22, v26\n"
  "v_add_f32 v14, v14, v22\n"
  "v_add_f32 v15, v15, v19\n"
  "v_add_f32 v23, v23, v27\n"
  "v_add_f32 v15, v15, v23\n"
  "v_cvt_pk_bf16_f32 v12, v12, v13\n"
  "v_and_b32 v13, 0xffff0000, v12\n"
  "v_lshlrev_b32 v12, 16, v12\n"
  "v_cvt_pk_bf16_f32 v14, v14, v15\n"
  "v_and_b32 v15, 0xffff0000, v14\n"
  "v_lshlrev_b32 v14, 16, v14\n"
  "v_mul_f32 v12, v10, v12\n"
  "v_mul_f32 v13, v10, v13\n"
  "v_mul_f32 v14, v10, v14\n"
  "v_mul_f32 v15, v10, v15\n"
  "v_cvt_pk_bf16_f32 v12, v12, v13\n"
  "v_and_b32 v13, 0xffff0000, v12\n"
  "v_lshlrev_b32 v12, 16, v12\n"
  "v_cvt_pk_bf16_f32 v14, v14, v15\n"
  "v_and_b32 v15, 0xffff0000, v14\n"
  "v_lshlrev_b32 v14, 16, v14\n"
  "v_add_f32 v28, v28, v12\n"
  "v_add_f32 v29, v29, v13\n"
  "v_add_f32 v30, v30, v14\n"
  "v_add_f32 v31, v31, v15\n"
  "v_cvt_pk_bf16_f32 v28, v28, v29\n"
  "v_cvt_pk_bf16_f32 v29, v30, v31\n"
  "s_bfm_b64 exec, 48, 0\n"
  "global_store_dwordx2 v4, v[28:29], s[70:71]\n"
  "s_mov_b64 exec, -1\n"
  "s_mov_b32 s54, s55\n"
  "s_branch ONEXT\n"
  "OQUIT:\n"
  "s_endpgm\n"
      : "+{v[0:15]}"(vin), "+{v[16:31]}"(wa), "+{s[36:43]}"(sa), "+{s[44:59]}"(sb) : "{s[4:5]}"(ask) : CLOB);
  }
}

extern "C" void kernel_launch(void* const* d_in, const int* in_sizes, int n_in, void* d_out, int out_size, void* d_ws, size_t ws_size,
                              hipStream_t stream) {
  Params p;
  p.x = (const u16*)d_in[0]; p.wqkv = (const u16*)d_in[1]; p.wo_a = (const u16*)d_in[2];
  p.mem0 = (const u16*)d_in[4]; p.Wq = (const u16*)d_in[5]; p.Wk = (const u16*)d_in[6]; p.Wv = (const u16*)d_in[7];
  p.Wo = (const u16*)d_in[8]; p.gate = (const u16*)d_in[10];
  p.out = (u16*)d_out; p.ws = (unsigned char*)d_ws + ((0x200000ull - ((unsigned long long)d_ws & 0x1FFFFFull)) & 0x1FFFFFull);
  (void)hipFuncSetAttribute((const void*)gm_kernel, hipFuncAttributeMaxDynamicSharedMemorySize, LDS_BYTES);
  hipLaunchKernelGGL(gm_kernel, dim3(256), dim3(256), LDS_BYTES, stream, p);
}
```
